```python
import torch

def _softmax(x, axis=-1):
    return torch.softmax(x, dim=axis)

def _gelu(x):
    return torch.nn.functional.gelu(x, approximate='tanh')

def _silu(x):
    return torch.nn.functional.silu(x)

def _sigmoid(x):
    return torch.sigmoid(x)

def _layernorm(x, axis=-1, eps=1e-05):
    m = x.mean(dim=axis, keepdim=True)
    v = x.var(dim=axis, keepdim=True, unbiased=False)
    return (x - m) / torch.sqrt(v + eps)

def _scatter_add(tgt, idx, src, axis=0):
    return tgt.clone().index_add_(axis, idx, src)

def _segment_sum(src, seg, n):
    out = torch.zeros((n,) + src.shape[1:], dtype=src.dtype)
    return out.index_add_(0, seg, src)

def _pad(x, pw):
    flat = tuple((v for pair in reversed(pw) for v in pair))
    return torch.nn.functional.pad(x, flat)
kernel_name = 'recurrent_scan__memory_aug_recurrent_mem_tr__moe_arch_mod__prompt'

def setup_inputs():

    _w = lambda a: (lambda t: t.to(torch.bfloat16) if t.is_floating_point() else t)(torch.as_tensor(a))

    def _A_setup():
        B, S, Dm = (1, 2048, 640)
        H, Dh = (32, 128)
        M, seg = (8, 32)
        seg = min(seg, S)
        S = S // seg * seg
        return {'x': _w((lambda *s: torch.randn(*s, dtype=torch.bfloat16) * 0.05)(B, S, Dm)), 'mem0': _w((lambda *s: torch.randn(*s, dtype=torch.bfloat16) * 0.2)(B, M, Dm)), 'Wq': _w((lambda *s: torch.randn(*s, dtype=torch.bfloat16) * 1.6)(Dm, H, Dh) * Dm ** (-0.5)), 'Wk': _w((lambda *s: torch.randn(*s, dtype=torch.bfloat16) * 1.6)(Dm, H, Dh) * Dm ** (-0.5)), 'Wv': _w((lambda *s: torch.randn(*s, dtype=torch.bfloat16) * 0.2)(Dm, H, Dh) * Dm ** (-0.5)), 'Wo': _w((lambda *s: torch.randn(*s, dtype=torch.bfloat16) * 0.2)(H, Dh, Dm) * (H * Dh) ** (-0.5)), 'seg_len': seg}
    _A_in = _A_setup()

    def _B_setup():
        B, S, D, F, C = (1, 2048, 640, 1728, 8)
        return {'x': _w((lambda *s: torch.randn(*s, dtype=torch.bfloat16) * 0.05)(B, S, D)), 'w_r': _w((lambda *s: torch.randn(*s, dtype=torch.bfloat16) * 0.2)(D)), 'w_gate': _w((lambda *s: torch.randn(*s, dtype=torch.bfloat16) * 0.1)(D, F)), 'w_up': _w((lambda *s: torch.randn(*s, dtype=torch.bfloat16) * 0.1)(D, F)), 'w_down': _w((lambda *s: torch.randn(*s, dtype=torch.bfloat16) * 0.1)(F, D))}
    _B_in = _B_setup()
    return {'A_x': _A_in['x'], 'A_mem0': _A_in['mem0'], 'A_Wq': _A_in['Wq'], 'A_Wk': _A_in['Wk'], 'A_Wv': _A_in['Wv'], 'A_Wo': _A_in['Wo'], 'A_seg_len': _A_in['seg_len'], 'B_x': _B_in['x'], 'B_w_r': _B_in['w_r'], 'B_w_gate': _B_in['w_gate'], 'B_w_up': _B_in['w_up'], 'B_w_down': _B_in['w_down']}

def reference(A_x, A_mem0, A_Wq, A_Wk, A_Wv, A_Wo, A_seg_len, B_x, B_w_r, B_w_gate, B_w_up, B_w_down):

    def _A_ref(x, mem0, Wq, Wk, Wv, Wo, seg_len):
        B, S, Dm = x.shape
        H, Dh = (Wq.shape[1], Wq.shape[2])
        M = mem0.shape[1]
        scale = Dh ** (-0.5)
        n_seg = S // seg_len
        L = M + seg_len
        idx = torch.arange(L)
        mask = (idx[:, None] < M) | (idx[:, None] >= idx[None, :])
        mem = mem0
        outs = []
        for s in range(n_seg):
            xs = x[:, s * seg_len:(s + 1) * seg_len, :].clone()
            h = torch.concatenate([mem, xs], axis=1)
            q = torch.einsum('btd,dhe->bhte', h, Wq)
            k = torch.einsum('btd,dhe->bhte', h, Wk)
            v = torch.einsum('btd,dhe->bhte', h, Wv)
            scores = torch.einsum('bhqe,bhke->bhqk', q, k) * scale
            scores = torch.where(mask[None, None], scores, -1000000000.0)
            p = _softmax(scores, axis=-1)
            o = torch.einsum('bhqk,bhke->bhqe', p, v)
            o = torch.einsum('bhte,hed->btd', o, Wo)
            mem = o[:, :M, :].clone()
            outs.append(o[:, M:, :])
        return torch.concatenate(outs, axis=1)

    def _B_ref(x, w_r, w_gate, w_up, w_down):
        B, S, D = x.shape
        C = 8
        r = x @ w_r
        idx = torch.argsort(-r, axis=-1)[:, :C]
        idx_e = (lambda x, n, axis: torch.repeat_interleave(x, n, dim=axis))(idx.reshape(B, C, 1), D, axis=2)
        x_sel = (lambda x, i, axis: torch.take_along_dim(x, i, dim=axis))(x, idx_e, axis=1)
        g = _silu(x_sel @ w_gate)
        u = x_sel @ w_up
        y = g * u @ w_down
        r_sel = _sigmoid((lambda x, i, axis: torch.take_along_dim(x, i, dim=axis))(r, idx, axis=1))
        y = y * r_sel.reshape(B, C, 1)
        b_off = (torch.arange(B) * S).reshape(B, 1)
        flat_idx = (idx + b_off).reshape(B * C)
        zero = (x * 0.0).reshape(B * S, D)
        upd = _scatter_add(zero, flat_idx, y.reshape(B * C, D), axis=0)
        return x + upd.reshape(B, S, D)
    x = A_x
    xn = _layernorm(x, axis=-1)
    s = _A_ref(xn, A_mem0, A_Wq, A_Wk, A_Wv, A_Wo, A_seg_len)
    g = _silu(_B_ref(xn, B_w_r, B_w_gate, B_w_up, B_w_down))
    return x + s * g
```

```cpp
#define CFG 0
#include <hip/hip_runtime.h>
#include <hip/hip_bf16.h>
#include <stdint.h>

constexpr int S_ = 2048, D_ = 640, H_ = 32, DH_ = 128, NSEG = 64, SEGL = 32, FF_ = 1728, TOPC = 8;
constexpr int HD_ = H_ * DH_;
constexpr int NRED = 40;

typedef unsigned short u16;
typedef unsigned int u32;
typedef unsigned long long u64;
typedef __attribute__((ext_vector_type(8))) __bf16 bf16x8;
typedef __attribute__((ext_vector_type(16))) __bf16 bf16x16;
typedef __attribute__((ext_vector_type(4))) float f32x4;
typedef __attribute__((ext_vector_type(4))) unsigned int u32x4;
typedef __attribute__((ext_vector_type(8))) unsigned int u32x8;
typedef __attribute__((ext_vector_type(2))) unsigned int u32x2;

__device__ __forceinline__ float bf2f(u16 h) { return __uint_as_float(((u32)h) << 16); }
typedef __bf16 bf16x2_t __attribute__((ext_vector_type(2)));
typedef float f32x2_t __attribute__((ext_vector_type(2)));
__device__ __forceinline__ u16 f2bf(float f) {
    __bf16 h = (__bf16)f;
    return __builtin_bit_cast(u16, h);
}
__device__ __forceinline__ float bfr(float f) { return (float)(__bf16)f; }
__device__ __forceinline__ u32 pack2(float a, float b) {
    f32x2_t f = {a, b};
    bf16x2_t h = __builtin_convertvector(f, bf16x2_t);
    return __builtin_bit_cast(u32, h);
}
__device__ __forceinline__ bf16x8 as_bf(u32x4 v) { return __builtin_bit_cast(bf16x8, v); }
__device__ __forceinline__ f32x4 mfma16(u32x4 a, u32x4 b, f32x4 c) {
    return __builtin_amdgcn_mfma_f32_16x16x32_bf16(as_bf(a), as_bf(b), c, 0, 0, 0);
}
__device__ __forceinline__ f32x4 smf(u32x4 a, u32x4 b0, u32x4 b1, f32x4 c, int idx) {
    u32x8 b = {b0[0], b0[1], b0[2], b0[3], b1[0], b1[1], b1[2], b1[3]};
    return __builtin_amdgcn_smfmac_f32_16x16x64_bf16(as_bf(a), __builtin_bit_cast(bf16x16, b), c, idx, 0, 0);
}
__device__ __forceinline__ int dmap(int p) {
    int q = p & 15;
    return (p & ~15) | (q & 1) | ((q & 2) << 2) | ((q & 4) >> 1) | ((q & 8) >> 1);
}

constexpr size_t al256(size_t x) { return (x + 255) & ~(size_t)255; }
constexpr size_t OFF_XN = 0;
constexpr size_t OFF_XNP = OFF_XN + al256((size_t)S_ * D_ * 2);
constexpr size_t OFF_R = OFF_XNP + al256((size_t)S_ * D_ * 2);
constexpr size_t OFF_PX = OFF_R + al256((size_t)S_ * 4);
constexpr size_t OFF_PXM = OFF_PX + al256((size_t)NSEG * H_ * 2048);
constexpr size_t OFF_HB = OFF_PXM + al256((size_t)NSEG * 8 * 2048);
constexpr size_t OFF_UPD = OFF_HB + al256((size_t)8 * FF_ * 2);
constexpr size_t OFF_GEN = OFF_UPD + al256((size_t)8 * D_ * 2);
constexpr size_t OFF_CLAIM = OFF_GEN + 256;
constexpr size_t OFF_INI = OFF_CLAIM + 16384;
constexpr size_t OFF_ERR = OFF_INI + 2048;
constexpr size_t OFF_FA = OFF_ERR + 256;
constexpr size_t OFF_FB = OFF_FA + 4096;
constexpr size_t OFF_FP = OFF_FB + 4096;
constexpr size_t OFF_FZ = OFF_FP + 1024;
constexpr size_t OFF_RDY = OFF_FZ + 2048;
constexpr size_t OFF_IDX = OFF_RDY + 1024;
constexpr size_t OFF_CNT = OFF_IDX + 256;
constexpr size_t ZERO_BEGIN = OFF_ERR, ZERO_END = OFF_CNT + 1024;
constexpr size_t OFF_WIN = (size_t)12 << 20;
constexpr size_t WS_TOTAL = OFF_WIN + ((size_t)188 << 20);
static_assert(ZERO_END <= OFF_WIN && WS_TOTAL <= ((size_t)256 << 20), "ws");

constexpr u32 SLOT0 = 52u << 20;
constexpr u32 SL_P = 4 * 32768, SL_TS = 2 * 32768 + 1024;
constexpr u32 FAR_BX = 1u << 20;
constexpr u32 NEAR_QX = 59u << 20;
constexpr u32 NEAR_KX = NEAR_QX + NSEG * H_ * 8192;
static_assert(2 * ((size_t)NEAR_KX + (size_t)NSEG * H_ * 8192) + ((size_t)2 << 20) <= WS_TOTAL - OFF_WIN && SLOT0 + (64u << 20) <= 2 * NEAR_QX
              && 2 * ((size_t)FAR_BX + (size_t)NSEG * NRED * 32768) + ((size_t)2 << 20) <= WS_TOTAL - OFF_WIN, "far");
__device__ __forceinline__ u32 slot_off(u32 wp, int s) {
    u32 r, t;
    asm("s_lshr_b32 %0, %3, 1\n\ts_xor_b32 %0, %0, %2\n\ts_and_b32 %0, %0, 1\n\ts_lshl_b32 %0, %0, 13\n\ts_lshl_b32 %1, %3, 20\n\t"
        "s_add_u32 %0, %0, %1\n\ts_andn2_b32 %1, %2, 15\n\ts_add_u32 %0, %0, %1\n\ts_add_u32 %0, %0, %4" : "=&s"(r), "=&s"(t) : "s"(wp), "s"(s), "n"(SLOT0) : "scc");
    return r;
}
__device__ __forceinline__ u32 slot_z(u32 wp, u32 s) { return (wp & ~15u) + SLOT0 + (s << 20) + (((wp ^ (s >> 1)) & 1u) << 13); }
__device__ __forceinline__ u32 slot_v(u32 wp, u32 s) { return slot_z(wp, s) + ((((wp >> 3) ^ (s >> 5)) & 1u) << 18); }
__device__ __forceinline__ u32 far_v(u32 wp, u32 L) { return (wp & ~15u) + 2 * L + ((~(wp ^ (L >> 20)) & 1u) << 13); }
__device__ __forceinline__ u32 tab(u32 t, int s) { return __builtin_amdgcn_readlane(t, s); }
__device__ __forceinline__ u32 copy_off(u32 c, u32 s) { return ((c & 1u) << 14) | ((((c >> 1) ^ s) & 1u) << 12); }
__device__ __forceinline__ u32 z_off(u32 wp, u32 ct) {
    const u32 zb = ct >> 1, pg = zb < 16 ? zb + 16 : 6 + (zb & 1u) + (zb & 2u) * 4;
    return pg * 32768 + (((ct ^ (wp >> 1) ^ (pg >> 4)) & 1u) << 14);
}
constexpr int NCP = CFG ? 2 : 4;
__device__ __forceinline__ u32 put_off(u32 wp, u32 x0, u32 s) { return CFG ? copy_off(x0 ^ (wp >> 1), s) & 4096u : 0u; }
__device__ __forceinline__ u32 cpy(u32 c) { return CFG ? (c & 1u) * 16384u : (c & 1u) * 4096u + (c >> 1) * 16384u; }
__device__ __forceinline__ u32 far_off(u32 wp, u32 L) {
    u32 r, t;
    asm("s_lshr_b32 %0, %3, 20\n\ts_xnor_b32 %0, %0, %2\n\ts_and_b32 %0, %0, 1\n\ts_lshl_b32 %0, %0, 13\n\ts_lshl_b32 %1, %3, 1\n\t"
        "s_add_u32 %0, %0, %1\n\ts_andn2_b32 %1, %2, 15\n\ts_add_u32 %0, %0, %1" : "=&s"(r), "=&s"(t) : "s"(wp), "s"(L) : "scc");
    return r;
}
constexpr u32 SENT = 0xffbfffbfu;
constexpr u32 MARK = 0xffbeffbeu;
constexpr u32 OOB = 0x80000000u;
__device__ __forceinline__ u32 amap(u32 L) { return ((L >> 13) << 14) | (L & 8191u); }
__device__ __forceinline__ u32 win_off(const char* ws) {
    u64 a = (u64)(uintptr_t)ws;
    u64 w = (a + OFF_WIN + ((u64)2 << 20) - 1) & ~(((u64)2 << 20) - 1);
    return (u32)(w - a);
}

constexpr int AUX_SC1 = 16;
typedef __amdgpu_buffer_rsrc_t rsrc_t;
__device__ __forceinline__ rsrc_t mk_rsrc(const void* p) {
    return __builtin_amdgcn_make_buffer_rsrc((void*)p, 0, 0x7fffffff, 0x00020000);
}
__device__ __forceinline__ u32x4 ldx4(rsrc_t r, u32 off, u32 so = 0) { return __builtin_amdgcn_raw_buffer_load_b128(r, off, so, AUX_SC1); }
__device__ __forceinline__ u32x2 ldx2(rsrc_t r, u32 off, u32 so = 0) { return __builtin_amdgcn_raw_buffer_load_b64(r, off, so, AUX_SC1); }
__device__ __forceinline__ u32 ldx1(rsrc_t r, u32 off, u32 so = 0) { return __builtin_amdgcn_raw_buffer_load_b32(r, off, so, AUX_SC1); }
__device__ __forceinline__ void stx4(rsrc_t r, u32 off, u32x4 v, u32 so = 0) { __builtin_amdgcn_raw_buffer_store_b128(v, r, off + so, 0, AUX_SC1); }
__device__ __forceinline__ void stx2(rsrc_t r, u32 off, u32x2 v, u32 so = 0) { __builtin_amdgcn_raw_buffer_store_b64(v, r, off, so, AUX_SC1); }
__device__ __forceinline__ void stx1(rsrc_t r, u32 off, u32 v, u32 so = 0) { __builtin_amdgcn_raw_buffer_store_b32(v, r, off, so, AUX_SC1); }
#define DRAIN() asm volatile("s_waitcnt vmcnt(0)" ::: "memory")
#define CFENCE() asm volatile("" ::: "memory")
#define LBAR() asm volatile("s_waitcnt lgkmcnt(0)\n\ts_barrier" ::: "memory")

constexpr u32 SPIN_CAP = 1u << 18;
__device__ __forceinline__ int lane_id() {
    int l;
    asm volatile("v_mbcnt_lo_u32_b32 %0, -1, 0\n\tv_mbcnt_hi_u32_b32 %0, -1, %0" : "=v"(l));
    __builtin_assume(l >= 0 && l < 64);
    return l;
}

__device__ __forceinline__ float xmax16(float x) {
    u32 a = __float_as_uint(x);
    auto r = __builtin_amdgcn_permlane16_swap(a, a, false, false);
    return fmaxf(__uint_as_float(r[0]), __uint_as_float(r[1]));
}
__device__ __forceinline__ float xmax32(float x) {
    u32 a = __float_as_uint(x);
    auto r = __builtin_amdgcn_permlane32_swap(a, a, false, false);
    return fmaxf(__uint_as_float(r[0]), __uint_as_float(r[1]));
}
__device__ __forceinline__ float xadd16(float x) {
    u32 a = __float_as_uint(x);
    auto r = __builtin_amdgcn_permlane16_swap(a, a, false, false);
    return __uint_as_float(r[0]) + __uint_as_float(r[1]);
}
__device__ __forceinline__ float xadd32(float x) {
    u32 a = __float_as_uint(x);
    auto r = __builtin_amdgcn_permlane32_swap(a, a, false, false);
    return __uint_as_float(r[0]) + __uint_as_float(r[1]);
}
__device__ __forceinline__ u32 umax3(u32 a, u32 b, u32 c) { return max(max(a, b), c); }

__device__ __forceinline__ void wait_flags(rsrc_t rs, u32 off, bool act, u32 target, bool& dead, u32* errw, u32 code, int lane) {
    u32 spins = 0;
#pragma unroll 1
    while (!dead) {
        CFENCE();
        u32x4 f = ldx4(rs, off);
        bool ok = !act || ((f[0] >= target) & (f[1] >= target) & (f[2] >= target) & (f[3] >= target));
        if (__all(ok)) break;
        __builtin_amdgcn_s_sleep(2);
        if (++spins > SPIN_CAP) { if (lane == 0) atomicOr(errw, code); dead = true; }
        if ((spins & 63) == 0 && ldx1(rs, (u32)OFF_ERR) != 0u) dead = true;
    }
    CFENCE();
}
__device__ __forceinline__ void wait_word(rsrc_t rs, u32 off, bool act, u32 target, bool& dead, u32* errw, u32 code, int lane) {
    u32 spins = 0;
#pragma unroll 1
    while (!dead) {
        CFENCE();
        u32 f = ldx1(rs, off);
        if (__all(!act || f >= target)) break;
        __builtin_amdgcn_s_sleep(4);
        if (++spins > SPIN_CAP) { if (lane == 0) atomicOr(errw, code); dead = true; }
        if ((spins & 63) == 0 && ldx1(rs, (u32)OFF_ERR) != 0u) dead = true;
    }
    CFENCE();
}

__device__ __forceinline__ u32 cal_tag(u64 tag, u32 v) { return ((u32)tag ^ (u32)(tag >> 32)) + v * 0x9E3779B1u; }
__device__ __forceinline__ void wait_level(rsrc_t rs, u64 tag, int lane, int level) {
#pragma unroll 1
    for (u32 spins = 0; spins < SPIN_CAP; ++spins) {
        CFENCE();
        u32x2 f = ldx2(rs, (u32)(OFF_INI + level * 512 + lane * 8));
        if (__all((f[0] == (u32)tag) & (f[1] == (u32)(tag >> 32)))) break;
        __builtin_amdgcn_s_sleep(4);
    }
    CFENCE();
}
__device__ __forceinline__ u32 to_sgpr(u32 x) {
    u32 r;
    asm volatile("s_nop 0\n\tv_readfirstlane_b32 %0, %1\n\ts_nop 4" : "=s"(r) : "v"(x));
    return r;
}
__device__ __forceinline__ u32 wp_of(const char* ws, u32 v) {
    v += __builtin_amdgcn_update_dpp(0u, v, 0xb1, 0xf, 0xf, false);
    v += __builtin_amdgcn_update_dpp(0u, v, 0x4e, 0xf, 0xf, false);
    v += __builtin_amdgcn_update_dpp(0u, v, 0x124, 0xf, 0xf, false);
    v += __builtin_amdgcn_update_dpp(0u, v, 0x128, 0xf, 0xf, false);
    const u32 r = __builtin_amdgcn_readlane(v, 48) + 0x6f6f6f6fu;
    return to_sgpr(win_off(ws) | ((r >> 7) & 1u) | ((r >> 14) & 2u) | ((r >> 21) & 4u) | ((r >> 28) & 8u));
}
__device__ __forceinline__ u32 wait_init(rsrc_t rs, const char* ws, u64 tag, int lane) {
    u32x2 c = {0u, 0u};
#pragma unroll 1
    for (u32 spins = 0; spins < SPIN_CAP; ++spins) {
        CFENCE();
        c = ldx2(rs, (u32)(OFF_INI + lane * 8));
        if (__all(c[1] == cal_tag(tag, c[0]))) break;
        __builtin_amdgcn_s_sleep(4);
    }
    return wp_of(ws, c[0]);
}

#define STAMPS() do { } while (0)
#define STAMP(k) do { } while (0)
#define RSTAMP(k) do { } while (0)
#define STAMP2(k, r) do { } while (0)
#define FLUSH() do { } while (0)
#define WSTAMP(id) do { } while (0)
#define CSTAMP(id) do { } while (0)
constexpr u32 INI_ZERO_CHUNKS = (u32)((ZERO_END - ZERO_BEGIN) / 16);
constexpr u32 INI_CHUNKS = INI_ZERO_CHUNKS + 65536;
constexpr int INI_PER = (INI_CHUNKS + 48 * 384 - 1) / (48 * 384);

__device__ __forceinline__ float rsum16(float x) {
    x += __uint_as_float(__builtin_amdgcn_update_dpp(0u, __float_as_uint(x), 0xb1, 0xf, 0xf, false));
    x += __uint_as_float(__builtin_amdgcn_update_dpp(0u, __float_as_uint(x), 0x4e, 0xf, 0xf, false));
    x += __uint_as_float(__builtin_amdgcn_update_dpp(0u, __float_as_uint(x), 0x124, 0xf, 0xf, false));
    x += __uint_as_float(__builtin_amdgcn_update_dpp(0u, __float_as_uint(x), 0x128, 0xf, 0xf, false));
    return x;
}

__device__ __forceinline__ void ln_regs(u32x4 (&xv)[5]) {
    float sum = 0.f;
#pragma unroll
    for (int i = 0; i < 5; ++i)
#pragma unroll
        for (int j = 0; j < 4; ++j) sum += __uint_as_float(xv[i][j] << 16) + __uint_as_float(xv[i][j] & 0xffff0000u);
    const float mean = rsum16(sum) * (1.0f / D_);
    float vs = 0.f;
#pragma unroll
    for (int i = 0; i < 5; ++i)
#pragma unroll
        for (int j = 0; j < 4; ++j) {
            float d0 = __uint_as_float(xv[i][j] << 16) - mean, d1 = __uint_as_float(xv[i][j] & 0xffff0000u) - mean;
            vs += d0 * d0 + d1 * d1;
        }
    const float var = bfr(rsum16(vs) * (1.0f / D_));
    const float m_bf = bfr(mean);
    const float rden = 1.0f / bfr(sqrtf(bfr(var + 1e-05f)));
#pragma unroll
    for (int i = 0; i < 5; ++i)
#pragma unroll
        for (int j = 0; j < 4; ++j)
            xv[i][j] = pack2(bfr(__uint_as_float(xv[i][j] << 16) - m_bf) * rden, bfr(__uint_as_float(xv[i][j] & 0xffff0000u) - m_bf) * rden);
}
__device__ __forceinline__ u32x4 ln_perm(const u32x4 o, const int n) {
    const u32 s0 = (n & 1) ? o[0] : o[2], s1 = (n & 1) ? o[1] : o[3];
    const u32 t0 = __builtin_amdgcn_update_dpp(0u, s0, 0xb1, 0xf, 0xf, false), t1 = __builtin_amdgcn_update_dpp(0u, s1, 0xb1, 0xf, 0xf, false);
    return (n & 1) ? (u32x4){t0, o[2], t1, o[3]} : (u32x4){o[0], t0, o[1], t1};
}
__device__ __forceinline__ u32 img_off(const int row, const int n, const int i) {
    return (u32)((row >> 4) * 20480 + (4 * i + (n >> 2)) * 1024 + (n & 3) * 256 + (row & 15) * 16);
}
__device__ __forceinline__ void ln_load(const int row0, const int lane, const u16* __restrict__ x, const u16* __restrict__ w_r, u32x4 (&xv)[5], u32x4 (&wv)[5]) {
    const int n = lane & 15, row = row0 + (lane >> 4);
#pragma unroll
    for (int i = 0; i < 5; ++i) xv[i] = *(const u32x4*)(x + (size_t)row * D_ + (n + 16 * i) * 8);
#pragma unroll
    for (int i = 0; i < 5; ++i) wv[i] = *(const u32x4*)(w_r + (n + 16 * i) * 8);
}
__device__ __forceinline__ void ln_store(const int row0, const int lane, const u32x4 (&xv)[5], const u32x4 (&wv)[5], rsrc_t rs) {
    const int n = lane & 15, row = row0 + (lane >> 4);
#pragma unroll
    for (int i = 0; i < 5; ++i) stx4(rs, (u32)OFF_XNP + img_off(row, n, i), ln_perm(xv[i], n));
    CFENCE();
    float dot = 0.f;
#pragma unroll
    for (int i = 0; i < 5; ++i) {
        stx4(rs, (u32)OFF_XN + (u32)(row * D_ + (n + 16 * i) * 8) * 2, xv[i]);
#pragma unroll
        for (int j = 0; j < 4; ++j)
            dot += __uint_as_float(xv[i][j] << 16) * __uint_as_float(wv[i][j] << 16) + __uint_as_float(xv[i][j] & 0xffff0000u) * __uint_as_float(wv[i][j] & 0xffff0000u);
    }
    dot = rsum16(dot);
    stx1(rs, n == 0 ? (u32)(OFF_R + row * 4) : OOB, __float_as_uint(bfr(dot)));
}
__device__ __forceinline__ u32x4 mk_srd(const char* ws) {
    const u64 a = (u64)(uintptr_t)ws;
    return (u32x4){(u32)a, (u32)(a >> 32), 0x7fffffffu, 0x00020000u};
}

__device__ __forceinline__ u32 probe(const int pi, const u32 xcc, const u32 hl, char* __restrict__ ws) {
    const u32x4 srd = mk_srd(ws);
    const u32 base = win_off(ws) + SLOT0 + (4u << 20) + 6u * 32768u + (u32)pi * 128u;
    const int lane = lane_id();
    const u32 zero = 0;
    asm volatile("buffer_atomic_or %0, %1, %2, %3 offen sc1\n\ts_waitcnt vmcnt(0)" :: "v"(zero), "v"(lane < 16 ? (u32)lane * 2048u : OOB), "s"(srd), "s"(base) : "memory");
    const u32 one = lane == 0 ? 0u : OOB;
    auto lat = [&](u32 q) {
        u64 t0, t1, t2;
        asm volatile("s_memtime %0\n\tbuffer_atomic_or %3, %4, %5, %6 offen sc1\n\ts_waitcnt vmcnt(0)\n\ts_memtime %1\n\t"
                     "buffer_atomic_or %3, %4, %5, %6 offen sc1\n\ts_waitcnt vmcnt(0)\n\ts_memtime %2\n\ts_waitcnt lgkmcnt(0)"
                     : "=&s"(t0), "=&s"(t1), "=&s"(t2) : "v"(zero), "v"(one), "s"(srd), "s"(base + q * 2048u) : "memory");
        return min((u32)t1 - (u32)t0, (u32)t2 - (u32)t1);
    };
    u32 q = (u32)pi * 5u & 15u, best = lat(q), votes = 0;
    const u32 flip = (xcc >> 2) | ((xcc & 3u) << 1) | (hl << 3);
#pragma unroll 1
    for (u32 i = 0; i < 4; ++i) {
        const u32 bit = (0x1284u >> (4 * i)) & 15u;
        const u32 t = lat(q ^ bit);
        u32 v = 1;
        if (t + 6u < best || best + 6u < t) v = (((q & bit) != 0) ^ (t < best) ^ ((flip >> i) & 1u)) * 2u;
        if (i && xcc >= 4) v = 1;
        if (t < best) { best = t; q ^= bit; }
        votes |= v << (8 * i);
    }
    return votes;
}

typedef __attribute__((ext_vector_type(32))) unsigned int u32x32;
__device__ __forceinline__ u32x4 wfrag(const u32x32 (&W)[5], const int i) {
    return (u32x4){W[i >> 3][(i & 7) * 4], W[i >> 3][(i & 7) * 4 + 1], W[i >> 3][(i & 7) * 4 + 2], W[i >> 3][(i & 7) * 4 + 3]};
}
#define PIN_W(W) asm volatile("" : "+{v[16:47]}"(W[0]), "+{v[48:79]}"(W[1]), "+{v[80:111]}"(W[2]), "+{v[112:143]}"(W[3]), "+{v[144:175]}"(W[4]))
typedef __attribute__((ext_vector_type(4))) short s16x4;
typedef __attribute__((address_space(3))) s16x4* lds_s16x4p;
__device__ __forceinline__ u32x2 trd(u32 ldsaddr) {
    return __builtin_bit_cast(u32x2, __builtin_amdgcn_ds_read_tr16_b64_v4i16((lds_s16x4p)ldsaddr));
}
template <bool NT>
__device__ __forceinline__ void dma16(const void* g, u32 lds_base) {
    u32 sv;
    if (NT)
        asm volatile("s_mov_b32 %0, m0\n\ts_mov_b32 m0, %2\n\ts_nop 0\n\tglobal_load_lds_dwordx4 %1, off nt\n\ts_mov_b32 m0, %0"
                     : "=&s"(sv) : "v"(g), "s"(lds_base) : "memory");
    else
        asm volatile("s_mov_b32 %0, m0\n\ts_mov_b32 m0, %2\n\ts_nop 0\n\tglobal_load_lds_dwordx4 %1, off\n\ts_mov_b32 m0, %0"
                     : "=&s"(sv) : "v"(g), "s"(lds_base) : "memory");
}
__device__ __forceinline__ void dma16_sc1(const void* g, u32 lds_base) {
    u32 sv;
    asm volatile("s_mov_b32 %0, m0\n\ts_mov_b32 m0, %2\n\ts_nop 0\n\tglobal_load_lds_dwordx4 %1, off sc1\n\ts_mov_b32 m0, %0"
                 : "=&s"(sv) : "v"(g), "s"(lds_base) : "memory");
}
template <int ORD, typename F>
__device__ __forceinline__ void load_weights(F&& after, const u16* __restrict__ p0, const u16* __restrict__ p1, const bool wo1, char* smem,
                                             const int wave, const int lane, const int sa, const int ta, const int sb, const int tb,
                                             u32x32 (&W)[5]) {
    const u32 lbase = (u32)(uintptr_t)(__attribute__((address_space(3))) char*)smem;
    const int strm = wave >> 2;
    const bool wo = wo1 && strm == 1;
    const u16* src = strm ? p1 : p0;
    const u32 sstep = wo ? 128u : 128u * HD_;
    u32 so[8];
#pragma unroll
    for (int i = 0; i < 8; ++i) {
        const int j = (wave & 3) * 8 + i, r = 4 * j + (lane >> 4);
        const int f = (lane >> 4) | (((j >> 1) & 1) << 2);
        const int ch = (lane & 15) ^ (f << 1);
        const int e = (r & ~31) | (((r >> 2) & 1) << 4) | (((r >> 3) & 3) << 2) | (r & 3);
        so[i] = wo ? (u32)(e * D_ + ch * 8) : (u32)(((ORD >> 21) & 1 ? e : dmap(r)) * HD_ + ch * 8);
    }
    auto issue = [&](int st) {
        const u32 dst = __builtin_amdgcn_readfirstlane(lbase + (u32)((st & 1) * 65536 + strm * 32768 + (wave & 3) * 8192));
#pragma unroll
        for (int i = 0; i < 8; ++i) dma16<((ORD >> 20) & 1) != 0>(src + so[i] + (size_t)((ORD >> (4 * st)) & 15) * sstep, dst + i * 1024);
    };
    const int g = lane >> 4, q = (lane >> 2) & 3, p = lane & 3;
    const int f = q | ((g & 1) << 2);
    const u32 ra = lbase + (u32)(sa * 32768 + (8 * g + q) * 256 + (((ta ^ f) << 1) | (p >> 1)) * 16 + (p & 1) * 8);
    const u32 rb = lbase + (u32)(sb * 32768 + (8 * g + q) * 256 + (((tb ^ f) << 1) | (p >> 1)) * 16 + (p & 1) * 8);
    issue(0);
    issue(1);
#pragma unroll
    for (int st = 0; st < 5; ++st) {
        if (st < 4) asm volatile("s_waitcnt vmcnt(8)" ::: "memory"); else asm volatile("s_waitcnt vmcnt(0)" ::: "memory");
        LBAR();
#pragma unroll
        for (int k = 0; k < 4; ++k) {
            const u32 o = (u32)((st & 1) * 65536 + k * 8192);
            u32x2 a0 = trd(ra + o), a1 = trd(ra + o + 1024), b0 = trd(rb + o), b1 = trd(rb + o + 1024);
            const int ia = (((ORD >> (4 * st)) & 15) * 4 + k) * 4, ib = 80 + ia;
            if ((ORD >> 21) & 1) {
                u32x32& w = W[(ORD >> (4 * st)) & 15];
                w[k * 8] = a0[0]; w[k * 8 + 1] = b0[0]; w[k * 8 + 2] = a0[1]; w[k * 8 + 3] = b0[1];
                w[k * 8 + 4] = a1[0]; w[k * 8 + 5] = b1[0]; w[k * 8 + 6] = a1[1]; w[k * 8 + 7] = b1[1];
            } else {
                W[ia >> 5][ia & 31] = a0[0]; W[ia >> 5][(ia & 31) + 1] = a0[1]; W[ia >> 5][(ia & 31) + 2] = a1[0]; W[ia >> 5][(ia & 31) + 3] = a1[1];
                W[ib >> 5][ib & 31] = b0[0]; W[ib >> 5][(ib & 31) + 1] = b0[1]; W[ib >> 5][(ib & 31) + 2] = b1[0]; W[ib >> 5][(ib & 31) + 3] = b1[1];
            }
        }
        LBAR();
        after(st + 16);
        if (st + 2 < 5) issue(st + 2);
        after(st);
    }
}

template <typename F>
__device__ __forceinline__ void init_role(F&& settle, const int wave, const int ii, const u32 xcc, const u32 hl, const u16* __restrict__ x, const u16* __restrict__ w_r,
                                          char* __restrict__ ws, char* smem) {
    const int lane = lane_id(), tid = wave * 64 + lane;
    const rsrc_t rs = mk_rsrc(ws);
    u32* cnt = (u32*)(smem + 131072);
    if (tid < 3) cnt[tid] = 0u;
    u64 tag;
    auto arrive = [&](int level, u32 total, u32x2 rec) {
        if (lane == 0 && __hip_atomic_fetch_add(cnt + level, 1u, __ATOMIC_RELAXED, __HIP_MEMORY_SCOPE_WORKGROUP) == total - 1)
            stx2(rs, (u32)(OFF_INI + (level * 64 + ii) * 8), rec);
    };
    u32x4 xv[5], wv[5];
    if (ii >= 48) {
        u32 votes = 0;
        if (wave == 1) votes = probe(ii - 48, xcc, hl, ws);
        tag = settle();
        if (tid == 64) {
            stx2(rs, (u32)(OFF_INI + ii * 8), (u32x2){votes, cal_tag(tag, votes)});
            stx2(rs, (u32)(OFF_INI + (64 + ii) * 8), (u32x2){(u32)tag, (u32)(tag >> 32)});
        }
        const int row = 1536 + (ii - 48) * 32 + wave * 4;
        ln_load(row, lane, x, w_r, xv, wv);
        ln_regs(xv);
        ln_store(row, lane, xv, wv, rs);
    } else if (wave >= 6) {
        const int row = ii * 8 + (wave - 6) * 4;
        ln_load(row, lane, x, w_r, xv, wv);
        ln_regs(xv);
        tag = settle();
        ln_store(row, lane, xv, wv, rs);
        asm volatile("s_waitcnt vmcnt(6)" ::: "memory");
        arrive(1, 2u, (u32x2){(u32)tag, (u32)(tag >> 32)});
    } else {
        tag = settle();
        const u32 win = win_off(ws);
#pragma unroll
        for (int q = 0; q < INI_PER; ++q) {
            const u32 i = ((u32)q * 48 + ii) * 384 + tid;
            const bool z = i < INI_ZERO_CHUNKS;
            const u32 v = z ? 0u : SENT;
            stx4(rs, i < INI_CHUNKS ? (z ? (u32)ZERO_BEGIN + i * 16 : win + SLOT0 + (i - INI_ZERO_CHUNKS) * 16) : OOB, (u32x4){v, v, v, v});
        }
        DRAIN();
        arrive(0, 6u, (u32x2){0u, cal_tag(tag, 0u)});
        const int row = 384 + ii * 24 + wave * 4;
        ln_load(row, lane, x, w_r, xv, wv);
        ln_regs(xv);
        ln_store(row, lane, xv, wv, rs);
    }
    DRAIN();
    arrive(2, 8u, (u32x2){(u32)tag, (u32)(tag >> 32)});
    LBAR();
}

#define STP
#define STA
__device__ __forceinline__ void s_p0(const u32x32 (&W)[5], u32& redo, u32 va, u32 sidx, u32 qw, u32 kw, u32 fv, u32 vm, u32x4 rs, u32 pfo, u32 pfd, u32 tg STP) {
    asm volatile(
        "v_mov_b64 v[248:249], 0\n\t"
        "v_mov_b64 v[250:251], 0\n\t"
        "v_mov_b64 v[252:253], 0\n\t"
        "v_mov_b64 v[254:255], 0\n\t"
        "v_mov_b32 v205, %[fv]\n\t"
        "s_cmp_eq_u32 %[pfd], 0\n\t"
        "s_cbranch_scc1 Lnf%=\n\t"
        "s_mov_b32 s92, m0\n\t"
        "s_mov_b32 m0, %[pfd]\n\t"
        "s_add_u32 s93, %[pfo], 0x1000\n\t"
        "Ld%=:\n\t"
        "s_memrealtime s[90:91]\n\t"
        "s_waitcnt lgkmcnt(0)\n\t"
        "s_mul_i32 s90, s90, 24\n\t"
        "s_sub_u32 s88, %[tg], s90\n\t"
        "s_cmp_lt_u32 s88, 0x4000\n\t"
        "s_cbranch_scc1 Ld%=\n\t"
        "buffer_load_dwordx4 %[vm], %[rs], %[pfo] offen offset:0 sc1 lds\n\t"
        "buffer_load_dwordx4 %[vm], %[rs], %[pfo] offen offset:1024 sc1 lds\n\t"
        "buffer_load_dwordx4 %[vm], %[rs], %[pfo] offen offset:2048 sc1 lds\n\t"
        "buffer_load_dwordx4 %[vm], %[rs], %[pfo] offen offset:3072 sc1 lds\n\t"
        "s_add_u32 m0, m0, 0x1000\n\t"
        "s_nop 0\n\t"
        "buffer_load_dwordx4 %[vm], %[rs], s93 offen offset:0 sc1 lds\n\t"
        "buffer_load_dwordx4 %[vm], %[rs], s93 offen offset:1024 sc1 lds\n\t"
        "buffer_load_dwordx4 %[vm], %[rs], s93 offen offset:2048 sc1 lds\n\t"
        "buffer_load_dwordx4 %[vm], %[rs], s93 offen offset:3072 sc1 lds\n\t"
        "s_mov_b32 m0, s92\n\t"
        "Lnf%=:\n\t"
        "s_setprio 3\n\t"
        "s_barrier\n\t"
        "ds_read_b128 v[208:211], %[va] offset:0\n\t"
        "ds_read_b128 v[212:215], %[va] offset:1024\n\t"
        "ds_read_b128 v[216:219], %[va] offset:2048\n\t"
        "ds_read_b128 v[220:223], %[va] offset:3072\n\t"
        "ds_read_b128 v[224:227], %[va] offset:4096\n\t"
        "ds_read_b128 v[228:231], %[va] offset:5120\n\t"
        "s_waitcnt lgkmcnt(5)\n\t"
        "v_smfmac_f32_16x16x64_bf16 v[248:251], v[208:211], v[16:23], %[sidx]\n\t"
        "ds_read_b128 v[232:235], %[va] offset:6144\n\t"
        "s_waitcnt lgkmcnt(5)\n\t"
        "v_smfmac_f32_16x16x64_bf16 v[248:251], v[212:215], v[24:31], %[sidx]\n\t"
        "ds_read_b128 v[236:239], %[va] offset:7168\n\t"
        "s_waitcnt lgkmcnt(5)\n\t"
        "v_smfmac_f32_16x16x64_bf16 v[248:251], v[216:219], v[32:39], %[sidx]\n\t"
        "ds_read_b128 v[240:243], %[va] offset:8192\n\t"
        "s_waitcnt lgkmcnt(5)\n\t"
        "v_smfmac_f32_16x16x64_bf16 v[248:251], v[220:223], v[40:47], %[sidx]\n\t"
        "ds_read_b128 v[244:247], %[va] offset:10240\n\t"
        "s_waitcnt lgkmcnt(5)\n\t"
        "v_smfmac_f32_16x16x64_bf16 v[248:251], v[224:227], v[48:55], %[sidx]\n\t"
        "s_waitcnt lgkmcnt(4)\n\t"
        "v_smfmac_f32_16x16x64_bf16 v[248:251], v[228:231], v[56:63], %[sidx]\n\t"
        "s_waitcnt lgkmcnt(3)\n\t"
        "v_smfmac_f32_16x16x64_bf16 v[248:251], v[232:235], v[64:71], %[sidx]\n\t"
        "s_waitcnt lgkmcnt(2)\n\t"
        "v_smfmac_f32_16x16x64_bf16 v[248:251], v[236:239], v[72:79], %[sidx]\n\t"
        "s_waitcnt lgkmcnt(1)\n\t"
        "v_smfmac_f32_16x16x64_bf16 v[248:251], v[240:243], v[80:87], %[sidx]\n\t"
        "s_waitcnt lgkmcnt(0)\n\t"
        "v_smfmac_f32_16x16x64_bf16 v[248:251], v[244:247], v[88:95], %[sidx]\n\t"
        "s_setprio 0\n\t"
        "s_nop 6\n\t"
        "v_cvt_pk_bf16_f32 v200, v248, v249\n\t"
        "v_cvt_pk_bf16_f32 v201, v250, v251\n\t"
        "ds_write2_b32 %[qw], v200, v201 offset1:96\n\t"
        "v_fma_f32 v202, v248, v249, v250\n\t"
        "v_cmp_u_f32 vcc, v202, v251\n\t"
        "s_cbranch_vccnz Lnan%=\n\t"
        "Lok%=:\n\t"
        "s_waitcnt lgkmcnt(0)\n\t"
        "s_barrier\n\t"
        "v_smfmac_f32_16x16x64_bf16 v[252:255], v[208:211], v[96:103], %[sidx]\n\t"
        "v_smfmac_f32_16x16x64_bf16 v[252:255], v[212:215], v[104:111], %[sidx]\n\t"
        "v_smfmac_f32_16x16x64_bf16 v[252:255], v[216:219], v[112:119], %[sidx]\n\t"
        "v_smfmac_f32_16x16x64_bf16 v[252:255], v[220:223], v[120:127], %[sidx]\n\t"
        "v_smfmac_f32_16x16x64_bf16 v[252:255], v[224:227], v[128:135], %[sidx]\n\t"
        "v_smfmac_f32_16x16x64_bf16 v[252:255], v[228:231], v[136:143], %[sidx]\n\t"
        "v_smfmac_f32_16x16x64_bf16 v[252:255], v[232:235], v[144:151], %[sidx]\n\t"
        "v_smfmac_f32_16x16x64_bf16 v[252:255], v[236:239], v[152:159], %[sidx]\n\t"
        "v_smfmac_f32_16x16x64_bf16 v[252:255], v[240:243], v[160:167], %[sidx]\n\t"
        "v_smfmac_f32_16x16x64_bf16 v[252:255], v[244:247], v[168:175], %[sidx]\n\t"
        "s_waitcnt vmcnt(0)\n\t"
        "s_nop 6\n\t"
        "v_add_f32 v252, v252, v254\n\t"
        "v_add_f32 v253, v253, v255\n\t"
        "v_cvt_pk_bf16_f32 v204, v252, v253\n\t"
        "ds_write_b64 %[kw], v[204:205]\n\t"
        "s_barrier\n\t"
        "s_branch Lend%=\n\t"
        "Lnan%=:\n\t"
        "v_max_u32 v207, v208, v209\n\t"
        "v_max3_u32 v207, v207, v210, v211\n\t"
        "v_max3_u32 v207, v207, v212, v213\n\t"
        "v_max3_u32 v207, v207, v214, v215\n\t"
        "v_max3_u32 v207, v207, v216, v217\n\t"
        "v_max3_u32 v207, v207, v218, v219\n\t"
        "v_max3_u32 v207, v207, v220, v221\n\t"
        "v_max3_u32 v207, v207, v222, v223\n\t"
        "v_max3_u32 v207, v207, v224, v225\n\t"
        "v_max3_u32 v207, v207, v226, v227\n\t"
        "v_max3_u32 v207, v207, v228, v229\n\t"
        "v_max3_u32 v207, v207, v230, v231\n\t"
        "v_max3_u32 v207, v207, v232, v233\n\t"
        "v_max3_u32 v207, v207, v234, v235\n\t"
        "v_max3_u32 v207, v207, v236, v237\n\t"
        "v_max3_u32 v207, v207, v238, v239\n\t"
        "v_max3_u32 v207, v207, v240, v241\n\t"
        "v_max3_u32 v207, v207, v242, v243\n\t"
        "v_max3_u32 v207, v207, v244, v245\n\t"
        "v_max3_u32 v207, v207, v246, v247\n\t"
        "v_cmp_eq_u32 vcc, 0xffbfffbf, v207\n\t"
        "s_cbranch_vccz Lok%=\n\t"
        "s_mov_b32 %[redo], 1\n\t"
        "s_barrier\n\t"
        "s_branch Lend%=\n\t"
        "Lend%=:\n\t"
        : [redo] "+&s"(redo)
        : [w0] "{v[16:47]}"(W[0]), [w1] "{v[48:79]}"(W[1]), [w2] "{v[80:111]}"(W[2]), [w3] "{v[112:143]}"(W[3]), [w4] "{v[144:175]}"(W[4]), [va] "v"(va), [sidx] "v"(sidx), [qw] "v"(qw), [kw] "v"(kw), [fv] "s"(fv), [vm] "v"(vm), [rs] "s"(rs), [pfo] "s"(pfo), [pfd] "s"(pfd), [tg] "s"(tg)
        : "s88", "s89", "s90", "s91", "s92", "s93", "v200", "v201", "v202", "v203", "v204", "v205", "v206", "v207", "v208", "v209", "v210", "v211", "v212", "v213", "v214", "v215", "v216", "v217", "v218", "v219", "v220", "v221", "v222", "v223", "v224", "v225", "v226", "v227", "v228", "v229", "v230", "v231", "v232", "v233", "v234", "v235", "v236", "v237", "v238", "v239", "v240", "v241", "v242", "v243", "v244", "v245", "v246", "v247", "v248", "v249", "v250", "v251", "v252", "v253", "v254", "v255", "vcc", "scc", "memory");
}

__device__ __forceinline__ void s_p1(const u32x32 (&W)[5], u32& redo, u32 va, u32 sidx, u32 qw, u32 kw, u32 fv, u32x4 p0, u32 p1, u32 o0, u32 o1, u32x4 rs STP) {
    asm volatile(
        "v_mov_b64 v[248:249], 0\n\t"
        "v_mov_b64 v[250:251], 0\n\t"
        "v_mov_b64 v[252:253], 0\n\t"
        "v_mov_b64 v[254:255], 0\n\t"
        "v_mov_b32 v205, %[fv]\n\t"
        "s_setprio 3\n\t"
        "s_waitcnt vmcnt(0)\n\t"
        "s_barrier\n\t"
        "ds_read_b128 v[208:211], %[va] offset:0\n\t"
        "ds_read_b128 v[212:215], %[va] offset:1024\n\t"
        "ds_read_b128 v[216:219], %[va] offset:2048\n\t"
        "ds_read_b128 v[220:223], %[va] offset:3072\n\t"
        "ds_read_b128 v[224:227], %[va] offset:4096\n\t"
        "ds_read_b128 v[228:231], %[va] offset:5120\n\t"
        "s_waitcnt lgkmcnt(5)\n\t"
        "v_smfmac_f32_16x16x64_bf16 v[248:251], v[208:211], v[16:23], %[sidx]\n\t"
        "ds_read_b128 v[232:235], %[va] offset:6144\n\t"
        "s_waitcnt lgkmcnt(5)\n\t"
        "v_smfmac_f32_16x16x64_bf16 v[248:251], v[212:215], v[24:31], %[sidx]\n\t"
        "ds_read_b128 v[236:239], %[va] offset:7168\n\t"
        "s_waitcnt lgkmcnt(5)\n\t"
        "v_smfmac_f32_16x16x64_bf16 v[248:251], v[216:219], v[32:39], %[sidx]\n\t"
        "ds_read_b128 v[240:243], %[va] offset:8192\n\t"
        "s_waitcnt lgkmcnt(5)\n\t"
        "v_smfmac_f32_16x16x64_bf16 v[248:251], v[220:223], v[40:47], %[sidx]\n\t"
        "ds_read_b128 v[244:247], %[va] offset:10240\n\t"
        "s_waitcnt lgkmcnt(5)\n\t"
        "v_smfmac_f32_16x16x64_bf16 v[248:251], v[224:227], v[48:55], %[sidx]\n\t"
        "s_waitcnt lgkmcnt(4)\n\t"
        "v_smfmac_f32_16x16x64_bf16 v[248:251], v[228:231], v[56:63], %[sidx]\n\t"
        "s_waitcnt lgkmcnt(3)\n\t"
        "v_smfmac_f32_16x16x64_bf16 v[248:251], v[232:235], v[64:71], %[sidx]\n\t"
        "s_waitcnt lgkmcnt(2)\n\t"
        "v_smfmac_f32_16x16x64_bf16 v[248:251], v[236:239], v[72:79], %[sidx]\n\t"
        "s_waitcnt lgkmcnt(1)\n\t"
        "v_smfmac_f32_16x16x64_bf16 v[248:251], v[240:243], v[80:87], %[sidx]\n\t"
        "s_waitcnt lgkmcnt(0)\n\t"
        "v_smfmac_f32_16x16x64_bf16 v[248:251], v[244:247], v[88:95], %[sidx]\n\t"
        "s_setprio 0\n\t"
        "s_nop 6\n\t"
        "v_cvt_pk_bf16_f32 v200, v248, v249\n\t"
        "v_cvt_pk_bf16_f32 v201, v250, v251\n\t"
        "ds_write2_b32 %[qw], v200, v201 offset1:96\n\t"
        "v_fma_f32 v202, v248, v249, v250\n\t"
        "v_cmp_u_f32 vcc, v202, v251\n\t"
        "buffer_store_dwordx4 v[192:195], %[o0], %[rs], 0 offen sc1\n\t"
        "buffer_store_dword v196, %[o1], %[rs], 0 offen sc1\n\t"
        "s_cbranch_vccnz Lnan%=\n\t"
        "Lok%=:\n\t"
        "s_waitcnt lgkmcnt(0)\n\t"
        "s_barrier\n\t"
        "s_nop 15\n\t"
        "s_nop 15\n\t"
        "s_nop 15\n\t"
        "s_nop 3\n\t"
        "v_smfmac_f32_16x16x64_bf16 v[252:255], v[208:211], v[96:103], %[sidx]\n\t"
        "v_smfmac_f32_16x16x64_bf16 v[252:255], v[212:215], v[104:111], %[sidx]\n\t"
        "v_smfmac_f32_16x16x64_bf16 v[252:255], v[216:219], v[112:119], %[sidx]\n\t"
        "v_smfmac_f32_16x16x64_bf16 v[252:255], v[220:223], v[120:127], %[sidx]\n\t"
        "v_smfmac_f32_16x16x64_bf16 v[252:255], v[224:227], v[128:135], %[sidx]\n\t"
        "v_smfmac_f32_16x16x64_bf16 v[252:255], v[228:231], v[136:143], %[sidx]\n\t"
        "v_smfmac_f32_16x16x64_bf16 v[252:255], v[232:235], v[144:151], %[sidx]\n\t"
        "v_smfmac_f32_16x16x64_bf16 v[252:255], v[236:239], v[152:159], %[sidx]\n\t"
        "v_smfmac_f32_16x16x64_bf16 v[252:255], v[240:243], v[160:167], %[sidx]\n\t"
        "v_smfmac_f32_16x16x64_bf16 v[252:255], v[244:247], v[168:175], %[sidx]\n\t"
        "s_nop 7\n\t"
        "v_add_f32 v252, v252, v254\n\t"
        "v_add_f32 v253, v253, v255\n\t"
        "v_cvt_pk_bf16_f32 v204, v252, v253\n\t"
        "ds_write_b64 %[kw], v[204:205]\n\t"
        "s_barrier\n\t"
        "s_branch Lend%=\n\t"
        "Lnan%=:\n\t"
        "v_max_u32 v207, v208, v209\n\t"
        "v_max3_u32 v207, v207, v210, v211\n\t"
        "v_max3_u32 v207, v207, v212, v213\n\t"
        "v_max3_u32 v207, v207, v214, v215\n\t"
        "v_max3_u32 v207, v207, v216, v217\n\t"
        "v_max3_u32 v207, v207, v218, v219\n\t"
        "v_max3_u32 v207, v207, v220, v221\n\t"
        "v_max3_u32 v207, v207, v222, v223\n\t"
        "v_max3_u32 v207, v207, v224, v225\n\t"
        "v_max3_u32 v207, v207, v226, v227\n\t"
        "v_max3_u32 v207, v207, v228, v229\n\t"
        "v_max3_u32 v207, v207, v230, v231\n\t"
        "v_max3_u32 v207, v207, v232, v233\n\t"
        "v_max3_u32 v207, v207, v234, v235\n\t"
        "v_max3_u32 v207, v207, v236, v237\n\t"
        "v_max3_u32 v207, v207, v238, v239\n\t"
        "v_max3_u32 v207, v207, v240, v241\n\t"
        "v_max3_u32 v207, v207, v242, v243\n\t"
        "v_max3_u32 v207, v207, v244, v245\n\t"
        "v_max3_u32 v207, v207, v246, v247\n\t"
        "v_cmp_eq_u32 vcc, 0xffbfffbf, v207\n\t"
        "s_cbranch_vccz Lok%=\n\t"
        "s_mov_b32 %[redo], 1\n\t"
        "s_barrier\n\t"
        "s_branch Lend%=\n\t"
        "Lend%=:\n\t"
        : [redo] "+&s"(redo)
        : [w0] "{v[16:47]}"(W[0]), [w1] "{v[48:79]}"(W[1]), [w2] "{v[80:111]}"(W[2]), [w3] "{v[112:143]}"(W[3]), [w4] "{v[144:175]}"(W[4]), [va] "v"(va), [sidx] "v"(sidx), [qw] "v"(qw), [kw] "v"(kw), [fv] "s"(fv), [p0] "{v[192:195]}"(p0), [p1] "{v196}"(p1), [o0] "v"(o0), [o1] "v"(o1), [rs] "s"(rs)
        : "v200", "v201", "v202", "v203", "v204", "v205", "v206", "v207", "v208", "v209", "v210", "v211", "v212", "v213", "v214", "v215", "v216", "v217", "v218", "v219", "v220", "v221", "v222", "v223", "v224", "v225", "v226", "v227", "v228", "v229", "v230", "v231", "v232", "v233", "v234", "v235", "v236", "v237", "v238", "v239", "v240", "v241", "v242", "v243", "v244", "v245", "v246", "v247", "v248", "v249", "v250", "v251", "v252", "v253", "v254", "v255", "vcc", "scc", "memory");
}

__device__ __forceinline__ void s_p2(const u32x32 (&W)[5], u32& redo, u32 va, u32 sidx, u32 qw, u32 kw, u32 fv, u32 vm, u32x4 rs, u32 pfo, u32 pfd STP) {
    asm volatile(
        "v_mov_b64 v[248:249], 0\n\t"
        "v_mov_b64 v[250:251], 0\n\t"
        "v_mov_b64 v[252:253], 0\n\t"
        "v_mov_b64 v[254:255], 0\n\t"
        "v_mov_b32 v205, %[fv]\n\t"
        "s_setprio 3\n\t"
        "s_waitcnt vmcnt(0)\n\t"
        "s_barrier\n\t"
        "ds_read_b128 v[208:211], %[va] offset:0\n\t"
        "ds_read_b128 v[212:215], %[va] offset:1024\n\t"
        "ds_read_b128 v[216:219], %[va] offset:2048\n\t"
        "ds_read_b128 v[220:223], %[va] offset:3072\n\t"
        "ds_read_b128 v[224:227], %[va] offset:4096\n\t"
        "ds_read_b128 v[228:231], %[va] offset:5120\n\t"
        "s_waitcnt lgkmcnt(5)\n\t"
        "v_smfmac_f32_16x16x64_bf16 v[248:251], v[208:211], v[16:23], %[sidx]\n\t"
        "ds_read_b128 v[232:235], %[va] offset:6144\n\t"
        "s_waitcnt lgkmcnt(5)\n\t"
        "v_smfmac_f32_16x16x64_bf16 v[248:251], v[212:215], v[24:31], %[sidx]\n\t"
        "ds_read_b128 v[236:239], %[va] offset:7168\n\t"
        "s_waitcnt lgkmcnt(5)\n\t"
        "v_smfmac_f32_16x16x64_bf16 v[248:251], v[216:219], v[32:39], %[sidx]\n\t"
        "ds_read_b128 v[240:243], %[va] offset:8192\n\t"
        "s_waitcnt lgkmcnt(5)\n\t"
        "v_smfmac_f32_16x16x64_bf16 v[248:251], v[220:223], v[40:47], %[sidx]\n\t"
        "ds_read_b128 v[244:247], %[va] offset:10240\n\t"
        "s_waitcnt lgkmcnt(5)\n\t"
        "v_smfmac_f32_16x16x64_bf16 v[248:251], v[224:227], v[48:55], %[sidx]\n\t"
        "s_waitcnt lgkmcnt(4)\n\t"
        "v_smfmac_f32_16x16x64_bf16 v[248:251], v[228:231], v[56:63], %[sidx]\n\t"
        "s_waitcnt lgkmcnt(3)\n\t"
        "v_smfmac_f32_16x16x64_bf16 v[248:251], v[232:235], v[64:71], %[sidx]\n\t"
        "s_waitcnt lgkmcnt(2)\n\t"
        "v_smfmac_f32_16x16x64_bf16 v[248:251], v[236:239], v[72:79], %[sidx]\n\t"
        "s_waitcnt lgkmcnt(1)\n\t"
        "v_smfmac_f32_16x16x64_bf16 v[248:251], v[240:243], v[80:87], %[sidx]\n\t"
        "s_waitcnt lgkmcnt(0)\n\t"
        "v_smfmac_f32_16x16x64_bf16 v[248:251], v[244:247], v[88:95], %[sidx]\n\t"
        "s_setprio 0\n\t"
        "s_nop 6\n\t"
        "v_cvt_pk_bf16_f32 v200, v248, v249\n\t"
        "v_cvt_pk_bf16_f32 v201, v250, v251\n\t"
        "ds_write2_b32 %[qw], v200, v201 offset1:96\n\t"
        "v_fma_f32 v202, v248, v249, v250\n\t"
        "v_cmp_u_f32 vcc, v202, v251\n\t"
        "s_cmp_eq_u32 %[pfd], 0\n\t"
        "s_cbranch_scc1 Lnf%=\n\t"
        "s_mov_b32 s92, m0\n\t"
        "s_mov_b32 m0, %[pfd]\n\t"
        "s_add_u32 s93, %[pfo], 0x1000\n\t"
        "buffer_load_dwordx4 %[vm], %[rs], %[pfo] offen offset:0 sc1 lds\n\t"
        "buffer_load_dwordx4 %[vm], %[rs], %[pfo] offen offset:1024 sc1 lds\n\t"
        "buffer_load_dwordx4 %[vm], %[rs], %[pfo] offen offset:2048 sc1 lds\n\t"
        "buffer_load_dwordx4 %[vm], %[rs], %[pfo] offen offset:3072 sc1 lds\n\t"
        "s_add_u32 m0, m0, 0x1000\n\t"
        "s_nop 0\n\t"
        "buffer_load_dwordx4 %[vm], %[rs], s93 offen offset:0 sc1 lds\n\t"
        "buffer_load_dwordx4 %[vm], %[rs], s93 offen offset:1024 sc1 lds\n\t"
        "buffer_load_dwordx4 %[vm], %[rs], s93 offen offset:2048 sc1 lds\n\t"
        "buffer_load_dwordx4 %[vm], %[rs], s93 offen offset:3072 sc1 lds\n\t"
        "s_mov_b32 m0, s92\n\t"
        "Lnf%=:\n\t"
        "s_cbranch_vccnz Lnan%=\n\t"
        "Lok%=:\n\t"
        "s_waitcnt lgkmcnt(0)\n\t"
        "s_barrier\n\t"
        "v_smfmac_f32_16x16x64_bf16 v[252:255], v[208:211], v[96:103], %[sidx]\n\t"
        "v_smfmac_f32_16x16x64_bf16 v[252:255], v[212:215], v[104:111], %[sidx]\n\t"
        "v_smfmac_f32_16x16x64_bf16 v[252:255], v[216:219], v[112:119], %[sidx]\n\t"
        "v_smfmac_f32_16x16x64_bf16 v[252:255], v[220:223], v[120:127], %[sidx]\n\t"
        "v_smfmac_f32_16x16x64_bf16 v[252:255], v[224:227], v[128:135], %[sidx]\n\t"
        "v_smfmac_f32_16x16x64_bf16 v[252:255], v[228:231], v[136:143], %[sidx]\n\t"
        "v_smfmac_f32_16x16x64_bf16 v[252:255], v[232:235], v[144:151], %[sidx]\n\t"
        "v_smfmac_f32_16x16x64_bf16 v[252:255], v[236:239], v[152:159], %[sidx]\n\t"
        "v_smfmac_f32_16x16x64_bf16 v[252:255], v[240:243], v[160:167], %[sidx]\n\t"
        "v_smfmac_f32_16x16x64_bf16 v[252:255], v[244:247], v[168:175], %[sidx]\n\t"
        "s_nop 7\n\t"
        "v_add_f32 v252, v252, v254\n\t"
        "v_add_f32 v253, v253, v255\n\t"
        "v_cvt_pk_bf16_f32 v204, v252, v253\n\t"
        "ds_write_b64 %[kw], v[204:205]\n\t"
        "s_barrier\n\t"
        "s_branch Lend%=\n\t"
        "Lnan%=:\n\t"
        "v_max_u32 v207, v208, v209\n\t"
        "v_max3_u32 v207, v207, v210, v211\n\t"
        "v_max3_u32 v207, v207, v212, v213\n\t"
        "v_max3_u32 v207, v207, v214, v215\n\t"
        "v_max3_u32 v207, v207, v216, v217\n\t"
        "v_max3_u32 v207, v207, v218, v219\n\t"
        "v_max3_u32 v207, v207, v220, v221\n\t"
        "v_max3_u32 v207, v207, v222, v223\n\t"
        "v_max3_u32 v207, v207, v224, v225\n\t"
        "v_max3_u32 v207, v207, v226, v227\n\t"
        "v_max3_u32 v207, v207, v228, v229\n\t"
        "v_max3_u32 v207, v207, v230, v231\n\t"
        "v_max3_u32 v207, v207, v232, v233\n\t"
        "v_max3_u32 v207, v207, v234, v235\n\t"
        "v_max3_u32 v207, v207, v236, v237\n\t"
        "v_max3_u32 v207, v207, v238, v239\n\t"
        "v_max3_u32 v207, v207, v240, v241\n\t"
        "v_max3_u32 v207, v207, v242, v243\n\t"
        "v_max3_u32 v207, v207, v244, v245\n\t"
        "v_max3_u32 v207, v207, v246, v247\n\t"
        "v_cmp_eq_u32 vcc, 0xffbfffbf, v207\n\t"
        "s_cbranch_vccz Lok%=\n\t"
        "s_mov_b32 %[redo], 1\n\t"
        "s_barrier\n\t"
        "s_branch Lend%=\n\t"
        "Lend%=:\n\t"
        : [redo] "+&s"(redo)
        : [w0] "{v[16:47]}"(W[0]), [w1] "{v[48:79]}"(W[1]), [w2] "{v[80:111]}"(W[2]), [w3] "{v[112:143]}"(W[3]), [w4] "{v[144:175]}"(W[4]), [va] "v"(va), [sidx] "v"(sidx), [qw] "v"(qw), [kw] "v"(kw), [fv] "s"(fv), [vm] "v"(vm), [rs] "s"(rs), [pfo] "s"(pfo), [pfd] "s"(pfd)
        : "s92", "s93", "v200", "v201", "v202", "v203", "v204", "v205", "v206", "v207", "v208", "v209", "v210", "v211", "v212", "v213", "v214", "v215", "v216", "v217", "v218", "v219", "v220", "v221", "v222", "v223", "v224", "v225", "v226", "v227", "v228", "v229", "v230", "v231", "v232", "v233", "v234", "v235", "v236", "v237", "v238", "v239", "v240", "v241", "v242", "v243", "v244", "v245", "v246", "v247", "v248", "v249", "v250", "v251", "v252", "v253", "v254", "v255", "vcc", "scc", "memory");
}

__device__ __forceinline__ void s_e(const u32x32 (&W)[5], u32& redo, u32 va, u32 sidx, u32 qw, u32 kw, u32 fv, u32 kxa, u32 qtr, u32 eo, u32 ew, float lim, u32x4 rs, u32 so STP) {
    asm volatile(
        "v_mov_b64 v[248:249], 0\n\t"
        "v_mov_b64 v[250:251], 0\n\t"
        "v_mov_b64 v[252:253], 0\n\t"
        "v_mov_b64 v[254:255], 0\n\t"
        "v_mov_b32 v205, %[fv]\n\t"
        "ds_read_b128 v[176:179], %[kxa] offset:0\n\t"
        "ds_read_b128 v[180:183], %[kxa] offset:1024\n\t"
        "ds_read_b128 v[184:187], %[kxa] offset:2048\n\t"
        "ds_read_b128 v[188:191], %[kxa] offset:3072\n\t"
        "s_add_u32 s92, %[so], 0x1000\n\t"
        "s_add_u32 s93, %[so], 0x4000\n\t"
        "s_add_u32 s94, %[so], 0x5000\n\t"
        "s_setprio 2\n\t"
        "s_waitcnt lgkmcnt(0)\n\t"
        "s_barrier\n\t"
        "s_nop 11\n\t"
        "ds_read_b128 v[208:211], %[va] offset:0\n\t"
        "ds_read_b128 v[212:215], %[va] offset:1024\n\t"
        "ds_read_b128 v[216:219], %[va] offset:2048\n\t"
        "ds_read_b128 v[220:223], %[va] offset:3072\n\t"
        "ds_read_b128 v[224:227], %[va] offset:4096\n\t"
        "ds_read_b128 v[228:231], %[va] offset:5120\n\t"
        "s_waitcnt lgkmcnt(5)\n\t"
        "v_smfmac_f32_16x16x64_bf16 v[248:251], v[208:211], v[16:23], %[sidx]\n\t"
        "ds_read_b128 v[232:235], %[va] offset:6144\n\t"
        "s_waitcnt lgkmcnt(5)\n\t"
        "v_smfmac_f32_16x16x64_bf16 v[248:251], v[212:215], v[24:31], %[sidx]\n\t"
        "ds_read_b128 v[236:239], %[va] offset:7168\n\t"
        "s_waitcnt lgkmcnt(5)\n\t"
        "v_smfmac_f32_16x16x64_bf16 v[248:251], v[216:219], v[32:39], %[sidx]\n\t"
        "ds_read_b128 v[240:243], %[va] offset:8192\n\t"
        "s_waitcnt lgkmcnt(5)\n\t"
        "v_smfmac_f32_16x16x64_bf16 v[248:251], v[220:223], v[40:47], %[sidx]\n\t"
        "ds_read_b128 v[244:247], %[va] offset:10240\n\t"
        "s_waitcnt lgkmcnt(5)\n\t"
        "v_smfmac_f32_16x16x64_bf16 v[248:251], v[224:227], v[48:55], %[sidx]\n\t"
        "s_waitcnt lgkmcnt(4)\n\t"
        "v_smfmac_f32_16x16x64_bf16 v[248:251], v[228:231], v[56:63], %[sidx]\n\t"
        "s_waitcnt lgkmcnt(3)\n\t"
        "v_smfmac_f32_16x16x64_bf16 v[248:251], v[232:235], v[64:71], %[sidx]\n\t"
        "s_waitcnt lgkmcnt(2)\n\t"
        "v_smfmac_f32_16x16x64_bf16 v[248:251], v[236:239], v[72:79], %[sidx]\n\t"
        "s_waitcnt lgkmcnt(1)\n\t"
        "v_smfmac_f32_16x16x64_bf16 v[248:251], v[240:243], v[80:87], %[sidx]\n\t"
        "s_waitcnt lgkmcnt(0)\n\t"
        "v_smfmac_f32_16x16x64_bf16 v[248:251], v[244:247], v[88:95], %[sidx]\n\t"
        "v_smfmac_f32_16x16x64_bf16 v[252:255], v[208:211], v[96:103], %[sidx]\n\t"
        "v_smfmac_f32_16x16x64_bf16 v[252:255], v[212:215], v[104:111], %[sidx]\n\t"
        "v_cvt_pk_bf16_f32 v200, v248, v249\n\t"
        "v_cvt_pk_bf16_f32 v201, v250, v251\n\t"
        "ds_write2_b32 %[qw], v200, v201 offset1:96\n\t"
        "v_fma_f32 v202, v248, v249, v250\n\t"
        "v_smfmac_f32_16x16x64_bf16 v[252:255], v[216:219], v[112:119], %[sidx]\n\t"
        "v_cmp_u_f32 vcc, v202, v251\n\t"
        "v_smfmac_f32_16x16x64_bf16 v[252:255], v[220:223], v[120:127], %[sidx]\n\t"
        "s_waitcnt lgkmcnt(0)\n\t"
        "s_cbranch_vccnz Lnan%=\n\t"
        "Lok%=:\n\t"
        "s_barrier\n\t"
        "ds_read_b64_tr_b16 v[192:193], %[qtr] offset:0\n\t"
        "ds_read_b64_tr_b16 v[194:195], %[qtr] offset:768\n\t"
        "v_smfmac_f32_16x16x64_bf16 v[252:255], v[224:227], v[128:135], %[sidx]\n\t"
        "ds_read_b64_tr_b16 v[196:197], %[qtr] offset:1536\n\t"
        "ds_read_b64_tr_b16 v[198:199], %[qtr] offset:2304\n\t"
        "v_smfmac_f32_16x16x64_bf16 v[252:255], v[228:231], v[136:143], %[sidx]\n\t"
        "ds_read_b64_tr_b16 v[208:209], %[qtr] offset:3072\n\t"
        "ds_read_b64_tr_b16 v[210:211], %[qtr] offset:3840\n\t"
        "v_smfmac_f32_16x16x64_bf16 v[252:255], v[232:235], v[144:151], %[sidx]\n\t"
        "ds_read_b64_tr_b16 v[212:213], %[qtr] offset:4608\n\t"
        "ds_read_b64_tr_b16 v[214:215], %[qtr] offset:5376\n\t"
        "v_smfmac_f32_16x16x64_bf16 v[252:255], v[236:239], v[152:159], %[sidx]\n\t"
        "s_waitcnt lgkmcnt(6)\n\t"
        "v_mfma_f32_16x16x32_bf16 v[248:251], v[176:179], v[192:195], 0\n\t"
        "s_waitcnt lgkmcnt(4)\n\t"
        "v_mfma_f32_16x16x32_bf16 v[248:251], v[180:183], v[196:199], v[248:251]\n\t"
        "s_waitcnt lgkmcnt(2)\n\t"
        "v_mfma_f32_16x16x32_bf16 v[248:251], v[184:187], v[208:211], v[248:251]\n\t"
        "s_waitcnt lgkmcnt(0)\n\t"
        "v_mfma_f32_16x16x32_bf16 v[248:251], v[188:191], v[212:215], v[248:251]\n\t"
        "v_smfmac_f32_16x16x64_bf16 v[252:255], v[240:243], v[160:167], %[sidx]\n\t"
        "v_smfmac_f32_16x16x64_bf16 v[252:255], v[244:247], v[168:175], %[sidx]\n\t"
        "v_add_f32_dpp v192, v248, v248 row_ror:8 row_mask:0xf bank_mask:0x3\n\t"
        "v_add_f32_dpp v193, v249, v249 row_ror:8 row_mask:0xf bank_mask:0x3\n\t"
        "v_add_f32_dpp v192, v250, v250 row_ror:8 row_mask:0xf bank_mask:0xc\n\t"
        "v_add_f32_dpp v193, v251, v251 row_ror:8 row_mask:0xf bank_mask:0xc\n\t"
        "v_exp_f32 v194, v192\n\t"
        "v_exp_f32 v195, v193\n\t"
        "v_max_f32_e64 v196, |v192|, |v193|\n\t"
        "v_cvt_pk_bf16_f32 v194, v194, v195\n\t"
        "v_cmpx_le_f32 vcc, v196, %[lim]\n\t"
        "buffer_store_dword v194, %[eo], %[rs], %[so] offen sc1\n\t"
        "buffer_store_dword v194, %[eo], %[rs], s92 offen sc1\n\t"
        "buffer_store_dword v194, %[eo], %[rs], s93 offen sc1\n\t"
        "buffer_store_dword v194, %[eo], %[rs], s94 offen sc1\n\t"
        "s_mov_b64 exec, -1\n\t"
        "v_cndmask_b32 v194, -1, v194, vcc\n\t"
        "ds_write_b32 %[ew], v194\n\t"
        "v_add_f32 v252, v252, v254\n\t"
        "v_add_f32 v253, v253, v255\n\t"
        "v_cvt_pk_bf16_f32 v204, v252, v253\n\t"
        "ds_write_b64 %[kw], v[204:205]\n\t"
        "s_barrier\n\t"
        "s_branch Lend%=\n\t"
        "Lnan%=:\n\t"
        "v_max_u32 v207, v208, v209\n\t"
        "v_max3_u32 v207, v207, v210, v211\n\t"
        "v_max3_u32 v207, v207, v212, v213\n\t"
        "v_max3_u32 v207, v207, v214, v215\n\t"
        "v_max3_u32 v207, v207, v216, v217\n\t"
        "v_max3_u32 v207, v207, v218, v219\n\t"
        "v_max3_u32 v207, v207, v220, v221\n\t"
        "v_max3_u32 v207, v207, v222, v223\n\t"
        "v_max3_u32 v207, v207, v224, v225\n\t"
        "v_max3_u32 v207, v207, v226, v227\n\t"
        "v_max3_u32 v207, v207, v228, v229\n\t"
        "v_max3_u32 v207, v207, v230, v231\n\t"
        "v_max3_u32 v207, v207, v232, v233\n\t"
        "v_max3_u32 v207, v207, v234, v235\n\t"
        "v_max3_u32 v207, v207, v236, v237\n\t"
        "v_max3_u32 v207, v207, v238, v239\n\t"
        "v_max3_u32 v207, v207, v240, v241\n\t"
        "v_max3_u32 v207, v207, v242, v243\n\t"
        "v_max3_u32 v207, v207, v244, v245\n\t"
        "v_max3_u32 v207, v207, v246, v247\n\t"
        "v_cmp_eq_u32 vcc, 0xffbfffbf, v207\n\t"
        "s_cbranch_vccz Lok%=\n\t"
        "s_mov_b32 %[redo], 1\n\t"
        "s_barrier\n\t"
        "s_branch Lend%=\n\t"
        "Lend%=:\n\t"
        : [redo] "+&s"(redo)
        : [w0] "{v[16:47]}"(W[0]), [w1] "{v[48:79]}"(W[1]), [w2] "{v[80:111]}"(W[2]), [w3] "{v[112:143]}"(W[3]), [w4] "{v[144:175]}"(W[4]), [va] "v"(va), [sidx] "v"(sidx), [qw] "v"(qw), [kw] "v"(kw), [fv] "s"(fv), [kxa] "v"(kxa), [qtr] "v"(qtr), [eo] "v"(eo), [ew] "v"(ew), [lim] "v"(lim), [rs] "s"(rs), [so] "s"(so)
        : "s92", "s93", "s94", "v176", "v177", "v178", "v179", "v180", "v181", "v182", "v183", "v184", "v185", "v186", "v187", "v188", "v189", "v190", "v191", "v192", "v193", "v194", "v195", "v196", "v197", "v198", "v199", "v200", "v201", "v202", "v203", "v204", "v205", "v206", "v207", "v208", "v209", "v210", "v211", "v212", "v213", "v214", "v215", "v216", "v217", "v218", "v219", "v220", "v221", "v222", "v223", "v224", "v225", "v226", "v227", "v228", "v229", "v230", "v231", "v232", "v233", "v234", "v235", "v236", "v237", "v238", "v239", "v240", "v241", "v242", "v243", "v244", "v245", "v246", "v247", "v248", "v249", "v250", "v251", "v252", "v253", "v254", "v255", "vcc", "scc", "memory");
}

__device__ __forceinline__ void s_f(const u32x32 (&W)[5], u32& redo, u32 va, u32 sidx, u32 qw, u32 kw, u32 fv, u32& bad, u32x4 ones, u32 qtr, u32 er, u32 po, float c1, float ind0, float ind1, u32x4 rs, u32 so, float ce, float big, u64 jm STP) {
    asm volatile(
        "v_mov_b64 v[248:249], 0\n\t"
        "v_mov_b64 v[250:251], 0\n\t"
        "v_mov_b64 v[252:253], 0\n\t"
        "v_mov_b64 v[254:255], 0\n\t"
        "v_mov_b32 v205, %[fv]\n\t"
        "s_add_u32 s92, %[so], 0x1000\n\t"
        "s_add_u32 s93, %[so], 0x4000\n\t"
        "s_add_u32 s94, %[so], 0x5000\n\t"
        "s_mov_b32 %[bad], 0\n\t"
        "s_setprio 2\n\t"
        "s_barrier\n\t"
        "s_nop 11\n\t"
        "ds_read_b128 v[208:211], %[va] offset:0\n\t"
        "ds_read_b128 v[212:215], %[va] offset:1024\n\t"
        "ds_read_b128 v[216:219], %[va] offset:2048\n\t"
        "ds_read_b128 v[220:223], %[va] offset:3072\n\t"
        "ds_read_b128 v[224:227], %[va] offset:4096\n\t"
        "ds_read_b128 v[228:231], %[va] offset:5120\n\t"
        "s_waitcnt lgkmcnt(5)\n\t"
        "v_smfmac_f32_16x16x64_bf16 v[248:251], v[208:211], v[16:23], %[sidx]\n\t"
        "ds_read_b128 v[232:235], %[va] offset:6144\n\t"
        "s_waitcnt lgkmcnt(5)\n\t"
        "v_smfmac_f32_16x16x64_bf16 v[248:251], v[212:215], v[24:31], %[sidx]\n\t"
        "ds_read_b128 v[236:239], %[va] offset:7168\n\t"
        "s_waitcnt lgkmcnt(5)\n\t"
        "v_smfmac_f32_16x16x64_bf16 v[248:251], v[216:219], v[32:39], %[sidx]\n\t"
        "ds_read_b128 v[240:243], %[va] offset:8192\n\t"
        "s_waitcnt lgkmcnt(5)\n\t"
        "v_smfmac_f32_16x16x64_bf16 v[248:251], v[220:223], v[40:47], %[sidx]\n\t"
        "ds_read_b128 v[244:247], %[va] offset:10240\n\t"
        "s_waitcnt lgkmcnt(5)\n\t"
        "v_smfmac_f32_16x16x64_bf16 v[248:251], v[224:227], v[48:55], %[sidx]\n\t"
        "s_waitcnt lgkmcnt(4)\n\t"
        "v_smfmac_f32_16x16x64_bf16 v[248:251], v[228:231], v[56:63], %[sidx]\n\t"
        "s_waitcnt lgkmcnt(3)\n\t"
        "v_smfmac_f32_16x16x64_bf16 v[248:251], v[232:235], v[64:71], %[sidx]\n\t"
        "s_waitcnt lgkmcnt(2)\n\t"
        "v_smfmac_f32_16x16x64_bf16 v[248:251], v[236:239], v[72:79], %[sidx]\n\t"
        "s_waitcnt lgkmcnt(1)\n\t"
        "v_smfmac_f32_16x16x64_bf16 v[248:251], v[240:243], v[80:87], %[sidx]\n\t"
        "s_waitcnt lgkmcnt(0)\n\t"
        "v_smfmac_f32_16x16x64_bf16 v[248:251], v[244:247], v[88:95], %[sidx]\n\t"
        "v_smfmac_f32_16x16x64_bf16 v[252:255], v[208:211], v[96:103], %[sidx]\n\t"
        "v_smfmac_f32_16x16x64_bf16 v[252:255], v[212:215], v[104:111], %[sidx]\n\t"
        "v_cvt_pk_bf16_f32 v200, v248, v249\n\t"
        "v_cvt_pk_bf16_f32 v201, v250, v251\n\t"
        "ds_write2_b32 %[qw], v200, v201 offset1:96\n\t"
        "v_fma_f32 v202, v248, v249, v250\n\t"
        "v_smfmac_f32_16x16x64_bf16 v[252:255], v[216:219], v[112:119], %[sidx]\n\t"
        "v_cmp_u_f32 vcc, v202, v251\n\t"
        "v_smfmac_f32_16x16x64_bf16 v[252:255], v[220:223], v[120:127], %[sidx]\n\t"
        "s_waitcnt lgkmcnt(0)\n\t"
        "s_cbranch_vccnz Lnan%=\n\t"
        "Lok%=:\n\t"
        "s_barrier\n\t"
        "v_smfmac_f32_16x16x64_bf16 v[252:255], v[224:227], v[128:135], %[sidx]\n\t"
        "v_smfmac_f32_16x16x64_bf16 v[252:255], v[228:231], v[136:143], %[sidx]\n\t"
        "v_smfmac_f32_16x16x64_bf16 v[252:255], v[232:235], v[144:151], %[sidx]\n\t"
        "v_smfmac_f32_16x16x64_bf16 v[252:255], v[236:239], v[152:159], %[sidx]\n\t"
        "v_smfmac_f32_16x16x64_bf16 v[252:255], v[240:243], v[160:167], %[sidx]\n\t"
        "v_smfmac_f32_16x16x64_bf16 v[252:255], v[244:247], v[168:175], %[sidx]\n\t"
        "s_nop 7\n\t"
        "v_add_f32 v252, v252, v254\n\t"
        "v_add_f32 v253, v253, v255\n\t"
        "v_cvt_pk_bf16_f32 v204, v252, v253\n\t"
        "ds_write_b64 %[kw], v[204:205]\n\t"
        "v_mov_b32 v237, 0\n\t"
        "ds_read_b64_tr_b16 v[192:193], %[qtr] offset:0\n\t"
        "ds_read_b64_tr_b16 v[194:195], %[qtr] offset:768\n\t"
        "ds_read_b64_tr_b16 v[196:197], %[qtr] offset:1536\n\t"
        "ds_read_b64_tr_b16 v[198:199], %[qtr] offset:2304\n\t"
        "ds_read_b64_tr_b16 v[208:209], %[qtr] offset:3072\n\t"
        "ds_read_b64_tr_b16 v[210:211], %[qtr] offset:3840\n\t"
        "ds_read_b64_tr_b16 v[212:213], %[qtr] offset:4608\n\t"
        "ds_read_b64_tr_b16 v[214:215], %[qtr] offset:5376\n\t"
        "s_nop 15\n\t"
        "s_nop 5\n\t"
        "ds_read_b64_tr_b16 v[224:225], %[qtr] offset:7680\n\t"
        "ds_read_b64_tr_b16 v[226:227], %[qtr] offset:8448\n\t"
        "ds_read_b64_tr_b16 v[228:229], %[qtr] offset:10752\n\t"
        "ds_read_b64_tr_b16 v[230:231], %[qtr] offset:11520\n\t"
        "s_waitcnt lgkmcnt(2)\n\t"
        "v_mfma_f32_16x16x32_bf16 v[248:251], v[224:227], v[196:199], 0\n\t"
        "s_waitcnt lgkmcnt(0)\n\t"
        "v_mfma_f32_16x16x32_bf16 v[248:251], v[228:231], v[212:215], v[248:251]\n\t"
        "v_min3_u32 v203, v224, v226, v228\n\t"
        "v_min_u32 v203, v203, v230\n\t"
        "s_setprio 3\n\t"
        "s_barrier\n\t"
        "ds_read_b64_tr_b16 v[224:225], %[qtr] offset:6144\n\t"
        "ds_read_b64_tr_b16 v[226:227], %[qtr] offset:6912\n\t"
        "ds_read_b64_tr_b16 v[228:229], %[qtr] offset:9216\n\t"
        "ds_read_b64_tr_b16 v[230:231], %[qtr] offset:9984\n\t"
        "ds_read_b128 v[240:243], %[er]\n\t"
        "s_waitcnt lgkmcnt(3)\n\t"
        "v_mfma_f32_16x16x32_bf16 v[248:251], v[224:227], v[192:195], v[248:251]\n\t"
        "s_waitcnt lgkmcnt(1)\n\t"
        "v_mfma_f32_16x16x32_bf16 v[248:251], v[228:231], v[208:211], v[248:251]\n\t"
        "v_min3_u32 v203, v224, v226, v203\n\t"
        "s_waitcnt lgkmcnt(0)\n\t"
        "Lgo%=:\n\t"
        "v_mfma_f32_16x16x32_bf16 v[244:247], v[176:179], v[240:243], 0\n\t"
        "v_min3_u32 v203, v228, v230, v203\n\t"
        "v_cmp_ne_u32 s[96:97], %[fv], v203\n\t"
        "s_nop 0\n\t"
        "v_add_f32_dpp v248, v248, v248 row_ror:8 row_mask:0xf bank_mask:0xf\n\t"
        "v_add_f32_dpp v249, v249, v249 row_ror:8 row_mask:0xf bank_mask:0xf\n\t"
        "v_mul_f32 v248, %[ce], v248\n\t"
        "v_mul_f32 v249, %[ce], v249\n\t"
        "v_exp_f32 v250, v248\n\t"
        "v_exp_f32 v251, v249\n\t"
        "s_and_b64 s[96:97], s[96:97], %[jm]\n\t"
        "v_cvt_pk_bf16_f32 v236, v250, v251\n\t"
        "v_fma_f32 v250, v250, %[c1], %[ind0]\n\t"
        "v_fma_f32 v251, v251, %[c1], %[ind1]\n\t"
        "v_mfma_f32_16x16x16_bf16 v[244:247], v[176:177], v[236:237], v[244:247]\n\t"
        "s_cbranch_scc1 Lt%=\n\t"
        "s_nop 6\n\t"
        "v_rcp_f32 v246, v244\n\t"
        "v_cmpx_gt_f32 vcc, %[big], v244\n\t"
        "v_pk_mul_f32 v[250:251], v[250:251], v[246:247] op_sel_hi:[1,0]\n\t"
        "v_cvt_pk_bf16_f32 v250, v250, v251\n\t"
        "buffer_store_dword v250, %[po], %[rs], %[so] offen sc1\n\t"
        "buffer_store_dword v250, %[po], %[rs], s92 offen sc1\n\t"
        "buffer_store_dword v250, %[po], %[rs], s93 offen sc1\n\t"
        "buffer_store_dword v250, %[po], %[rs], s94 offen sc1\n\t"
        "s_cmp_lg_u64 exec, -1\n\t"
        "s_mov_b64 exec, -1\n\t"
        "s_cselect_b32 %[bad], 1, 0\n\t"
        "s_branch Lend%=\n\t"
        "Lt%=:\n\t"
        "s_nop 7\n\t"
        "ds_read_b64_tr_b16 v[216:217], %[qtr] offset:6144\n\t"
        "ds_read_b64_tr_b16 v[218:219], %[qtr] offset:6912\n\t"
        "ds_read_b64_tr_b16 v[220:221], %[qtr] offset:7680\n\t"
        "ds_read_b64_tr_b16 v[222:223], %[qtr] offset:8448\n\t"
        "ds_read_b64_tr_b16 v[224:225], %[qtr] offset:9216\n\t"
        "ds_read_b64_tr_b16 v[226:227], %[qtr] offset:9984\n\t"
        "ds_read_b64_tr_b16 v[228:229], %[qtr] offset:10752\n\t"
        "ds_read_b64_tr_b16 v[230:231], %[qtr] offset:11520\n\t"
        "ds_read_b128 v[240:243], %[er]\n\t"
        "s_waitcnt lgkmcnt(0)\n\t"
        "v_mfma_f32_16x16x32_bf16 v[248:251], v[216:219], v[192:195], 0\n\t"
        "v_mfma_f32_16x16x32_bf16 v[248:251], v[220:223], v[196:199], v[248:251]\n\t"
        "v_mfma_f32_16x16x32_bf16 v[248:251], v[224:227], v[208:211], v[248:251]\n\t"
        "v_mfma_f32_16x16x32_bf16 v[248:251], v[228:231], v[212:215], v[248:251]\n\t"
        "v_min3_u32 v203, v216, v218, v220\n\t"
        "v_min3_u32 v203, v222, v224, v203\n\t"
        "v_min_u32 v203, v226, v203\n\t"
        "s_branch Lgo%=\n\t"
        "Lnan%=:\n\t"
        "v_max_u32 v207, v208, v209\n\t"
        "v_max3_u32 v207, v207, v210, v211\n\t"
        "v_max3_u32 v207, v207, v212, v213\n\t"
        "v_max3_u32 v207, v207, v214, v215\n\t"
        "v_max3_u32 v207, v207, v216, v217\n\t"
        "v_max3_u32 v207, v207, v218, v219\n\t"
        "v_max3_u32 v207, v207, v220, v221\n\t"
        "v_max3_u32 v207, v207, v222, v223\n\t"
        "v_max3_u32 v207, v207, v224, v225\n\t"
        "v_max3_u32 v207, v207, v226, v227\n\t"
        "v_max3_u32 v207, v207, v228, v229\n\t"
        "v_max3_u32 v207, v207, v230, v231\n\t"
        "v_max3_u32 v207, v207, v232, v233\n\t"
        "v_max3_u32 v207, v207, v234, v235\n\t"
        "v_max3_u32 v207, v207, v236, v237\n\t"
        "v_max3_u32 v207, v207, v238, v239\n\t"
        "v_max3_u32 v207, v207, v240, v241\n\t"
        "v_max3_u32 v207, v207, v242, v243\n\t"
        "v_max3_u32 v207, v207, v244, v245\n\t"
        "v_max3_u32 v207, v207, v246, v247\n\t"
        "v_cmp_eq_u32 vcc, 0xffbfffbf, v207\n\t"
        "s_cbranch_vccz Lok%=\n\t"
        "s_mov_b32 %[redo], 1\n\t"
        "s_barrier\n\t"
        "s_branch Lend%=\n\t"
        "Lend%=:\n\t"
        : [redo] "+&s"(redo), [bad] "=&s"(bad)
        : [w0] "{v[16:47]}"(W[0]), [w1] "{v[48:79]}"(W[1]), [w2] "{v[80:111]}"(W[2]), [w3] "{v[112:143]}"(W[3]), [w4] "{v[144:175]}"(W[4]), [va] "v"(va), [sidx] "v"(sidx), [qw] "v"(qw), [kw] "v"(kw), [fv] "s"(fv), [ones] "{v[176:179]}"(ones), [qtr] "v"(qtr), [er] "v"(er), [po] "v"(po), [c1] "v"(c1), [ind0] "v"(ind0), [ind1] "v"(ind1), [rs] "s"(rs), [so] "s"(so), [ce] "s"(ce), [big] "s"(big), [jm] "s"(jm)
        : "s92", "s93", "s94", "s95", "s96", "s97", "v192", "v193", "v194", "v195", "v196", "v197", "v198", "v199", "v200", "v201", "v202", "v203", "v204", "v205", "v206", "v207", "v208", "v209", "v210", "v211", "v212", "v213", "v214", "v215", "v216", "v217", "v218", "v219", "v220", "v221", "v222", "v223", "v224", "v225", "v226", "v227", "v228", "v229", "v230", "v231", "v232", "v233", "v234", "v235", "v236", "v237", "v238", "v239", "v240", "v241", "v242", "v243", "v244", "v245", "v246", "v247", "v248", "v249", "v250", "v251", "v252", "v253", "v254", "v255", "vcc", "scc", "memory");
}

__device__ __forceinline__ void s_m(const u32x32 (&W)[5], u32& redo, u32 va, u32 sidx, u32 qw, u32 kw, u32 fv, u32& left, u32& c0, u32& r0, u32& t1, u32& t3, u32 tg, u32 kr, u32 vm, u32 so, u32 lb, u32x4 rs, u32 poll, u32 tv, u32 tsr STP) {
    asm volatile(
        "v_mov_b64 v[248:249], 0\n\t"
        "v_mov_b64 v[250:251], 0\n\t"
        "v_mov_b64 v[252:253], 0\n\t"
        "v_mov_b64 v[254:255], 0\n\t"
        "v_mov_b32 v205, %[fv]\n\t"
        "s_mov_b32 s94, m0\n\t"
        "s_add_u32 s92, %[so], 0x8000\n\t"
        "s_add_u32 s93, %[so], 0x10000\n\t"
        "s_mov_b32 m0, %[lb]\n\t"
        "s_setprio 2\n\t"
        "s_cmp_eq_u32 %[poll], 0\n\t"
        "s_cbranch_scc1 Lpb%=\n\t"
        "Lda_%=:\n\t"
        "s_memrealtime s[90:91]\n\t"
        "s_waitcnt lgkmcnt(0)\n\t"
        "s_mul_i32 s90, s90, 24\n\t"
        "s_sub_u32 s88, %[tg], s90\n\t"
        "s_sub_u32 s89, s88, 620\n\t"
        "s_cmp_gt_u32 s89, 0x4000\n\t"
        "s_cbranch_scc1 Lea_%=\n\t"
        "s_sleep 1\n\t"
        "s_branch Lda_%=\n\t"
        "Lea_%=:\n\t"
        "s_memrealtime s[64:65]\n\t"
        "s_nop 0\n\t"
        "s_memrealtime s[66:67]\n\t"
        "s_nop 0\n\t"
        "s_memrealtime s[68:69]\n\t"
        "s_nop 0\n\t"
        "s_memrealtime s[70:71]\n\t"
        "s_nop 0\n\t"
        "s_memrealtime s[72:73]\n\t"
        "s_nop 0\n\t"
        "s_memrealtime s[74:75]\n\t"
        "s_memtime s[90:91]\n\t"
        "s_memrealtime s[76:77]\n\t"
        "s_nop 0\n\t"
        "s_memrealtime s[78:79]\n\t"
        "s_nop 0\n\t"
        "s_memrealtime s[80:81]\n\t"
        "s_nop 0\n\t"
        "s_memrealtime s[82:83]\n\t"
        "s_nop 0\n\t"
        "s_memrealtime s[84:85]\n\t"
        "s_nop 0\n\t"
        "s_memrealtime s[86:87]\n\t"
        "s_nop 0\n\t"
        "s_nop 7\n\t"
        "s_waitcnt lgkmcnt(0)\n\t"
        "s_add_u32 s64, s64, s66\n\t"
        "s_add_u32 s64, s64, s68\n\t"
        "s_add_u32 s64, s64, s70\n\t"
        "s_add_u32 s64, s64, s72\n\t"
        "s_add_u32 s64, s64, s74\n\t"
        "s_add_u32 s64, s64, s76\n\t"
        "s_add_u32 s64, s64, s78\n\t"
        "s_add_u32 s64, s64, s80\n\t"
        "s_add_u32 s64, s64, s82\n\t"
        "s_add_u32 s64, s64, s84\n\t"
        "s_add_u32 s64, s64, s86\n\t"
        "s_lshl_b32 s64, s64, 1\n\t"
        "s_mov_b32 %[c0], s90\n\t"
        "s_mov_b32 %[r0], s64\n\t"
        "s_sub_u32 s88, %[tg], s64\n\t"
        "s_mul_i32 s88, s88, %[kr]\n\t"
        "s_ashr_i32 s88, s88, 12\n\t"
        "s_sub_u32 s88, s88, 244\n\t"
        "s_nop 1\n\t"
        "s_max_i32 s88, s88, 0\n\t"
        "s_min_i32 s88, s88, 384\n\t"
        "s_andn2_b32 s88, s88, 3\n\t"
        "s_getpc_b64 s[90:91]\n\t"
        "Lga_%=:\n\t"
        "s_sub_u32 s90, s90, s88\n\t"
        "s_subb_u32 s91, s91, 0\n\t"
        "s_add_u32 s90, s90, Lza_%=-Lga_%=\n\t"
        "s_addc_u32 s91, s91, 0\n\t"
        "s_setpc_b64 s[90:91]\n\t"
        "s_nop 0\n\ts_nop 0\n\ts_nop 0\n\ts_nop 0\n\t"
        "s_nop 0\n\ts_nop 0\n\ts_nop 0\n\ts_nop 0\n\t"
        "s_nop 0\n\ts_nop 0\n\ts_nop 0\n\ts_nop 0\n\t"
        "s_nop 0\n\ts_nop 0\n\ts_nop 0\n\ts_nop 0\n\t"
        "s_nop 0\n\ts_nop 0\n\ts_nop 0\n\ts_nop 0\n\t"
        "s_nop 0\n\ts_nop 0\n\ts_nop 0\n\ts_nop 0\n\t"
        "s_nop 0\n\ts_nop 0\n\ts_nop 0\n\ts_nop 0\n\t"
        "s_nop 0\n\ts_nop 0\n\ts_nop 0\n\ts_nop 0\n\t"
        "s_nop 0\n\ts_nop 0\n\ts_nop 0\n\ts_nop 0\n\t"
        "s_nop 0\n\ts_nop 0\n\ts_nop 0\n\ts_nop 0\n\t"
        "s_nop 0\n\ts_nop 0\n\ts_nop 0\n\ts_nop 0\n\t"
        "s_nop 0\n\ts_nop 0\n\ts_nop 0\n\ts_nop 0\n\t"
        "s_nop 0\n\ts_nop 0\n\ts_nop 0\n\ts_nop 0\n\t"
        "s_nop 0\n\ts_nop 0\n\ts_nop 0\n\ts_nop 0\n\t"
        "s_nop 0\n\ts_nop 0\n\ts_nop 0\n\ts_nop 0\n\t"
        "s_nop 0\n\ts_nop 0\n\ts_nop 0\n\ts_nop 0\n\t"
        "s_nop 0\n\ts_nop 0\n\ts_nop 0\n\ts_nop 0\n\t"
        "s_nop 0\n\ts_nop 0\n\ts_nop 0\n\ts_nop 0\n\t"
        "s_nop 0\n\ts_nop 0\n\ts_nop 0\n\ts_nop 0\n\t"
        "s_nop 0\n\ts_nop 0\n\ts_nop 0\n\ts_nop 0\n\t"
        "s_nop 0\n\ts_nop 0\n\ts_nop 0\n\ts_nop 0\n\t"
        "s_nop 0\n\ts_nop 0\n\ts_nop 0\n\ts_nop 0\n\t"
        "s_nop 0\n\ts_nop 0\n\ts_nop 0\n\ts_nop 0\n\t"
        "s_nop 0\n\ts_nop 0\n\ts_nop 0\n\ts_nop 0\n\t"
        "Lza_%=:\n\t"
        "buffer_load_dwordx4 %[vm], %[rs], %[so] offen offset:0 sc1 lds\n\t"
        "buffer_load_dwordx4 %[vm], %[rs], %[so] offen offset:1024 sc1 lds\n\t"
        "buffer_load_dwordx4 %[vm], %[rs], %[so] offen offset:2048 sc1 lds\n\t"
        "buffer_load_dwordx4 %[vm], %[rs], %[so] offen offset:3072 sc1 lds\n\t"
        "s_add_u32 m0, m0, 0x1000\n\t"
        "s_nop 0\n\t"
        "buffer_load_dwordx4 %[vm], %[rs], s92 offen offset:0 sc1 lds\n\t"
        "buffer_load_dwordx4 %[vm], %[rs], s92 offen offset:1024 sc1 lds\n\t"
        "buffer_load_dwordx4 %[vm], %[rs], s92 offen offset:2048 sc1 lds\n\t"
        "buffer_load_dwordx4 %[vm], %[rs], s92 offen offset:3072 sc1 lds\n\t"
        "s_add_u32 m0, m0, 0x1000\n\t"
        "s_nop 0\n\t"
        "buffer_load_dwordx4 %[vm], %[rs], s93 offen offset:0 sc1 lds\n\t"
        "buffer_load_dwordx4 %[vm], %[rs], s93 offen offset:2048 sc1 lds\n\t"
        "s_waitcnt vmcnt(0)\n\t"
        "Lpb%=:\n\t"
        "s_barrier\n\t"
        "s_nop 11\n\t"
        "ds_read_b128 v[208:211], %[va] offset:0\n\t"
        "ds_read_b128 v[212:215], %[va] offset:1024\n\t"
        "ds_read_b128 v[216:219], %[va] offset:2048\n\t"
        "ds_read_b128 v[220:223], %[va] offset:3072\n\t"
        "ds_read_b128 v[224:227], %[va] offset:4096\n\t"
        "ds_read_b128 v[228:231], %[va] offset:5120\n\t"
        "s_waitcnt lgkmcnt(5)\n\t"
        "v_smfmac_f32_16x16x64_bf16 v[248:251], v[208:211], v[16:23], %[sidx]\n\t"
        "ds_read_b128 v[232:235], %[va] offset:6144\n\t"
        "s_waitcnt lgkmcnt(5)\n\t"
        "v_smfmac_f32_16x16x64_bf16 v[248:251], v[212:215], v[24:31], %[sidx]\n\t"
        "ds_read_b128 v[236:239], %[va] offset:7168\n\t"
        "s_waitcnt lgkmcnt(5)\n\t"
        "v_smfmac_f32_16x16x64_bf16 v[248:251], v[216:219], v[32:39], %[sidx]\n\t"
        "ds_read_b128 v[240:243], %[va] offset:8192\n\t"
        "s_waitcnt lgkmcnt(5)\n\t"
        "v_smfmac_f32_16x16x64_bf16 v[248:251], v[220:223], v[40:47], %[sidx]\n\t"
        "ds_read_b128 v[244:247], %[va] offset:10240\n\t"
        "s_waitcnt lgkmcnt(5)\n\t"
        "v_smfmac_f32_16x16x64_bf16 v[248:251], v[224:227], v[48:55], %[sidx]\n\t"
        "s_waitcnt lgkmcnt(4)\n\t"
        "v_smfmac_f32_16x16x64_bf16 v[248:251], v[228:231], v[56:63], %[sidx]\n\t"
        "s_waitcnt lgkmcnt(3)\n\t"
        "v_smfmac_f32_16x16x64_bf16 v[248:251], v[232:235], v[64:71], %[sidx]\n\t"
        "s_waitcnt lgkmcnt(2)\n\t"
        "v_smfmac_f32_16x16x64_bf16 v[248:251], v[236:239], v[72:79], %[sidx]\n\t"
        "s_waitcnt lgkmcnt(1)\n\t"
        "v_smfmac_f32_16x16x64_bf16 v[248:251], v[240:243], v[80:87], %[sidx]\n\t"
        "s_waitcnt lgkmcnt(0)\n\t"
        "v_smfmac_f32_16x16x64_bf16 v[248:251], v[244:247], v[88:95], %[sidx]\n\t"
        "v_smfmac_f32_16x16x64_bf16 v[252:255], v[208:211], v[96:103], %[sidx]\n\t"
        "v_smfmac_f32_16x16x64_bf16 v[252:255], v[212:215], v[104:111], %[sidx]\n\t"
        "v_cvt_pk_bf16_f32 v200, v248, v249\n\t"
        "v_cvt_pk_bf16_f32 v201, v250, v251\n\t"
        "ds_write2_b32 %[qw], v200, v201 offset1:96\n\t"
        "v_fma_f32 v202, v248, v249, v250\n\t"
        "v_smfmac_f32_16x16x64_bf16 v[252:255], v[216:219], v[112:119], %[sidx]\n\t"
        "v_cmp_u_f32 vcc, v202, v251\n\t"
        "v_smfmac_f32_16x16x64_bf16 v[252:255], v[220:223], v[120:127], %[sidx]\n\t"
        "s_waitcnt lgkmcnt(0)\n\t"
        "s_cbranch_vccnz Lnan%=\n\t"
        "Lok%=:\n\t"
        "s_barrier\n\t"
        "buffer_load_dwordx4 v[196:199], %[tv], %[rs], %[tsr] offen sc1\n\t"
        "v_smfmac_f32_16x16x64_bf16 v[252:255], v[224:227], v[128:135], %[sidx]\n\t"
        "v_smfmac_f32_16x16x64_bf16 v[252:255], v[228:231], v[136:143], %[sidx]\n\t"
        "v_smfmac_f32_16x16x64_bf16 v[252:255], v[232:235], v[144:151], %[sidx]\n\t"
        "v_smfmac_f32_16x16x64_bf16 v[252:255], v[236:239], v[152:159], %[sidx]\n\t"
        "v_smfmac_f32_16x16x64_bf16 v[252:255], v[240:243], v[160:167], %[sidx]\n\t"
        "v_smfmac_f32_16x16x64_bf16 v[252:255], v[244:247], v[168:175], %[sidx]\n\t"
        "s_nop 7\n\t"
        "v_add_f32 v252, v252, v254\n\t"
        "v_add_f32 v253, v253, v255\n\t"
        "v_cvt_pk_bf16_f32 v204, v252, v253\n\t"
        "ds_write_b64 %[kw], v[204:205]\n\t"
        "s_barrier\n\t"
        "s_sleep 5\n\t"
        "s_waitcnt vmcnt(0)\n\t"
        "v_cmp_eq_u32 vcc, %[t3], v199\n\t"
        "s_nop 4\n\t"
        "s_mov_b32 %[t3], vcc_lo\n\t"
        "s_andn2_b32 s92, 1, vcc_lo\n\t"
        "s_nop 3\n\t"
        "v_readlane_b32 %[t1], v197, s92\n\t"
        "s_branch Lend%=\n\t"
        "Lck%=:\n\t"
        "v_add_u32 v204, %[lb], %[vm]\n\t"
        "Lcl%=:\n\t"
        "s_mov_b32 m0, %[lb]\n\t"
        "s_nop 0\n\t"
        "buffer_load_dwordx4 %[vm], %[rs], %[so] offen offset:0 sc1 lds\n\t"
        "buffer_load_dwordx4 %[vm], %[rs], %[so] offen offset:1024 sc1 lds\n\t"
        "buffer_load_dwordx4 %[vm], %[rs], %[so] offen offset:2048 sc1 lds\n\t"
        "buffer_load_dwordx4 %[vm], %[rs], %[so] offen offset:3072 sc1 lds\n\t"
        "s_add_u32 m0, m0, 0x1000\n\t"
        "s_nop 0\n\t"
        "buffer_load_dwordx4 %[vm], %[rs], s92 offen offset:0 sc1 lds\n\t"
        "buffer_load_dwordx4 %[vm], %[rs], s92 offen offset:1024 sc1 lds\n\t"
        "buffer_load_dwordx4 %[vm], %[rs], s92 offen offset:2048 sc1 lds\n\t"
        "buffer_load_dwordx4 %[vm], %[rs], s92 offen offset:3072 sc1 lds\n\t"
        "s_add_u32 m0, m0, 0x1000\n\t"
        "s_nop 0\n\t"
        "buffer_load_dwordx4 %[vm], %[rs], s93 offen offset:0 sc1 lds\n\t"
        "buffer_load_dwordx4 %[vm], %[rs], s93 offen offset:2048 sc1 lds\n\t"
        "s_sub_u32 %[left], %[left], 1\n\t"
        "s_waitcnt vmcnt(9)\n\t"
        "ds_read_b128 v[208:211], v204 offset:0\n\t"
        "s_waitcnt vmcnt(8)\n\t"
        "ds_read_b128 v[212:215], v204 offset:1024\n\t"
        "s_waitcnt vmcnt(7)\n\t"
        "ds_read_b128 v[216:219], v204 offset:2048\n\t"
        "s_waitcnt vmcnt(6)\n\t"
        "ds_read_b128 v[220:223], v204 offset:3072\n\t"
        "s_waitcnt vmcnt(5)\n\t"
        "ds_read_b128 v[224:227], v204 offset:4096\n\t"
        "s_waitcnt vmcnt(4)\n\t"
        "ds_read_b128 v[228:231], v204 offset:5120\n\t"
        "s_waitcnt lgkmcnt(5)\n\t"
        "v_max3_u32 v207, v208, v208, v209\n\t"
        "v_max3_u32 v207, v207, v210, v211\n\t"
        "s_waitcnt vmcnt(3)\n\t"
        "ds_read_b128 v[232:235], v204 offset:6144\n\t"
        "s_waitcnt lgkmcnt(5)\n\t"
        "v_max3_u32 v207, v207, v212, v213\n\t"
        "v_max3_u32 v207, v207, v214, v215\n\t"
        "s_waitcnt vmcnt(2)\n\t"
        "ds_read_b128 v[236:239], v204 offset:7168\n\t"
        "s_waitcnt lgkmcnt(5)\n\t"
        "v_max3_u32 v207, v207, v216, v217\n\t"
        "v_max3_u32 v207, v207, v218, v219\n\t"
        "s_waitcnt vmcnt(1)\n\t"
        "ds_read_b128 v[240:243], v204 offset:8192\n\t"
        "s_waitcnt lgkmcnt(5)\n\t"
        "v_max3_u32 v207, v207, v220, v221\n\t"
        "v_max3_u32 v207, v207, v222, v223\n\t"
        "s_waitcnt vmcnt(0)\n\t"
        "ds_read_b128 v[244:247], v204 offset:10240\n\t"
        "s_waitcnt lgkmcnt(5)\n\t"
        "v_max3_u32 v207, v207, v224, v225\n\t"
        "v_max3_u32 v207, v207, v226, v227\n\t"
        "s_waitcnt lgkmcnt(4)\n\t"
        "v_max3_u32 v207, v207, v228, v229\n\t"
        "v_max3_u32 v207, v207, v230, v231\n\t"
        "s_waitcnt lgkmcnt(3)\n\t"
        "v_max3_u32 v207, v207, v232, v233\n\t"
        "v_max3_u32 v207, v207, v234, v235\n\t"
        "s_waitcnt lgkmcnt(2)\n\t"
        "v_max3_u32 v207, v207, v236, v237\n\t"
        "v_max3_u32 v207, v207, v238, v239\n\t"
        "s_waitcnt lgkmcnt(1)\n\t"
        "v_max3_u32 v207, v207, v240, v241\n\t"
        "v_max3_u32 v207, v207, v242, v243\n\t"
        "s_waitcnt lgkmcnt(0)\n\t"
        "v_max3_u32 v207, v207, v244, v245\n\t"
        "v_max3_u32 v207, v207, v246, v247\n\t"
        "v_cmp_eq_u32 vcc, 0xffbfffbf, v207\n\t"
        "s_cbranch_vccz Lpb%=\n\t"
        "s_cbranch_scc0 Lcl%=\n\t"
        "s_branch Lend%=\n\t"
        "Lnan%=:\n\t"
        "v_max_u32 v207, v208, v209\n\t"
        "v_max3_u32 v207, v207, v210, v211\n\t"
        "v_max3_u32 v207, v207, v212, v213\n\t"
        "v_max3_u32 v207, v207, v214, v215\n\t"
        "v_max3_u32 v207, v207, v216, v217\n\t"
        "v_max3_u32 v207, v207, v218, v219\n\t"
        "v_max3_u32 v207, v207, v220, v221\n\t"
        "v_max3_u32 v207, v207, v222, v223\n\t"
        "v_max3_u32 v207, v207, v224, v225\n\t"
        "v_max3_u32 v207, v207, v226, v227\n\t"
        "v_max3_u32 v207, v207, v228, v229\n\t"
        "v_max3_u32 v207, v207, v230, v231\n\t"
        "v_max3_u32 v207, v207, v232, v233\n\t"
        "v_max3_u32 v207, v207, v234, v235\n\t"
        "v_max3_u32 v207, v207, v236, v237\n\t"
        "v_max3_u32 v207, v207, v238, v239\n\t"
        "v_max3_u32 v207, v207, v240, v241\n\t"
        "v_max3_u32 v207, v207, v242, v243\n\t"
        "v_max3_u32 v207, v207, v244, v245\n\t"
        "v_max3_u32 v207, v207, v246, v247\n\t"
        "v_cmp_eq_u32 vcc, 0xffbfffbf, v207\n\t"
        "s_cbranch_vccz Lok%=\n\t"
        "s_mov_b32 %[redo], 1\n\t"
        "s_barrier\n\t"
        "v_mov_b64 v[248:249], 0\n\t"
        "v_mov_b64 v[250:251], 0\n\t"
        "v_mov_b64 v[252:253], 0\n\t"
        "v_mov_b64 v[254:255], 0\n\t"
        "s_branch Lck%=\n\t"
        "Lend%=:\n\t"
        "s_mov_b32 m0, s94\n\t"
        : [redo] "+&s"(redo), [c0] "+&s"(c0), [r0] "+&s"(r0), [t1] "+&s"(t1), [t3] "+&s"(t3), [left] "+&s"(left)
        : [w0] "{v[16:47]}"(W[0]), [w1] "{v[48:79]}"(W[1]), [w2] "{v[80:111]}"(W[2]), [w3] "{v[112:143]}"(W[3]), [w4] "{v[144:175]}"(W[4]), [va] "v"(va), [sidx] "v"(sidx), [qw] "v"(qw), [kw] "v"(kw), [fv] "s"(fv), [rs] "s"(rs), [poll] "s"(poll), [tg] "s"(tg), [kr] "s"(kr), [tsr] "s"(tsr), [tv] "v"(tv), [vm] "v"(vm), [so] "s"(so), [lb] "s"(lb)
        : "s64", "s65", "s66", "s67", "s68", "s69", "s70", "s71", "s72", "s73", "s74", "s75", "s76", "s77", "s78", "s79", "s80", "s81", "s82", "s83", "s84", "s85", "s86", "s87", "s88", "s89", "s90", "s91", "s92", "s93", "s94", "v196", "v197", "v198", "v199", "v200", "v201", "v202", "v203", "v204", "v205", "v206", "v207", "v208", "v209", "v210", "v211", "v212", "v213", "v214", "v215", "v216", "v217", "v218", "v219", "v220", "v221", "v222", "v223", "v224", "v225", "v226", "v227", "v228", "v229", "v230", "v231", "v232", "v233", "v234", "v235", "v236", "v237", "v238", "v239", "v240", "v241", "v242", "v243", "v244", "v245", "v246", "v247", "v248", "v249", "v250", "v251", "v252", "v253", "v254", "v255", "vcc", "scc", "memory");
}

__device__ __forceinline__ void s_l(const u32x32 (&W)[5], u32& redo, u32 va, u32 sidx, u32 qw, u32 kw, u32 fv) {
    asm volatile(
        "v_mov_b64 v[248:249], 0\n\t"
        "v_mov_b64 v[250:251], 0\n\t"
        "v_mov_b64 v[252:253], 0\n\t"
        "v_mov_b64 v[254:255], 0\n\t"
        "v_mov_b32 v205, %[fv]\n\t"
        "s_waitcnt vmcnt(0)\n\t"
        "s_barrier\n\t"
        "ds_read_b128 v[208:211], %[va] offset:0\n\t"
        "ds_read_b128 v[212:215], %[va] offset:1024\n\t"
        "ds_read_b128 v[216:219], %[va] offset:2048\n\t"
        "ds_read_b128 v[220:223], %[va] offset:3072\n\t"
        "ds_read_b128 v[224:227], %[va] offset:4096\n\t"
        "ds_read_b128 v[228:231], %[va] offset:5120\n\t"
        "ds_read_b128 v[232:235], %[va] offset:6144\n\t"
        "ds_read_b128 v[236:239], %[va] offset:7168\n\t"
        "ds_read_b128 v[240:243], %[va] offset:8192\n\t"
        "ds_read_b128 v[244:247], %[va] offset:10240\n\t"
        "s_waitcnt lgkmcnt(9)\n\t"
        "v_smfmac_f32_16x16x64_bf16 v[252:255], v[208:211], v[96:103], %[sidx]\n\t"
        "s_waitcnt lgkmcnt(8)\n\t"
        "v_smfmac_f32_16x16x64_bf16 v[252:255], v[212:215], v[104:111], %[sidx]\n\t"
        "s_waitcnt lgkmcnt(7)\n\t"
        "v_smfmac_f32_16x16x64_bf16 v[252:255], v[216:219], v[112:119], %[sidx]\n\t"
        "s_waitcnt lgkmcnt(6)\n\t"
        "v_smfmac_f32_16x16x64_bf16 v[252:255], v[220:223], v[120:127], %[sidx]\n\t"
        "s_waitcnt lgkmcnt(5)\n\t"
        "v_smfmac_f32_16x16x64_bf16 v[252:255], v[224:227], v[128:135], %[sidx]\n\t"
        "s_waitcnt lgkmcnt(4)\n\t"
        "v_smfmac_f32_16x16x64_bf16 v[252:255], v[228:231], v[136:143], %[sidx]\n\t"
        "s_waitcnt lgkmcnt(3)\n\t"
        "v_smfmac_f32_16x16x64_bf16 v[252:255], v[232:235], v[144:151], %[sidx]\n\t"
        "s_waitcnt lgkmcnt(2)\n\t"
        "v_smfmac_f32_16x16x64_bf16 v[252:255], v[236:239], v[152:159], %[sidx]\n\t"
        "s_waitcnt lgkmcnt(1)\n\t"
        "v_smfmac_f32_16x16x64_bf16 v[252:255], v[240:243], v[160:167], %[sidx]\n\t"
        "s_waitcnt lgkmcnt(0)\n\t"
        "v_smfmac_f32_16x16x64_bf16 v[252:255], v[244:247], v[168:175], %[sidx]\n\t"
        "s_nop 7\n\t"
        "v_add_f32 v252, v252, v254\n\t"
        "v_add_f32 v253, v253, v255\n\t"
        "v_cvt_pk_bf16_f32 v204, v252, v253\n\t"
        "ds_write_b64 %[kw], v[204:205]\n\t"
        "v_cmp_u_f32 vcc, v252, v253\n\t"
        "s_cbranch_vccnz Lnan%=\n\t"
        "Lok%=:\n\t"
        "s_waitcnt lgkmcnt(0)\n\t"
        "s_barrier\n\t"
        "s_branch Lend%=\n\t"
        "Lnan%=:\n\t"
        "v_max_u32 v207, v208, v209\n\t"
        "v_max3_u32 v207, v207, v210, v211\n\t"
        "v_max3_u32 v207, v207, v212, v213\n\t"
        "v_max3_u32 v207, v207, v214, v215\n\t"
        "v_max3_u32 v207, v207, v216, v217\n\t"
        "v_max3_u32 v207, v207, v218, v219\n\t"
        "v_max3_u32 v207, v207, v220, v221\n\t"
        "v_max3_u32 v207, v207, v222, v223\n\t"
        "v_max3_u32 v207, v207, v224, v225\n\t"
        "v_max3_u32 v207, v207, v226, v227\n\t"
        "v_max3_u32 v207, v207, v228, v229\n\t"
        "v_max3_u32 v207, v207, v230, v231\n\t"
        "v_max3_u32 v207, v207, v232, v233\n\t"
        "v_max3_u32 v207, v207, v234, v235\n\t"
        "v_max3_u32 v207, v207, v236, v237\n\t"
        "v_max3_u32 v207, v207, v238, v239\n\t"
        "v_max3_u32 v207, v207, v240, v241\n\t"
        "v_max3_u32 v207, v207, v242, v243\n\t"
        "v_max3_u32 v207, v207, v244, v245\n\t"
        "v_max3_u32 v207, v207, v246, v247\n\t"
        "v_cmp_eq_u32 vcc, 0xffbfffbf, v207\n\t"
        "s_cbranch_vccz Lok%=\n\t"
        "s_mov_b32 %[redo], 1\n\t"
        "s_barrier\n\t"
        "s_branch Lend%=\n\t"
        "Lend%=:\n\t"
        : [redo] "+&s"(redo)
        : [w0] "{v[16:47]}"(W[0]), [w1] "{v[48:79]}"(W[1]), [w2] "{v[80:111]}"(W[2]), [w3] "{v[112:143]}"(W[3]), [w4] "{v[144:175]}"(W[4]), [va] "v"(va), [sidx] "v"(sidx), [qw] "v"(qw), [kw] "v"(kw), [fv] "s"(fv)
        : "v200", "v201", "v202", "v203", "v204", "v205", "v206", "v207", "v208", "v209", "v210", "v211", "v212", "v213", "v214", "v215", "v216", "v217", "v218", "v219", "v220", "v221", "v222", "v223", "v224", "v225", "v226", "v227", "v228", "v229", "v230", "v231", "v232", "v233", "v234", "v235", "v236", "v237", "v238", "v239", "v240", "v241", "v242", "v243", "v244", "v245", "v246", "v247", "v248", "v249", "v250", "v251", "v252", "v253", "v254", "v255", "vcc", "scc", "memory");
}

__device__ __forceinline__ void gather(u32 tg, u32 kr, u32 vm, u32 so, u32 lb, u32x4 rs) {
    asm volatile(
        "s_mov_b32 s94, m0\n\t"
        "s_add_u32 s92, %[so], 0x8000\n\t"
        "s_add_u32 s93, %[so], 0x10000\n\t"
        "s_mov_b32 m0, %[lb]\n\t"
        "Lda_%=:\n\t"
        "s_memrealtime s[90:91]\n\t"
        "s_waitcnt lgkmcnt(0)\n\t"
        "s_mul_i32 s90, s90, 24\n\t"
        "s_sub_u32 s88, %[tg], s90\n\t"
        "s_sub_u32 s89, s88, 620\n\t"
        "s_cmp_gt_u32 s89, 0x4000\n\t"
        "s_cbranch_scc1 Lea_%=\n\t"
        "s_sleep 1\n\t"
        "s_branch Lda_%=\n\t"
        "Lea_%=:\n\t"
        "s_cmp_lt_i32 s88, 120\n\t"
        "s_cbranch_scc1 Lza_%=\n\t"
        "s_memrealtime s[64:65]\n\t"
        "s_nop 0\n\t"
        "s_memrealtime s[66:67]\n\t"
        "s_nop 0\n\t"
        "s_memrealtime s[68:69]\n\t"
        "s_nop 0\n\t"
        "s_memrealtime s[70:71]\n\t"
        "s_nop 0\n\t"
        "s_memrealtime s[72:73]\n\t"
        "s_nop 0\n\t"
        "s_memrealtime s[74:75]\n\t"
        "s_memtime s[90:91]\n\t"
        "s_memrealtime s[76:77]\n\t"
        "s_nop 0\n\t"
        "s_memrealtime s[78:79]\n\t"
        "s_nop 0\n\t"
        "s_memrealtime s[80:81]\n\t"
        "s_nop 0\n\t"
        "s_memrealtime s[82:83]\n\t"
        "s_nop 0\n\t"
        "s_memrealtime s[84:85]\n\t"
        "s_nop 0\n\t"
        "s_memrealtime s[86:87]\n\t"
        "s_nop 0\n\t"
        "s_nop 7\n\t"
        "s_waitcnt lgkmcnt(0)\n\t"
        "s_add_u32 s64, s64, s66\n\t"
        "s_add_u32 s64, s64, s68\n\t"
        "s_add_u32 s64, s64, s70\n\t"
        "s_add_u32 s64, s64, s72\n\t"
        "s_add_u32 s64, s64, s74\n\t"
        "s_add_u32 s64, s64, s76\n\t"
        "s_add_u32 s64, s64, s78\n\t"
        "s_add_u32 s64, s64, s80\n\t"
        "s_add_u32 s64, s64, s82\n\t"
        "s_add_u32 s64, s64, s84\n\t"
        "s_add_u32 s64, s64, s86\n\t"
        "s_lshl_b32 s64, s64, 1\n\t"
        "s_nop 1\n\t"
        "s_sub_u32 s88, %[tg], s64\n\t"
        "s_mul_i32 s88, s88, %[kr]\n\t"
        "s_ashr_i32 s88, s88, 12\n\t"
        "s_sub_u32 s88, s88, 244\n\t"
        "s_nop 1\n\t"
        "s_max_i32 s88, s88, 0\n\t"
        "s_min_i32 s88, s88, 384\n\t"
        "s_andn2_b32 s88, s88, 3\n\t"
        "s_getpc_b64 s[90:91]\n\t"
        "Lga_%=:\n\t"
        "s_sub_u32 s90, s90, s88\n\t"
        "s_subb_u32 s91, s91, 0\n\t"
        "s_add_u32 s90, s90, Lza_%=-Lga_%=\n\t"
        "s_addc_u32 s91, s91, 0\n\t"
        "s_setpc_b64 s[90:91]\n\t"
        "s_nop 0\n\ts_nop 0\n\ts_nop 0\n\ts_nop 0\n\t"
        "s_nop 0\n\ts_nop 0\n\ts_nop 0\n\ts_nop 0\n\t"
        "s_nop 0\n\ts_nop 0\n\ts_nop 0\n\ts_nop 0\n\t"
        "s_nop 0\n\ts_nop 0\n\ts_nop 0\n\ts_nop 0\n\t"
        "s_nop 0\n\ts_nop 0\n\ts_nop 0\n\ts_nop 0\n\t"
        "s_nop 0\n\ts_nop 0\n\ts_nop 0\n\ts_nop 0\n\t"
        "s_nop 0\n\ts_nop 0\n\ts_nop 0\n\ts_nop 0\n\t"
        "s_nop 0\n\ts_nop 0\n\ts_nop 0\n\ts_nop 0\n\t"
        "s_nop 0\n\ts_nop 0\n\ts_nop 0\n\ts_nop 0\n\t"
        "s_nop 0\n\ts_nop 0\n\ts_nop 0\n\ts_nop 0\n\t"
        "s_nop 0\n\ts_nop 0\n\ts_nop 0\n\ts_nop 0\n\t"
        "s_nop 0\n\ts_nop 0\n\ts_nop 0\n\ts_nop 0\n\t"
        "s_nop 0\n\ts_nop 0\n\ts_nop 0\n\ts_nop 0\n\t"
        "s_nop 0\n\ts_nop 0\n\ts_nop 0\n\ts_nop 0\n\t"
        "s_nop 0\n\ts_nop 0\n\ts_nop 0\n\ts_nop 0\n\t"
        "s_nop 0\n\ts_nop 0\n\ts_nop 0\n\ts_nop 0\n\t"
        "s_nop 0\n\ts_nop 0\n\ts_nop 0\n\ts_nop 0\n\t"
        "s_nop 0\n\ts_nop 0\n\ts_nop 0\n\ts_nop 0\n\t"
        "s_nop 0\n\ts_nop 0\n\ts_nop 0\n\ts_nop 0\n\t"
        "s_nop 0\n\ts_nop 0\n\ts_nop 0\n\ts_nop 0\n\t"
        "s_nop 0\n\ts_nop 0\n\ts_nop 0\n\ts_nop 0\n\t"
        "s_nop 0\n\ts_nop 0\n\ts_nop 0\n\ts_nop 0\n\t"
        "s_nop 0\n\ts_nop 0\n\ts_nop 0\n\ts_nop 0\n\t"
        "s_nop 0\n\ts_nop 0\n\ts_nop 0\n\ts_nop 0\n\t"
        "Lza_%=:\n\t"
        "buffer_load_dwordx4 %[vm], %[rs], %[so] offen offset:0 sc1 lds\n\t"
        "buffer_load_dwordx4 %[vm], %[rs], %[so] offen offset:1024 sc1 lds\n\t"
        "buffer_load_dwordx4 %[vm], %[rs], %[so] offen offset:2048 sc1 lds\n\t"
        "buffer_load_dwordx4 %[vm], %[rs], %[so] offen offset:3072 sc1 lds\n\t"
        "s_add_u32 m0, m0, 0x1000\n\t"
        "s_nop 0\n\t"
        "buffer_load_dwordx4 %[vm], %[rs], s92 offen offset:0 sc1 lds\n\t"
        "buffer_load_dwordx4 %[vm], %[rs], s92 offen offset:1024 sc1 lds\n\t"
        "buffer_load_dwordx4 %[vm], %[rs], s92 offen offset:2048 sc1 lds\n\t"
        "buffer_load_dwordx4 %[vm], %[rs], s92 offen offset:3072 sc1 lds\n\t"
        "s_add_u32 m0, m0, 0x1000\n\t"
        "s_nop 0\n\t"
        "buffer_load_dwordx4 %[vm], %[rs], s93 offen offset:0 sc1 lds\n\t"
        "buffer_load_dwordx4 %[vm], %[rs], s93 offen offset:2048 sc1 lds\n\t"
        "s_mov_b32 m0, s94\n\t"
        :
        : [rs] "s"(rs), [tg] "s"(tg), [kr] "s"(kr), [vm] "v"(vm), [so] "s"(so), [lb] "s"(lb)
        : "s64", "s65", "s66", "s67", "s68", "s69", "s70", "s71", "s72", "s73", "s74", "s75", "s76", "s77", "s78", "s79", "s80", "s81", "s82", "s83", "s84", "s85", "s86", "s87", "s88", "s89", "s90", "s91", "s92", "s93", "s94", "vcc", "scc", "memory");
}

__device__ __forceinline__ void calib(u32& c0, u32& r0) {
    asm volatile(
        "s_memrealtime s[64:65]\n\t"
        "s_nop 0\n\t"
        "s_memrealtime s[66:67]\n\t"
        "s_nop 0\n\t"
        "s_memrealtime s[68:69]\n\t"
        "s_nop 0\n\t"
        "s_memrealtime s[70:71]\n\t"
        "s_nop 0\n\t"
        "s_memrealtime s[72:73]\n\t"
        "s_nop 0\n\t"
        "s_memrealtime s[74:75]\n\t"
        "s_memtime s[90:91]\n\t"
        "s_memrealtime s[76:77]\n\t"
        "s_nop 0\n\t"
        "s_memrealtime s[78:79]\n\t"
        "s_nop 0\n\t"
        "s_memrealtime s[80:81]\n\t"
        "s_nop 0\n\t"
        "s_memrealtime s[82:83]\n\t"
        "s_nop 0\n\t"
        "s_memrealtime s[84:85]\n\t"
        "s_nop 0\n\t"
        "s_memrealtime s[86:87]\n\t"
        "s_nop 0\n\t"
        "s_nop 7\n\t"
        "s_waitcnt lgkmcnt(0)\n\t"
        "s_add_u32 s64, s64, s66\n\t"
        "s_add_u32 s64, s64, s68\n\t"
        "s_add_u32 s64, s64, s70\n\t"
        "s_add_u32 s64, s64, s72\n\t"
        "s_add_u32 s64, s64, s74\n\t"
        "s_add_u32 s64, s64, s76\n\t"
        "s_add_u32 s64, s64, s78\n\t"
        "s_add_u32 s64, s64, s80\n\t"
        "s_add_u32 s64, s64, s82\n\t"
        "s_add_u32 s64, s64, s84\n\t"
        "s_add_u32 s64, s64, s86\n\t"
        "s_lshl_b32 s64, s64, 1\n\t"
        "s_mov_b32 %[c0], s90\n\t"
        "s_mov_b32 %[r0], s64\n\t"
        : [c0] "=&s"(c0), [r0] "=&s"(r0)
        :
        : "s64", "s65", "s66", "s67", "s68", "s69", "s70", "s71", "s72", "s73", "s74", "s75", "s76", "s77", "s78", "s79", "s80", "s81", "s82", "s83", "s84", "s85", "s86", "s87", "s88", "s89", "s90", "s91", "vcc", "scc", "memory");
}

__device__ __forceinline__ void px_step(u32x4& p0, u32& p1, u32 ka, u32 qa, u32 ma, u32 ktr, float ce) {
    asm volatile(
        "ds_read_b128 v[240:243], %[ma] offset:0\n\t"
        "ds_read_b128 v[244:247], %[ma] offset:1024\n\t"
        "ds_read_b128 v[208:211], %[qa] offset:0\n\t"
        "ds_read_b128 v[224:227], %[ka] offset:0\n\t"
        "ds_read_b128 v[212:215], %[qa] offset:1024\n\t"
        "ds_read_b128 v[228:231], %[ka] offset:1024\n\t"
        "ds_read_b128 v[216:219], %[qa] offset:2048\n\t"
        "ds_read_b128 v[232:235], %[ka] offset:2048\n\t"
        "ds_read_b128 v[220:223], %[qa] offset:3072\n\t"
        "ds_read_b128 v[236:239], %[ka] offset:3072\n\t"
        "s_waitcnt lgkmcnt(6)\n\t"
        "v_mfma_f32_16x16x32_bf16 v[240:243], v[224:227], v[208:211], v[240:243]\n\t"
        "ds_read_b128 v[224:227], %[ka] offset:4096\n\t"
        "s_waitcnt lgkmcnt(5)\n\t"
        "v_mfma_f32_16x16x32_bf16 v[240:243], v[228:231], v[212:215], v[240:243]\n\t"
        "ds_read_b128 v[228:231], %[ka] offset:5120\n\t"
        "s_waitcnt lgkmcnt(4)\n\t"
        "v_mfma_f32_16x16x32_bf16 v[240:243], v[232:235], v[216:219], v[240:243]\n\t"
        "ds_read_b128 v[232:235], %[ka] offset:6144\n\t"
        "s_waitcnt lgkmcnt(3)\n\t"
        "v_mfma_f32_16x16x32_bf16 v[240:243], v[236:239], v[220:223], v[240:243]\n\t"
        "ds_read_b128 v[236:239], %[ka] offset:7168\n\t"
        "s_waitcnt lgkmcnt(3)\n\t"
        "v_mfma_f32_16x16x32_bf16 v[244:247], v[224:227], v[208:211], v[244:247]\n\t"
        "ds_read_b64_tr_b16 v[224:225], %[ktr] offset:0\n\t"
        "ds_read_b64_tr_b16 v[226:227], %[ktr] offset:768\n\t"
        "s_waitcnt lgkmcnt(4)\n\t"
        "v_mfma_f32_16x16x32_bf16 v[244:247], v[228:231], v[212:215], v[244:247]\n\t"
        "ds_read_b64_tr_b16 v[228:229], %[ktr] offset:1536\n\t"
        "ds_read_b64_tr_b16 v[230:231], %[ktr] offset:2304\n\t"
        "s_waitcnt lgkmcnt(5)\n\t"
        "v_mfma_f32_16x16x32_bf16 v[244:247], v[232:235], v[216:219], v[244:247]\n\t"
        "ds_read_b64_tr_b16 v[232:233], %[ktr] offset:3072\n\t"
        "ds_read_b64_tr_b16 v[234:235], %[ktr] offset:3840\n\t"
        "s_waitcnt lgkmcnt(6)\n\t"
        "v_mfma_f32_16x16x32_bf16 v[244:247], v[236:239], v[220:223], v[244:247]\n\t"
        "ds_read_b64_tr_b16 v[236:237], %[ktr] offset:4608\n\t"
        "ds_read_b64_tr_b16 v[238:239], %[ktr] offset:5376\n\t"
        "s_waitcnt lgkmcnt(6)\n\t"
        "v_mfma_f32_16x16x32_bf16 v[248:251], v[224:227], v[208:211], 0\n\t"
        "s_waitcnt lgkmcnt(4)\n\t"
        "v_mfma_f32_16x16x32_bf16 v[248:251], v[228:231], v[212:215], v[248:251]\n\t"
        "s_waitcnt lgkmcnt(2)\n\t"
        "v_mfma_f32_16x16x32_bf16 v[248:251], v[232:235], v[216:219], v[248:251]\n\t"
        "s_waitcnt lgkmcnt(0)\n\t"
        "v_mfma_f32_16x16x32_bf16 v[248:251], v[236:239], v[220:223], v[248:251]\n\t"
        "v_max3_f32 v253, v240, v241, v242\n\t"
        "v_max3_f32 v253, v253, v243, v244\n\t"
        "v_max3_f32 v253, v253, v245, v246\n\t"
        "v_max_f32 v253, v253, v247\n\t"
        "s_nop 3\n\t"
        "v_mul_f32 v248, %[ce], v248\n\t"
        "v_mul_f32 v249, %[ce], v249\n\t"
        "v_max3_f32 v253, v253, v248, v249\n\t"
        "v_mov_b32 v254, v253\n\t"
        "s_nop 1\n\t"
        "v_permlane16_swap_b32 v253, v254\n\t"
        "v_max_f32 v253, v253, v254\n\t"
        "v_mov_b32 v254, v253\n\t"
        "s_nop 1\n\t"
        "v_permlane32_swap_b32 v253, v254\n\t"
        "v_max_f32 v253, v253, v254\n\t"
        "v_sub_f32 v240, v240, v253\n\t"
        "v_sub_f32 v241, v241, v253\n\t"
        "v_sub_f32 v242, v242, v253\n\t"
        "v_sub_f32 v243, v243, v253\n\t"
        "v_sub_f32 v244, v244, v253\n\t"
        "v_sub_f32 v245, v245, v253\n\t"
        "v_sub_f32 v246, v246, v253\n\t"
        "v_sub_f32 v247, v247, v253\n\t"
        "v_sub_f32 v248, v248, v253\n\t"
        "v_sub_f32 v249, v249, v253\n\t"
        "v_exp_f32 v240, v240\n\t"
        "v_exp_f32 v241, v241\n\t"
        "v_exp_f32 v242, v242\n\t"
        "v_exp_f32 v243, v243\n\t"
        "v_exp_f32 v244, v244\n\t"
        "v_exp_f32 v245, v245\n\t"
        "v_exp_f32 v246, v246\n\t"
        "v_exp_f32 v247, v247\n\t"
        "v_exp_f32 v248, v248\n\t"
        "v_exp_f32 v249, v249\n\t"
        "v_pk_add_f32 v[252:253], v[240:241], v[242:243]\n\t"
        "v_pk_add_f32 v[254:255], v[244:245], v[246:247]\n\t"
        "v_pk_add_f32 v[252:253], v[252:253], v[248:249]\n\t"
        "v_pk_add_f32 v[252:253], v[252:253], v[254:255]\n\t"
        "v_add_f32 v252, v252, v253\n\t"
        "v_mov_b32 v253, v252\n\t"
        "s_nop 1\n\t"
        "v_permlane16_swap_b32 v252, v253\n\t"
        "v_add_f32 v252, v252, v253\n\t"
        "v_mov_b32 v253, v252\n\t"
        "s_nop 1\n\t"
        "v_permlane32_swap_b32 v252, v253\n\t"
        "v_add_f32 v252, v252, v253\n\t"
        "v_rcp_f32 v252, v252\n\t"
        "s_nop 0\n\t"
        "v_pk_mul_f32 v[240:241], v[240:241], v[252:253] op_sel_hi:[1,0]\n\t"
        "v_pk_mul_f32 v[242:243], v[242:243], v[252:253] op_sel_hi:[1,0]\n\t"
        "v_pk_mul_f32 v[244:245], v[244:245], v[252:253] op_sel_hi:[1,0]\n\t"
        "v_pk_mul_f32 v[246:247], v[246:247], v[252:253] op_sel_hi:[1,0]\n\t"
        "v_pk_mul_f32 v[248:249], v[248:249], v[252:253] op_sel_hi:[1,0]\n\t"
        "v_cvt_pk_bf16_f32 v192, v240, v241\n\t"
        "v_cvt_pk_bf16_f32 v193, v242, v243\n\t"
        "v_cvt_pk_bf16_f32 v194, v244, v245\n\t"
        "v_cvt_pk_bf16_f32 v195, v246, v247\n\t"
        "v_cvt_pk_bf16_f32 v196, v248, v249\n\t"
        : [p0] "=&{v[192:195]}"(p0), [p1] "=&{v196}"(p1)
        : [ka] "v"(ka), [qa] "v"(qa), [ma] "v"(ma), [ktr] "v"(ktr), [ce] "s"(ce)
        : "v200", "v201", "v202", "v203", "v204", "v205", "v206", "v207", "v208", "v209", "v210", "v211", "v212", "v213", "v214", "v215", "v216", "v217", "v218", "v219", "v220", "v221", "v222", "v223", "v224", "v225", "v226", "v227", "v228", "v229", "v230", "v231", "v232", "v233", "v234", "v235", "v236", "v237", "v238", "v239", "v240", "v241", "v242", "v243", "v244", "v245", "v246", "v247", "v248", "v249", "v250", "v251", "v252", "v253", "v254", "v255", "vcc", "scc", "memory");
}

__device__ __forceinline__ void v_a0(const u32x32 (&W)[5], u32 va, u32 sidx, u32 vw STP) {
    asm volatile(
        "v_mov_b64 v[252:253], 0\n\t"
        "v_mov_b64 v[254:255], 0\n\t"
        "s_setprio 2\n\t"
        "s_barrier\n\t"
        "ds_read_b128 v[208:211], %[va] offset:0\n\t"
        "ds_read_b128 v[212:215], %[va] offset:512\n\t"
        "ds_read_b128 v[216:219], %[va] offset:1024\n\t"
        "ds_read_b128 v[220:223], %[va] offset:1536\n\t"
        "ds_read_b128 v[224:227], %[va] offset:2048\n\t"
        "ds_read_b128 v[228:231], %[va] offset:2560\n\t"
        "s_waitcnt lgkmcnt(5)\n\t"
        "v_smfmac_f32_16x16x64_bf16 v[252:255], v[208:211], v[16:23], %[sidx]\n\t"
        "ds_read_b128 v[232:235], %[va] offset:3072\n\t"
        "s_waitcnt lgkmcnt(5)\n\t"
        "v_smfmac_f32_16x16x64_bf16 v[252:255], v[212:215], v[24:31], %[sidx]\n\t"
        "ds_read_b128 v[236:239], %[va] offset:3584\n\t"
        "s_waitcnt lgkmcnt(5)\n\t"
        "v_smfmac_f32_16x16x64_bf16 v[252:255], v[216:219], v[32:39], %[sidx]\n\t"
        "ds_read_b128 v[240:243], %[va] offset:4096\n\t"
        "s_waitcnt lgkmcnt(5)\n\t"
        "v_smfmac_f32_16x16x64_bf16 v[252:255], v[220:223], v[40:47], %[sidx]\n\t"
        "ds_read_b128 v[244:247], %[va] offset:4608\n\t"
        "s_waitcnt lgkmcnt(5)\n\t"
        "v_smfmac_f32_16x16x64_bf16 v[252:255], v[224:227], v[48:55], %[sidx]\n\t"
        "ds_read_b128 v[208:211], %[va] offset:5120\n\t"
        "s_waitcnt lgkmcnt(5)\n\t"
        "v_smfmac_f32_16x16x64_bf16 v[252:255], v[228:231], v[56:63], %[sidx]\n\t"
        "ds_read_b128 v[212:215], %[va] offset:5632\n\t"
        "s_waitcnt lgkmcnt(5)\n\t"
        "v_smfmac_f32_16x16x64_bf16 v[252:255], v[232:235], v[64:71], %[sidx]\n\t"
        "ds_read_b128 v[216:219], %[va] offset:6144\n\t"
        "s_waitcnt lgkmcnt(5)\n\t"
        "v_smfmac_f32_16x16x64_bf16 v[252:255], v[236:239], v[72:79], %[sidx]\n\t"
        "ds_read_b128 v[220:223], %[va] offset:6656\n\t"
        "s_waitcnt lgkmcnt(5)\n\t"
        "v_smfmac_f32_16x16x64_bf16 v[252:255], v[240:243], v[80:87], %[sidx]\n\t"
        "ds_read_b128 v[224:227], %[va] offset:7168\n\t"
        "s_waitcnt lgkmcnt(5)\n\t"
        "v_smfmac_f32_16x16x64_bf16 v[252:255], v[244:247], v[88:95], %[sidx]\n\t"
        "ds_read_b128 v[228:231], %[va] offset:7680\n\t"
        "s_waitcnt lgkmcnt(5)\n\t"
        "v_smfmac_f32_16x16x64_bf16 v[252:255], v[208:211], v[96:103], %[sidx]\n\t"
        "ds_read_b128 v[232:235], %[va] offset:8192\n\t"
        "s_waitcnt lgkmcnt(5)\n\t"
        "v_smfmac_f32_16x16x64_bf16 v[252:255], v[212:215], v[104:111], %[sidx]\n\t"
        "ds_read_b128 v[236:239], %[va] offset:8704\n\t"
        "s_waitcnt lgkmcnt(5)\n\t"
        "v_smfmac_f32_16x16x64_bf16 v[252:255], v[216:219], v[112:119], %[sidx]\n\t"
        "ds_read_b128 v[240:243], %[va] offset:10240\n\t"
        "s_waitcnt lgkmcnt(5)\n\t"
        "v_smfmac_f32_16x16x64_bf16 v[252:255], v[220:223], v[120:127], %[sidx]\n\t"
        "ds_read_b128 v[244:247], %[va] offset:10752\n\t"
        "s_waitcnt lgkmcnt(5)\n\t"
        "v_smfmac_f32_16x16x64_bf16 v[252:255], v[224:227], v[128:135], %[sidx]\n\t"
        "s_waitcnt lgkmcnt(4)\n\t"
        "v_smfmac_f32_16x16x64_bf16 v[252:255], v[228:231], v[136:143], %[sidx]\n\t"
        "s_waitcnt lgkmcnt(3)\n\t"
        "v_smfmac_f32_16x16x64_bf16 v[252:255], v[232:235], v[144:151], %[sidx]\n\t"
        "s_waitcnt lgkmcnt(2)\n\t"
        "v_smfmac_f32_16x16x64_bf16 v[252:255], v[236:239], v[152:159], %[sidx]\n\t"
        "s_waitcnt lgkmcnt(1)\n\t"
        "v_smfmac_f32_16x16x64_bf16 v[252:255], v[240:243], v[160:167], %[sidx]\n\t"
        "s_waitcnt lgkmcnt(0)\n\t"
        "v_smfmac_f32_16x16x64_bf16 v[252:255], v[244:247], v[168:175], %[sidx]\n\t"
        "s_nop 7\n\t"
        "v_cvt_pk_bf16_f32 v248, v252, v253\n\t"
        "v_cvt_pk_bf16_f32 v249, v254, v255\n\t"
        "ds_write_b64 %[vw], v[248:249]\n\t"
        "s_waitcnt lgkmcnt(0)\n\t"
        "s_barrier\n\t"
        "s_setprio 0\n\t"
        :
        : [w0] "{v[16:47]}"(W[0]), [w1] "{v[48:79]}"(W[1]), [w2] "{v[80:111]}"(W[2]), [w3] "{v[112:143]}"(W[3]), [w4] "{v[144:175]}"(W[4]), [va] "v"(va), [sidx] "v"(sidx), [vw] "v"(vw)
        : "v204", "v205", "v206", "v207", "v208", "v209", "v210", "v211", "v212", "v213", "v214", "v215", "v216", "v217", "v218", "v219", "v220", "v221", "v222", "v223", "v224", "v225", "v226", "v227", "v228", "v229", "v230", "v231", "v232", "v233", "v234", "v235", "v236", "v237", "v238", "v239", "v240", "v241", "v242", "v243", "v244", "v245", "v246", "v247", "v248", "v249", "v250", "v251", "v252", "v253", "v254", "v255", "vcc", "scc", "memory");
}

__device__ __forceinline__ void v_a1(const u32x32 (&W)[5], u32 va, u32 sidx, u32 vw, u32& left, u32& c0, u32& r0, u32 tg, u32 kr, u32 poll, u32 vm, u32 so, u32 lb, u32x4 rs STP) {
    asm volatile(
        "v_mov_b64 v[252:253], 0\n\t"
        "v_mov_b64 v[254:255], 0\n\t"
        "s_setprio 2\n\t"
        "s_mov_b32 s94, m0\n\t"
        "s_add_u32 s92, %[so], 0x8000\n\t"
        "s_add_u32 s93, %[so], 0x10000\n\t"
        "s_mov_b32 m0, %[lb]\n\t"
        "s_cmp_eq_u32 %[poll], 2\n\t"
        "s_cbranch_scc1 Lck%=\n\t"
        "s_cmp_eq_u32 %[poll], 0\n\t"
        "s_cbranch_scc1 Lpb%=\n\t"
        "Lda_%=:\n\t"
        "s_memrealtime s[90:91]\n\t"
        "s_waitcnt lgkmcnt(0)\n\t"
        "s_mul_i32 s90, s90, 24\n\t"
        "s_sub_u32 s88, %[tg], s90\n\t"
        "s_sub_u32 s89, s88, 620\n\t"
        "s_cmp_gt_u32 s89, 0x4000\n\t"
        "s_cbranch_scc1 Lea_%=\n\t"
        "s_sleep 1\n\t"
        "s_branch Lda_%=\n\t"
        "Lea_%=:\n\t"
        "s_memrealtime s[64:65]\n\t"
        "s_nop 0\n\t"
        "s_memrealtime s[66:67]\n\t"
        "s_nop 0\n\t"
        "s_memrealtime s[68:69]\n\t"
        "s_nop 0\n\t"
        "s_memrealtime s[70:71]\n\t"
        "s_nop 0\n\t"
        "s_memrealtime s[72:73]\n\t"
        "s_nop 0\n\t"
        "s_memrealtime s[74:75]\n\t"
        "s_memtime s[90:91]\n\t"
        "s_memrealtime s[76:77]\n\t"
        "s_nop 0\n\t"
        "s_memrealtime s[78:79]\n\t"
        "s_nop 0\n\t"
        "s_memrealtime s[80:81]\n\t"
        "s_nop 0\n\t"
        "s_memrealtime s[82:83]\n\t"
        "s_nop 0\n\t"
        "s_memrealtime s[84:85]\n\t"
        "s_nop 0\n\t"
        "s_memrealtime s[86:87]\n\t"
        "s_nop 0\n\t"
        "s_nop 7\n\t"
        "s_waitcnt lgkmcnt(0)\n\t"
        "s_add_u32 s64, s64, s66\n\t"
        "s_add_u32 s64, s64, s68\n\t"
        "s_add_u32 s64, s64, s70\n\t"
        "s_add_u32 s64, s64, s72\n\t"
        "s_add_u32 s64, s64, s74\n\t"
        "s_add_u32 s64, s64, s76\n\t"
        "s_add_u32 s64, s64, s78\n\t"
        "s_add_u32 s64, s64, s80\n\t"
        "s_add_u32 s64, s64, s82\n\t"
        "s_add_u32 s64, s64, s84\n\t"
        "s_add_u32 s64, s64, s86\n\t"
        "s_lshl_b32 s64, s64, 1\n\t"
        "s_mov_b32 %[c0], s90\n\t"
        "s_mov_b32 %[r0], s64\n\t"
        "s_sub_u32 s88, %[tg], s64\n\t"
        "s_mul_i32 s88, s88, %[kr]\n\t"
        "s_ashr_i32 s88, s88, 12\n\t"
        "s_sub_u32 s88, s88, 244\n\t"
        "s_nop 1\n\t"
        "s_max_i32 s88, s88, 0\n\t"
        "s_min_i32 s88, s88, 384\n\t"
        "s_andn2_b32 s88, s88, 3\n\t"
        "s_getpc_b64 s[90:91]\n\t"
        "Lga_%=:\n\t"
        "s_sub_u32 s90, s90, s88\n\t"
        "s_subb_u32 s91, s91, 0\n\t"
        "s_add_u32 s90, s90, Lza_%=-Lga_%=\n\t"
        "s_addc_u32 s91, s91, 0\n\t"
        "s_setpc_b64 s[90:91]\n\t"
        "s_nop 0\n\ts_nop 0\n\ts_nop 0\n\ts_nop 0\n\t"
        "s_nop 0\n\ts_nop 0\n\ts_nop 0\n\ts_nop 0\n\t"
        "s_nop 0\n\ts_nop 0\n\ts_nop 0\n\ts_nop 0\n\t"
        "s_nop 0\n\ts_nop 0\n\ts_nop 0\n\ts_nop 0\n\t"
        "s_nop 0\n\ts_nop 0\n\ts_nop 0\n\ts_nop 0\n\t"
        "s_nop 0\n\ts_nop 0\n\ts_nop 0\n\ts_nop 0\n\t"
        "s_nop 0\n\ts_nop 0\n\ts_nop 0\n\ts_nop 0\n\t"
        "s_nop 0\n\ts_nop 0\n\ts_nop 0\n\ts_nop 0\n\t"
        "s_nop 0\n\ts_nop 0\n\ts_nop 0\n\ts_nop 0\n\t"
        "s_nop 0\n\ts_nop 0\n\ts_nop 0\n\ts_nop 0\n\t"
        "s_nop 0\n\ts_nop 0\n\ts_nop 0\n\ts_nop 0\n\t"
        "s_nop 0\n\ts_nop 0\n\ts_nop 0\n\ts_nop 0\n\t"
        "s_nop 0\n\ts_nop 0\n\ts_nop 0\n\ts_nop 0\n\t"
        "s_nop 0\n\ts_nop 0\n\ts_nop 0\n\ts_nop 0\n\t"
        "s_nop 0\n\ts_nop 0\n\ts_nop 0\n\ts_nop 0\n\t"
        "s_nop 0\n\ts_nop 0\n\ts_nop 0\n\ts_nop 0\n\t"
        "s_nop 0\n\ts_nop 0\n\ts_nop 0\n\ts_nop 0\n\t"
        "s_nop 0\n\ts_nop 0\n\ts_nop 0\n\ts_nop 0\n\t"
        "s_nop 0\n\ts_nop 0\n\ts_nop 0\n\ts_nop 0\n\t"
        "s_nop 0\n\ts_nop 0\n\ts_nop 0\n\ts_nop 0\n\t"
        "s_nop 0\n\ts_nop 0\n\ts_nop 0\n\ts_nop 0\n\t"
        "s_nop 0\n\ts_nop 0\n\ts_nop 0\n\ts_nop 0\n\t"
        "s_nop 0\n\ts_nop 0\n\ts_nop 0\n\ts_nop 0\n\t"
        "s_nop 0\n\ts_nop 0\n\ts_nop 0\n\ts_nop 0\n\t"
        "Lza_%=:\n\t"
        "buffer_load_dwordx4 %[vm], %[rs], %[so] offen offset:0 sc1 lds\n\t"
        "buffer_load_dwordx4 %[vm], %[rs], %[so] offen offset:1024 sc1 lds\n\t"
        "buffer_load_dwordx4 %[vm], %[rs], %[so] offen offset:2048 sc1 lds\n\t"
        "buffer_load_dwordx4 %[vm], %[rs], %[so] offen offset:3072 sc1 lds\n\t"
        "s_add_u32 m0, m0, 0x1000\n\t"
        "s_nop 0\n\t"
        "buffer_load_dwordx4 %[vm], %[rs], s92 offen offset:0 sc1 lds\n\t"
        "buffer_load_dwordx4 %[vm], %[rs], s92 offen offset:1024 sc1 lds\n\t"
        "buffer_load_dwordx4 %[vm], %[rs], s92 offen offset:2048 sc1 lds\n\t"
        "buffer_load_dwordx4 %[vm], %[rs], s92 offen offset:3072 sc1 lds\n\t"
        "s_add_u32 m0, m0, 0x1000\n\t"
        "s_nop 0\n\t"
        "buffer_load_dwordx4 %[vm], %[rs], s93 offen offset:0 sc1 lds\n\t"
        "buffer_load_dwordx4 %[vm], %[rs], s93 offen offset:2048 sc1 lds\n\t"
        "s_waitcnt vmcnt(0)\n\t"
        "Lpb%=:\n\t"
        "s_barrier\n\t"
        "ds_read_b128 v[208:211], %[va] offset:0\n\t"
        "ds_read_b128 v[212:215], %[va] offset:512\n\t"
        "ds_read_b128 v[216:219], %[va] offset:1024\n\t"
        "ds_read_b128 v[220:223], %[va] offset:1536\n\t"
        "ds_read_b128 v[224:227], %[va] offset:2048\n\t"
        "ds_read_b128 v[228:231], %[va] offset:2560\n\t"
        "s_waitcnt lgkmcnt(5)\n\t"
        "v_smfmac_f32_16x16x64_bf16 v[252:255], v[208:211], v[16:23], %[sidx]\n\t"
        "ds_read_b128 v[232:235], %[va] offset:3072\n\t"
        "s_waitcnt lgkmcnt(5)\n\t"
        "v_smfmac_f32_16x16x64_bf16 v[252:255], v[212:215], v[24:31], %[sidx]\n\t"
        "ds_read_b128 v[236:239], %[va] offset:3584\n\t"
        "s_waitcnt lgkmcnt(5)\n\t"
        "v_smfmac_f32_16x16x64_bf16 v[252:255], v[216:219], v[32:39], %[sidx]\n\t"
        "ds_read_b128 v[240:243], %[va] offset:4096\n\t"
        "s_waitcnt lgkmcnt(5)\n\t"
        "v_smfmac_f32_16x16x64_bf16 v[252:255], v[220:223], v[40:47], %[sidx]\n\t"
        "ds_read_b128 v[244:247], %[va] offset:4608\n\t"
        "s_waitcnt lgkmcnt(5)\n\t"
        "v_smfmac_f32_16x16x64_bf16 v[252:255], v[224:227], v[48:55], %[sidx]\n\t"
        "ds_read_b128 v[208:211], %[va] offset:5120\n\t"
        "s_waitcnt lgkmcnt(5)\n\t"
        "v_smfmac_f32_16x16x64_bf16 v[252:255], v[228:231], v[56:63], %[sidx]\n\t"
        "ds_read_b128 v[212:215], %[va] offset:5632\n\t"
        "s_waitcnt lgkmcnt(5)\n\t"
        "v_smfmac_f32_16x16x64_bf16 v[252:255], v[232:235], v[64:71], %[sidx]\n\t"
        "ds_read_b128 v[216:219], %[va] offset:6144\n\t"
        "s_waitcnt lgkmcnt(5)\n\t"
        "v_smfmac_f32_16x16x64_bf16 v[252:255], v[236:239], v[72:79], %[sidx]\n\t"
        "ds_read_b128 v[220:223], %[va] offset:6656\n\t"
        "s_waitcnt lgkmcnt(5)\n\t"
        "v_smfmac_f32_16x16x64_bf16 v[252:255], v[240:243], v[80:87], %[sidx]\n\t"
        "ds_read_b128 v[224:227], %[va] offset:7168\n\t"
        "s_waitcnt lgkmcnt(5)\n\t"
        "v_smfmac_f32_16x16x64_bf16 v[252:255], v[244:247], v[88:95], %[sidx]\n\t"
        "ds_read_b128 v[228:231], %[va] offset:7680\n\t"
        "s_waitcnt lgkmcnt(5)\n\t"
        "v_smfmac_f32_16x16x64_bf16 v[252:255], v[208:211], v[96:103], %[sidx]\n\t"
        "ds_read_b128 v[232:235], %[va] offset:8192\n\t"
        "s_waitcnt lgkmcnt(5)\n\t"
        "v_smfmac_f32_16x16x64_bf16 v[252:255], v[212:215], v[104:111], %[sidx]\n\t"
        "ds_read_b128 v[236:239], %[va] offset:8704\n\t"
        "s_waitcnt lgkmcnt(5)\n\t"
        "v_smfmac_f32_16x16x64_bf16 v[252:255], v[216:219], v[112:119], %[sidx]\n\t"
        "ds_read_b128 v[240:243], %[va] offset:10240\n\t"
        "s_waitcnt lgkmcnt(5)\n\t"
        "v_smfmac_f32_16x16x64_bf16 v[252:255], v[220:223], v[120:127], %[sidx]\n\t"
        "ds_read_b128 v[244:247], %[va] offset:10752\n\t"
        "s_waitcnt lgkmcnt(5)\n\t"
        "v_smfmac_f32_16x16x64_bf16 v[252:255], v[224:227], v[128:135], %[sidx]\n\t"
        "s_waitcnt lgkmcnt(4)\n\t"
        "v_smfmac_f32_16x16x64_bf16 v[252:255], v[228:231], v[136:143], %[sidx]\n\t"
        "s_waitcnt lgkmcnt(3)\n\t"
        "v_smfmac_f32_16x16x64_bf16 v[252:255], v[232:235], v[144:151], %[sidx]\n\t"
        "s_waitcnt lgkmcnt(2)\n\t"
        "v_smfmac_f32_16x16x64_bf16 v[252:255], v[236:239], v[152:159], %[sidx]\n\t"
        "s_waitcnt lgkmcnt(1)\n\t"
        "v_smfmac_f32_16x16x64_bf16 v[252:255], v[240:243], v[160:167], %[sidx]\n\t"
        "s_waitcnt lgkmcnt(0)\n\t"
        "v_smfmac_f32_16x16x64_bf16 v[252:255], v[244:247], v[168:175], %[sidx]\n\t"
        "s_nop 7\n\t"
        "v_cvt_pk_bf16_f32 v248, v252, v253\n\t"
        "v_cvt_pk_bf16_f32 v249, v254, v255\n\t"
        "ds_write_b64 %[vw], v[248:249]\n\t"
        "s_waitcnt lgkmcnt(0)\n\t"
        "s_barrier\n\t"
        "s_setprio 0\n\t"
        "s_branch Lend%=\n\t"
        "Lck%=:\n\t"
        "v_add_u32 v204, %[lb], %[vm]\n\t"
        "Lcl%=:\n\t"
        "s_mov_b32 m0, %[lb]\n\t"
        "s_nop 0\n\t"
        "buffer_load_dwordx4 %[vm], %[rs], %[so] offen offset:0 sc1 lds\n\t"
        "buffer_load_dwordx4 %[vm], %[rs], %[so] offen offset:1024 sc1 lds\n\t"
        "buffer_load_dwordx4 %[vm], %[rs], %[so] offen offset:2048 sc1 lds\n\t"
        "buffer_load_dwordx4 %[vm], %[rs], %[so] offen offset:3072 sc1 lds\n\t"
        "s_add_u32 m0, m0, 0x1000\n\t"
        "s_nop 0\n\t"
        "buffer_load_dwordx4 %[vm], %[rs], s92 offen offset:0 sc1 lds\n\t"
        "buffer_load_dwordx4 %[vm], %[rs], s92 offen offset:1024 sc1 lds\n\t"
        "buffer_load_dwordx4 %[vm], %[rs], s92 offen offset:2048 sc1 lds\n\t"
        "buffer_load_dwordx4 %[vm], %[rs], s92 offen offset:3072 sc1 lds\n\t"
        "s_add_u32 m0, m0, 0x1000\n\t"
        "s_nop 0\n\t"
        "buffer_load_dwordx4 %[vm], %[rs], s93 offen offset:0 sc1 lds\n\t"
        "buffer_load_dwordx4 %[vm], %[rs], s93 offen offset:2048 sc1 lds\n\t"
        "s_sub_u32 %[left], %[left], 1\n\t"
        "s_waitcnt vmcnt(9)\n\t"
        "ds_read_b128 v[208:211], v204 offset:0\n\t"
        "s_waitcnt vmcnt(8)\n\t"
        "ds_read_b128 v[212:215], v204 offset:1024\n\t"
        "s_waitcnt vmcnt(7)\n\t"
        "ds_read_b128 v[216:219], v204 offset:2048\n\t"
        "s_waitcnt vmcnt(6)\n\t"
        "ds_read_b128 v[220:223], v204 offset:3072\n\t"
        "s_waitcnt vmcnt(5)\n\t"
        "ds_read_b128 v[224:227], v204 offset:4096\n\t"
        "s_waitcnt vmcnt(4)\n\t"
        "ds_read_b128 v[228:231], v204 offset:5120\n\t"
        "s_waitcnt lgkmcnt(5)\n\t"
        "v_max3_u32 v207, v208, v208, v209\n\t"
        "v_max3_u32 v207, v207, v210, v211\n\t"
        "s_waitcnt vmcnt(3)\n\t"
        "ds_read_b128 v[232:235], v204 offset:6144\n\t"
        "s_waitcnt lgkmcnt(5)\n\t"
        "v_max3_u32 v207, v207, v212, v213\n\t"
        "v_max3_u32 v207, v207, v214, v215\n\t"
        "s_waitcnt vmcnt(2)\n\t"
        "ds_read_b128 v[236:239], v204 offset:7168\n\t"
        "s_waitcnt lgkmcnt(5)\n\t"
        "v_max3_u32 v207, v207, v216, v217\n\t"
        "v_max3_u32 v207, v207, v218, v219\n\t"
        "s_waitcnt vmcnt(1)\n\t"
        "ds_read_b128 v[240:243], v204 offset:8192\n\t"
        "s_waitcnt lgkmcnt(5)\n\t"
        "v_max3_u32 v207, v207, v220, v221\n\t"
        "v_max3_u32 v207, v207, v222, v223\n\t"
        "s_waitcnt vmcnt(0)\n\t"
        "ds_read_b128 v[244:247], v204 offset:10240\n\t"
        "s_waitcnt lgkmcnt(5)\n\t"
        "v_max3_u32 v207, v207, v224, v225\n\t"
        "v_max3_u32 v207, v207, v226, v227\n\t"
        "s_waitcnt lgkmcnt(4)\n\t"
        "v_max3_u32 v207, v207, v228, v229\n\t"
        "v_max3_u32 v207, v207, v230, v231\n\t"
        "s_waitcnt lgkmcnt(3)\n\t"
        "v_max3_u32 v207, v207, v232, v233\n\t"
        "v_max3_u32 v207, v207, v234, v235\n\t"
        "s_waitcnt lgkmcnt(2)\n\t"
        "v_max3_u32 v207, v207, v236, v237\n\t"
        "v_max3_u32 v207, v207, v238, v239\n\t"
        "s_waitcnt lgkmcnt(1)\n\t"
        "v_max3_u32 v207, v207, v240, v241\n\t"
        "v_max3_u32 v207, v207, v242, v243\n\t"
        "s_waitcnt lgkmcnt(0)\n\t"
        "v_max3_u32 v207, v207, v244, v245\n\t"
        "v_max3_u32 v207, v207, v246, v247\n\t"
        "v_cmp_eq_u32 vcc, 0xffbfffbf, v207\n\t"
        "s_cbranch_vccz Lpb%=\n\t"
        "s_cbranch_scc0 Lcl%=\n\t"
        "s_branch Lpb%=\n\t"
        "Lend%=:\n\t"
        "s_mov_b32 m0, s94\n\t"
        : [c0] "+&s"(c0), [r0] "+&s"(r0), [left] "+&s"(left)
        : [w0] "{v[16:47]}"(W[0]), [w1] "{v[48:79]}"(W[1]), [w2] "{v[80:111]}"(W[2]), [w3] "{v[112:143]}"(W[3]), [w4] "{v[144:175]}"(W[4]), [va] "v"(va), [sidx] "v"(sidx), [vw] "v"(vw), [rs] "s"(rs), [tg] "s"(tg), [kr] "s"(kr), [poll] "s"(poll), [vm] "v"(vm), [so] "s"(so), [lb] "s"(lb)
        : "s64", "s65", "s66", "s67", "s68", "s69", "s70", "s71", "s72", "s73", "s74", "s75", "s76", "s77", "s78", "s79", "s80", "s81", "s82", "s83", "s84", "s85", "s86", "s87", "s88", "s89", "s90", "s91", "s92", "s93", "s94", "v204", "v205", "v206", "v207", "v208", "v209", "v210", "v211", "v212", "v213", "v214", "v215", "v216", "v217", "v218", "v219", "v220", "v221", "v222", "v223", "v224", "v225", "v226", "v227", "v228", "v229", "v230", "v231", "v232", "v233", "v234", "v235", "v236", "v237", "v238", "v239", "v240", "v241", "v242", "v243", "v244", "v245", "v246", "v247", "v248", "v249", "v250", "v251", "v252", "v253", "v254", "v255", "vcc", "scc", "memory");
}

__device__ __forceinline__ void v_b(const u32x32 (&W)[5], u32& redo, u32 sidx, u32 vr, u32 vc, u32 z0, u32 z1, u32 z2, u32x4 rs, u32 lb, u32 fv, u32 fw STP) {
    asm volatile(
        "v_mov_b64 v[224:225], 0\n\t"
        "v_mov_b64 v[226:227], 0\n\t"
        "v_mov_b64 v[228:229], 0\n\t"
        "v_mov_b64 v[230:231], 0\n\t"
        "v_mov_b64 v[232:233], 0\n\t"
        "v_mov_b64 v[234:235], 0\n\t"
        "v_mov_b64 v[236:237], 0\n\t"
        "v_mov_b64 v[238:239], 0\n\t"
        "v_mov_b64 v[240:241], 0\n\t"
        "v_mov_b64 v[242:243], 0\n\t"
        "v_mov_b32 v205, %[fw]\n\t"
        "v_mov_b32 v206, %[lb]\n\t"
        "s_mov_b32 %[redo], 1\n\t"
        "s_barrier\n\t"
        "s_nop 15\n\t"
        "ds_read_b128 v[208:211], %[vc] offset:0\n\t"
        "ds_read_b128 v[212:215], %[vc] offset:4096\n\t"
        "ds_read_b128 v[216:219], %[vc] offset:8192\n\t"
        "s_waitcnt lgkmcnt(0)\n\t"
        "v_max3_u32 v207, v208, v209, v210\n\t"
        "v_max3_u32 v207, v207, v211, v212\n\t"
        "v_max3_u32 v207, v207, v213, v214\n\t"
        "v_max3_u32 v207, v207, v215, v216\n\t"
        "v_max3_u32 v207, v207, v217, v218\n\t"
        "v_max_u32 v207, v207, v219\n\t"
        "v_cmpx_eq_u32 vcc, 0xffbfffbf, v207\n\t"
        "ds_write_b32 v206, v205 offset:14336\n\t"
        "s_mov_b64 exec, -1\n\t"
        "s_setprio 2\n\t"
        "s_waitcnt lgkmcnt(0)\n\t"
        "s_barrier\n\t"
        "ds_read_b64_tr_b16 v[208:209], %[vr] offset:0\n\t"
        "ds_read_b64_tr_b16 v[210:211], %[vr] offset:64\n\t"
        "ds_read_b64_tr_b16 v[212:213], %[vr] offset:512\n\t"
        "ds_read_b64_tr_b16 v[214:215], %[vr] offset:576\n\t"
        "ds_read_b64_tr_b16 v[216:217], %[vr] offset:1024\n\t"
        "ds_read_b64_tr_b16 v[218:219], %[vr] offset:1088\n\t"
        "ds_read_b64_tr_b16 v[220:221], %[vr] offset:1536\n\t"
        "ds_read_b64_tr_b16 v[222:223], %[vr] offset:1600\n\t"
        "ds_read_b32 v207, v206 offset:14336\n\t"
        "s_waitcnt lgkmcnt(7)\n\t"
        "v_smfmac_f32_16x16x64_bf16 v[224:227], v[208:211], v[16:23], %[sidx]\n\t"
        "s_waitcnt lgkmcnt(5)\n\t"
        "v_smfmac_f32_16x16x64_bf16 v[224:227], v[212:215], v[24:31], %[sidx]\n\t"
        "s_waitcnt lgkmcnt(3)\n\t"
        "v_smfmac_f32_16x16x64_bf16 v[224:227], v[216:219], v[32:39], %[sidx]\n\t"
        "s_waitcnt lgkmcnt(1)\n\t"
        "v_smfmac_f32_16x16x64_bf16 v[224:227], v[220:223], v[40:47], %[sidx]\n\t"
        "v_smfmac_f32_16x16x64_bf16 v[228:231], v[208:211], v[48:55], %[sidx]\n\t"
        "s_waitcnt lgkmcnt(0)\n\t"
        "v_cmp_eq_u32 vcc, %[fv], v207\n\t"
        "v_smfmac_f32_16x16x64_bf16 v[228:231], v[212:215], v[56:63], %[sidx]\n\t"
        "v_cvt_pk_bf16_f32 v244, v224, v225\n\t"
        "v_smfmac_f32_16x16x64_bf16 v[228:231], v[216:219], v[64:71], %[sidx]\n\t"
        "v_cvt_pk_bf16_f32 v245, v226, v227\n\t"
        "v_smfmac_f32_16x16x64_bf16 v[228:231], v[220:223], v[72:79], %[sidx]\n\t"
        "s_cbranch_vccnz Lend%=\n\t"
        "v_smfmac_f32_16x16x64_bf16 v[232:235], v[208:211], v[80:87], %[sidx]\n\t"
        "v_smfmac_f32_16x16x64_bf16 v[232:235], v[212:215], v[88:95], %[sidx]\n\t"
        "v_cvt_pk_bf16_f32 v246, v228, v229\n\t"
        "v_smfmac_f32_16x16x64_bf16 v[232:235], v[216:219], v[96:103], %[sidx]\n\t"
        "v_cvt_pk_bf16_f32 v247, v230, v231\n\t"
        "v_smfmac_f32_16x16x64_bf16 v[232:235], v[220:223], v[104:111], %[sidx]\n\t"
        "v_permlane16_swap_b32 v244, v246\n\t"
        "v_smfmac_f32_16x16x64_bf16 v[236:239], v[208:211], v[112:119], %[sidx]\n\t"
        "v_permlane16_swap_b32 v245, v247\n\t"
        "buffer_store_dwordx4 v[244:247], %[z0], %[rs], 0 offen sc1\n\t"
        "v_smfmac_f32_16x16x64_bf16 v[236:239], v[212:215], v[120:127], %[sidx]\n\t"
        "v_cvt_pk_bf16_f32 v248, v232, v233\n\t"
        "v_smfmac_f32_16x16x64_bf16 v[236:239], v[216:219], v[128:135], %[sidx]\n\t"
        "v_cvt_pk_bf16_f32 v249, v234, v235\n\t"
        "v_smfmac_f32_16x16x64_bf16 v[236:239], v[220:223], v[136:143], %[sidx]\n\t"
        "v_smfmac_f32_16x16x64_bf16 v[240:243], v[208:211], v[144:151], %[sidx]\n\t"
        "v_smfmac_f32_16x16x64_bf16 v[240:243], v[212:215], v[152:159], %[sidx]\n\t"
        "v_cvt_pk_bf16_f32 v250, v236, v237\n\t"
        "v_smfmac_f32_16x16x64_bf16 v[240:243], v[216:219], v[160:167], %[sidx]\n\t"
        "v_cvt_pk_bf16_f32 v251, v238, v239\n\t"
        "v_smfmac_f32_16x16x64_bf16 v[240:243], v[220:223], v[168:175], %[sidx]\n\t"
        "v_permlane16_swap_b32 v248, v250\n\t"
        "v_permlane16_swap_b32 v249, v251\n\t"
        "buffer_store_dwordx4 v[248:251], %[z1], %[rs], 0 offen sc1\n\t"
        "s_nop 4\n\t"
        "v_cvt_pk_bf16_f32 v252, v240, v241\n\t"
        "v_cvt_pk_bf16_f32 v254, v240, v241\n\t"
        "v_cvt_pk_bf16_f32 v253, v242, v243\n\t"
        "v_cvt_pk_bf16_f32 v255, v242, v243\n\t"
        "s_nop 0\n\t"
        "v_permlane16_swap_b32 v252, v254\n\t"
        "v_permlane16_swap_b32 v253, v255\n\t"
        "buffer_store_dwordx4 v[252:255], %[z2], %[rs], 0 offen sc1\n\t"
        "s_mov_b32 %[redo], 0\n\t"
        "Lend%=:\n\t"
        "s_setprio 0\n\t"
        "s_nop 7\n\t"
        : [redo] "=&s"(redo)
        : [w0] "{v[16:47]}"(W[0]), [w1] "{v[48:79]}"(W[1]), [w2] "{v[80:111]}"(W[2]), [w3] "{v[112:143]}"(W[3]), [w4] "{v[144:175]}"(W[4]), [sidx] "v"(sidx), [vr] "v"(vr), [vc] "v"(vc), [z0] "v"(z0), [z1] "v"(z1), [z2] "v"(z2), [rs] "s"(rs), [lb] "s"(lb), [fv] "s"(fv), [fw] "s"(fw)
        : "v204", "v205", "v206", "v207", "v208", "v209", "v210", "v211", "v212", "v213", "v214", "v215", "v216", "v217", "v218", "v219", "v220", "v221", "v222", "v223", "v224", "v225", "v226", "v227", "v228", "v229", "v230", "v231", "v232", "v233", "v234", "v235", "v236", "v237", "v238", "v239", "v240", "v241", "v242", "v243", "v244", "v245", "v246", "v247", "v248", "v249", "v250", "v251", "v252", "v253", "v254", "v255", "vcc", "scc", "memory");
}

__device__ __forceinline__ void l_g(u32& miss, u32& c0, u32& r0, u64& tl, u32 tg, u32 kr, u32 vm, u32 so, u32x4 rs, u32x4 rec, u32 sr STP) {
    asm volatile(
        "s_add_u32 s92, %[so], 0x8000\n\t"
        "s_add_u32 s93, %[so], 0x10000\n\t"
        "s_mov_b32 %[miss], 1\n\t"
        "s_cmp_eq_u32 %[kr], 0\n\t"
        "s_cbranch_scc1 Lza_%=\n\t"
        "Lda_%=:\n\t"
        "s_memrealtime s[90:91]\n\t"
        "s_waitcnt lgkmcnt(0)\n\t"
        "s_mul_i32 s90, s90, 24\n\t"
        "s_sub_u32 s88, %[tg], s90\n\t"
        "s_sub_u32 s89, s88, 620\n\t"
        "s_cmp_gt_u32 s89, 0x4000\n\t"
        "s_cbranch_scc1 Lea_%=\n\t"
        "s_sleep 1\n\t"
        "s_branch Lda_%=\n\t"
        "Lea_%=:\n\t"
        "s_memrealtime s[64:65]\n\t"
        "s_nop 0\n\t"
        "s_memrealtime s[66:67]\n\t"
        "s_nop 0\n\t"
        "s_memrealtime s[68:69]\n\t"
        "s_nop 0\n\t"
        "s_memrealtime s[70:71]\n\t"
        "s_nop 0\n\t"
        "s_memrealtime s[72:73]\n\t"
        "s_nop 0\n\t"
        "s_memrealtime s[74:75]\n\t"
        "s_memtime s[90:91]\n\t"
        "s_memrealtime s[76:77]\n\t"
        "s_nop 0\n\t"
        "s_memrealtime s[78:79]\n\t"
        "s_nop 0\n\t"
        "s_memrealtime s[80:81]\n\t"
        "s_nop 0\n\t"
        "s_memrealtime s[82:83]\n\t"
        "s_nop 0\n\t"
        "s_memrealtime s[84:85]\n\t"
        "s_nop 0\n\t"
        "s_memrealtime s[86:87]\n\t"
        "s_nop 0\n\t"
        "s_nop 7\n\t"
        "s_waitcnt lgkmcnt(0)\n\t"
        "s_add_u32 s64, s64, s66\n\t"
        "s_add_u32 s64, s64, s68\n\t"
        "s_add_u32 s64, s64, s70\n\t"
        "s_add_u32 s64, s64, s72\n\t"
        "s_add_u32 s64, s64, s74\n\t"
        "s_add_u32 s64, s64, s76\n\t"
        "s_add_u32 s64, s64, s78\n\t"
        "s_add_u32 s64, s64, s80\n\t"
        "s_add_u32 s64, s64, s82\n\t"
        "s_add_u32 s64, s64, s84\n\t"
        "s_add_u32 s64, s64, s86\n\t"
        "s_lshl_b32 s64, s64, 1\n\t"
        "s_mov_b32 %[c0], s90\n\t"
        "s_mov_b32 %[r0], s64\n\t"
        "s_sub_u32 s88, %[tg], s64\n\t"
        "s_mul_i32 s88, s88, %[kr]\n\t"
        "s_ashr_i32 s88, s88, 12\n\t"
        "s_sub_u32 s88, s88, 244\n\t"
        "s_nop 1\n\t"
        "s_max_i32 s88, s88, 0\n\t"
        "s_min_i32 s88, s88, 384\n\t"
        "s_andn2_b32 s88, s88, 3\n\t"
        "s_getpc_b64 s[90:91]\n\t"
        "Lga_%=:\n\t"
        "s_sub_u32 s90, s90, s88\n\t"
        "s_subb_u32 s91, s91, 0\n\t"
        "s_add_u32 s90, s90, Lza_%=-Lga_%=\n\t"
        "s_addc_u32 s91, s91, 0\n\t"
        "s_setpc_b64 s[90:91]\n\t"
        "s_nop 0\n\ts_nop 0\n\ts_nop 0\n\ts_nop 0\n\t"
        "s_nop 0\n\ts_nop 0\n\ts_nop 0\n\ts_nop 0\n\t"
        "s_nop 0\n\ts_nop 0\n\ts_nop 0\n\ts_nop 0\n\t"
        "s_nop 0\n\ts_nop 0\n\ts_nop 0\n\ts_nop 0\n\t"
        "s_nop 0\n\ts_nop 0\n\ts_nop 0\n\ts_nop 0\n\t"
        "s_nop 0\n\ts_nop 0\n\ts_nop 0\n\ts_nop 0\n\t"
        "s_nop 0\n\ts_nop 0\n\ts_nop 0\n\ts_nop 0\n\t"
        "s_nop 0\n\ts_nop 0\n\ts_nop 0\n\ts_nop 0\n\t"
        "s_nop 0\n\ts_nop 0\n\ts_nop 0\n\ts_nop 0\n\t"
        "s_nop 0\n\ts_nop 0\n\ts_nop 0\n\ts_nop 0\n\t"
        "s_nop 0\n\ts_nop 0\n\ts_nop 0\n\ts_nop 0\n\t"
        "s_nop 0\n\ts_nop 0\n\ts_nop 0\n\ts_nop 0\n\t"
        "s_nop 0\n\ts_nop 0\n\ts_nop 0\n\ts_nop 0\n\t"
        "s_nop 0\n\ts_nop 0\n\ts_nop 0\n\ts_nop 0\n\t"
        "s_nop 0\n\ts_nop 0\n\ts_nop 0\n\ts_nop 0\n\t"
        "s_nop 0\n\ts_nop 0\n\ts_nop 0\n\ts_nop 0\n\t"
        "s_nop 0\n\ts_nop 0\n\ts_nop 0\n\ts_nop 0\n\t"
        "s_nop 0\n\ts_nop 0\n\ts_nop 0\n\ts_nop 0\n\t"
        "s_nop 0\n\ts_nop 0\n\ts_nop 0\n\ts_nop 0\n\t"
        "s_nop 0\n\ts_nop 0\n\ts_nop 0\n\ts_nop 0\n\t"
        "s_nop 0\n\ts_nop 0\n\ts_nop 0\n\ts_nop 0\n\t"
        "s_nop 0\n\ts_nop 0\n\ts_nop 0\n\ts_nop 0\n\t"
        "s_nop 0\n\ts_nop 0\n\ts_nop 0\n\ts_nop 0\n\t"
        "s_nop 0\n\ts_nop 0\n\ts_nop 0\n\ts_nop 0\n\t"
        "Lza_%=:\n\t"
        "buffer_load_dwordx4 v[16:19], %[vm], %[rs], %[so] offen offset:0 sc1\n\t"
        "buffer_load_dwordx4 v[20:23], %[vm], %[rs], %[so] offen offset:1024 sc1\n\t"
        "buffer_load_dwordx4 v[24:27], %[vm], %[rs], %[so] offen offset:2048 sc1\n\t"
        "buffer_load_dwordx4 v[28:31], %[vm], %[rs], %[so] offen offset:3072 sc1\n\t"
        "buffer_load_dwordx4 v[32:35], %[vm], %[rs], s92 offen offset:0 sc1\n\t"
        "buffer_load_dwordx4 v[36:39], %[vm], %[rs], s92 offen offset:1024 sc1\n\t"
        "buffer_load_dwordx4 v[40:43], %[vm], %[rs], s92 offen offset:2048 sc1\n\t"
        "buffer_load_dwordx4 v[44:47], %[vm], %[rs], s92 offen offset:3072 sc1\n\t"
        "buffer_load_dwordx4 v[48:51], %[vm], %[rs], s93 offen offset:0 sc1\n\t"
        "buffer_load_dwordx4 v[52:55], %[vm], %[rs], s93 offen offset:2048 sc1\n\t"
        "s_memrealtime %[tl]\n\t"
        "s_waitcnt vmcnt(9)\n\t"
        "v_max3_u32 v60, v16, v16, v17\n\t"
        "v_max3_u32 v60, v60, v18, v19\n\t"
        "s_waitcnt vmcnt(8)\n\t"
        "v_max3_u32 v60, v60, v20, v21\n\t"
        "v_max3_u32 v60, v60, v22, v23\n\t"
        "s_waitcnt vmcnt(7)\n\t"
        "v_max3_u32 v60, v60, v24, v25\n\t"
        "v_max3_u32 v60, v60, v26, v27\n\t"
        "s_waitcnt vmcnt(6)\n\t"
        "v_max3_u32 v60, v60, v28, v29\n\t"
        "v_max3_u32 v60, v60, v30, v31\n\t"
        "s_waitcnt vmcnt(5)\n\t"
        "v_max3_u32 v60, v60, v32, v33\n\t"
        "v_max3_u32 v60, v60, v34, v35\n\t"
        "s_waitcnt vmcnt(4)\n\t"
        "v_max3_u32 v60, v60, v36, v37\n\t"
        "v_max3_u32 v60, v60, v38, v39\n\t"
        "s_waitcnt vmcnt(3)\n\t"
        "v_max3_u32 v60, v60, v40, v41\n\t"
        "v_max3_u32 v60, v60, v42, v43\n\t"
        "s_waitcnt vmcnt(2)\n\t"
        "v_max3_u32 v60, v60, v44, v45\n\t"
        "v_max3_u32 v60, v60, v46, v47\n\t"
        "s_waitcnt vmcnt(1)\n\t"
        "v_max3_u32 v60, v60, v48, v49\n\t"
        "v_max3_u32 v60, v60, v50, v51\n\t"
        "s_waitcnt vmcnt(0)\n\t"
        "v_max3_u32 v60, v60, v52, v53\n\t"
        "v_max3_u32 v60, v60, v54, v55\n\t"
        "v_cmp_eq_u32 vcc, 0xffbfffbf, v60\n\t"
        "s_cbranch_vccnz Lend%=\n\t"
        "buffer_store_dwordx4 v[56:59], %[sr], %[rs], 0 offen sc1\n\t"
        "s_mov_b32 %[miss], 0\n\t"
        "Lend%=:\n\t"
        "s_waitcnt lgkmcnt(0)\n\t"
        : [c0] "+&s"(c0), [r0] "+&s"(r0), [tl] "=&s"(tl), [miss] "=&s"(miss)
        : [vm] "v"(vm), [sr] "v"(sr), [rs] "s"(rs), [so] "s"(so), [tg] "s"(tg), [kr] "s"(kr), [rec] "{v[56:59]}"(rec)
        : "s64", "s65", "s66", "s67", "s68", "s69", "s70", "s71", "s72", "s73", "s74", "s75", "s76", "s77", "s78", "s79", "s80", "s81", "s82", "s83", "s84", "s85", "s86", "s87", "s88", "s89", "s90", "s91", "s92", "s93", "v16", "v17", "v18", "v19", "v20", "v21", "v22", "v23", "v24", "v25", "v26", "v27", "v28", "v29", "v30", "v31", "v32", "v33", "v34", "v35", "v36", "v37", "v38", "v39", "v40", "v41", "v42", "v43", "v44", "v45", "v46", "v47", "v48", "v49", "v50", "v51", "v52", "v53", "v54", "v55", "v60", "vcc", "scc", "memory");
}

__device__ __forceinline__ void r_t(u32& c0, u32& r0, u32& t1, u32& t3, u32 kr, u32 fv, u32 lb, u32x4 rs, u32 so STP) {
    asm volatile(
        "s_setprio 3\n\t"
        "s_mov_b32 s92, 0x4000\n\t"
        "v_mov_b32 v20, %[lb]\n\t"
        "Lr%=:\n\t"
        "buffer_load_dwordx4 v[16:19], off, %[rs], %[so] sc1\n\t"
        "s_memrealtime s[64:65]\n\t"
        "s_nop 0\n\t"
        "s_memrealtime s[66:67]\n\t"
        "s_nop 0\n\t"
        "s_memrealtime s[68:69]\n\t"
        "s_nop 0\n\t"
        "s_memrealtime s[70:71]\n\t"
        "s_nop 0\n\t"
        "s_memrealtime s[72:73]\n\t"
        "s_nop 0\n\t"
        "s_memrealtime s[74:75]\n\t"
        "s_memtime s[90:91]\n\t"
        "s_memrealtime s[76:77]\n\t"
        "s_nop 0\n\t"
        "s_memrealtime s[78:79]\n\t"
        "s_nop 0\n\t"
        "s_memrealtime s[80:81]\n\t"
        "s_nop 0\n\t"
        "s_memrealtime s[82:83]\n\t"
        "s_nop 0\n\t"
        "s_memrealtime s[84:85]\n\t"
        "s_nop 0\n\t"
        "s_memrealtime s[86:87]\n\t"
        "s_nop 0\n\t"
        "s_nop 7\n\t"
        "s_waitcnt lgkmcnt(0)\n\t"
        "s_add_u32 s64, s64, s66\n\t"
        "s_add_u32 s64, s64, s68\n\t"
        "s_add_u32 s64, s64, s70\n\t"
        "s_add_u32 s64, s64, s72\n\t"
        "s_add_u32 s64, s64, s74\n\t"
        "s_add_u32 s64, s64, s76\n\t"
        "s_add_u32 s64, s64, s78\n\t"
        "s_add_u32 s64, s64, s80\n\t"
        "s_add_u32 s64, s64, s82\n\t"
        "s_add_u32 s64, s64, s84\n\t"
        "s_add_u32 s64, s64, s86\n\t"
        "s_lshl_b32 s64, s64, 1\n\t"
        "s_mov_b32 %[c0], s90\n\t"
        "s_mov_b32 %[r0], s64\n\t"
        "s_waitcnt vmcnt(0)\n\t"
        "v_readfirstlane_b32 %[t3], v19\n\t"
        "v_readfirstlane_b32 s94, v18\n\t"
        "v_readfirstlane_b32 s93, v16\n\t"
        "v_readfirstlane_b32 %[t1], v17\n\t"
        "s_cmp_eq_u32 %[t3], %[fv]\n\t"
        "s_cbranch_scc0 Lx%=\n\t"
        "Lgo%=:\n\t"
        "s_lshr_b32 s95, s94, 16\n\t"
        "s_and_b32 s94, s94, 0xffff\n\t"
        "s_add_u32 s93, s93, s94\n\t"
        "s_mul_i32 s95, s95, %[kr]\n\t"
        "s_lshr_b32 s95, s95, 12\n\t"
        "s_min_u32 s95, s95, 512\n\t"
        "s_andn2_b32 s95, s95, 3\n\t"
        "v_mov_b32 v21, s95\n\t"
        "ds_write_b32 v20, v21 offset:16384\n\t"
        "s_sub_u32 s95, s93, %[r0]\n\t"
        "s_mul_i32 s95, s95, %[kr]\n\t"
        "s_ashr_i32 s95, s95, 12\n\t"
        "s_add_u32 s95, s95, %[c0]\n\t"
        "Lda_%=:\n\t"
        "s_memtime s[90:91]\n\t"
        "s_waitcnt lgkmcnt(0)\n\t"
        "s_sub_u32 s88, s95, s90\n\t"
        "s_sub_u32 s89, s88, 336\n\t"
        "s_cmp_gt_u32 s89, 0x4000\n\t"
        "s_cbranch_scc1 Lea_%=\n\t"
        "s_sleep 1\n\t"
        "s_branch Lda_%=\n\t"
        "Lea_%=:\n\t"
        "s_sub_u32 s88, s88, 112\n\t"
        "s_max_i32 s88, s88, 0\n\t"
        "s_min_i32 s88, s88, 224\n\t"
        "s_andn2_b32 s88, s88, 3\n\t"
        "s_getpc_b64 s[90:91]\n\t"
        "Lga_%=:\n\t"
        "s_sub_u32 s90, s90, s88\n\t"
        "s_subb_u32 s91, s91, 0\n\t"
        "s_add_u32 s90, s90, Lza_%=-Lga_%=\n\t"
        "s_addc_u32 s91, s91, 0\n\t"
        "s_setpc_b64 s[90:91]\n\t"
        "s_nop 0\n\ts_nop 0\n\ts_nop 0\n\ts_nop 0\n\t"
        "s_nop 0\n\ts_nop 0\n\ts_nop 0\n\ts_nop 0\n\t"
        "s_nop 0\n\ts_nop 0\n\ts_nop 0\n\ts_nop 0\n\t"
        "s_nop 0\n\ts_nop 0\n\ts_nop 0\n\ts_nop 0\n\t"
        "s_nop 0\n\ts_nop 0\n\ts_nop 0\n\ts_nop 0\n\t"
        "s_nop 0\n\ts_nop 0\n\ts_nop 0\n\ts_nop 0\n\t"
        "s_nop 0\n\ts_nop 0\n\ts_nop 0\n\ts_nop 0\n\t"
        "s_nop 0\n\ts_nop 0\n\ts_nop 0\n\ts_nop 0\n\t"
        "s_nop 0\n\ts_nop 0\n\ts_nop 0\n\ts_nop 0\n\t"
        "s_nop 0\n\ts_nop 0\n\ts_nop 0\n\ts_nop 0\n\t"
        "s_nop 0\n\ts_nop 0\n\ts_nop 0\n\ts_nop 0\n\t"
        "s_nop 0\n\ts_nop 0\n\ts_nop 0\n\ts_nop 0\n\t"
        "s_nop 0\n\ts_nop 0\n\ts_nop 0\n\ts_nop 0\n\t"
        "s_nop 0\n\ts_nop 0\n\ts_nop 0\n\ts_nop 0\n\t"
        "Lza_%=:\n\t"
        "s_barrier\n\t"
        "s_setprio 0\n\t"
        "s_barrier\n\t"
        "s_branch Lend%=\n\t"
        "Lx%=:\n\t"
        "s_xor_b32 s95, %[t3], %[fv]\n\t"
        "s_cmp_eq_u32 s95, 0x10000\n\t"
        "s_cbranch_scc1 Lend%=\n\t"
        "s_sub_u32 s92, s92, 1\n\t"
        "s_cbranch_scc0 Lr%=\n\t"
        "s_mov_b32 %[t3], %[fv]\n\t"
        "s_branch Lgo%=\n\t"
        "Lend%=:\n\t"
        : [c0] "+&s"(c0), [r0] "+&s"(r0), [t1] "=&s"(t1), [t3] "=&s"(t3)
        : [rs] "s"(rs), [so] "s"(so), [kr] "s"(kr), [fv] "s"(fv), [lb] "s"(lb)
        : "s64", "s65", "s66", "s67", "s68", "s69", "s70", "s71", "s72", "s73", "s74", "s75", "s76", "s77", "s78", "s79", "s80", "s81", "s82", "s83", "s84", "s85", "s86", "s87", "s88", "s89", "s90", "s91", "s92", "s93", "s94", "s95", "v16", "v17", "v18", "v19", "v20", "v21", "vcc", "scc", "memory");
}

__device__ __forceinline__ void r_u(u32& left, const u32x32& bn, u32 sidx, u32 eo, u32 uw, u32x4 rs, u32 so, u32 lag STP) {
    asm volatile(
        "v_mov_b32 v80, 0xffbfffbf\n\t"
        "v_mov_b32 v81, v80\n\t"
        "s_mov_b32 s88, %[lag]\n\t"
        "s_cmp_lt_i32 s88, 0\n\t"
        "s_cbranch_scc1 Lg%=\n\t"
        "ds_write_b64 %[uw], v[80:81] offset:0\n\t"
        "ds_write_b64 %[uw], v[80:81] offset:512\n\t"
        "ds_write_b64 %[uw], v[80:81] offset:1024\n\t"
        "ds_write_b64 %[uw], v[80:81] offset:1536\n\t"
        "s_setprio 2\n\t"
        "s_cmp_eq_u32 s88, 0\n\t"
        "s_waitcnt lgkmcnt(0)\n\t"
        "s_barrier\n\t"
        "s_cbranch_scc1 Lg%=\n\t"
        "Ll%=:\n\t"
        "s_nop 5\n\t"
        "s_sub_u32 s88, s88, 1\n\t"
        "s_cmp_lg_u32 s88, 0\n\t"
        "s_cbranch_scc1 Ll%=\n\t"
        "Lg%=:\n\t"
        "buffer_load_dwordx4 v[48:51], %[eo], %[rs], %[so] offen offset:0 sc1\n\t"
        "buffer_load_dwordx4 v[52:55], %[eo], %[rs], %[so] offen offset:1024 sc1\n\t"
        "buffer_load_dwordx4 v[56:59], %[eo], %[rs], %[so] offen offset:2048 sc1\n\t"
        "buffer_load_dwordx4 v[60:63], %[eo], %[rs], %[so] offen offset:3072 sc1\n\t"
        "v_mov_b64 v[64:65], 0\n\t"
        "v_mov_b64 v[66:67], 0\n\t"
        "v_mov_b64 v[68:69], 0\n\t"
        "v_mov_b64 v[70:71], 0\n\t"
        "v_mov_b64 v[72:73], 0\n\t"
        "v_mov_b64 v[74:75], 0\n\t"
        "v_mov_b64 v[76:77], 0\n\t"
        "v_mov_b64 v[78:79], 0\n\t"
        "s_waitcnt vmcnt(3)\n\t"
        "v_smfmac_f32_16x16x64_bf16 v[64:67], v[48:51], v[16:23], %[sidx]\n\t"
        "s_waitcnt vmcnt(2)\n\t"
        "v_smfmac_f32_16x16x64_bf16 v[68:71], v[52:55], v[24:31], %[sidx]\n\t"
        "s_waitcnt vmcnt(1)\n\t"
        "v_smfmac_f32_16x16x64_bf16 v[72:75], v[56:59], v[32:39], %[sidx]\n\t"
        "v_cvt_pk_bf16_f32 v80, v64, v65\n\t"
        "v_cvt_pk_bf16_f32 v81, v66, v67\n\t"
        "ds_write_b64 %[uw], v[80:81] offset:0\n\t"
        "s_waitcnt vmcnt(0)\n\t"
        "v_smfmac_f32_16x16x64_bf16 v[76:79], v[60:63], v[40:47], %[sidx]\n\t"
        "v_cvt_pk_bf16_f32 v82, v68, v69\n\t"
        "v_cvt_pk_bf16_f32 v83, v70, v71\n\t"
        "ds_write_b64 %[uw], v[82:83] offset:512\n\t"
        "v_cvt_pk_bf16_f32 v84, v72, v73\n\t"
        "v_cvt_pk_bf16_f32 v85, v74, v75\n\t"
        "ds_write_b64 %[uw], v[84:85] offset:1024\n\t"
        "s_nop 1\n\t"
        "v_cvt_pk_bf16_f32 v86, v76, v77\n\t"
        "v_cvt_pk_bf16_f32 v87, v78, v79\n\t"
        "ds_write_b64 %[uw], v[86:87] offset:1536\n\t"
        "s_setprio 0\n\t"
        "s_sub_u32 %[left], %[left], 1\n\t"
        "v_max_u32 v88, v48, v49\n\t"
        "v_max3_u32 v88, v88, v50, v51\n\t"
        "v_max3_u32 v88, v88, v52, v53\n\t"
        "v_max3_u32 v88, v88, v54, v55\n\t"
        "v_max3_u32 v88, v88, v56, v57\n\t"
        "v_max3_u32 v88, v88, v58, v59\n\t"
        "v_max3_u32 v88, v88, v60, v61\n\t"
        "v_max3_u32 v88, v88, v62, v63\n\t"
        "v_cmp_eq_u32 vcc, 0xffbfffbf, v88\n\t"
        "s_cbranch_vccz Lend%=\n\t"
        "s_cbranch_scc0 Lg%=\n\t"
        "Lend%=:\n\t"
        "s_waitcnt lgkmcnt(0)\n\t"
        : [left] "+&s"(left)
        : [bn] "{v[16:47]}"(bn), [sidx] "v"(sidx), [eo] "v"(eo), [uw] "v"(uw), [rs] "s"(rs), [so] "s"(so), [lag] "s"(lag)
        : "s88", "v48", "v49", "v50", "v51", "v52", "v53", "v54", "v55", "v56", "v57", "v58", "v59", "v60", "v61", "v62", "v63", "v64", "v65", "v66", "v67", "v68", "v69", "v70", "v71", "v72", "v73", "v74", "v75", "v76", "v77", "v78", "v79", "v80", "v81", "v82", "v83", "v84", "v85", "v86", "v87", "v88", "vcc", "scc", "memory");
}

__device__ __forceinline__ void r_f(u32& bad, u32& pass, u32 sidx, u32 vm, u32 ra, u32 mo, u32x4 rs, u32 so, u32 sz, u32 sm, u32 lb, u32 fv, u32 cap STP) {
    asm volatile(
        "v_mov_b32 v120, %[lb]\n\t"
        "v_mov_b32 v121, %[fv]\n\t"
        "s_add_u32 s92, %[sm], 0x1000\n\t"
        "s_add_u32 s93, %[sm], 0x4000\n\t"
        "s_add_u32 s94, %[sm], 0x5000\n\t"
        "s_add_u32 s95, %[so], 0x8000\n\t"
        "s_add_u32 s96, %[sz], 0x1000\n\t"
        "s_mov_b32 s97, m0\n\t"
        "s_mov_b32 m0, %[lb]\n\t"
        "s_mov_b32 %[pass], 0\n\t"
        "s_mov_b32 %[bad], 0\n\t"
        "s_setprio 3\n\t"
        "s_waitcnt vmcnt(0)\n\t"
        "s_barrier\n\t"
        "ds_read_b32 v116, v120 offset:16384\n\t"
        "s_waitcnt lgkmcnt(0)\n\t"
        "v_readfirstlane_b32 s98, v116\n\t"
        "s_nop 3\n\t"
        "s_getpc_b64 s[90:91]\n\t"
        "Lgp_%=:\n\t"
        "s_sub_u32 s90, s90, s98\n\t"
        "s_subb_u32 s91, s91, 0\n\t"
        "s_add_u32 s90, s90, Lzp_%=-Lgp_%=\n\t"
        "s_addc_u32 s91, s91, 0\n\t"
        "s_setpc_b64 s[90:91]\n\t"
        "s_nop 0\n\ts_nop 0\n\ts_nop 0\n\ts_nop 0\n\t"
        "s_nop 0\n\ts_nop 0\n\ts_nop 0\n\ts_nop 0\n\t"
        "s_nop 0\n\ts_nop 0\n\ts_nop 0\n\ts_nop 0\n\t"
        "s_nop 0\n\ts_nop 0\n\ts_nop 0\n\ts_nop 0\n\t"
        "s_nop 0\n\ts_nop 0\n\ts_nop 0\n\ts_nop 0\n\t"
        "s_nop 0\n\ts_nop 0\n\ts_nop 0\n\ts_nop 0\n\t"
        "s_nop 0\n\ts_nop 0\n\ts_nop 0\n\ts_nop 0\n\t"
        "s_nop 0\n\ts_nop 0\n\ts_nop 0\n\ts_nop 0\n\t"
        "s_nop 0\n\ts_nop 0\n\ts_nop 0\n\ts_nop 0\n\t"
        "s_nop 0\n\ts_nop 0\n\ts_nop 0\n\ts_nop 0\n\t"
        "s_nop 0\n\ts_nop 0\n\ts_nop 0\n\ts_nop 0\n\t"
        "s_nop 0\n\ts_nop 0\n\ts_nop 0\n\ts_nop 0\n\t"
        "s_nop 0\n\ts_nop 0\n\ts_nop 0\n\ts_nop 0\n\t"
        "s_nop 0\n\ts_nop 0\n\ts_nop 0\n\ts_nop 0\n\t"
        "s_nop 0\n\ts_nop 0\n\ts_nop 0\n\ts_nop 0\n\t"
        "s_nop 0\n\ts_nop 0\n\ts_nop 0\n\ts_nop 0\n\t"
        "s_nop 0\n\ts_nop 0\n\ts_nop 0\n\ts_nop 0\n\t"
        "s_nop 0\n\ts_nop 0\n\ts_nop 0\n\ts_nop 0\n\t"
        "s_nop 0\n\ts_nop 0\n\ts_nop 0\n\ts_nop 0\n\t"
        "s_nop 0\n\ts_nop 0\n\ts_nop 0\n\ts_nop 0\n\t"
        "s_nop 0\n\ts_nop 0\n\ts_nop 0\n\ts_nop 0\n\t"
        "s_nop 0\n\ts_nop 0\n\ts_nop 0\n\ts_nop 0\n\t"
        "s_nop 0\n\ts_nop 0\n\ts_nop 0\n\ts_nop 0\n\t"
        "s_nop 0\n\ts_nop 0\n\ts_nop 0\n\ts_nop 0\n\t"
        "s_nop 0\n\ts_nop 0\n\ts_nop 0\n\ts_nop 0\n\t"
        "s_nop 0\n\ts_nop 0\n\ts_nop 0\n\ts_nop 0\n\t"
        "s_nop 0\n\ts_nop 0\n\ts_nop 0\n\ts_nop 0\n\t"
        "s_nop 0\n\ts_nop 0\n\ts_nop 0\n\ts_nop 0\n\t"
        "s_nop 0\n\ts_nop 0\n\ts_nop 0\n\ts_nop 0\n\t"
        "s_nop 0\n\ts_nop 0\n\ts_nop 0\n\ts_nop 0\n\t"
        "s_nop 0\n\ts_nop 0\n\ts_nop 0\n\ts_nop 0\n\t"
        "s_nop 0\n\ts_nop 0\n\ts_nop 0\n\ts_nop 0\n\t"
        "Lzp_%=:\n\t"
        "buffer_load_dwordx4 %[vm], %[rs], %[sz] offen offset:0 sc1 lds\n\t"
        "buffer_load_dwordx4 %[vm], %[rs], %[sz] offen offset:1024 sc1 lds\n\t"
        "buffer_load_dwordx4 %[vm], %[rs], %[sz] offen offset:2048 sc1 lds\n\t"
        "buffer_load_dwordx4 %[vm], %[rs], %[sz] offen offset:3072 sc1 lds\n\t"
        "s_add_u32 m0, m0, 0x1000\n\t"
        "s_nop 0\n\t"
        "buffer_load_dwordx4 %[vm], %[rs], s96 offen offset:0 sc1 lds\n\t"
        "buffer_load_dwordx4 %[vm], %[rs], s96 offen offset:1024 sc1 lds\n\t"
        "buffer_load_dwordx4 %[vm], %[rs], s96 offen offset:2048 sc1 lds\n\t"
        "buffer_load_dwordx4 %[vm], %[rs], s96 offen offset:3072 sc1 lds\n\t"
        "buffer_load_dwordx4 v[16:19], %[vm], %[rs], %[so] offen offset:0 sc1\n\t"
        "buffer_load_dwordx4 v[20:23], %[vm], %[rs], %[so] offen offset:2048 sc1\n\t"
        "buffer_load_dwordx4 v[24:27], %[vm], %[rs], %[so] offen offset:1024 sc1\n\t"
        "buffer_load_dwordx4 v[28:31], %[vm], %[rs], %[so] offen offset:3072 sc1\n\t"
        "buffer_load_dwordx4 v[32:35], %[vm], %[rs], s95 offen offset:0 sc1\n\t"
        "buffer_load_dwordx4 v[36:39], %[vm], %[rs], s95 offen offset:2048 sc1\n\t"
        "buffer_load_dwordx4 v[40:43], %[vm], %[rs], s95 offen offset:1024 sc1\n\t"
        "buffer_load_dwordx4 v[44:47], %[vm], %[rs], s95 offen offset:3072 sc1\n\t"
        "v_mov_b64 v[112:113], 0\n\t"
        "v_mov_b64 v[114:115], 0\n\t"
        "s_mov_b32 m0, %[lb]\n\t"
        "s_cmp_eq_u32 %[pass], 1\n\t"
        "s_cbranch_scc1 Lb%=\n\t"
        "Lc%=:\n\t"
        "s_waitcnt vmcnt(15)\n\t"
        "ds_read_b128 v[48:51], %[ra] offset:0\n\t"
        "ds_read_b128 v[52:55], %[ra] offset:512\n\t"
        "s_waitcnt vmcnt(14)\n\t"
        "ds_read_b128 v[56:59], %[ra] offset:1024\n\t"
        "ds_read_b128 v[60:63], %[ra] offset:1536\n\t"
        "s_waitcnt vmcnt(13)\n\t"
        "ds_read_b128 v[64:67], %[ra] offset:2048\n\t"
        "ds_read_b128 v[68:71], %[ra] offset:2560\n\t"
        "s_waitcnt vmcnt(12)\n\t"
        "ds_read_b128 v[72:75], %[ra] offset:3072\n\t"
        "ds_read_b128 v[76:79], %[ra] offset:3584\n\t"
        "s_waitcnt vmcnt(11)\n\t"
        "ds_read_b128 v[80:83], %[ra] offset:4096\n\t"
        "ds_read_b128 v[84:87], %[ra] offset:4608\n\t"
        "s_waitcnt vmcnt(10)\n\t"
        "ds_read_b128 v[88:91], %[ra] offset:5120\n\t"
        "ds_read_b128 v[92:95], %[ra] offset:5632\n\t"
        "s_waitcnt vmcnt(7) lgkmcnt(10)\n\t"
        "v_smfmac_f32_16x16x64_bf16 v[112:115], v[16:19], v[48:55], %[sidx]\n\t"
        "s_waitcnt vmcnt(6) lgkmcnt(8)\n\t"
        "v_smfmac_f32_16x16x64_bf16 v[112:115], v[20:23], v[56:63], %[sidx]\n\t"
        "ds_read_b128 v[96:99], %[ra] offset:6144\n\t"
        "ds_read_b128 v[100:103], %[ra] offset:6656\n\t"
        "s_waitcnt vmcnt(5) lgkmcnt(8)\n\t"
        "v_smfmac_f32_16x16x64_bf16 v[112:115], v[24:27], v[64:71], %[sidx]\n\t"
        "ds_read_b128 v[104:107], %[ra] offset:7168\n\t"
        "ds_read_b128 v[108:111], %[ra] offset:7680\n\t"
        "s_waitcnt vmcnt(4) lgkmcnt(8)\n\t"
        "v_smfmac_f32_16x16x64_bf16 v[112:115], v[28:31], v[72:79], %[sidx]\n\t"
        "s_waitcnt vmcnt(3) lgkmcnt(6)\n\t"
        "v_smfmac_f32_16x16x64_bf16 v[112:115], v[32:35], v[80:87], %[sidx]\n\t"
        "s_waitcnt vmcnt(2) lgkmcnt(4)\n\t"
        "v_smfmac_f32_16x16x64_bf16 v[112:115], v[36:39], v[88:95], %[sidx]\n\t"
        "s_waitcnt vmcnt(1) lgkmcnt(2)\n\t"
        "v_smfmac_f32_16x16x64_bf16 v[112:115], v[40:43], v[96:103], %[sidx]\n\t"
        "s_waitcnt vmcnt(0) lgkmcnt(0)\n\t"
        "v_smfmac_f32_16x16x64_bf16 v[112:115], v[44:47], v[104:111], %[sidx]\n\t"
        "s_nop 7\n\t"
        "v_add_f32 v116, v112, v114\n\t"
        "v_add_f32 v117, v113, v115\n\t"
        "v_add_f32 v118, v113, v115\n\t"
        "v_mov_b32_dpp v118, v116 row_ror:8 row_mask:0xf bank_mask:0x3\n\t"
        "v_mov_b32_dpp v116, v117 row_ror:8 row_mask:0xf bank_mask:0xc\n\t"
        "v_cvt_pk_bf16_f32 v119, v116, v118\n\t"
        "v_cmpx_o_f32 vcc, v116, v118\n\t"
        "buffer_store_dword v119, %[mo], %[rs], %[sm] offen sc1\n\t"
        "buffer_store_dword v119, %[mo], %[rs], s92 offen sc1\n\t"
        "buffer_store_dword v119, %[mo], %[rs], s93 offen sc1\n\t"
        "buffer_store_dword v119, %[mo], %[rs], s94 offen sc1\n\t"
        "s_nop 2\n\t"
        "s_cmp_eq_u64 exec, -1\n\t"
        "s_mov_b64 exec, -1\n\t"
        "s_cbranch_scc1 Lend%=\n\t"
        "s_add_u32 %[pass], %[pass], 1\n\t"
        "s_cmp_eq_u32 %[pass], 1\n\t"
        "s_cbranch_scc1 Lzp_%=\n\t"
        "v_max_u32 v116, v16, v17\n\t"
        "v_max3_u32 v116, v116, v18, v19\n\t"
        "v_max3_u32 v116, v116, v20, v21\n\t"
        "v_max3_u32 v116, v116, v22, v23\n\t"
        "v_max3_u32 v116, v116, v24, v25\n\t"
        "v_max3_u32 v116, v116, v26, v27\n\t"
        "v_max3_u32 v116, v116, v28, v29\n\t"
        "v_max3_u32 v116, v116, v30, v31\n\t"
        "v_max3_u32 v116, v116, v32, v33\n\t"
        "v_max3_u32 v116, v116, v34, v35\n\t"
        "v_max3_u32 v116, v116, v36, v37\n\t"
        "v_max3_u32 v116, v116, v38, v39\n\t"
        "v_max3_u32 v116, v116, v40, v41\n\t"
        "v_max3_u32 v116, v116, v42, v43\n\t"
        "v_max3_u32 v116, v116, v44, v45\n\t"
        "v_max3_u32 v116, v116, v46, v47\n\t"
        "v_max3_u32 v116, v116, v48, v49\n\t"
        "v_max3_u32 v116, v116, v50, v51\n\t"
        "v_max3_u32 v116, v116, v52, v53\n\t"
        "v_max3_u32 v116, v116, v54, v55\n\t"
        "v_max3_u32 v116, v116, v56, v57\n\t"
        "v_max3_u32 v116, v116, v58, v59\n\t"
        "v_max3_u32 v116, v116, v60, v61\n\t"
        "v_max3_u32 v116, v116, v62, v63\n\t"
        "v_max3_u32 v116, v116, v64, v65\n\t"
        "v_max3_u32 v116, v116, v66, v67\n\t"
        "v_max3_u32 v116, v116, v68, v69\n\t"
        "v_max3_u32 v116, v116, v70, v71\n\t"
        "v_max3_u32 v116, v116, v72, v73\n\t"
        "v_max3_u32 v116, v116, v74, v75\n\t"
        "v_max3_u32 v116, v116, v76, v77\n\t"
        "v_max3_u32 v116, v116, v78, v79\n\t"
        "v_max3_u32 v116, v116, v80, v81\n\t"
        "v_max3_u32 v116, v116, v82, v83\n\t"
        "v_max3_u32 v116, v116, v84, v85\n\t"
        "v_max3_u32 v116, v116, v86, v87\n\t"
        "v_max3_u32 v116, v116, v88, v89\n\t"
        "v_max3_u32 v116, v116, v90, v91\n\t"
        "v_max3_u32 v116, v116, v92, v93\n\t"
        "v_max3_u32 v116, v116, v94, v95\n\t"
        "v_max3_u32 v116, v116, v96, v97\n\t"
        "v_max3_u32 v116, v116, v98, v99\n\t"
        "v_max3_u32 v116, v116, v100, v101\n\t"
        "v_max3_u32 v116, v116, v102, v103\n\t"
        "v_max3_u32 v116, v116, v104, v105\n\t"
        "v_max3_u32 v116, v116, v106, v107\n\t"
        "v_max3_u32 v116, v116, v108, v109\n\t"
        "v_max3_u32 v116, v116, v110, v111\n\t"
        "v_cmp_eq_u32 vcc, 0xffbfffbf, v116\n\t"
        "s_cmp_lt_u32 %[pass], %[cap]\n\t"
        "s_cbranch_vccz Lbad%=\n\t"
        "s_cbranch_scc1 Lzp_%=\n\t"
        "Lbad%=:\n\t"
        "s_mov_b32 %[bad], 1\n\t"
        "s_branch Lend%=\n\t"
        "Lb%=:\n\t"
        "ds_write_b32 v120, v121 offset:16392\n\t"
        "s_waitcnt lgkmcnt(0)\n\t"
        "s_barrier\n\t"
        "s_branch Lc%=\n\t"
        "Lend%=:\n\t"
        "s_mov_b32 m0, s97\n\t"
        "s_setprio 0\n\t"
        : [bad] "=&s"(bad), [pass] "=&s"(pass)
        : [sidx] "v"(sidx), [vm] "v"(vm), [ra] "v"(ra), [mo] "v"(mo), [rs] "s"(rs), [so] "s"(so), [sz] "s"(sz), [sm] "s"(sm), [lb] "s"(lb), [fv] "s"(fv), [cap] "s"(cap)
        : "s88", "s89", "s90", "s91", "s92", "s93", "s94", "s95", "s96", "s97", "s98", "v16", "v17", "v18", "v19", "v20", "v21", "v22", "v23", "v24", "v25", "v26", "v27", "v28", "v29", "v30", "v31", "v32", "v33", "v34", "v35", "v36", "v37", "v38", "v39", "v40", "v41", "v42", "v43", "v44", "v45", "v46", "v47", "v48", "v49", "v50", "v51", "v52", "v53", "v54", "v55", "v56", "v57", "v58", "v59", "v60", "v61", "v62", "v63", "v64", "v65", "v66", "v67", "v68", "v69", "v70", "v71", "v72", "v73", "v74", "v75", "v76", "v77", "v78", "v79", "v80", "v81", "v82", "v83", "v84", "v85", "v86", "v87", "v88", "v89", "v90", "v91", "v92", "v93", "v94", "v95", "v96", "v97", "v98", "v99", "v100", "v101", "v102", "v103", "v104", "v105", "v106", "v107", "v108", "v109", "v110", "v111", "v112", "v113", "v114", "v115", "v116", "v117", "v118", "v119", "v120", "v121", "vcc", "scc", "memory");
}

#define TP0 231
#define TP1 20
#define TP2 166
#define TP3 4
#define TP4 12
#define TP5 184
#define TP6 105
#define TP7 248
#define TP8 16
#define TP9 20
#define TP10 2
#define TP11 4
#define TP12 16
#define TP13 100
#define TP14 64
#define TP15 4
#define WAHEAD 2
#define EARLY_N 35
#define BUMP_N 3
#define BUMP_WIN 0xffu
#define BUMP_S0 8
#define BUMP_MAX 3
#define TV(idx) ((u32)(TP##idx) * 8u)
#define TLOAD() do { } while (0)

__device__ __forceinline__ void nap(u32 n) {
    u32 hi = n >> 6, lo = (n >> 4) & 3u;
    asm volatile("s_cmp_eq_u32 %0, 0\n\ts_cbranch_scc1 Ln%=\nLm%=:\n\ts_sleep 1\n\ts_sub_u32 %0, %0, 1\n\ts_cmp_lg_u32 %0, 0\n\ts_cbranch_scc1 Lm%=\nLn%=:\n\t"
                 "s_cmp_eq_u32 %1, 0\n\ts_cbranch_scc1 Lp%=\nLo%=:\n\ts_sub_u32 %1, %1, 1\n\ts_cmp_lg_u32 %1, 0\n\ts_cbranch_scc1 Lo%=\nLp%=:"
                 : "+s"(hi), "+s"(lo) :: "scc", "memory");
}
typedef __attribute__((address_space(3))) u32* lds_u32p;
typedef __attribute__((address_space(3))) volatile u32* lds_vu32p;
typedef __attribute__((address_space(3))) u16* lds_u16p;
typedef __attribute__((address_space(3))) u32x2* lds_u32x2p;
typedef __attribute__((address_space(3))) u32x4* lds_u32x4p;
__device__ __forceinline__ u32 rfl(u32 x) { return __builtin_amdgcn_readfirstlane(x); }
__device__ __forceinline__ bool has_sent(u32x4 v) { return (v[0] == SENT) | (v[1] == SENT) | (v[2] == SENT) | (v[3] == SENT); }
__device__ __forceinline__ u32 now32() { return (u32)__builtin_amdgcn_s_memtime(); }
__device__ __forceinline__ u32 step_tag(int s) { return (u32)(s + 1) * 0x10001u; }

__device__ __forceinline__ void mem0_image(const u16* __restrict__ mem0, char* smem, const int tid) {
    for (int c = tid; c < 640; c += 512) {
        u32x4 v = *(const u32x4*)(mem0 + (c & 7) * D_ + (c >> 3) * 8);
#pragma unroll
        for (int i = 0; i < 4; ++i) v[i] = v[i] == SENT ? ~0u : v[i];
        ((u32x4*)smem)[c + (c >= 576 ? 64 : 0)] = v;
    }
}
__device__ __forceinline__ u32 once_more(u32& left, u32 lbase, u32* errw) {
    if (__builtin_expect(left != 0, 1)) { --left; return 2u; }
    atomicOr(errw, 1u);
    const int lane = lane_id();
    for (int k = 0; k < 11; ++k) *(lds_u32x4p)(lbase + (u32)(k * 1024 + lane * 16)) = (u32x4){0u, 0u, 0u, 0u};
    asm volatile("s_waitcnt lgkmcnt(0)" ::: "memory");
    return 0u;
}
constexpr u32 TS_MISS = 0x10000u, NLOOK = 1;
__device__ __forceinline__ u32 read_ts(rsrc_t rs, u32 off, int s, bool* ok = nullptr) {
    const u32 vo = (u32)(lane_id() & 1) * 128u;
    u32x4 t;
    u64 m = 0;
#pragma unroll 1
    for (u32 spins = 0; spins < 0x4000u; ++spins) {
        CFENCE();
        t = ldx4(rs, vo, off);
        m = __ballot(t[3] == (u32)s + 1u);
        if (m) break;
        __builtin_amdgcn_s_sleep(4);
    }
    if (ok) *ok = m != 0;
    return to_sgpr(__builtin_amdgcn_readlane(t[1], (int)__builtin_ctz((u32)m | 2u)));
}
__device__ __forceinline__ u32 read_ts_late(rsrc_t rs, u32 off, int s, bool* ok = nullptr) {
    const u32x4 t = ldx4(rs, 0u, off);
    if (rfl(t[3]) == (u32)s + 1u) { if (ok) *ok = true; return rfl(t[1]); }
    nap(1024);
    return read_ts(rs, off, s, ok);
}
__device__ __forceinline__ void until(u32 t) { for (int q = 0; q < 256 && (int)(t - (u32)__builtin_amdgcn_s_memrealtime() * 24u) > 0; ++q) __builtin_amdgcn_s_sleep(1); }
__device__ __forceinline__ u32 umin3(u32 a, u32 b, u32 c) { return min(min(a, b), c); }
__device__ __forceinline__ u32 dist(u32 d, u32x4 v, u32 k) { return umin3(umin3(d, v[0] ^ k, v[1] ^ k), v[2] ^ k, v[3] ^ k); }
#define COUNT(i) do { } while (0)
#define COUNT0() do { } while (0)
#define COUNT1(i) do { } while (0)
__device__ __forceinline__ void touch(u32x4 srd, u32 wp, int s, u32 lds) {
    u32 sv;
    const u32 off = slot_off(wp, s) + 3u * 32768u + ((~((wp >> 3) ^ ((u32)s >> 5)) & 1u) << 18);
    asm volatile("s_mov_b32 %0, m0\n\ts_mov_b32 m0, %3\n\ts_nop 3\n\tbuffer_load_dword off, %1, %2 lds\n\ts_mov_b32 m0, %0" : "=&s"(sv) : "s"(srd), "s"(off), "s"(lds) : "memory");
}
__device__ __forceinline__ void track_rate(u32 c0, u32 r0, u32& pc, u32& pr, u32& kr) {
    const u32 dr = r0 - pr;
    if (dr >= 6000u) {
        if (dr < 40000u) kr = to_sgpr(min(max((u32)(4096.f * (float)(c0 - pc) * __builtin_amdgcn_rcpf((float)dr) + 0.5f), 2560u), 4352u));
        pc = c0; pr = r0;
    }
}

#define LANES() const int lane = lane_id(), n = lane & 15, g = lane >> 4; (void)n; (void)g
constexpr u32 L_Q = 12288, L_K = 18432, L_E = 24576, L_DR = 25088, L_MSK = 25600, L_KQ = 32768;
constexpr float LIM_X = 32.f, LIM_D = 1.0995116e12f;
__device__ __forceinline__ u32 kq_buf(int sg) { return L_KQ + (u32)(sg % 3) * 16384u; }

__device__ __forceinline__ u32 p_pos(int row, int g, int dg) { return (u32)((row >> 1) * 64 + (g & 1) * 32 + (row & 1) * 16 + dg * 8 + (g >> 1) * 4); }
__device__ __forceinline__ void s_slow(const int h, const int s, rsrc_t rs, u32 so, u32 lbase) {
    const int lane = lane_id(), row = lane & 7, j = lane >> 3;
    const float ce = 0.08838834764831845f * 1.4426950408889634f;
    float sc[5] = {0.f, 0.f, 0.f, 0.f, 0.f};
    const u32 kx = lbase + kq_buf(s) + (u32)((j >> 2) * 4096 + (j & 3) * 64);
#pragma unroll 1
    for (int e = 0; e < DH_; ++e) {
        const u32 qa = lbase + L_Q + (u32)((e >> 4) * 768 + (row >> 2) * 192 + (e & 15) * 8 + (row & 3) * 2);
        const float q = bf2f(*(lds_u16p)qa) + bf2f(*(lds_u16p)(qa + 384));
        const u32 ka = kx + (u32)((e >> 5) * 1024 + ((e >> 2) & 3) * 256 + ((e >> 4) & 1) * 8 + (e & 3) * 2);
#pragma unroll
        for (int i = 0; i < 4; ++i) sc[i] += q * bf2f(*(lds_u16p)(ka + i * 16));
        sc[4] += q * bf2f(*(lds_u16p)(lbase + L_K + (u32)((e >> 4) * 768 + (j >> 1) * 192 + (e & 15) * 8 + (j & 1) * 2)));
    }
    sc[4] *= ce;
    const u32x2 ev = *(lds_u32x2p)(lbase + L_E + (u32)((j >> 1) * 128 + row * 16 + (j & 1) * 8));
    float se = (bf2f((u16)ev[0]) + bf2f((u16)(ev[0] >> 16))) + (bf2f((u16)ev[1]) + bf2f((u16)(ev[1] >> 16)));
    float mx = fmaxf(fmaxf(fmaxf(sc[0], sc[1]), fmaxf(sc[2], sc[3])), sc[4]);
#pragma unroll
    for (int o = 8; o < 64; o <<= 1) { se += __shfl_xor(se, o); mx = fmaxf(mx, __shfl_xor(mx, o)); }
    const bool rew = ev[0] == ~0u || ev[1] == ~0u;
    u64 rw = __ballot(rew);
    rw |= rw >> 32; rw |= rw >> 16; rw |= rw >> 8;
    const bool again = (rw >> row) & 1u;
    float al = exp2f(-mx);
    if (again) {
        const u32x2 en = {pack2(exp2f(sc[0] - mx), exp2f(sc[1] - mx)), pack2(exp2f(sc[2] - mx), exp2f(sc[3] - mx))};
        se = (bf2f((u16)en[0]) + bf2f((u16)(en[0] >> 16))) + (bf2f((u16)en[1]) + bf2f((u16)(en[1] >> 16)));
        al = 1.f;
    }
    float sn = se;
#pragma unroll
    for (int o = 8; o < 64; o <<= 1) sn += __shfl_xor(sn, o);
    if (again) se = sn;
    const float em = exp2f(sc[4] - mx);
    float sm = em;
#pragma unroll
    for (int o = 8; o < 64; o <<= 1) sm += __shfl_xor(sm, o);
    const float rd = 1.f / (sm + al * se);
    if (rw & 255u) {
#pragma unroll 1
        for (u32 spins = 0; spins < SPIN_CAP && !__all(*(lds_vu32p)(lbase + L_DR + (u32)(lane & 1) * 4) == step_tag(s)); ++spins) __builtin_amdgcn_s_sleep(1);
        const u32x2 en = {pack2(exp2f(sc[0] - mx), exp2f(sc[1] - mx)), pack2(exp2f(sc[2] - mx), exp2f(sc[3] - mx))};
        const u32 eo = again ? (u32)((h >> 3) * 32768 + ((h >> 1) & 3) * 1024 + (j >> 1) * 256 + (h & 1) * 128 + row * 16 + (j & 1) * 8) : OOB;
#pragma unroll
        for (int c = 0; c < NCP; ++c) stx2(rs, eo, en, so + cpy(c));
    }
    DRAIN();
    const u32 pm = pack2(em * rd, __shfl_xor(em * rd, 8));
    const int g = j >> 1;
    u32 dg = pack2(row == 2 * g ? al * rd : 0.f, row == 2 * g + 1 ? al * rd : 0.f);
    if (again && (row >> 1) == (g ^ 1)) dg = MARK;
    const int gj = h >> 2;
    const u32 po = SL_P + (u32)((gj >> 2) * 32768 + (gj & 1) * 2048 + ((gj >> 1) & 1) * 1024 + (h & 3) * 256) + p_pos(row, g, j & 1);
    const bool done = ldx1(rs, po, so) != SENT;
#pragma unroll
    for (int c = 0; c < NCP; ++c) stx1(rs, done ? OOB : po, (j & 1) ? dg : pm, so + cpy(c));
}

__device__ __forceinline__ void s_role(const int wave, const int h, const int xg, const u64 tag, const u16* __restrict__ mem0, const u32x32 (&W)[5],
                                       char* __restrict__ ws, char* smem) {
    const int lane = lane_id(), tid = wave * 64 + lane, n = lane & 15, g = lane >> 4;
    u32* errw = (u32*)(ws + OFF_ERR);
    const rsrc_t rs = mk_rsrc(ws);
    const u32x4 srd = mk_srd(ws);
    const u32 lbase = (u32)(uintptr_t)(__attribute__((address_space(3))) char*)smem;
    TLOAD();
    int wslot = 0; const int wid = 128 + h;
    WSTAMP(2);
    const u32 wp = wait_init(rs, ws, tag, lane);
    mem0_image(mem0, smem, tid);
    if (tid < 448) *(u32x4*)(smem + L_K + tid * 16) = (u32x4){0u, 0u, 0u, 0u};
    const u32 sidx = (n & 2) ? 0xEEEE : 0x4444;
    const u32 va = lbase + (u32)(g * 256 + (n & 2) * 64 + (n >> 2) * 32 + (n & 1) * 16);
    const u32 qw = lbase + L_Q + (u32)(wave * 768 + (g >> 1) * 192 + n * 8 + (g & 1) * 4);
    const u32 kw = lbase + L_K + (u32)(wave * 768 + g * 192 + n * 8);
    const u32 qtr = lbase + L_Q + (u32)((lane & 3) * 192 + (lane >> 2) * 8);
    const float ce = 0.08838834764831845f * 1.4426950408889634f;
    STAMPS();
    COUNT0();
#define S_LAST() do { u32 redo; do { redo = 0; s_l(W, redo, va, sidx, qw, kw, step_tag(NSEG - 1)); } while (__builtin_expect(redo, 0)); } while (0)

    if (wave < 2) {
        LANES();
        const u32 kxa = lbase + (u32)(wave * 4096 + lane * 16);
        const u32 ep = (u32)((n & 7) * 16 + (g & 1) * 8 + (n >> 3) * 4);
        const u32 eo = (u32)((h >> 3) * 32768 + ((h >> 1) & 3) * 1024 + (2 * wave + (g >> 1)) * 256 + (h & 1) * 128) + ep;
        const u32 ew = lbase + L_E + (u32)((2 * wave + (g >> 1)) * 128) + ep;
        const u32 ts = slot_v(wp, lane) + put_off(wp, 2u, lane), tk = kq_buf(lane);
        LBAR();
#pragma unroll 1
        for (int s = 0; s + 1 < NSEG; ++s) {
            u32 redo;
            do { redo = 0; s_e(W, redo, va, sidx, qw, kw, step_tag(s), kxa + tab(tk, s), qtr, eo, ew, LIM_X, srd, tab(ts, s) STA); } while (__builtin_expect(redo, 0));
            nap(256);
            DRAIN();
            *(lds_u32p)(lbase + L_DR + (u32)wave * 4) = step_tag(s);
            FLUSH();
        }
        S_LAST();
    } else if (wave == 2) {
        LANES();
        const int gj = h >> 2;
        const u32 po = SL_P + (u32)((gj >> 2) * 32768 + (gj & 1) * 2048 + ((gj >> 1) & 1) * 1024 + (h & 3) * 256) + p_pos(n & 7, g, n >> 3);
        const u32 er = lbase + L_E + (u32)(g * 128 + (n & 7) * 16);
        const u32x4 ones = {0x3f803f80u, 0x3f803f80u, 0x3f803f80u, 0x3f803f80u};
        const u32 ts = slot_v(wp, lane) + put_off(wp, 2u, lane);
        LBAR();
#pragma unroll 1
        for (int s = 0; s + 1 < NSEG; ++s) {
            const u32 so = tab(ts, s);
            u32 redo, bad;
            do {
                redo = 0;
                s_f(W, redo, va, sidx, qw, kw, step_tag(s), bad, ones, qtr, er, po, n < 8 ? 1.f : 0.f, n == 8 + 2 * g ? 1.f : 0.f, n == 9 + 2 * g ? 1.f : 0.f, srd, so, ce, LIM_D,
                    0xccccccccccccccccull STA);
            } while (__builtin_expect(redo, 0));
            if (__builtin_expect(bad, 0)) s_slow(h, s, rs, so, lbase);
            FLUSH();
        }
        S_LAST();
    } else if (wave == 3) {
        LANES();
        const u32 vm = (u32)lane * 16, cp = (u32)xg ^ (wp >> 1);
        LBAR();
        u32 c0, r0, kr = 3950;
        calib(c0, r0);
        const u32 tc = slot_v(wp ^ 8u, lane) + copy_off(cp, lane);
        u32 pc = c0, pr = r0, tg = 0, sc = 0, left = SPIN_CAP;
#pragma unroll 1
        for (int s = 0; s + 1 < NSEG; ++s) {
            const u32 so = sc;
            sc = tab(tc, s);
            u32 redo = 0, poll = ((u32)s + 63u) >> 6;
            u32 t1 = 0, tm = (u32)s + 1u;
            for (;;) {
                s_m(W, redo, va, sidx, qw, kw, step_tag(s), left, c0, r0, t1, tm, tg, kr, vm, so, lbase, srd, poll, (vm & 16u) * 8u, sc + SL_TS STA);
                if (__builtin_expect(left != ~0u, 1)) break;
                left = 0;
                once_more(left, lbase, errw);
                poll = 0;
            }
            if (redo) COUNT(0);
            tg = t1;
            if (__builtin_expect(!tm, 0)) tg = read_ts(rs, sc + SL_TS, s);
            *(lds_u32x2p)(lbase + L_DR + 8) = (u32x2){tg, (u32)s + 1u};
            track_rate(c0, r0, pc, pr, kr);
            FLUSH();
        }
        const u32 so = sc;
        for (u32 poll = 1;;) {
            if (poll) gather(tg, kr, vm, so, lbase, srd);
            u32 redo = 0;
            s_l(W, redo, va, sidx, qw, kw, step_tag(NSEG - 1));
            if (__builtin_expect(!redo, 1)) break;
            poll = once_more(left, lbase, errw);
        }
    } else if (wave < 6) {
        LANES();
        const int nt = wave & 1;
        f32x4* msk = (f32x4*)(smem + L_MSK) + nt * 128 + lane;
#pragma unroll
        for (int q = 0; q < 2; ++q) {
            f32x4 m;
#pragma unroll
            for (int i = 0; i < 4; ++i) m[i] = q * 16 + 4 * g + i <= nt * 16 + n ? 0.f : -__builtin_huge_valf();
            msk[q * 64] = m;
        }
        const u32 vl = lbase + (u32)lane * 16;
        const u32 o0 = (u32)OFF_PX + (u32)((h * 2 + nt) * 1024 + lane * 16);
        const u32 o1 = (u32)OFF_PXM + (u32)(((h >> 2) * 2 + nt) * 1024 + ((h & 3) * 16 + n) * 16 + g * 4);
        u32x4 p0 = {0u, 0u, 0u, 0u};
        u32 p1 = 0;
        LBAR();
#pragma unroll 1
        for (int s = 0; s < NSEG; ++s) {
            if (s + 1 < NSEG) {
                u32 redo = s == 0;
                do {
                    const u32 off = redo << 31;
                    redo = 0;
                    s_p1(W, redo, va, sidx, qw, kw, step_tag(s), p0, p1, (o0 + (u32)s * 65536 - 65536) | off, (o1 + (u32)s * 16384 - 16384) | off, srd STA);
                } while (__builtin_expect(redo, 0));
                nap(TV(8));
            } else {
                stx4(rs, o0 + (NSEG - 2) * 65536, p0);
                stx1(rs, o1 + (NSEG - 2) * 16384, p1);
                S_LAST();
                stx1(rs, lane == 0 ? (u32)(OFF_FP + (h >> 2) * 128 + ((h & 3) * 2 + nt) * 4) : OOB, 1u);
            }
            px_step(p0, p1, vl + kq_buf(s), vl + kq_buf(s) + 8192 + (u32)nt * 4096, vl + L_MSK + (u32)nt * 2048, qtr + (L_K - L_Q), ce);
            STAMP(3);
            FLUSH();
        }
        stx4(rs, o0 + (NSEG - 1) * 65536, p0);
        stx1(rs, o1 + (NSEG - 1) * 16384, p1);
        DRAIN();
        stx1(rs, lane == 0 ? (u32)(OFF_FZ + ((h >> 2) * 2 + nt) * 128 + (h & 3) * 4) : OOB, 1u);
    } else if (wave == 6) {
        LANES();
        u32 cap = SPIN_CAP;
        u32 have0 = 0, have1 = 0;
        auto wait_qk = [&](int sg) {
            const u32 need = (u32)(sg >> 1) + 1;
            u32 spins = 0;
#pragma unroll 1
            for (; ((sg & 1) ? have1 : have0) < need && spins < cap; ++spins) {
                CFENCE();
                const u32x2 f = ldx2(rs, 0u, (u32)(OFF_FA + h * 128));
                have0 = rfl(f[0]); have1 = rfl(f[1]);
                if (spins) __builtin_amdgcn_s_sleep(2);
            }
            if (spins >= cap) { atomicOr(errw, 8u); cap = 1; }
            CFENCE();
        };
        auto fetch = [&](u32 src, u32 dst) {
            const char* p = ws + far_off(wp ^ 1u, src + (u32)h * 8192) + lane * 16;
#pragma unroll
            for (int i = 0; i < 8; ++i) dma16_sc1(p + i * 1024, lbase + dst + i * 1024);
        };
        wait_qk(0);
        fetch(NEAR_KX, L_KQ);
        fetch(NEAR_QX, L_KQ + 8192);
        wait_qk(1);
        DRAIN();
        LBAR();
        u32 tk = (u32)__builtin_amdgcn_s_memrealtime() * 24u - 1024u;
#pragma unroll 1
        for (int s = 0; s + 1 < NSEG; ++s) {
            u32 redo, pfd = lbase + kq_buf(s + 1);
            do {
                redo = 0;
                s_p0(W, redo, va, sidx, qw, kw, step_tag(s), (u32)lane * 16, srd, far_off(wp ^ 1u, NEAR_KX + (u32)((s + 1) * H_ + h) * 8192), pfd, tk STA);
                pfd = 0;
            } while (__builtin_expect(redo, 0));
            nap(320);
            FLUSH();
            if (s + 2 < NSEG) {
                wait_qk(s + 2);
#pragma unroll 1
                for (int q = 0; q < 64 && rfl(*(lds_vu32p)(lbase + L_DR + 12)) != (u32)s + 1u; ++q) __builtin_amdgcn_s_sleep(1);
                tk = to_sgpr(*(lds_vu32p)(lbase + L_DR + 8)) + TV(4) - 1024u;
            }
        }
        S_LAST();
    } else {
        LANES();
        LBAR();
#pragma unroll 1
        for (int s = 0; s + 1 < NSEG; ++s) {
            u32 redo, pfd = lbase + kq_buf(s + 1) + 8192;
            do {
                redo = 0;
                s_p2(W, redo, va, sidx, qw, kw, step_tag(s), (u32)lane * 16, srd, far_off(wp ^ 1u, NEAR_QX + (u32)((s + 1) * H_ + h) * 8192), pfd STA);
                pfd = 0;
            } while (__builtin_expect(redo, 0));
            nap(384u + TV(9));
            touch(srd, wp, s + 1, lbase + 11264u);
            FLUSH();
        }
        S_LAST();
    }
    COUNT1(0);
}

__device__ __forceinline__ void v_role(const int wave, const int h, const int xg, const u64 tag, const u16* __restrict__ mem0, const u32x32 (&W)[5],
                                       char* __restrict__ ws, char* smem) {
    const int lane = lane_id(), tid = wave * 64 + lane, n = lane & 15, g = lane >> 4;
    u32* errw = (u32*)(ws + OFF_ERR);
    const rsrc_t rs = mk_rsrc(ws);
    const u32x4 srd = mk_srd(ws);
    const u32 lbase = (u32)(uintptr_t)(__attribute__((address_space(3))) char*)smem;
    TLOAD();
    int wslot = 0; const int wid = 160 + h;
    WSTAMP(2);
    const u32 wp = wait_init(rs, ws, tag, lane);
    mem0_image(mem0, smem, tid);
    *(lds_u32p)(lbase + 14336) = 0u;
    *(lds_u32p)(lbase + 9216 + (u32)((tid & 256) * 8 + (tid & 255) * 4)) = 0u;
    const u32 sidx = n >= 8 ? 0xEEEE : 0x4444;
    asm volatile("s_waitcnt vmcnt(0) lgkmcnt(0)" ::: "memory");
    STAMPS();
    COUNT0();
    u32 rnd = 1;
    if (wave < 4) {
        const u32 va = lbase + (u32)(g * 128 + (n & 7) * 16), vw = lbase + 12288 + (u32)((wave * 32 + (g >> 1) * 16 + n) * 16 + (g & 1) * 8);
        if (wave) {
#pragma unroll 1
            for (int s = 0; s < NSEG; ++rnd) {
                v_a0(W, va, sidx, vw STA);
                FLUSH();
                s += rfl(*(lds_vu32p)(lbase + 14336)) != rnd;
                if (wave == 1 && s < NSEG) touch(srd, wp, s, lbase + 15360u);
            }
        } else {
            const u32 tc = slot_v(wp ^ 8u, lane) + copy_off((u32)xg ^ (wp >> 1), lane);
            u32 c0, r0, kr = 3950;
            calib(c0, r0);
            u32 pc = c0, pr = r0, tg = 0, sc = 0, cap = SPIN_CAP;
#pragma unroll 1
            for (int s = 0; s < NSEG; ++s) {
                const u32 so = sc;
                sc = tab(tc, s);
                u32 look = 0;
#pragma unroll 1
                do {
                    u32 left = cap;
                    v_a1(W, va, sidx, vw, left, c0, r0, tg + TV(3), kr, s ? min(look + 1u, 2u) : 0u, (u32)lane * 16, so, lbase, srd STA);
                    if (__builtin_expect(left == ~0u || look >= 8u, 0)) { atomicOr(errw, 4u); cap = 1; }
                    ++look;
                } while (__builtin_expect(rfl(*(lds_vu32p)(lbase + 14336)) == rnd++, 0));
                if (look > 1) COUNT(3);
                if (s + 1 < NSEG) tg = read_ts(rs, sc + SL_TS, s);
                track_rate(c0, r0, pc, pr, kr);
                FLUSH();
            }
        }
    } else {
        const u32 vr = lbase + 12288 + (u32)((g * 8 + (n >> 2)) * 16 + (n & 1) * 8), vc = lbase + (u32)(tid & 255) * 16;
        u32 z[3];
#pragma unroll
        for (int p = 0; p < 3; ++p) z[p] = p == 2 && (g & 1) ? OOB : z_off(wp, (u32)((p * 2 + (g & 1)) * 8 + (wave & 3) * 2 + (g >> 1))) + (u32)(h * 256 + n * 16);
#pragma unroll 1
        for (int s = 0; s < NSEG; ++s) {
            const u32 sz = slot_off(wp, s);
            u32 look = 0, redo;
#pragma unroll 1
            do {
                v_b(W, redo, sidx, vr, vc, z[0] + sz, z[1] + sz, z[2] + sz, srd, lbase, rnd, look < 8u ? rnd : 0u STA);
                ++look; ++rnd;
            } while (__builtin_expect(redo, 0));
            FLUSH();
        }
    }
    DRAIN();
    LBAR();
    stx1(rs, wave == 0 && lane_id() < 2 ? (u32)(OFF_FZ + ((h >> 2) * 2 + lane_id()) * 128 + 16 + (h & 3) * 4) : OOB, 1u);
    COUNT1(3);
}

constexpr u32 L_U = 8192, L_T = 16384;
__device__ __forceinline__ void r_slow(const int s, rsrc_t rs, u32 so, u32 sz, u32 sm, u32 ra, u32 mo, u32 sidx, u32 lbase, u32& cap, u32* errw) {
    const int lane = lane_id(), n = lane & 15;
    u32 spins = 0;
    u32x4 p[8], z[8];
#pragma unroll 1
    for (; spins < cap; ++spins) {
        CFENCE();
        u32 d = 1;
#pragma unroll
        for (int j = 0; j < 8; ++j) p[j] = ldx4(rs, (u32)(lane * 16 + (j & 1) * 2048 + ((j >> 1) & 1) * 1024), so + (u32)(j >> 2) * 32768u);
#pragma unroll
        for (int j = 0; j < 8; ++j) z[j] = ldx4(rs, (u32)(lane * 16 + j * 1024), sz);
#pragma unroll
        for (int j = 0; j < 8; ++j) d = dist(dist(d, p[j], SENT), z[j], SENT);
        if (!__any(d == 0)) break;
    }
    if (spins >= cap) { atomicOr(errw, 2u); cap = 2; }
#pragma unroll
    for (int j = 0; j < 8; ++j) {
        *(lds_u32x4p)(lbase + (u32)(j * 1024 + lane * 16)) = z[j];
#pragma unroll
        for (int i = 0; i < 4; ++i) p[j][i] = p[j][i] == MARK ? 0u : p[j][i];
    }
    *(lds_vu32p)(lbase + L_T + 12) = step_tag(s);
    LBAR();
    LBAR();
    f32x4 acc = {0.f, 0.f, 0.f, 0.f};
#pragma unroll
    for (int j = 0; j < 8; ++j) acc = smf(p[j], *(lds_u32x4p)(ra + (u32)j * 1024), *(lds_u32x4p)(ra + (u32)j * 1024 + 512), acc, (int)sidx);
    const float x0 = acc[0] + acc[2], x1 = acc[1] + acc[3], o = __shfl_xor(n < 8 ? x1 : x0, 8);
    const u32 r = n < 8 ? pack2(x0, o) : pack2(o, x1);
#pragma unroll
    for (int c = 0; c < NCP; ++c) stx1(rs, mo, r, sm + cpy(c));
}

__device__ __forceinline__ void r_role(const int wave, const int ct, const int xg, const u64 tag, char* __restrict__ ws, char* smem) {
    const int lane = lane_id(), tid = wave * 64 + lane, n = lane & 15, g = lane >> 4;
    u32* errw = (u32*)(ws + OFF_ERR);
    const rsrc_t rs = mk_rsrc(ws);
    const u32x4 srd = mk_srd(ws);
    const u32 lbase = (u32)(uintptr_t)(__attribute__((address_space(3))) char*)smem;
    if (tid < 4) *(u32x4*)(smem + L_T + tid * 16) = (u32x4){0u, 0u, 0u, 0u};
    LBAR();
    if (wave >= 6) return;
    const u32 wp = wait_init(rs, ws, tag, lane);
    TLOAD();
    const u32 cp = (u32)xg ^ (wp >> 1);
    u32 cap = SPIN_CAP;
    STAMPS();
    COUNT0();
    if (wave == 0) {
        const int kp = ct >= 36 ? 10 : ct >> 2;
        const u32 mb = (u32)((kp >> 2) * 32768 + (kp & 3) * 1024 + (ct & 3) * 256);
        const u32 mo = mb + (u32)(((n & 7) >> 2) * 128 + (2 * g + (n >> 3)) * 16 + (n & 3) * 4);
        const u32 ra = lbase + (u32)((g & 1) * L_U + (g >> 1) * 256 + (n & 7) * 32 + (n >> 3) * 16), sidx = (n & 2) ? 0xEEEE : 0x4444;
        const u32 te = slot_v(wp, lane) + copy_off(cp, lane) + SL_P, tm = slot_v(wp ^ 8u, lane) + put_off(wp, 0u, lane), tz = slot_z(wp, lane) + z_off(wp, (u32)ct);
#pragma unroll 1
        for (int s = 0; s + 1 < NSEG; ++s) {
            const u32 so = tab(te, s), sm = tab(tm, s), sz = tab(tz, s);
            u32 bad, pass;
            r_f(bad, pass, sidx, (u32)lane * 16, ra, mo, srd, so, sz, sm, lbase, step_tag(s), cap STA);
            if (__builtin_expect(bad, 0)) r_slow(s, rs, so, sz, sm, ra, mo, sidx, lbase, cap, errw);
            LBAR();
            if (pass) COUNT(2);
            touch(srd, wp, s + 1, lbase + 17408u);
            FLUSH();
        }
    } else if (wave == 4) {
        u32 c0, r0, kr = 3950;
        calib(c0, r0);
        const u32 tc = slot_v(wp ^ 8u, lane) + copy_off(cp, lane) + SL_TS;
        u32 pc = c0, pr = r0, tg = r0 - 0x10000u;
#pragma unroll 1
        for (int s = 0; s + 1 < NSEG; ++s) {
            u32 t1, t3;
#pragma unroll 1
            for (u32 look = 0;; ++look) {
                until(tg + TV(6) + look * TV(7));
                r_t(c0, r0, t1, t3, kr, (u32)s + 1u, lbase, srd, tab(tc, s) + look * 128u STA);
                if (__builtin_expect(t3 == (u32)s + 1u, 1)) break;
                if (look == NLOOK) { LBAR(); LBAR(); break; }
            }
            tg = t1;
            if (__builtin_expect(rfl(*(lds_vu32p)(lbase + L_T + 8)) == step_tag(s), 0)) {
                LBAR();
                if (rfl(*(lds_vu32p)(lbase + L_T + 12)) == step_tag(s)) { LBAR(); LBAR(); }
            }
            track_rate(c0, r0, pc, pr, kr);
            FLUSH();
        }
    } else {
        const int q = wave == 5 ? 3 : wave - 1;
        u32 have = 0;
        u32x32 bn;
        auto load_b = [&](int sg) {
            u32 spins = 0;
#pragma unroll 1
            for (; have <= (u32)sg && spins < cap; ++spins) {
                CFENCE();
                const u32x2 f = ldx2(rs, (u32)(OFF_FB + (q * 8 + (lane & 7)) * 128));
                u32 a = min(2 * f[0], 2 * f[1] + 1);
                a = min(a, (u32)__builtin_amdgcn_update_dpp(0u, a, 0xb1, 0xf, 0xf, false));
                a = min(a, (u32)__builtin_amdgcn_update_dpp(0u, a, 0x4e, 0xf, 0xf, false));
                a = min(a, (u32)__builtin_amdgcn_update_dpp(0u, a, 0x124, 0xf, 0xf, false));
                have = rfl(a);
                if (spins) __builtin_amdgcn_s_sleep(4);
            }
            if (spins >= cap) { atomicOr(errw, 16u); cap = 1; }
            CFENCE();
            const u32 bs = far_off(wp, FAR_BX + (u32)(sg * NRED + ct) * 32768 + (u32)q * 8192);
            u32x4 t[8];
#pragma unroll
            for (int i = 0; i < 8; ++i) t[i] = ldx4(rs, (u32)(lane * 16 + i * 1024), bs);
#pragma unroll
            for (int i = 0; i < 8; ++i)
#pragma unroll
                for (int j = 0; j < 4; ++j) bn[(i >> 1) * 8 + j * 2 + (i & 1)] = t[i][j];
        };
        const u32 eo = (u32)(q * 32768 + lane * 16), te = slot_v(wp, lane) + copy_off(cp, lane), sidx = n >= 8 ? 0xEEEE : 0x4444;
        const u32 uw = lbase + L_U + (u32)(q * 2048 + (g >> 1) * 256 + n * 16 + (g & 1) * 8);
        load_b(0);
#pragma unroll 1
        for (int s = 0; s + 1 < NSEG; ++s) {
            const u32 so = tab(te, s);
            u32 left = cap;
            r_u(left, bn, sidx, eo, uw, srd, so, (u32)q STA);
            LBAR();
            if (cap - left > 1u) COUNT(1);
            if (__builtin_expect(rfl(*(lds_vu32p)(lbase + L_T + 8)) == step_tag(s), 0)) {
                if (left == ~0u) { atomicOr(errw, 4u); cap = 1; }
                LBAR();
                if (rfl(*(lds_vu32p)(lbase + L_T + 12)) == step_tag(s)) {
                    left = 0;
                    r_u(left, bn, sidx, eo, uw, srd, so, ~0u STA);
                    LBAR();
                    LBAR();
                }
            }
            if (s + 2 < NSEG) load_b(s + 1);
            FLUSH();
        }
    }
    COUNT1(wave ? 1 : 2);
}

__device__ __forceinline__ void relay_role(const u32 wp, const int xg, char* __restrict__ ws) {
    const int lane = lane_id();
    const rsrc_t rs = mk_rsrc(ws);
    const u32 tc = slot_v(wp ^ 8u, lane) + copy_off((u32)xg ^ (wp >> 1), lane) + SL_TS;
#pragma unroll 1
    for (int k = 0; k + 1 < NSEG; ++k) {
        u32 t;
#pragma unroll 1
        for (u32 spins = 0;; ++spins) {
            bool ok;
            t = read_ts_late(rs, tab(tc, k), k, &ok);
            if (ok) break;
            if (spins >= 16u || ldx1(rs, (u32)OFF_ERR) != 0u) return;
        }
        if (k >= 3) stx1(rs, lane < 8 ? (u32)(OFF_RDY + lane * 128) : OOB, (u32)k - 2u);
        for (int q = 0; q < 64 && (int)((u32)__builtin_amdgcn_s_memrealtime() * 24u - t) < 1400; ++q) __builtin_amdgcn_s_sleep(2);
    }
}

__device__ __forceinline__ void lead_role(const u32 wp, const int xg, char* __restrict__ ws, char* smem) {
    const int lane = lane_id();
    u32* errw = (u32*)(ws + OFF_ERR);
    const rsrc_t rs = mk_rsrc(ws);
    const u32x4 srd = mk_srd(ws);
    TLOAD();
    STAMPS();
    const u32 tsw = lane < 4 ? SL_TS + (u32)((lane & 1) * 4096 + (lane >> 1) * 16384) : OOB;
    const u32 tm = slot_v(wp ^ 8u, lane), tc = tm + copy_off((u32)xg ^ (wp >> 1), lane);
    {
        bool dead = false;
        wait_word(rs, (u32)(OFF_FA + (lane & 31) * 128 + (lane >> 5) * 4), true, 1u, dead, errw, 8u, lane);
    }
    u32 c0, r0, kr = 3950, ki = 265, bump = 0, hist = 0;
    calib(c0, r0);
    u32 pc = c0, pr = r0, tg = r0 + TV(13) - 512u, cap = SPIN_CAP;
#pragma unroll 1
    for (int s = 0; s + 1 < NSEG; ++s) {
        u32 sr = tsw + tab(tm, s);
        const u32 mg = bump + (s < EARLY_N ? TV(10) : 0u);
        const u32 de = (TV(0) * ki >> 8) + mg, dz = TV(1) * ki >> 8, per = de + dz + TV(2) + mg;
        u32 look = 0;
        DRAIN();
        if (s) {
            const u32 so = tab(tc, s - 1);
#pragma unroll 1
            for (;; ++look) {
                u64 tl;
                u32 miss;
                l_g(miss, c0, r0, tl, tg + TV(15), look < NLOOK ? kr : 0u, (u32)lane * 16, so, srd, (u32x4){tg, tg + per, de | (dz << 16), (u32)s + 1u}, look < NLOOK ? sr : OOB STA);
                if (look >= NLOOK && (int)((u32)tl * 24u + TV(14) - tg) > 0) tg = (u32)tl * 24u + TV(14);
                if (__builtin_expect(!miss, 1)) break;
                if (look < NLOOK) {
                    stx4(rs, sr, (u32x4){0u, 0u, 0u, (u32)s + 1u + TS_MISS});
                    sr += 128u;
                    tg += TV(7);
                }
                if (look >= cap) { atomicOr(errw, 4u); cap = NLOOK; break; }
            }
        }
        if (!s || look >= NLOOK) stx4(rs, sr, (u32x4){tg, tg + per, de | (dz << 16), (u32)s + 1u});
        tg += per;
        touch(srd, wp, s + 1, (u32)(uintptr_t)(__attribute__((address_space(3))) char*)smem);
        track_rate(c0, r0, pc, pr, kr);
        hist = ((hist << 1) | (s >= BUMP_S0 && look ? 1u : 0u)) & BUMP_WIN;
        if (__builtin_popcount(hist) >= BUMP_N && bump < BUMP_MAX * TV(11)) { bump += TV(11); hist = 0; }
        if (pc == c0) ki = to_sgpr((u32)(1048576.f * __builtin_amdgcn_rcpf((float)kr) + 0.5f));
        FLUSH();
    }
}

__device__ __forceinline__ int tile_of(const int xg, const int hl, const int i) {
    const u32 pieces = hl ? 0x85410u : 0x97632u;
    return CFG ? (int)((pieces >> ((i >> 1) * 4)) & 15u) * 4 + (i & 1) * 2 + (xg & 1) : (int)((pieces >> (i * 4)) & 15u) * 4 + xg;
}
__device__ __forceinline__ void warm_role(const u32 wp, const int xg, const int wi, char* __restrict__ ws, char* smem) {
    const int lane = lane_id();
    const rsrc_t rs = mk_rsrc(ws);
    const u32x4 srd = mk_srd(ws);
    const u32 lbase = (u32)(uintptr_t)(__attribute__((address_space(3))) char*)smem;
    TLOAD();
    u32 have = 0, ta = 0;
    int next = 1;
    const u32 tc = slot_v(wp ^ 8u, lane) + copy_off((u32)xg ^ (wp >> 1), lane) + SL_TS;
    auto spin = [&](u32 t) { for (int q = 0; q < 512 && (int)(t - (u32)__builtin_amdgcn_s_memrealtime() * 24u) > 0; ++q) ; };
#pragma unroll 1
    for (int s = 0; s + 2 < NSEG; ++s) {
        if (s) until(ta + 1400u);
        ta = read_ts_late(rs, tab(tc, s), s);
        if (have <= (u32)s + WAHEAD) {
            const u32x2 f = ldx2(rs, (u32)(OFF_FB + (lane & 31) * 128));
            u32 a = min(2 * f[0], 2 * f[1] + 1);
#pragma unroll
            for (int q = 1; q < 32; q <<= 1) a = min(a, (u32)__shfl_xor((int)a, q));
            have = rfl(a);
        }
        if (next < s + 2) next = s + 2;
        u32 tw = ta + TV(5) - 1024u;
#pragma unroll 1
        for (; next <= s + WAHEAD && next + 1 < NSEG && have > (u32)next; ++next)
#pragma unroll 1
            for (int q = 0; q < 8; ++q) {
                const u32 o = far_off(wp, FAR_BX + (u32)(next * NRED + tile_of(xg, q >> 2, wi)) * 32768 + (u32)(q & 3) * 8192);
                spin(tw);
                tw += TV(12);
                u32 sv;
                asm volatile("s_mov_b32 %0, m0\n\ts_mov_b32 m0, %4\n\ts_nop 3\n\tbuffer_load_dword %1, %2, %3 offen sc1 lds\n\ts_mov_b32 m0, %0"
                             : "=&s"(sv) : "v"((u32)lane * 128), "s"(srd), "s"(o), "s"(lbase + 69632u) : "memory");
            }
    }
    DRAIN();
}

__device__ __forceinline__ void topk_role(const u64 tag, char* __restrict__ ws) {
    const int lane = lane_id();
    const rsrc_t rs = mk_rsrc(ws);
    wait_level(rs, tag, lane, 2);
    wait_init(rs, ws, tag, lane);
    u32 v[32];
#pragma unroll
    for (int i = 0; i < 32; ++i) v[i] = ldx1(rs, (u32)lane * 4, (u32)(OFF_R + i * 256));
#pragma unroll
    for (int i = 0; i < 32; ++i) v[i] = ((v[i] ^ ((u32)((int)v[i] >> 31) | 0x80000000u)) & 0xffff0000u) | (u32)(2047 - i * 64 - lane);
    u32 top = ~0u, res = 0;
#pragma unroll 1
    for (int c = 0; c < TOPC; ++c) {
        u32 d = ~0u;
#pragma unroll
        for (int i = 0; i < 32; ++i) d = min(d, top - v[i]);
        d = min(d, (u32)__builtin_amdgcn_update_dpp(0u, d, 0xb1, 0xf, 0xf, false));
        d = min(d, (u32)__builtin_amdgcn_update_dpp(0u, d, 0x4e, 0xf, 0xf, false));
        d = min(d, (u32)__builtin_amdgcn_update_dpp(0u, d, 0x124, 0xf, 0xf, false));
        d = min(d, (u32)__builtin_amdgcn_update_dpp(0u, d, 0x128, 0xf, 0xf, false));
        const u32 dm = min(min((u32)__builtin_amdgcn_readlane(d, 0), (u32)__builtin_amdgcn_readlane(d, 16)), min((u32)__builtin_amdgcn_readlane(d, 32), (u32)__builtin_amdgcn_readlane(d, 48)));
        top -= dm;
        if (lane == c) res = 2048u - (top & 2047u);
        top -= 1u;
    }
    stx1(rs, lane < TOPC ? (u32)(OFF_IDX + lane * 4) : OOB, res);
}

template <int NK>
__device__ __forceinline__ void moe_ld(const u16* __restrict__ wp, const int ldw, const int nvalid, u32 (&r)[NK][8]) {
#pragma unroll
    for (int k = 0; k < NK; ++k)
#pragma unroll
        for (int i = 0; i < 8; ++i) r[k][i] = k < nvalid ? __builtin_nontemporal_load((const u32*)(wp + (size_t)(k * 32 + i) * ldw)) : 0u;
}
template <int NK>
__device__ __forceinline__ void moe_mma(const u32 (&r)[NK][8], const u32x4 (&af)[NK], f32x4& ce, f32x4& co) {
#pragma unroll
    for (int k = 0; k < NK; ++k) {
        u32x4 be, bo;
#pragma unroll
        for (int j = 0; j < 4; ++j) {
            be[j] = __builtin_amdgcn_perm(r[k][2 * j + 1], r[k][2 * j], 0x05040100u);
            bo[j] = __builtin_amdgcn_perm(r[k][2 * j + 1], r[k][2 * j], 0x07060302u);
        }
        ce = mfma16(af[k], be, ce);
        co = mfma16(af[k], bo, co);
    }
}
constexpr int NGU = FF_ / 32, NDN = D_ / 32;
__device__ __forceinline__ void moe_part(const int w, const int tid, const u16* __restrict__ wg, const u16* __restrict__ wu,
                                         const u16* __restrict__ wd, char* __restrict__ ws, char* smem) {
    const int wave = __builtin_amdgcn_readfirstlane(tid >> 6), lane = tid & 63;
    const int n = lane & 15, g = lane >> 4;
    u32* errw = (u32*)(ws + OFF_ERR);
    const rsrc_t rs = mk_rsrc(ws);
    bool dead = false;
    f32x4 (*red)[2][64] = (f32x4(*)[2][64])smem;
    const f32x4 z = {0.f, 0.f, 0.f, 0.f};
    u32 r1[5][8], r2[7][8];
    const int kq = wave >> 1, dg = w - (64 - NDN), k0 = wave * 7, nk = wave == 7 ? 5 : 7;
    if (w < NGU) moe_ld<5>(((wave & 1) ? wu : wg) + (size_t)(kq * 160 + g * 8) * FF_ + w * 32 + n * 2, FF_, 5, r1);
    if (dg >= 0) moe_ld<7>(wd + (size_t)(k0 * 32 + g * 8) * D_ + dg * 32 + n * 2, D_, nk, r2);
    wait_word(rs, (u32)(OFF_IDX + (n & 7) * 4), true, 1u, dead, errw, 16u, lane);
    const u32 row = (ldx1(rs, (u32)(OFF_IDX + (n & 7) * 4)) - 1u) & (S_ - 1);
    if (w < NGU) {
        u32x4 af[5];
#pragma unroll
        for (int k = 0; k < 5; ++k) af[k] = ldx4(rs, (u32)OFF_XN + (row * D_ + (u32)((kq * 5 + k) * 32 + g * 8)) * 2);
        f32x4 ce = z, co = z;
        moe_mma<5>(r1, af, ce, co);
        red[wave][0][lane] = ce; red[wave][1][lane] = co;
        __syncthreads();
        if (wave == 0 && g < 2) {
            f32x4 t[4];
#pragma unroll
            for (int q = 0; q < 4; ++q) t[q] = (red[q >> 1][q & 1][lane] + red[2 + (q >> 1)][q & 1][lane]) + (red[4 + (q >> 1)][q & 1][lane] + red[6 + (q >> 1)][q & 1][lane]);
#pragma unroll
            for (int i = 0; i < 4; ++i) {
                float res[2];
#pragma unroll
                for (int q = 0; q < 2; ++q) {
                    float gg = bfr(t[q][i]), uu = bfr(t[2 + q][i]);
                    float sg = bfr(gg / (1.0f + __expf(-gg)));
                    res[q] = sg * uu;
                }
                stx1(rs, (u32)(OFF_HB + ((size_t)(4 * g + i) * FF_ + w * 32 + n * 2) * 2), pack2(res[0], res[1]));
            }
        }
        if (wave == 0) {
            DRAIN();
            if (lane == 0) __hip_atomic_fetch_add((u32*)(ws + OFF_CNT), 1u, __ATOMIC_RELAXED, __HIP_MEMORY_SCOPE_AGENT);
        }
        __syncthreads();
    }
    if (dg >= 0) {
        const float rv = __uint_as_float(ldx1(rs, (u32)OFF_R + row * 4));
        wait_word(rs, (u32)OFF_CNT, true, (u32)NGU, dead, errw, 16u, lane);
        u32x4 af[7];
#pragma unroll
        for (int k = 0; k < 7; ++k) af[k] = ldx4(rs, (u32)(OFF_HB + ((size_t)(n & 7) * FF_ + (k0 + (k < nk ? k : 0)) * 32 + g * 8) * 2));
        f32x4 ye = z, yo = z;
        moe_mma<7>(r2, af, ye, yo);
        red[wave][0][lane] = ye; red[wave][1][lane] = yo;
        __syncthreads();
        if (wave == 0) {
            f32x4 t[2];
#pragma unroll
            for (int q = 0; q < 2; ++q)
                t[q] = ((red[0][q][lane] + red[1][q][lane]) + (red[2][q][lane] + red[3][q][lane])) + ((red[4][q][lane] + red[5][q][lane]) + (red[6][q][lane] + red[7][q][lane]));
            const float rs1 = bfr(1.0f / (1.0f + __expf(-rv)));
#pragma unroll
            for (int i = 0; i < 4; ++i) {
                const float rsg = __shfl(rs1, (4 * g + i) & 7);
                stx1(rs, g < 2 ? (u32)(OFF_UPD + ((size_t)(4 * g + i) * D_ + dg * 32 + n * 2) * 2) : OOB, pack2(bfr(t[0][i]) * rsg, bfr(t[1][i]) * rsg));
            }
            DRAIN();
            if (lane == 0) __hip_atomic_fetch_add((u32*)(ws + OFF_CNT + 256), 1u, __ATOMIC_RELAXED, __HIP_MEMORY_SCOPE_AGENT);
        }
        __syncthreads();
    }
}

#define VMQ "9"
#define VMV "10"
#define LOOKST 1
#define NOWV 1
#define NOWIT 4
constexpr int NBIG = (NSEG - 1) * 10, NTASK = NBIG + 80;

struct OutCtx { rsrc_t rs; const u16* x; u16* out; u32* errw; u32 wp; int wid; char* ws; };
template <int OT, int NT>
__device__ __forceinline__ void out_task(const OutCtx& cx, const int sg, const int c0, const int n0, const int tid, const int (&sel_idx)[TOPC], bool& adone, u32& rdy,
                                         bool& dead, char* redp, u32* nextp, u32* tctr) {
    const int wave = __builtin_amdgcn_readfirstlane(tid >> 6), lane = tid & 63, n = lane & 15, g = lane >> 4;
    const rsrc_t rs = cx.rs;
    if (!adone && rdy <= (u32)sg) {
        wait_word(rs, (u32)(OFF_FB + (wave * 4 + (lane & 3)) * 128 + (sg & 1) * 4), true, (u32)(sg >> 1) + 1, dead, cx.errw, 32u, lane);
        u32x2 f = ldx2(rs, (u32)(OFF_FB + (wave * 4 + (lane & 3)) * 128));
        adone = __all(f[0] >= NSEG / 2 && f[1] >= NSEG / 2);
    }
    u32x4 af[OT][5], bq[NT][5];
    const u32 bo = amap((u32)(wave * 4096 + lane * 16));
#pragma unroll
    for (int c = 0; c < OT; ++c) {
        const u32 bb = far_off(cx.wp, FAR_BX + (u32)(sg * 40 + c0 + c) * 32768);
#pragma unroll
        for (int hh = 0; hh < 4; ++hh) af[c][hh] = ldx4(rs, bo + hh * 1024, bb);
    }
    const int ti = (tid >> 6) & (OT * NT - 1);
    const int token = sg * SEGL + (n0 + (ti & (NT - 1))) * 16 + n;
    const int col = (c0 + ti / NT) * 16 + 4 * g;
    int sel = -1;
#pragma unroll
    for (int c = 0; c < TOPC; ++c) if (sel_idx[c] == token) sel = c;
    const u32x2 xv = *(const u32x2*)(cx.x + (size_t)token * D_ + col);
    const u32x2 nv = ldx2(rs, (u32)(OFF_XN + ((size_t)token * D_ + col) * 2));
    if (__any(sel >= 0)) wait_word(rs, (u32)(OFF_CNT + 256), true, (u32)NDN, dead, cx.errw, 256u, lane);
    const u32x2 uv = ldx2(rs, sel >= 0 ? (u32)(OFF_UPD + ((size_t)sel * D_ + col) * 2) : OOB);
    __builtin_amdgcn_sched_barrier(0);
    const bool late = sg + 4 >= NSEG;
    if (late) wait_flags(rs, (u32)((OT == 1 ? OFF_FZ + (wave * 2 + n0) * 128 : OFF_FP + wave * 128) + (lane & 1) * 16), true, 1u, dead, cx.errw, 64u, lane);
    else if (rdy <= (u32)sg) {
#pragma unroll 1
        for (u32 spins = 0; !dead; ++spins) {
            CFENCE();
            rdy = __builtin_amdgcn_readfirstlane(ldx1(rs, (u32)(OFF_RDY + (cx.wid & 7) * 128)));
            if (rdy > (u32)sg) break;
            __builtin_amdgcn_s_sleep(8);
            if (spins > SPIN_CAP) { if (lane == 0) atomicOr(cx.errw, 64u); dead = true; }
            if ((spins & 63) == 63 && ldx1(rs, (u32)OFF_ERR) != 0u) dead = true;
        }
        CFENCE();
    }
#pragma unroll
    for (int c = 0; c < OT; ++c) af[c][4] = ldx4(rs, (u32)(wave * 64 + lane) * 16, slot_off(cx.wp, sg) + z_off(cx.wp, (u32)(c0 + c)));
#pragma unroll
    for (int nt = 0; nt < NT; ++nt) {
#pragma unroll
        for (int hh = 0; hh < 4; ++hh) bq[nt][hh] = ldx4(rs, (u32)lane * 16, (u32)OFF_PX + (u32)((sg * H_ + wave * 4 + hh) * 2 + n0 + nt) * 1024);
        bq[nt][4] = ldx4(rs, (u32)lane * 16, (u32)OFF_PXM + (u32)((sg * 8 + wave) * 2 + n0 + nt) * 1024);
    }
    CFENCE();
    u32 next = 0;
    if (tid == 0) next = !late ? __hip_atomic_fetch_add(tctr, 1u, __ATOMIC_RELAXED, __HIP_MEMORY_SCOPE_AGENT)
                                 : __hip_atomic_load(tctr, __ATOMIC_RELAXED, __HIP_MEMORY_SCOPE_AGENT);
    f32x4 (*red)[OT * NT][64] = (f32x4(*)[OT * NT][64])redp;
#pragma unroll
    for (int c = 0; c < OT; ++c)
#pragma unroll
        for (int nt = 0; nt < NT; ++nt) {
            f32x4 acc = {0.f, 0.f, 0.f, 0.f};
#pragma unroll
            for (int k = 0; k < 5; ++k) acc = mfma16(af[c][k], bq[nt][k], acc);
            red[wave][c * NT + nt][lane] = acc;
        }
    if (tid == 0) {
        if (late && next < (u32)NTASK) next = __hip_atomic_fetch_add(tctr, 1u, __ATOMIC_RELAXED, __HIP_MEMORY_SCOPE_AGENT);
        *nextp = next;
    }
    LBAR();
    if (tid < OT * NT * 64) {
        f32x4 sum = red[0][ti][lane];
#pragma unroll
        for (int wv = 1; wv < 8; ++wv) sum += red[wv][ti][lane];
        float res[4];
#pragma unroll
        for (int q = 0; q < 4; ++q) {
            float xx = bf2f((u16)((xv[q >> 1] >> ((q & 1) * 16)) & 0xffff));
            float nn = bf2f((u16)((nv[q >> 1] >> ((q & 1) * 16)) & 0xffff));
            float bo2 = nn;
            if (sel >= 0) bo2 = bfr(nn + bf2f((u16)((uv[q >> 1] >> ((q & 1) * 16)) & 0xffff)));
            float gg = bfr(bo2 / (1.0f + __expf(-bo2)));
            float sv = bfr(sum[q]);
            res[q] = bfr(xx + bfr(sv * gg));
        }
        *(u32x2*)(cx.out + (size_t)token * D_ + col) = (u32x2){pack2(res[0], res[1]), pack2(res[2], res[3])};
    }
}

__device__ __forceinline__ u32 xbuf(int b) { return b == 0 ? 90624u : (b == 1 ? 0u : 49664u); }
__device__ __forceinline__ void worker_role(const int wave, const int wid, const u64 tag, const u32 wp, const u16* __restrict__ x, const u32x32 (&W)[5],
                                            const u16* __restrict__ wg, const u16* __restrict__ wu,
                                            const u16* __restrict__ wd, char* __restrict__ ws, u16* __restrict__ out, char* smem) {
    const int lane = lane_id(), tid = wave * 64 + lane;
    const int n = lane & 15, g = lane >> 4;
    const int par = (wid >> 2) & 1, kind = (wid >> 3) & 1, h = (wid & 3) * 8 + (wid >> 4), type = kind * 2 + par;
    u32* errw = (u32*)(ws + OFF_ERR);
    const rsrc_t rs = mk_rsrc(ws);
    bool dead = false;
    const bool isqk = type < 2;
    int wslot = 1;

    {
        u16 (*vS)[136] = (u16(*)[136])(smem + 40960);
        const u32 lbase = (u32)(uintptr_t)(__attribute__((address_space(3))) char*)smem;
        const char* xsrc = ws + OFF_XNP + wave * 5120 + lane * 16;
        auto xpiece = [&](int sg, u32 bo, int i) { dma16_sc1(xsrc + (size_t)sg * 40960 + i * 1024, __builtin_amdgcn_readfirstlane(lbase + bo + (u32)(wave * 5 + i) * 1024)); };
        const u32 o1 = isqk ? (u32)((wave & 3) * 64 + lane) * 16 : amap((u32)(h * 1024 + lane * 16));
        const int pg = tid >> 5, hf = pg >= 6, zi = tid + 224;
        const u32 o2 = tid < 288 ? (u32)((pg - 6 * hf) * 32768 + ((hf ^ (wp >> 3)) & 1) * 262144 + (tid & 8) * 512 + (tid & 16) * 1024 + h * 128 + (tid & 7) * 16)
                                 : z_off(wp, (u32)(tid - 288) >> 4) + (u32)(h * 256 + (tid & 15) * 16);
        const u32 o3 = zi < 640 ? z_off(wp, (u32)zi >> 4) + (u32)(h * 256 + (zi & 15) * 16) : OOB, ox = tid < 288 ? 262144u : 0u;
        const float ksc = wave >= 4 ? 0.08838834764831845f * 1.4426950408889634f : 1.f;
        WSTAMP(4);
        int b3 = 0;
#pragma unroll 1
        for (int it = 0; it < NSEG / 2; ++it) {
            const int sg = it * 2 + par;
            if (it == 4) {
                wait_level(rs, tag, lane_id(), 2);
            }
            const bool more = it + 2 < NSEG / 2;
            CSTAMP(0);
            const u32 nb = xbuf(b3 == 0 ? 2 : b3 - 1);
            const int nsg = more ? sg + 4 : sg;
            const u32x4* xb = (const u32x4*)(smem + xbuf(b3)) + lane;
            b3 = b3 == 2 ? 0 : b3 + 1;
            const bool now = it < (isqk ? NOWIT : NOWV) || it + 1 == NSEG / 2;
            CSTAMP(1);
            u32x4 xf[2][4];
#pragma unroll
            for (int q = 0; q < 4; ++q) xf[0][q] = xb[((q & 1) * 20 + (q >> 1)) * 64];
            if (isqk) {
                const u32 so = sg + 1 < NSEG ? slot_off(wp, sg + 1) : OOB;
                f32x4 acc[2][2];
#pragma unroll
                for (int a = 0; a < 2; ++a)
#pragma unroll
                    for (int b = 0; b < 2; ++b) acc[a][b] = (f32x4){0.f, 0.f, 0.f, 0.f};
#pragma unroll
                for (int gr = 0; gr < 10; ++gr) {
                    if (gr + 1 < 10) {
#pragma unroll
                        for (int q = 0; q < 4; ++q) xf[(gr + 1) & 1][q] = xb[((q & 1) * 20 + (gr + 1) * 2 + (q >> 1)) * 64];
                    }
                    CFENCE();
#pragma unroll
                    for (int q = 0; q < 4; ++q) {
                        acc[q & 1][0] = mfma16(wfrag(W, gr * 2 + (q >> 1)), xf[gr & 1][q], acc[q & 1][0]);
                        acc[q & 1][1] = mfma16(wfrag(W, 20 + gr * 2 + (q >> 1)), xf[gr & 1][q], acc[q & 1][1]);
                    }
                    asm volatile("" : "+v"(acc[0][0]), "+v"(acc[0][1]), "+v"(acc[1][0]), "+v"(acc[1][1]) :: "memory");
                    if (gr < 5) xpiece(nsg, nb, gr);
                    if (gr == 5) stx4(rs, (o2 ^ (sg + 1 >= 32 ? ox : 0u)) | (so & OOB), (u32x4){SENT, SENT, SENT, SENT}, so & ~OOB);
                    if (gr == 6) stx4(rs, o3 | (so & OOB), (u32x4){SENT, SENT, SENT, SENT}, so & ~OOB);
                }
                CSTAMP(2);
                const u32 ob = far_off(wp ^ 1u, ((wave >> 2) ? NEAR_KX : NEAR_QX) + (u32)(sg * H_ + h) * 8192);
#pragma unroll
                for (int tt = 0; tt < 2; ++tt) {
                    acc[tt][0] *= ksc; acc[tt][1] *= ksc;
                    u32x4 v = {pack2(acc[tt][0][0], acc[tt][0][1]), pack2(acc[tt][0][2], acc[tt][0][3]),
                               pack2(acc[tt][1][0], acc[tt][1][1]), pack2(acc[tt][1][2], acc[tt][1][3])};
                    stx4(rs, o1 + tt * 4096, v, ob);
                }
                CSTAMP(4);
                if (now) DRAIN(); else asm volatile("s_waitcnt vmcnt(" VMQ ")" ::: "memory");
            } else {
                f32x4 acc[2] = {{0.f, 0.f, 0.f, 0.f}, {0.f, 0.f, 0.f, 0.f}};
                u32x4 xg[4];
#pragma unroll
                for (int q = 0; q < 4; ++q) xf[1][q] = xb[((q & 1) * 20 + 2 + (q >> 1)) * 64];
#pragma unroll
                for (int gr = 0; gr < 10; ++gr) {
                    if (gr + 2 < 10) {
#pragma unroll
                        for (int q = 0; q < 4; ++q) {
                            u32x4 t = xb[((q & 1) * 20 + (gr + 2) * 2 + (q >> 1)) * 64];
                            if ((gr + 2) % 3 == 2) xg[q] = t; else xf[(gr + 2) % 3][q] = t;
                        }
                    }
                    CFENCE();
#pragma unroll
                    for (int q = 0; q < 4; ++q) acc[q & 1] = mfma16(wfrag(W, gr * 2 + (q >> 1)), gr % 3 == 2 ? xg[q] : xf[gr % 3][q], acc[q & 1]);
                    asm volatile("" : "+v"(acc[0]), "+v"(acc[1]) :: "memory");
                    if ((gr & 1) == 0) xpiece(nsg, nb, gr >> 1);
                }
#pragma unroll
                for (int tt = 0; tt < 2; ++tt)
                    *(u32x2*)&vS[tt * 16 + n][32 * (wave >> 1) + 8 * g + 4 * (wave & 1)] = (u32x2){pack2(acc[tt][0], acc[tt][1]), pack2(acc[tt][2], acc[tt][3])};
                CSTAMP(2);
                asm volatile("s_waitcnt vmcnt(5)" ::: "memory");
                LBAR();
                if (tid == 0) stx1(rs, (u32)(OFF_FA + kind * 4096 + h * 128 + par * 4), (u32)it);
                CSTAMP(3);
                u32x4 vf[2][4];
#pragma unroll
                for (int tt = 0; tt < 2; ++tt)
#pragma unroll
                    for (int kk = 0; kk < 4; ++kk) vf[tt][kk] = *(const u32x4*)&vS[tt * 16 + n][kk * 32 + g * 8];
#pragma unroll
                for (int ct = 0; ct < 5; ++ct) {
                    f32x4 c0 = {0.f, 0.f, 0.f, 0.f}, c1 = {0.f, 0.f, 0.f, 0.f};
#pragma unroll
                    for (int kk = 0; kk < 4; ++kk) {
                        c0 = mfma16(vf[0][kk], wfrag(W, 20 + ct * 4 + kk), c0);
                        c1 = mfma16(vf[1][kk], wfrag(W, 20 + ct * 4 + kk), c1);
                    }
                    stx4(rs, o1, (u32x4){pack2(c0[0], c0[1]), pack2(c0[2], c0[3]), pack2(c1[0], c1[1]), pack2(c1[2], c1[3])},
                         far_off(wp, FAR_BX + (u32)(sg * 40 + ct * 8 + wave) * 32768));
                }
                CSTAMP(4);
                if (now) DRAIN();
            }
            CSTAMP(5);
            LBAR();
            CSTAMP(6);
            if (tid == 0 && (isqk || now)) stx1(rs, (u32)(OFF_FA + kind * 4096 + h * 128 + par * 4), (u32)(now ? it + 1 : it));
            if (it < 3 || (it & 3) == 3) WSTAMP(0x10 + it);
        }
    }
    WSTAMP(2);

    {
    const int lane = lane_id(), tid = wave * 64 + lane;
    if (isqk) moe_part(h * 2 + par, tid, wg, wu, wd, ws, smem);
    WSTAMP(5);
    u32* nextp = (u32*)(smem + 131072);
    u32* tctr = (u32*)(ws + OFF_CNT + 512);
    if (tid == 0) nextp[0] = __hip_atomic_fetch_add(tctr, 1u, __ATOMIC_RELAXED, __HIP_MEMORY_SCOPE_AGENT);
    wait_flags(rs, (u32)OFF_IDX + (lane & 1) * 16, true, 1u, dead, errw, 256u, lane);
    __syncthreads();
    int sel_idx[TOPC];
    {
        u32x4 i0 = ldx4(rs, (u32)OFF_IDX), i1 = ldx4(rs, (u32)OFF_IDX + 16);
#pragma unroll
        for (int c = 0; c < 4; ++c) { sel_idx[c] = (int)i0[c] - 1; sel_idx[4 + c] = (int)i1[c] - 1; }
    }
    WSTAMP(3);
    const OutCtx cx = {rs, x, out, errw, wp, wid, ws};
    bool adone = false;
    u32 rdy = 0;
#pragma unroll 1
    for (int tc = 0;; ++tc) {
        const int task = __builtin_amdgcn_readfirstlane(nextp[tc & 1]);
        if (task >= NTASK) break;
        char* redp = smem + (tc & 1) * 65536;
        if (task < NBIG) {
            WSTAMP(0x100 + task / 10);
            out_task<4, 2>(cx, task / 10, (task % 10) * 4, 0, tid, sel_idx, adone, rdy, dead, redp, nextp + ((tc + 1) & 1), tctr);
        } else {
            WSTAMP(0x100 + NSEG - 1);
            out_task<1, 1>(cx, NSEG - 1, (task - NBIG) >> 1, (task - NBIG) & 1, tid, sel_idx, adone, rdy, dead, redp, nextp + ((tc + 1) & 1), tctr);
        }
        WSTAMP(0x300);
        if (task == NTASK - 1) {
            wait_word(rs, (u32)(OFF_CNT + 768), true, 22u, dead, errw, 64u, lane);
            if (tid < 192) stx2(rs, (u32)(OFF_INI + tid * 8), (u32x2){~(u32)tag, ~cal_tag(tag, ~(u32)tag)});
        }
    }
    }
}

constexpr int FUSED_LDS = 132 * 1024;
#define ORD_A 0x143210
#define ORD_C 0x101234
#define KERNEL_SYM "fused_kernel"
extern "C" __global__ __launch_bounds__(512, 2) void fused_kernel(const u16* __restrict__ x, const u16* __restrict__ mem0, const u16* __restrict__ w_r,
                                                        const u16* __restrict__ Wq, const u16* __restrict__ Wk,
                                                        const u16* __restrict__ Wv, const u16* __restrict__ Wo,
                                                        const u16* __restrict__ wg, const u16* __restrict__ wu,
                                                        const u16* __restrict__ wd, char* __restrict__ ws, u16* __restrict__ out) {
    extern __shared__ __attribute__((aligned(16))) char smem[];
    const int wave = __builtin_amdgcn_readfirstlane(threadIdx.x >> 6);
    u64 gen = 0, tag = 0, old = 0;
    u32 xcc, se;
    asm volatile("s_getreg_b32 %0, hwreg(HW_REG_XCC_ID, 0, 3)" : "=s"(xcc));
    asm volatile("s_getreg_b32 %0, hwreg(HW_REG_HW_ID, 13, 2)" : "=s"(se));
    u32 bid = blockIdx.x;
    asm volatile("" : "+s"(bid));
    const bool first = (int)bid >= 0;
    u32 bq = first ? (bid & ~31u) + se * 8u + xcc : bid & 255u;
    asm volatile("" : "+s"(bq));
    const int b = (int)bq, k = b >> 3, xg = b & 3;
    if (threadIdx.x >= 448 && threadIdx.x < 453) {
        const int lane = threadIdx.x & 63;
        const u32 pg = (bid >> 3 & 31u) + 32u * (u32)lane;
        const char* tp = lane < 4 ? ws + min(pg << 21, (u32)WS_TOTAL - 4u) : (const char*)out + (size_t)(bid >> 3 & 3u) * 655360;
        u32 sv;
        asm volatile("s_mov_b32 %0, m0\n\ts_mov_b32 m0, %2\n\ts_nop 0\n\tglobal_load_lds_dword %1, off\n\ts_mov_b32 m0, %0"
                     : "=&s"(sv) : "v"(tp), "s"((u32)(uintptr_t)(__attribute__((address_space(3))) char*)smem + 132096u) : "memory");
    }
    u64* claim = (u64*)(ws + OFF_CLAIM);
    u32* sh = (u32*)(smem + 132000);
    auto try_claim = [&] {
        if (wave == 0 && first) {
            asm volatile("s_load_dwordx2 %0, %1, 0x0\n\ts_waitcnt lgkmcnt(0)" : "=s"(gen) : "s"(ws + OFF_GEN) : "memory");
            tag = (gen + 0x9E3779B97F4A7C15ull) * 0xBF58476D1CE4E5B9ull;
            tag ^= tag >> 31;
            tag *= 0x94D049BB133111EBull;
            tag ^= tag >> 29;
            if (lane_id() == 0) {
                old = __hip_atomic_exchange(claim + b * 8, tag, __ATOMIC_RELAXED, __HIP_MEMORY_SCOPE_AGENT);
                sh[1] = (u32)tag; sh[2] = (u32)(tag >> 32); sh[3] = (u32)gen; sh[4] = (u32)(gen >> 32);
            }
        }
    };
    auto settle = [&] {
        const int tid = wave * 64 + lane_id();
        if (tid == 0 && first) {
            u32 vb = (u32)b;
#pragma unroll 1
            for (int i = 0; i < 256 && old == tag; ++i) {
                vb = (vb + 1) & 255u;
                old = __hip_atomic_exchange(claim + vb * 8, tag, __ATOMIC_RELAXED, __HIP_MEMORY_SCOPE_AGENT);
            }
            sh[0] = vb;
        }
        LBAR();
        const u32 bc = __builtin_amdgcn_readfirstlane(sh[0]);
        asm volatile("s_cmp_eq_u32 %0, %1\n\ts_cbranch_scc1 Lgo%=\n\t"
                     "s_or_b32 s2, %0, 0x80000000\n\ts_mov_b64 s[0:1], %2\n\tv_mov_b32 v0, %3\n\ts_waitcnt vmcnt(0) lgkmcnt(0)\n\t"
                     "s_getpc_b64 s[4:5]\nLpc%=:\n\ts_sub_u32 s4, s4, Lpc%=-" KERNEL_SYM "\n\ts_subb_u32 s5, s5, 0\n\ts_setpc_b64 s[4:5]\nLgo%=:"
                     :: "s"(bc), "s"(b), "s"(__builtin_amdgcn_kernarg_segment_ptr()), "v"(tid) : "s2", "scc", "memory");
        tag = (u64)(u32)__builtin_amdgcn_readfirstlane(sh[1]) | ((u64)(u32)__builtin_amdgcn_readfirstlane(sh[2]) << 32);
    };
    if (b & 4) {
        const int wid = k * 4 + xg, h = (wid & 3) * 8 + (wid >> 4), par = (wid >> 2) & 1;
        const bool isqk = ((wid >> 3) & 1) == 0;
        const int lane = lane_id();
        u32x32 W[5];
        u32 wp = 0;
        u32x2 f0, f1;
        u64 tg2 = 0;
        bool got = false;
        auto xtile = [&](int t) {
#pragma unroll
            for (int i = 0; i < 5; ++i)
                dma16_sc1(ws + OFF_XNP + (size_t)(par + 2 * t) * 40960 + (wave * 5 + i) * 1024 + lane * 16,
                          __builtin_amdgcn_readfirstlane((u32)(uintptr_t)(__attribute__((address_space(3))) char*)smem + xbuf(t) + (u32)(wave * 5 + i) * 1024));
        };
        load_weights<ORD_A>([&](int st) {
            if (st == 0) try_claim();
            if (st == LOOKST) {
                asm volatile("global_load_dwordx2 %0, %2, off sc1\n\tglobal_load_dwordx2 %1, %2, off offset:512 sc1"
                             : "=&v"(f0), "=&v"(f1) : "v"(ws + OFF_INI + lane * 8) : "memory");
                tg2 = (u64)(u32)__builtin_amdgcn_readfirstlane(sh[1]) | ((u64)(u32)__builtin_amdgcn_readfirstlane(sh[2]) << 32);
            }
            if (st == 3) {
                asm volatile("" : "+v"(f0), "+v"(f1));
                got = __all((f1[0] == (u32)tg2) & (f1[1] == (u32)(tg2 >> 32)));
                if (got) xtile(0);
                if (__all(f0[1] == cal_tag(tg2, f0[0]))) wp = wp_of(ws, f0[0]);
            }
        }, (isqk ? Wq : Wv) + h * DH_, isqk ? Wk + h * DH_ : Wo + (size_t)h * DH_ * D_, !isqk, smem, wave, lane,
           isqk ? (wave >> 2) : 0, isqk ? 2 * (wave & 3) : wave, isqk ? (wave >> 2) : 1, isqk ? 2 * (wave & 3) + 1 : wave, W);
        settle();
        const rsrc_t rs = mk_rsrc(ws);
        if (!got) { wait_level(rs, tag, lane, 1); xtile(0); }
        if (wp == 0) wp = wait_init(rs, ws, tag, lane);
        DRAIN();
        xtile(1);
        LBAR();
        PIN_W(W);
        worker_role(wave, wid, tag, wp, x, W, wg, wu, wd, ws, out, smem);
        __builtin_amdgcn_endpgm();
    }
    const int hl = ((k >> 1) ^ (xg >> 1)) & 1, j = (k >> 2) * 2 + (k & 1);
    if (CFG ? xg < 2 : j < 8) {
        const int h = CFG ? xg * 16 + (j >> 3) * 8 + hl * 4 + ((j >> 1) & 3) : xg * 8 + hl * 4 + (j >> 1);
        u32x32 W[5];
        auto hook = [&](int st) {
            if (st == 0) try_claim();
        };
        if ((j & 1) == 0) {
            load_weights<ORD_C>(hook, Wq + h * DH_, Wk + h * DH_, false, smem, wave, lane_id(), 0, wave, 1, wave, W);
            settle();
            PIN_W(W);
            s_role(wave, h, xg, tag, mem0, W, ws, smem);
            __builtin_amdgcn_endpgm();
        }
        load_weights<ORD_C | (1 << 21)>(hook, Wv + h * DH_, Wo + (size_t)h * DH_ * D_, true, smem, wave, lane_id(), wave >> 2, 2 * (wave & 3), wave >> 2, 2 * (wave & 3) + 1, W);
        settle();
        PIN_W(W);
        v_role(wave, h, xg, tag, mem0, W, ws, smem);
        __builtin_amdgcn_endpgm();
    }
    constexpr int NR = CFG ? 10 : 5, NS = CFG ? 6 : 3, NP = CFG ? 4 : 2;
    const int jr = CFG ? j : j - 8, hi = (CFG ? xg - 2 : xg) * 2 + hl;
    try_claim();
    init_role([&] { settle(); return tag; }, wave, jr < NR + NS - NP ? hi * (NR + NS - NP) + jr : 48 + hi * NP + jr - (NR + NS - NP), xcc, ((se ^ xcc) >> 1) & 1u, x, w_r, ws, smem);
    gen = (u64)sh[3] | ((u64)sh[4] << 32);
    tag = (u64)(u32)__builtin_amdgcn_readfirstlane(sh[1]) | ((u64)(u32)__builtin_amdgcn_readfirstlane(sh[2]) << 32);
    if (jr < NR) { r_role(wave, tile_of(xg, hl, jr), xg, tag, ws, smem); return; }
    const int wi = hl * NS + jr - NR;
    const bool aux = wi == 2 * NS - 1;
    if (wave == 1 && (wi < NR || (aux && (xg == 3 || xg == 0)))) {
        const u32 wp = wait_init(mk_rsrc(ws), ws, tag, lane_id());
        if (lane_id() == 0) __hip_atomic_fetch_add((u32*)(ws + OFF_CNT + 768), 1u, __ATOMIC_RELAXED, __HIP_MEMORY_SCOPE_AGENT);
        if (!aux) warm_role(wp, xg, wi, ws, smem); else if (xg) relay_role(wp, xg, ws); else lead_role(wp, xg, ws, smem);
    }
    else if (aux && xg == 2 && wave == 7) topk_role(tag, ws);
    else if (aux && xg == 2 && wave == 0) {
        const rsrc_t rs = mk_rsrc(ws);
        const int lane = lane_id();
#pragma unroll 1
        for (u32 spins = 0; spins < SPIN_CAP; ++spins) {
            CFENCE();
            bool ok = true;
#pragma unroll
            for (int q = 0; q < 4; ++q) {
                u32x2 f = ldx2(rs, (u32)(OFF_CLAIM + (q * 64 + lane) * 64));
                ok = ok && f[0] == (u32)tag && f[1] == (u32)(tag >> 32);
            }
            if (__all(ok)) break;
            __builtin_amdgcn_s_sleep(32);
        }
        if (lane == 0) __hip_atomic_store((u64*)(ws + OFF_GEN), gen + 1ull, __ATOMIC_RELAXED, __HIP_MEMORY_SCOPE_AGENT);
#pragma unroll
        for (int q = 0; q < 4; ++q) stx2(rs, (u32)(OFF_CLAIM + (q * 64 + lane) * 64), (u32x2){~(u32)tag, 0u});
    }
}

extern "C" void kernel_launch(void* const* d_in, const int* in_sizes, int n_in, void* d_out, int out_size,
                              void* d_ws, size_t ws_size, hipStream_t stream) {
    const u16* x = (const u16*)d_in[0];
    const u16* mem0 = (const u16*)d_in[1];
    const u16* Wq = (const u16*)d_in[2];
    const u16* Wk = (const u16*)d_in[3];
    const u16* Wv = (const u16*)d_in[4];
    const u16* Wo = (const u16*)d_in[5];
    const u16* w_r = (const u16*)d_in[8];
    const u16* w_gate = (const u16*)d_in[9];
    const u16* w_up = (const u16*)d_in[10];
    const u16* w_down = (const u16*)d_in[11];
    char* ws = (char*)d_ws;
    u16* out = (u16*)d_out;

    hipFuncSetAttribute((const void*)fused_kernel, hipFuncAttributeMaxDynamicSharedMemorySize, FUSED_LDS);
    fused_kernel<<<256, 512, FUSED_LDS, stream>>>(x, mem0, w_r, Wq, Wk, Wv, Wo, w_gate, w_up, w_down, ws, out);
}
```

```cpp
#define CFG 0
#include <hip/hip_runtime.h>
#include <hip/hip_bf16.h>
#include <stdint.h>

constexpr int S_ = 2048, D_ = 640, H_ = 32, DH_ = 128, NSEG = 64, SEGL = 32, FF_ = 1728, TOPC = 8;
constexpr int HD_ = H_ * DH_;
constexpr int NRED = 40;

typedef unsigned short u16;
typedef unsigned int u32;
typedef unsigned long long u64;
typedef __attribute__((ext_vector_type(8))) __bf16 bf16x8;
typedef __attribute__((ext_vector_type(16))) __bf16 bf16x16;
typedef __attribute__((ext_vector_type(4))) float f32x4;
typedef __attribute__((ext_vector_type(4))) unsigned int u32x4;
typedef __attribute__((ext_vector_type(8))) unsigned int u32x8;
typedef __attribute__((ext_vector_type(2))) unsigned int u32x2;

__device__ __forceinline__ float bf2f(u16 h) { return __uint_as_float(((u32)h) << 16); }
typedef __bf16 bf16x2_t __attribute__((ext_vector_type(2)));
typedef float f32x2_t __attribute__((ext_vector_type(2)));
__device__ __forceinline__ u16 f2bf(float f) {
    __bf16 h = (__bf16)f;
    return __builtin_bit_cast(u16, h);
}
__device__ __forceinline__ float bfr(float f) { return (float)(__bf16)f; }
__device__ __forceinline__ u32 pack2(float a, float b) {
    f32x2_t f = {a, b};
    bf16x2_t h = __builtin_convertvector(f, bf16x2_t);
    return __builtin_bit_cast(u32, h);
}
__device__ __forceinline__ bf16x8 as_bf(u32x4 v) { return __builtin_bit_cast(bf16x8, v); }
__device__ __forceinline__ f32x4 mfma16(u32x4 a, u32x4 b, f32x4 c) {
    return __builtin_amdgcn_mfma_f32_16x16x32_bf16(as_bf(a), as_bf(b), c, 0, 0, 0);
}
__device__ __forceinline__ f32x4 smf(u32x4 a, u32x4 b0, u32x4 b1, f32x4 c, int idx) {
    u32x8 b = {b0[0], b0[1], b0[2], b0[3], b1[0], b1[1], b1[2], b1[3]};
    return __builtin_amdgcn_smfmac_f32_16x16x64_bf16(as_bf(a), __builtin_bit_cast(bf16x16, b), c, idx, 0, 0);
}
__device__ __forceinline__ int dmap(int p) {
    int q = p & 15;
    return (p & ~15) | (q & 1) | ((q & 2) << 2) | ((q & 4) >> 1) | ((q & 8) >> 1);
}

constexpr size_t al256(size_t x) { return (x + 255) & ~(size_t)255; }
constexpr size_t OFF_XN = 0;
constexpr size_t OFF_XNP = OFF_XN + al256((size_t)S_ * D_ * 2);
constexpr size_t OFF_R = OFF_XNP + al256((size_t)S_ * D_ * 2);
constexpr size_t OFF_PX = OFF_R + al256((size_t)S_ * 4);
constexpr size_t OFF_PXM = OFF_PX + al256((size_t)NSEG * H_ * 2048);
constexpr size_t OFF_HB = OFF_PXM + al256((size_t)NSEG * 8 * 2048);
constexpr size_t OFF_UPD = OFF_HB + al256((size_t)8 * FF_ * 2);
constexpr size_t OFF_GEN = OFF_UPD + al256((size_t)8 * D_ * 2);
constexpr size_t OFF_CLAIM = OFF_GEN + 256;
constexpr size_t OFF_INI = OFF_CLAIM + 16384;
constexpr size_t OFF_ERR = OFF_INI + 2048;
constexpr size_t OFF_FA = OFF_ERR + 256;
constexpr size_t OFF_FB = OFF_FA + 4096;
constexpr size_t OFF_FP = OFF_FB + 4096;
constexpr size_t OFF_FZ = OFF_FP + 1024;
constexpr size_t OFF_RDY = OFF_FZ + 2048;
constexpr size_t OFF_IDX = OFF_RDY + 1024;
constexpr size_t OFF_CNT = OFF_IDX + 256;
constexpr size_t ZERO_BEGIN = OFF_ERR, ZERO_END = OFF_CNT + 1024;
constexpr size_t OFF_WIN = (size_t)12 << 20;
constexpr size_t WS_TOTAL = OFF_WIN + ((size_t)188 << 20);
static_assert(ZERO_END <= OFF_WIN && WS_TOTAL <= ((size_t)256 << 20), "ws");

constexpr u32 SLOT0 = 52u << 20;
constexpr u32 SL_P = 4 * 32768, SL_TS = 2 * 32768 + 1024;
constexpr u32 FAR_BX = 1u << 20;
constexpr u32 NEAR_QX = 59u << 20;
constexpr u32 NEAR_KX = NEAR_QX + NSEG * H_ * 8192;
static_assert(2 * ((size_t)NEAR_KX + (size_t)NSEG * H_ * 8192) + ((size_t)2 << 20) <= WS_TOTAL - OFF_WIN && SLOT0 + (64u << 20) <= 2 * NEAR_QX
              && 2 * ((size_t)FAR_BX + (size_t)NSEG * NRED * 32768) + ((size_t)2 << 20) <= WS_TOTAL - OFF_WIN, "far");
__device__ __forceinline__ u32 slot_off(u32 wp, int s) {
    u32 r, t;
    asm("s_lshr_b32 %0, %3, 1\n\ts_xor_b32 %0, %0, %2\n\ts_and_b32 %0, %0, 1\n\ts_lshl_b32 %0, %0, 13\n\ts_lshl_b32 %1, %3, 20\n\t"
        "s_add_u32 %0, %0, %1\n\ts_andn2_b32 %1, %2, 15\n\ts_add_u32 %0, %0, %1\n\ts_add_u32 %0, %0, %4" : "=&s"(r), "=&s"(t) : "s"(wp), "s"(s), "n"(SLOT0) : "scc");
    return r;
}
__device__ __forceinline__ u32 slot_z(u32 wp, u32 s) { return (wp & ~15u) + SLOT0 + (s << 20) + (((wp ^ (s >> 1)) & 1u) << 13); }
__device__ __forceinline__ u32 slot_v(u32 wp, u32 s) { return slot_z(wp, s) + ((((wp >> 3) ^ (s >> 5)) & 1u) << 18); }
__device__ __forceinline__ u32 far_v(u32 wp, u32 L) { return (wp & ~15u) + 2 * L + ((~(wp ^ (L >> 20)) & 1u) << 13); }
__device__ __forceinline__ u32 tab(u32 t, int s) { return __builtin_amdgcn_readlane(t, s); }
__device__ __forceinline__ u32 copy_off(u32 c, u32 s) { return ((c & 1u) << 14) | ((((c >> 1) ^ s) & 1u) << 12); }
__device__ __forceinline__ u32 z_off(u32 wp, u32 ct) {
    const u32 zb = ct >> 1, pg = zb < 16 ? zb + 16 : 6 + (zb & 1u) + (zb & 2u) * 4;
    return pg * 32768 + (((ct ^ (wp >> 1) ^ (pg >> 4)) & 1u) << 14);
}
constexpr int NCP = CFG ? 2 : 4;
__device__ __forceinline__ u32 put_off(u32 wp, u32 x0, u32 s) { return CFG ? copy_off(x0 ^ (wp >> 1), s) & 4096u : 0u; }
__device__ __forceinline__ u32 cpy(u32 c) { return CFG ? (c & 1u) * 16384u : (c & 1u) * 4096u + (c >> 1) * 16384u; }
__device__ __forceinline__ u32 far_off(u32 wp, u32 L) {
    u32 r, t;
    asm("s_lshr_b32 %0, %3, 20\n\ts_xnor_b32 %0, %0, %2\n\ts_and_b32 %0, %0, 1\n\ts_lshl_b32 %0, %0, 13\n\ts_lshl_b32 %1, %3, 1\n\t"
        "s_add_u32 %0, %0, %1\n\ts_andn2_b32 %1, %2, 15\n\ts_add_u32 %0, %0, %1" : "=&s"(r), "=&s"(t) : "s"(wp), "s"(L) : "scc");
    return r;
}
constexpr u32 SENT = 0xffbfffbfu;
constexpr u32 MARK = 0xffbeffbeu;
constexpr u32 OOB = 0x80000000u;
__device__ __forceinline__ u32 amap(u32 L) { return ((L >> 13) << 14) | (L & 8191u); }
__device__ __forceinline__ u32 win_off(const char* ws) {
    u64 a = (u64)(uintptr_t)ws;
    u64 w = (a + OFF_WIN + ((u64)2 << 20) - 1) & ~(((u64)2 << 20) - 1);
    return (u32)(w - a);
}

constexpr int AUX_SC1 = 16;
typedef __amdgpu_buffer_rsrc_t rsrc_t;
__device__ __forceinline__ rsrc_t mk_rsrc(const void* p) {
    return __builtin_amdgcn_make_buffer_rsrc((void*)p, 0, 0x7fffffff, 0x00020000);
}
__device__ __forceinline__ u32x4 ldx4(rsrc_t r, u32 off, u32 so = 0) { return __builtin_amdgcn_raw_buffer_load_b128(r, off, so, AUX_SC1); }
__device__ __forceinline__ u32x2 ldx2(rsrc_t r, u32 off, u32 so = 0) { return __builtin_amdgcn_raw_buffer_load_b64(r, off, so, AUX_SC1); }
__device__ __forceinline__ u32 ldx1(rsrc_t r, u32 off, u32 so = 0) { return __builtin_amdgcn_raw_buffer_load_b32(r, off, so, AUX_SC1); }
__device__ __forceinline__ void stx4(rsrc_t r, u32 off, u32x4 v, u32 so = 0) { __builtin_amdgcn_raw_buffer_store_b128(v, r, off + so, 0, AUX_SC1); }
__device__ __forceinline__ void stx2(rsrc_t r, u32 off, u32x2 v, u32 so = 0) { __builtin_amdgcn_raw_buffer_store_b64(v, r, off, so, AUX_SC1); }
__device__ __forceinline__ void stx1(rsrc_t r, u32 off, u32 v, u32 so = 0) { __builtin_amdgcn_raw_buffer_store_b32(v, r, off, so, AUX_SC1); }
#define DRAIN() asm volatile("s_waitcnt vmcnt(0)" ::: "memory")
#define CFENCE() asm volatile("" ::: "memory")
#define LBAR() asm volatile("s_waitcnt lgkmcnt(0)\n\ts_barrier" ::: "memory")

constexpr u32 L_MISC = 147456;
constexpr u32 SPIN_CAP = 1u << 18;
__device__ __forceinline__ int lane_id() {
    int l;
    asm volatile("v_mbcnt_lo_u32_b32 %0, -1, 0\n\tv_mbcnt_hi_u32_b32 %0, -1, %0" : "=v"(l));
    __builtin_assume(l >= 0 && l < 64);
    return l;
}

__device__ __forceinline__ float xmax16(float x) {
    u32 a = __float_as_uint(x);
    auto r = __builtin_amdgcn_permlane16_swap(a, a, false, false);
    return fmaxf(__uint_as_float(r[0]), __uint_as_float(r[1]));
}
__device__ __forceinline__ float xmax32(float x) {
    u32 a = __float_as_uint(x);
    auto r = __builtin_amdgcn_permlane32_swap(a, a, false, false);
    return fmaxf(__uint_as_float(r[0]), __uint_as_float(r[1]));
}
__device__ __forceinline__ float xadd16(float x) {
    u32 a = __float_as_uint(x);
    auto r = __builtin_amdgcn_permlane16_swap(a, a, false, false);
    return __uint_as_float(r[0]) + __uint_as_float(r[1]);
}
__device__ __forceinline__ float xadd32(float x) {
    u32 a = __float_as_uint(x);
    auto r = __builtin_amdgcn_permlane32_swap(a, a, false, false);
    return __uint_as_float(r[0]) + __uint_as_float(r[1]);
}
__device__ __forceinline__ u32 umax3(u32 a, u32 b, u32 c) { return max(max(a, b), c); }

__device__ __forceinline__ void wait_flags(rsrc_t rs, u32 off, bool act, u32 target, bool& dead, u32* errw, u32 code, int lane) {
    u32 spins = 0;
#pragma unroll 1
    while (!dead) {
        CFENCE();
        u32x4 f = ldx4(rs, off);
        bool ok = !act || ((f[0] >= target) & (f[1] >= target) & (f[2] >= target) & (f[3] >= target));
        if (__all(ok)) break;
        __builtin_amdgcn_s_sleep(2);
        if (++spins > SPIN_CAP) { if (lane == 0) atomicOr(errw, code); dead = true; }
        if ((spins & 63) == 0 && ldx1(rs, (u32)OFF_ERR) != 0u) dead = true;
    }
    CFENCE();
}
__device__ __forceinline__ void wait_word(rsrc_t rs, u32 off, bool act, u32 target, bool& dead, u32* errw, u32 code, int lane) {
    u32 spins = 0;
#pragma unroll 1
    while (!dead) {
        CFENCE();
        u32 f = ldx1(rs, off);
        if (__all(!act || f >= target)) break;
        __builtin_amdgcn_s_sleep(4);
        if (++spins > SPIN_CAP) { if (lane == 0) atomicOr(errw, code); dead = true; }
        if ((spins & 63) == 0 && ldx1(rs, (u32)OFF_ERR) != 0u) dead = true;
    }
    CFENCE();
}

__device__ __forceinline__ u32 cal_tag(u64 tag, u32 v) { return ((u32)tag ^ (u32)(tag >> 32)) + v * 0x9E3779B1u; }
__device__ __forceinline__ void wait_level(rsrc_t rs, u64 tag, int lane, int level) {
#pragma unroll 1
    for (u32 spins = 0; spins < SPIN_CAP; ++spins) {
        CFENCE();
        u32x2 f = ldx2(rs, (u32)(OFF_INI + level * 512 + lane * 8));
        if (__all((f[0] == (u32)tag) & (f[1] == (u32)(tag >> 32)))) break;
        __builtin_amdgcn_s_sleep(4);
    }
    CFENCE();
}
__device__ __forceinline__ u32 to_sgpr(u32 x) {
    u32 r;
    asm volatile("s_nop 0\n\tv_readfirstlane_b32 %0, %1\n\ts_nop 4" : "=s"(r) : "v"(x));
    return r;
}
__device__ __forceinline__ u32 wp_of(const char* ws, u32 v) {
    v += __builtin_amdgcn_update_dpp(0u, v, 0xb1, 0xf, 0xf, false);
    v += __builtin_amdgcn_update_dpp(0u, v, 0x4e, 0xf, 0xf, false);
    v += __builtin_amdgcn_update_dpp(0u, v, 0x124, 0xf, 0xf, false);
    v += __builtin_amdgcn_update_dpp(0u, v, 0x128, 0xf, 0xf, false);
    const u32 r = __builtin_amdgcn_readlane(v, 48) + 0x0f0f0f0fu;
    return to_sgpr(win_off(ws) | ((r >> 7) & 1u) | ((r >> 14) & 2u) | ((r >> 21) & 4u) | ((r >> 28) & 8u));
}
__device__ __forceinline__ u32 wait_init(rsrc_t rs, const char* ws, u64 tag, int lane, bool all = true) {
    u32x2 c = {0u, 0u};
#pragma unroll 1
    for (u32 spins = 0; spins < SPIN_CAP; ++spins) {
        CFENCE();
        c = ldx2(rs, (u32)(OFF_INI + lane * 8 + (all ? 1536 : 0)));
        if (__all(c[1] == cal_tag(tag, c[0]))) break;
        __builtin_amdgcn_s_sleep(4);
    }
    return wp_of(ws, c[0]);
}

#define STAMPS() do { } while (0)
#define STAMP(k) do { } while (0)
#define RSTAMP(k) do { } while (0)
#define STAMP2(k, r) do { } while (0)
#define FLUSH() do { } while (0)
#define LOGS() do { } while (0)
#define LOG() do { } while (0)
#define LOGOUT(cu) do { } while (0)
#define CFLUSH() do { } while (0)
#define XSTAMP(w, sl) do { } while (0)
#define WSTAMP(id) do { } while (0)
#define CSTAMP(id) do { } while (0)
#define NPROBE 2
constexpr u32 INI_ZERO_CHUNKS = (u32)((ZERO_END - ZERO_BEGIN) / 16);
constexpr u32 INI_CHUNKS = INI_ZERO_CHUNKS + 65536;
constexpr int INI_PER = (INI_CHUNKS + 48 * 384 - 1) / (48 * 384);

__device__ __forceinline__ float rsum16(float x) {
    x += __uint_as_float(__builtin_amdgcn_update_dpp(0u, __float_as_uint(x), 0xb1, 0xf, 0xf, false));
    x += __uint_as_float(__builtin_amdgcn_update_dpp(0u, __float_as_uint(x), 0x4e, 0xf, 0xf, false));
    x += __uint_as_float(__builtin_amdgcn_update_dpp(0u, __float_as_uint(x), 0x124, 0xf, 0xf, false));
    x += __uint_as_float(__builtin_amdgcn_update_dpp(0u, __float_as_uint(x), 0x128, 0xf, 0xf, false));
    return x;
}

__device__ __forceinline__ void ln_regs(u32x4 (&xv)[5]) {
    float sum = 0.f;
#pragma unroll
    for (int i = 0; i < 5; ++i)
#pragma unroll
        for (int j = 0; j < 4; ++j) sum += __uint_as_float(xv[i][j] << 16) + __uint_as_float(xv[i][j] & 0xffff0000u);
    const float mean = rsum16(sum) * (1.0f / D_);
    float vs = 0.f;
#pragma unroll
    for (int i = 0; i < 5; ++i)
#pragma unroll
        for (int j = 0; j < 4; ++j) {
            float d0 = __uint_as_float(xv[i][j] << 16) - mean, d1 = __uint_as_float(xv[i][j] & 0xffff0000u) - mean;
            vs += d0 * d0 + d1 * d1;
        }
    const float var = bfr(rsum16(vs) * (1.0f / D_));
    const float m_bf = bfr(mean);
    const float rden = 1.0f / bfr(sqrtf(bfr(var + 1e-05f)));
#pragma unroll
    for (int i = 0; i < 5; ++i)
#pragma unroll
        for (int j = 0; j < 4; ++j)
            xv[i][j] = pack2(bfr(__uint_as_float(xv[i][j] << 16) - m_bf) * rden, bfr(__uint_as_float(xv[i][j] & 0xffff0000u) - m_bf) * rden);
}
__device__ __forceinline__ u32x4 ln_perm(const u32x4 o, const int n) {
    const u32 s0 = (n & 1) ? o[0] : o[2], s1 = (n & 1) ? o[1] : o[3];
    const u32 t0 = __builtin_amdgcn_update_dpp(0u, s0, 0xb1, 0xf, 0xf, false), t1 = __builtin_amdgcn_update_dpp(0u, s1, 0xb1, 0xf, 0xf, false);
    return (n & 1) ? (u32x4){t0, o[2], t1, o[3]} : (u32x4){o[0], t0, o[1], t1};
}
__device__ __forceinline__ u32 img_off(const int row, const int n, const int i) {
    return (u32)((row >> 4) * 20480 + (4 * i + (n >> 2)) * 1024 + (n & 3) * 256 + (row & 15) * 16);
}
__device__ __forceinline__ void ln_load(const int row0, const int lane, const u16* __restrict__ x, const u16* __restrict__ w_r, u32x4 (&xv)[5], u32x4 (&wv)[5]) {
    const int n = lane & 15, row = row0 + (lane >> 4);
#pragma unroll
    for (int i = 0; i < 5; ++i) xv[i] = *(const u32x4*)(x + (size_t)row * D_ + (n + 16 * i) * 8);
#pragma unroll
    for (int i = 0; i < 5; ++i) wv[i] = *(const u32x4*)(w_r + (n + 16 * i) * 8);
}
__device__ __forceinline__ void ln_store(const int row0, const int lane, const u32x4 (&xv)[5], const u32x4 (&wv)[5], rsrc_t rs) {
    const int n = lane & 15, row = row0 + (lane >> 4);
#pragma unroll
    for (int i = 0; i < 5; ++i) stx4(rs, (u32)OFF_XNP + img_off(row, n, i), ln_perm(xv[i], n));
    CFENCE();
    float dot = 0.f;
#pragma unroll
    for (int i = 0; i < 5; ++i) {
        stx4(rs, (u32)OFF_XN + (u32)(row * D_ + (n + 16 * i) * 8) * 2, xv[i]);
#pragma unroll
        for (int j = 0; j < 4; ++j)
            dot += __uint_as_float(xv[i][j] << 16) * __uint_as_float(wv[i][j] << 16) + __uint_as_float(xv[i][j] & 0xffff0000u) * __uint_as_float(wv[i][j] & 0xffff0000u);
    }
    dot = rsum16(dot);
    stx1(rs, n == 0 ? (u32)(OFF_R + row * 4) : OOB, __float_as_uint(bfr(dot)));
}
__device__ __forceinline__ u32x4 mk_srd(const char* ws) {
    const u64 a = (u64)(uintptr_t)ws;
    return (u32x4){(u32)a, (u32)(a >> 32), 0x7fffffffu, 0x00020000u};
}

template <typename F>
__device__ __forceinline__ u32 probe(F&& early, const int pi, const u32 xcc, const u32 hl, char* __restrict__ ws) {
    const u32x4 srd = mk_srd(ws);
    const u32 base = win_off(ws) + SLOT0 + (4u << 20) + 6u * 32768u + (u32)pi * 128u;
    const int lane = lane_id();
    const u32 zero = 0;
    asm volatile("buffer_atomic_or %0, %1, %2, %3 offen sc1\n\ts_waitcnt vmcnt(0)" :: "v"(zero), "v"(lane < 16 ? (u32)lane * 2048u : OOB), "s"(srd), "s"(base) : "memory");
    const u32 one = lane == 0 ? 0u : OOB;
    auto lat = [&](u32 q) {
        u64 t0, t1;
        asm volatile("s_memtime %0\n\tbuffer_atomic_or %2, %3, %4, %5 offen sc1\n\ts_waitcnt vmcnt(0)\n\ts_memtime %1\n\ts_waitcnt lgkmcnt(0)"
                     : "=&s"(t0), "=&s"(t1) : "v"(zero), "v"(one), "s"(srd), "s"(base + q * 2048u) : "memory");
        return (u32)t1 - (u32)t0;
    };
    const u32 q0 = (u32)pi & 11u, a0 = lat(q0), a1 = lat(q0 | 4u), a2 = lat(q0 ^ 8u), a3 = lat((q0 ^ 8u) | 4u), a4 = lat(q0 ^ 2u), a5 = lat((q0 ^ 2u) | 4u);
    const int d13 = (int)min(min(a0, a2), a4) - (int)min(min(a1, a3), a5), v13 = min(max(d13 >> 4, -7), 7);
    const u32 b13 = d13 > 0 ? 4u : 0u;
    const u32 flip = (xcc >> 2) | ((xcc & 3u) << 1) | (hl << 3);
    u32 votes = (u32)(7 + ((flip & 1u) ? -v13 : v13));
    early(votes);
    u32 best = ~0u;
#pragma unroll 1
    for (u32 i = 0; i < 8 * NPROBE; ++i) {
        const u32 c = (i + (u32)pi) & 7u, t = lat((c & 3u) | b13 | (c & 4u) * 2u);
        if ((u32)lane == c) best = min(best, t);
    }
#pragma unroll
    for (u32 i = 1; i < 4; ++i) {
        const u32 bit = 8u >> i;
        const int d = (int)best - __shfl_xor((int)best, (int)bit);
        int v = 0;
#pragma unroll
        for (u32 c = 0; c < 8; ++c) if (!(c & bit)) v += min(max(__builtin_amdgcn_readlane(d, c), -28), 28);
        v = (v + 8) >> 4;
        if ((flip >> i) & 1u) v = -v;
        votes |= (u32)(xcc >= 4 ? 7 : 7 + v) << (8 * i);
    }
    return votes;
}

typedef __attribute__((ext_vector_type(32))) unsigned int u32x32;
__device__ __forceinline__ u32x4 wfrag(const u32x32 (&W)[5], const int i) {
    return (u32x4){W[i >> 3][(i & 7) * 4], W[i >> 3][(i & 7) * 4 + 1], W[i >> 3][(i & 7) * 4 + 2], W[i >> 3][(i & 7) * 4 + 3]};
}
#define PIN_W(W) asm volatile("" : "+{v[16:47]}"(W[0]), "+{v[48:79]}"(W[1]), "+{v[80:111]}"(W[2]), "+{v[112:143]}"(W[3]), "+{v[144:175]}"(W[4]))
typedef __attribute__((ext_vector_type(4))) short s16x4;
typedef __attribute__((address_space(3))) s16x4* lds_s16x4p;
__device__ __forceinline__ u32x2 trd(u32 ldsaddr) {
    return __builtin_bit_cast(u32x2, __builtin_amdgcn_ds_read_tr16_b64_v4i16((lds_s16x4p)ldsaddr));
}
template <bool NT>
__device__ __forceinline__ void dma16(const void* g, u32 lds_base) {
    u32 sv;
    if (NT)
        asm volatile("s_mov_b32 %0, m0\n\ts_mov_b32 m0, %2\n\ts_nop 0\n\tglobal_load_lds_dwordx4 %1, off nt\n\ts_mov_b32 m0, %0"
                     : "=&s"(sv) : "v"(g), "s"(lds_base) : "memory");
    else
        asm volatile("s_mov_b32 %0, m0\n\ts_mov_b32 m0, %2\n\ts_nop 0\n\tglobal_load_lds_dwordx4 %1, off\n\ts_mov_b32 m0, %0"
                     : "=&s"(sv) : "v"(g), "s"(lds_base) : "memory");
}
__device__ __forceinline__ void dma16_sc1(const void* g, u32 lds_base) {
    u32 sv;
    asm volatile("s_mov_b32 %0, m0\n\ts_mov_b32 m0, %2\n\ts_nop 0\n\tglobal_load_lds_dwordx4 %1, off sc1\n\ts_mov_b32 m0, %0"
                 : "=&s"(sv) : "v"(g), "s"(lds_base) : "memory");
}
template <int ORD, typename F>
__device__ __forceinline__ void load_weights(F&& after, const u16* __restrict__ p0, const u16* __restrict__ p1, const bool wo1, char* smem,
                                             const int wave, const int lane, const int sa, const int ta, const int sb, const int tb,
                                             u32x32 (&W)[5]) {
    const u32 lbase = (u32)(uintptr_t)(__attribute__((address_space(3))) char*)smem;
    const int strm = wave >> 2;
    const bool wo = wo1 && strm == 1;
    const u16* src = strm ? p1 : p0;
    const u32 sstep = wo ? 128u : 128u * HD_;
    u32 so[8];
#pragma unroll
    for (int i = 0; i < 8; ++i) {
        const int j = (wave & 3) * 8 + i, r = 4 * j + (lane >> 4);
        const int f = (lane >> 4) | (((j >> 1) & 1) << 2);
        const int ch = (lane & 15) ^ (f << 1);
        const int e = (r & ~31) | (((r >> 2) & 1) << 4) | (((r >> 3) & 3) << 2) | (r & 3);
        so[i] = wo ? (u32)(e * D_ + ch * 8) : (u32)(((ORD >> 21) & 1 ? e : dmap(r)) * HD_ + ch * 8);
    }
    auto issue = [&](int st) {
        const u32 dst = __builtin_amdgcn_readfirstlane(lbase + (u32)((st & 1) * 65536 + strm * 32768 + (wave & 3) * 8192));
#pragma unroll
        for (int i = 0; i < 8; ++i) dma16<((ORD >> 20) & 1) != 0>(src + so[i] + (size_t)((ORD >> (4 * st)) & 15) * sstep, dst + i * 1024);
    };
    const int g = lane >> 4, q = (lane >> 2) & 3, p = lane & 3;
    const int f = q | ((g & 1) << 2);
    const u32 ra = lbase + (u32)(sa * 32768 + (8 * g + q) * 256 + (((ta ^ f) << 1) | (p >> 1)) * 16 + (p & 1) * 8);
    const u32 rb = lbase + (u32)(sb * 32768 + (8 * g + q) * 256 + (((tb ^ f) << 1) | (p >> 1)) * 16 + (p & 1) * 8);
    issue(0);
    issue(1);
#pragma unroll
    for (int st = 0; st < 5; ++st) {
        if (st < 4) asm volatile("s_waitcnt vmcnt(8)" ::: "memory"); else asm volatile("s_waitcnt vmcnt(0)" ::: "memory");
        LBAR();
#pragma unroll
        for (int k = 0; k < 4; ++k) {
            const u32 o = (u32)((st & 1) * 65536 + k * 8192);
            u32x2 a0 = trd(ra + o), a1 = trd(ra + o + 1024), b0 = trd(rb + o), b1 = trd(rb + o + 1024);
            const int ia = (((ORD >> (4 * st)) & 15) * 4 + k) * 4, ib = 80 + ia;
            if ((ORD >> 21) & 1) {
                u32x32& w = W[(ORD >> (4 * st)) & 15];
                w[k * 8] = a0[0]; w[k * 8 + 1] = b0[0]; w[k * 8 + 2] = a0[1]; w[k * 8 + 3] = b0[1];
                w[k * 8 + 4] = a1[0]; w[k * 8 + 5] = b1[0]; w[k * 8 + 6] = a1[1]; w[k * 8 + 7] = b1[1];
            } else {
                W[ia >> 5][ia & 31] = a0[0]; W[ia >> 5][(ia & 31) + 1] = a0[1]; W[ia >> 5][(ia & 31) + 2] = a1[0]; W[ia >> 5][(ia & 31) + 3] = a1[1];
                W[ib >> 5][ib & 31] = b0[0]; W[ib >> 5][(ib & 31) + 1] = b0[1]; W[ib >> 5][(ib & 31) + 2] = b1[0]; W[ib >> 5][(ib & 31) + 3] = b1[1];
            }
        }
        LBAR();
        after(st + 16);
        if (st + 2 < 5) issue(st + 2);
        after(st);
    }
}

template <typename F>
__device__ __forceinline__ void init_role(F&& settle, const int wave, const int ii, const u32 xcc, const u32 hl, const u16* __restrict__ x, const u16* __restrict__ w_r,
                                          char* __restrict__ ws, char* smem) {
    const int lane = lane_id(), tid = wave * 64 + lane;
    const rsrc_t rs = mk_rsrc(ws);
    u32* cnt = (u32*)(smem + L_MISC + 128);
    if (tid < 4) cnt[tid] = 0u;
    u64 tag;
    auto arrive = [&](int level, u32 total, u32x2 rec) {
        if (lane == 0 && __hip_atomic_fetch_add(cnt + level, 1u, __ATOMIC_RELAXED, __HIP_MEMORY_SCOPE_WORKGROUP) == total - 1)
            stx2(rs, (u32)(OFF_INI + (level * 64 + ii) * 8), rec);
    };
    u32x4 xv[5], wv[5];
    if (ii >= 48) {
        if (wave == 1) {
            const u32 votes = probe([&](u32 v) {
                tag = settle();
                stx2(rs, lane < 3 ? (u32)(OFF_INI + (lane * 64 + ii) * 8) : OOB, lane ? (u32x2){(u32)tag, (u32)(tag >> 32)} : (u32x2){v, cal_tag(tag, v)});
                DRAIN();
                XSTAMP(192 + ii, 220);
            }, ii - 48, xcc, hl, ws);
            XSTAMP(192 + ii, 223);
            stx2(rs, lane == 0 ? (u32)(OFF_INI + (192 + ii) * 8) : OOB, (u32x2){votes, cal_tag(tag, votes)});
        } else tag = settle();
        LBAR();
        return;
    }
    const bool two = wave >= 6 || (wave == 5 && ii < 32);
    const int row = wave >= 6 ? ii * 8 + (wave - 6) * 4 : 384 + ii * 24 + wave * 4, row2 = wave == 5 ? 1920 + (ii & 31) * 4 : 1536 + ii * 8 + (wave & 1) * 4;
    u32x4 xw[5];
    ln_load(row, lane, x, w_r, xv, wv);
    if (two) {
#pragma unroll
        for (int i = 0; i < 5; ++i) xw[i] = *(const u32x4*)(x + (size_t)(row2 + (lane >> 4)) * D_ + ((lane & 15) + 16 * i) * 8);
    }
    if (wave >= 6) {
        ln_regs(xv);
        tag = settle();
        ln_store(row, lane, xv, wv, rs);
        asm volatile("s_waitcnt vmcnt(6)" ::: "memory");
        XSTAMP(192 + ii, 221);
        arrive(1, 2u, (u32x2){(u32)tag, (u32)(tag >> 32)});
    } else {
        tag = settle();
        const u32 win = win_off(ws);
#pragma unroll
        for (int q = 0; q < INI_PER; ++q) {
            const u32 i = ((u32)q * 48 + ii) * 384 + tid;
            const bool z = i < INI_ZERO_CHUNKS;
            const u32 v = z ? 0u : SENT;
            stx4(rs, i < INI_CHUNKS ? (z ? (u32)ZERO_BEGIN + i * 16 : win + SLOT0 + (i - INI_ZERO_CHUNKS) * 16) : OOB, (u32x4){v, v, v, v});
        }
        ln_regs(xv);
        DRAIN();
        XSTAMP(192 + ii, 220);
        arrive(0, 6u, (u32x2){0u, cal_tag(tag, 0u)});
        ln_store(row, lane, xv, wv, rs);
    }
    if (two) {
        ln_regs(xw);
        ln_store(row2, lane, xw, wv, rs);
    }
    DRAIN();
    XSTAMP(192 + ii, 222);
    arrive(2, 8u, (u32x2){(u32)tag, (u32)(tag >> 32)});
    const u32 wp = wait_init(rs, ws, tag, lane, false), st = (u32)(ii * 8 + wave), so = slot_off(wp, 1 + (int)(st >> 7)) + (st & 126u) * 8192u + (st & 1u) * 4096u + (u32)lane * 16;
#pragma unroll
    for (int i = 0; i < 4; ++i) stx4(rs, so + i * 1024, (u32x4){SENT, SENT, SENT, SENT});
    DRAIN();
    XSTAMP(192 + ii, 225);
    arrive(3, 8u, (u32x2){0u, cal_tag(tag, 0u)});
    LBAR();
}

#define NOWV 2
#define STP
#define STA
#define LGP
#define LGA
__device__ __forceinline__ void s_fetch(u32 vm, u32 xo, u32x4 rs, u32 pfo, u32 pfd, u32 xd) {
    asm volatile(
        "s_mov_b32 s92, m0\n\t"
        "s_mov_b32 m0, %[pfd]\n\t"
        "s_add_u32 s93, %[pfo], 0x1000\n\t"
        "s_waitcnt vmcnt(0) lgkmcnt(0)\n\t"
        "s_barrier\n\t"
        "buffer_load_dwordx4 %[vm], %[rs], %[pfo] offen offset:0 sc1 lds\n\t"
        "buffer_load_dwordx4 %[vm], %[rs], %[pfo] offen offset:1024 sc1 lds\n\t"
        "buffer_load_dwordx4 %[vm], %[rs], %[pfo] offen offset:2048 sc1 lds\n\t"
        "buffer_load_dwordx4 %[vm], %[rs], %[pfo] offen offset:3072 sc1 lds\n\t"
        "s_add_u32 m0, m0, 0x1000\n\t"
        "s_nop 0\n\t"
        "buffer_load_dwordx4 %[vm], %[rs], s93 offen offset:0 sc1 lds\n\t"
        "buffer_load_dwordx4 %[vm], %[rs], s93 offen offset:1024 sc1 lds\n\t"
        "buffer_load_dwordx4 %[vm], %[rs], s93 offen offset:2048 sc1 lds\n\t"
        "buffer_load_dwordx4 %[vm], %[rs], s93 offen offset:3072 sc1 lds\n\t"
        "s_mov_b32 m0, %[xd]\n\t"
        "s_nop 0\n\t"
        "buffer_load_dword %[xo], %[rs], 0 offen sc1 lds\n\t"
        "s_mov_b32 m0, s92\n\t"
        :
        : [vm] "v"(vm), [xo] "v"(xo), [rs] "s"(rs), [pfo] "s"(pfo), [pfd] "s"(pfd), [xd] "s"(xd)
        : "s92", "s93", "vcc", "scc", "memory");
}

__device__ __forceinline__ void s_p0(const u32x32 (&W)[5], u32& redo, u32 va, u32 sidx, u32 qw, u32 kw, u32 fv STP) {
    asm volatile(
        "v_mov_b64 v[248:249], 0\n\t"
        "v_mov_b64 v[250:251], 0\n\t"
        "v_mov_b64 v[252:253], 0\n\t"
        "v_mov_b64 v[254:255], 0\n\t"
        "v_mov_b32 v205, %[fv]\n\t"
        "s_setprio 3\n\t"
        "s_barrier\n\t"
        "ds_read_b128 v[208:211], %[va] offset:0\n\t"
        "ds_read_b128 v[212:215], %[va] offset:1024\n\t"
        "ds_read_b128 v[216:219], %[va] offset:2048\n\t"
        "ds_read_b128 v[220:223], %[va] offset:3072\n\t"
        "ds_read_b128 v[224:227], %[va] offset:4096\n\t"
        "ds_read_b128 v[228:231], %[va] offset:5120\n\t"
        "s_waitcnt lgkmcnt(5)\n\t"
        "v_smfmac_f32_16x16x64_bf16 v[248:251], v[208:211], v[16:23], %[sidx]\n\t"
        "ds_read_b128 v[232:235], %[va] offset:6144\n\t"
        "s_waitcnt lgkmcnt(5)\n\t"
        "v_smfmac_f32_16x16x64_bf16 v[248:251], v[212:215], v[24:31], %[sidx]\n\t"
        "ds_read_b128 v[236:239], %[va] offset:7168\n\t"
        "s_waitcnt lgkmcnt(5)\n\t"
        "v_smfmac_f32_16x16x64_bf16 v[248:251], v[216:219], v[32:39], %[sidx]\n\t"
        "ds_read_b128 v[240:243], %[va] offset:8192\n\t"
        "s_waitcnt lgkmcnt(5)\n\t"
        "v_smfmac_f32_16x16x64_bf16 v[248:251], v[220:223], v[40:47], %[sidx]\n\t"
        "ds_read_b128 v[244:247], %[va] offset:10240\n\t"
        "s_waitcnt lgkmcnt(5)\n\t"
        "v_smfmac_f32_16x16x64_bf16 v[248:251], v[224:227], v[48:55], %[sidx]\n\t"
        "s_waitcnt lgkmcnt(4)\n\t"
        "v_smfmac_f32_16x16x64_bf16 v[248:251], v[228:231], v[56:63], %[sidx]\n\t"
        "s_waitcnt lgkmcnt(3)\n\t"
        "v_smfmac_f32_16x16x64_bf16 v[248:251], v[232:235], v[64:71], %[sidx]\n\t"
        "s_waitcnt lgkmcnt(2)\n\t"
        "v_smfmac_f32_16x16x64_bf16 v[248:251], v[236:239], v[72:79], %[sidx]\n\t"
        "s_waitcnt lgkmcnt(1)\n\t"
        "v_smfmac_f32_16x16x64_bf16 v[248:251], v[240:243], v[80:87], %[sidx]\n\t"
        "s_waitcnt lgkmcnt(0)\n\t"
        "v_smfmac_f32_16x16x64_bf16 v[248:251], v[244:247], v[88:95], %[sidx]\n\t"
        "s_setprio 0\n\t"
        "s_nop 6\n\t"
        "v_cvt_pk_bf16_f32 v200, v248, v249\n\t"
        "v_cvt_pk_bf16_f32 v201, v250, v251\n\t"
        "ds_write2_b32 %[qw], v200, v201 offset1:96\n\t"
        "v_fma_f32 v202, v248, v249, v250\n\t"
        "v_cmp_u_f32 vcc, v202, v251\n\t"
        "s_cbranch_vccnz Lnan%=\n\t"
        "Lok%=:\n\t"
        "s_waitcnt lgkmcnt(0)\n\t"
        "s_barrier\n\t"
        "v_smfmac_f32_16x16x64_bf16 v[252:255], v[208:211], v[96:103], %[sidx]\n\t"
        "v_smfmac_f32_16x16x64_bf16 v[252:255], v[212:215], v[104:111], %[sidx]\n\t"
        "v_smfmac_f32_16x16x64_bf16 v[252:255], v[216:219], v[112:119], %[sidx]\n\t"
        "v_smfmac_f32_16x16x64_bf16 v[252:255], v[220:223], v[120:127], %[sidx]\n\t"
        "v_smfmac_f32_16x16x64_bf16 v[252:255], v[224:227], v[128:135], %[sidx]\n\t"
        "v_smfmac_f32_16x16x64_bf16 v[252:255], v[228:231], v[136:143], %[sidx]\n\t"
        "v_smfmac_f32_16x16x64_bf16 v[252:255], v[232:235], v[144:151], %[sidx]\n\t"
        "v_smfmac_f32_16x16x64_bf16 v[252:255], v[236:239], v[152:159], %[sidx]\n\t"
        "v_smfmac_f32_16x16x64_bf16 v[252:255], v[240:243], v[160:167], %[sidx]\n\t"
        "v_smfmac_f32_16x16x64_bf16 v[252:255], v[244:247], v[168:175], %[sidx]\n\t"
        "s_nop 7\n\t"
        "v_add_f32 v252, v252, v254\n\t"
        "v_add_f32 v253, v253, v255\n\t"
        "v_cvt_pk_bf16_f32 v204, v252, v253\n\t"
        "ds_write_b64 %[kw], v[204:205]\n\t"
        "s_barrier\n\t"
        "s_branch Lend%=\n\t"
        "Lnan%=:\n\t"
        "v_max_u32 v207, v208, v209\n\t"
        "v_max3_u32 v207, v207, v210, v211\n\t"
        "v_max3_u32 v207, v207, v212, v213\n\t"
        "v_max3_u32 v207, v207, v214, v215\n\t"
        "v_max3_u32 v207, v207, v216, v217\n\t"
        "v_max3_u32 v207, v207, v218, v219\n\t"
        "v_max3_u32 v207, v207, v220, v221\n\t"
        "v_max3_u32 v207, v207, v222, v223\n\t"
        "v_max3_u32 v207, v207, v224, v225\n\t"
        "v_max3_u32 v207, v207, v226, v227\n\t"
        "v_max3_u32 v207, v207, v228, v229\n\t"
        "v_max3_u32 v207, v207, v230, v231\n\t"
        "v_max3_u32 v207, v207, v232, v233\n\t"
        "v_max3_u32 v207, v207, v234, v235\n\t"
        "v_max3_u32 v207, v207, v236, v237\n\t"
        "v_max3_u32 v207, v207, v238, v239\n\t"
        "v_max3_u32 v207, v207, v240, v241\n\t"
        "v_max3_u32 v207, v207, v242, v243\n\t"
        "v_max3_u32 v207, v207, v244, v245\n\t"
        "v_max3_u32 v207, v207, v246, v247\n\t"
        "v_cmp_eq_u32 vcc, 0xffbfffbf, v207\n\t"
        "s_cbranch_vccz Lok%=\n\t"
        "s_mov_b32 %[redo], 1\n\t"
        "s_barrier\n\t"
        "s_branch Lend%=\n\t"
        "Lend%=:\n\t"
        : [redo] "+&s"(redo)
        : [w0] "{v[16:47]}"(W[0]), [w1] "{v[48:79]}"(W[1]), [w2] "{v[80:111]}"(W[2]), [w3] "{v[112:143]}"(W[3]), [w4] "{v[144:175]}"(W[4]), [va] "v"(va), [sidx] "v"(sidx), [qw] "v"(qw), [kw] "v"(kw), [fv] "s"(fv)
        : "v200", "v201", "v202", "v203", "v204", "v205", "v206", "v207", "v208", "v209", "v210", "v211", "v212", "v213", "v214", "v215", "v216", "v217", "v218", "v219", "v220", "v221", "v222", "v223", "v224", "v225", "v226", "v227", "v228", "v229", "v230", "v231", "v232", "v233", "v234", "v235", "v236", "v237", "v238", "v239", "v240", "v241", "v242", "v243", "v244", "v245", "v246", "v247", "v248", "v249", "v250", "v251", "v252", "v253", "v254", "v255", "vcc", "scc", "memory");
}

__device__ __forceinline__ void s_p1(const u32x32 (&W)[5], u32& redo, u32 va, u32 sidx, u32 qw, u32 kw, u32 fv STP) {
    asm volatile(
        "v_mov_b64 v[248:249], 0\n\t"
        "v_mov_b64 v[250:251], 0\n\t"
        "v_mov_b64 v[252:253], 0\n\t"
        "v_mov_b64 v[254:255], 0\n\t"
        "v_mov_b32 v205, %[fv]\n\t"
        "s_setprio 3\n\t"
        "s_barrier\n\t"
        "ds_read_b128 v[208:211], %[va] offset:0\n\t"
        "ds_read_b128 v[212:215], %[va] offset:1024\n\t"
        "ds_read_b128 v[216:219], %[va] offset:2048\n\t"
        "ds_read_b128 v[220:223], %[va] offset:3072\n\t"
        "ds_read_b128 v[224:227], %[va] offset:4096\n\t"
        "ds_read_b128 v[228:231], %[va] offset:5120\n\t"
        "s_waitcnt lgkmcnt(5)\n\t"
        "v_smfmac_f32_16x16x64_bf16 v[248:251], v[208:211], v[16:23], %[sidx]\n\t"
        "ds_read_b128 v[232:235], %[va] offset:6144\n\t"
        "s_waitcnt lgkmcnt(5)\n\t"
        "v_smfmac_f32_16x16x64_bf16 v[248:251], v[212:215], v[24:31], %[sidx]\n\t"
        "ds_read_b128 v[236:239], %[va] offset:7168\n\t"
        "s_waitcnt lgkmcnt(5)\n\t"
        "v_smfmac_f32_16x16x64_bf16 v[248:251], v[216:219], v[32:39], %[sidx]\n\t"
        "ds_read_b128 v[240:243], %[va] offset:8192\n\t"
        "s_waitcnt lgkmcnt(5)\n\t"
        "v_smfmac_f32_16x16x64_bf16 v[248:251], v[220:223], v[40:47], %[sidx]\n\t"
        "ds_read_b128 v[244:247], %[va] offset:10240\n\t"
        "s_waitcnt lgkmcnt(5)\n\t"
        "v_smfmac_f32_16x16x64_bf16 v[248:251], v[224:227], v[48:55], %[sidx]\n\t"
        "s_waitcnt lgkmcnt(4)\n\t"
        "v_smfmac_f32_16x16x64_bf16 v[248:251], v[228:231], v[56:63], %[sidx]\n\t"
        "s_waitcnt lgkmcnt(3)\n\t"
        "v_smfmac_f32_16x16x64_bf16 v[248:251], v[232:235], v[64:71], %[sidx]\n\t"
        "s_waitcnt lgkmcnt(2)\n\t"
        "v_smfmac_f32_16x16x64_bf16 v[248:251], v[236:239], v[72:79], %[sidx]\n\t"
        "s_waitcnt lgkmcnt(1)\n\t"
        "v_smfmac_f32_16x16x64_bf16 v[248:251], v[240:243], v[80:87], %[sidx]\n\t"
        "s_waitcnt lgkmcnt(0)\n\t"
        "v_smfmac_f32_16x16x64_bf16 v[248:251], v[244:247], v[88:95], %[sidx]\n\t"
        "s_setprio 0\n\t"
        "s_nop 6\n\t"
        "v_cvt_pk_bf16_f32 v200, v248, v249\n\t"
        "v_cvt_pk_bf16_f32 v201, v250, v251\n\t"
        "ds_write2_b32 %[qw], v200, v201 offset1:96\n\t"
        "v_fma_f32 v202, v248, v249, v250\n\t"
        "v_cmp_u_f32 vcc, v202, v251\n\t"
        "s_cbranch_vccnz Lnan%=\n\t"
        "Lok%=:\n\t"
        "s_waitcnt lgkmcnt(0)\n\t"
        "s_barrier\n\t"
        "s_nop 15\n\t"
        "s_nop 15\n\t"
        "s_nop 15\n\t"
        "s_nop 3\n\t"
        "v_smfmac_f32_16x16x64_bf16 v[252:255], v[208:211], v[96:103], %[sidx]\n\t"
        "v_smfmac_f32_16x16x64_bf16 v[252:255], v[212:215], v[104:111], %[sidx]\n\t"
        "v_smfmac_f32_16x16x64_bf16 v[252:255], v[216:219], v[112:119], %[sidx]\n\t"
        "v_smfmac_f32_16x16x64_bf16 v[252:255], v[220:223], v[120:127], %[sidx]\n\t"
        "v_smfmac_f32_16x16x64_bf16 v[252:255], v[224:227], v[128:135], %[sidx]\n\t"
        "v_smfmac_f32_16x16x64_bf16 v[252:255], v[228:231], v[136:143], %[sidx]\n\t"
        "v_smfmac_f32_16x16x64_bf16 v[252:255], v[232:235], v[144:151], %[sidx]\n\t"
        "v_smfmac_f32_16x16x64_bf16 v[252:255], v[236:239], v[152:159], %[sidx]\n\t"
        "v_smfmac_f32_16x16x64_bf16 v[252:255], v[240:243], v[160:167], %[sidx]\n\t"
        "v_smfmac_f32_16x16x64_bf16 v[252:255], v[244:247], v[168:175], %[sidx]\n\t"
        "s_nop 7\n\t"
        "v_add_f32 v252, v252, v254\n\t"
        "v_add_f32 v253, v253, v255\n\t"
        "v_cvt_pk_bf16_f32 v204, v252, v253\n\t"
        "ds_write_b64 %[kw], v[204:205]\n\t"
        "s_barrier\n\t"
        "s_branch Lend%=\n\t"
        "Lnan%=:\n\t"
        "v_max_u32 v207, v208, v209\n\t"
        "v_max3_u32 v207, v207, v210, v211\n\t"
        "v_max3_u32 v207, v207, v212, v213\n\t"
        "v_max3_u32 v207, v207, v214, v215\n\t"
        "v_max3_u32 v207, v207, v216, v217\n\t"
        "v_max3_u32 v207, v207, v218, v219\n\t"
        "v_max3_u32 v207, v207, v220, v221\n\t"
        "v_max3_u32 v207, v207, v222, v223\n\t"
        "v_max3_u32 v207, v207, v224, v225\n\t"
        "v_max3_u32 v207, v207, v226, v227\n\t"
        "v_max3_u32 v207, v207, v228, v229\n\t"
        "v_max3_u32 v207, v207, v230, v231\n\t"
        "v_max3_u32 v207, v207, v232, v233\n\t"
        "v_max3_u32 v207, v207, v234, v235\n\t"
        "v_max3_u32 v207, v207, v236, v237\n\t"
        "v_max3_u32 v207, v207, v238, v239\n\t"
        "v_max3_u32 v207, v207, v240, v241\n\t"
        "v_max3_u32 v207, v207, v242, v243\n\t"
        "v_max3_u32 v207, v207, v244, v245\n\t"
        "v_max3_u32 v207, v207, v246, v247\n\t"
        "v_cmp_eq_u32 vcc, 0xffbfffbf, v207\n\t"
        "s_cbranch_vccz Lok%=\n\t"
        "s_mov_b32 %[redo], 1\n\t"
        "s_barrier\n\t"
        "s_branch Lend%=\n\t"
        "Lend%=:\n\t"
        : [redo] "+&s"(redo)
        : [w0] "{v[16:47]}"(W[0]), [w1] "{v[48:79]}"(W[1]), [w2] "{v[80:111]}"(W[2]), [w3] "{v[112:143]}"(W[3]), [w4] "{v[144:175]}"(W[4]), [va] "v"(va), [sidx] "v"(sidx), [qw] "v"(qw), [kw] "v"(kw), [fv] "s"(fv)
        : "v200", "v201", "v202", "v203", "v204", "v205", "v206", "v207", "v208", "v209", "v210", "v211", "v212", "v213", "v214", "v215", "v216", "v217", "v218", "v219", "v220", "v221", "v222", "v223", "v224", "v225", "v226", "v227", "v228", "v229", "v230", "v231", "v232", "v233", "v234", "v235", "v236", "v237", "v238", "v239", "v240", "v241", "v242", "v243", "v244", "v245", "v246", "v247", "v248", "v249", "v250", "v251", "v252", "v253", "v254", "v255", "vcc", "scc", "memory");
}

__device__ __forceinline__ void s_p2(const u32x32 (&W)[5], u32& redo, u32 va, u32 sidx, u32 qw, u32 kw, u32 fv, u32 vm, u32x4 rs, u32 pfo, u32 pfd STP) {
    asm volatile(
        "v_mov_b64 v[248:249], 0\n\t"
        "v_mov_b64 v[250:251], 0\n\t"
        "v_mov_b64 v[252:253], 0\n\t"
        "v_mov_b64 v[254:255], 0\n\t"
        "v_mov_b32 v205, %[fv]\n\t"
        "s_setprio 3\n\t"
        "s_barrier\n\t"
        "ds_read_b128 v[208:211], %[va] offset:0\n\t"
        "ds_read_b128 v[212:215], %[va] offset:1024\n\t"
        "ds_read_b128 v[216:219], %[va] offset:2048\n\t"
        "ds_read_b128 v[220:223], %[va] offset:3072\n\t"
        "ds_read_b128 v[224:227], %[va] offset:4096\n\t"
        "ds_read_b128 v[228:231], %[va] offset:5120\n\t"
        "s_waitcnt lgkmcnt(5)\n\t"
        "v_smfmac_f32_16x16x64_bf16 v[248:251], v[208:211], v[16:23], %[sidx]\n\t"
        "ds_read_b128 v[232:235], %[va] offset:6144\n\t"
        "s_waitcnt lgkmcnt(5)\n\t"
        "v_smfmac_f32_16x16x64_bf16 v[248:251], v[212:215], v[24:31], %[sidx]\n\t"
        "ds_read_b128 v[236:239], %[va] offset:7168\n\t"
        "s_waitcnt lgkmcnt(5)\n\t"
        "v_smfmac_f32_16x16x64_bf16 v[248:251], v[216:219], v[32:39], %[sidx]\n\t"
        "ds_read_b128 v[240:243], %[va] offset:8192\n\t"
        "s_waitcnt lgkmcnt(5)\n\t"
        "v_smfmac_f32_16x16x64_bf16 v[248:251], v[220:223], v[40:47], %[sidx]\n\t"
        "ds_read_b128 v[244:247], %[va] offset:10240\n\t"
        "s_waitcnt lgkmcnt(5)\n\t"
        "v_smfmac_f32_16x16x64_bf16 v[248:251], v[224:227], v[48:55], %[sidx]\n\t"
        "s_waitcnt lgkmcnt(4)\n\t"
        "v_smfmac_f32_16x16x64_bf16 v[248:251], v[228:231], v[56:63], %[sidx]\n\t"
        "s_waitcnt lgkmcnt(3)\n\t"
        "v_smfmac_f32_16x16x64_bf16 v[248:251], v[232:235], v[64:71], %[sidx]\n\t"
        "s_waitcnt lgkmcnt(2)\n\t"
        "v_smfmac_f32_16x16x64_bf16 v[248:251], v[236:239], v[72:79], %[sidx]\n\t"
        "s_waitcnt lgkmcnt(1)\n\t"
        "v_smfmac_f32_16x16x64_bf16 v[248:251], v[240:243], v[80:87], %[sidx]\n\t"
        "s_waitcnt lgkmcnt(0)\n\t"
        "v_smfmac_f32_16x16x64_bf16 v[248:251], v[244:247], v[88:95], %[sidx]\n\t"
        "s_setprio 0\n\t"
        "s_nop 6\n\t"
        "v_cvt_pk_bf16_f32 v200, v248, v249\n\t"
        "v_cvt_pk_bf16_f32 v201, v250, v251\n\t"
        "ds_write2_b32 %[qw], v200, v201 offset1:96\n\t"
        "v_fma_f32 v202, v248, v249, v250\n\t"
        "v_cmp_u_f32 vcc, v202, v251\n\t"
        "s_cmp_eq_u32 %[pfd], 0\n\t"
        "s_cbranch_scc1 Lnf%=\n\t"
        "s_mov_b32 s92, m0\n\t"
        "s_mov_b32 m0, %[pfd]\n\t"
        "s_add_u32 s93, %[pfo], 0x1000\n\t"
        "buffer_load_dwordx4 %[vm], %[rs], %[pfo] offen offset:0 sc1 lds\n\t"
        "buffer_load_dwordx4 %[vm], %[rs], %[pfo] offen offset:1024 sc1 lds\n\t"
        "buffer_load_dwordx4 %[vm], %[rs], %[pfo] offen offset:2048 sc1 lds\n\t"
        "buffer_load_dwordx4 %[vm], %[rs], %[pfo] offen offset:3072 sc1 lds\n\t"
        "s_add_u32 m0, m0, 0x1000\n\t"
        "s_nop 0\n\t"
        "buffer_load_dwordx4 %[vm], %[rs], s93 offen offset:0 sc1 lds\n\t"
        "buffer_load_dwordx4 %[vm], %[rs], s93 offen offset:1024 sc1 lds\n\t"
        "buffer_load_dwordx4 %[vm], %[rs], s93 offen offset:2048 sc1 lds\n\t"
        "buffer_load_dwordx4 %[vm], %[rs], s93 offen offset:3072 sc1 lds\n\t"
        "s_mov_b32 m0, s92\n\t"
        "Lnf%=:\n\t"
        "s_cbranch_vccnz Lnan%=\n\t"
        "Lok%=:\n\t"
        "s_waitcnt lgkmcnt(0)\n\t"
        "s_barrier\n\t"
        "v_smfmac_f32_16x16x64_bf16 v[252:255], v[208:211], v[96:103], %[sidx]\n\t"
        "v_smfmac_f32_16x16x64_bf16 v[252:255], v[212:215], v[104:111], %[sidx]\n\t"
        "v_smfmac_f32_16x16x64_bf16 v[252:255], v[216:219], v[112:119], %[sidx]\n\t"
        "v_smfmac_f32_16x16x64_bf16 v[252:255], v[220:223], v[120:127], %[sidx]\n\t"
        "v_smfmac_f32_16x16x64_bf16 v[252:255], v[224:227], v[128:135], %[sidx]\n\t"
        "v_smfmac_f32_16x16x64_bf16 v[252:255], v[228:231], v[136:143], %[sidx]\n\t"
        "v_smfmac_f32_16x16x64_bf16 v[252:255], v[232:235], v[144:151], %[sidx]\n\t"
        "v_smfmac_f32_16x16x64_bf16 v[252:255], v[236:239], v[152:159], %[sidx]\n\t"
        "v_smfmac_f32_16x16x64_bf16 v[252:255], v[240:243], v[160:167], %[sidx]\n\t"
        "v_smfmac_f32_16x16x64_bf16 v[252:255], v[244:247], v[168:175], %[sidx]\n\t"
        "s_nop 7\n\t"
        "v_add_f32 v252, v252, v254\n\t"
        "v_add_f32 v253, v253, v255\n\t"
        "v_cvt_pk_bf16_f32 v204, v252, v253\n\t"
        "ds_write_b64 %[kw], v[204:205]\n\t"
        "s_barrier\n\t"
        "s_branch Lend%=\n\t"
        "Lnan%=:\n\t"
        "v_max_u32 v207, v208, v209\n\t"
        "v_max3_u32 v207, v207, v210, v211\n\t"
        "v_max3_u32 v207, v207, v212, v213\n\t"
        "v_max3_u32 v207, v207, v214, v215\n\t"
        "v_max3_u32 v207, v207, v216, v217\n\t"
        "v_max3_u32 v207, v207, v218, v219\n\t"
        "v_max3_u32 v207, v207, v220, v221\n\t"
        "v_max3_u32 v207, v207, v222, v223\n\t"
        "v_max3_u32 v207, v207, v224, v225\n\t"
        "v_max3_u32 v207, v207, v226, v227\n\t"
        "v_max3_u32 v207, v207, v228, v229\n\t"
        "v_max3_u32 v207, v207, v230, v231\n\t"
        "v_max3_u32 v207, v207, v232, v233\n\t"
        "v_max3_u32 v207, v207, v234, v235\n\t"
        "v_max3_u32 v207, v207, v236, v237\n\t"
        "v_max3_u32 v207, v207, v238, v239\n\t"
        "v_max3_u32 v207, v207, v240, v241\n\t"
        "v_max3_u32 v207, v207, v242, v243\n\t"
        "v_max3_u32 v207, v207, v244, v245\n\t"
        "v_max3_u32 v207, v207, v246, v247\n\t"
        "v_cmp_eq_u32 vcc, 0xffbfffbf, v207\n\t"
        "s_cbranch_vccz Lok%=\n\t"
        "s_mov_b32 %[redo], 1\n\t"
        "s_barrier\n\t"
        "s_branch Lend%=\n\t"
        "Lend%=:\n\t"
        : [redo] "+&s"(redo)
        : [w0] "{v[16:47]}"(W[0]), [w1] "{v[48:79]}"(W[1]), [w2] "{v[80:111]}"(W[2]), [w3] "{v[112:143]}"(W[3]), [w4] "{v[144:175]}"(W[4]), [va] "v"(va), [sidx] "v"(sidx), [qw] "v"(qw), [kw] "v"(kw), [fv] "s"(fv), [vm] "v"(vm), [rs] "s"(rs), [pfo] "s"(pfo), [pfd] "s"(pfd)
        : "s92", "s93", "v200", "v201", "v202", "v203", "v204", "v205", "v206", "v207", "v208", "v209", "v210", "v211", "v212", "v213", "v214", "v215", "v216", "v217", "v218", "v219", "v220", "v221", "v222", "v223", "v224", "v225", "v226", "v227", "v228", "v229", "v230", "v231", "v232", "v233", "v234", "v235", "v236", "v237", "v238", "v239", "v240", "v241", "v242", "v243", "v244", "v245", "v246", "v247", "v248", "v249", "v250", "v251", "v252", "v253", "v254", "v255", "vcc", "scc", "memory");
}

__device__ __forceinline__ void s_e(const u32x32 (&W)[5], u32& redo, u32 va, u32 sidx, u32 qw, u32 kw, u32 fv, u32 kxa, u32 qtr, u32 eo, u32 ew, float lim, u32x4 rs, u32 so STP) {
    asm volatile(
        "v_mov_b64 v[248:249], 0\n\t"
        "v_mov_b64 v[250:251], 0\n\t"
        "v_mov_b64 v[252:253], 0\n\t"
        "v_mov_b64 v[254:255], 0\n\t"
        "v_mov_b32 v205, %[fv]\n\t"
        "ds_read_b128 v[176:179], %[kxa] offset:0\n\t"
        "ds_read_b128 v[180:183], %[kxa] offset:1024\n\t"
        "ds_read_b128 v[184:187], %[kxa] offset:2048\n\t"
        "ds_read_b128 v[188:191], %[kxa] offset:3072\n\t"
        "s_add_u32 s92, %[so], 0x1000\n\t"
        "s_add_u32 s93, %[so], 0x4000\n\t"
        "s_add_u32 s94, %[so], 0x5000\n\t"
        "s_setprio 2\n\t"
        "s_waitcnt lgkmcnt(0)\n\t"
        "s_barrier\n\t"
        "s_nop 11\n\t"
        "ds_read_b128 v[208:211], %[va] offset:0\n\t"
        "ds_read_b128 v[212:215], %[va] offset:1024\n\t"
        "ds_read_b128 v[216:219], %[va] offset:2048\n\t"
        "ds_read_b128 v[220:223], %[va] offset:3072\n\t"
        "ds_read_b128 v[224:227], %[va] offset:4096\n\t"
        "ds_read_b128 v[228:231], %[va] offset:5120\n\t"
        "s_waitcnt lgkmcnt(5)\n\t"
        "v_smfmac_f32_16x16x64_bf16 v[248:251], v[208:211], v[16:23], %[sidx]\n\t"
        "ds_read_b128 v[232:235], %[va] offset:6144\n\t"
        "s_waitcnt lgkmcnt(5)\n\t"
        "v_smfmac_f32_16x16x64_bf16 v[248:251], v[212:215], v[24:31], %[sidx]\n\t"
        "ds_read_b128 v[236:239], %[va] offset:7168\n\t"
        "s_waitcnt lgkmcnt(5)\n\t"
        "v_smfmac_f32_16x16x64_bf16 v[248:251], v[216:219], v[32:39], %[sidx]\n\t"
        "ds_read_b128 v[240:243], %[va] offset:8192\n\t"
        "s_waitcnt lgkmcnt(5)\n\t"
        "v_smfmac_f32_16x16x64_bf16 v[248:251], v[220:223], v[40:47], %[sidx]\n\t"
        "ds_read_b128 v[244:247], %[va] offset:10240\n\t"
        "s_waitcnt lgkmcnt(5)\n\t"
        "v_smfmac_f32_16x16x64_bf16 v[248:251], v[224:227], v[48:55], %[sidx]\n\t"
        "s_waitcnt lgkmcnt(4)\n\t"
        "v_smfmac_f32_16x16x64_bf16 v[248:251], v[228:231], v[56:63], %[sidx]\n\t"
        "s_waitcnt lgkmcnt(3)\n\t"
        "v_smfmac_f32_16x16x64_bf16 v[248:251], v[232:235], v[64:71], %[sidx]\n\t"
        "s_waitcnt lgkmcnt(2)\n\t"
        "v_smfmac_f32_16x16x64_bf16 v[248:251], v[236:239], v[72:79], %[sidx]\n\t"
        "s_waitcnt lgkmcnt(1)\n\t"
        "v_smfmac_f32_16x16x64_bf16 v[248:251], v[240:243], v[80:87], %[sidx]\n\t"
        "s_waitcnt lgkmcnt(0)\n\t"
        "v_smfmac_f32_16x16x64_bf16 v[248:251], v[244:247], v[88:95], %[sidx]\n\t"
        "v_smfmac_f32_16x16x64_bf16 v[252:255], v[208:211], v[96:103], %[sidx]\n\t"
        "v_smfmac_f32_16x16x64_bf16 v[252:255], v[212:215], v[104:111], %[sidx]\n\t"
        "v_cvt_pk_bf16_f32 v200, v248, v249\n\t"
        "v_cvt_pk_bf16_f32 v201, v250, v251\n\t"
        "ds_write2_b32 %[qw], v200, v201 offset1:96\n\t"
        "v_fma_f32 v202, v248, v249, v250\n\t"
        "v_smfmac_f32_16x16x64_bf16 v[252:255], v[216:219], v[112:119], %[sidx]\n\t"
        "v_cmp_u_f32 vcc, v202, v251\n\t"
        "v_smfmac_f32_16x16x64_bf16 v[252:255], v[220:223], v[120:127], %[sidx]\n\t"
        "s_waitcnt lgkmcnt(0)\n\t"
        "s_cbranch_vccnz Lnan%=\n\t"
        "Lok%=:\n\t"
        "s_barrier\n\t"
        "ds_read_b64_tr_b16 v[192:193], %[qtr] offset:0\n\t"
        "ds_read_b64_tr_b16 v[194:195], %[qtr] offset:768\n\t"
        "v_smfmac_f32_16x16x64_bf16 v[252:255], v[224:227], v[128:135], %[sidx]\n\t"
        "ds_read_b64_tr_b16 v[196:197], %[qtr] offset:1536\n\t"
        "ds_read_b64_tr_b16 v[198:199], %[qtr] offset:2304\n\t"
        "v_smfmac_f32_16x16x64_bf16 v[252:255], v[228:231], v[136:143], %[sidx]\n\t"
        "ds_read_b64_tr_b16 v[208:209], %[qtr] offset:3072\n\t"
        "ds_read_b64_tr_b16 v[210:211], %[qtr] offset:3840\n\t"
        "v_smfmac_f32_16x16x64_bf16 v[252:255], v[232:235], v[144:151], %[sidx]\n\t"
        "ds_read_b64_tr_b16 v[212:213], %[qtr] offset:4608\n\t"
        "ds_read_b64_tr_b16 v[214:215], %[qtr] offset:5376\n\t"
        "v_smfmac_f32_16x16x64_bf16 v[252:255], v[236:239], v[152:159], %[sidx]\n\t"
        "s_waitcnt lgkmcnt(6)\n\t"
        "v_mfma_f32_16x16x32_bf16 v[248:251], v[176:179], v[192:195], 0\n\t"
        "s_waitcnt lgkmcnt(4)\n\t"
        "v_mfma_f32_16x16x32_bf16 v[248:251], v[180:183], v[196:199], v[248:251]\n\t"
        "s_waitcnt lgkmcnt(2)\n\t"
        "v_mfma_f32_16x16x32_bf16 v[248:251], v[184:187], v[208:211], v[248:251]\n\t"
        "s_waitcnt lgkmcnt(0)\n\t"
        "v_mfma_f32_16x16x32_bf16 v[248:251], v[188:191], v[212:215], v[248:251]\n\t"
        "v_smfmac_f32_16x16x64_bf16 v[252:255], v[240:243], v[160:167], %[sidx]\n\t"
        "v_smfmac_f32_16x16x64_bf16 v[252:255], v[244:247], v[168:175], %[sidx]\n\t"
        "v_add_f32_dpp v192, v248, v248 row_ror:8 row_mask:0xf bank_mask:0x3\n\t"
        "v_add_f32_dpp v193, v249, v249 row_ror:8 row_mask:0xf bank_mask:0x3\n\t"
        "v_add_f32_dpp v192, v250, v250 row_ror:8 row_mask:0xf bank_mask:0xc\n\t"
        "v_add_f32_dpp v193, v251, v251 row_ror:8 row_mask:0xf bank_mask:0xc\n\t"
        "v_exp_f32 v194, v192\n\t"
        "v_exp_f32 v195, v193\n\t"
        "v_max_f32_e64 v196, |v192|, |v193|\n\t"
        "v_cvt_pk_bf16_f32 v194, v194, v195\n\t"
        "v_cmpx_le_f32 vcc, v196, %[lim]\n\t"
        "buffer_store_dword v194, %[eo], %[rs], %[so] offen sc1\n\t"
        "buffer_store_dword v194, %[eo], %[rs], s92 offen sc1\n\t"
        "buffer_store_dword v194, %[eo], %[rs], s93 offen sc1\n\t"
        "buffer_store_dword v194, %[eo], %[rs], s94 offen sc1\n\t"
        "s_mov_b64 exec, -1\n\t"
        "v_cndmask_b32 v194, -1, v194, vcc\n\t"
        "ds_write_b32 %[ew], v194\n\t"
        "v_add_f32 v252, v252, v254\n\t"
        "v_add_f32 v253, v253, v255\n\t"
        "v_cvt_pk_bf16_f32 v204, v252, v253\n\t"
        "ds_write_b64 %[kw], v[204:205]\n\t"
        "s_barrier\n\t"
        "s_branch Lend%=\n\t"
        "Lnan%=:\n\t"
        "v_max_u32 v207, v208, v209\n\t"
        "v_max3_u32 v207, v207, v210, v211\n\t"
        "v_max3_u32 v207, v207, v212, v213\n\t"
        "v_max3_u32 v207, v207, v214, v215\n\t"
        "v_max3_u32 v207, v207, v216, v217\n\t"
        "v_max3_u32 v207, v207, v218, v219\n\t"
        "v_max3_u32 v207, v207, v220, v221\n\t"
        "v_max3_u32 v207, v207, v222, v223\n\t"
        "v_max3_u32 v207, v207, v224, v225\n\t"
        "v_max3_u32 v207, v207, v226, v227\n\t"
        "v_max3_u32 v207, v207, v228, v229\n\t"
        "v_max3_u32 v207, v207, v230, v231\n\t"
        "v_max3_u32 v207, v207, v232, v233\n\t"
        "v_max3_u32 v207, v207, v234, v235\n\t"
        "v_max3_u32 v207, v207, v236, v237\n\t"
        "v_max3_u32 v207, v207, v238, v239\n\t"
        "v_max3_u32 v207, v207, v240, v241\n\t"
        "v_max3_u32 v207, v207, v242, v243\n\t"
        "v_max3_u32 v207, v207, v244, v245\n\t"
        "v_max3_u32 v207, v207, v246, v247\n\t"
        "v_cmp_eq_u32 vcc, 0xffbfffbf, v207\n\t"
        "s_cbranch_vccz Lok%=\n\t"
        "s_mov_b32 %[redo], 1\n\t"
        "s_barrier\n\t"
        "s_branch Lend%=\n\t"
        "Lend%=:\n\t"
        : [redo] "+&s"(redo)
        : [w0] "{v[16:47]}"(W[0]), [w1] "{v[48:79]}"(W[1]), [w2] "{v[80:111]}"(W[2]), [w3] "{v[112:143]}"(W[3]), [w4] "{v[144:175]}"(W[4]), [va] "v"(va), [sidx] "v"(sidx), [qw] "v"(qw), [kw] "v"(kw), [fv] "s"(fv), [kxa] "v"(kxa), [qtr] "v"(qtr), [eo] "v"(eo), [ew] "v"(ew), [lim] "v"(lim), [rs] "s"(rs), [so] "s"(so)
        : "s92", "s93", "s94", "v176", "v177", "v178", "v179", "v180", "v181", "v182", "v183", "v184", "v185", "v186", "v187", "v188", "v189", "v190", "v191", "v192", "v193", "v194", "v195", "v196", "v197", "v198", "v199", "v200", "v201", "v202", "v203", "v204", "v205", "v206", "v207", "v208", "v209", "v210", "v211", "v212", "v213", "v214", "v215", "v216", "v217", "v218", "v219", "v220", "v221", "v222", "v223", "v224", "v225", "v226", "v227", "v228", "v229", "v230", "v231", "v232", "v233", "v234", "v235", "v236", "v237", "v238", "v239", "v240", "v241", "v242", "v243", "v244", "v245", "v246", "v247", "v248", "v249", "v250", "v251", "v252", "v253", "v254", "v255", "vcc", "scc", "memory");
}

__device__ __forceinline__ void s_f(const u32x32 (&W)[5], u32& redo, u32 va, u32 sidx, u32 qw, u32 kw, u32 fv, u32& bad, u32x4 ones, u32 qtr, u32 er, u32 po, float c1, float ind0, float ind1, u32x4 rs, u32 so, float ce, float big, u64 jm STP) {
    asm volatile(
        "v_mov_b64 v[248:249], 0\n\t"
        "v_mov_b64 v[250:251], 0\n\t"
        "v_mov_b64 v[252:253], 0\n\t"
        "v_mov_b64 v[254:255], 0\n\t"
        "v_mov_b32 v205, %[fv]\n\t"
        "s_add_u32 s92, %[so], 0x1000\n\t"
        "s_add_u32 s93, %[so], 0x4000\n\t"
        "s_add_u32 s94, %[so], 0x5000\n\t"
        "s_mov_b32 %[bad], 0\n\t"
        "s_setprio 2\n\t"
        "s_barrier\n\t"
        "s_nop 11\n\t"
        "ds_read_b128 v[208:211], %[va] offset:0\n\t"
        "ds_read_b128 v[212:215], %[va] offset:1024\n\t"
        "ds_read_b128 v[216:219], %[va] offset:2048\n\t"
        "ds_read_b128 v[220:223], %[va] offset:3072\n\t"
        "ds_read_b128 v[224:227], %[va] offset:4096\n\t"
        "ds_read_b128 v[228:231], %[va] offset:5120\n\t"
        "s_waitcnt lgkmcnt(5)\n\t"
        "v_smfmac_f32_16x16x64_bf16 v[248:251], v[208:211], v[16:23], %[sidx]\n\t"
        "ds_read_b128 v[232:235], %[va] offset:6144\n\t"
        "s_waitcnt lgkmcnt(5)\n\t"
        "v_smfmac_f32_16x16x64_bf16 v[248:251], v[212:215], v[24:31], %[sidx]\n\t"
        "ds_read_b128 v[236:239], %[va] offset:7168\n\t"
        "s_waitcnt lgkmcnt(5)\n\t"
        "v_smfmac_f32_16x16x64_bf16 v[248:251], v[216:219], v[32:39], %[sidx]\n\t"
        "ds_read_b128 v[240:243], %[va] offset:8192\n\t"
        "s_waitcnt lgkmcnt(5)\n\t"
        "v_smfmac_f32_16x16x64_bf16 v[248:251], v[220:223], v[40:47], %[sidx]\n\t"
        "ds_read_b128 v[244:247], %[va] offset:10240\n\t"
        "s_waitcnt lgkmcnt(5)\n\t"
        "v_smfmac_f32_16x16x64_bf16 v[248:251], v[224:227], v[48:55], %[sidx]\n\t"
        "s_waitcnt lgkmcnt(4)\n\t"
        "v_smfmac_f32_16x16x64_bf16 v[248:251], v[228:231], v[56:63], %[sidx]\n\t"
        "s_waitcnt lgkmcnt(3)\n\t"
        "v_smfmac_f32_16x16x64_bf16 v[248:251], v[232:235], v[64:71], %[sidx]\n\t"
        "s_waitcnt lgkmcnt(2)\n\t"
        "v_smfmac_f32_16x16x64_bf16 v[248:251], v[236:239], v[72:79], %[sidx]\n\t"
        "s_waitcnt lgkmcnt(1)\n\t"
        "v_smfmac_f32_16x16x64_bf16 v[248:251], v[240:243], v[80:87], %[sidx]\n\t"
        "s_waitcnt lgkmcnt(0)\n\t"
        "v_smfmac_f32_16x16x64_bf16 v[248:251], v[244:247], v[88:95], %[sidx]\n\t"
        "v_smfmac_f32_16x16x64_bf16 v[252:255], v[208:211], v[96:103], %[sidx]\n\t"
        "v_smfmac_f32_16x16x64_bf16 v[252:255], v[212:215], v[104:111], %[sidx]\n\t"
        "v_cvt_pk_bf16_f32 v200, v248, v249\n\t"
        "v_cvt_pk_bf16_f32 v201, v250, v251\n\t"
        "ds_write2_b32 %[qw], v200, v201 offset1:96\n\t"
        "v_fma_f32 v202, v248, v249, v250\n\t"
        "v_smfmac_f32_16x16x64_bf16 v[252:255], v[216:219], v[112:119], %[sidx]\n\t"
        "v_cmp_u_f32 vcc, v202, v251\n\t"
        "v_smfmac_f32_16x16x64_bf16 v[252:255], v[220:223], v[120:127], %[sidx]\n\t"
        "s_waitcnt lgkmcnt(0)\n\t"
        "s_cbranch_vccnz Lnan%=\n\t"
        "Lok%=:\n\t"
        "s_barrier\n\t"
        "v_smfmac_f32_16x16x64_bf16 v[252:255], v[224:227], v[128:135], %[sidx]\n\t"
        "v_smfmac_f32_16x16x64_bf16 v[252:255], v[228:231], v[136:143], %[sidx]\n\t"
        "v_smfmac_f32_16x16x64_bf16 v[252:255], v[232:235], v[144:151], %[sidx]\n\t"
        "v_smfmac_f32_16x16x64_bf16 v[252:255], v[236:239], v[152:159], %[sidx]\n\t"
        "v_smfmac_f32_16x16x64_bf16 v[252:255], v[240:243], v[160:167], %[sidx]\n\t"
        "v_smfmac_f32_16x16x64_bf16 v[252:255], v[244:247], v[168:175], %[sidx]\n\t"
        "s_nop 7\n\t"
        "v_add_f32 v252, v252, v254\n\t"
        "v_add_f32 v253, v253, v255\n\t"
        "v_cvt_pk_bf16_f32 v204, v252, v253\n\t"
        "ds_write_b64 %[kw], v[204:205]\n\t"
        "v_mov_b32 v237, 0\n\t"
        "ds_read_b64_tr_b16 v[192:193], %[qtr] offset:0\n\t"
        "ds_read_b64_tr_b16 v[194:195], %[qtr] offset:768\n\t"
        "ds_read_b64_tr_b16 v[196:197], %[qtr] offset:1536\n\t"
        "ds_read_b64_tr_b16 v[198:199], %[qtr] offset:2304\n\t"
        "ds_read_b64_tr_b16 v[208:209], %[qtr] offset:3072\n\t"
        "ds_read_b64_tr_b16 v[210:211], %[qtr] offset:3840\n\t"
        "ds_read_b64_tr_b16 v[212:213], %[qtr] offset:4608\n\t"
        "ds_read_b64_tr_b16 v[214:215], %[qtr] offset:5376\n\t"
        "s_nop 15\n\t"
        "s_nop 5\n\t"
        "ds_read_b64_tr_b16 v[224:225], %[qtr] offset:7680\n\t"
        "ds_read_b64_tr_b16 v[226:227], %[qtr] offset:8448\n\t"
        "ds_read_b64_tr_b16 v[228:229], %[qtr] offset:10752\n\t"
        "ds_read_b64_tr_b16 v[230:231], %[qtr] offset:11520\n\t"
        "s_waitcnt lgkmcnt(2)\n\t"
        "v_mfma_f32_16x16x32_bf16 v[248:251], v[224:227], v[196:199], 0\n\t"
        "s_waitcnt lgkmcnt(0)\n\t"
        "v_mfma_f32_16x16x32_bf16 v[248:251], v[228:231], v[212:215], v[248:251]\n\t"
        "v_min3_u32 v203, v224, v226, v228\n\t"
        "v_min_u32 v203, v203, v230\n\t"
        "s_setprio 3\n\t"
        "s_barrier\n\t"
        "ds_read_b64_tr_b16 v[224:225], %[qtr] offset:6144\n\t"
        "ds_read_b64_tr_b16 v[226:227], %[qtr] offset:6912\n\t"
        "ds_read_b64_tr_b16 v[228:229], %[qtr] offset:9216\n\t"
        "ds_read_b64_tr_b16 v[230:231], %[qtr] offset:9984\n\t"
        "ds_read_b128 v[240:243], %[er]\n\t"
        "s_waitcnt lgkmcnt(3)\n\t"
        "v_mfma_f32_16x16x32_bf16 v[248:251], v[224:227], v[192:195], v[248:251]\n\t"
        "s_waitcnt lgkmcnt(1)\n\t"
        "v_mfma_f32_16x16x32_bf16 v[248:251], v[228:231], v[208:211], v[248:251]\n\t"
        "v_min3_u32 v203, v224, v226, v203\n\t"
        "s_waitcnt lgkmcnt(0)\n\t"
        "Lgo%=:\n\t"
        "v_mfma_f32_16x16x32_bf16 v[244:247], v[176:179], v[240:243], 0\n\t"
        "v_min3_u32 v203, v228, v230, v203\n\t"
        "v_cmp_ne_u32 s[96:97], %[fv], v203\n\t"
        "s_nop 0\n\t"
        "v_add_f32_dpp v248, v248, v248 row_ror:8 row_mask:0xf bank_mask:0xf\n\t"
        "v_add_f32_dpp v249, v249, v249 row_ror:8 row_mask:0xf bank_mask:0xf\n\t"
        "v_mul_f32 v248, %[ce], v248\n\t"
        "v_mul_f32 v249, %[ce], v249\n\t"
        "v_exp_f32 v250, v248\n\t"
        "v_exp_f32 v251, v249\n\t"
        "s_and_b64 s[96:97], s[96:97], %[jm]\n\t"
        "v_cvt_pk_bf16_f32 v236, v250, v251\n\t"
        "v_fma_f32 v250, v250, %[c1], %[ind0]\n\t"
        "v_fma_f32 v251, v251, %[c1], %[ind1]\n\t"
        "v_mfma_f32_16x16x16_bf16 v[244:247], v[176:177], v[236:237], v[244:247]\n\t"
        "s_cbranch_scc1 Lt%=\n\t"
        "s_nop 6\n\t"
        "v_rcp_f32 v246, v244\n\t"
        "v_cmpx_gt_f32 vcc, %[big], v244\n\t"
        "v_pk_mul_f32 v[250:251], v[250:251], v[246:247] op_sel_hi:[1,0]\n\t"
        "v_cvt_pk_bf16_f32 v250, v250, v251\n\t"
        "buffer_store_dword v250, %[po], %[rs], %[so] offen sc1\n\t"
        "buffer_store_dword v250, %[po], %[rs], s92 offen sc1\n\t"
        "buffer_store_dword v250, %[po], %[rs], s93 offen sc1\n\t"
        "buffer_store_dword v250, %[po], %[rs], s94 offen sc1\n\t"
        "s_cmp_lg_u64 exec, -1\n\t"
        "s_mov_b64 exec, -1\n\t"
        "s_cselect_b32 %[bad], 1, 0\n\t"
        "s_branch Lend%=\n\t"
        "Lt%=:\n\t"
        "s_nop 7\n\t"
        "ds_read_b64_tr_b16 v[216:217], %[qtr] offset:6144\n\t"
        "ds_read_b64_tr_b16 v[218:219], %[qtr] offset:6912\n\t"
        "ds_read_b64_tr_b16 v[220:221], %[qtr] offset:7680\n\t"
        "ds_read_b64_tr_b16 v[222:223], %[qtr] offset:8448\n\t"
        "ds_read_b64_tr_b16 v[224:225], %[qtr] offset:9216\n\t"
        "ds_read_b64_tr_b16 v[226:227], %[qtr] offset:9984\n\t"
        "ds_read_b64_tr_b16 v[228:229], %[qtr] offset:10752\n\t"
        "ds_read_b64_tr_b16 v[230:231], %[qtr] offset:11520\n\t"
        "ds_read_b128 v[240:243], %[er]\n\t"
        "s_waitcnt lgkmcnt(0)\n\t"
        "v_mfma_f32_16x16x32_bf16 v[248:251], v[216:219], v[192:195], 0\n\t"
        "v_mfma_f32_16x16x32_bf16 v[248:251], v[220:223], v[196:199], v[248:251]\n\t"
        "v_mfma_f32_16x16x32_bf16 v[248:251], v[224:227], v[208:211], v[248:251]\n\t"
        "v_mfma_f32_16x16x32_bf16 v[248:251], v[228:231], v[212:215], v[248:251]\n\t"
        "v_min3_u32 v203, v216, v218, v220\n\t"
        "v_min3_u32 v203, v222, v224, v203\n\t"
        "v_min_u32 v203, v226, v203\n\t"
        "s_branch Lgo%=\n\t"
        "Lnan%=:\n\t"
        "v_max_u32 v207, v208, v209\n\t"
        "v_max3_u32 v207, v207, v210, v211\n\t"
        "v_max3_u32 v207, v207, v212, v213\n\t"
        "v_max3_u32 v207, v207, v214, v215\n\t"
        "v_max3_u32 v207, v207, v216, v217\n\t"
        "v_max3_u32 v207, v207, v218, v219\n\t"
        "v_max3_u32 v207, v207, v220, v221\n\t"
        "v_max3_u32 v207, v207, v222, v223\n\t"
        "v_max3_u32 v207, v207, v224, v225\n\t"
        "v_max3_u32 v207, v207, v226, v227\n\t"
        "v_max3_u32 v207, v207, v228, v229\n\t"
        "v_max3_u32 v207, v207, v230, v231\n\t"
        "v_max3_u32 v207, v207, v232, v233\n\t"
        "v_max3_u32 v207, v207, v234, v235\n\t"
        "v_max3_u32 v207, v207, v236, v237\n\t"
        "v_max3_u32 v207, v207, v238, v239\n\t"
        "v_max3_u32 v207, v207, v240, v241\n\t"
        "v_max3_u32 v207, v207, v242, v243\n\t"
        "v_max3_u32 v207, v207, v244, v245\n\t"
        "v_max3_u32 v207, v207, v246, v247\n\t"
        "v_cmp_eq_u32 vcc, 0xffbfffbf, v207\n\t"
        "s_cbranch_vccz Lok%=\n\t"
        "s_mov_b32 %[redo], 1\n\t"
        "s_barrier\n\t"
        "s_branch Lend%=\n\t"
        "Lend%=:\n\t"
        : [redo] "+&s"(redo), [bad] "=&s"(bad)
        : [w0] "{v[16:47]}"(W[0]), [w1] "{v[48:79]}"(W[1]), [w2] "{v[80:111]}"(W[2]), [w3] "{v[112:143]}"(W[3]), [w4] "{v[144:175]}"(W[4]), [va] "v"(va), [sidx] "v"(sidx), [qw] "v"(qw), [kw] "v"(kw), [fv] "s"(fv), [ones] "{v[176:179]}"(ones), [qtr] "v"(qtr), [er] "v"(er), [po] "v"(po), [c1] "v"(c1), [ind0] "v"(ind0), [ind1] "v"(ind1), [rs] "s"(rs), [so] "s"(so), [ce] "s"(ce), [big] "s"(big), [jm] "s"(jm)
        : "s92", "s93", "s94", "s95", "s96", "s97", "v192", "v193", "v194", "v195", "v196", "v197", "v198", "v199", "v200", "v201", "v202", "v203", "v204", "v205", "v206", "v207", "v208", "v209", "v210", "v211", "v212", "v213", "v214", "v215", "v216", "v217", "v218", "v219", "v220", "v221", "v222", "v223", "v224", "v225", "v226", "v227", "v228", "v229", "v230", "v231", "v232", "v233", "v234", "v235", "v236", "v237", "v238", "v239", "v240", "v241", "v242", "v243", "v244", "v245", "v246", "v247", "v248", "v249", "v250", "v251", "v252", "v253", "v254", "v255", "vcc", "scc", "memory");
}

__device__ __forceinline__ void s_m(const u32x32 (&W)[5], u32& redo, u32 va, u32 sidx, u32 qw, u32 kw, u32 fv, u32& left, u32& c0, u32& r0, u32& t1, u32& t3, u32 tg, u32 kr, u32 vm, u32 so, u32 lb, u32x4 rs, u32 poll, u32 tsr STP) {
    asm volatile(
        "v_mov_b64 v[248:249], 0\n\t"
        "v_mov_b64 v[250:251], 0\n\t"
        "v_mov_b64 v[252:253], 0\n\t"
        "v_mov_b64 v[254:255], 0\n\t"
        "v_mov_b32 v205, %[fv]\n\t"
        "s_mov_b32 s94, m0\n\t"
        "s_add_u32 s92, %[so], 0x8000\n\t"
        "s_add_u32 s93, %[so], 0x10000\n\t"
        "s_mov_b32 m0, %[lb]\n\t"
        "s_setprio 2\n\t"
        "s_cmp_eq_u32 %[poll], 0\n\t"
        "s_cbranch_scc1 Lpb%=\n\t"
        "Lda_%=:\n\t"
        "s_memrealtime s[90:91]\n\t"
        "s_waitcnt lgkmcnt(0)\n\t"
        "s_mul_i32 s90, s90, 24\n\t"
        "s_sub_u32 s88, %[tg], s90\n\t"
        "s_sub_u32 s89, s88, 620\n\t"
        "s_cmp_gt_u32 s89, 0x4000\n\t"
        "s_cbranch_scc1 Lea_%=\n\t"
        "s_sleep 1\n\t"
        "s_branch Lda_%=\n\t"
        "Lea_%=:\n\t"
        "s_memrealtime s[64:65]\n\t"
        "s_nop 0\n\t"
        "s_memrealtime s[66:67]\n\t"
        "s_nop 0\n\t"
        "s_memrealtime s[68:69]\n\t"
        "s_nop 0\n\t"
        "s_memrealtime s[70:71]\n\t"
        "s_nop 0\n\t"
        "s_memrealtime s[72:73]\n\t"
        "s_nop 0\n\t"
        "s_memrealtime s[74:75]\n\t"
        "s_memtime s[90:91]\n\t"
        "s_memrealtime s[76:77]\n\t"
        "s_nop 0\n\t"
        "s_memrealtime s[78:79]\n\t"
        "s_nop 0\n\t"
        "s_memrealtime s[80:81]\n\t"
        "s_nop 0\n\t"
        "s_memrealtime s[82:83]\n\t"
        "s_nop 0\n\t"
        "s_memrealtime s[84:85]\n\t"
        "s_nop 0\n\t"
        "s_memrealtime s[86:87]\n\t"
        "s_nop 0\n\t"
        "s_nop 7\n\t"
        "s_waitcnt lgkmcnt(0)\n\t"
        "s_add_u32 s64, s64, s66\n\t"
        "s_add_u32 s64, s64, s68\n\t"
        "s_add_u32 s64, s64, s70\n\t"
        "s_add_u32 s64, s64, s72\n\t"
        "s_add_u32 s64, s64, s74\n\t"
        "s_add_u32 s64, s64, s76\n\t"
        "s_add_u32 s64, s64, s78\n\t"
        "s_add_u32 s64, s64, s80\n\t"
        "s_add_u32 s64, s64, s82\n\t"
        "s_add_u32 s64, s64, s84\n\t"
        "s_add_u32 s64, s64, s86\n\t"
        "s_lshl_b32 s64, s64, 1\n\t"
        "s_mov_b32 %[c0], s90\n\t"
        "s_mov_b32 %[r0], s64\n\t"
        "s_sub_u32 s88, %[tg], s64\n\t"
        "s_mul_i32 s88, s88, %[kr]\n\t"
        "s_ashr_i32 s88, s88, 12\n\t"
        "s_sub_u32 s88, s88, 244\n\t"
        "s_nop 1\n\t"
        "s_max_i32 s88, s88, 0\n\t"
        "s_min_i32 s88, s88, 384\n\t"
        "s_andn2_b32 s88, s88, 3\n\t"
        "s_getpc_b64 s[90:91]\n\t"
        "Lga_%=:\n\t"
        "s_sub_u32 s90, s90, s88\n\t"
        "s_subb_u32 s91, s91, 0\n\t"
        "s_add_u32 s90, s90, Lza_%=-Lga_%=\n\t"
        "s_addc_u32 s91, s91, 0\n\t"
        "s_setpc_b64 s[90:91]\n\t"
        "s_nop 0\n\ts_nop 0\n\ts_nop 0\n\ts_nop 0\n\t"
        "s_nop 0\n\ts_nop 0\n\ts_nop 0\n\ts_nop 0\n\t"
        "s_nop 0\n\ts_nop 0\n\ts_nop 0\n\ts_nop 0\n\t"
        "s_nop 0\n\ts_nop 0\n\ts_nop 0\n\ts_nop 0\n\t"
        "s_nop 0\n\ts_nop 0\n\ts_nop 0\n\ts_nop 0\n\t"
        "s_nop 0\n\ts_nop 0\n\ts_nop 0\n\ts_nop 0\n\t"
        "s_nop 0\n\ts_nop 0\n\ts_nop 0\n\ts_nop 0\n\t"
        "s_nop 0\n\ts_nop 0\n\ts_nop 0\n\ts_nop 0\n\t"
        "s_nop 0\n\ts_nop 0\n\ts_nop 0\n\ts_nop 0\n\t"
        "s_nop 0\n\ts_nop 0\n\ts_nop 0\n\ts_nop 0\n\t"
        "s_nop 0\n\ts_nop 0\n\ts_nop 0\n\ts_nop 0\n\t"
        "s_nop 0\n\ts_nop 0\n\ts_nop 0\n\ts_nop 0\n\t"
        "s_nop 0\n\ts_nop 0\n\ts_nop 0\n\ts_nop 0\n\t"
        "s_nop 0\n\ts_nop 0\n\ts_nop 0\n\ts_nop 0\n\t"
        "s_nop 0\n\ts_nop 0\n\ts_nop 0\n\ts_nop 0\n\t"
        "s_nop 0\n\ts_nop 0\n\ts_nop 0\n\ts_nop 0\n\t"
        "s_nop 0\n\ts_nop 0\n\ts_nop 0\n\ts_nop 0\n\t"
        "s_nop 0\n\ts_nop 0\n\ts_nop 0\n\ts_nop 0\n\t"
        "s_nop 0\n\ts_nop 0\n\ts_nop 0\n\ts_nop 0\n\t"
        "s_nop 0\n\ts_nop 0\n\ts_nop 0\n\ts_nop 0\n\t"
        "s_nop 0\n\ts_nop 0\n\ts_nop 0\n\ts_nop 0\n\t"
        "s_nop 0\n\ts_nop 0\n\ts_nop 0\n\ts_nop 0\n\t"
        "s_nop 0\n\ts_nop 0\n\ts_nop 0\n\ts_nop 0\n\t"
        "s_nop 0\n\ts_nop 0\n\ts_nop 0\n\ts_nop 0\n\t"
        "Lza_%=:\n\t"
        "buffer_load_dwordx4 %[vm], %[rs], %[so] offen offset:0 sc1 lds\n\t"
        "buffer_load_dwordx4 %[vm], %[rs], %[so] offen offset:1024 sc1 lds\n\t"
        "buffer_load_dwordx4 %[vm], %[rs], %[so] offen offset:2048 sc1 lds\n\t"
        "buffer_load_dwordx4 %[vm], %[rs], %[so] offen offset:3072 sc1 lds\n\t"
        "s_add_u32 m0, m0, 0x1000\n\t"
        "s_nop 0\n\t"
        "buffer_load_dwordx4 %[vm], %[rs], s92 offen offset:0 sc1 lds\n\t"
        "buffer_load_dwordx4 %[vm], %[rs], s92 offen offset:1024 sc1 lds\n\t"
        "buffer_load_dwordx4 %[vm], %[rs], s92 offen offset:2048 sc1 lds\n\t"
        "buffer_load_dwordx4 %[vm], %[rs], s92 offen offset:3072 sc1 lds\n\t"
        "s_add_u32 m0, m0, 0x1000\n\t"
        "s_nop 0\n\t"
        "buffer_load_dwordx4 %[vm], %[rs], s93 offen offset:0 sc1 lds\n\t"
        "buffer_load_dwordx4 %[vm], %[rs], s93 offen offset:2048 sc1 lds\n\t"
        "s_barrier\n\t"
        "s_waitcnt vmcnt(0)\n\t"
        "Lpb%=:\n\t"
        "s_barrier\n\t"
        "s_nop 11\n\t"
        "ds_read_b128 v[208:211], %[va] offset:0\n\t"
        "ds_read_b128 v[212:215], %[va] offset:1024\n\t"
        "ds_read_b128 v[216:219], %[va] offset:2048\n\t"
        "ds_read_b128 v[220:223], %[va] offset:3072\n\t"
        "ds_read_b128 v[224:227], %[va] offset:4096\n\t"
        "ds_read_b128 v[228:231], %[va] offset:5120\n\t"
        "s_waitcnt lgkmcnt(5)\n\t"
        "v_smfmac_f32_16x16x64_bf16 v[248:251], v[208:211], v[16:23], %[sidx]\n\t"
        "ds_read_b128 v[232:235], %[va] offset:6144\n\t"
        "s_waitcnt lgkmcnt(5)\n\t"
        "v_smfmac_f32_16x16x64_bf16 v[248:251], v[212:215], v[24:31], %[sidx]\n\t"
        "ds_read_b128 v[236:239], %[va] offset:7168\n\t"
        "s_waitcnt lgkmcnt(5)\n\t"
        "v_smfmac_f32_16x16x64_bf16 v[248:251], v[216:219], v[32:39], %[sidx]\n\t"
        "ds_read_b128 v[240:243], %[va] offset:8192\n\t"
        "s_waitcnt lgkmcnt(5)\n\t"
        "v_smfmac_f32_16x16x64_bf16 v[248:251], v[220:223], v[40:47], %[sidx]\n\t"
        "ds_read_b128 v[244:247], %[va] offset:10240\n\t"
        "s_waitcnt lgkmcnt(5)\n\t"
        "v_smfmac_f32_16x16x64_bf16 v[248:251], v[224:227], v[48:55], %[sidx]\n\t"
        "s_waitcnt lgkmcnt(4)\n\t"
        "v_smfmac_f32_16x16x64_bf16 v[248:251], v[228:231], v[56:63], %[sidx]\n\t"
        "s_waitcnt lgkmcnt(3)\n\t"
        "v_smfmac_f32_16x16x64_bf16 v[248:251], v[232:235], v[64:71], %[sidx]\n\t"
        "s_waitcnt lgkmcnt(2)\n\t"
        "v_smfmac_f32_16x16x64_bf16 v[248:251], v[236:239], v[72:79], %[sidx]\n\t"
        "s_waitcnt lgkmcnt(1)\n\t"
        "v_smfmac_f32_16x16x64_bf16 v[248:251], v[240:243], v[80:87], %[sidx]\n\t"
        "s_waitcnt lgkmcnt(0)\n\t"
        "v_smfmac_f32_16x16x64_bf16 v[248:251], v[244:247], v[88:95], %[sidx]\n\t"
        "v_smfmac_f32_16x16x64_bf16 v[252:255], v[208:211], v[96:103], %[sidx]\n\t"
        "v_smfmac_f32_16x16x64_bf16 v[252:255], v[212:215], v[104:111], %[sidx]\n\t"
        "v_cvt_pk_bf16_f32 v200, v248, v249\n\t"
        "v_cvt_pk_bf16_f32 v201, v250, v251\n\t"
        "ds_write2_b32 %[qw], v200, v201 offset1:96\n\t"
        "v_fma_f32 v202, v248, v249, v250\n\t"
        "v_smfmac_f32_16x16x64_bf16 v[252:255], v[216:219], v[112:119], %[sidx]\n\t"
        "v_cmp_u_f32 vcc, v202, v251\n\t"
        "v_smfmac_f32_16x16x64_bf16 v[252:255], v[220:223], v[120:127], %[sidx]\n\t"
        "s_waitcnt lgkmcnt(0)\n\t"
        "s_cbranch_vccnz Lnan%=\n\t"
        "Lok%=:\n\t"
        "s_barrier\n\t"
        "v_smfmac_f32_16x16x64_bf16 v[252:255], v[224:227], v[128:135], %[sidx]\n\t"
        "v_smfmac_f32_16x16x64_bf16 v[252:255], v[228:231], v[136:143], %[sidx]\n\t"
        "v_smfmac_f32_16x16x64_bf16 v[252:255], v[232:235], v[144:151], %[sidx]\n\t"
        "v_smfmac_f32_16x16x64_bf16 v[252:255], v[236:239], v[152:159], %[sidx]\n\t"
        "v_smfmac_f32_16x16x64_bf16 v[252:255], v[240:243], v[160:167], %[sidx]\n\t"
        "v_smfmac_f32_16x16x64_bf16 v[252:255], v[244:247], v[168:175], %[sidx]\n\t"
        "s_nop 7\n\t"
        "v_add_f32 v252, v252, v254\n\t"
        "v_add_f32 v253, v253, v255\n\t"
        "v_cvt_pk_bf16_f32 v204, v252, v253\n\t"
        "ds_write_b64 %[kw], v[204:205]\n\t"
        "s_barrier\n\t"
        "s_buffer_load_dwordx4 s[64:67], %[rs], %[tsr] offset:0x0\n\t"
        "s_buffer_load_dwordx4 s[68:71], %[rs], %[tsr] offset:0x80\n\t"
        "s_buffer_load_dwordx4 s[72:75], %[rs], %[tsr] offset:0x100\n\t"
        "s_buffer_load_dwordx4 s[76:79], %[rs], %[tsr] offset:0x180\n\t"
        "s_waitcnt lgkmcnt(0)\n\t"
        "s_xor_b32 s67, s67, %[t3]\n\t"
        "s_xor_b32 s71, s71, %[t3]\n\t"
        "s_xor_b32 s75, s75, %[t3]\n\t"
        "s_xor_b32 s79, s79, %[t3]\n\t"
        "s_sub_u32 s69, s69, s65\n\t"
        "s_sub_u32 s73, s73, s65\n\t"
        "s_sub_u32 s77, s77, s65\n\t"
        "s_max_i32 s69, s69, s73\n\t"
        "s_max_i32 s77, s77, 0\n\t"
        "s_max_i32 s69, s69, s77\n\t"
        "s_add_u32 s65, s65, s69\n\t"
        "s_or_b32 s66, s67, s71\n\t"
        "s_or_b32 s70, s75, s79\n\t"
        "s_or_b32 s70, s70, s66\n\t"
        "s_cmp_eq_u32 s70, 0\n\t"
        "s_cselect_b32 %[t3], 1, 0\n\t"
        "s_mov_b32 %[t1], s65\n\t"
        "s_branch Lend%=\n\t"
        "Lck%=:\n\t"
        "v_add_u32 v204, %[lb], %[vm]\n\t"
        "Lcl%=:\n\t"
        "s_mov_b32 m0, %[lb]\n\t"
        "s_nop 0\n\t"
        "buffer_load_dwordx4 %[vm], %[rs], %[so] offen offset:0 sc1 lds\n\t"
        "buffer_load_dwordx4 %[vm], %[rs], %[so] offen offset:1024 sc1 lds\n\t"
        "buffer_load_dwordx4 %[vm], %[rs], %[so] offen offset:2048 sc1 lds\n\t"
        "buffer_load_dwordx4 %[vm], %[rs], %[so] offen offset:3072 sc1 lds\n\t"
        "s_add_u32 m0, m0, 0x1000\n\t"
        "s_nop 0\n\t"
        "buffer_load_dwordx4 %[vm], %[rs], s92 offen offset:0 sc1 lds\n\t"
        "buffer_load_dwordx4 %[vm], %[rs], s92 offen offset:1024 sc1 lds\n\t"
        "buffer_load_dwordx4 %[vm], %[rs], s92 offen offset:2048 sc1 lds\n\t"
        "buffer_load_dwordx4 %[vm], %[rs], s92 offen offset:3072 sc1 lds\n\t"
        "s_add_u32 m0, m0, 0x1000\n\t"
        "s_nop 0\n\t"
        "buffer_load_dwordx4 %[vm], %[rs], s93 offen offset:0 sc1 lds\n\t"
        "buffer_load_dwordx4 %[vm], %[rs], s93 offen offset:2048 sc1 lds\n\t"
        "s_sub_u32 %[left], %[left], 1\n\t"
        "s_waitcnt vmcnt(9)\n\t"
        "ds_read_b128 v[208:211], v204 offset:0\n\t"
        "s_waitcnt vmcnt(8)\n\t"
        "ds_read_b128 v[212:215], v204 offset:1024\n\t"
        "s_waitcnt vmcnt(7)\n\t"
        "ds_read_b128 v[216:219], v204 offset:2048\n\t"
        "s_waitcnt vmcnt(6)\n\t"
        "ds_read_b128 v[220:223], v204 offset:3072\n\t"
        "s_waitcnt vmcnt(5)\n\t"
        "ds_read_b128 v[224:227], v204 offset:4096\n\t"
        "s_waitcnt vmcnt(4)\n\t"
        "ds_read_b128 v[228:231], v204 offset:5120\n\t"
        "s_waitcnt lgkmcnt(5)\n\t"
        "v_max3_u32 v207, v208, v208, v209\n\t"
        "v_max3_u32 v207, v207, v210, v211\n\t"
        "s_waitcnt vmcnt(3)\n\t"
        "ds_read_b128 v[232:235], v204 offset:6144\n\t"
        "s_waitcnt lgkmcnt(5)\n\t"
        "v_max3_u32 v207, v207, v212, v213\n\t"
        "v_max3_u32 v207, v207, v214, v215\n\t"
        "s_waitcnt vmcnt(2)\n\t"
        "ds_read_b128 v[236:239], v204 offset:7168\n\t"
        "s_waitcnt lgkmcnt(5)\n\t"
        "v_max3_u32 v207, v207, v216, v217\n\t"
        "v_max3_u32 v207, v207, v218, v219\n\t"
        "s_waitcnt vmcnt(1)\n\t"
        "ds_read_b128 v[240:243], v204 offset:8192\n\t"
        "s_waitcnt lgkmcnt(5)\n\t"
        "v_max3_u32 v207, v207, v220, v221\n\t"
        "v_max3_u32 v207, v207, v222, v223\n\t"
        "s_waitcnt vmcnt(0)\n\t"
        "ds_read_b128 v[244:247], v204 offset:10240\n\t"
        "s_waitcnt lgkmcnt(5)\n\t"
        "v_max3_u32 v207, v207, v224, v225\n\t"
        "v_max3_u32 v207, v207, v226, v227\n\t"
        "s_waitcnt lgkmcnt(4)\n\t"
        "v_max3_u32 v207, v207, v228, v229\n\t"
        "v_max3_u32 v207, v207, v230, v231\n\t"
        "s_waitcnt lgkmcnt(3)\n\t"
        "v_max3_u32 v207, v207, v232, v233\n\t"
        "v_max3_u32 v207, v207, v234, v235\n\t"
        "s_waitcnt lgkmcnt(2)\n\t"
        "v_max3_u32 v207, v207, v236, v237\n\t"
        "v_max3_u32 v207, v207, v238, v239\n\t"
        "s_waitcnt lgkmcnt(1)\n\t"
        "v_max3_u32 v207, v207, v240, v241\n\t"
        "v_max3_u32 v207, v207, v242, v243\n\t"
        "s_waitcnt lgkmcnt(0)\n\t"
        "v_max3_u32 v207, v207, v244, v245\n\t"
        "v_max3_u32 v207, v207, v246, v247\n\t"
        "v_cmp_eq_u32 vcc, 0xffbfffbf, v207\n\t"
        "s_cbranch_vccz Lpb%=\n\t"
        "s_cbranch_scc0 Lcl%=\n\t"
        "s_branch Lend%=\n\t"
        "Lnan%=:\n\t"
        "v_max_u32 v207, v208, v209\n\t"
        "v_max3_u32 v207, v207, v210, v211\n\t"
        "v_max3_u32 v207, v207, v212, v213\n\t"
        "v_max3_u32 v207, v207, v214, v215\n\t"
        "v_max3_u32 v207, v207, v216, v217\n\t"
        "v_max3_u32 v207, v207, v218, v219\n\t"
        "v_max3_u32 v207, v207, v220, v221\n\t"
        "v_max3_u32 v207, v207, v222, v223\n\t"
        "v_max3_u32 v207, v207, v224, v225\n\t"
        "v_max3_u32 v207, v207, v226, v227\n\t"
        "v_max3_u32 v207, v207, v228, v229\n\t"
        "v_max3_u32 v207, v207, v230, v231\n\t"
        "v_max3_u32 v207, v207, v232, v233\n\t"
        "v_max3_u32 v207, v207, v234, v235\n\t"
        "v_max3_u32 v207, v207, v236, v237\n\t"
        "v_max3_u32 v207, v207, v238, v239\n\t"
        "v_max3_u32 v207, v207, v240, v241\n\t"
        "v_max3_u32 v207, v207, v242, v243\n\t"
        "v_max3_u32 v207, v207, v244, v245\n\t"
        "v_max3_u32 v207, v207, v246, v247\n\t"
        "v_cmp_eq_u32 vcc, 0xffbfffbf, v207\n\t"
        "s_cbranch_vccz Lok%=\n\t"
        "s_mov_b32 %[redo], 1\n\t"
        "s_barrier\n\t"
        "v_mov_b64 v[248:249], 0\n\t"
        "v_mov_b64 v[250:251], 0\n\t"
        "v_mov_b64 v[252:253], 0\n\t"
        "v_mov_b64 v[254:255], 0\n\t"
        "s_branch Lck%=\n\t"
        "Lend%=:\n\t"
        "s_mov_b32 m0, s94\n\t"
        : [redo] "+&s"(redo), [c0] "+&s"(c0), [r0] "+&s"(r0), [t1] "+&s"(t1), [t3] "+&s"(t3), [left] "+&s"(left)
        : [w0] "{v[16:47]}"(W[0]), [w1] "{v[48:79]}"(W[1]), [w2] "{v[80:111]}"(W[2]), [w3] "{v[112:143]}"(W[3]), [w4] "{v[144:175]}"(W[4]), [va] "v"(va), [sidx] "v"(sidx), [qw] "v"(qw), [kw] "v"(kw), [fv] "s"(fv), [rs] "s"(rs), [poll] "s"(poll), [tg] "s"(tg), [kr] "s"(kr), [tsr] "s"(tsr), [vm] "v"(vm), [so] "s"(so), [lb] "s"(lb)
        : "s64", "s65", "s66", "s67", "s68", "s69", "s70", "s71", "s72", "s73", "s74", "s75", "s76", "s77", "s78", "s79", "s80", "s81", "s82", "s83", "s84", "s85", "s86", "s87", "s88", "s89", "s90", "s91", "s92", "s93", "s94", "v200", "v201", "v202", "v203", "v204", "v205", "v206", "v207", "v208", "v209", "v210", "v211", "v212", "v213", "v214", "v215", "v216", "v217", "v218", "v219", "v220", "v221", "v222", "v223", "v224", "v225", "v226", "v227", "v228", "v229", "v230", "v231", "v232", "v233", "v234", "v235", "v236", "v237", "v238", "v239", "v240", "v241", "v242", "v243", "v244", "v245", "v246", "v247", "v248", "v249", "v250", "v251", "v252", "v253", "v254", "v255", "vcc", "scc", "memory");
}

__device__ __forceinline__ void s_l(const u32x32 (&W)[5], u32& redo, u32 va, u32 sidx, u32 qw, u32 kw, u32 fv) {
    asm volatile(
        "v_mov_b64 v[248:249], 0\n\t"
        "v_mov_b64 v[250:251], 0\n\t"
        "v_mov_b64 v[252:253], 0\n\t"
        "v_mov_b64 v[254:255], 0\n\t"
        "v_mov_b32 v205, %[fv]\n\t"
        "s_barrier\n\t"
        "ds_read_b128 v[208:211], %[va] offset:0\n\t"
        "ds_read_b128 v[212:215], %[va] offset:1024\n\t"
        "ds_read_b128 v[216:219], %[va] offset:2048\n\t"
        "ds_read_b128 v[220:223], %[va] offset:3072\n\t"
        "ds_read_b128 v[224:227], %[va] offset:4096\n\t"
        "ds_read_b128 v[228:231], %[va] offset:5120\n\t"
        "ds_read_b128 v[232:235], %[va] offset:6144\n\t"
        "ds_read_b128 v[236:239], %[va] offset:7168\n\t"
        "ds_read_b128 v[240:243], %[va] offset:8192\n\t"
        "ds_read_b128 v[244:247], %[va] offset:10240\n\t"
        "s_waitcnt lgkmcnt(9)\n\t"
        "v_smfmac_f32_16x16x64_bf16 v[252:255], v[208:211], v[96:103], %[sidx]\n\t"
        "s_waitcnt lgkmcnt(8)\n\t"
        "v_smfmac_f32_16x16x64_bf16 v[252:255], v[212:215], v[104:111], %[sidx]\n\t"
        "s_waitcnt lgkmcnt(7)\n\t"
        "v_smfmac_f32_16x16x64_bf16 v[252:255], v[216:219], v[112:119], %[sidx]\n\t"
        "s_waitcnt lgkmcnt(6)\n\t"
        "v_smfmac_f32_16x16x64_bf16 v[252:255], v[220:223], v[120:127], %[sidx]\n\t"
        "s_waitcnt lgkmcnt(5)\n\t"
        "v_smfmac_f32_16x16x64_bf16 v[252:255], v[224:227], v[128:135], %[sidx]\n\t"
        "s_waitcnt lgkmcnt(4)\n\t"
        "v_smfmac_f32_16x16x64_bf16 v[252:255], v[228:231], v[136:143], %[sidx]\n\t"
        "s_waitcnt lgkmcnt(3)\n\t"
        "v_smfmac_f32_16x16x64_bf16 v[252:255], v[232:235], v[144:151], %[sidx]\n\t"
        "s_waitcnt lgkmcnt(2)\n\t"
        "v_smfmac_f32_16x16x64_bf16 v[252:255], v[236:239], v[152:159], %[sidx]\n\t"
        "s_waitcnt lgkmcnt(1)\n\t"
        "v_smfmac_f32_16x16x64_bf16 v[252:255], v[240:243], v[160:167], %[sidx]\n\t"
        "s_waitcnt lgkmcnt(0)\n\t"
        "v_smfmac_f32_16x16x64_bf16 v[252:255], v[244:247], v[168:175], %[sidx]\n\t"
        "s_nop 7\n\t"
        "v_add_f32 v252, v252, v254\n\t"
        "v_add_f32 v253, v253, v255\n\t"
        "v_cvt_pk_bf16_f32 v204, v252, v253\n\t"
        "ds_write_b64 %[kw], v[204:205]\n\t"
        "v_cmp_u_f32 vcc, v252, v253\n\t"
        "s_cbranch_vccnz Lnan%=\n\t"
        "Lok%=:\n\t"
        "s_waitcnt lgkmcnt(0)\n\t"
        "s_barrier\n\t"
        "s_branch Lend%=\n\t"
        "Lnan%=:\n\t"
        "v_max_u32 v207, v208, v209\n\t"
        "v_max3_u32 v207, v207, v210, v211\n\t"
        "v_max3_u32 v207, v207, v212, v213\n\t"
        "v_max3_u32 v207, v207, v214, v215\n\t"
        "v_max3_u32 v207, v207, v216, v217\n\t"
        "v_max3_u32 v207, v207, v218, v219\n\t"
        "v_max3_u32 v207, v207, v220, v221\n\t"
        "v_max3_u32 v207, v207, v222, v223\n\t"
        "v_max3_u32 v207, v207, v224, v225\n\t"
        "v_max3_u32 v207, v207, v226, v227\n\t"
        "v_max3_u32 v207, v207, v228, v229\n\t"
        "v_max3_u32 v207, v207, v230, v231\n\t"
        "v_max3_u32 v207, v207, v232, v233\n\t"
        "v_max3_u32 v207, v207, v234, v235\n\t"
        "v_max3_u32 v207, v207, v236, v237\n\t"
        "v_max3_u32 v207, v207, v238, v239\n\t"
        "v_max3_u32 v207, v207, v240, v241\n\t"
        "v_max3_u32 v207, v207, v242, v243\n\t"
        "v_max3_u32 v207, v207, v244, v245\n\t"
        "v_max3_u32 v207, v207, v246, v247\n\t"
        "v_cmp_eq_u32 vcc, 0xffbfffbf, v207\n\t"
        "s_cbranch_vccz Lok%=\n\t"
        "s_mov_b32 %[redo], 1\n\t"
        "s_barrier\n\t"
        "s_branch Lend%=\n\t"
        "Lend%=:\n\t"
        : [redo] "+&s"(redo)
        : [w0] "{v[16:47]}"(W[0]), [w1] "{v[48:79]}"(W[1]), [w2] "{v[80:111]}"(W[2]), [w3] "{v[112:143]}"(W[3]), [w4] "{v[144:175]}"(W[4]), [va] "v"(va), [sidx] "v"(sidx), [qw] "v"(qw), [kw] "v"(kw), [fv] "s"(fv)
        : "v200", "v201", "v202", "v203", "v204", "v205", "v206", "v207", "v208", "v209", "v210", "v211", "v212", "v213", "v214", "v215", "v216", "v217", "v218", "v219", "v220", "v221", "v222", "v223", "v224", "v225", "v226", "v227", "v228", "v229", "v230", "v231", "v232", "v233", "v234", "v235", "v236", "v237", "v238", "v239", "v240", "v241", "v242", "v243", "v244", "v245", "v246", "v247", "v248", "v249", "v250", "v251", "v252", "v253", "v254", "v255", "vcc", "scc", "memory");
}

__device__ __forceinline__ void gather(u32 tg, u32 kr, u32 vm, u32 so, u32 lb, u32x4 rs) {
    asm volatile(
        "s_mov_b32 s94, m0\n\t"
        "s_add_u32 s92, %[so], 0x8000\n\t"
        "s_add_u32 s93, %[so], 0x10000\n\t"
        "s_mov_b32 m0, %[lb]\n\t"
        "Lda_%=:\n\t"
        "s_memrealtime s[90:91]\n\t"
        "s_waitcnt lgkmcnt(0)\n\t"
        "s_mul_i32 s90, s90, 24\n\t"
        "s_sub_u32 s88, %[tg], s90\n\t"
        "s_sub_u32 s89, s88, 620\n\t"
        "s_cmp_gt_u32 s89, 0x4000\n\t"
        "s_cbranch_scc1 Lea_%=\n\t"
        "s_sleep 1\n\t"
        "s_branch Lda_%=\n\t"
        "Lea_%=:\n\t"
        "s_cmp_lt_i32 s88, 120\n\t"
        "s_cbranch_scc1 Lza_%=\n\t"
        "s_memrealtime s[64:65]\n\t"
        "s_nop 0\n\t"
        "s_memrealtime s[66:67]\n\t"
        "s_nop 0\n\t"
        "s_memrealtime s[68:69]\n\t"
        "s_nop 0\n\t"
        "s_memrealtime s[70:71]\n\t"
        "s_nop 0\n\t"
        "s_memrealtime s[72:73]\n\t"
        "s_nop 0\n\t"
        "s_memrealtime s[74:75]\n\t"
        "s_memtime s[90:91]\n\t"
        "s_memrealtime s[76:77]\n\t"
        "s_nop 0\n\t"
        "s_memrealtime s[78:79]\n\t"
        "s_nop 0\n\t"
        "s_memrealtime s[80:81]\n\t"
        "s_nop 0\n\t"
        "s_memrealtime s[82:83]\n\t"
        "s_nop 0\n\t"
        "s_memrealtime s[84:85]\n\t"
        "s_nop 0\n\t"
        "s_memrealtime s[86:87]\n\t"
        "s_nop 0\n\t"
        "s_nop 7\n\t"
        "s_waitcnt lgkmcnt(0)\n\t"
        "s_add_u32 s64, s64, s66\n\t"
        "s_add_u32 s64, s64, s68\n\t"
        "s_add_u32 s64, s64, s70\n\t"
        "s_add_u32 s64, s64, s72\n\t"
        "s_add_u32 s64, s64, s74\n\t"
        "s_add_u32 s64, s64, s76\n\t"
        "s_add_u32 s64, s64, s78\n\t"
        "s_add_u32 s64, s64, s80\n\t"
        "s_add_u32 s64, s64, s82\n\t"
        "s_add_u32 s64, s64, s84\n\t"
        "s_add_u32 s64, s64, s86\n\t"
        "s_lshl_b32 s64, s64, 1\n\t"
        "s_nop 1\n\t"
        "s_sub_u32 s88, %[tg], s64\n\t"
        "s_mul_i32 s88, s88, %[kr]\n\t"
        "s_ashr_i32 s88, s88, 12\n\t"
        "s_sub_u32 s88, s88, 244\n\t"
        "s_nop 1\n\t"
        "s_max_i32 s88, s88, 0\n\t"
        "s_min_i32 s88, s88, 384\n\t"
        "s_andn2_b32 s88, s88, 3\n\t"
        "s_getpc_b64 s[90:91]\n\t"
        "Lga_%=:\n\t"
        "s_sub_u32 s90, s90, s88\n\t"
        "s_subb_u32 s91, s91, 0\n\t"
        "s_add_u32 s90, s90, Lza_%=-Lga_%=\n\t"
        "s_addc_u32 s91, s91, 0\n\t"
        "s_setpc_b64 s[90:91]\n\t"
        "s_nop 0\n\ts_nop 0\n\ts_nop 0\n\ts_nop 0\n\t"
        "s_nop 0\n\ts_nop 0\n\ts_nop 0\n\ts_nop 0\n\t"
        "s_nop 0\n\ts_nop 0\n\ts_nop 0\n\ts_nop 0\n\t"
        "s_nop 0\n\ts_nop 0\n\ts_nop 0\n\ts_nop 0\n\t"
        "s_nop 0\n\ts_nop 0\n\ts_nop 0\n\ts_nop 0\n\t"
        "s_nop 0\n\ts_nop 0\n\ts_nop 0\n\ts_nop 0\n\t"
        "s_nop 0\n\ts_nop 0\n\ts_nop 0\n\ts_nop 0\n\t"
        "s_nop 0\n\ts_nop 0\n\ts_nop 0\n\ts_nop 0\n\t"
        "s_nop 0\n\ts_nop 0\n\ts_nop 0\n\ts_nop 0\n\t"
        "s_nop 0\n\ts_nop 0\n\ts_nop 0\n\ts_nop 0\n\t"
        "s_nop 0\n\ts_nop 0\n\ts_nop 0\n\ts_nop 0\n\t"
        "s_nop 0\n\ts_nop 0\n\ts_nop 0\n\ts_nop 0\n\t"
        "s_nop 0\n\ts_nop 0\n\ts_nop 0\n\ts_nop 0\n\t"
        "s_nop 0\n\ts_nop 0\n\ts_nop 0\n\ts_nop 0\n\t"
        "s_nop 0\n\ts_nop 0\n\ts_nop 0\n\ts_nop 0\n\t"
        "s_nop 0\n\ts_nop 0\n\ts_nop 0\n\ts_nop 0\n\t"
        "s_nop 0\n\ts_nop 0\n\ts_nop 0\n\ts_nop 0\n\t"
        "s_nop 0\n\ts_nop 0\n\ts_nop 0\n\ts_nop 0\n\t"
        "s_nop 0\n\ts_nop 0\n\ts_nop 0\n\ts_nop 0\n\t"
        "s_nop 0\n\ts_nop 0\n\ts_nop 0\n\ts_nop 0\n\t"
        "s_nop 0\n\ts_nop 0\n\ts_nop 0\n\ts_nop 0\n\t"
        "s_nop 0\n\ts_nop 0\n\ts_nop 0\n\ts_nop 0\n\t"
        "s_nop 0\n\ts_nop 0\n\ts_nop 0\n\ts_nop 0\n\t"
        "s_nop 0\n\ts_nop 0\n\ts_nop 0\n\ts_nop 0\n\t"
        "Lza_%=:\n\t"
        "buffer_load_dwordx4 %[vm], %[rs], %[so] offen offset:0 sc1 lds\n\t"
        "buffer_load_dwordx4 %[vm], %[rs], %[so] offen offset:1024 sc1 lds\n\t"
        "buffer_load_dwordx4 %[vm], %[rs], %[so] offen offset:2048 sc1 lds\n\t"
        "buffer_load_dwordx4 %[vm], %[rs], %[so] offen offset:3072 sc1 lds\n\t"
        "s_add_u32 m0, m0, 0x1000\n\t"
        "s_nop 0\n\t"
        "buffer_load_dwordx4 %[vm], %[rs], s92 offen offset:0 sc1 lds\n\t"
        "buffer_load_dwordx4 %[vm], %[rs], s92 offen offset:1024 sc1 lds\n\t"
        "buffer_load_dwordx4 %[vm], %[rs], s92 offen offset:2048 sc1 lds\n\t"
        "buffer_load_dwordx4 %[vm], %[rs], s92 offen offset:3072 sc1 lds\n\t"
        "s_add_u32 m0, m0, 0x1000\n\t"
        "s_nop 0\n\t"
        "buffer_load_dwordx4 %[vm], %[rs], s93 offen offset:0 sc1 lds\n\t"
        "buffer_load_dwordx4 %[vm], %[rs], s93 offen offset:2048 sc1 lds\n\t"
        "s_mov_b32 m0, s94\n\t"
        :
        : [rs] "s"(rs), [tg] "s"(tg), [kr] "s"(kr), [vm] "v"(vm), [so] "s"(so), [lb] "s"(lb)
        : "s64", "s65", "s66", "s67", "s68", "s69", "s70", "s71", "s72", "s73", "s74", "s75", "s76", "s77", "s78", "s79", "s80", "s81", "s82", "s83", "s84", "s85", "s86", "s87", "s88", "s89", "s90", "s91", "s92", "s93", "s94", "vcc", "scc", "memory");
}

__device__ __forceinline__ void ts_look(u32& t1, u32& ok, u32x4 rs, u32 tsr, u32 want) {
    asm volatile(
        "s_nop 4\n\t"
        "s_buffer_load_dwordx4 s[64:67], %[rs], %[tsr] offset:0x0\n\t"
        "s_buffer_load_dwordx4 s[68:71], %[rs], %[tsr] offset:0x80\n\t"
        "s_buffer_load_dwordx4 s[72:75], %[rs], %[tsr] offset:0x100\n\t"
        "s_buffer_load_dwordx4 s[76:79], %[rs], %[tsr] offset:0x180\n\t"
        "s_waitcnt lgkmcnt(0)\n\t"
        "s_xor_b32 s67, s67, %[want]\n\t"
        "s_xor_b32 s71, s71, %[want]\n\t"
        "s_xor_b32 s75, s75, %[want]\n\t"
        "s_xor_b32 s79, s79, %[want]\n\t"
        "s_sub_u32 s69, s69, s65\n\t"
        "s_sub_u32 s73, s73, s65\n\t"
        "s_sub_u32 s77, s77, s65\n\t"
        "s_max_i32 s69, s69, s73\n\t"
        "s_max_i32 s77, s77, 0\n\t"
        "s_max_i32 s69, s69, s77\n\t"
        "s_add_u32 s65, s65, s69\n\t"
        "s_or_b32 s66, s67, s71\n\t"
        "s_or_b32 s70, s75, s79\n\t"
        "s_or_b32 s70, s70, s66\n\t"
        "s_cmp_eq_u32 s70, 0\n\t"
        "s_cselect_b32 %[ok], 15, 0\n\t"
        "s_mov_b32 %[t1], s65\n\t"
        : [t1] "=&s"(t1), [ok] "=&s"(ok)
        : [rs] "s"(rs), [tsr] "s"(tsr), [want] "s"(want)
        : "s64", "s65", "s66", "s67", "s68", "s69", "s70", "s71", "s72", "s73", "s74", "s75", "s76", "s77", "s78", "s79", "vcc", "scc", "memory");
}

__device__ __forceinline__ void calib(u32& c0, u32& r0) {
    asm volatile(
        "s_memrealtime s[64:65]\n\t"
        "s_nop 0\n\t"
        "s_memrealtime s[66:67]\n\t"
        "s_nop 0\n\t"
        "s_memrealtime s[68:69]\n\t"
        "s_nop 0\n\t"
        "s_memrealtime s[70:71]\n\t"
        "s_nop 0\n\t"
        "s_memrealtime s[72:73]\n\t"
        "s_nop 0\n\t"
        "s_memrealtime s[74:75]\n\t"
        "s_memtime s[90:91]\n\t"
        "s_memrealtime s[76:77]\n\t"
        "s_nop 0\n\t"
        "s_memrealtime s[78:79]\n\t"
        "s_nop 0\n\t"
        "s_memrealtime s[80:81]\n\t"
        "s_nop 0\n\t"
        "s_memrealtime s[82:83]\n\t"
        "s_nop 0\n\t"
        "s_memrealtime s[84:85]\n\t"
        "s_nop 0\n\t"
        "s_memrealtime s[86:87]\n\t"
        "s_nop 0\n\t"
        "s_nop 7\n\t"
        "s_waitcnt lgkmcnt(0)\n\t"
        "s_add_u32 s64, s64, s66\n\t"
        "s_add_u32 s64, s64, s68\n\t"
        "s_add_u32 s64, s64, s70\n\t"
        "s_add_u32 s64, s64, s72\n\t"
        "s_add_u32 s64, s64, s74\n\t"
        "s_add_u32 s64, s64, s76\n\t"
        "s_add_u32 s64, s64, s78\n\t"
        "s_add_u32 s64, s64, s80\n\t"
        "s_add_u32 s64, s64, s82\n\t"
        "s_add_u32 s64, s64, s84\n\t"
        "s_add_u32 s64, s64, s86\n\t"
        "s_lshl_b32 s64, s64, 1\n\t"
        "s_mov_b32 %[c0], s90\n\t"
        "s_mov_b32 %[r0], s64\n\t"
        : [c0] "=&s"(c0), [r0] "=&s"(r0)
        :
        : "s64", "s65", "s66", "s67", "s68", "s69", "s70", "s71", "s72", "s73", "s74", "s75", "s76", "s77", "s78", "s79", "s80", "s81", "s82", "s83", "s84", "s85", "s86", "s87", "s88", "s89", "s90", "s91", "vcc", "scc", "memory");
}

__device__ __forceinline__ void px_step(u32x4& p0, u32& p1, u32 ka, u32 qa, u32 ma, u32 ktr, float ce) {
    asm volatile(
        "ds_read_b128 v[240:243], %[ma] offset:0\n\t"
        "ds_read_b128 v[244:247], %[ma] offset:1024\n\t"
        "ds_read_b128 v[208:211], %[qa] offset:0\n\t"
        "ds_read_b128 v[224:227], %[ka] offset:0\n\t"
        "ds_read_b128 v[212:215], %[qa] offset:1024\n\t"
        "ds_read_b128 v[228:231], %[ka] offset:1024\n\t"
        "ds_read_b128 v[216:219], %[qa] offset:2048\n\t"
        "ds_read_b128 v[232:235], %[ka] offset:2048\n\t"
        "ds_read_b128 v[220:223], %[qa] offset:3072\n\t"
        "ds_read_b128 v[236:239], %[ka] offset:3072\n\t"
        "s_waitcnt lgkmcnt(6)\n\t"
        "v_mfma_f32_16x16x32_bf16 v[240:243], v[224:227], v[208:211], v[240:243]\n\t"
        "ds_read_b128 v[224:227], %[ka] offset:4096\n\t"
        "s_waitcnt lgkmcnt(5)\n\t"
        "v_mfma_f32_16x16x32_bf16 v[240:243], v[228:231], v[212:215], v[240:243]\n\t"
        "ds_read_b128 v[228:231], %[ka] offset:5120\n\t"
        "s_waitcnt lgkmcnt(4)\n\t"
        "v_mfma_f32_16x16x32_bf16 v[240:243], v[232:235], v[216:219], v[240:243]\n\t"
        "ds_read_b128 v[232:235], %[ka] offset:6144\n\t"
        "s_waitcnt lgkmcnt(3)\n\t"
        "v_mfma_f32_16x16x32_bf16 v[240:243], v[236:239], v[220:223], v[240:243]\n\t"
        "ds_read_b128 v[236:239], %[ka] offset:7168\n\t"
        "s_waitcnt lgkmcnt(3)\n\t"
        "v_mfma_f32_16x16x32_bf16 v[244:247], v[224:227], v[208:211], v[244:247]\n\t"
        "ds_read_b64_tr_b16 v[224:225], %[ktr] offset:0\n\t"
        "ds_read_b64_tr_b16 v[226:227], %[ktr] offset:768\n\t"
        "s_waitcnt lgkmcnt(4)\n\t"
        "v_mfma_f32_16x16x32_bf16 v[244:247], v[228:231], v[212:215], v[244:247]\n\t"
        "ds_read_b64_tr_b16 v[228:229], %[ktr] offset:1536\n\t"
        "ds_read_b64_tr_b16 v[230:231], %[ktr] offset:2304\n\t"
        "s_waitcnt lgkmcnt(5)\n\t"
        "v_mfma_f32_16x16x32_bf16 v[244:247], v[232:235], v[216:219], v[244:247]\n\t"
        "ds_read_b64_tr_b16 v[232:233], %[ktr] offset:3072\n\t"
        "ds_read_b64_tr_b16 v[234:235], %[ktr] offset:3840\n\t"
        "s_waitcnt lgkmcnt(6)\n\t"
        "v_mfma_f32_16x16x32_bf16 v[244:247], v[236:239], v[220:223], v[244:247]\n\t"
        "ds_read_b64_tr_b16 v[236:237], %[ktr] offset:4608\n\t"
        "ds_read_b64_tr_b16 v[238:239], %[ktr] offset:5376\n\t"
        "s_waitcnt lgkmcnt(6)\n\t"
        "v_mfma_f32_16x16x32_bf16 v[248:251], v[224:227], v[208:211], 0\n\t"
        "s_waitcnt lgkmcnt(4)\n\t"
        "v_mfma_f32_16x16x32_bf16 v[248:251], v[228:231], v[212:215], v[248:251]\n\t"
        "s_waitcnt lgkmcnt(2)\n\t"
        "v_mfma_f32_16x16x32_bf16 v[248:251], v[232:235], v[216:219], v[248:251]\n\t"
        "s_waitcnt lgkmcnt(0)\n\t"
        "v_mfma_f32_16x16x32_bf16 v[248:251], v[236:239], v[220:223], v[248:251]\n\t"
        "v_max3_f32 v253, v240, v241, v242\n\t"
        "v_max3_f32 v253, v253, v243, v244\n\t"
        "v_max3_f32 v253, v253, v245, v246\n\t"
        "v_max_f32 v253, v253, v247\n\t"
        "s_nop 3\n\t"
        "v_mul_f32 v248, %[ce], v248\n\t"
        "v_mul_f32 v249, %[ce], v249\n\t"
        "v_max3_f32 v253, v253, v248, v249\n\t"
        "v_mov_b32 v254, v253\n\t"
        "s_nop 1\n\t"
        "v_permlane16_swap_b32 v253, v254\n\t"
        "v_max_f32 v253, v253, v254\n\t"
        "v_mov_b32 v254, v253\n\t"
        "s_nop 1\n\t"
        "v_permlane32_swap_b32 v253, v254\n\t"
        "v_max_f32 v253, v253, v254\n\t"
        "v_sub_f32 v240, v240, v253\n\t"
        "v_sub_f32 v241, v241, v253\n\t"
        "v_sub_f32 v242, v242, v253\n\t"
        "v_sub_f32 v243, v243, v253\n\t"
        "v_sub_f32 v244, v244, v253\n\t"
        "v_sub_f32 v245, v245, v253\n\t"
        "v_sub_f32 v246, v246, v253\n\t"
        "v_sub_f32 v247, v247, v253\n\t"
        "v_sub_f32 v248, v248, v253\n\t"
        "v_sub_f32 v249, v249, v253\n\t"
        "v_exp_f32 v240, v240\n\t"
        "v_exp_f32 v241, v241\n\t"
        "v_exp_f32 v242, v242\n\t"
        "v_exp_f32 v243, v243\n\t"
        "v_exp_f32 v244, v244\n\t"
        "v_exp_f32 v245, v245\n\t"
        "v_exp_f32 v246, v246\n\t"
        "v_exp_f32 v247, v247\n\t"
        "v_exp_f32 v248, v248\n\t"
        "v_exp_f32 v249, v249\n\t"
        "v_pk_add_f32 v[252:253], v[240:241], v[242:243]\n\t"
        "v_pk_add_f32 v[254:255], v[244:245], v[246:247]\n\t"
        "v_pk_add_f32 v[252:253], v[252:253], v[248:249]\n\t"
        "v_pk_add_f32 v[252:253], v[252:253], v[254:255]\n\t"
        "v_add_f32 v252, v252, v253\n\t"
        "v_mov_b32 v253, v252\n\t"
        "s_nop 1\n\t"
        "v_permlane16_swap_b32 v252, v253\n\t"
        "v_add_f32 v252, v252, v253\n\t"
        "v_mov_b32 v253, v252\n\t"
        "s_nop 1\n\t"
        "v_permlane32_swap_b32 v252, v253\n\t"
        "v_add_f32 v252, v252, v253\n\t"
        "v_rcp_f32 v252, v252\n\t"
        "s_nop 0\n\t"
        "v_pk_mul_f32 v[240:241], v[240:241], v[252:253] op_sel_hi:[1,0]\n\t"
        "v_pk_mul_f32 v[242:243], v[242:243], v[252:253] op_sel_hi:[1,0]\n\t"
        "v_pk_mul_f32 v[244:245], v[244:245], v[252:253] op_sel_hi:[1,0]\n\t"
        "v_pk_mul_f32 v[246:247], v[246:247], v[252:253] op_sel_hi:[1,0]\n\t"
        "v_pk_mul_f32 v[248:249], v[248:249], v[252:253] op_sel_hi:[1,0]\n\t"
        "v_cvt_pk_bf16_f32 v192, v240, v241\n\t"
        "v_cvt_pk_bf16_f32 v193, v242, v243\n\t"
        "v_cvt_pk_bf16_f32 v194, v244, v245\n\t"
        "v_cvt_pk_bf16_f32 v195, v246, v247\n\t"
        "v_cvt_pk_bf16_f32 v196, v248, v249\n\t"
        : [p0] "=&{v[192:195]}"(p0), [p1] "=&{v196}"(p1)
        : [ka] "v"(ka), [qa] "v"(qa), [ma] "v"(ma), [ktr] "v"(ktr), [ce] "s"(ce)
        : "v200", "v201", "v202", "v203", "v204", "v205", "v206", "v207", "v208", "v209", "v210", "v211", "v212", "v213", "v214", "v215", "v216", "v217", "v218", "v219", "v220", "v221", "v222", "v223", "v224", "v225", "v226", "v227", "v228", "v229", "v230", "v231", "v232", "v233", "v234", "v235", "v236", "v237", "v238", "v239", "v240", "v241", "v242", "v243", "v244", "v245", "v246", "v247", "v248", "v249", "v250", "v251", "v252", "v253", "v254", "v255", "vcc", "scc", "memory");
}

__device__ __forceinline__ void v_a0(const u32x32 (&W)[5], u32 va, u32 sidx, u32 vw STP) {
    asm volatile(
        "v_mov_b64 v[252:253], 0\n\t"
        "v_mov_b64 v[254:255], 0\n\t"
        "s_setprio 2\n\t"
        "s_barrier\n\t"
        "ds_read_b128 v[208:211], %[va] offset:0\n\t"
        "ds_read_b128 v[212:215], %[va] offset:512\n\t"
        "ds_read_b128 v[216:219], %[va] offset:1024\n\t"
        "ds_read_b128 v[220:223], %[va] offset:1536\n\t"
        "ds_read_b128 v[224:227], %[va] offset:2048\n\t"
        "ds_read_b128 v[228:231], %[va] offset:2560\n\t"
        "s_waitcnt lgkmcnt(5)\n\t"
        "v_smfmac_f32_16x16x64_bf16 v[252:255], v[208:211], v[16:23], %[sidx]\n\t"
        "ds_read_b128 v[232:235], %[va] offset:3072\n\t"
        "s_waitcnt lgkmcnt(5)\n\t"
        "v_smfmac_f32_16x16x64_bf16 v[252:255], v[212:215], v[24:31], %[sidx]\n\t"
        "ds_read_b128 v[236:239], %[va] offset:3584\n\t"
        "s_waitcnt lgkmcnt(5)\n\t"
        "v_smfmac_f32_16x16x64_bf16 v[252:255], v[216:219], v[32:39], %[sidx]\n\t"
        "ds_read_b128 v[240:243], %[va] offset:4096\n\t"
        "s_waitcnt lgkmcnt(5)\n\t"
        "v_smfmac_f32_16x16x64_bf16 v[252:255], v[220:223], v[40:47], %[sidx]\n\t"
        "ds_read_b128 v[244:247], %[va] offset:4608\n\t"
        "s_waitcnt lgkmcnt(5)\n\t"
        "v_smfmac_f32_16x16x64_bf16 v[252:255], v[224:227], v[48:55], %[sidx]\n\t"
        "ds_read_b128 v[208:211], %[va] offset:5120\n\t"
        "s_waitcnt lgkmcnt(5)\n\t"
        "v_smfmac_f32_16x16x64_bf16 v[252:255], v[228:231], v[56:63], %[sidx]\n\t"
        "ds_read_b128 v[212:215], %[va] offset:5632\n\t"
        "s_waitcnt lgkmcnt(5)\n\t"
        "v_smfmac_f32_16x16x64_bf16 v[252:255], v[232:235], v[64:71], %[sidx]\n\t"
        "ds_read_b128 v[216:219], %[va] offset:6144\n\t"
        "s_waitcnt lgkmcnt(5)\n\t"
        "v_smfmac_f32_16x16x64_bf16 v[252:255], v[236:239], v[72:79], %[sidx]\n\t"
        "ds_read_b128 v[220:223], %[va] offset:6656\n\t"
        "s_waitcnt lgkmcnt(5)\n\t"
        "v_smfmac_f32_16x16x64_bf16 v[252:255], v[240:243], v[80:87], %[sidx]\n\t"
        "ds_read_b128 v[224:227], %[va] offset:7168\n\t"
        "s_waitcnt lgkmcnt(5)\n\t"
        "v_smfmac_f32_16x16x64_bf16 v[252:255], v[244:247], v[88:95], %[sidx]\n\t"
        "ds_read_b128 v[228:231], %[va] offset:7680\n\t"
        "s_waitcnt lgkmcnt(5)\n\t"
        "v_smfmac_f32_16x16x64_bf16 v[252:255], v[208:211], v[96:103], %[sidx]\n\t"
        "ds_read_b128 v[232:235], %[va] offset:8192\n\t"
        "s_waitcnt lgkmcnt(5)\n\t"
        "v_smfmac_f32_16x16x64_bf16 v[252:255], v[212:215], v[104:111], %[sidx]\n\t"
        "ds_read_b128 v[236:239], %[va] offset:8704\n\t"
        "s_waitcnt lgkmcnt(5)\n\t"
        "v_smfmac_f32_16x16x64_bf16 v[252:255], v[216:219], v[112:119], %[sidx]\n\t"
        "ds_read_b128 v[240:243], %[va] offset:10240\n\t"
        "s_waitcnt lgkmcnt(5)\n\t"
        "v_smfmac_f32_16x16x64_bf16 v[252:255], v[220:223], v[120:127], %[sidx]\n\t"
        "ds_read_b128 v[244:247], %[va] offset:10752\n\t"
        "s_waitcnt lgkmcnt(5)\n\t"
        "v_smfmac_f32_16x16x64_bf16 v[252:255], v[224:227], v[128:135], %[sidx]\n\t"
        "s_waitcnt lgkmcnt(4)\n\t"
        "v_smfmac_f32_16x16x64_bf16 v[252:255], v[228:231], v[136:143], %[sidx]\n\t"
        "s_waitcnt lgkmcnt(3)\n\t"
        "v_smfmac_f32_16x16x64_bf16 v[252:255], v[232:235], v[144:151], %[sidx]\n\t"
        "s_waitcnt lgkmcnt(2)\n\t"
        "v_smfmac_f32_16x16x64_bf16 v[252:255], v[236:239], v[152:159], %[sidx]\n\t"
        "s_waitcnt lgkmcnt(1)\n\t"
        "v_smfmac_f32_16x16x64_bf16 v[252:255], v[240:243], v[160:167], %[sidx]\n\t"
        "s_waitcnt lgkmcnt(0)\n\t"
        "v_smfmac_f32_16x16x64_bf16 v[252:255], v[244:247], v[168:175], %[sidx]\n\t"
        "s_nop 7\n\t"
        "v_cvt_pk_bf16_f32 v248, v252, v253\n\t"
        "v_cvt_pk_bf16_f32 v249, v254, v255\n\t"
        "ds_write_b64 %[vw], v[248:249]\n\t"
        "s_waitcnt lgkmcnt(0)\n\t"
        "s_barrier\n\t"
        "s_setprio 0\n\t"
        :
        : [w0] "{v[16:47]}"(W[0]), [w1] "{v[48:79]}"(W[1]), [w2] "{v[80:111]}"(W[2]), [w3] "{v[112:143]}"(W[3]), [w4] "{v[144:175]}"(W[4]), [va] "v"(va), [sidx] "v"(sidx), [vw] "v"(vw)
        : "v204", "v205", "v206", "v207", "v208", "v209", "v210", "v211", "v212", "v213", "v214", "v215", "v216", "v217", "v218", "v219", "v220", "v221", "v222", "v223", "v224", "v225", "v226", "v227", "v228", "v229", "v230", "v231", "v232", "v233", "v234", "v235", "v236", "v237", "v238", "v239", "v240", "v241", "v242", "v243", "v244", "v245", "v246", "v247", "v248", "v249", "v250", "v251", "v252", "v253", "v254", "v255", "vcc", "scc", "memory");
}

__device__ __forceinline__ void v_a1(const u32x32 (&W)[5], u32 va, u32 sidx, u32 vw, u32& left, u32& c0, u32& r0, u32 tg, u32 kr, u32 poll, u32 vm, u32 so, u32 lb, u32x4 rs STP) {
    asm volatile(
        "v_mov_b64 v[252:253], 0\n\t"
        "v_mov_b64 v[254:255], 0\n\t"
        "s_setprio 2\n\t"
        "s_mov_b32 s94, m0\n\t"
        "s_add_u32 s92, %[so], 0x8000\n\t"
        "s_add_u32 s93, %[so], 0x10000\n\t"
        "s_mov_b32 m0, %[lb]\n\t"
        "s_cmp_eq_u32 %[poll], 2\n\t"
        "s_cbranch_scc1 Lck%=\n\t"
        "s_cmp_eq_u32 %[poll], 0\n\t"
        "s_cbranch_scc1 Lpb%=\n\t"
        "Lda_%=:\n\t"
        "s_memrealtime s[90:91]\n\t"
        "s_waitcnt lgkmcnt(0)\n\t"
        "s_mul_i32 s90, s90, 24\n\t"
        "s_sub_u32 s88, %[tg], s90\n\t"
        "s_sub_u32 s89, s88, 620\n\t"
        "s_cmp_gt_u32 s89, 0x4000\n\t"
        "s_cbranch_scc1 Lea_%=\n\t"
        "s_sleep 1\n\t"
        "s_branch Lda_%=\n\t"
        "Lea_%=:\n\t"
        "s_memrealtime s[64:65]\n\t"
        "s_nop 0\n\t"
        "s_memrealtime s[66:67]\n\t"
        "s_nop 0\n\t"
        "s_memrealtime s[68:69]\n\t"
        "s_nop 0\n\t"
        "s_memrealtime s[70:71]\n\t"
        "s_nop 0\n\t"
        "s_memrealtime s[72:73]\n\t"
        "s_nop 0\n\t"
        "s_memrealtime s[74:75]\n\t"
        "s_memtime s[90:91]\n\t"
        "s_memrealtime s[76:77]\n\t"
        "s_nop 0\n\t"
        "s_memrealtime s[78:79]\n\t"
        "s_nop 0\n\t"
        "s_memrealtime s[80:81]\n\t"
        "s_nop 0\n\t"
        "s_memrealtime s[82:83]\n\t"
        "s_nop 0\n\t"
        "s_memrealtime s[84:85]\n\t"
        "s_nop 0\n\t"
        "s_memrealtime s[86:87]\n\t"
        "s_nop 0\n\t"
        "s_nop 7\n\t"
        "s_waitcnt lgkmcnt(0)\n\t"
        "s_add_u32 s64, s64, s66\n\t"
        "s_add_u32 s64, s64, s68\n\t"
        "s_add_u32 s64, s64, s70\n\t"
        "s_add_u32 s64, s64, s72\n\t"
        "s_add_u32 s64, s64, s74\n\t"
        "s_add_u32 s64, s64, s76\n\t"
        "s_add_u32 s64, s64, s78\n\t"
        "s_add_u32 s64, s64, s80\n\t"
        "s_add_u32 s64, s64, s82\n\t"
        "s_add_u32 s64, s64, s84\n\t"
        "s_add_u32 s64, s64, s86\n\t"
        "s_lshl_b32 s64, s64, 1\n\t"
        "s_mov_b32 %[c0], s90\n\t"
        "s_mov_b32 %[r0], s64\n\t"
        "s_sub_u32 s88, %[tg], s64\n\t"
        "s_mul_i32 s88, s88, %[kr]\n\t"
        "s_ashr_i32 s88, s88, 12\n\t"
        "s_sub_u32 s88, s88, 244\n\t"
        "s_nop 1\n\t"
        "s_max_i32 s88, s88, 0\n\t"
        "s_min_i32 s88, s88, 384\n\t"
        "s_andn2_b32 s88, s88, 3\n\t"
        "s_getpc_b64 s[90:91]\n\t"
        "Lga_%=:\n\t"
        "s_sub_u32 s90, s90, s88\n\t"
        "s_subb_u32 s91, s91, 0\n\t"
        "s_add_u32 s90, s90, Lza_%=-Lga_%=\n\t"
        "s_addc_u32 s91, s91, 0\n\t"
        "s_setpc_b64 s[90:91]\n\t"
        "s_nop 0\n\ts_nop 0\n\ts_nop 0\n\ts_nop 0\n\t"
        "s_nop 0\n\ts_nop 0\n\ts_nop 0\n\ts_nop 0\n\t"
        "s_nop 0\n\ts_nop 0\n\ts_nop 0\n\ts_nop 0\n\t"
        "s_nop 0\n\ts_nop 0\n\ts_nop 0\n\ts_nop 0\n\t"
        "s_nop 0\n\ts_nop 0\n\ts_nop 0\n\ts_nop 0\n\t"
        "s_nop 0\n\ts_nop 0\n\ts_nop 0\n\ts_nop 0\n\t"
        "s_nop 0\n\ts_nop 0\n\ts_nop 0\n\ts_nop 0\n\t"
        "s_nop 0\n\ts_nop 0\n\ts_nop 0\n\ts_nop 0\n\t"
        "s_nop 0\n\ts_nop 0\n\ts_nop 0\n\ts_nop 0\n\t"
        "s_nop 0\n\ts_nop 0\n\ts_nop 0\n\ts_nop 0\n\t"
        "s_nop 0\n\ts_nop 0\n\ts_nop 0\n\ts_nop 0\n\t"
        "s_nop 0\n\ts_nop 0\n\ts_nop 0\n\ts_nop 0\n\t"
        "s_nop 0\n\ts_nop 0\n\ts_nop 0\n\ts_nop 0\n\t"
        "s_nop 0\n\ts_nop 0\n\ts_nop 0\n\ts_nop 0\n\t"
        "s_nop 0\n\ts_nop 0\n\ts_nop 0\n\ts_nop 0\n\t"
        "s_nop 0\n\ts_nop 0\n\ts_nop 0\n\ts_nop 0\n\t"
        "s_nop 0\n\ts_nop 0\n\ts_nop 0\n\ts_nop 0\n\t"
        "s_nop 0\n\ts_nop 0\n\ts_nop 0\n\ts_nop 0\n\t"
        "s_nop 0\n\ts_nop 0\n\ts_nop 0\n\ts_nop 0\n\t"
        "s_nop 0\n\ts_nop 0\n\ts_nop 0\n\ts_nop 0\n\t"
        "s_nop 0\n\ts_nop 0\n\ts_nop 0\n\ts_nop 0\n\t"
        "s_nop 0\n\ts_nop 0\n\ts_nop 0\n\ts_nop 0\n\t"
        "s_nop 0\n\ts_nop 0\n\ts_nop 0\n\ts_nop 0\n\t"
        "s_nop 0\n\ts_nop 0\n\ts_nop 0\n\ts_nop 0\n\t"
        "Lza_%=:\n\t"
        "buffer_load_dwordx4 %[vm], %[rs], %[so] offen offset:0 sc1 lds\n\t"
        "buffer_load_dwordx4 %[vm], %[rs], %[so] offen offset:1024 sc1 lds\n\t"
        "buffer_load_dwordx4 %[vm], %[rs], %[so] offen offset:2048 sc1 lds\n\t"
        "buffer_load_dwordx4 %[vm], %[rs], %[so] offen offset:3072 sc1 lds\n\t"
        "s_add_u32 m0, m0, 0x1000\n\t"
        "s_nop 0\n\t"
        "buffer_load_dwordx4 %[vm], %[rs], s92 offen offset:0 sc1 lds\n\t"
        "buffer_load_dwordx4 %[vm], %[rs], s92 offen offset:1024 sc1 lds\n\t"
        "buffer_load_dwordx4 %[vm], %[rs], s92 offen offset:2048 sc1 lds\n\t"
        "buffer_load_dwordx4 %[vm], %[rs], s92 offen offset:3072 sc1 lds\n\t"
        "s_add_u32 m0, m0, 0x1000\n\t"
        "s_nop 0\n\t"
        "buffer_load_dwordx4 %[vm], %[rs], s93 offen offset:0 sc1 lds\n\t"
        "buffer_load_dwordx4 %[vm], %[rs], s93 offen offset:2048 sc1 lds\n\t"
        "s_waitcnt vmcnt(0)\n\t"
        "Lpb%=:\n\t"
        "s_barrier\n\t"
        "ds_read_b128 v[208:211], %[va] offset:0\n\t"
        "ds_read_b128 v[212:215], %[va] offset:512\n\t"
        "ds_read_b128 v[216:219], %[va] offset:1024\n\t"
        "ds_read_b128 v[220:223], %[va] offset:1536\n\t"
        "ds_read_b128 v[224:227], %[va] offset:2048\n\t"
        "ds_read_b128 v[228:231], %[va] offset:2560\n\t"
        "s_waitcnt lgkmcnt(5)\n\t"
        "v_smfmac_f32_16x16x64_bf16 v[252:255], v[208:211], v[16:23], %[sidx]\n\t"
        "ds_read_b128 v[232:235], %[va] offset:3072\n\t"
        "s_waitcnt lgkmcnt(5)\n\t"
        "v_smfmac_f32_16x16x64_bf16 v[252:255], v[212:215], v[24:31], %[sidx]\n\t"
        "ds_read_b128 v[236:239], %[va] offset:3584\n\t"
        "s_waitcnt lgkmcnt(5)\n\t"
        "v_smfmac_f32_16x16x64_bf16 v[252:255], v[216:219], v[32:39], %[sidx]\n\t"
        "ds_read_b128 v[240:243], %[va] offset:4096\n\t"
        "s_waitcnt lgkmcnt(5)\n\t"
        "v_smfmac_f32_16x16x64_bf16 v[252:255], v[220:223], v[40:47], %[sidx]\n\t"
        "ds_read_b128 v[244:247], %[va] offset:4608\n\t"
        "s_waitcnt lgkmcnt(5)\n\t"
        "v_smfmac_f32_16x16x64_bf16 v[252:255], v[224:227], v[48:55], %[sidx]\n\t"
        "ds_read_b128 v[208:211], %[va] offset:5120\n\t"
        "s_waitcnt lgkmcnt(5)\n\t"
        "v_smfmac_f32_16x16x64_bf16 v[252:255], v[228:231], v[56:63], %[sidx]\n\t"
        "ds_read_b128 v[212:215], %[va] offset:5632\n\t"
        "s_waitcnt lgkmcnt(5)\n\t"
        "v_smfmac_f32_16x16x64_bf16 v[252:255], v[232:235], v[64:71], %[sidx]\n\t"
        "ds_read_b128 v[216:219], %[va] offset:6144\n\t"
        "s_waitcnt lgkmcnt(5)\n\t"
        "v_smfmac_f32_16x16x64_bf16 v[252:255], v[236:239], v[72:79], %[sidx]\n\t"
        "ds_read_b128 v[220:223], %[va] offset:6656\n\t"
        "s_waitcnt lgkmcnt(5)\n\t"
        "v_smfmac_f32_16x16x64_bf16 v[252:255], v[240:243], v[80:87], %[sidx]\n\t"
        "ds_read_b128 v[224:227], %[va] offset:7168\n\t"
        "s_waitcnt lgkmcnt(5)\n\t"
        "v_smfmac_f32_16x16x64_bf16 v[252:255], v[244:247], v[88:95], %[sidx]\n\t"
        "ds_read_b128 v[228:231], %[va] offset:7680\n\t"
        "s_waitcnt lgkmcnt(5)\n\t"
        "v_smfmac_f32_16x16x64_bf16 v[252:255], v[208:211], v[96:103], %[sidx]\n\t"
        "ds_read_b128 v[232:235], %[va] offset:8192\n\t"
        "s_waitcnt lgkmcnt(5)\n\t"
        "v_smfmac_f32_16x16x64_bf16 v[252:255], v[212:215], v[104:111], %[sidx]\n\t"
        "ds_read_b128 v[236:239], %[va] offset:8704\n\t"
        "s_waitcnt lgkmcnt(5)\n\t"
        "v_smfmac_f32_16x16x64_bf16 v[252:255], v[216:219], v[112:119], %[sidx]\n\t"
        "ds_read_b128 v[240:243], %[va] offset:10240\n\t"
        "s_waitcnt lgkmcnt(5)\n\t"
        "v_smfmac_f32_16x16x64_bf16 v[252:255], v[220:223], v[120:127], %[sidx]\n\t"
        "ds_read_b128 v[244:247], %[va] offset:10752\n\t"
        "s_waitcnt lgkmcnt(5)\n\t"
        "v_smfmac_f32_16x16x64_bf16 v[252:255], v[224:227], v[128:135], %[sidx]\n\t"
        "s_waitcnt lgkmcnt(4)\n\t"
        "v_smfmac_f32_16x16x64_bf16 v[252:255], v[228:231], v[136:143], %[sidx]\n\t"
        "s_waitcnt lgkmcnt(3)\n\t"
        "v_smfmac_f32_16x16x64_bf16 v[252:255], v[232:235], v[144:151], %[sidx]\n\t"
        "s_waitcnt lgkmcnt(2)\n\t"
        "v_smfmac_f32_16x16x64_bf16 v[252:255], v[236:239], v[152:159], %[sidx]\n\t"
        "s_waitcnt lgkmcnt(1)\n\t"
        "v_smfmac_f32_16x16x64_bf16 v[252:255], v[240:243], v[160:167], %[sidx]\n\t"
        "s_waitcnt lgkmcnt(0)\n\t"
        "v_smfmac_f32_16x16x64_bf16 v[252:255], v[244:247], v[168:175], %[sidx]\n\t"
        "s_nop 7\n\t"
        "v_cvt_pk_bf16_f32 v248, v252, v253\n\t"
        "v_cvt_pk_bf16_f32 v249, v254, v255\n\t"
        "ds_write_b64 %[vw], v[248:249]\n\t"
        "s_waitcnt lgkmcnt(0)\n\t"
        "s_barrier\n\t"
        "s_setprio 0\n\t"
        "s_branch Lend%=\n\t"
        "Lck%=:\n\t"
        "v_add_u32 v204, %[lb], %[vm]\n\t"
        "Lcl%=:\n\t"
        "s_mov_b32 m0, %[lb]\n\t"
        "s_nop 0\n\t"
        "buffer_load_dwordx4 %[vm], %[rs], %[so] offen offset:0 sc1 lds\n\t"
        "buffer_load_dwordx4 %[vm], %[rs], %[so] offen offset:1024 sc1 lds\n\t"
        "buffer_load_dwordx4 %[vm], %[rs], %[so] offen offset:2048 sc1 lds\n\t"
        "buffer_load_dwordx4 %[vm], %[rs], %[so] offen offset:3072 sc1 lds\n\t"
        "s_add_u32 m0, m0, 0x1000\n\t"
        "s_nop 0\n\t"
        "buffer_load_dwordx4 %[vm], %[rs], s92 offen offset:0 sc1 lds\n\t"
        "buffer_load_dwordx4 %[vm], %[rs], s92 offen offset:1024 sc1 lds\n\t"
        "buffer_load_dwordx4 %[vm], %[rs], s92 offen offset:2048 sc1 lds\n\t"
        "buffer_load_dwordx4 %[vm], %[rs], s92 offen offset:3072 sc1 lds\n\t"
        "s_add_u32 m0, m0, 0x1000\n\t"
        "s_nop 0\n\t"
        "buffer_load_dwordx4 %[vm], %[rs], s93 offen offset:0 sc1 lds\n\t"
        "buffer_load_dwordx4 %[vm], %[rs], s93 offen offset:2048 sc1 lds\n\t"
        "s_sub_u32 %[left], %[left], 1\n\t"
        "s_waitcnt vmcnt(9)\n\t"
        "ds_read_b128 v[208:211], v204 offset:0\n\t"
        "s_waitcnt vmcnt(8)\n\t"
        "ds_read_b128 v[212:215], v204 offset:1024\n\t"
        "s_waitcnt vmcnt(7)\n\t"
        "ds_read_b128 v[216:219], v204 offset:2048\n\t"
        "s_waitcnt vmcnt(6)\n\t"
        "ds_read_b128 v[220:223], v204 offset:3072\n\t"
        "s_waitcnt vmcnt(5)\n\t"
        "ds_read_b128 v[224:227], v204 offset:4096\n\t"
        "s_waitcnt vmcnt(4)\n\t"
        "ds_read_b128 v[228:231], v204 offset:5120\n\t"
        "s_waitcnt lgkmcnt(5)\n\t"
        "v_max3_u32 v207, v208, v208, v209\n\t"
        "v_max3_u32 v207, v207, v210, v211\n\t"
        "s_waitcnt vmcnt(3)\n\t"
        "ds_read_b128 v[232:235], v204 offset:6144\n\t"
        "s_waitcnt lgkmcnt(5)\n\t"
        "v_max3_u32 v207, v207, v212, v213\n\t"
        "v_max3_u32 v207, v207, v214, v215\n\t"
        "s_waitcnt vmcnt(2)\n\t"
        "ds_read_b128 v[236:239], v204 offset:7168\n\t"
        "s_waitcnt lgkmcnt(5)\n\t"
        "v_max3_u32 v207, v207, v216, v217\n\t"
        "v_max3_u32 v207, v207, v218, v219\n\t"
        "s_waitcnt vmcnt(1)\n\t"
        "ds_read_b128 v[240:243], v204 offset:8192\n\t"
        "s_waitcnt lgkmcnt(5)\n\t"
        "v_max3_u32 v207, v207, v220, v221\n\t"
        "v_max3_u32 v207, v207, v222, v223\n\t"
        "s_waitcnt vmcnt(0)\n\t"
        "ds_read_b128 v[244:247], v204 offset:10240\n\t"
        "s_waitcnt lgkmcnt(5)\n\t"
        "v_max3_u32 v207, v207, v224, v225\n\t"
        "v_max3_u32 v207, v207, v226, v227\n\t"
        "s_waitcnt lgkmcnt(4)\n\t"
        "v_max3_u32 v207, v207, v228, v229\n\t"
        "v_max3_u32 v207, v207, v230, v231\n\t"
        "s_waitcnt lgkmcnt(3)\n\t"
        "v_max3_u32 v207, v207, v232, v233\n\t"
        "v_max3_u32 v207, v207, v234, v235\n\t"
        "s_waitcnt lgkmcnt(2)\n\t"
        "v_max3_u32 v207, v207, v236, v237\n\t"
        "v_max3_u32 v207, v207, v238, v239\n\t"
        "s_waitcnt lgkmcnt(1)\n\t"
        "v_max3_u32 v207, v207, v240, v241\n\t"
        "v_max3_u32 v207, v207, v242, v243\n\t"
        "s_waitcnt lgkmcnt(0)\n\t"
        "v_max3_u32 v207, v207, v244, v245\n\t"
        "v_max3_u32 v207, v207, v246, v247\n\t"
        "v_cmp_eq_u32 vcc, 0xffbfffbf, v207\n\t"
        "s_cbranch_vccz Lpb%=\n\t"
        "s_cbranch_scc0 Lcl%=\n\t"
        "s_branch Lpb%=\n\t"
        "Lend%=:\n\t"
        "s_mov_b32 m0, s94\n\t"
        : [c0] "+&s"(c0), [r0] "+&s"(r0), [left] "+&s"(left)
        : [w0] "{v[16:47]}"(W[0]), [w1] "{v[48:79]}"(W[1]), [w2] "{v[80:111]}"(W[2]), [w3] "{v[112:143]}"(W[3]), [w4] "{v[144:175]}"(W[4]), [va] "v"(va), [sidx] "v"(sidx), [vw] "v"(vw), [rs] "s"(rs), [tg] "s"(tg), [kr] "s"(kr), [poll] "s"(poll), [vm] "v"(vm), [so] "s"(so), [lb] "s"(lb)
        : "s64", "s65", "s66", "s67", "s68", "s69", "s70", "s71", "s72", "s73", "s74", "s75", "s76", "s77", "s78", "s79", "s80", "s81", "s82", "s83", "s84", "s85", "s86", "s87", "s88", "s89", "s90", "s91", "s92", "s93", "s94", "v204", "v205", "v206", "v207", "v208", "v209", "v210", "v211", "v212", "v213", "v214", "v215", "v216", "v217", "v218", "v219", "v220", "v221", "v222", "v223", "v224", "v225", "v226", "v227", "v228", "v229", "v230", "v231", "v232", "v233", "v234", "v235", "v236", "v237", "v238", "v239", "v240", "v241", "v242", "v243", "v244", "v245", "v246", "v247", "v248", "v249", "v250", "v251", "v252", "v253", "v254", "v255", "vcc", "scc", "memory");
}

__device__ __forceinline__ void v_b(const u32x32 (&W)[5], u32& redo, u32 sidx, u32 vr, u32 vc, u32 z0, u32 z1, u32 z2, u32x4 rs, u32 lb, u32 fv, u32 fw STP) {
    asm volatile(
        "v_mov_b64 v[224:225], 0\n\t"
        "v_mov_b64 v[226:227], 0\n\t"
        "v_mov_b64 v[228:229], 0\n\t"
        "v_mov_b64 v[230:231], 0\n\t"
        "v_mov_b64 v[232:233], 0\n\t"
        "v_mov_b64 v[234:235], 0\n\t"
        "v_mov_b64 v[236:237], 0\n\t"
        "v_mov_b64 v[238:239], 0\n\t"
        "v_mov_b64 v[240:241], 0\n\t"
        "v_mov_b64 v[242:243], 0\n\t"
        "v_mov_b32 v205, %[fw]\n\t"
        "v_mov_b32 v206, %[lb]\n\t"
        "s_mov_b32 %[redo], 1\n\t"
        "s_barrier\n\t"
        "s_nop 15\n\t"
        "ds_read_b128 v[208:211], %[vc] offset:0\n\t"
        "ds_read_b128 v[212:215], %[vc] offset:4096\n\t"
        "ds_read_b128 v[216:219], %[vc] offset:8192\n\t"
        "s_waitcnt lgkmcnt(0)\n\t"
        "v_max3_u32 v207, v208, v209, v210\n\t"
        "v_max3_u32 v207, v207, v211, v212\n\t"
        "v_max3_u32 v207, v207, v213, v214\n\t"
        "v_max3_u32 v207, v207, v215, v216\n\t"
        "v_max3_u32 v207, v207, v217, v218\n\t"
        "v_max_u32 v207, v207, v219\n\t"
        "v_cmpx_eq_u32 vcc, 0xffbfffbf, v207\n\t"
        "ds_write_b32 v206, v205 offset:14336\n\t"
        "s_mov_b64 exec, -1\n\t"
        "s_setprio 2\n\t"
        "s_waitcnt lgkmcnt(0)\n\t"
        "s_barrier\n\t"
        "ds_read_b64_tr_b16 v[208:209], %[vr] offset:0\n\t"
        "ds_read_b64_tr_b16 v[210:211], %[vr] offset:64\n\t"
        "ds_read_b64_tr_b16 v[212:213], %[vr] offset:512\n\t"
        "ds_read_b64_tr_b16 v[214:215], %[vr] offset:576\n\t"
        "ds_read_b64_tr_b16 v[216:217], %[vr] offset:1024\n\t"
        "ds_read_b64_tr_b16 v[218:219], %[vr] offset:1088\n\t"
        "ds_read_b64_tr_b16 v[220:221], %[vr] offset:1536\n\t"
        "ds_read_b64_tr_b16 v[222:223], %[vr] offset:1600\n\t"
        "ds_read_b32 v207, v206 offset:14336\n\t"
        "s_waitcnt lgkmcnt(7)\n\t"
        "v_smfmac_f32_16x16x64_bf16 v[224:227], v[208:211], v[16:23], %[sidx]\n\t"
        "s_waitcnt lgkmcnt(5)\n\t"
        "v_smfmac_f32_16x16x64_bf16 v[224:227], v[212:215], v[24:31], %[sidx]\n\t"
        "s_waitcnt lgkmcnt(3)\n\t"
        "v_smfmac_f32_16x16x64_bf16 v[224:227], v[216:219], v[32:39], %[sidx]\n\t"
        "s_waitcnt lgkmcnt(1)\n\t"
        "v_smfmac_f32_16x16x64_bf16 v[224:227], v[220:223], v[40:47], %[sidx]\n\t"
        "v_smfmac_f32_16x16x64_bf16 v[228:231], v[208:211], v[48:55], %[sidx]\n\t"
        "s_waitcnt lgkmcnt(0)\n\t"
        "v_cmp_eq_u32 vcc, %[fv], v207\n\t"
        "v_smfmac_f32_16x16x64_bf16 v[228:231], v[212:215], v[56:63], %[sidx]\n\t"
        "v_cvt_pk_bf16_f32 v244, v224, v225\n\t"
        "v_smfmac_f32_16x16x64_bf16 v[228:231], v[216:219], v[64:71], %[sidx]\n\t"
        "v_cvt_pk_bf16_f32 v245, v226, v227\n\t"
        "v_smfmac_f32_16x16x64_bf16 v[228:231], v[220:223], v[72:79], %[sidx]\n\t"
        "s_cbranch_vccnz Lend%=\n\t"
        "v_smfmac_f32_16x16x64_bf16 v[232:235], v[208:211], v[80:87], %[sidx]\n\t"
        "v_smfmac_f32_16x16x64_bf16 v[232:235], v[212:215], v[88:95], %[sidx]\n\t"
        "v_cvt_pk_bf16_f32 v246, v228, v229\n\t"
        "v_smfmac_f32_16x16x64_bf16 v[232:235], v[216:219], v[96:103], %[sidx]\n\t"
        "v_cvt_pk_bf16_f32 v247, v230, v231\n\t"
        "v_smfmac_f32_16x16x64_bf16 v[232:235], v[220:223], v[104:111], %[sidx]\n\t"
        "v_permlane16_swap_b32 v244, v246\n\t"
        "v_smfmac_f32_16x16x64_bf16 v[236:239], v[208:211], v[112:119], %[sidx]\n\t"
        "v_permlane16_swap_b32 v245, v247\n\t"
        "v_smfmac_f32_16x16x64_bf16 v[236:239], v[212:215], v[120:127], %[sidx]\n\t"
        "v_cvt_pk_bf16_f32 v248, v232, v233\n\t"
        "v_smfmac_f32_16x16x64_bf16 v[236:239], v[216:219], v[128:135], %[sidx]\n\t"
        "v_cvt_pk_bf16_f32 v249, v234, v235\n\t"
        "v_smfmac_f32_16x16x64_bf16 v[236:239], v[220:223], v[136:143], %[sidx]\n\t"
        "v_smfmac_f32_16x16x64_bf16 v[240:243], v[208:211], v[144:151], %[sidx]\n\t"
        "v_smfmac_f32_16x16x64_bf16 v[240:243], v[212:215], v[152:159], %[sidx]\n\t"
        "v_cvt_pk_bf16_f32 v250, v236, v237\n\t"
        "v_smfmac_f32_16x16x64_bf16 v[240:243], v[216:219], v[160:167], %[sidx]\n\t"
        "v_cvt_pk_bf16_f32 v251, v238, v239\n\t"
        "v_smfmac_f32_16x16x64_bf16 v[240:243], v[220:223], v[168:175], %[sidx]\n\t"
        "v_permlane16_swap_b32 v248, v250\n\t"
        "v_permlane16_swap_b32 v249, v251\n\t"
        "buffer_store_dwordx4 v[248:251], %[z1], %[rs], 0 offen sc1\n\t"
        "buffer_store_dwordx4 v[244:247], %[z0], %[rs], 0 offen sc1\n\t"
        "s_nop 4\n\t"
        "v_cvt_pk_bf16_f32 v252, v240, v241\n\t"
        "v_cvt_pk_bf16_f32 v254, v240, v241\n\t"
        "v_cvt_pk_bf16_f32 v253, v242, v243\n\t"
        "v_cvt_pk_bf16_f32 v255, v242, v243\n\t"
        "s_nop 0\n\t"
        "v_permlane16_swap_b32 v252, v254\n\t"
        "v_permlane16_swap_b32 v253, v255\n\t"
        "buffer_store_dwordx4 v[252:255], %[z2], %[rs], 0 offen sc1\n\t"
        "s_mov_b32 %[redo], 0\n\t"
        "Lend%=:\n\t"
        "s_setprio 0\n\t"
        "s_nop 7\n\t"
        : [redo] "=&s"(redo)
        : [w0] "{v[16:47]}"(W[0]), [w1] "{v[48:79]}"(W[1]), [w2] "{v[80:111]}"(W[2]), [w3] "{v[112:143]}"(W[3]), [w4] "{v[144:175]}"(W[4]), [sidx] "v"(sidx), [vr] "v"(vr), [vc] "v"(vc), [z0] "v"(z0), [z1] "v"(z1), [z2] "v"(z2), [rs] "s"(rs), [lb] "s"(lb), [fv] "s"(fv), [fw] "s"(fw)
        : "v204", "v205", "v206", "v207", "v208", "v209", "v210", "v211", "v212", "v213", "v214", "v215", "v216", "v217", "v218", "v219", "v220", "v221", "v222", "v223", "v224", "v225", "v226", "v227", "v228", "v229", "v230", "v231", "v232", "v233", "v234", "v235", "v236", "v237", "v238", "v239", "v240", "v241", "v242", "v243", "v244", "v245", "v246", "v247", "v248", "v249", "v250", "v251", "v252", "v253", "v254", "v255", "vcc", "scc", "memory");
}

__device__ __forceinline__ void l_g(u32& miss, u32& c0, u32& r0, u64& tl, u32 tg, u32 kr, u32 vm, u32 so, u32x4 rs, u32x4 rec, u32 sr STP) {
    asm volatile(
        "s_add_u32 s92, %[so], 0x8000\n\t"
        "s_add_u32 s93, %[so], 0x10000\n\t"
        "s_mov_b32 %[miss], 1\n\t"
        "s_cmp_eq_u32 %[kr], 0\n\t"
        "s_cbranch_scc1 Lza_%=\n\t"
        "Lda_%=:\n\t"
        "s_memrealtime s[90:91]\n\t"
        "s_waitcnt lgkmcnt(0)\n\t"
        "s_mul_i32 s90, s90, 24\n\t"
        "s_sub_u32 s88, %[tg], s90\n\t"
        "s_sub_u32 s89, s88, 620\n\t"
        "s_cmp_gt_u32 s89, 0x4000\n\t"
        "s_cbranch_scc1 Lea_%=\n\t"
        "s_sleep 1\n\t"
        "s_branch Lda_%=\n\t"
        "Lea_%=:\n\t"
        "s_memrealtime s[64:65]\n\t"
        "s_nop 0\n\t"
        "s_memrealtime s[66:67]\n\t"
        "s_nop 0\n\t"
        "s_memrealtime s[68:69]\n\t"
        "s_nop 0\n\t"
        "s_memrealtime s[70:71]\n\t"
        "s_nop 0\n\t"
        "s_memrealtime s[72:73]\n\t"
        "s_nop 0\n\t"
        "s_memrealtime s[74:75]\n\t"
        "s_memtime s[90:91]\n\t"
        "s_memrealtime s[76:77]\n\t"
        "s_nop 0\n\t"
        "s_memrealtime s[78:79]\n\t"
        "s_nop 0\n\t"
        "s_memrealtime s[80:81]\n\t"
        "s_nop 0\n\t"
        "s_memrealtime s[82:83]\n\t"
        "s_nop 0\n\t"
        "s_memrealtime s[84:85]\n\t"
        "s_nop 0\n\t"
        "s_memrealtime s[86:87]\n\t"
        "s_nop 0\n\t"
        "s_nop 7\n\t"
        "s_waitcnt lgkmcnt(0)\n\t"
        "s_add_u32 s64, s64, s66\n\t"
        "s_add_u32 s64, s64, s68\n\t"
        "s_add_u32 s64, s64, s70\n\t"
        "s_add_u32 s64, s64, s72\n\t"
        "s_add_u32 s64, s64, s74\n\t"
        "s_add_u32 s64, s64, s76\n\t"
        "s_add_u32 s64, s64, s78\n\t"
        "s_add_u32 s64, s64, s80\n\t"
        "s_add_u32 s64, s64, s82\n\t"
        "s_add_u32 s64, s64, s84\n\t"
        "s_add_u32 s64, s64, s86\n\t"
        "s_lshl_b32 s64, s64, 1\n\t"
        "s_mov_b32 %[c0], s90\n\t"
        "s_mov_b32 %[r0], s64\n\t"
        "s_sub_u32 s88, %[tg], s64\n\t"
        "s_mul_i32 s88, s88, %[kr]\n\t"
        "s_ashr_i32 s88, s88, 12\n\t"
        "s_sub_u32 s88, s88, 244\n\t"
        "s_nop 1\n\t"
        "s_max_i32 s88, s88, 0\n\t"
        "s_min_i32 s88, s88, 384\n\t"
        "s_andn2_b32 s88, s88, 3\n\t"
        "s_getpc_b64 s[90:91]\n\t"
        "Lga_%=:\n\t"
        "s_sub_u32 s90, s90, s88\n\t"
        "s_subb_u32 s91, s91, 0\n\t"
        "s_add_u32 s90, s90, Lza_%=-Lga_%=\n\t"
        "s_addc_u32 s91, s91, 0\n\t"
        "s_setpc_b64 s[90:91]\n\t"
        "s_nop 0\n\ts_nop 0\n\ts_nop 0\n\ts_nop 0\n\t"
        "s_nop 0\n\ts_nop 0\n\ts_nop 0\n\ts_nop 0\n\t"
        "s_nop 0\n\ts_nop 0\n\ts_nop 0\n\ts_nop 0\n\t"
        "s_nop 0\n\ts_nop 0\n\ts_nop 0\n\ts_nop 0\n\t"
        "s_nop 0\n\ts_nop 0\n\ts_nop 0\n\ts_nop 0\n\t"
        "s_nop 0\n\ts_nop 0\n\ts_nop 0\n\ts_nop 0\n\t"
        "s_nop 0\n\ts_nop 0\n\ts_nop 0\n\ts_nop 0\n\t"
        "s_nop 0\n\ts_nop 0\n\ts_nop 0\n\ts_nop 0\n\t"
        "s_nop 0\n\ts_nop 0\n\ts_nop 0\n\ts_nop 0\n\t"
        "s_nop 0\n\ts_nop 0\n\ts_nop 0\n\ts_nop 0\n\t"
        "s_nop 0\n\ts_nop 0\n\ts_nop 0\n\ts_nop 0\n\t"
        "s_nop 0\n\ts_nop 0\n\ts_nop 0\n\ts_nop 0\n\t"
        "s_nop 0\n\ts_nop 0\n\ts_nop 0\n\ts_nop 0\n\t"
        "s_nop 0\n\ts_nop 0\n\ts_nop 0\n\ts_nop 0\n\t"
        "s_nop 0\n\ts_nop 0\n\ts_nop 0\n\ts_nop 0\n\t"
        "s_nop 0\n\ts_nop 0\n\ts_nop 0\n\ts_nop 0\n\t"
        "s_nop 0\n\ts_nop 0\n\ts_nop 0\n\ts_nop 0\n\t"
        "s_nop 0\n\ts_nop 0\n\ts_nop 0\n\ts_nop 0\n\t"
        "s_nop 0\n\ts_nop 0\n\ts_nop 0\n\ts_nop 0\n\t"
        "s_nop 0\n\ts_nop 0\n\ts_nop 0\n\ts_nop 0\n\t"
        "s_nop 0\n\ts_nop 0\n\ts_nop 0\n\ts_nop 0\n\t"
        "s_nop 0\n\ts_nop 0\n\ts_nop 0\n\ts_nop 0\n\t"
        "s_nop 0\n\ts_nop 0\n\ts_nop 0\n\ts_nop 0\n\t"
        "s_nop 0\n\ts_nop 0\n\ts_nop 0\n\ts_nop 0\n\t"
        "Lza_%=:\n\t"
        "buffer_load_dwordx4 v[16:19], %[vm], %[rs], %[so] offen offset:0 sc1\n\t"
        "buffer_load_dwordx4 v[20:23], %[vm], %[rs], %[so] offen offset:1024 sc1\n\t"
        "buffer_load_dwordx4 v[24:27], %[vm], %[rs], %[so] offen offset:2048 sc1\n\t"
        "buffer_load_dwordx4 v[28:31], %[vm], %[rs], %[so] offen offset:3072 sc1\n\t"
        "buffer_load_dwordx4 v[32:35], %[vm], %[rs], s92 offen offset:0 sc1\n\t"
        "buffer_load_dwordx4 v[36:39], %[vm], %[rs], s92 offen offset:1024 sc1\n\t"
        "buffer_load_dwordx4 v[40:43], %[vm], %[rs], s92 offen offset:2048 sc1\n\t"
        "buffer_load_dwordx4 v[44:47], %[vm], %[rs], s92 offen offset:3072 sc1\n\t"
        "buffer_load_dwordx4 v[48:51], %[vm], %[rs], s93 offen offset:0 sc1\n\t"
        "buffer_load_dwordx4 v[52:55], %[vm], %[rs], s93 offen offset:2048 sc1\n\t"
        "s_memrealtime %[tl]\n\t"
        "s_waitcnt vmcnt(9)\n\t"
        "v_max3_u32 v60, v16, v16, v17\n\t"
        "v_max3_u32 v60, v60, v18, v19\n\t"
        "s_waitcnt vmcnt(8)\n\t"
        "v_max3_u32 v60, v60, v20, v21\n\t"
        "v_max3_u32 v60, v60, v22, v23\n\t"
        "s_waitcnt vmcnt(7)\n\t"
        "v_max3_u32 v60, v60, v24, v25\n\t"
        "v_max3_u32 v60, v60, v26, v27\n\t"
        "s_waitcnt vmcnt(6)\n\t"
        "v_max3_u32 v60, v60, v28, v29\n\t"
        "v_max3_u32 v60, v60, v30, v31\n\t"
        "s_waitcnt vmcnt(5)\n\t"
        "v_max3_u32 v60, v60, v32, v33\n\t"
        "v_max3_u32 v60, v60, v34, v35\n\t"
        "s_waitcnt vmcnt(4)\n\t"
        "v_max3_u32 v60, v60, v36, v37\n\t"
        "v_max3_u32 v60, v60, v38, v39\n\t"
        "s_waitcnt vmcnt(3)\n\t"
        "v_max3_u32 v60, v60, v40, v41\n\t"
        "v_max3_u32 v60, v60, v42, v43\n\t"
        "s_waitcnt vmcnt(2)\n\t"
        "v_max3_u32 v60, v60, v44, v45\n\t"
        "v_max3_u32 v60, v60, v46, v47\n\t"
        "s_waitcnt vmcnt(1)\n\t"
        "v_max3_u32 v60, v60, v48, v49\n\t"
        "v_max3_u32 v60, v60, v50, v51\n\t"
        "s_waitcnt vmcnt(0)\n\t"
        "v_max3_u32 v60, v60, v52, v53\n\t"
        "v_max3_u32 v60, v60, v54, v55\n\t"
        "v_cmp_eq_u32 vcc, 0xffbfffbf, v60\n\t"
        "s_cbranch_vccnz Lend%=\n\t"
        "buffer_store_dwordx4 v[56:59], %[sr], %[rs], 0 offen sc1\n\t"
        "s_mov_b32 %[miss], 0\n\t"
        "Lend%=:\n\t"
        "s_waitcnt lgkmcnt(0)\n\t"
        : [c0] "+&s"(c0), [r0] "+&s"(r0), [tl] "=&s"(tl), [miss] "=&s"(miss)
        : [vm] "v"(vm), [sr] "v"(sr), [rs] "s"(rs), [so] "s"(so), [tg] "s"(tg), [kr] "s"(kr), [rec] "{v[56:59]}"(rec)
        : "s64", "s65", "s66", "s67", "s68", "s69", "s70", "s71", "s72", "s73", "s74", "s75", "s76", "s77", "s78", "s79", "s80", "s81", "s82", "s83", "s84", "s85", "s86", "s87", "s88", "s89", "s90", "s91", "s92", "s93", "v16", "v17", "v18", "v19", "v20", "v21", "v22", "v23", "v24", "v25", "v26", "v27", "v28", "v29", "v30", "v31", "v32", "v33", "v34", "v35", "v36", "v37", "v38", "v39", "v40", "v41", "v42", "v43", "v44", "v45", "v46", "v47", "v48", "v49", "v50", "v51", "v52", "v53", "v54", "v55", "v60", "vcc", "scc", "memory");
}

__device__ __forceinline__ void r_t0(u32& c0, u32& r0, u32& t0, u32& t1, u32& t2, u32& ok, u32 kr, u32 fv, u32 lb, u32x4 rs, u32 so STP) {
    asm volatile(
        "s_setprio 3\n\t"
        "s_mov_b32 s92, 0x4000\n\t"
        "v_mov_b32 v20, %[lb]\n\t"
        "s_nop 2\n\t"
        "s_mov_b32 %[ok], 1\n\t"
        "s_buffer_load_dwordx4 s[48:51], %[rs], %[so] offset:0x0\n\t"
        "s_buffer_load_dwordx4 s[52:55], %[rs], %[so] offset:0x80\n\t"
        "s_buffer_load_dwordx4 s[56:59], %[rs], %[so] offset:0x100\n\t"
        "s_buffer_load_dwordx4 s[60:63], %[rs], %[so] offset:0x180\n\t"
        "s_memrealtime s[64:65]\n\t"
        "s_nop 0\n\t"
        "s_memrealtime s[66:67]\n\t"
        "s_nop 0\n\t"
        "s_memrealtime s[68:69]\n\t"
        "s_nop 0\n\t"
        "s_memrealtime s[70:71]\n\t"
        "s_nop 0\n\t"
        "s_memrealtime s[72:73]\n\t"
        "s_nop 0\n\t"
        "s_memrealtime s[74:75]\n\t"
        "s_memtime s[90:91]\n\t"
        "s_memrealtime s[76:77]\n\t"
        "s_nop 0\n\t"
        "s_memrealtime s[78:79]\n\t"
        "s_nop 0\n\t"
        "s_memrealtime s[80:81]\n\t"
        "s_nop 0\n\t"
        "s_memrealtime s[82:83]\n\t"
        "s_nop 0\n\t"
        "s_memrealtime s[84:85]\n\t"
        "s_nop 0\n\t"
        "s_memrealtime s[86:87]\n\t"
        "s_nop 0\n\t"
        "s_nop 7\n\t"
        "s_waitcnt lgkmcnt(0)\n\t"
        "s_add_u32 s64, s64, s66\n\t"
        "s_add_u32 s64, s64, s68\n\t"
        "s_add_u32 s64, s64, s70\n\t"
        "s_add_u32 s64, s64, s72\n\t"
        "s_add_u32 s64, s64, s74\n\t"
        "s_add_u32 s64, s64, s76\n\t"
        "s_add_u32 s64, s64, s78\n\t"
        "s_add_u32 s64, s64, s80\n\t"
        "s_add_u32 s64, s64, s82\n\t"
        "s_add_u32 s64, s64, s84\n\t"
        "s_add_u32 s64, s64, s86\n\t"
        "s_lshl_b32 s64, s64, 1\n\t"
        "s_mov_b32 %[c0], s90\n\t"
        "s_mov_b32 %[r0], s64\n\t"
        "s_xor_b32 s51, s51, %[fv]\n\t"
        "s_xor_b32 s55, s55, %[fv]\n\t"
        "s_xor_b32 s59, s59, %[fv]\n\t"
        "s_xor_b32 s63, s63, %[fv]\n\t"
        "s_sub_u32 s52, s52, s48\n\t"
        "s_sub_u32 s56, s56, s48\n\t"
        "s_sub_u32 s60, s60, s48\n\t"
        "s_max_i32 s52, s52, s56\n\t"
        "s_max_i32 s60, s60, 0\n\t"
        "s_max_i32 s52, s52, s60\n\t"
        "s_add_u32 s48, s48, s52\n\t"
        "s_sub_u32 s53, s53, s49\n\t"
        "s_sub_u32 s57, s57, s49\n\t"
        "s_sub_u32 s61, s61, s49\n\t"
        "s_max_i32 s53, s53, s57\n\t"
        "s_max_i32 s61, s61, 0\n\t"
        "s_max_i32 s53, s53, s61\n\t"
        "s_add_u32 s49, s49, s53\n\t"
        "s_max_u32 s50, s50, s54\n\t"
        "s_max_u32 s50, s50, s58\n\t"
        "s_max_u32 s50, s50, s62\n\t"
        "s_or_b32 s58, s51, s55\n\t"
        "s_or_b32 s54, s59, s63\n\t"
        "s_or_b32 s54, s54, s58\n\t"
        "s_cmp_eq_u32 s54, 0\n\t"
        "s_cbranch_scc0 Lx%=\n\t"
        "s_mov_b32 %[t0], s48\n\t"
        "s_mov_b32 %[t1], s49\n\t"
        "s_mov_b32 %[t2], s50\n\t"
        "Lgo%=:\n\t"
        "s_lshr_b32 s95, %[t2], 16\n\t"
        "s_mul_i32 s95, s95, %[kr]\n\t"
        "s_lshr_b32 s95, s95, 12\n\t"
        "s_min_u32 s95, s95, 512\n\t"
        "s_andn2_b32 s95, s95, 3\n\t"
        "v_mov_b32 v21, s95\n\t"
        "ds_write_b32 v20, v21 offset:16384\n\t"
        "s_sub_u32 s95, %[t0], %[r0]\n\t"
        "s_mul_i32 s95, s95, %[kr]\n\t"
        "s_ashr_i32 s95, s95, 12\n\t"
        "s_add_u32 s95, s95, %[c0]\n\t"
        "Lda_%=:\n\t"
        "s_memtime s[90:91]\n\t"
        "s_waitcnt lgkmcnt(0)\n\t"
        "s_sub_u32 s88, s95, s90\n\t"
        "s_sub_u32 s89, s88, 336\n\t"
        "s_cmp_gt_u32 s89, 0x4000\n\t"
        "s_cbranch_scc1 Lea_%=\n\t"
        "s_sleep 1\n\t"
        "s_branch Lda_%=\n\t"
        "Lea_%=:\n\t"
        "s_sub_u32 s88, s88, 112\n\t"
        "s_max_i32 s88, s88, 0\n\t"
        "s_min_i32 s88, s88, 224\n\t"
        "s_andn2_b32 s88, s88, 3\n\t"
        "s_getpc_b64 s[90:91]\n\t"
        "Lga_%=:\n\t"
        "s_sub_u32 s90, s90, s88\n\t"
        "s_subb_u32 s91, s91, 0\n\t"
        "s_add_u32 s90, s90, Lza_%=-Lga_%=\n\t"
        "s_addc_u32 s91, s91, 0\n\t"
        "s_setpc_b64 s[90:91]\n\t"
        "s_nop 0\n\ts_nop 0\n\ts_nop 0\n\ts_nop 0\n\t"
        "s_nop 0\n\ts_nop 0\n\ts_nop 0\n\ts_nop 0\n\t"
        "s_nop 0\n\ts_nop 0\n\ts_nop 0\n\ts_nop 0\n\t"
        "s_nop 0\n\ts_nop 0\n\ts_nop 0\n\ts_nop 0\n\t"
        "s_nop 0\n\ts_nop 0\n\ts_nop 0\n\ts_nop 0\n\t"
        "s_nop 0\n\ts_nop 0\n\ts_nop 0\n\ts_nop 0\n\t"
        "s_nop 0\n\ts_nop 0\n\ts_nop 0\n\ts_nop 0\n\t"
        "s_nop 0\n\ts_nop 0\n\ts_nop 0\n\ts_nop 0\n\t"
        "s_nop 0\n\ts_nop 0\n\ts_nop 0\n\ts_nop 0\n\t"
        "s_nop 0\n\ts_nop 0\n\ts_nop 0\n\ts_nop 0\n\t"
        "s_nop 0\n\ts_nop 0\n\ts_nop 0\n\ts_nop 0\n\t"
        "s_nop 0\n\ts_nop 0\n\ts_nop 0\n\ts_nop 0\n\t"
        "s_nop 0\n\ts_nop 0\n\ts_nop 0\n\ts_nop 0\n\t"
        "s_nop 0\n\ts_nop 0\n\ts_nop 0\n\ts_nop 0\n\t"
        "Lza_%=:\n\t"
        "s_barrier\n\t"
        "s_setprio 0\n\t"
        "s_barrier\n\t"
        "s_branch Lend%=\n\t"
        "Lx%=:\n\t"
        "s_mov_b32 %[ok], 2\n\t"
        "s_cmp_eq_u32 s51, 0x10000\n\t"
        "s_cselect_b32 %[ok], 0, %[ok]\n\t"
        "s_cmp_eq_u32 s55, 0x10000\n\t"
        "s_cselect_b32 %[ok], 0, %[ok]\n\t"
        "s_cmp_eq_u32 s59, 0x10000\n\t"
        "s_cselect_b32 %[ok], 0, %[ok]\n\t"
        "s_cmp_eq_u32 s63, 0x10000\n\t"
        "s_cselect_b32 %[ok], 0, %[ok]\n\t"
        "s_cmp_eq_u32 %[ok], 2\n\t"
        "s_cbranch_scc1 Lgo%=\n\t"
        "Lend%=:\n\t"
        : [c0] "+&s"(c0), [r0] "+&s"(r0), [t0] "+&s"(t0), [t1] "+&s"(t1), [t2] "+&s"(t2), [ok] "=&s"(ok)
        : [rs] "s"(rs), [so] "s"(so), [kr] "s"(kr), [fv] "s"(fv), [lb] "s"(lb)
        : "s48", "s49", "s50", "s51", "s52", "s53", "s54", "s55", "s56", "s57", "s58", "s59", "s60", "s61", "s62", "s63", "s64", "s65", "s66", "s67", "s68", "s69", "s70", "s71", "s72", "s73", "s74", "s75", "s76", "s77", "s78", "s79", "s80", "s81", "s82", "s83", "s84", "s85", "s86", "s87", "s88", "s89", "s90", "s91", "s92", "s93", "s94", "s95", "s96", "s97", "s98", "v16", "v17", "v18", "v19", "v20", "v21", "vcc", "scc", "memory");
}

__device__ __forceinline__ void r_t1(u32& c0, u32& r0, u32& t0, u32& t1, u32& t2, u32 tv, u32 kr, u32 fv, u32 lb, u32x4 rs, u32 so STP) {
    asm volatile(
        "s_setprio 3\n\t"
        "s_mov_b32 s92, 0x4000\n\t"
        "v_mov_b32 v20, %[lb]\n\t"
        "s_nop 2\n\t"
        "Lr%=:\n\t"
        "buffer_load_dwordx4 v[16:19], %[tv], %[rs], %[so] offen sc1\n\t"
        "s_memrealtime s[64:65]\n\t"
        "s_nop 0\n\t"
        "s_memrealtime s[66:67]\n\t"
        "s_nop 0\n\t"
        "s_memrealtime s[68:69]\n\t"
        "s_nop 0\n\t"
        "s_memrealtime s[70:71]\n\t"
        "s_nop 0\n\t"
        "s_memrealtime s[72:73]\n\t"
        "s_nop 0\n\t"
        "s_memrealtime s[74:75]\n\t"
        "s_memtime s[90:91]\n\t"
        "s_memrealtime s[76:77]\n\t"
        "s_nop 0\n\t"
        "s_memrealtime s[78:79]\n\t"
        "s_nop 0\n\t"
        "s_memrealtime s[80:81]\n\t"
        "s_nop 0\n\t"
        "s_memrealtime s[82:83]\n\t"
        "s_nop 0\n\t"
        "s_memrealtime s[84:85]\n\t"
        "s_nop 0\n\t"
        "s_memrealtime s[86:87]\n\t"
        "s_nop 0\n\t"
        "s_nop 7\n\t"
        "s_waitcnt lgkmcnt(0)\n\t"
        "s_add_u32 s64, s64, s66\n\t"
        "s_add_u32 s64, s64, s68\n\t"
        "s_add_u32 s64, s64, s70\n\t"
        "s_add_u32 s64, s64, s72\n\t"
        "s_add_u32 s64, s64, s74\n\t"
        "s_add_u32 s64, s64, s76\n\t"
        "s_add_u32 s64, s64, s78\n\t"
        "s_add_u32 s64, s64, s80\n\t"
        "s_add_u32 s64, s64, s82\n\t"
        "s_add_u32 s64, s64, s84\n\t"
        "s_add_u32 s64, s64, s86\n\t"
        "s_lshl_b32 s64, s64, 1\n\t"
        "s_mov_b32 %[c0], s90\n\t"
        "s_mov_b32 %[r0], s64\n\t"
        "s_sub_u32 s96, %[r0], 0x100000\n\t"
        "s_waitcnt vmcnt(0)\n\t"
        "v_cmp_eq_u32 vcc, %[fv], v19\n\t"
        "v_subrev_u32 v16, s96, v16\n\t"
        "v_subrev_u32 v17, s96, v17\n\t"
        "v_cndmask_b32 v16, 0, v16, vcc\n\t"
        "v_cndmask_b32 v17, 0, v17, vcc\n\t"
        "v_cndmask_b32 v18, 0, v18, vcc\n\t"
        "v_max_u32_dpp v16, v16, v16 quad_perm:[1,0,3,2] row_mask:0xf bank_mask:0xf\n\t"
        "v_max_u32_dpp v17, v17, v17 quad_perm:[1,0,3,2] row_mask:0xf bank_mask:0xf\n\t"
        "v_max_u32_dpp v18, v18, v18 quad_perm:[1,0,3,2] row_mask:0xf bank_mask:0xf\n\t"
        "v_max_u32_dpp v16, v16, v16 quad_perm:[2,3,0,1] row_mask:0xf bank_mask:0xf\n\t"
        "v_max_u32_dpp v17, v17, v17 quad_perm:[2,3,0,1] row_mask:0xf bank_mask:0xf\n\t"
        "v_max_u32_dpp v18, v18, v18 quad_perm:[2,3,0,1] row_mask:0xf bank_mask:0xf\n\t"
        "v_max_u32_dpp v16, v16, v16 row_half_mirror row_mask:0xf bank_mask:0xf\n\t"
        "v_max_u32_dpp v17, v17, v17 row_half_mirror row_mask:0xf bank_mask:0xf\n\t"
        "v_max_u32_dpp v18, v18, v18 row_half_mirror row_mask:0xf bank_mask:0xf\n\t"
        "s_lshr_b32 s94, vcc_lo, 4\n\t"
        "v_readfirstlane_b32 s93, v16\n\t"
        "v_readfirstlane_b32 s97, v17\n\t"
        "v_readfirstlane_b32 s98, v18\n\t"
        "s_add_u32 s93, s93, s96\n\t"
        "s_add_u32 s97, s97, s96\n\t"
        "s_or_b32 s94, s94, vcc_lo\n\t"
        "s_and_b32 s94, s94, 15\n\t"
        "s_cmp_eq_u32 s94, 15\n\t"
        "s_cbranch_scc0 Lx%=\n\t"
        "s_mov_b32 %[t0], s93\n\t"
        "s_mov_b32 %[t1], s97\n\t"
        "s_mov_b32 %[t2], s98\n\t"
        "Lgo%=:\n\t"
        "s_lshr_b32 s95, %[t2], 16\n\t"
        "s_mul_i32 s95, s95, %[kr]\n\t"
        "s_lshr_b32 s95, s95, 12\n\t"
        "s_min_u32 s95, s95, 512\n\t"
        "s_andn2_b32 s95, s95, 3\n\t"
        "v_mov_b32 v21, s95\n\t"
        "ds_write_b32 v20, v21 offset:16384\n\t"
        "s_sub_u32 s95, %[t0], %[r0]\n\t"
        "s_mul_i32 s95, s95, %[kr]\n\t"
        "s_ashr_i32 s95, s95, 12\n\t"
        "s_add_u32 s95, s95, %[c0]\n\t"
        "Lda_%=:\n\t"
        "s_memtime s[90:91]\n\t"
        "s_waitcnt lgkmcnt(0)\n\t"
        "s_sub_u32 s88, s95, s90\n\t"
        "s_sub_u32 s89, s88, 336\n\t"
        "s_cmp_gt_u32 s89, 0x4000\n\t"
        "s_cbranch_scc1 Lea_%=\n\t"
        "s_sleep 1\n\t"
        "s_branch Lda_%=\n\t"
        "Lea_%=:\n\t"
        "s_sub_u32 s88, s88, 112\n\t"
        "s_max_i32 s88, s88, 0\n\t"
        "s_min_i32 s88, s88, 224\n\t"
        "s_andn2_b32 s88, s88, 3\n\t"
        "s_getpc_b64 s[90:91]\n\t"
        "Lga_%=:\n\t"
        "s_sub_u32 s90, s90, s88\n\t"
        "s_subb_u32 s91, s91, 0\n\t"
        "s_add_u32 s90, s90, Lza_%=-Lga_%=\n\t"
        "s_addc_u32 s91, s91, 0\n\t"
        "s_setpc_b64 s[90:91]\n\t"
        "s_nop 0\n\ts_nop 0\n\ts_nop 0\n\ts_nop 0\n\t"
        "s_nop 0\n\ts_nop 0\n\ts_nop 0\n\ts_nop 0\n\t"
        "s_nop 0\n\ts_nop 0\n\ts_nop 0\n\ts_nop 0\n\t"
        "s_nop 0\n\ts_nop 0\n\ts_nop 0\n\ts_nop 0\n\t"
        "s_nop 0\n\ts_nop 0\n\ts_nop 0\n\ts_nop 0\n\t"
        "s_nop 0\n\ts_nop 0\n\ts_nop 0\n\ts_nop 0\n\t"
        "s_nop 0\n\ts_nop 0\n\ts_nop 0\n\ts_nop 0\n\t"
        "s_nop 0\n\ts_nop 0\n\ts_nop 0\n\ts_nop 0\n\t"
        "s_nop 0\n\ts_nop 0\n\ts_nop 0\n\ts_nop 0\n\t"
        "s_nop 0\n\ts_nop 0\n\ts_nop 0\n\ts_nop 0\n\t"
        "s_nop 0\n\ts_nop 0\n\ts_nop 0\n\ts_nop 0\n\t"
        "s_nop 0\n\ts_nop 0\n\ts_nop 0\n\ts_nop 0\n\t"
        "s_nop 0\n\ts_nop 0\n\ts_nop 0\n\ts_nop 0\n\t"
        "s_nop 0\n\ts_nop 0\n\ts_nop 0\n\ts_nop 0\n\t"
        "Lza_%=:\n\t"
        "s_barrier\n\t"
        "s_setprio 0\n\t"
        "s_barrier\n\t"
        "s_branch Lend%=\n\t"
        "Lx%=:\n\t"
        "s_sub_u32 s92, s92, 1\n\t"
        "s_cbranch_scc1 Lgo%=\n\t"
        "s_branch Lr%=\n\t"
        "Lend%=:\n\t"
        : [c0] "+&s"(c0), [r0] "+&s"(r0), [t0] "+&s"(t0), [t1] "+&s"(t1), [t2] "+&s"(t2)
        : [rs] "s"(rs), [so] "s"(so), [kr] "s"(kr), [fv] "s"(fv), [lb] "s"(lb), [tv] "v"(tv)
        : "s64", "s65", "s66", "s67", "s68", "s69", "s70", "s71", "s72", "s73", "s74", "s75", "s76", "s77", "s78", "s79", "s80", "s81", "s82", "s83", "s84", "s85", "s86", "s87", "s88", "s89", "s90", "s91", "s92", "s93", "s94", "s95", "s96", "s97", "s98", "v16", "v17", "v18", "v19", "v20", "v21", "vcc", "scc", "memory");
}

__device__ __forceinline__ void r_u(u32x32& bn, u32& s, u32& have, u32& left, u32& why, u32 sidx, u32 eo, u32 uw, u32 fa, u32 vm, u32 fo, u32 te, u32 tb, u32x4 rs, u32 lag, u32 lb STP LGP) {
    asm volatile(
        "v_mov_b32 v125, %[lb]\n\t"
        "v_mov_b32 v80, 0xffbfffbf\n\t"
        "v_mov_b32 v81, v80\n\t"
        "ds_write_b64 %[uw], v[80:81] offset:0\n\t"
        "ds_write_b64 %[uw], v[80:81] offset:512\n\t"
        "ds_write_b64 %[uw], v[80:81] offset:1024\n\t"
        "ds_write_b64 %[uw], v[80:81] offset:1536\n\t"
        "Lp%=:\n\t"
        "v_readlane_b32 s90, %[te], %[s]\n\t"
        "v_readlane_b32 s91, %[tb], %[s]\n\t"
        "s_add_u32 s92, %[s], 1\n\t"
        "s_mul_i32 s93, s92, 0x10001\n\t"
        "s_mov_b32 s95, %[left]\n\t"
        "v_mov_b32 v124, s93\n\t"
        "s_add_u32 s97, %[s], 4\n\t"
        "s_cmp_le_u32 %[have], s97\n\t"
        "s_cselect_b32 s96, 0, 0x80000000\n\t"
        "v_or_b32 v126, s96, %[fo]\n\t"
        "s_add_u32 s89, s91, 0x1000\n\t"
        "s_mov_b32 s88, %[lag]\n\t"
        "s_setprio 2\n\t"
        "s_cmp_eq_u32 s88, 0\n\t"
        "s_waitcnt lgkmcnt(0)\n\t"
        "s_barrier\n\t"
        "s_cbranch_scc1 Lg%=\n\t"
        "Ll%=:\n\t"
        "s_nop 5\n\t"
        "s_sub_u32 s88, s88, 1\n\t"
        "s_cmp_lg_u32 s88, 0\n\t"
        "s_cbranch_scc1 Ll%=\n\t"
        "Lg%=:\n\t"
        "buffer_load_dwordx4 v[48:51], %[eo], %[rs], s90 offen offset:0 sc1\n\t"
        "buffer_load_dwordx4 v[52:55], %[eo], %[rs], s90 offen offset:1024 sc1\n\t"
        "buffer_load_dwordx4 v[56:59], %[eo], %[rs], s90 offen offset:2048 sc1\n\t"
        "buffer_load_dwordx4 v[60:63], %[eo], %[rs], s90 offen offset:3072 sc1\n\t"
        "v_mov_b64 v[64:65], 0\n\t"
        "v_mov_b64 v[66:67], 0\n\t"
        "v_mov_b64 v[68:69], 0\n\t"
        "v_mov_b64 v[70:71], 0\n\t"
        "v_mov_b64 v[72:73], 0\n\t"
        "v_mov_b64 v[74:75], 0\n\t"
        "v_mov_b64 v[76:77], 0\n\t"
        "v_mov_b64 v[78:79], 0\n\t"
        "s_waitcnt vmcnt(3)\n\t"
        "v_smfmac_f32_16x16x64_bf16 v[64:67], v[48:51], v[16:23], %[sidx]\n\t"
        "s_waitcnt vmcnt(2)\n\t"
        "v_smfmac_f32_16x16x64_bf16 v[68:71], v[52:55], v[24:31], %[sidx]\n\t"
        "s_waitcnt vmcnt(1)\n\t"
        "v_smfmac_f32_16x16x64_bf16 v[72:75], v[56:59], v[32:39], %[sidx]\n\t"
        "v_cvt_pk_bf16_f32 v80, v64, v65\n\t"
        "v_cvt_pk_bf16_f32 v81, v66, v67\n\t"
        "ds_write_b64 %[uw], v[80:81] offset:0\n\t"
        "s_waitcnt vmcnt(0)\n\t"
        "v_smfmac_f32_16x16x64_bf16 v[76:79], v[60:63], v[40:47], %[sidx]\n\t"
        "v_cvt_pk_bf16_f32 v82, v68, v69\n\t"
        "v_cvt_pk_bf16_f32 v83, v70, v71\n\t"
        "ds_write_b64 %[uw], v[82:83] offset:512\n\t"
        "v_cvt_pk_bf16_f32 v84, v72, v73\n\t"
        "v_cvt_pk_bf16_f32 v85, v74, v75\n\t"
        "ds_write_b64 %[uw], v[84:85] offset:1024\n\t"
        "s_nop 1\n\t"
        "v_cvt_pk_bf16_f32 v86, v76, v77\n\t"
        "v_cvt_pk_bf16_f32 v87, v78, v79\n\t"
        "ds_write_b64 %[uw], v[86:87] offset:1536\n\t"
        "s_setprio 0\n\t"
        "v_max_u32 v88, v48, v49\n\t"
        "v_max3_u32 v88, v88, v50, v51\n\t"
        "v_max3_u32 v88, v88, v52, v53\n\t"
        "v_max3_u32 v88, v88, v54, v55\n\t"
        "v_max3_u32 v88, v88, v56, v57\n\t"
        "v_max3_u32 v88, v88, v58, v59\n\t"
        "v_max3_u32 v88, v88, v60, v61\n\t"
        "v_max3_u32 v88, v88, v62, v63\n\t"
        "v_cmp_eq_u32 vcc, 0xffbfffbf, v88\n\t"
        "v_mov_b32 v80, 0xffbfffbf\n\t"
        "v_mov_b32 v81, v80\n\t"
        "s_cbranch_vccnz Lm%=\n\t"
        "Lok%=:\n\t"
        "ds_write_b32 %[fa], v124\n\t"
        "s_cmp_gt_u32 %[have], s92\n\t"
        "s_cselect_b32 %[why], 0, 2\n\t"
        "s_waitcnt lgkmcnt(0)\n\t"
        "s_barrier\n\t"
        "buffer_load_dwordx2 v[122:123], v126, %[rs], 0 offen sc1\n\t"
        "s_cbranch_scc0 Ln%=\n\t"
        "buffer_load_dwordx4 v[90:93], %[vm], %[rs], s91 offen offset:0 sc1\n\t"
        "buffer_load_dwordx4 v[94:97], %[vm], %[rs], s91 offen offset:1024 sc1\n\t"
        "buffer_load_dwordx4 v[98:101], %[vm], %[rs], s91 offen offset:2048 sc1\n\t"
        "buffer_load_dwordx4 v[102:105], %[vm], %[rs], s91 offen offset:3072 sc1\n\t"
        "buffer_load_dwordx4 v[106:109], %[vm], %[rs], s89 offen offset:0 sc1\n\t"
        "buffer_load_dwordx4 v[110:113], %[vm], %[rs], s89 offen offset:1024 sc1\n\t"
        "buffer_load_dwordx4 v[114:117], %[vm], %[rs], s89 offen offset:2048 sc1\n\t"
        "buffer_load_dwordx4 v[118:121], %[vm], %[rs], s89 offen offset:3072 sc1\n\t"
        "Ln%=:\n\t"
        "ds_read_b32 v88, v125 offset:16396\n\t"
        "s_sub_u32 s95, s95, %[left]\n\t"
        "s_mov_b32 %[s], s92\n\t"
        "s_waitcnt lgkmcnt(0)\n\t"
        "v_cmp_eq_u32 vcc, s93, v88\n\t"
        "s_cmp_eq_u32 %[s], 63\n\t"
        "s_cbranch_vccnz Lr%=\n\t"
        "s_cbranch_scc1 Lz%=\n\t"
        "s_cmp_lg_u32 %[why], 0\n\t"
        "s_cbranch_scc1 Lend%=\n\t"
        "ds_write_b64 %[uw], v[80:81] offset:0\n\t"
        "ds_write_b64 %[uw], v[80:81] offset:512\n\t"
        "ds_write_b64 %[uw], v[80:81] offset:1024\n\t"
        "ds_write_b64 %[uw], v[80:81] offset:1536\n\t"
        "s_waitcnt vmcnt(8)\n\t"
        "v_lshlrev_b32 v122, 1, v122\n\t"
        "v_lshl_or_b32 v123, v123, 1, 1\n\t"
        "v_min_u32 v122, v122, v123\n\t"
        "s_waitcnt vmcnt(6)\n\t"
        "v_mov_b32 v16, v90\n\t"
        "v_mov_b32 v17, v94\n\t"
        "v_min_u32_dpp v122, v122, v122 quad_perm:[1,0,3,2] row_mask:0xf bank_mask:0xf\n\t"
        "v_mov_b32 v18, v91\n\t"
        "v_mov_b32 v19, v95\n\t"
        "v_min_u32_dpp v122, v122, v122 quad_perm:[2,3,0,1] row_mask:0xf bank_mask:0xf\n\t"
        "v_mov_b32 v20, v92\n\t"
        "v_mov_b32 v21, v96\n\t"
        "v_min_u32_dpp v122, v122, v122 row_ror:4 row_mask:0xf bank_mask:0xf\n\t"
        "v_mov_b32 v22, v93\n\t"
        "v_mov_b32 v23, v97\n\t"
        "v_readfirstlane_b32 s96, v122\n\t"
        "s_waitcnt vmcnt(4)\n\t"
        "v_mov_b32 v24, v98\n\t"
        "v_mov_b32 v25, v102\n\t"
        "v_mov_b32 v26, v99\n\t"
        "v_mov_b32 v27, v103\n\t"
        "v_mov_b32 v28, v100\n\t"
        "v_mov_b32 v29, v104\n\t"
        "v_mov_b32 v30, v101\n\t"
        "v_mov_b32 v31, v105\n\t"
        "s_waitcnt vmcnt(2)\n\t"
        "v_mov_b32 v32, v106\n\t"
        "v_mov_b32 v33, v110\n\t"
        "v_mov_b32 v34, v107\n\t"
        "v_mov_b32 v35, v111\n\t"
        "v_mov_b32 v36, v108\n\t"
        "v_mov_b32 v37, v112\n\t"
        "v_mov_b32 v38, v109\n\t"
        "v_mov_b32 v39, v113\n\t"
        "s_max_u32 %[have], %[have], s96\n\t"
        "s_waitcnt vmcnt(0)\n\t"
        "v_mov_b32 v40, v114\n\t"
        "v_mov_b32 v41, v118\n\t"
        "v_mov_b32 v42, v115\n\t"
        "v_mov_b32 v43, v119\n\t"
        "v_mov_b32 v44, v116\n\t"
        "v_mov_b32 v45, v120\n\t"
        "v_mov_b32 v46, v117\n\t"
        "v_mov_b32 v47, v121\n\t"
        "s_branch Lp%=\n\t"
        "Lm%=:\n\t"
        "s_sub_u32 %[left], %[left], 1\n\t"
        "s_cbranch_scc0 Lg%=\n\t"
        "s_mov_b32 %[left], 0\n\t"
        "s_branch Lok%=\n\t"
        "Lr%=:\n\t"
        "s_mov_b32 %[why], 3\n\t"
        "s_branch Lend%=\n\t"
        "Lz%=:\n\t"
        "s_mov_b32 %[why], 0\n\t"
        "Lend%=:\n\t"
        "s_waitcnt vmcnt(0)\n\t"
        : [bn] "+{v[16:47]}"(bn), [s] "+&s"(s), [have] "+&s"(have), [left] "+&s"(left), [why] "=&s"(why)
        : [sidx] "v"(sidx), [eo] "v"(eo), [uw] "v"(uw), [fa] "v"(fa), [vm] "v"(vm), [fo] "v"(fo), [te] "v"(te), [tb] "v"(tb), [rs] "s"(rs), [lag] "s"(lag), [lb] "s"(lb)
        : "s88", "s89", "s90", "s91", "s92", "s93", "s94", "s95", "s96", "s97", "s98", "s99", "v48", "v49", "v50", "v51", "v52", "v53", "v54", "v55", "v56", "v57", "v58", "v59", "v60", "v61", "v62", "v63", "v64", "v65", "v66", "v67", "v68", "v69", "v70", "v71", "v72", "v73", "v74", "v75", "v76", "v77", "v78", "v79", "v80", "v81", "v82", "v83", "v84", "v85", "v86", "v87", "v88", "v89", "v90", "v91", "v92", "v93", "v94", "v95", "v96", "v97", "v98", "v99", "v100", "v101", "v102", "v103", "v104", "v105", "v106", "v107", "v108", "v109", "v110", "v111", "v112", "v113", "v114", "v115", "v116", "v117", "v118", "v119", "v120", "v121", "v122", "v123", "v124", "v125", "v126", "vcc", "scc", "memory");
}

__device__ __forceinline__ void r_f(u32& bad, u32& pass, u32 sidx, u32 vm, u32 ra, u32 mo, u32 uf, u32x4 rs, u32 so, u32 sz, u32 sm, u32 lb, u32 fv, u32 cap, u32 zp STP) {
    asm volatile(
        "v_mov_b32 v120, %[lb]\n\t"
        "s_add_u32 s92, %[sm], 0x1000\n\t"
        "s_add_u32 s93, %[sm], 0x4000\n\t"
        "s_add_u32 s94, %[sm], 0x5000\n\t"
        "s_add_u32 s95, %[so], 0x8000\n\t"
        "s_add_u32 s96, %[sz], 0x1000\n\t"
        "s_mov_b32 s97, m0\n\t"
        "s_mov_b32 m0, %[lb]\n\t"
        "s_mov_b32 %[pass], 0\n\t"
        "s_mov_b32 %[bad], 0\n\t"
        "s_setprio 3\n\t"
        "s_waitcnt vmcnt(0)\n\t"
        "s_barrier\n\t"
        "ds_read_b32 v116, v120 offset:16384\n\t"
        "s_waitcnt lgkmcnt(0)\n\t"
        "v_readfirstlane_b32 s98, v116\n\t"
        "s_nop 3\n\t"
        "s_getpc_b64 s[90:91]\n\t"
        "Lgp_%=:\n\t"
        "s_sub_u32 s90, s90, s98\n\t"
        "s_subb_u32 s91, s91, 0\n\t"
        "s_add_u32 s90, s90, Lzp_%=-Lgp_%=\n\t"
        "s_addc_u32 s91, s91, 0\n\t"
        "s_setpc_b64 s[90:91]\n\t"
        "s_nop 0\n\ts_nop 0\n\ts_nop 0\n\ts_nop 0\n\t"
        "s_nop 0\n\ts_nop 0\n\ts_nop 0\n\ts_nop 0\n\t"
        "s_nop 0\n\ts_nop 0\n\ts_nop 0\n\ts_nop 0\n\t"
        "s_nop 0\n\ts_nop 0\n\ts_nop 0\n\ts_nop 0\n\t"
        "s_nop 0\n\ts_nop 0\n\ts_nop 0\n\ts_nop 0\n\t"
        "s_nop 0\n\ts_nop 0\n\ts_nop 0\n\ts_nop 0\n\t"
        "s_nop 0\n\ts_nop 0\n\ts_nop 0\n\ts_nop 0\n\t"
        "s_nop 0\n\ts_nop 0\n\ts_nop 0\n\ts_nop 0\n\t"
        "s_nop 0\n\ts_nop 0\n\ts_nop 0\n\ts_nop 0\n\t"
        "s_nop 0\n\ts_nop 0\n\ts_nop 0\n\ts_nop 0\n\t"
        "s_nop 0\n\ts_nop 0\n\ts_nop 0\n\ts_nop 0\n\t"
        "s_nop 0\n\ts_nop 0\n\ts_nop 0\n\ts_nop 0\n\t"
        "s_nop 0\n\ts_nop 0\n\ts_nop 0\n\ts_nop 0\n\t"
        "s_nop 0\n\ts_nop 0\n\ts_nop 0\n\ts_nop 0\n\t"
        "s_nop 0\n\ts_nop 0\n\ts_nop 0\n\ts_nop 0\n\t"
        "s_nop 0\n\ts_nop 0\n\ts_nop 0\n\ts_nop 0\n\t"
        "s_nop 0\n\ts_nop 0\n\ts_nop 0\n\ts_nop 0\n\t"
        "s_nop 0\n\ts_nop 0\n\ts_nop 0\n\ts_nop 0\n\t"
        "s_nop 0\n\ts_nop 0\n\ts_nop 0\n\ts_nop 0\n\t"
        "s_nop 0\n\ts_nop 0\n\ts_nop 0\n\ts_nop 0\n\t"
        "s_nop 0\n\ts_nop 0\n\ts_nop 0\n\ts_nop 0\n\t"
        "s_nop 0\n\ts_nop 0\n\ts_nop 0\n\ts_nop 0\n\t"
        "s_nop 0\n\ts_nop 0\n\ts_nop 0\n\ts_nop 0\n\t"
        "s_nop 0\n\ts_nop 0\n\ts_nop 0\n\ts_nop 0\n\t"
        "s_nop 0\n\ts_nop 0\n\ts_nop 0\n\ts_nop 0\n\t"
        "s_nop 0\n\ts_nop 0\n\ts_nop 0\n\ts_nop 0\n\t"
        "s_nop 0\n\ts_nop 0\n\ts_nop 0\n\ts_nop 0\n\t"
        "s_nop 0\n\ts_nop 0\n\ts_nop 0\n\ts_nop 0\n\t"
        "s_nop 0\n\ts_nop 0\n\ts_nop 0\n\ts_nop 0\n\t"
        "s_nop 0\n\ts_nop 0\n\ts_nop 0\n\ts_nop 0\n\t"
        "s_nop 0\n\ts_nop 0\n\ts_nop 0\n\ts_nop 0\n\t"
        "s_nop 0\n\ts_nop 0\n\ts_nop 0\n\ts_nop 0\n\t"
        "Lzp_%=:\n\t"
        "buffer_load_dwordx4 %[vm], %[rs], %[sz] offen offset:0 sc1 lds\n\t"
        "buffer_load_dwordx4 %[vm], %[rs], %[sz] offen offset:1024 sc1 lds\n\t"
        "buffer_load_dwordx4 %[vm], %[rs], %[sz] offen offset:2048 sc1 lds\n\t"
        "buffer_load_dwordx4 %[vm], %[rs], %[sz] offen offset:3072 sc1 lds\n\t"
        "s_add_u32 m0, m0, 0x1000\n\t"
        "s_nop 0\n\t"
        "buffer_load_dwordx4 %[vm], %[rs], s96 offen offset:0 sc1 lds\n\t"
        "buffer_load_dwordx4 %[vm], %[rs], s96 offen offset:1024 sc1 lds\n\t"
        "buffer_load_dwordx4 %[vm], %[rs], s96 offen offset:2048 sc1 lds\n\t"
        "buffer_load_dwordx4 %[vm], %[rs], s96 offen offset:3072 sc1 lds\n\t"
        "s_getpc_b64 s[90:91]\n\t"
        "Lgq_%=:\n\t"
        "s_sub_u32 s90, s90, %[zp]\n\t"
        "s_subb_u32 s91, s91, 0\n\t"
        "s_add_u32 s90, s90, Lzq_%=-Lgq_%=\n\t"
        "s_addc_u32 s91, s91, 0\n\t"
        "s_setpc_b64 s[90:91]\n\t"
        "s_nop 0\n\ts_nop 0\n\ts_nop 0\n\ts_nop 0\n\t"
        "s_nop 0\n\ts_nop 0\n\ts_nop 0\n\ts_nop 0\n\t"
        "s_nop 0\n\ts_nop 0\n\ts_nop 0\n\ts_nop 0\n\t"
        "s_nop 0\n\ts_nop 0\n\ts_nop 0\n\ts_nop 0\n\t"
        "s_nop 0\n\ts_nop 0\n\ts_nop 0\n\ts_nop 0\n\t"
        "s_nop 0\n\ts_nop 0\n\ts_nop 0\n\ts_nop 0\n\t"
        "s_nop 0\n\ts_nop 0\n\ts_nop 0\n\ts_nop 0\n\t"
        "s_nop 0\n\ts_nop 0\n\ts_nop 0\n\ts_nop 0\n\t"
        "s_nop 0\n\ts_nop 0\n\ts_nop 0\n\ts_nop 0\n\t"
        "s_nop 0\n\ts_nop 0\n\ts_nop 0\n\ts_nop 0\n\t"
        "s_nop 0\n\ts_nop 0\n\ts_nop 0\n\ts_nop 0\n\t"
        "s_nop 0\n\ts_nop 0\n\ts_nop 0\n\ts_nop 0\n\t"
        "s_nop 0\n\ts_nop 0\n\ts_nop 0\n\ts_nop 0\n\t"
        "s_nop 0\n\ts_nop 0\n\ts_nop 0\n\ts_nop 0\n\t"
        "s_nop 0\n\ts_nop 0\n\ts_nop 0\n\ts_nop 0\n\t"
        "s_nop 0\n\ts_nop 0\n\ts_nop 0\n\ts_nop 0\n\t"
        "Lzq_%=:\n\t"
        "buffer_load_dwordx4 v[16:19], %[vm], %[rs], %[so] offen offset:0 sc1\n\t"
        "buffer_load_dwordx4 v[20:23], %[vm], %[rs], %[so] offen offset:2048 sc1\n\t"
        "buffer_load_dwordx4 v[24:27], %[vm], %[rs], %[so] offen offset:1024 sc1\n\t"
        "buffer_load_dwordx4 v[28:31], %[vm], %[rs], %[so] offen offset:3072 sc1\n\t"
        "buffer_load_dwordx4 v[32:35], %[vm], %[rs], s95 offen offset:0 sc1\n\t"
        "buffer_load_dwordx4 v[36:39], %[vm], %[rs], s95 offen offset:2048 sc1\n\t"
        "buffer_load_dwordx4 v[40:43], %[vm], %[rs], s95 offen offset:1024 sc1\n\t"
        "buffer_load_dwordx4 v[44:47], %[vm], %[rs], s95 offen offset:3072 sc1\n\t"
        "v_mov_b64 v[112:113], 0\n\t"
        "v_mov_b64 v[114:115], 0\n\t"
        "s_mov_b32 m0, %[lb]\n\t"
        "ds_read_b32 v121, %[uf]\n\t"
        "s_waitcnt vmcnt(13)\n\t"
        "ds_read_b128 v[48:51], %[ra] offset:0\n\t"
        "ds_read_b128 v[52:55], %[ra] offset:512\n\t"
        "s_waitcnt vmcnt(12)\n\t"
        "ds_read_b128 v[56:59], %[ra] offset:1024\n\t"
        "ds_read_b128 v[60:63], %[ra] offset:1536\n\t"
        "s_waitcnt vmcnt(11)\n\t"
        "ds_read_b128 v[64:67], %[ra] offset:2048\n\t"
        "ds_read_b128 v[68:71], %[ra] offset:2560\n\t"
        "s_waitcnt vmcnt(10)\n\t"
        "ds_read_b128 v[72:75], %[ra] offset:3072\n\t"
        "ds_read_b128 v[76:79], %[ra] offset:3584\n\t"
        "s_waitcnt vmcnt(7) lgkmcnt(6)\n\t"
        "v_smfmac_f32_16x16x64_bf16 v[112:115], v[16:19], v[48:55], %[sidx]\n\t"
        "ds_read_b128 v[80:83], %[ra] offset:4096\n\t"
        "ds_read_b128 v[84:87], %[ra] offset:4608\n\t"
        "s_waitcnt vmcnt(6) lgkmcnt(6)\n\t"
        "v_smfmac_f32_16x16x64_bf16 v[112:115], v[20:23], v[56:63], %[sidx]\n\t"
        "ds_read_b128 v[88:91], %[ra] offset:5120\n\t"
        "ds_read_b128 v[92:95], %[ra] offset:5632\n\t"
        "s_waitcnt vmcnt(5) lgkmcnt(6)\n\t"
        "v_smfmac_f32_16x16x64_bf16 v[112:115], v[24:27], v[64:71], %[sidx]\n\t"
        "ds_read_b128 v[96:99], %[ra] offset:6144\n\t"
        "ds_read_b128 v[100:103], %[ra] offset:6656\n\t"
        "s_waitcnt vmcnt(4) lgkmcnt(6)\n\t"
        "v_smfmac_f32_16x16x64_bf16 v[112:115], v[28:31], v[72:79], %[sidx]\n\t"
        "ds_read_b128 v[104:107], %[ra] offset:7168\n\t"
        "ds_read_b128 v[108:111], %[ra] offset:7680\n\t"
        "s_waitcnt vmcnt(3) lgkmcnt(6)\n\t"
        "v_smfmac_f32_16x16x64_bf16 v[112:115], v[32:35], v[80:87], %[sidx]\n\t"
        "s_waitcnt vmcnt(2) lgkmcnt(4)\n\t"
        "v_smfmac_f32_16x16x64_bf16 v[112:115], v[36:39], v[88:95], %[sidx]\n\t"
        "s_waitcnt vmcnt(1) lgkmcnt(2)\n\t"
        "v_smfmac_f32_16x16x64_bf16 v[112:115], v[40:43], v[96:103], %[sidx]\n\t"
        "s_waitcnt vmcnt(0) lgkmcnt(0)\n\t"
        "v_smfmac_f32_16x16x64_bf16 v[112:115], v[44:47], v[104:111], %[sidx]\n\t"
        "s_nop 7\n\t"
        "v_add_f32 v116, v112, v114\n\t"
        "v_add_f32 v117, v113, v115\n\t"
        "v_add_f32 v118, v113, v115\n\t"
        "v_mov_b32_dpp v118, v116 row_ror:8 row_mask:0xf bank_mask:0x3\n\t"
        "v_mov_b32_dpp v116, v117 row_ror:8 row_mask:0xf bank_mask:0xc\n\t"
        "v_cvt_pk_bf16_f32 v119, v116, v118\n\t"
        "v_cmpx_o_f32 vcc, v116, v118\n\t"
        "buffer_store_dword v119, %[mo], %[rs], %[sm] offen sc1\n\t"
        "buffer_store_dword v119, %[mo], %[rs], s92 offen sc1\n\t"
        "buffer_store_dword v119, %[mo], %[rs], s93 offen sc1\n\t"
        "buffer_store_dword v119, %[mo], %[rs], s94 offen sc1\n\t"
        "s_nop 2\n\t"
        "s_cmp_eq_u64 exec, -1\n\t"
        "s_mov_b64 exec, -1\n\t"
        "s_cbranch_scc0 Lag%=\n\t"
        "s_barrier\n\t"
        "s_branch Lend%=\n\t"
        "Lag%=:\n\t"
        "v_cmp_ne_u32 vcc, %[fv], v121\n\t"
        "s_add_u32 %[pass], %[pass], 1\n\t"
        "s_cbranch_vccnz Lzp_%=\n\t"
        "v_max_u32 v116, v16, v17\n\t"
        "v_max3_u32 v116, v116, v18, v19\n\t"
        "v_max3_u32 v116, v116, v20, v21\n\t"
        "v_max3_u32 v116, v116, v22, v23\n\t"
        "v_max3_u32 v116, v116, v24, v25\n\t"
        "v_max3_u32 v116, v116, v26, v27\n\t"
        "v_max3_u32 v116, v116, v28, v29\n\t"
        "v_max3_u32 v116, v116, v30, v31\n\t"
        "v_max3_u32 v116, v116, v32, v33\n\t"
        "v_max3_u32 v116, v116, v34, v35\n\t"
        "v_max3_u32 v116, v116, v36, v37\n\t"
        "v_max3_u32 v116, v116, v38, v39\n\t"
        "v_max3_u32 v116, v116, v40, v41\n\t"
        "v_max3_u32 v116, v116, v42, v43\n\t"
        "v_max3_u32 v116, v116, v44, v45\n\t"
        "v_max3_u32 v116, v116, v46, v47\n\t"
        "v_max3_u32 v116, v116, v48, v49\n\t"
        "v_max3_u32 v116, v116, v50, v51\n\t"
        "v_max3_u32 v116, v116, v52, v53\n\t"
        "v_max3_u32 v116, v116, v54, v55\n\t"
        "v_max3_u32 v116, v116, v56, v57\n\t"
        "v_max3_u32 v116, v116, v58, v59\n\t"
        "v_max3_u32 v116, v116, v60, v61\n\t"
        "v_max3_u32 v116, v116, v62, v63\n\t"
        "v_max3_u32 v116, v116, v64, v65\n\t"
        "v_max3_u32 v116, v116, v66, v67\n\t"
        "v_max3_u32 v116, v116, v68, v69\n\t"
        "v_max3_u32 v116, v116, v70, v71\n\t"
        "v_max3_u32 v116, v116, v72, v73\n\t"
        "v_max3_u32 v116, v116, v74, v75\n\t"
        "v_max3_u32 v116, v116, v76, v77\n\t"
        "v_max3_u32 v116, v116, v78, v79\n\t"
        "v_max3_u32 v116, v116, v80, v81\n\t"
        "v_max3_u32 v116, v116, v82, v83\n\t"
        "v_max3_u32 v116, v116, v84, v85\n\t"
        "v_max3_u32 v116, v116, v86, v87\n\t"
        "v_max3_u32 v116, v116, v88, v89\n\t"
        "v_max3_u32 v116, v116, v90, v91\n\t"
        "v_max3_u32 v116, v116, v92, v93\n\t"
        "v_max3_u32 v116, v116, v94, v95\n\t"
        "v_max3_u32 v116, v116, v96, v97\n\t"
        "v_max3_u32 v116, v116, v98, v99\n\t"
        "v_max3_u32 v116, v116, v100, v101\n\t"
        "v_max3_u32 v116, v116, v102, v103\n\t"
        "v_max3_u32 v116, v116, v104, v105\n\t"
        "v_max3_u32 v116, v116, v106, v107\n\t"
        "v_max3_u32 v116, v116, v108, v109\n\t"
        "v_max3_u32 v116, v116, v110, v111\n\t"
        "v_cmp_eq_u32 vcc, 0xffbfffbf, v116\n\t"
        "s_cmp_lt_u32 %[pass], %[cap]\n\t"
        "s_cbranch_vccz Lbad%=\n\t"
        "s_cbranch_scc1 Lzp_%=\n\t"
        "Lbad%=:\n\t"
        "s_mov_b32 %[bad], 1\n\t"
        "Lend%=:\n\t"
        "s_mov_b32 m0, s97\n\t"
        "s_setprio 0\n\t"
        : [bad] "=&s"(bad), [pass] "=&s"(pass)
        : [sidx] "v"(sidx), [vm] "v"(vm), [ra] "v"(ra), [mo] "v"(mo), [rs] "s"(rs), [so] "s"(so), [sz] "s"(sz), [sm] "s"(sm), [lb] "s"(lb), [fv] "s"(fv), [cap] "s"(cap), [uf] "v"(uf), [zp] "s"(zp)
        : "s88", "s89", "s90", "s91", "s92", "s93", "s94", "s95", "s96", "s97", "s98", "v16", "v17", "v18", "v19", "v20", "v21", "v22", "v23", "v24", "v25", "v26", "v27", "v28", "v29", "v30", "v31", "v32", "v33", "v34", "v35", "v36", "v37", "v38", "v39", "v40", "v41", "v42", "v43", "v44", "v45", "v46", "v47", "v48", "v49", "v50", "v51", "v52", "v53", "v54", "v55", "v56", "v57", "v58", "v59", "v60", "v61", "v62", "v63", "v64", "v65", "v66", "v67", "v68", "v69", "v70", "v71", "v72", "v73", "v74", "v75", "v76", "v77", "v78", "v79", "v80", "v81", "v82", "v83", "v84", "v85", "v86", "v87", "v88", "v89", "v90", "v91", "v92", "v93", "v94", "v95", "v96", "v97", "v98", "v99", "v100", "v101", "v102", "v103", "v104", "v105", "v106", "v107", "v108", "v109", "v110", "v111", "v112", "v113", "v114", "v115", "v116", "v117", "v118", "v119", "v120", "v121", "vcc", "scc", "memory");
}

__device__ __forceinline__ void wx0(const u32x32 (&W)[5], u32 xa, u32 oa, u32x4 rs, u32 xv, u32 xs, u32 xd, u32 o0, u32 o1 STP) {
    asm volatile(
        "ds_read_b128 v[192:195], %[xa] offset:0\n\t"
        "ds_read_b128 v[196:199], %[xa] offset:20480\n\t"
        "ds_read_b128 v[200:203], %[xa] offset:1024\n\t"
        "ds_read_b128 v[204:207], %[xa] offset:21504\n\t"
        "ds_read_b128 v[208:211], %[xa] offset:2048\n\t"
        "ds_read_b128 v[212:215], %[xa] offset:22528\n\t"
        "s_waitcnt vmcnt(8)\n\t"
        "s_barrier\n\t"
        "s_waitcnt lgkmcnt(4)\n\t"
        "v_mfma_f32_16x16x32_bf16 v[176:179], v[16:19], v[192:195], 0\n\t"
        "ds_read_b128 v[216:219], %[xa] offset:3072\n\t"
        "v_mfma_f32_16x16x32_bf16 v[184:187], v[16:19], v[196:199], 0\n\t"
        "ds_read_b128 v[220:223], %[xa] offset:23552\n\t"
        "v_mfma_f32_16x16x32_bf16 v[180:183], v[96:99], v[192:195], 0\n\t"
        "v_mfma_f32_16x16x32_bf16 v[188:191], v[96:99], v[196:199], 0\n\t"
        "s_waitcnt lgkmcnt(4)\n\t"
        "v_mfma_f32_16x16x32_bf16 v[176:179], v[20:23], v[200:203], v[176:179]\n\t"
        "ds_read_b128 v[192:195], %[xa] offset:4096\n\t"
        "v_mfma_f32_16x16x32_bf16 v[184:187], v[20:23], v[204:207], v[184:187]\n\t"
        "ds_read_b128 v[196:199], %[xa] offset:24576\n\t"
        "v_mfma_f32_16x16x32_bf16 v[180:183], v[100:103], v[200:203], v[180:183]\n\t"
        "v_mfma_f32_16x16x32_bf16 v[188:191], v[100:103], v[204:207], v[188:191]\n\t"
        "s_waitcnt lgkmcnt(4)\n\t"
        "v_mfma_f32_16x16x32_bf16 v[176:179], v[24:27], v[208:211], v[176:179]\n\t"
        "ds_read_b128 v[200:203], %[xa] offset:5120\n\t"
        "v_mfma_f32_16x16x32_bf16 v[184:187], v[24:27], v[212:215], v[184:187]\n\t"
        "ds_read_b128 v[204:207], %[xa] offset:25600\n\t"
        "v_mfma_f32_16x16x32_bf16 v[180:183], v[104:107], v[208:211], v[180:183]\n\t"
        "v_mfma_f32_16x16x32_bf16 v[188:191], v[104:107], v[212:215], v[188:191]\n\t"
        "s_waitcnt lgkmcnt(4)\n\t"
        "v_mfma_f32_16x16x32_bf16 v[176:179], v[28:31], v[216:219], v[176:179]\n\t"
        "ds_read_b128 v[208:211], %[xa] offset:6144\n\t"
        "v_mfma_f32_16x16x32_bf16 v[184:187], v[28:31], v[220:223], v[184:187]\n\t"
        "ds_read_b128 v[212:215], %[xa] offset:26624\n\t"
        "v_mfma_f32_16x16x32_bf16 v[180:183], v[108:111], v[216:219], v[180:183]\n\t"
        "v_mfma_f32_16x16x32_bf16 v[188:191], v[108:111], v[220:223], v[188:191]\n\t"
        "s_waitcnt lgkmcnt(4)\n\t"
        "v_mfma_f32_16x16x32_bf16 v[176:179], v[32:35], v[192:195], v[176:179]\n\t"
        "ds_read_b128 v[216:219], %[xa] offset:7168\n\t"
        "v_mfma_f32_16x16x32_bf16 v[184:187], v[32:35], v[196:199], v[184:187]\n\t"
        "ds_read_b128 v[220:223], %[xa] offset:27648\n\t"
        "v_mfma_f32_16x16x32_bf16 v[180:183], v[112:115], v[192:195], v[180:183]\n\t"
        "v_mfma_f32_16x16x32_bf16 v[188:191], v[112:115], v[196:199], v[188:191]\n\t"
        "s_waitcnt lgkmcnt(4)\n\t"
        "v_mfma_f32_16x16x32_bf16 v[176:179], v[36:39], v[200:203], v[176:179]\n\t"
        "ds_read_b128 v[192:195], %[xa] offset:8192\n\t"
        "v_mfma_f32_16x16x32_bf16 v[184:187], v[36:39], v[204:207], v[184:187]\n\t"
        "ds_read_b128 v[196:199], %[xa] offset:28672\n\t"
        "v_mfma_f32_16x16x32_bf16 v[180:183], v[116:119], v[200:203], v[180:183]\n\t"
        "v_mfma_f32_16x16x32_bf16 v[188:191], v[116:119], v[204:207], v[188:191]\n\t"
        "s_waitcnt lgkmcnt(4)\n\t"
        "v_mfma_f32_16x16x32_bf16 v[176:179], v[40:43], v[208:211], v[176:179]\n\t"
        "ds_read_b128 v[200:203], %[xa] offset:9216\n\t"
        "v_mfma_f32_16x16x32_bf16 v[184:187], v[40:43], v[212:215], v[184:187]\n\t"
        "ds_read_b128 v[204:207], %[xa] offset:29696\n\t"
        "v_mfma_f32_16x16x32_bf16 v[180:183], v[120:123], v[208:211], v[180:183]\n\t"
        "v_mfma_f32_16x16x32_bf16 v[188:191], v[120:123], v[212:215], v[188:191]\n\t"
        "s_waitcnt lgkmcnt(4)\n\t"
        "v_mfma_f32_16x16x32_bf16 v[176:179], v[44:47], v[216:219], v[176:179]\n\t"
        "ds_read_b128 v[208:211], %[xa] offset:10240\n\t"
        "v_mfma_f32_16x16x32_bf16 v[184:187], v[44:47], v[220:223], v[184:187]\n\t"
        "ds_read_b128 v[212:215], %[xa] offset:30720\n\t"
        "v_mfma_f32_16x16x32_bf16 v[180:183], v[124:127], v[216:219], v[180:183]\n\t"
        "v_mfma_f32_16x16x32_bf16 v[188:191], v[124:127], v[220:223], v[188:191]\n\t"
        "s_waitcnt lgkmcnt(4)\n\t"
        "v_mfma_f32_16x16x32_bf16 v[176:179], v[48:51], v[192:195], v[176:179]\n\t"
        "ds_read_b128 v[216:219], %[xa] offset:11264\n\t"
        "v_mfma_f32_16x16x32_bf16 v[184:187], v[48:51], v[196:199], v[184:187]\n\t"
        "ds_read_b128 v[220:223], %[xa] offset:31744\n\t"
        "v_mfma_f32_16x16x32_bf16 v[180:183], v[128:131], v[192:195], v[180:183]\n\t"
        "v_mfma_f32_16x16x32_bf16 v[188:191], v[128:131], v[196:199], v[188:191]\n\t"
        "s_waitcnt lgkmcnt(4)\n\t"
        "v_mfma_f32_16x16x32_bf16 v[176:179], v[52:55], v[200:203], v[176:179]\n\t"
        "ds_read_b128 v[192:195], %[xa] offset:12288\n\t"
        "v_mfma_f32_16x16x32_bf16 v[184:187], v[52:55], v[204:207], v[184:187]\n\t"
        "ds_read_b128 v[196:199], %[xa] offset:32768\n\t"
        "v_mfma_f32_16x16x32_bf16 v[180:183], v[132:135], v[200:203], v[180:183]\n\t"
        "v_mfma_f32_16x16x32_bf16 v[188:191], v[132:135], v[204:207], v[188:191]\n\t"
        "s_waitcnt lgkmcnt(4)\n\t"
        "v_mfma_f32_16x16x32_bf16 v[176:179], v[56:59], v[208:211], v[176:179]\n\t"
        "ds_read_b128 v[200:203], %[xa] offset:13312\n\t"
        "v_mfma_f32_16x16x32_bf16 v[184:187], v[56:59], v[212:215], v[184:187]\n\t"
        "ds_read_b128 v[204:207], %[xa] offset:33792\n\t"
        "v_mfma_f32_16x16x32_bf16 v[180:183], v[136:139], v[208:211], v[180:183]\n\t"
        "v_mfma_f32_16x16x32_bf16 v[188:191], v[136:139], v[212:215], v[188:191]\n\t"
        "s_waitcnt lgkmcnt(4)\n\t"
        "v_mfma_f32_16x16x32_bf16 v[176:179], v[60:63], v[216:219], v[176:179]\n\t"
        "ds_read_b128 v[208:211], %[xa] offset:14336\n\t"
        "v_mfma_f32_16x16x32_bf16 v[184:187], v[60:63], v[220:223], v[184:187]\n\t"
        "ds_read_b128 v[212:215], %[xa] offset:34816\n\t"
        "v_mfma_f32_16x16x32_bf16 v[180:183], v[140:143], v[216:219], v[180:183]\n\t"
        "v_mfma_f32_16x16x32_bf16 v[188:191], v[140:143], v[220:223], v[188:191]\n\t"
        "s_waitcnt lgkmcnt(4)\n\t"
        "v_mfma_f32_16x16x32_bf16 v[176:179], v[64:67], v[192:195], v[176:179]\n\t"
        "ds_read_b128 v[216:219], %[xa] offset:15360\n\t"
        "v_mfma_f32_16x16x32_bf16 v[184:187], v[64:67], v[196:199], v[184:187]\n\t"
        "ds_read_b128 v[220:223], %[xa] offset:35840\n\t"
        "v_mfma_f32_16x16x32_bf16 v[180:183], v[144:147], v[192:195], v[180:183]\n\t"
        "v_mfma_f32_16x16x32_bf16 v[188:191], v[144:147], v[196:199], v[188:191]\n\t"
        "s_waitcnt lgkmcnt(4)\n\t"
        "v_mfma_f32_16x16x32_bf16 v[176:179], v[68:71], v[200:203], v[176:179]\n\t"
        "ds_read_b128 v[192:195], %[xa] offset:16384\n\t"
        "v_mfma_f32_16x16x32_bf16 v[184:187], v[68:71], v[204:207], v[184:187]\n\t"
        "ds_read_b128 v[196:199], %[xa] offset:36864\n\t"
        "v_mfma_f32_16x16x32_bf16 v[180:183], v[148:151], v[200:203], v[180:183]\n\t"
        "v_mfma_f32_16x16x32_bf16 v[188:191], v[148:151], v[204:207], v[188:191]\n\t"
        "s_waitcnt lgkmcnt(4)\n\t"
        "v_mfma_f32_16x16x32_bf16 v[176:179], v[72:75], v[208:211], v[176:179]\n\t"
        "ds_read_b128 v[200:203], %[xa] offset:17408\n\t"
        "v_mfma_f32_16x16x32_bf16 v[184:187], v[72:75], v[212:215], v[184:187]\n\t"
        "ds_read_b128 v[204:207], %[xa] offset:37888\n\t"
        "v_mfma_f32_16x16x32_bf16 v[180:183], v[152:155], v[208:211], v[180:183]\n\t"
        "v_mfma_f32_16x16x32_bf16 v[188:191], v[152:155], v[212:215], v[188:191]\n\t"
        "s_waitcnt lgkmcnt(4)\n\t"
        "v_mfma_f32_16x16x32_bf16 v[176:179], v[76:79], v[216:219], v[176:179]\n\t"
        "ds_read_b128 v[208:211], %[xa] offset:18432\n\t"
        "v_mfma_f32_16x16x32_bf16 v[184:187], v[76:79], v[220:223], v[184:187]\n\t"
        "ds_read_b128 v[212:215], %[xa] offset:38912\n\t"
        "v_mfma_f32_16x16x32_bf16 v[180:183], v[156:159], v[216:219], v[180:183]\n\t"
        "v_mfma_f32_16x16x32_bf16 v[188:191], v[156:159], v[220:223], v[188:191]\n\t"
        "s_waitcnt lgkmcnt(4)\n\t"
        "v_mfma_f32_16x16x32_bf16 v[176:179], v[80:83], v[192:195], v[176:179]\n\t"
        "ds_read_b128 v[216:219], %[xa] offset:19456\n\t"
        "v_mfma_f32_16x16x32_bf16 v[184:187], v[80:83], v[196:199], v[184:187]\n\t"
        "ds_read_b128 v[220:223], %[xa] offset:39936\n\t"
        "v_mfma_f32_16x16x32_bf16 v[180:183], v[160:163], v[192:195], v[180:183]\n\t"
        "v_mfma_f32_16x16x32_bf16 v[188:191], v[160:163], v[196:199], v[188:191]\n\t"
        "s_waitcnt lgkmcnt(4)\n\t"
        "v_mfma_f32_16x16x32_bf16 v[176:179], v[84:87], v[200:203], v[176:179]\n\t"
        "v_mfma_f32_16x16x32_bf16 v[184:187], v[84:87], v[204:207], v[184:187]\n\t"
        "v_mfma_f32_16x16x32_bf16 v[180:183], v[164:167], v[200:203], v[180:183]\n\t"
        "v_mfma_f32_16x16x32_bf16 v[188:191], v[164:167], v[204:207], v[188:191]\n\t"
        "s_waitcnt lgkmcnt(2)\n\t"
        "v_mfma_f32_16x16x32_bf16 v[176:179], v[88:91], v[208:211], v[176:179]\n\t"
        "v_mfma_f32_16x16x32_bf16 v[184:187], v[88:91], v[212:215], v[184:187]\n\t"
        "v_mfma_f32_16x16x32_bf16 v[180:183], v[168:171], v[208:211], v[180:183]\n\t"
        "v_mfma_f32_16x16x32_bf16 v[188:191], v[168:171], v[212:215], v[188:191]\n\t"
        "s_waitcnt lgkmcnt(0)\n\t"
        "v_mfma_f32_16x16x32_bf16 v[176:179], v[92:95], v[216:219], v[176:179]\n\t"
        "v_mfma_f32_16x16x32_bf16 v[184:187], v[92:95], v[220:223], v[184:187]\n\t"
        "v_mfma_f32_16x16x32_bf16 v[180:183], v[172:175], v[216:219], v[180:183]\n\t"
        "v_mfma_f32_16x16x32_bf16 v[188:191], v[172:175], v[220:223], v[188:191]\n\t"
        "s_nop 4\n\t"
        "v_cvt_pk_bf16_f32 v224, v176, v177\n\t"
        "v_cvt_pk_bf16_f32 v225, v178, v179\n\t"
        "v_cvt_pk_bf16_f32 v226, v180, v181\n\t"
        "v_cvt_pk_bf16_f32 v227, v182, v183\n\t"
        "buffer_store_dwordx4 v[224:227], %[oa], %[rs], %[o0] offen sc1\n\t"
        "v_cvt_pk_bf16_f32 v228, v184, v185\n\t"
        "v_cvt_pk_bf16_f32 v229, v186, v187\n\t"
        "v_cvt_pk_bf16_f32 v230, v188, v189\n\t"
        "v_cvt_pk_bf16_f32 v231, v190, v191\n\t"
        "buffer_store_dwordx4 v[228:231], %[oa], %[rs], %[o1] offen sc1\n\t"
        "s_mov_b32 s88, m0\n\t"
        "s_mov_b32 m0, %[xd]\n\t"
        "s_add_u32 s89, %[xs], 0x1000\n\t"
        "buffer_load_dwordx4 %[xv], %[rs], %[xs] offen offset:0 sc1 lds\n\t"
        "buffer_load_dwordx4 %[xv], %[rs], %[xs] offen offset:1024 sc1 lds\n\t"
        "buffer_load_dwordx4 %[xv], %[rs], %[xs] offen offset:2048 sc1 lds\n\t"
        "buffer_load_dwordx4 %[xv], %[rs], %[xs] offen offset:3072 sc1 lds\n\t"
        "s_add_u32 m0, m0, 0x1000\n\t"
        "s_nop 0\n\t"
        "buffer_load_dwordx4 %[xv], %[rs], s89 offen offset:0 sc1 lds\n\t"
        "buffer_load_dwordx4 %[xv], %[rs], s89 offen offset:1024 sc1 lds\n\t"
        "buffer_load_dwordx4 %[xv], %[rs], s89 offen offset:2048 sc1 lds\n\t"
        "buffer_load_dwordx4 %[xv], %[rs], s89 offen offset:3072 sc1 lds\n\t"
        "s_mov_b32 m0, s88\n\t"
        :
        : [w0] "{v[16:47]}"(W[0]), [w1] "{v[48:79]}"(W[1]), [w2] "{v[80:111]}"(W[2]), [w3] "{v[112:143]}"(W[3]), [w4] "{v[144:175]}"(W[4]), [xa] "v"(xa), [oa] "v"(oa), [xv] "v"(xv), [xs] "s"(xs), [xd] "s"(xd), [rs] "s"(rs), [o0] "s"(o0), [o1] "s"(o1)
        : "s88", "s89", "v176", "v177", "v178", "v179", "v180", "v181", "v182", "v183", "v184", "v185", "v186", "v187", "v188", "v189", "v190", "v191", "v192", "v193", "v194", "v195", "v196", "v197", "v198", "v199", "v200", "v201", "v202", "v203", "v204", "v205", "v206", "v207", "v208", "v209", "v210", "v211", "v212", "v213", "v214", "v215", "v216", "v217", "v218", "v219", "v220", "v221", "v222", "v223", "v224", "v225", "v226", "v227", "v228", "v229", "v230", "v231", "vcc", "scc", "memory");
}

__device__ __forceinline__ void wx1(const u32x32 (&W)[5], u32 xa, u32 oa STP) {
    asm volatile(
        "ds_read_b128 v[192:195], %[xa] offset:0\n\t"
        "ds_read_b128 v[196:199], %[xa] offset:20480\n\t"
        "ds_read_b128 v[200:203], %[xa] offset:1024\n\t"
        "ds_read_b128 v[204:207], %[xa] offset:21504\n\t"
        "ds_read_b128 v[208:211], %[xa] offset:2048\n\t"
        "ds_read_b128 v[212:215], %[xa] offset:22528\n\t"
        "s_waitcnt lgkmcnt(6)\n\t"
        "s_barrier\n\t"
        "s_waitcnt lgkmcnt(4)\n\t"
        "v_mfma_f32_16x16x32_bf16 v[176:179], v[16:19], v[192:195], 0\n\t"
        "ds_read_b128 v[216:219], %[xa] offset:3072\n\t"
        "v_mfma_f32_16x16x32_bf16 v[184:187], v[16:19], v[196:199], 0\n\t"
        "ds_read_b128 v[220:223], %[xa] offset:23552\n\t"
        "v_mfma_f32_16x16x32_bf16 v[180:183], v[96:99], v[192:195], 0\n\t"
        "v_mfma_f32_16x16x32_bf16 v[188:191], v[96:99], v[196:199], 0\n\t"
        "s_waitcnt lgkmcnt(4)\n\t"
        "v_mfma_f32_16x16x32_bf16 v[176:179], v[20:23], v[200:203], v[176:179]\n\t"
        "ds_read_b128 v[192:195], %[xa] offset:4096\n\t"
        "v_mfma_f32_16x16x32_bf16 v[184:187], v[20:23], v[204:207], v[184:187]\n\t"
        "ds_read_b128 v[196:199], %[xa] offset:24576\n\t"
        "v_mfma_f32_16x16x32_bf16 v[180:183], v[100:103], v[200:203], v[180:183]\n\t"
        "v_mfma_f32_16x16x32_bf16 v[188:191], v[100:103], v[204:207], v[188:191]\n\t"
        "s_waitcnt lgkmcnt(4)\n\t"
        "v_mfma_f32_16x16x32_bf16 v[176:179], v[24:27], v[208:211], v[176:179]\n\t"
        "ds_read_b128 v[200:203], %[xa] offset:5120\n\t"
        "v_mfma_f32_16x16x32_bf16 v[184:187], v[24:27], v[212:215], v[184:187]\n\t"
        "ds_read_b128 v[204:207], %[xa] offset:25600\n\t"
        "v_mfma_f32_16x16x32_bf16 v[180:183], v[104:107], v[208:211], v[180:183]\n\t"
        "v_mfma_f32_16x16x32_bf16 v[188:191], v[104:107], v[212:215], v[188:191]\n\t"
        "s_waitcnt lgkmcnt(4)\n\t"
        "v_mfma_f32_16x16x32_bf16 v[176:179], v[28:31], v[216:219], v[176:179]\n\t"
        "ds_read_b128 v[208:211], %[xa] offset:6144\n\t"
        "v_mfma_f32_16x16x32_bf16 v[184:187], v[28:31], v[220:223], v[184:187]\n\t"
        "ds_read_b128 v[212:215], %[xa] offset:26624\n\t"
        "v_mfma_f32_16x16x32_bf16 v[180:183], v[108:111], v[216:219], v[180:183]\n\t"
        "v_mfma_f32_16x16x32_bf16 v[188:191], v[108:111], v[220:223], v[188:191]\n\t"
        "s_waitcnt lgkmcnt(4)\n\t"
        "v_mfma_f32_16x16x32_bf16 v[176:179], v[32:35], v[192:195], v[176:179]\n\t"
        "ds_read_b128 v[216:219], %[xa] offset:7168\n\t"
        "v_mfma_f32_16x16x32_bf16 v[184:187], v[32:35], v[196:199], v[184:187]\n\t"
        "ds_read_b128 v[220:223], %[xa] offset:27648\n\t"
        "v_mfma_f32_16x16x32_bf16 v[180:183], v[112:115], v[192:195], v[180:183]\n\t"
        "v_mfma_f32_16x16x32_bf16 v[188:191], v[112:115], v[196:199], v[188:191]\n\t"
        "s_waitcnt lgkmcnt(4)\n\t"
        "v_mfma_f32_16x16x32_bf16 v[176:179], v[36:39], v[200:203], v[176:179]\n\t"
        "ds_read_b128 v[192:195], %[xa] offset:8192\n\t"
        "v_mfma_f32_16x16x32_bf16 v[184:187], v[36:39], v[204:207], v[184:187]\n\t"
        "ds_read_b128 v[196:199], %[xa] offset:28672\n\t"
        "v_mfma_f32_16x16x32_bf16 v[180:183], v[116:119], v[200:203], v[180:183]\n\t"
        "v_mfma_f32_16x16x32_bf16 v[188:191], v[116:119], v[204:207], v[188:191]\n\t"
        "s_waitcnt lgkmcnt(4)\n\t"
        "v_mfma_f32_16x16x32_bf16 v[176:179], v[40:43], v[208:211], v[176:179]\n\t"
        "ds_read_b128 v[200:203], %[xa] offset:9216\n\t"
        "v_mfma_f32_16x16x32_bf16 v[184:187], v[40:43], v[212:215], v[184:187]\n\t"
        "ds_read_b128 v[204:207], %[xa] offset:29696\n\t"
        "v_mfma_f32_16x16x32_bf16 v[180:183], v[120:123], v[208:211], v[180:183]\n\t"
        "v_mfma_f32_16x16x32_bf16 v[188:191], v[120:123], v[212:215], v[188:191]\n\t"
        "s_waitcnt lgkmcnt(4)\n\t"
        "v_mfma_f32_16x16x32_bf16 v[176:179], v[44:47], v[216:219], v[176:179]\n\t"
        "ds_read_b128 v[208:211], %[xa] offset:10240\n\t"
        "v_mfma_f32_16x16x32_bf16 v[184:187], v[44:47], v[220:223], v[184:187]\n\t"
        "ds_read_b128 v[212:215], %[xa] offset:30720\n\t"
        "v_mfma_f32_16x16x32_bf16 v[180:183], v[124:127], v[216:219], v[180:183]\n\t"
        "v_mfma_f32_16x16x32_bf16 v[188:191], v[124:127], v[220:223], v[188:191]\n\t"
        "s_waitcnt lgkmcnt(4)\n\t"
        "v_mfma_f32_16x16x32_bf16 v[176:179], v[48:51], v[192:195], v[176:179]\n\t"
        "ds_read_b128 v[216:219], %[xa] offset:11264\n\t"
        "v_mfma_f32_16x16x32_bf16 v[184:187], v[48:51], v[196:199], v[184:187]\n\t"
        "ds_read_b128 v[220:223], %[xa] offset:31744\n\t"
        "v_mfma_f32_16x16x32_bf16 v[180:183], v[128:131], v[192:195], v[180:183]\n\t"
        "v_mfma_f32_16x16x32_bf16 v[188:191], v[128:131], v[196:199], v[188:191]\n\t"
        "s_waitcnt lgkmcnt(4)\n\t"
        "v_mfma_f32_16x16x32_bf16 v[176:179], v[52:55], v[200:203], v[176:179]\n\t"
        "ds_read_b128 v[192:195], %[xa] offset:12288\n\t"
        "v_mfma_f32_16x16x32_bf16 v[184:187], v[52:55], v[204:207], v[184:187]\n\t"
        "ds_read_b128 v[196:199], %[xa] offset:32768\n\t"
        "v_mfma_f32_16x16x32_bf16 v[180:183], v[132:135], v[200:203], v[180:183]\n\t"
        "v_mfma_f32_16x16x32_bf16 v[188:191], v[132:135], v[204:207], v[188:191]\n\t"
        "s_waitcnt lgkmcnt(4)\n\t"
        "v_mfma_f32_16x16x32_bf16 v[176:179], v[56:59], v[208:211], v[176:179]\n\t"
        "ds_read_b128 v[200:203], %[xa] offset:13312\n\t"
        "v_mfma_f32_16x16x32_bf16 v[184:187], v[56:59], v[212:215], v[184:187]\n\t"
        "ds_read_b128 v[204:207], %[xa] offset:33792\n\t"
        "v_mfma_f32_16x16x32_bf16 v[180:183], v[136:139], v[208:211], v[180:183]\n\t"
        "v_mfma_f32_16x16x32_bf16 v[188:191], v[136:139], v[212:215], v[188:191]\n\t"
        "s_waitcnt lgkmcnt(4)\n\t"
        "v_mfma_f32_16x16x32_bf16 v[176:179], v[60:63], v[216:219], v[176:179]\n\t"
        "ds_read_b128 v[208:211], %[xa] offset:14336\n\t"
        "v_mfma_f32_16x16x32_bf16 v[184:187], v[60:63], v[220:223], v[184:187]\n\t"
        "ds_read_b128 v[212:215], %[xa] offset:34816\n\t"
        "v_mfma_f32_16x16x32_bf16 v[180:183], v[140:143], v[216:219], v[180:183]\n\t"
        "v_mfma_f32_16x16x32_bf16 v[188:191], v[140:143], v[220:223], v[188:191]\n\t"
        "s_waitcnt lgkmcnt(4)\n\t"
        "v_mfma_f32_16x16x32_bf16 v[176:179], v[64:67], v[192:195], v[176:179]\n\t"
        "ds_read_b128 v[216:219], %[xa] offset:15360\n\t"
        "v_mfma_f32_16x16x32_bf16 v[184:187], v[64:67], v[196:199], v[184:187]\n\t"
        "ds_read_b128 v[220:223], %[xa] offset:35840\n\t"
        "v_mfma_f32_16x16x32_bf16 v[180:183], v[144:147], v[192:195], v[180:183]\n\t"
        "v_mfma_f32_16x16x32_bf16 v[188:191], v[144:147], v[196:199], v[188:191]\n\t"
        "s_waitcnt lgkmcnt(4)\n\t"
        "v_mfma_f32_16x16x32_bf16 v[176:179], v[68:71], v[200:203], v[176:179]\n\t"
        "ds_read_b128 v[192:195], %[xa] offset:16384\n\t"
        "v_mfma_f32_16x16x32_bf16 v[184:187], v[68:71], v[204:207], v[184:187]\n\t"
        "ds_read_b128 v[196:199], %[xa] offset:36864\n\t"
        "v_mfma_f32_16x16x32_bf16 v[180:183], v[148:151], v[200:203], v[180:183]\n\t"
        "v_mfma_f32_16x16x32_bf16 v[188:191], v[148:151], v[204:207], v[188:191]\n\t"
        "s_waitcnt lgkmcnt(4)\n\t"
        "v_mfma_f32_16x16x32_bf16 v[176:179], v[72:75], v[208:211], v[176:179]\n\t"
        "ds_read_b128 v[200:203], %[xa] offset:17408\n\t"
        "v_mfma_f32_16x16x32_bf16 v[184:187], v[72:75], v[212:215], v[184:187]\n\t"
        "ds_read_b128 v[204:207], %[xa] offset:37888\n\t"
        "v_mfma_f32_16x16x32_bf16 v[180:183], v[152:155], v[208:211], v[180:183]\n\t"
        "v_mfma_f32_16x16x32_bf16 v[188:191], v[152:155], v[212:215], v[188:191]\n\t"
        "s_waitcnt lgkmcnt(4)\n\t"
        "v_mfma_f32_16x16x32_bf16 v[176:179], v[76:79], v[216:219], v[176:179]\n\t"
        "ds_read_b128 v[208:211], %[xa] offset:18432\n\t"
        "v_mfma_f32_16x16x32_bf16 v[184:187], v[76:79], v[220:223], v[184:187]\n\t"
        "ds_read_b128 v[212:215], %[xa] offset:38912\n\t"
        "v_mfma_f32_16x16x32_bf16 v[180:183], v[156:159], v[216:219], v[180:183]\n\t"
        "v_mfma_f32_16x16x32_bf16 v[188:191], v[156:159], v[220:223], v[188:191]\n\t"
        "s_waitcnt lgkmcnt(4)\n\t"
        "v_mfma_f32_16x16x32_bf16 v[176:179], v[80:83], v[192:195], v[176:179]\n\t"
        "ds_read_b128 v[216:219], %[xa] offset:19456\n\t"
        "v_mfma_f32_16x16x32_bf16 v[184:187], v[80:83], v[196:199], v[184:187]\n\t"
        "ds_read_b128 v[220:223], %[xa] offset:39936\n\t"
        "v_mfma_f32_16x16x32_bf16 v[180:183], v[160:163], v[192:195], v[180:183]\n\t"
        "v_mfma_f32_16x16x32_bf16 v[188:191], v[160:163], v[196:199], v[188:191]\n\t"
        "s_waitcnt lgkmcnt(4)\n\t"
        "v_mfma_f32_16x16x32_bf16 v[176:179], v[84:87], v[200:203], v[176:179]\n\t"
        "v_mfma_f32_16x16x32_bf16 v[184:187], v[84:87], v[204:207], v[184:187]\n\t"
        "v_mfma_f32_16x16x32_bf16 v[180:183], v[164:167], v[200:203], v[180:183]\n\t"
        "v_mfma_f32_16x16x32_bf16 v[188:191], v[164:167], v[204:207], v[188:191]\n\t"
        "s_waitcnt lgkmcnt(2)\n\t"
        "v_mfma_f32_16x16x32_bf16 v[176:179], v[88:91], v[208:211], v[176:179]\n\t"
        "v_mfma_f32_16x16x32_bf16 v[184:187], v[88:91], v[212:215], v[184:187]\n\t"
        "v_mfma_f32_16x16x32_bf16 v[180:183], v[168:171], v[208:211], v[180:183]\n\t"
        "v_mfma_f32_16x16x32_bf16 v[188:191], v[168:171], v[212:215], v[188:191]\n\t"
        "s_waitcnt lgkmcnt(0)\n\t"
        "v_mfma_f32_16x16x32_bf16 v[176:179], v[92:95], v[216:219], v[176:179]\n\t"
        "v_mfma_f32_16x16x32_bf16 v[184:187], v[92:95], v[220:223], v[184:187]\n\t"
        "v_mfma_f32_16x16x32_bf16 v[180:183], v[172:175], v[216:219], v[180:183]\n\t"
        "v_mfma_f32_16x16x32_bf16 v[188:191], v[172:175], v[220:223], v[188:191]\n\t"
        "s_nop 4\n\t"
        "v_cvt_pk_bf16_f32 v224, v176, v177\n\t"
        "v_cvt_pk_bf16_f32 v225, v178, v179\n\t"
        "v_cvt_pk_bf16_f32 v226, v180, v181\n\t"
        "v_cvt_pk_bf16_f32 v227, v182, v183\n\t"
        "ds_write_b128 %[oa], v[224:227] offset:0\n\t"
        "v_cvt_pk_bf16_f32 v228, v184, v185\n\t"
        "v_cvt_pk_bf16_f32 v229, v186, v187\n\t"
        "v_cvt_pk_bf16_f32 v230, v188, v189\n\t"
        "v_cvt_pk_bf16_f32 v231, v190, v191\n\t"
        "ds_write_b128 %[oa], v[228:231] offset:4096\n\t"
        :
        : [w0] "{v[16:47]}"(W[0]), [w1] "{v[48:79]}"(W[1]), [w2] "{v[80:111]}"(W[2]), [w3] "{v[112:143]}"(W[3]), [w4] "{v[144:175]}"(W[4]), [xa] "v"(xa), [oa] "v"(oa)
        : "v176", "v177", "v178", "v179", "v180", "v181", "v182", "v183", "v184", "v185", "v186", "v187", "v188", "v189", "v190", "v191", "v192", "v193", "v194", "v195", "v196", "v197", "v198", "v199", "v200", "v201", "v202", "v203", "v204", "v205", "v206", "v207", "v208", "v209", "v210", "v211", "v212", "v213", "v214", "v215", "v216", "v217", "v218", "v219", "v220", "v221", "v222", "v223", "v224", "v225", "v226", "v227", "v228", "v229", "v230", "v231", "vcc", "scc", "memory");
}

__device__ __forceinline__ void wk(const u32x32 (&W)[5], u32 xa, u32 oa, u32x4 rs, u32 o0, u32 o1, float sc, u32 xv, u32 xs, u32 xd, u32 ca, u32 fc, u32 fw, u32 fo STP) {
    asm volatile(
        "ds_read_b128 v[192:195], %[xa] offset:0\n\t"
        "ds_read_b128 v[196:199], %[xa] offset:20480\n\t"
        "ds_read_b128 v[200:203], %[xa] offset:1024\n\t"
        "ds_read_b128 v[204:207], %[xa] offset:21504\n\t"
        "ds_read_b128 v[208:211], %[xa] offset:2048\n\t"
        "ds_read_b128 v[212:215], %[xa] offset:22528\n\t"
        "v_mov_b32 v232, %[ca]\n\t"
        "v_mov_b32 v233, 1\n\t"
        "v_mov_b32 v234, %[fw]\n\t"
        "s_mov_b32 s88, m0\n\t"
        "s_mov_b32 m0, %[xd]\n\t"
        "s_add_u32 s89, %[xs], 0x1000\n\t"
        "buffer_load_dwordx4 %[xv], %[rs], %[xs] offen offset:0 sc1 lds\n\t"
        "buffer_load_dwordx4 %[xv], %[rs], %[xs] offen offset:1024 sc1 lds\n\t"
        "s_mov_b32 m0, s88\n\t"
        "s_waitcnt lgkmcnt(4)\n\t"
        "v_mfma_f32_16x16x32_bf16 v[176:179], v[16:19], v[192:195], 0\n\t"
        "ds_read_b128 v[216:219], %[xa] offset:3072\n\t"
        "v_mfma_f32_16x16x32_bf16 v[184:187], v[16:19], v[196:199], 0\n\t"
        "ds_read_b128 v[220:223], %[xa] offset:23552\n\t"
        "v_mfma_f32_16x16x32_bf16 v[180:183], v[96:99], v[192:195], 0\n\t"
        "v_mfma_f32_16x16x32_bf16 v[188:191], v[96:99], v[196:199], 0\n\t"
        "s_waitcnt lgkmcnt(4)\n\t"
        "v_mfma_f32_16x16x32_bf16 v[176:179], v[20:23], v[200:203], v[176:179]\n\t"
        "ds_read_b128 v[192:195], %[xa] offset:4096\n\t"
        "v_mfma_f32_16x16x32_bf16 v[184:187], v[20:23], v[204:207], v[184:187]\n\t"
        "ds_read_b128 v[196:199], %[xa] offset:24576\n\t"
        "v_mfma_f32_16x16x32_bf16 v[180:183], v[100:103], v[200:203], v[180:183]\n\t"
        "v_mfma_f32_16x16x32_bf16 v[188:191], v[100:103], v[204:207], v[188:191]\n\t"
        "s_waitcnt lgkmcnt(4)\n\t"
        "v_mfma_f32_16x16x32_bf16 v[176:179], v[24:27], v[208:211], v[176:179]\n\t"
        "ds_read_b128 v[200:203], %[xa] offset:5120\n\t"
        "v_mfma_f32_16x16x32_bf16 v[184:187], v[24:27], v[212:215], v[184:187]\n\t"
        "ds_read_b128 v[204:207], %[xa] offset:25600\n\t"
        "v_mfma_f32_16x16x32_bf16 v[180:183], v[104:107], v[208:211], v[180:183]\n\t"
        "v_mfma_f32_16x16x32_bf16 v[188:191], v[104:107], v[212:215], v[188:191]\n\t"
        "s_waitcnt lgkmcnt(4)\n\t"
        "v_mfma_f32_16x16x32_bf16 v[176:179], v[28:31], v[216:219], v[176:179]\n\t"
        "ds_read_b128 v[208:211], %[xa] offset:6144\n\t"
        "v_mfma_f32_16x16x32_bf16 v[184:187], v[28:31], v[220:223], v[184:187]\n\t"
        "ds_read_b128 v[212:215], %[xa] offset:26624\n\t"
        "v_mfma_f32_16x16x32_bf16 v[180:183], v[108:111], v[216:219], v[180:183]\n\t"
        "v_mfma_f32_16x16x32_bf16 v[188:191], v[108:111], v[220:223], v[188:191]\n\t"
        "s_waitcnt lgkmcnt(4)\n\t"
        "v_mfma_f32_16x16x32_bf16 v[176:179], v[32:35], v[192:195], v[176:179]\n\t"
        "ds_read_b128 v[216:219], %[xa] offset:7168\n\t"
        "v_mfma_f32_16x16x32_bf16 v[184:187], v[32:35], v[196:199], v[184:187]\n\t"
        "ds_read_b128 v[220:223], %[xa] offset:27648\n\t"
        "v_mfma_f32_16x16x32_bf16 v[180:183], v[112:115], v[192:195], v[180:183]\n\t"
        "v_mfma_f32_16x16x32_bf16 v[188:191], v[112:115], v[196:199], v[188:191]\n\t"
        "s_waitcnt lgkmcnt(4)\n\t"
        "v_mfma_f32_16x16x32_bf16 v[176:179], v[36:39], v[200:203], v[176:179]\n\t"
        "ds_read_b128 v[192:195], %[xa] offset:8192\n\t"
        "v_mfma_f32_16x16x32_bf16 v[184:187], v[36:39], v[204:207], v[184:187]\n\t"
        "ds_read_b128 v[196:199], %[xa] offset:28672\n\t"
        "v_mfma_f32_16x16x32_bf16 v[180:183], v[116:119], v[200:203], v[180:183]\n\t"
        "v_mfma_f32_16x16x32_bf16 v[188:191], v[116:119], v[204:207], v[188:191]\n\t"
        "s_waitcnt lgkmcnt(4)\n\t"
        "v_mfma_f32_16x16x32_bf16 v[176:179], v[40:43], v[208:211], v[176:179]\n\t"
        "ds_read_b128 v[200:203], %[xa] offset:9216\n\t"
        "v_mfma_f32_16x16x32_bf16 v[184:187], v[40:43], v[212:215], v[184:187]\n\t"
        "ds_read_b128 v[204:207], %[xa] offset:29696\n\t"
        "v_mfma_f32_16x16x32_bf16 v[180:183], v[120:123], v[208:211], v[180:183]\n\t"
        "s_waitcnt vmcnt(0)\n\t"
        "s_mov_b64 exec, 1\n\t"
        "ds_add_rtn_u32 v233, v232, v233\n\t"
        "s_mov_b64 exec, -1\n\t"
        "v_mfma_f32_16x16x32_bf16 v[188:191], v[120:123], v[212:215], v[188:191]\n\t"
        "s_waitcnt lgkmcnt(5)\n\t"
        "v_mfma_f32_16x16x32_bf16 v[176:179], v[44:47], v[216:219], v[176:179]\n\t"
        "ds_read_b128 v[208:211], %[xa] offset:10240\n\t"
        "v_mfma_f32_16x16x32_bf16 v[184:187], v[44:47], v[220:223], v[184:187]\n\t"
        "ds_read_b128 v[212:215], %[xa] offset:30720\n\t"
        "v_mfma_f32_16x16x32_bf16 v[180:183], v[124:127], v[216:219], v[180:183]\n\t"
        "v_mfma_f32_16x16x32_bf16 v[188:191], v[124:127], v[220:223], v[188:191]\n\t"
        "s_waitcnt lgkmcnt(5)\n\t"
        "v_mfma_f32_16x16x32_bf16 v[176:179], v[48:51], v[192:195], v[176:179]\n\t"
        "ds_read_b128 v[216:219], %[xa] offset:11264\n\t"
        "v_mfma_f32_16x16x32_bf16 v[184:187], v[48:51], v[196:199], v[184:187]\n\t"
        "ds_read_b128 v[220:223], %[xa] offset:31744\n\t"
        "v_mfma_f32_16x16x32_bf16 v[180:183], v[128:131], v[192:195], v[180:183]\n\t"
        "s_waitcnt lgkmcnt(4)\n\t"
        "s_mov_b64 exec, 1\n\t"
        "v_cmpx_eq_u32 vcc, %[fc], v233\n\t"
        "buffer_store_dword v234, off, %[rs], %[fo] sc1\n\t"
        "s_mov_b64 exec, -1\n\t"
        "v_mfma_f32_16x16x32_bf16 v[188:191], v[128:131], v[196:199], v[188:191]\n\t"
        "s_waitcnt lgkmcnt(5)\n\t"
        "v_mfma_f32_16x16x32_bf16 v[176:179], v[52:55], v[200:203], v[176:179]\n\t"
        "ds_read_b128 v[192:195], %[xa] offset:12288\n\t"
        "v_mfma_f32_16x16x32_bf16 v[184:187], v[52:55], v[204:207], v[184:187]\n\t"
        "ds_read_b128 v[196:199], %[xa] offset:32768\n\t"
        "v_mfma_f32_16x16x32_bf16 v[180:183], v[132:135], v[200:203], v[180:183]\n\t"
        "v_mfma_f32_16x16x32_bf16 v[188:191], v[132:135], v[204:207], v[188:191]\n\t"
        "s_waitcnt lgkmcnt(4)\n\t"
        "v_mfma_f32_16x16x32_bf16 v[176:179], v[56:59], v[208:211], v[176:179]\n\t"
        "ds_read_b128 v[200:203], %[xa] offset:13312\n\t"
        "v_mfma_f32_16x16x32_bf16 v[184:187], v[56:59], v[212:215], v[184:187]\n\t"
        "ds_read_b128 v[204:207], %[xa] offset:33792\n\t"
        "v_mfma_f32_16x16x32_bf16 v[180:183], v[136:139], v[208:211], v[180:183]\n\t"
        "v_mfma_f32_16x16x32_bf16 v[188:191], v[136:139], v[212:215], v[188:191]\n\t"
        "s_waitcnt lgkmcnt(4)\n\t"
        "v_mfma_f32_16x16x32_bf16 v[176:179], v[60:63], v[216:219], v[176:179]\n\t"
        "ds_read_b128 v[208:211], %[xa] offset:14336\n\t"
        "v_mfma_f32_16x16x32_bf16 v[184:187], v[60:63], v[220:223], v[184:187]\n\t"
        "ds_read_b128 v[212:215], %[xa] offset:34816\n\t"
        "v_mfma_f32_16x16x32_bf16 v[180:183], v[140:143], v[216:219], v[180:183]\n\t"
        "v_mfma_f32_16x16x32_bf16 v[188:191], v[140:143], v[220:223], v[188:191]\n\t"
        "s_waitcnt lgkmcnt(4)\n\t"
        "v_mfma_f32_16x16x32_bf16 v[176:179], v[64:67], v[192:195], v[176:179]\n\t"
        "ds_read_b128 v[216:219], %[xa] offset:15360\n\t"
        "v_mfma_f32_16x16x32_bf16 v[184:187], v[64:67], v[196:199], v[184:187]\n\t"
        "ds_read_b128 v[220:223], %[xa] offset:35840\n\t"
        "v_mfma_f32_16x16x32_bf16 v[180:183], v[144:147], v[192:195], v[180:183]\n\t"
        "v_mfma_f32_16x16x32_bf16 v[188:191], v[144:147], v[196:199], v[188:191]\n\t"
        "s_waitcnt lgkmcnt(4)\n\t"
        "v_mfma_f32_16x16x32_bf16 v[176:179], v[68:71], v[200:203], v[176:179]\n\t"
        "ds_read_b128 v[192:195], %[xa] offset:16384\n\t"
        "v_mfma_f32_16x16x32_bf16 v[184:187], v[68:71], v[204:207], v[184:187]\n\t"
        "ds_read_b128 v[196:199], %[xa] offset:36864\n\t"
        "v_mfma_f32_16x16x32_bf16 v[180:183], v[148:151], v[200:203], v[180:183]\n\t"
        "v_mfma_f32_16x16x32_bf16 v[188:191], v[148:151], v[204:207], v[188:191]\n\t"
        "s_waitcnt lgkmcnt(4)\n\t"
        "v_mfma_f32_16x16x32_bf16 v[176:179], v[72:75], v[208:211], v[176:179]\n\t"
        "ds_read_b128 v[200:203], %[xa] offset:17408\n\t"
        "v_mfma_f32_16x16x32_bf16 v[184:187], v[72:75], v[212:215], v[184:187]\n\t"
        "ds_read_b128 v[204:207], %[xa] offset:37888\n\t"
        "v_mfma_f32_16x16x32_bf16 v[180:183], v[152:155], v[208:211], v[180:183]\n\t"
        "v_mfma_f32_16x16x32_bf16 v[188:191], v[152:155], v[212:215], v[188:191]\n\t"
        "s_waitcnt lgkmcnt(4)\n\t"
        "v_mfma_f32_16x16x32_bf16 v[176:179], v[76:79], v[216:219], v[176:179]\n\t"
        "ds_read_b128 v[208:211], %[xa] offset:18432\n\t"
        "v_mfma_f32_16x16x32_bf16 v[184:187], v[76:79], v[220:223], v[184:187]\n\t"
        "ds_read_b128 v[212:215], %[xa] offset:38912\n\t"
        "v_mfma_f32_16x16x32_bf16 v[180:183], v[156:159], v[216:219], v[180:183]\n\t"
        "v_mfma_f32_16x16x32_bf16 v[188:191], v[156:159], v[220:223], v[188:191]\n\t"
        "s_waitcnt lgkmcnt(4)\n\t"
        "v_mfma_f32_16x16x32_bf16 v[176:179], v[80:83], v[192:195], v[176:179]\n\t"
        "ds_read_b128 v[216:219], %[xa] offset:19456\n\t"
        "v_mfma_f32_16x16x32_bf16 v[184:187], v[80:83], v[196:199], v[184:187]\n\t"
        "ds_read_b128 v[220:223], %[xa] offset:39936\n\t"
        "v_mfma_f32_16x16x32_bf16 v[180:183], v[160:163], v[192:195], v[180:183]\n\t"
        "v_mfma_f32_16x16x32_bf16 v[188:191], v[160:163], v[196:199], v[188:191]\n\t"
        "s_waitcnt lgkmcnt(4)\n\t"
        "v_mfma_f32_16x16x32_bf16 v[176:179], v[84:87], v[200:203], v[176:179]\n\t"
        "v_mfma_f32_16x16x32_bf16 v[184:187], v[84:87], v[204:207], v[184:187]\n\t"
        "v_mfma_f32_16x16x32_bf16 v[180:183], v[164:167], v[200:203], v[180:183]\n\t"
        "v_mfma_f32_16x16x32_bf16 v[188:191], v[164:167], v[204:207], v[188:191]\n\t"
        "s_waitcnt lgkmcnt(2)\n\t"
        "v_mfma_f32_16x16x32_bf16 v[176:179], v[88:91], v[208:211], v[176:179]\n\t"
        "v_mfma_f32_16x16x32_bf16 v[184:187], v[88:91], v[212:215], v[184:187]\n\t"
        "v_mfma_f32_16x16x32_bf16 v[180:183], v[168:171], v[208:211], v[180:183]\n\t"
        "v_mfma_f32_16x16x32_bf16 v[188:191], v[168:171], v[212:215], v[188:191]\n\t"
        "s_waitcnt lgkmcnt(0)\n\t"
        "v_mfma_f32_16x16x32_bf16 v[176:179], v[92:95], v[216:219], v[176:179]\n\t"
        "v_mfma_f32_16x16x32_bf16 v[184:187], v[92:95], v[220:223], v[184:187]\n\t"
        "v_mfma_f32_16x16x32_bf16 v[180:183], v[172:175], v[216:219], v[180:183]\n\t"
        "v_mfma_f32_16x16x32_bf16 v[188:191], v[172:175], v[220:223], v[188:191]\n\t"
        "s_nop 0\n\t"
        "s_barrier\n\t"
        "s_nop 4\n\t"
        "v_mul_f32 v176, %[sc], v176\n\t"
        "v_mul_f32 v177, %[sc], v177\n\t"
        "v_mul_f32 v178, %[sc], v178\n\t"
        "v_mul_f32 v179, %[sc], v179\n\t"
        "v_mul_f32 v180, %[sc], v180\n\t"
        "v_mul_f32 v181, %[sc], v181\n\t"
        "v_mul_f32 v182, %[sc], v182\n\t"
        "v_mul_f32 v183, %[sc], v183\n\t"
        "v_cvt_pk_bf16_f32 v224, v176, v177\n\t"
        "v_cvt_pk_bf16_f32 v225, v178, v179\n\t"
        "v_cvt_pk_bf16_f32 v226, v180, v181\n\t"
        "v_cvt_pk_bf16_f32 v227, v182, v183\n\t"
        "buffer_store_dwordx4 v[224:227], %[oa], %[rs], %[o0] offen sc1\n\t"
        "v_mul_f32 v184, %[sc], v184\n\t"
        "v_mul_f32 v185, %[sc], v185\n\t"
        "v_mul_f32 v186, %[sc], v186\n\t"
        "v_mul_f32 v187, %[sc], v187\n\t"
        "v_mul_f32 v188, %[sc], v188\n\t"
        "v_mul_f32 v189, %[sc], v189\n\t"
        "v_mul_f32 v190, %[sc], v190\n\t"
        "v_mul_f32 v191, %[sc], v191\n\t"
        "v_cvt_pk_bf16_f32 v228, v184, v185\n\t"
        "v_cvt_pk_bf16_f32 v229, v186, v187\n\t"
        "v_cvt_pk_bf16_f32 v230, v188, v189\n\t"
        "v_cvt_pk_bf16_f32 v231, v190, v191\n\t"
        "buffer_store_dwordx4 v[228:231], %[oa], %[rs], %[o1] offen sc1\n\t"
        "s_nop 1\n\t"
        :
        : [w0] "{v[16:47]}"(W[0]), [w1] "{v[48:79]}"(W[1]), [w2] "{v[80:111]}"(W[2]), [w3] "{v[112:143]}"(W[3]), [w4] "{v[144:175]}"(W[4]), [xa] "v"(xa), [oa] "v"(oa), [rs] "s"(rs), [o0] "s"(o0), [o1] "s"(o1), [sc] "s"(sc), [ca] "s"(ca), [fc] "s"(fc), [fw] "s"(fw), [fo] "s"(fo), [xv] "v"(xv), [xs] "s"(xs), [xd] "s"(xd)
        : "s88", "s89", "v176", "v177", "v178", "v179", "v180", "v181", "v182", "v183", "v184", "v185", "v186", "v187", "v188", "v189", "v190", "v191", "v192", "v193", "v194", "v195", "v196", "v197", "v198", "v199", "v200", "v201", "v202", "v203", "v204", "v205", "v206", "v207", "v208", "v209", "v210", "v211", "v212", "v213", "v214", "v215", "v216", "v217", "v218", "v219", "v220", "v221", "v222", "v223", "v224", "v225", "v226", "v227", "v228", "v229", "v230", "v231", "v232", "v233", "v234", "vcc", "scc", "memory");
}

__device__ __forceinline__ void wb(const u32x32 (&W)[5], u32 xa, u32 oa, u32x4 rs, u32 bl, u32 wp, u32 it, u32 xv, u32 xs, u32 xd, u32 ys, u32 yd, u32 ca, u32 fc, u32 fw, u32 fo STP) {
    asm volatile(
        "ds_read_b128 v[192:195], %[xa] offset:0\n\t"
        "ds_read_b128 v[196:199], %[xa] offset:1024\n\t"
        "ds_read_b128 v[200:203], %[xa] offset:2048\n\t"
        "ds_read_b128 v[204:207], %[xa] offset:3072\n\t"
        "ds_read_b128 v[208:211], %[xa] offset:4096\n\t"
        "ds_read_b128 v[212:215], %[xa] offset:5120\n\t"
        "ds_read_b128 v[216:219], %[xa] offset:6144\n\t"
        "ds_read_b128 v[220:223], %[xa] offset:7168\n\t"
        "v_mov_b32 v232, %[ca]\n\t"
        "v_mov_b32 v233, 1\n\t"
        "v_mov_b32 v234, %[fw]\n\t"
        "s_andn2_b32 s91, %[wp], 15\n\t"
        "s_mov_b32 s92, %[bl]\n\t"
        "s_mov_b32 s88, m0\n\t"
        "s_mov_b32 m0, %[xd]\n\t"
        "s_add_u32 s89, %[xs], 0x1000\n\t"
        "buffer_load_dwordx4 %[xv], %[rs], %[xs] offen offset:0 sc1 lds\n\t"
        "buffer_load_dwordx4 %[xv], %[rs], %[xs] offen offset:1024 sc1 lds\n\t"
        "s_mov_b32 m0, s88\n\t"
        "s_setprio 1\n\t"
        "s_waitcnt lgkmcnt(0)\n\t"
        "v_mfma_f32_16x16x32_bf16 v[176:179], v[192:195], v[16:19], 0\n\t"
        "s_lshr_b32 s95, s92, 20\n\t"
        "s_xnor_b32 s95, s95, %[wp]\n\t"
        "v_mfma_f32_16x16x32_bf16 v[180:183], v[208:211], v[16:19], 0\n\t"
        "s_and_b32 s95, s95, 1\n\t"
        "s_lshl_b32 s95, s95, 13\n\t"
        "v_mfma_f32_16x16x32_bf16 v[176:179], v[196:199], v[20:23], v[176:179]\n\t"
        "v_mfma_f32_16x16x32_bf16 v[180:183], v[212:215], v[20:23], v[180:183]\n\t"
        "v_mfma_f32_16x16x32_bf16 v[176:179], v[200:203], v[24:27], v[176:179]\n\t"
        "v_mfma_f32_16x16x32_bf16 v[180:183], v[216:219], v[24:27], v[180:183]\n\t"
        "v_mfma_f32_16x16x32_bf16 v[176:179], v[204:207], v[28:31], v[176:179]\n\t"
        "v_mfma_f32_16x16x32_bf16 v[180:183], v[220:223], v[28:31], v[180:183]\n\t"
        "s_lshl1_add_u32 s93, s92, s91\n\t"
        "s_add_u32 s93, s93, s95\n\t"
        "v_mfma_f32_16x16x32_bf16 v[184:187], v[192:195], v[96:99], 0\n\t"
        "s_add_u32 s92, s92, 0x40000\n\t"
        "v_mfma_f32_16x16x32_bf16 v[188:191], v[208:211], v[96:99], 0\n\t"
        "s_add_u32 s94, s93, 0x10000\n\t"
        "v_mfma_f32_16x16x32_bf16 v[184:187], v[196:199], v[100:103], v[184:187]\n\t"
        "v_cvt_pk_bf16_f32 v224, v176, v177\n\t"
        "v_mfma_f32_16x16x32_bf16 v[188:191], v[212:215], v[100:103], v[188:191]\n\t"
        "v_cvt_pk_bf16_f32 v225, v178, v179\n\t"
        "v_mfma_f32_16x16x32_bf16 v[184:187], v[200:203], v[104:107], v[184:187]\n\t"
        "v_cvt_pk_bf16_f32 v226, v180, v181\n\t"
        "v_mfma_f32_16x16x32_bf16 v[188:191], v[216:219], v[104:107], v[188:191]\n\t"
        "v_cvt_pk_bf16_f32 v227, v182, v183\n\t"
        "v_mfma_f32_16x16x32_bf16 v[184:187], v[204:207], v[108:111], v[184:187]\n\t"
        "buffer_store_dwordx4 v[224:227], %[oa], %[rs], s93 offen sc1\n\t"
        "v_mfma_f32_16x16x32_bf16 v[188:191], v[220:223], v[108:111], v[188:191]\n\t"
        "v_mfma_f32_16x16x32_bf16 v[176:179], v[192:195], v[32:35], 0\n\t"
        "s_lshr_b32 s95, s92, 20\n\t"
        "s_xnor_b32 s95, s95, %[wp]\n\t"
        "v_mfma_f32_16x16x32_bf16 v[180:183], v[208:211], v[32:35], 0\n\t"
        "s_and_b32 s95, s95, 1\n\t"
        "s_lshl_b32 s95, s95, 13\n\t"
        "v_mfma_f32_16x16x32_bf16 v[176:179], v[196:199], v[36:39], v[176:179]\n\t"
        "v_cvt_pk_bf16_f32 v228, v184, v185\n\t"
        "v_mfma_f32_16x16x32_bf16 v[180:183], v[212:215], v[36:39], v[180:183]\n\t"
        "v_cvt_pk_bf16_f32 v229, v186, v187\n\t"
        "v_mfma_f32_16x16x32_bf16 v[176:179], v[200:203], v[40:43], v[176:179]\n\t"
        "v_cvt_pk_bf16_f32 v230, v188, v189\n\t"
        "v_mfma_f32_16x16x32_bf16 v[180:183], v[216:219], v[40:43], v[180:183]\n\t"
        "v_cvt_pk_bf16_f32 v231, v190, v191\n\t"
        "v_mfma_f32_16x16x32_bf16 v[176:179], v[204:207], v[44:47], v[176:179]\n\t"
        "buffer_store_dwordx4 v[228:231], %[oa], %[rs], s94 offen sc1\n\t"
        "v_mfma_f32_16x16x32_bf16 v[180:183], v[220:223], v[44:47], v[180:183]\n\t"
        "s_lshl1_add_u32 s93, s92, s91\n\t"
        "s_add_u32 s93, s93, s95\n\t"
        "v_mfma_f32_16x16x32_bf16 v[184:187], v[192:195], v[112:115], 0\n\t"
        "s_add_u32 s92, s92, 0x40000\n\t"
        "v_mfma_f32_16x16x32_bf16 v[188:191], v[208:211], v[112:115], 0\n\t"
        "s_add_u32 s94, s93, 0x10000\n\t"
        "v_mfma_f32_16x16x32_bf16 v[184:187], v[196:199], v[116:119], v[184:187]\n\t"
        "v_cvt_pk_bf16_f32 v224, v176, v177\n\t"
        "v_mfma_f32_16x16x32_bf16 v[188:191], v[212:215], v[116:119], v[188:191]\n\t"
        "v_cvt_pk_bf16_f32 v225, v178, v179\n\t"
        "v_mfma_f32_16x16x32_bf16 v[184:187], v[200:203], v[120:123], v[184:187]\n\t"
        "v_cvt_pk_bf16_f32 v226, v180, v181\n\t"
        "v_mfma_f32_16x16x32_bf16 v[188:191], v[216:219], v[120:123], v[188:191]\n\t"
        "v_cvt_pk_bf16_f32 v227, v182, v183\n\t"
        "v_mfma_f32_16x16x32_bf16 v[184:187], v[204:207], v[124:127], v[184:187]\n\t"
        "buffer_store_dwordx4 v[224:227], %[oa], %[rs], s93 offen sc1\n\t"
        "v_mfma_f32_16x16x32_bf16 v[188:191], v[220:223], v[124:127], v[188:191]\n\t"
        "s_waitcnt vmcnt(5)\n\t"
        "s_mov_b64 exec, 1\n\t"
        "ds_add_rtn_u32 v233, v232, v233\n\t"
        "s_mov_b64 exec, -1\n\t"
        "v_mfma_f32_16x16x32_bf16 v[176:179], v[192:195], v[48:51], 0\n\t"
        "s_lshr_b32 s95, s92, 20\n\t"
        "s_xnor_b32 s95, s95, %[wp]\n\t"
        "v_mfma_f32_16x16x32_bf16 v[180:183], v[208:211], v[48:51], 0\n\t"
        "s_and_b32 s95, s95, 1\n\t"
        "s_lshl_b32 s95, s95, 13\n\t"
        "v_mfma_f32_16x16x32_bf16 v[176:179], v[196:199], v[52:55], v[176:179]\n\t"
        "v_cvt_pk_bf16_f32 v228, v184, v185\n\t"
        "v_mfma_f32_16x16x32_bf16 v[180:183], v[212:215], v[52:55], v[180:183]\n\t"
        "v_cvt_pk_bf16_f32 v229, v186, v187\n\t"
        "v_mfma_f32_16x16x32_bf16 v[176:179], v[200:203], v[56:59], v[176:179]\n\t"
        "v_cvt_pk_bf16_f32 v230, v188, v189\n\t"
        "v_mfma_f32_16x16x32_bf16 v[180:183], v[216:219], v[56:59], v[180:183]\n\t"
        "v_cvt_pk_bf16_f32 v231, v190, v191\n\t"
        "v_mfma_f32_16x16x32_bf16 v[176:179], v[204:207], v[60:63], v[176:179]\n\t"
        "buffer_store_dwordx4 v[228:231], %[oa], %[rs], s94 offen sc1\n\t"
        "v_mfma_f32_16x16x32_bf16 v[180:183], v[220:223], v[60:63], v[180:183]\n\t"
        "s_lshl1_add_u32 s93, s92, s91\n\t"
        "s_add_u32 s93, s93, s95\n\t"
        "v_mfma_f32_16x16x32_bf16 v[184:187], v[192:195], v[128:131], 0\n\t"
        "s_add_u32 s92, s92, 0x40000\n\t"
        "v_mfma_f32_16x16x32_bf16 v[188:191], v[208:211], v[128:131], 0\n\t"
        "s_add_u32 s94, s93, 0x10000\n\t"
        "v_mfma_f32_16x16x32_bf16 v[184:187], v[196:199], v[132:135], v[184:187]\n\t"
        "v_cvt_pk_bf16_f32 v224, v176, v177\n\t"
        "v_mfma_f32_16x16x32_bf16 v[188:191], v[212:215], v[132:135], v[188:191]\n\t"
        "v_cvt_pk_bf16_f32 v225, v178, v179\n\t"
        "v_mfma_f32_16x16x32_bf16 v[184:187], v[200:203], v[136:139], v[184:187]\n\t"
        "v_cvt_pk_bf16_f32 v226, v180, v181\n\t"
        "v_mfma_f32_16x16x32_bf16 v[188:191], v[216:219], v[136:139], v[188:191]\n\t"
        "v_cvt_pk_bf16_f32 v227, v182, v183\n\t"
        "v_mfma_f32_16x16x32_bf16 v[184:187], v[204:207], v[140:143], v[184:187]\n\t"
        "buffer_store_dwordx4 v[224:227], %[oa], %[rs], s93 offen sc1\n\t"
        "v_mfma_f32_16x16x32_bf16 v[188:191], v[220:223], v[140:143], v[188:191]\n\t"
        "s_waitcnt lgkmcnt(0)\n\t"
        "s_mov_b64 exec, 1\n\t"
        "v_cmpx_eq_u32 vcc, %[fc], v233\n\t"
        "buffer_store_dword v234, off, %[rs], %[fo] sc1\n\t"
        "s_mov_b64 exec, -1\n\t"
        "s_cmp_le_u32 %[it], %[nowv]\n\t"
        "s_cbranch_scc1 Lp%=\n\t"
        "s_setprio 0\n\t"
        "Lp%=:\n\t"
        "s_mov_b32 s88, m0\n\t"
        "s_mov_b32 m0, %[yd]\n\t"
        "s_add_u32 s89, %[ys], 0x1000\n\t"
        "buffer_load_dwordx4 %[xv], %[rs], %[ys] offen offset:0 sc1 lds\n\t"
        "buffer_load_dwordx4 %[xv], %[rs], %[ys] offen offset:1024 sc1 lds\n\t"
        "buffer_load_dwordx4 %[xv], %[rs], %[ys] offen offset:2048 sc1 lds\n\t"
        "buffer_load_dwordx4 %[xv], %[rs], %[ys] offen offset:3072 sc1 lds\n\t"
        "s_add_u32 m0, m0, 0x1000\n\t"
        "s_nop 0\n\t"
        "buffer_load_dwordx4 %[xv], %[rs], s89 offen offset:0 sc1 lds\n\t"
        "buffer_load_dwordx4 %[xv], %[rs], s89 offen offset:1024 sc1 lds\n\t"
        "buffer_load_dwordx4 %[xv], %[rs], s89 offen offset:2048 sc1 lds\n\t"
        "buffer_load_dwordx4 %[xv], %[rs], s89 offen offset:3072 sc1 lds\n\t"
        "s_mov_b32 m0, s88\n\t"
        "v_mfma_f32_16x16x32_bf16 v[176:179], v[192:195], v[64:67], 0\n\t"
        "s_lshr_b32 s95, s92, 20\n\t"
        "s_xnor_b32 s95, s95, %[wp]\n\t"
        "v_mfma_f32_16x16x32_bf16 v[180:183], v[208:211], v[64:67], 0\n\t"
        "s_and_b32 s95, s95, 1\n\t"
        "s_lshl_b32 s95, s95, 13\n\t"
        "v_mfma_f32_16x16x32_bf16 v[176:179], v[196:199], v[68:71], v[176:179]\n\t"
        "v_cvt_pk_bf16_f32 v228, v184, v185\n\t"
        "v_mfma_f32_16x16x32_bf16 v[180:183], v[212:215], v[68:71], v[180:183]\n\t"
        "v_cvt_pk_bf16_f32 v229, v186, v187\n\t"
        "v_mfma_f32_16x16x32_bf16 v[176:179], v[200:203], v[72:75], v[176:179]\n\t"
        "v_cvt_pk_bf16_f32 v230, v188, v189\n\t"
        "v_mfma_f32_16x16x32_bf16 v[180:183], v[216:219], v[72:75], v[180:183]\n\t"
        "v_cvt_pk_bf16_f32 v231, v190, v191\n\t"
        "v_mfma_f32_16x16x32_bf16 v[176:179], v[204:207], v[76:79], v[176:179]\n\t"
        "buffer_store_dwordx4 v[228:231], %[oa], %[rs], s94 offen sc1\n\t"
        "v_mfma_f32_16x16x32_bf16 v[180:183], v[220:223], v[76:79], v[180:183]\n\t"
        "s_lshl1_add_u32 s93, s92, s91\n\t"
        "s_add_u32 s93, s93, s95\n\t"
        "v_mfma_f32_16x16x32_bf16 v[184:187], v[192:195], v[144:147], 0\n\t"
        "s_add_u32 s92, s92, 0x40000\n\t"
        "v_mfma_f32_16x16x32_bf16 v[188:191], v[208:211], v[144:147], 0\n\t"
        "s_add_u32 s94, s93, 0x10000\n\t"
        "v_mfma_f32_16x16x32_bf16 v[184:187], v[196:199], v[148:151], v[184:187]\n\t"
        "v_cvt_pk_bf16_f32 v224, v176, v177\n\t"
        "v_mfma_f32_16x16x32_bf16 v[188:191], v[212:215], v[148:151], v[188:191]\n\t"
        "v_cvt_pk_bf16_f32 v225, v178, v179\n\t"
        "v_mfma_f32_16x16x32_bf16 v[184:187], v[200:203], v[152:155], v[184:187]\n\t"
        "v_cvt_pk_bf16_f32 v226, v180, v181\n\t"
        "v_mfma_f32_16x16x32_bf16 v[188:191], v[216:219], v[152:155], v[188:191]\n\t"
        "v_cvt_pk_bf16_f32 v227, v182, v183\n\t"
        "v_mfma_f32_16x16x32_bf16 v[184:187], v[204:207], v[156:159], v[184:187]\n\t"
        "buffer_store_dwordx4 v[224:227], %[oa], %[rs], s93 offen sc1\n\t"
        "v_mfma_f32_16x16x32_bf16 v[188:191], v[220:223], v[156:159], v[188:191]\n\t"
        "v_mfma_f32_16x16x32_bf16 v[176:179], v[192:195], v[80:83], 0\n\t"
        "s_lshr_b32 s95, s92, 20\n\t"
        "s_xnor_b32 s95, s95, %[wp]\n\t"
        "v_mfma_f32_16x16x32_bf16 v[180:183], v[208:211], v[80:83], 0\n\t"
        "s_and_b32 s95, s95, 1\n\t"
        "s_lshl_b32 s95, s95, 13\n\t"
        "v_mfma_f32_16x16x32_bf16 v[176:179], v[196:199], v[84:87], v[176:179]\n\t"
        "v_cvt_pk_bf16_f32 v228, v184, v185\n\t"
        "v_mfma_f32_16x16x32_bf16 v[180:183], v[212:215], v[84:87], v[180:183]\n\t"
        "v_cvt_pk_bf16_f32 v229, v186, v187\n\t"
        "v_mfma_f32_16x16x32_bf16 v[176:179], v[200:203], v[88:91], v[176:179]\n\t"
        "v_cvt_pk_bf16_f32 v230, v188, v189\n\t"
        "v_mfma_f32_16x16x32_bf16 v[180:183], v[216:219], v[88:91], v[180:183]\n\t"
        "v_cvt_pk_bf16_f32 v231, v190, v191\n\t"
        "v_mfma_f32_16x16x32_bf16 v[176:179], v[204:207], v[92:95], v[176:179]\n\t"
        "buffer_store_dwordx4 v[228:231], %[oa], %[rs], s94 offen sc1\n\t"
        "v_mfma_f32_16x16x32_bf16 v[180:183], v[220:223], v[92:95], v[180:183]\n\t"
        "s_lshl1_add_u32 s93, s92, s91\n\t"
        "s_add_u32 s93, s93, s95\n\t"
        "v_mfma_f32_16x16x32_bf16 v[184:187], v[192:195], v[160:163], 0\n\t"
        "s_add_u32 s92, s92, 0x40000\n\t"
        "v_mfma_f32_16x16x32_bf16 v[188:191], v[208:211], v[160:163], 0\n\t"
        "s_add_u32 s94, s93, 0x10000\n\t"
        "v_mfma_f32_16x16x32_bf16 v[184:187], v[196:199], v[164:167], v[184:187]\n\t"
        "v_cvt_pk_bf16_f32 v224, v176, v177\n\t"
        "v_mfma_f32_16x16x32_bf16 v[188:191], v[212:215], v[164:167], v[188:191]\n\t"
        "v_cvt_pk_bf16_f32 v225, v178, v179\n\t"
        "v_mfma_f32_16x16x32_bf16 v[184:187], v[200:203], v[168:171], v[184:187]\n\t"
        "v_cvt_pk_bf16_f32 v226, v180, v181\n\t"
        "v_mfma_f32_16x16x32_bf16 v[188:191], v[216:219], v[168:171], v[188:191]\n\t"
        "v_cvt_pk_bf16_f32 v227, v182, v183\n\t"
        "v_mfma_f32_16x16x32_bf16 v[184:187], v[204:207], v[172:175], v[184:187]\n\t"
        "buffer_store_dwordx4 v[224:227], %[oa], %[rs], s93 offen sc1\n\t"
        "v_mfma_f32_16x16x32_bf16 v[188:191], v[220:223], v[172:175], v[188:191]\n\t"
        "s_and_b32 s95, %[it], 31\n\t"
        "s_cmp_le_u32 s95, %[nowv]\n\t"
        "s_cbranch_scc1 Lnb%=\n\t"
        "s_waitcnt vmcnt(17)\n\t"
        "s_barrier\n\t"
        "Lnb%=:\n\t"
        "s_setprio 0\n\t"
        "s_nop 3\n\t"
        "v_cvt_pk_bf16_f32 v228, v184, v185\n\t"
        "v_cvt_pk_bf16_f32 v229, v186, v187\n\t"
        "v_cvt_pk_bf16_f32 v230, v188, v189\n\t"
        "v_cvt_pk_bf16_f32 v231, v190, v191\n\t"
        "buffer_store_dwordx4 v[228:231], %[oa], %[rs], s94 offen sc1\n\t"
        "s_nop 1\n\t"
        :
        : [w0] "{v[16:47]}"(W[0]), [w1] "{v[48:79]}"(W[1]), [w2] "{v[80:111]}"(W[2]), [w3] "{v[112:143]}"(W[3]), [w4] "{v[144:175]}"(W[4]), [xa] "v"(xa), [oa] "v"(oa), [rs] "s"(rs), [bl] "s"(bl), [wp] "s"(wp), [it] "s"(it), [nowv] "n"(NOWV), [ca] "s"(ca), [fc] "s"(fc), [fw] "s"(fw), [fo] "s"(fo), [xv] "v"(xv), [xs] "s"(xs), [xd] "s"(xd), [ys] "s"(ys), [yd] "s"(yd)
        : "s88", "s89", "s91", "s92", "s93", "s94", "s95", "v176", "v177", "v178", "v179", "v180", "v181", "v182", "v183", "v184", "v185", "v186", "v187", "v188", "v189", "v190", "v191", "v192", "v193", "v194", "v195", "v196", "v197", "v198", "v199", "v200", "v201", "v202", "v203", "v204", "v205", "v206", "v207", "v208", "v209", "v210", "v211", "v212", "v213", "v214", "v215", "v216", "v217", "v218", "v219", "v220", "v221", "v222", "v223", "v224", "v225", "v226", "v227", "v228", "v229", "v230", "v231", "v232", "v233", "v234", "vcc", "scc", "memory");
}

#define TP0 208
#define TP1 22
#define TP2 158
#define TP3 4
#define TP4 10
#define TP5 191
#define TP6 98
#define TP7 200
#define TP8 14
#define TP9 18
#define TP10 4
#define TP11 2
#define TP12 13
#define TP13 100
#define TP14 70
#define TP15 4
#define WAHEAD 3
#define FILLG 4
#define EARLY_N 35
#define BUMP_N 3
#define BUMP_WIN 0xffu
#define BUMP_S0 8
#define BUMP_MAX 3
#define TV(idx) ((u32)(TP##idx) * 8u)
#define TLOAD() do { } while (0)

__device__ __forceinline__ void nap(u32 n) {
    u32 hi = n >> 6, lo = (n >> 4) & 3u;
    asm volatile("s_cmp_eq_u32 %0, 0\n\ts_cbranch_scc1 Ln%=\nLm%=:\n\ts_sleep 1\n\ts_sub_u32 %0, %0, 1\n\ts_cmp_lg_u32 %0, 0\n\ts_cbranch_scc1 Lm%=\nLn%=:\n\t"
                 "s_cmp_eq_u32 %1, 0\n\ts_cbranch_scc1 Lp%=\nLo%=:\n\ts_sub_u32 %1, %1, 1\n\ts_cmp_lg_u32 %1, 0\n\ts_cbranch_scc1 Lo%=\nLp%=:"
                 : "+s"(hi), "+s"(lo) :: "scc", "memory");
}
typedef __attribute__((address_space(3))) u32* lds_u32p;
typedef __attribute__((address_space(3))) volatile u32* lds_vu32p;
typedef __attribute__((address_space(3))) u16* lds_u16p;
typedef __attribute__((address_space(3))) u32x2* lds_u32x2p;
typedef __attribute__((address_space(3))) u32x4* lds_u32x4p;
__device__ __forceinline__ u32 rfl(u32 x) { return __builtin_amdgcn_readfirstlane(x); }
__device__ __forceinline__ bool has_sent(u32x4 v) { return (v[0] == SENT) | (v[1] == SENT) | (v[2] == SENT) | (v[3] == SENT); }
__device__ __forceinline__ u32 now32() { return (u32)__builtin_amdgcn_s_memtime(); }
__device__ __forceinline__ u32 step_tag(int s) { return (u32)(s + 1) * 0x10001u; }

__device__ __forceinline__ void mem0_image(const u16* __restrict__ mem0, char* smem, const int tid) {
    for (int c = tid; c < 640; c += 512) {
        u32x4 v = *(const u32x4*)(mem0 + (c & 7) * D_ + (c >> 3) * 8);
#pragma unroll
        for (int i = 0; i < 4; ++i) v[i] = v[i] == SENT ? ~0u : v[i];
        ((u32x4*)smem)[c + (c >= 576 ? 64 : 0)] = v;
    }
}
__device__ __forceinline__ u32 once_more(u32& left, u32 lbase, u32* errw) {
    if (__builtin_expect(left != 0, 1)) { --left; return 2u; }
    atomicOr(errw, 1u);
    const int lane = lane_id();
    for (int k = 0; k < 11; ++k) *(lds_u32x4p)(lbase + (u32)(k * 1024 + lane * 16)) = (u32x4){0u, 0u, 0u, 0u};
    asm volatile("s_waitcnt lgkmcnt(0)" ::: "memory");
    return 0u;
}
constexpr u32 TS_MISS = 0x10000u;
__device__ __forceinline__ u32 read_ts(rsrc_t rs, u32 off, int s, u32& m) {
    const int lane = lane_id();
    const u32 vo = lane < 8 ? (u32)lane * 128u : OOB, base = (u32)__builtin_amdgcn_s_memrealtime() * 24u - 0x100000u;
    u32x4 t;
#pragma unroll 1
    for (u32 spins = 1;; ++spins) {
        CFENCE();
        t = ldx4(rs, vo, off);
        m = (u32)__ballot(t[3] == (u32)s + 1u);
        if (((m | (m >> 4)) & 15u) == 15u) break;
        m = 0;
        if (spins >= 0x4000u) break;
        __builtin_amdgcn_s_sleep(4);
    }
    u32 v = t[3] == (u32)s + 1u ? t[1] - base : 0u;
    v = max(v, (u32)__builtin_amdgcn_update_dpp(0u, v, 0xb1, 0xf, 0xf, false));
    v = max(v, (u32)__builtin_amdgcn_update_dpp(0u, v, 0x4e, 0xf, 0xf, false));
    v = max(v, (u32)__builtin_amdgcn_update_dpp(0u, v, 0x141, 0xf, 0xf, false));
    return to_sgpr(v + base);
}
__device__ __forceinline__ u32 read_ts_late(rsrc_t rs, u32x4 srd, u32 off, int s, u32& m, u32 gap = 1024) {
    u32 t;
    ts_look(t, m, srd, off, (u32)s + 1u);
    if (m) return t;
    nap(gap);
    return read_ts(rs, off, s, m);
}
__device__ __forceinline__ void until(u32 t) { for (int q = 0; q < 256 && (int)(t - (u32)__builtin_amdgcn_s_memrealtime() * 24u) > 0; ++q) __builtin_amdgcn_s_sleep(1); }
__device__ __forceinline__ u32 umin3(u32 a, u32 b, u32 c) { return min(min(a, b), c); }
__device__ __forceinline__ u32 dist(u32 d, u32x4 v, u32 k) { return umin3(umin3(d, v[0] ^ k, v[1] ^ k), v[2] ^ k, v[3] ^ k); }
#define COUNT(i) do { } while (0)
#define COUNT0() do { } while (0)
#define COUNT1(i) do { } while (0)
__device__ __forceinline__ void touch(u32x4 srd, u32 wp, int s, u32 lds) {
    u32 sv;
    const u32 off = slot_off(wp, s) + 3u * 32768u + ((~((wp >> 3) ^ ((u32)s >> 5)) & 1u) << 18);
    asm volatile("s_mov_b32 %0, m0\n\ts_mov_b32 m0, %3\n\ts_nop 3\n\tbuffer_load_dword off, %1, %2 lds\n\ts_mov_b32 m0, %0" : "=&s"(sv) : "s"(srd), "s"(off), "s"(lds) : "memory");
}
__device__ __forceinline__ void track_rate(u32 c0, u32 r0, u32& pc, u32& pr, u32& kr) {
    const u32 dr = r0 - pr;
    if (dr >= 6000u) {
        if (dr < 40000u) kr = to_sgpr(min(max((u32)(4096.f * (float)(c0 - pc) * __builtin_amdgcn_rcpf((float)dr) + 0.5f), 2560u), 4352u));
        pc = c0; pr = r0;
    }
}

#define LANES() const int lane = lane_id(), n = lane & 15, g = lane >> 4; (void)n; (void)g
constexpr u32 L_Q = 12288, L_K = 18432, L_E = 24576, L_DR = 25088, L_MSK = 25600, L_KQ = 32768;
constexpr float LIM_X = 32.f, LIM_D = 1.0995116e12f;
__device__ __forceinline__ u32 kq_buf(int sg) { return L_KQ + (u32)(sg % 3) * 16384u; }

__device__ __forceinline__ u32 p_pos(int row, int g, int dg) { return (u32)((row >> 1) * 64 + (g & 1) * 32 + (row & 1) * 16 + dg * 8 + (g >> 1) * 4); }
__device__ __forceinline__ void s_slow(const int h, const int s, rsrc_t rs, u32 so, u32 lbase) {
    const int lane = lane_id(), row = lane & 7, j = lane >> 3;
    const float ce = 0.08838834764831845f * 1.4426950408889634f;
    float sc[5] = {0.f, 0.f, 0.f, 0.f, 0.f};
    const u32 kx = lbase + kq_buf(s) + (u32)((j >> 2) * 4096 + (j & 3) * 64);
#pragma unroll 1
    for (int e = 0; e < DH_; ++e) {
        const u32 qa = lbase + L_Q + (u32)((e >> 4) * 768 + (row >> 2) * 192 + (e & 15) * 8 + (row & 3) * 2);
        const float q = bf2f(*(lds_u16p)qa) + bf2f(*(lds_u16p)(qa + 384));
        const u32 ka = kx + (u32)((e >> 5) * 1024 + ((e >> 2) & 3) * 256 + ((e >> 4) & 1) * 8 + (e & 3) * 2);
#pragma unroll
        for (int i = 0; i < 4; ++i) sc[i] += q * bf2f(*(lds_u16p)(ka + i * 16));
        sc[4] += q * bf2f(*(lds_u16p)(lbase + L_K + (u32)((e >> 4) * 768 + (j >> 1) * 192 + (e & 15) * 8 + (j & 1) * 2)));
    }
    sc[4] *= ce;
    const u32x2 ev = *(lds_u32x2p)(lbase + L_E + (u32)((j >> 1) * 128 + row * 16 + (j & 1) * 8));
    float se = (bf2f((u16)ev[0]) + bf2f((u16)(ev[0] >> 16))) + (bf2f((u16)ev[1]) + bf2f((u16)(ev[1] >> 16)));
    float mx = fmaxf(fmaxf(fmaxf(sc[0], sc[1]), fmaxf(sc[2], sc[3])), sc[4]);
#pragma unroll
    for (int o = 8; o < 64; o <<= 1) { se += __shfl_xor(se, o); mx = fmaxf(mx, __shfl_xor(mx, o)); }
    const bool rew = ev[0] == ~0u || ev[1] == ~0u;
    u64 rw = __ballot(rew);
    rw |= rw >> 32; rw |= rw >> 16; rw |= rw >> 8;
    const bool again = (rw >> row) & 1u;
    float al = exp2f(-mx);
    if (again) {
        const u32x2 en = {pack2(exp2f(sc[0] - mx), exp2f(sc[1] - mx)), pack2(exp2f(sc[2] - mx), exp2f(sc[3] - mx))};
        se = (bf2f((u16)en[0]) + bf2f((u16)(en[0] >> 16))) + (bf2f((u16)en[1]) + bf2f((u16)(en[1] >> 16)));
        al = 1.f;
    }
    float sn = se;
#pragma unroll
    for (int o = 8; o < 64; o <<= 1) sn += __shfl_xor(sn, o);
    if (again) se = sn;
    const float em = exp2f(sc[4] - mx);
    float sm = em;
#pragma unroll
    for (int o = 8; o < 64; o <<= 1) sm += __shfl_xor(sm, o);
    const float rd = 1.f / (sm + al * se);
    if (rw & 255u) {
#pragma unroll 1
        for (u32 spins = 0; spins < SPIN_CAP && !__all(*(lds_vu32p)(lbase + L_DR + (u32)(lane & 1) * 4) == step_tag(s)); ++spins) __builtin_amdgcn_s_sleep(1);
        const u32x2 en = {pack2(exp2f(sc[0] - mx), exp2f(sc[1] - mx)), pack2(exp2f(sc[2] - mx), exp2f(sc[3] - mx))};
        const u32 eo = again ? (u32)((h >> 3) * 32768 + ((h >> 1) & 3) * 1024 + (j >> 1) * 256 + (h & 1) * 128 + row * 16 + (j & 1) * 8) : OOB;
#pragma unroll
        for (int c = 0; c < NCP; ++c) stx2(rs, eo, en, so + cpy(c));
    }
    DRAIN();
    const u32 pm = pack2(em * rd, __shfl_xor(em * rd, 8));
    const int g = j >> 1;
    u32 dg = pack2(row == 2 * g ? al * rd : 0.f, row == 2 * g + 1 ? al * rd : 0.f);
    if (again && (row >> 1) == (g ^ 1)) dg = MARK;
    const int gj = h >> 2;
    const u32 po = SL_P + (u32)((gj >> 2) * 32768 + (gj & 1) * 2048 + ((gj >> 1) & 1) * 1024 + (h & 3) * 256) + p_pos(row, g, j & 1);
    const bool done = ldx1(rs, po, so) != SENT;
#pragma unroll
    for (int c = 0; c < NCP; ++c) stx1(rs, done ? OOB : po, (j & 1) ? dg : pm, so + cpy(c));
}

__device__ __forceinline__ void s_role(const int wave, const int h, const int xg, const u64 tag, const u16* __restrict__ mem0, const u32x32 (&W)[5],
                                       char* __restrict__ ws, char* smem) {
    const int lane = lane_id(), tid = wave * 64 + lane, n = lane & 15, g = lane >> 4;
    u32* errw = (u32*)(ws + OFF_ERR);
    const rsrc_t rs = mk_rsrc(ws);
    const u32x4 srd = mk_srd(ws);
    const u32 lbase = (u32)(uintptr_t)(__attribute__((address_space(3))) char*)smem;
    TLOAD();
    int wslot = 0; const int wid = 128 + h;
    WSTAMP(2);
    const u32 wp = wait_init(rs, ws, tag, lane);
    mem0_image(mem0, smem, tid);
    if (tid < 448) *(u32x4*)(smem + L_K + tid * 16) = (u32x4){0u, 0u, 0u, 0u};
    const u32 sidx = (n & 2) ? 0xEEEE : 0x4444;
    const u32 va = lbase + (u32)(g * 256 + (n & 2) * 64 + (n >> 2) * 32 + (n & 1) * 16);
    const u32 qw = lbase + L_Q + (u32)(wave * 768 + (g >> 1) * 192 + n * 8 + (g & 1) * 4);
    const u32 kw = lbase + L_K + (u32)(wave * 768 + g * 192 + n * 8);
    const u32 qtr = lbase + L_Q + (u32)((lane & 3) * 192 + (lane >> 2) * 8);
    const float ce = 0.08838834764831845f * 1.4426950408889634f;
    STAMPS();
    COUNT0();
#define BAR0() asm volatile("s_waitcnt vmcnt(0) lgkmcnt(0)\n\ts_barrier" ::: "memory")
#define S_LAST() do { u32 redo; do { redo = 0; s_l(W, redo, va, sidx, qw, kw, step_tag(NSEG - 1)); } while (__builtin_expect(redo, 0)); } while (0)

    if (wave < 2) {
        LANES();
        const u32 kxa = lbase + (u32)(wave * 4096 + lane * 16);
        const u32 ep = (u32)((n & 7) * 16 + (g & 1) * 8 + (n >> 3) * 4);
        const u32 eo = (u32)((h >> 3) * 32768 + ((h >> 1) & 3) * 1024 + (2 * wave + (g >> 1)) * 256 + (h & 1) * 128) + ep;
        const u32 ew = lbase + L_E + (u32)((2 * wave + (g >> 1)) * 128) + ep;
        const u32 ts = slot_v(wp, lane) + put_off(wp, 2u, lane), tk = kq_buf(lane);
        LBAR();
#pragma unroll 1
        for (int s = 0; s + 1 < NSEG; ++s) {
            u32 redo;
            BAR0();
            do { redo = 0; s_e(W, redo, va, sidx, qw, kw, step_tag(s), kxa + tab(tk, s), qtr, eo, ew, LIM_X, srd, tab(ts, s) STA); } while (__builtin_expect(redo, 0));
            nap(256);
            DRAIN();
            *(lds_u32p)(lbase + L_DR + (u32)wave * 4) = step_tag(s);
            FLUSH();
        }
        BAR0();
        S_LAST();
    } else if (wave == 2) {
        LANES();
        const int gj = h >> 2;
        const u32 po = SL_P + (u32)((gj >> 2) * 32768 + (gj & 1) * 2048 + ((gj >> 1) & 1) * 1024 + (h & 3) * 256) + p_pos(n & 7, g, n >> 3);
        const u32 er = lbase + L_E + (u32)(g * 128 + (n & 7) * 16);
        const u32x4 ones = {0x3f803f80u, 0x3f803f80u, 0x3f803f80u, 0x3f803f80u};
        const u32 ts = slot_v(wp, lane) + put_off(wp, 2u, lane);
        LBAR();
#pragma unroll 1
        for (int s = 0; s + 1 < NSEG; ++s) {
            const u32 so = tab(ts, s);
            u32 redo, bad;
            BAR0();
            do {
                redo = 0;
                s_f(W, redo, va, sidx, qw, kw, step_tag(s), bad, ones, qtr, er, po, n < 8 ? 1.f : 0.f, n == 8 + 2 * g ? 1.f : 0.f, n == 9 + 2 * g ? 1.f : 0.f, srd, so, ce, LIM_D,
                    0xccccccccccccccccull STA);
            } while (__builtin_expect(redo, 0));
            if (__builtin_expect(bad, 0)) s_slow(h, s, rs, so, lbase);
            FLUSH();
        }
        BAR0();
        S_LAST();
    } else if (wave == 3) {
        LANES();
        const u32 vm = (u32)lane * 16, cp = (u32)xg ^ (wp >> 1);
        LBAR();
        u32 c0, r0, kr = 3950;
        calib(c0, r0);
        const u32 tc = slot_v(wp ^ 8u, lane) + copy_off(cp, lane);
        u32 pc = c0, pr = r0, tg = 0, sc = 0, left = SPIN_CAP;
        BAR0();
#pragma unroll 1
        for (int s = 0; s + 1 < NSEG; ++s) {
            const u32 so = sc;
            sc = tab(tc, s);
            u32 redo = 0, poll = ((u32)s + 63u) >> 6;
            u32 t1 = 0, tm = (u32)s + 1u;
            for (;;) {
                s_m(W, redo, va, sidx, qw, kw, step_tag(s), left, c0, r0, t1, tm, tg, kr, vm, so, lbase, srd, poll, sc + SL_TS STA);
                if (__builtin_expect(left != ~0u, 1)) break;
                left = 0;
                once_more(left, lbase, errw);
                poll = 0;
            }
            if (redo) COUNT(0);
            tg = t1;
            if (__builtin_expect(!tm, 0)) tg = read_ts(rs, sc + SL_TS, s, tm);
            track_rate(c0, r0, pc, pr, kr);
            FLUSH();
        }
        const u32 so = sc;
        for (u32 poll = 1;;) {
            if (poll) gather(tg, kr, vm, so, lbase, srd);
            if (poll == 1) LBAR();
            DRAIN();
            u32 redo = 0;
            s_l(W, redo, va, sidx, qw, kw, step_tag(NSEG - 1));
            if (__builtin_expect(!redo, 1)) break;
            poll = once_more(left, lbase, errw);
        }
    } else if (wave < 6) {
        LANES();
        const int nt = wave & 1;
        f32x4* msk = (f32x4*)(smem + L_MSK) + nt * 128 + lane;
#pragma unroll
        for (int q = 0; q < 2; ++q) {
            f32x4 m;
#pragma unroll
            for (int i = 0; i < 4; ++i) m[i] = q * 16 + 4 * g + i <= nt * 16 + n ? 0.f : -__builtin_huge_valf();
            msk[q * 64] = m;
        }
        const u32 vl = lbase + (u32)lane * 16;
        const u32 o0 = (u32)OFF_PX + (u32)((h * 2 + nt) * 1024 + lane * 16);
        const u32 o1 = (u32)OFF_PXM + (u32)(((h >> 2) * 2 + nt) * 1024 + ((h & 3) * 16 + n) * 16 + g * 4);
        u32x4 p0 = {0u, 0u, 0u, 0u};
        u32 p1 = 0;
        LBAR();
#pragma unroll 1
        for (int s = 0; s < NSEG; ++s) {
            const u32 off = s ? 0u : OOB;
            BAR0();
            stx4(rs, (o0 + (u32)s * 65536 - 65536) | off, p0);
            stx1(rs, (o1 + (u32)s * 16384 - 16384) | off, p1);
            if (s + 1 < NSEG) {
                u32 redo;
                do { redo = 0; s_p1(W, redo, va, sidx, qw, kw, step_tag(s) STA); } while (__builtin_expect(redo, 0));
                nap(128);
            } else {
                S_LAST();
                DRAIN();
                stx1(rs, lane == 0 ? (u32)(OFF_FP + (h >> 2) * 128 + ((h & 3) * 2 + nt) * 4) : OOB, 1u);
            }
            px_step(p0, p1, vl + kq_buf(s), vl + kq_buf(s) + 8192 + (u32)nt * 4096, vl + L_MSK + (u32)nt * 2048, qtr + (L_K - L_Q), ce);
            STAMP(3);
            FLUSH();
        }
        stx4(rs, o0 + (NSEG - 1) * 65536, p0);
        stx1(rs, o1 + (NSEG - 1) * 16384, p1);
        DRAIN();
        stx1(rs, lane == 0 ? (u32)(OFF_FZ + ((h >> 2) * 2 + nt) * 128 + (h & 3) * 4) : OOB, 1u);
    } else if (wave == 6) {
        LANES();
        u32 cap = SPIN_CAP;
        u32 have = 0;
        auto seen = [&](u32x4 f) {
            u32 a = min(2 * rfl(f[0]), 2 * rfl(f[1]) + 1);
            if (h < 16) a = min(a, max(rfl(f[2]), 3u));
            have = max(have, a);
        };
        auto wait_qk = [&](int sg) {
            u32 spins = 0;
#pragma unroll 1
            for (; have <= (u32)sg && spins < cap; ++spins) {
                CFENCE();
                seen(ldx4(rs, 0u, (u32)(OFF_FA + h * 128)));
                if (spins) __builtin_amdgcn_s_sleep(2);
            }
            if (spins >= cap) { atomicOr(errw, 8u); cap = 1; }
            CFENCE();
        };
        auto fetch = [&](u32 src, u32 dst) {
            const char* p = ws + far_off(wp ^ 1u, src + (u32)h * 8192) + lane * 16;
#pragma unroll
            for (int i = 0; i < 8; ++i) dma16_sc1(p + i * 1024, lbase + dst + i * 1024);
        };
        *(lds_u32p)(lbase + 11520u + (u32)lane * 4) = 0u;
        wait_qk(0);
        fetch(NEAR_KX, L_KQ);
        fetch(NEAR_QX, L_KQ + 8192);
        wait_qk(1);
        XSTAMP(wid, 227);
        LBAR();
#pragma unroll 1
        for (int s = 0; s + 1 < NSEG; ++s) {
            s_fetch((u32)lane * 16, have <= (u32)s + 4u && lane < 4 ? (u32)(OFF_FA + h * 128 + lane * 4) : OOB, srd, far_off(wp ^ 1u, NEAR_KX + (u32)((s + 1) * H_ + h) * 8192),
                    lbase + kq_buf(s + 1), lbase + 11520u);
            u32 redo;
            do { redo = 0; s_p0(W, redo, va, sidx, qw, kw, step_tag(s) STA); } while (__builtin_expect(redo, 0));
            nap(320);
            FLUSH();
            DRAIN();
            seen(*(lds_u32x4p)(lbase + 11520u));
            if (s + 2 < NSEG) wait_qk(s + 2);
        }
        BAR0();
        S_LAST();
    } else {
        LANES();
        LBAR();
#pragma unroll 1
        for (int s = 0; s + 1 < NSEG; ++s) {
            u32 redo, pfd = lbase + kq_buf(s + 1) + 8192;
            const u32 pfo = far_off(wp ^ 1u, NEAR_QX + (u32)((s + 1) * H_ + h) * 8192);
            BAR0();
            touch(srd, wp, s + 1, lbase + 11264u);
            do {
                redo = 0;
                s_p2(W, redo, va, sidx, qw, kw, step_tag(s), (u32)lane * 16, srd, pfo, pfd STA);
                pfd = 0;
            } while (__builtin_expect(redo, 0));
            FLUSH();
        }
        BAR0();
        S_LAST();
    }
    COUNT1(0);
}

__device__ __forceinline__ void v_role(const int wave, const int h, const int xg, const u64 tag, const u16* __restrict__ mem0, const u32x32 (&W)[5],
                                       char* __restrict__ ws, char* smem) {
    const int lane = lane_id(), tid = wave * 64 + lane, n = lane & 15, g = lane >> 4;
    u32* errw = (u32*)(ws + OFF_ERR);
    const rsrc_t rs = mk_rsrc(ws);
    const u32x4 srd = mk_srd(ws);
    const u32 lbase = (u32)(uintptr_t)(__attribute__((address_space(3))) char*)smem;
    TLOAD();
    int wslot = 0; const int wid = 160 + h;
    WSTAMP(2);
    const u32 wp = wait_init(rs, ws, tag, lane);
    mem0_image(mem0, smem, tid);
    *(lds_u32p)(lbase + 14336) = 0u;
    *(lds_u32p)(lbase + 9216 + (u32)((tid & 256) * 8 + (tid & 255) * 4)) = 0u;
    const u32 sidx = n >= 8 ? 0xEEEE : 0x4444;
    asm volatile("s_waitcnt vmcnt(0) lgkmcnt(0)" ::: "memory");
    STAMPS();
    COUNT0();
    u32 rnd = 1;
    if (wave < 4) {
        const u32 va = lbase + (u32)(g * 128 + (n & 7) * 16), vw = lbase + 12288 + (u32)((wave * 32 + (g >> 1) * 16 + n) * 16 + (g & 1) * 8);
        if (wave) {
#pragma unroll 1
            for (int s = 0; s < NSEG; ++rnd) {
                v_a0(W, va, sidx, vw STA);
                FLUSH();
                s += rfl(*(lds_vu32p)(lbase + 14336)) != rnd;
                if (wave == 1 && s < NSEG) touch(srd, wp, s, lbase + 15360u);
            }
        } else {
            const u32 tc = slot_v(wp ^ 8u, lane) + copy_off((u32)xg ^ (wp >> 1), lane);
            u32 c0, r0, kr = 3950;
            calib(c0, r0);
            u32 pc = c0, pr = r0, tg = 0, sc = 0, cap = SPIN_CAP;
#pragma unroll 1
            for (int s = 0; s < NSEG; ++s) {
                const u32 so = sc;
                sc = tab(tc, s);
                u32 look = 0;
#pragma unroll 1
                do {
                    u32 left = cap;
                    v_a1(W, va, sidx, vw, left, c0, r0, tg + TV(3), kr, s ? min(look + 1u, 2u) : 0u, (u32)lane * 16, so, lbase, srd STA);
                    if (__builtin_expect(left == ~0u || look >= 8u, 0)) { atomicOr(errw, 4u); cap = 1; }
                    ++look;
                } while (__builtin_expect(rfl(*(lds_vu32p)(lbase + 14336)) == rnd++, 0));
                if (look > 1) COUNT(3);
                u32 tm;
                if (s + 1 < NSEG) tg = read_ts_late(rs, srd, sc + SL_TS, s, tm, 0u);
                track_rate(c0, r0, pc, pr, kr);
                FLUSH();
            }
        }
    } else {
        const u32 vr = lbase + 12288 + (u32)((g * 8 + (n >> 2)) * 16 + (n & 1) * 8), vc = lbase + (u32)(tid & 255) * 16;
        u32 z[3];
#pragma unroll
        for (int p = 0; p < 3; ++p) z[p] = p == 2 && (g & 1) ? OOB : z_off(wp, (u32)((p * 2 + (g & 1)) * 8 + (wave & 3) * 2 + (g >> 1))) + (u32)(h * 256 + n * 16);
#pragma unroll 1
        for (int s = 0; s < NSEG; ++s) {
            const u32 sz = slot_off(wp, s);
            u32 look = 0, redo;
#pragma unroll 1
            do {
                v_b(W, redo, sidx, vr, vc, z[0] + sz, z[1] + sz, z[2] + sz, srd, lbase, rnd, look < 8u ? rnd : 0u STA);
                ++look; ++rnd;
            } while (__builtin_expect(redo, 0));
            FLUSH();
        }
    }
    DRAIN();
    LBAR();
    stx1(rs, wave == 0 && lane_id() < 2 ? (u32)(OFF_FZ + ((h >> 2) * 2 + lane_id()) * 128 + 16 + (h & 3) * 4) : OOB, 1u);
    COUNT1(3);
}

constexpr u32 L_U = 8192, L_T = 16384;
__device__ __forceinline__ u32x4 sub4(const u32x32& v, const int i) { return (u32x4){v[i], v[i + 1], v[i + 2], v[i + 3]}; }
__device__ __forceinline__ void r_slow(const int s, rsrc_t rs, u32 so, u32 sz, u32 sm, u32 ra, u32 mo, u32 sidx, u32 lbase, u32& cap, u32* errw) {
    const int lane = lane_id(), n = lane & 15;
    u32 spins = 0;
    u32x4 p[8], z[8];
#pragma unroll 1
    for (; spins < cap; ++spins) {
        CFENCE();
        u32 d = 1;
#pragma unroll
        for (int j = 0; j < 8; ++j) p[j] = ldx4(rs, (u32)(lane * 16 + (j & 1) * 2048 + ((j >> 1) & 1) * 1024), so + (u32)(j >> 2) * 32768u);
#pragma unroll
        for (int j = 0; j < 8; ++j) z[j] = ldx4(rs, (u32)(lane * 16 + j * 1024), sz);
#pragma unroll
        for (int j = 0; j < 8; ++j) d = dist(dist(d, p[j], SENT), z[j], SENT);
        if (!__any(d == 0)) break;
    }
    if (spins >= cap) { atomicOr(errw, 2u); cap = 2; }
#pragma unroll
    for (int j = 0; j < 8; ++j) {
        *(lds_u32x4p)(lbase + (u32)(j * 1024 + lane * 16)) = z[j];
#pragma unroll
        for (int i = 0; i < 4; ++i) p[j][i] = p[j][i] == MARK ? 0u : p[j][i];
    }
    *(lds_vu32p)(lbase + L_T + 12) = step_tag(s);
    LBAR();
    LBAR();
    f32x4 acc = {0.f, 0.f, 0.f, 0.f};
#pragma unroll
    for (int j = 0; j < 8; ++j) acc = smf(p[j], *(lds_u32x4p)(ra + (u32)j * 1024), *(lds_u32x4p)(ra + (u32)j * 1024 + 512), acc, (int)sidx);
    const float x0 = acc[0] + acc[2], x1 = acc[1] + acc[3], o = __shfl_xor(n < 8 ? x1 : x0, 8);
    const u32 r = n < 8 ? pack2(x0, o) : pack2(o, x1);
#pragma unroll
    for (int c = 0; c < NCP; ++c) stx1(rs, mo, r, sm + cpy(c));
    LBAR();
}

__device__ __forceinline__ void r_role(const int wave, const int ct, const int xg, const u32 wp, char* __restrict__ ws, char* smem) {
    const int lane = lane_id(), tid = wave * 64 + lane, n = lane & 15, g = lane >> 4;
    u32* errw = (u32*)(ws + OFF_ERR);
    const rsrc_t rs = mk_rsrc(ws);
    const u32x4 srd = mk_srd(ws);
    const u32 lbase = (u32)(uintptr_t)(__attribute__((address_space(3))) char*)smem;
    if (tid < 6) *(u32*)(smem + L_T + 8 + tid * 4) = 0u;
    asm volatile("s_waitcnt lgkmcnt(0)" ::: "memory");
    int wslot = 0; const int wid = 192 + ct;
    WSTAMP(1);
    TLOAD();
    const u32 cp = (u32)xg ^ (wp >> 1);
    u32 cap = SPIN_CAP;
    STAMPS();
    LOGS();
    COUNT0();
    if (wave == 0) {
        const int kp = ct >= 36 ? 10 : ct >> 2;
        const u32 mb = (u32)((kp >> 2) * 32768 + (kp & 3) * 1024 + (ct & 3) * 256);
        const u32 mo = mb + (u32)(((n & 7) >> 2) * 128 + (2 * g + (n >> 3)) * 16 + (n & 3) * 4);
        const u32 ra = lbase + (u32)((g & 1) * L_U + (g >> 1) * 256 + (n & 7) * 32 + (n >> 3) * 16), sidx = (n & 2) ? 0xEEEE : 0x4444;
        const u32 te = slot_v(wp, lane) + copy_off(cp, lane) + SL_P, tm = slot_v(wp ^ 8u, lane) + put_off(wp, 0u, lane), tz = slot_z(wp, lane) + z_off(wp, (u32)ct);
#pragma unroll 1
        for (int s = 0; s + 1 < NSEG; ++s) {
            const u32 so = tab(te, s), sm = tab(tm, s), sz = tab(tz, s);
            u32 bad, pass;
            r_f(bad, pass, sidx, (u32)lane * 16, ra, mo, lbase + L_T + 16 + (u32)(lane & 3) * 4, srd, so, sz, sm, lbase, step_tag(s), cap, min(TV(8), 256u) STA);
            if (__builtin_expect(bad, 0)) r_slow(s, rs, so, sz, sm, ra, mo, sidx, lbase, cap, errw);
            if (pass) COUNT(2);
            touch(srd, wp, s + 1, lbase + 17408u);
            LOG();
        }
    } else if (wave == 4) {
        u32 c0, r0, kr = 3950;
        calib(c0, r0);
        const u32 tc = slot_v(wp ^ 8u, lane) + copy_off(cp, lane) + SL_TS;
        u32 pc = c0, pr = r0, tg = r0 - 0x10000u, t2 = 0, dn = 0;
#pragma unroll 1
        for (int s = 0; s + 1 < NSEG; ++s) {
            u32 t0 = tg + (t2 & 0xffffu), t1 = t0 + dn, ok = 0;
            until(tg + TV(6));
            if (s) r_t0(c0, r0, t0, t1, t2, ok, kr, (u32)s + 1u, lbase, srd, tab(tc, s) STA);
            if (__builtin_expect(!ok, 0)) {
                until(tg + TV(6) + TV(7) - 256u);
                r_t1(c0, r0, t0, t1, t2, lane < 8 ? (u32)lane * 128u : OOB, kr, (u32)s + 1u, lbase, srd, tab(tc, s) STA);
            }
            dn = t1 - t0;
            if (__builtin_expect(ok == 2u, 0)) t1 = read_ts(rs, tab(tc, s), s, ok);
            tg = t1;
            if (__builtin_expect(rfl(*(lds_vu32p)(lbase + L_T + 12)) == step_tag(s), 0)) { LBAR(); LBAR(); }
            track_rate(c0, r0, pc, pr, kr);
            LOG();
        }
    } else {
        const int q = wave == 5 ? 3 : wave - 1;
        u32 have = 0;
        u32x32 bn;
        auto seen = [&](u32x2 f) {
            u32 a = min(2 * f[0], 2 * f[1] + 1);
            a = min(a, (u32)__builtin_amdgcn_update_dpp(0u, a, 0xb1, 0xf, 0xf, false));
            a = min(a, (u32)__builtin_amdgcn_update_dpp(0u, a, 0x4e, 0xf, 0xf, false));
            a = min(a, (u32)__builtin_amdgcn_update_dpp(0u, a, 0x124, 0xf, 0xf, false));
            have = max(have, rfl(a));
        };
        const u32 fo = (u32)(OFF_FB + (q * 8 + (lane & 7)) * 128);
        auto load_b = [&](int sg, u32x32& bx) {
            u32 spins = 0;
#pragma unroll 1
            for (; have <= (u32)sg && spins < cap; ++spins) {
                CFENCE();
                seen(ldx2(rs, fo));
                if (spins) __builtin_amdgcn_s_sleep(4);
            }
            if (spins >= cap) { atomicOr(errw, 16u); cap = 1; }
            CFENCE();
            const u32 bs = far_off(wp, FAR_BX + (u32)(sg * NRED + ct) * 32768 + (u32)q * 8192);
            u32x4 t[8];
#pragma unroll
            for (int i = 0; i < 8; ++i) t[i] = ldx4(rs, (u32)(lane * 16 + i * 1024), bs);
#pragma unroll
            for (int i = 0; i < 8; ++i)
#pragma unroll
                for (int j = 0; j < 4; ++j) bx[(i >> 1) * 8 + j * 2 + (i & 1)] = t[i][j];
        };
        const u32 eo = (u32)(q * 32768 + lane * 16), te = slot_v(wp, lane) + copy_off(cp, lane), sidx = n >= 8 ? 0xEEEE : 0x4444;
        const u32 uw = lbase + L_U + (u32)(q * 2048 + (g >> 1) * 256 + n * 16 + (g & 1) * 8);
        const u32 tb = far_v(wp, FAR_BX + (u32)(min(lane + 1, NSEG - 1) * NRED + ct) * 32768 + (u32)q * 8192);
        u32 s = 0, why = 2, left = SPIN_CAP;
#pragma unroll 1
        for (;;) {
            if (why & 1u) {
#pragma unroll
                for (int i = 0; i < 4; ++i) {
                    const f32x4 u = smf(ldx4(rs, eo + (u32)i * 1024, tab(te, (int)s - 1)), sub4(bn, i * 8), sub4(bn, i * 8 + 4), (f32x4){0.f, 0.f, 0.f, 0.f}, (int)sidx);
                    *(lds_u32x2p)(uw + (u32)i * 512) = (u32x2){pack2(u[0], u[1]), pack2(u[2], u[3])};
                }
                LBAR();
                LBAR();
            }
            if (s + 1 == NSEG) break;
            load_b((int)s, bn);
            have = to_sgpr(have); left = to_sgpr(left);
            r_u(bn, s, have, left, why, sidx, eo, uw, lbase + L_T + 16 + (u32)q * 4, (u32)lane * 16, fo, te, tb, srd, (u32)q, lbase STA LGA);
            s = to_sgpr(s); have = to_sgpr(have); left = to_sgpr(left); why = to_sgpr(why);
        }
        if (left == 0) atomicOr(errw, 4u);
    }
    COUNT1(wave ? 1 : 2);
    LOGOUT(32 + ct);
}

__device__ __forceinline__ void relay_role(const u32 wp, const int xg, char* __restrict__ ws) {
    const int lane = lane_id();
    const rsrc_t rs = mk_rsrc(ws);
    const u32 tc = slot_v(wp ^ 8u, lane) + copy_off((u32)xg ^ (wp >> 1), lane) + SL_TS;
#pragma unroll 1
    for (int k = 0; k + 1 < NSEG; ++k) {
        u32 t;
#pragma unroll 1
        for (u32 spins = 0;; ++spins) {
            u32 ok;
            t = read_ts(rs, tab(tc, k), k, ok);
            if (ok) break;
            if (spins >= 16u || ldx1(rs, (u32)OFF_ERR) != 0u) return;
        }
        if (k >= 3) stx1(rs, lane < 8 ? (u32)(OFF_RDY + lane * 128) : OOB, (u32)k - 2u);
        for (int q = 0; q < 64 && (int)((u32)__builtin_amdgcn_s_memrealtime() * 24u - t) < 2400; ++q) __builtin_amdgcn_s_sleep(2);
    }
}

__device__ __forceinline__ void lead_role(const u32 wp, const int xg, char* __restrict__ ws, char* smem) {
    const int lane = lane_id();
    u32* errw = (u32*)(ws + OFF_ERR);
    const rsrc_t rs = mk_rsrc(ws);
    const u32x4 srd = mk_srd(ws);
    TLOAD();
    STAMPS();
    const u32 tsw = lane < 4 ? SL_TS + (u32)((lane & 1) * 4096 + (lane >> 1) * 16384 + xg * 128) : OOB;
    const u32 tm = slot_v(wp ^ 8u, lane), tc = tm + copy_off((u32)xg ^ (wp >> 1), lane);
    {
        bool dead = false;
        wait_word(rs, (u32)(OFF_FA + (lane & 31) * 128 + (lane >> 5) * 4), true, 1u, dead, errw, 8u, lane);
    }
    XSTAMP(192 + xg, 227);
    u32 c0, r0, kr = 3950, ki = 265, bump = 0, hist = 0;
    calib(c0, r0);
    u32 pc = c0, pr = r0, tg = r0 + TV(13) - 512u, cap = SPIN_CAP;
#pragma unroll 1
    for (int s = 0; s + 1 < NSEG; ++s) {
        u32 sr = tsw + tab(tm, s);
        const u32 mg = bump + (s < EARLY_N ? TV(10) : 0u);
        const u32 de = (TV(0) * ki >> 8) + mg, dz = TV(1) * ki >> 8, per = de + dz + TV(8) + TV(2) + mg;
        u32 look = 0;
        DRAIN();
        if (s) {
            const u32 so = tab(tc, s - 1);
#pragma unroll 1
            for (;; ++look) {
                u64 tl;
                u32 miss;
                l_g(miss, c0, r0, tl, tg + TV(15), look ? 0u : kr, (u32)lane * 16, so, srd, (u32x4){tg + de, tg + per, de | (dz << 16), (u32)s + 1u}, look ? OOB : sr STA);
                if (look && (int)((u32)tl * 24u + TV(14) - tg) > 0) tg = (u32)tl * 24u + TV(14);
                if (__builtin_expect(!miss, 1)) break;
                if (!look) {
                    stx4(rs, sr, (u32x4){0u, 0u, 0u, (u32)s + 1u + TS_MISS});
                    sr += 512u;
                    tg += TV(7);
                }
                if (look >= cap) { atomicOr(errw, 4u); cap = 1; break; }
            }
        }
        if (!s || look) stx4(rs, sr, (u32x4){tg + de, tg + per, de | (dz << 16), (u32)s + 1u});
        nap(384);
        u32 m;
        tg = look ? read_ts(rs, tab(tc, s) + SL_TS, s, m) : read_ts_late(rs, srd, tab(tc, s) + SL_TS, s, m);
        touch(srd, wp, s + 1, (u32)(uintptr_t)(__attribute__((address_space(3))) char*)smem);
        track_rate(c0, r0, pc, pr, kr);
        hist = ((hist << 1) | (s >= BUMP_S0 && m != 15u ? 1u : 0u)) & BUMP_WIN;
        if (__builtin_popcount(hist) >= BUMP_N && bump < BUMP_MAX * TV(11)) { bump += TV(11); hist = 0; }
        if (pc == c0) ki = to_sgpr((u32)(1048576.f * __builtin_amdgcn_rcpf((float)kr) + 0.5f));
        FLUSH();
    }
}

__device__ __forceinline__ void fill_role(const u32 wp, const int fi, char* __restrict__ ws) {
    const int lane = lane_id();
    const rsrc_t rs = mk_rsrc(ws);
    TLOAD();
    const u32 oz = (u32)(fi * 65536 + lane * 16);
    nap(TV(4) * 64u);
#pragma unroll 1
    for (int s = 4; s < NSEG; ++s) {
        const u32 gap = to_sgpr(s > 7 ? TV(9) : 0u);
        const u32 un = 0x38u << ((~((wp >> 3) ^ ((u32)s >> 5)) & 1u) * 8u), a = oz + slot_off(wp, s);
        const u32 a0 = (un >> (2 * fi)) & 1u ? OOB : a, a1 = (un >> (2 * fi + 1)) & 1u ? OOB : a + 32768u;
#pragma unroll
        for (u32 i = 0; i < 32; ++i) {
            stx4(rs, (i < 16 ? a0 : a1) + ((i >> 3) & 1u) * 16384 + (i & 7) * 1024, (u32x4){SENT, SENT, SENT, SENT});
            if (i == 15) {
                asm volatile("s_waitcnt vmcnt(16)" ::: "memory");
                stx1(rs, lane == 0 ? (u32)(OFF_FA + fi * 128 + 8) : OOB, (u32)s - 1u);
            }
            if (i % FILLG == FILLG - 1) nap(gap);
        }
    }
    DRAIN();
    stx1(rs, lane == 0 ? (u32)(OFF_FA + fi * 128 + 8) : OOB, (u32)NSEG);
}

__device__ __forceinline__ int tile_of(const int xg, const int hl, const int i) {
    const u32 pieces = hl ? 0x85410u : 0x97632u;
    return CFG ? (int)((pieces >> ((i >> 1) * 4)) & 15u) * 4 + (i & 1) * 2 + (xg & 1) : (int)((pieces >> (i * 4)) & 15u) * 4 + xg;
}
__device__ __forceinline__ void warm_role(const u32 wp, const int xg, const int wi, char* __restrict__ ws, char* smem) {
    const int lane = lane_id();
    const rsrc_t rs = mk_rsrc(ws);
    const u32x4 srd = mk_srd(ws);
    const u32 lbase = (u32)(uintptr_t)(__attribute__((address_space(3))) char*)smem;
    TLOAD();
    u32 have = 0, ta = 0;
    int next = 1;
    const u32 tc = slot_v(wp ^ 8u, lane) + copy_off((u32)xg ^ (wp >> 1), lane) + SL_TS;
    auto spin = [&](u32 t) { for (int q = 0; q < 512 && (int)(t - (u32)__builtin_amdgcn_s_memrealtime() * 24u) > 0; ++q) ; };
#pragma unroll 1
    for (int s = 0; s + 2 < NSEG; ++s) {
        if (s) until(ta + 1400u);
        u32 tm;
        ta = read_ts_late(rs, srd, tab(tc, s), s, tm);
        if (have <= (u32)s + WAHEAD) {
            const u32x2 f = ldx2(rs, (u32)(OFF_FB + (lane & 31) * 128));
            u32 a = min(2 * f[0], 2 * f[1] + 1);
#pragma unroll
            for (int q = 1; q < 32; q <<= 1) a = min(a, (u32)__shfl_xor((int)a, q));
            have = rfl(a);
        }
        if (next < s + 2) next = s + 2;
        u32 tw = ta + TV(5) - 1024u;
#pragma unroll 1
        for (; next <= s + WAHEAD && next + 1 < NSEG && have > (u32)next; ++next)
#pragma unroll 1
            for (int q = 0; q < 10; ++q) {
                const u32 o = far_off(wp, FAR_BX + (u32)(next * NRED + q * 4 + xg) * 32768 + (u32)wi * 8192);
                spin(tw);
                tw += TV(12);
                u32 sv;
                asm volatile("s_mov_b32 %0, m0\n\ts_mov_b32 m0, %4\n\ts_nop 3\n\tbuffer_load_dword %1, %2, %3 offen sc1 lds\n\ts_mov_b32 m0, %0"
                             : "=&s"(sv) : "v"((u32)lane * 128), "s"(srd), "s"(o), "s"(lbase + 69632u) : "memory");
            }
    }
    DRAIN();
}

__device__ __forceinline__ void topk_role(const u64 tag, char* __restrict__ ws) {
    const int lane = lane_id();
    const rsrc_t rs = mk_rsrc(ws);
    wait_level(rs, tag, lane, 2);
    u32 v[32];
#pragma unroll
    for (int i = 0; i < 8; ++i) {
        const u32x4 t = ldx4(rs, (u32)lane * 16, (u32)(OFF_R + i * 1024));
#pragma unroll
        for (int j = 0; j < 4; ++j) v[i * 4 + j] = ((t[j] ^ ((u32)((int)t[j] >> 31) | 0x80000000u)) & 0xffff0000u) | (u32)(2047 - i * 256 - j - lane * 4);
    }
    u32 top = ~0u, res = 0;
#pragma unroll 1
    for (int c = 0; c < TOPC; ++c) {
        u32 d = ~0u;
#pragma unroll
        for (int i = 0; i < 32; ++i) d = min(d, top - v[i]);
        d = min(d, (u32)__builtin_amdgcn_update_dpp(0u, d, 0xb1, 0xf, 0xf, false));
        d = min(d, (u32)__builtin_amdgcn_update_dpp(0u, d, 0x4e, 0xf, 0xf, false));
        d = min(d, (u32)__builtin_amdgcn_update_dpp(0u, d, 0x124, 0xf, 0xf, false));
        d = min(d, (u32)__builtin_amdgcn_update_dpp(0u, d, 0x128, 0xf, 0xf, false));
        const u32 dm = min(min((u32)__builtin_amdgcn_readlane(d, 0), (u32)__builtin_amdgcn_readlane(d, 16)), min((u32)__builtin_amdgcn_readlane(d, 32), (u32)__builtin_amdgcn_readlane(d, 48)));
        top -= dm;
        if (lane == c) res = 2048u - (top & 2047u);
        top -= 1u;
    }
    stx1(rs, lane < TOPC ? (u32)(OFF_IDX + lane * 4) : OOB, res);
    XSTAMP(192, 224);
}

#define MOEV 0
#define MOE_LD(p) __builtin_nontemporal_load(p)
#define MOE_NT " nt"
template <int NK>
__device__ __forceinline__ void moe_ld(const u16* __restrict__ wp, const int ldw, const int nvalid, u32 (&r)[NK][8]) {
#pragma unroll
    for (int k = 0; k < NK; ++k)
#pragma unroll
        for (int i = 0; i < 8; ++i) r[k][i] = k < nvalid ? MOE_LD((const u32*)(wp + (size_t)(k * 32 + i) * ldw)) : 0u;
}
template <int NK>
__device__ __forceinline__ void moe_mma(const u32 (&r)[NK][8], const u32x4 (&af)[NK], f32x4& ce, f32x4& co) {
#pragma unroll
    for (int k = 0; k < NK; ++k) {
        u32x4 be, bo;
#pragma unroll
        for (int j = 0; j < 4; ++j) {
            be[j] = __builtin_amdgcn_perm(r[k][2 * j + 1], r[k][2 * j], 0x05040100u);
            bo[j] = __builtin_amdgcn_perm(r[k][2 * j + 1], r[k][2 * j], 0x07060302u);
        }
        ce = mfma16(af[k], be, ce);
        co = mfma16(af[k], bo, co);
    }
}
constexpr int NGU = FF_ / 32, NDN = D_ / 32;
__device__ __forceinline__ void moe_part(const int w, const int tid, const u16* __restrict__ wg, const u16* __restrict__ wu,
                                         const u16* __restrict__ wd, char* __restrict__ ws, char* smem) {
    const int wave = __builtin_amdgcn_readfirstlane(tid >> 6), lane = tid & 63;
    const int n = lane & 15, g = lane >> 4;
    u32* errw = (u32*)(ws + OFF_ERR);
    const rsrc_t rs = mk_rsrc(ws);
    bool dead = false;
    f32x4 (*red)[2][64] = (f32x4(*)[2][64])smem;
    const f32x4 z = {0.f, 0.f, 0.f, 0.f};
    wait_word(rs, (u32)(OFF_IDX + (n & 7) * 4), true, 1u, dead, errw, 16u, lane);
    const u32 row = (ldx1(rs, (u32)(OFF_IDX + (n & 7) * 4)) - 1u) & (S_ - 1);
    u32 r1[5][8], r2[7][8];
    const int kq = wave >> 1, dg = w - (64 - NDN), k0 = wave * 7, nk = wave == 7 ? 5 : 7;
    if (w < NGU) moe_ld<5>(((wave & 1) ? wu : wg) + (size_t)(kq * 160 + g * 8) * FF_ + w * 32 + n * 2, FF_, 5, r1);
    if (dg >= 0) moe_ld<7>(wd + (size_t)(k0 * 32 + g * 8) * D_ + dg * 32 + n * 2, D_, nk, r2);
    if (w < NGU) {
        u32x4 af[5];
#pragma unroll
        for (int k = 0; k < 5; ++k) af[k] = ldx4(rs, (u32)OFF_XN + (row * D_ + (u32)((kq * 5 + k) * 32 + g * 8)) * 2);
        f32x4 ce = z, co = z;
        moe_mma<5>(r1, af, ce, co);
        red[wave][0][lane] = ce; red[wave][1][lane] = co;
        __syncthreads();
        if (wave == 0 && g < 2) {
            f32x4 t[4];
#pragma unroll
            for (int q = 0; q < 4; ++q) t[q] = (red[q >> 1][q & 1][lane] + red[2 + (q >> 1)][q & 1][lane]) + (red[4 + (q >> 1)][q & 1][lane] + red[6 + (q >> 1)][q & 1][lane]);
#pragma unroll
            for (int i = 0; i < 4; ++i) {
                float res[2];
#pragma unroll
                for (int q = 0; q < 2; ++q) {
                    float gg = bfr(t[q][i]), uu = bfr(t[2 + q][i]);
                    float sg = bfr(gg / (1.0f + __expf(-gg)));
                    res[q] = sg * uu;
                }
                stx1(rs, (u32)(OFF_HB + ((size_t)(4 * g + i) * FF_ + w * 32 + n * 2) * 2), pack2(res[0], res[1]));
            }
        }
        if (wave == 0) {
            DRAIN();
            if (lane == 0) __hip_atomic_fetch_add((u32*)(ws + OFF_CNT), 1u, __ATOMIC_RELAXED, __HIP_MEMORY_SCOPE_AGENT);
        }
        __syncthreads();
    }
    if (dg >= 0) {
        const float rv = __uint_as_float(ldx1(rs, (u32)OFF_R + row * 4));
        wait_word(rs, (u32)OFF_CNT, true, (u32)NGU, dead, errw, 16u, lane);
        u32x4 af[7];
#pragma unroll
        for (int k = 0; k < 7; ++k) af[k] = ldx4(rs, (u32)(OFF_HB + ((size_t)(n & 7) * FF_ + (k0 + (k < nk ? k : 0)) * 32 + g * 8) * 2));
        f32x4 ye = z, yo = z;
        moe_mma<7>(r2, af, ye, yo);
        red[wave][0][lane] = ye; red[wave][1][lane] = yo;
        __syncthreads();
        if (wave == 0) {
            f32x4 t[2];
#pragma unroll
            for (int q = 0; q < 2; ++q)
                t[q] = ((red[0][q][lane] + red[1][q][lane]) + (red[2][q][lane] + red[3][q][lane])) + ((red[4][q][lane] + red[5][q][lane]) + (red[6][q][lane] + red[7][q][lane]));
            const float rs1 = bfr(1.0f / (1.0f + __expf(-rv)));
#pragma unroll
            for (int i = 0; i < 4; ++i) {
                const float rsg = __shfl(rs1, (4 * g + i) & 7);
                stx1(rs, g < 2 ? (u32)(OFF_UPD + ((size_t)(4 * g + i) * D_ + dg * 32 + n * 2) * 2) : OOB, pack2(bfr(t[0][i]) * rsg, bfr(t[1][i]) * rsg));
            }
            DRAIN();
            if (lane == 0) __hip_atomic_fetch_add((u32*)(ws + OFF_CNT + 256), 1u, __ATOMIC_RELAXED, __HIP_MEMORY_SCOPE_AGENT);
        }
        __syncthreads();
    }
}

__device__ __forceinline__ void moe_ahead(const int w, const int t, const u16* __restrict__ wg, const u16* __restrict__ wu, const u16* __restrict__ wd, u32 lds) {
    const int lane = lane_id(), t2 = w < NGU ? t - 20 : t;
    if (t2 >= (w >= 64 - NDN ? 27 : 0)) return;
    const u16* p = t2 < 0 ? (t < 10 ? wg : wu - 640 * FF_) + (size_t)(t * 64 + lane) * FF_ + w * 32 : wd + (size_t)(t2 * 64 + lane) * D_ + (w - (64 - NDN)) * 32;
    u32 sv;
    asm volatile("s_mov_b32 %0, m0\n\ts_mov_b32 m0, %2\n\ts_nop 0\n\tglobal_load_lds_dword %1, off" MOE_NT "\n\ts_mov_b32 m0, %0" : "=&s"(sv) : "v"(p), "s"(lds) : "memory");
}

#define LOOKST 1
constexpr int NBIG = (NSEG - 1) * 10, NTASK = NBIG + 80;

struct OutCtx { rsrc_t rs; const u16* x; u16* out; u32* errw; u32 wp; int wid; char* ws; };
template <int OT, int NT>
__device__ __forceinline__ void out_task(const OutCtx& cx, const int sg, const int c0, const int n0, const int tid, const int (&sel_idx)[TOPC], bool& adone, u32& rdy,
                                         bool& dead, char* redp, u32* nextp, u32* tctr) {
    const int wave = __builtin_amdgcn_readfirstlane(tid >> 6), lane = tid & 63, n = lane & 15, g = lane >> 4;
    const rsrc_t rs = cx.rs;
    if (!adone && rdy <= (u32)sg) {
        wait_word(rs, (u32)(OFF_FB + (wave * 4 + (lane & 3)) * 128 + (sg & 1) * 4), true, (u32)(sg >> 1) + 1, dead, cx.errw, 32u, lane);
        u32x2 f = ldx2(rs, (u32)(OFF_FB + (wave * 4 + (lane & 3)) * 128));
        adone = __all(f[0] >= NSEG / 2 && f[1] >= NSEG / 2);
    }
    u32x4 af[OT][5], bq[NT][5];
    const u32 bo = amap((u32)(wave * 4096 + lane * 16));
#pragma unroll
    for (int c = 0; c < OT; ++c) {
        const u32 bb = far_off(cx.wp, FAR_BX + (u32)(sg * 40 + c0 + c) * 32768);
#pragma unroll
        for (int hh = 0; hh < 4; ++hh) af[c][hh] = ldx4(rs, bo + hh * 1024, bb);
    }
    const int ti = (tid >> 6) & (OT * NT - 1);
    const int token = sg * SEGL + (n0 + (ti & (NT - 1))) * 16 + n;
    const int col = (c0 + ti / NT) * 16 + 4 * g;
    int sel = -1;
#pragma unroll
    for (int c = 0; c < TOPC; ++c) if (sel_idx[c] == token) sel = c;
    const u32x2 xv = *(const u32x2*)(cx.x + (size_t)token * D_ + col);
    const u32x2 nv = ldx2(rs, (u32)(OFF_XN + ((size_t)token * D_ + col) * 2));
    if (__any(sel >= 0)) wait_word(rs, (u32)(OFF_CNT + 256), true, (u32)NDN, dead, cx.errw, 256u, lane);
    const u32x2 uv = ldx2(rs, sel >= 0 ? (u32)(OFF_UPD + ((size_t)sel * D_ + col) * 2) : OOB);
    __builtin_amdgcn_sched_barrier(0);
    const bool late = sg + 4 >= NSEG;
    if (late) wait_flags(rs, (u32)((OT == 1 ? OFF_FZ + (wave * 2 + n0) * 128 : OFF_FP + wave * 128) + (lane & 1) * 16), true, 1u, dead, cx.errw, 64u, lane);
    else if (rdy <= (u32)sg) {
#pragma unroll 1
        for (u32 spins = 0; !dead; ++spins) {
            CFENCE();
            rdy = __builtin_amdgcn_readfirstlane(ldx1(rs, (u32)(OFF_RDY + (cx.wid & 7) * 128)));
            if (rdy > (u32)sg) break;
            __builtin_amdgcn_s_sleep(8);
            if (spins > SPIN_CAP) { if (lane == 0) atomicOr(cx.errw, 64u); dead = true; }
            if ((spins & 63) == 63 && ldx1(rs, (u32)OFF_ERR) != 0u) dead = true;
        }
        CFENCE();
    }
#pragma unroll
    for (int c = 0; c < OT; ++c) af[c][4] = ldx4(rs, (u32)(wave * 64 + lane) * 16, slot_off(cx.wp, sg) + z_off(cx.wp, (u32)(c0 + c)));
#pragma unroll
    for (int nt = 0; nt < NT; ++nt) {
#pragma unroll
        for (int hh = 0; hh < 4; ++hh) bq[nt][hh] = ldx4(rs, (u32)lane * 16, (u32)OFF_PX + (u32)((sg * H_ + wave * 4 + hh) * 2 + n0 + nt) * 1024);
        bq[nt][4] = ldx4(rs, (u32)lane * 16, (u32)OFF_PXM + (u32)((sg * 8 + wave) * 2 + n0 + nt) * 1024);
    }
    CFENCE();
    u32 next = 0;
    if (tid == 0) next = !late ? __hip_atomic_fetch_add(tctr, 1u, __ATOMIC_RELAXED, __HIP_MEMORY_SCOPE_AGENT)
                                 : __hip_atomic_load(tctr, __ATOMIC_RELAXED, __HIP_MEMORY_SCOPE_AGENT);
    f32x4 (*red)[OT * NT][64] = (f32x4(*)[OT * NT][64])redp;
#pragma unroll
    for (int c = 0; c < OT; ++c)
#pragma unroll
        for (int nt = 0; nt < NT; ++nt) {
            f32x4 acc = {0.f, 0.f, 0.f, 0.f};
#pragma unroll
            for (int k = 0; k < 5; ++k) acc = mfma16(af[c][k], bq[nt][k], acc);
            red[wave][c * NT + nt][lane] = acc;
        }
    if (tid == 0) {
        if (late && next < (u32)NTASK) next = __hip_atomic_fetch_add(tctr, 1u, __ATOMIC_RELAXED, __HIP_MEMORY_SCOPE_AGENT);
        *nextp = next;
    }
    LBAR();
    if (tid < OT * NT * 64) {
        f32x4 sum = red[0][ti][lane];
#pragma unroll
        for (int wv = 1; wv < 8; ++wv) sum += red[wv][ti][lane];
        float res[4];
#pragma unroll
        for (int q = 0; q < 4; ++q) {
            float xx = bf2f((u16)((xv[q >> 1] >> ((q & 1) * 16)) & 0xffff));
            float nn = bf2f((u16)((nv[q >> 1] >> ((q & 1) * 16)) & 0xffff));
            float bo2 = nn;
            if (sel >= 0) bo2 = bfr(nn + bf2f((u16)((uv[q >> 1] >> ((q & 1) * 16)) & 0xffff)));
            float gg = bfr(bo2 / (1.0f + __expf(-bo2)));
            float sv = bfr(sum[q]);
            res[q] = bfr(xx + bfr(sv * gg));
        }
        *(u32x2*)(cx.out + (size_t)token * D_ + col) = (u32x2){pack2(res[0], res[1]), pack2(res[2], res[3])};
    }
}

constexpr u32 L_VS = 40960;
__device__ __forceinline__ u32 xbuf(int b) { return b == 0 ? 65536u : (b == 1 ? 106496u : 0u); }
__device__ __forceinline__ void worker_role(const int wave, const int wid, const u64 tag, const u32 wp, const u16* __restrict__ x, const u32x32 (&W)[5],
                                            const u16* __restrict__ wg, const u16* __restrict__ wu,
                                            const u16* __restrict__ wd, char* __restrict__ ws, u16* __restrict__ out, char* smem) {
    const int lane = lane_id(), tid = wave * 64 + lane;
    const int n = lane & 15, g = lane >> 4;
    const int par = (wid >> 2) & 1, kind = (wid >> 3) & 1, h = (wid & 3) * 8 + (wid >> 4), type = kind * 2 + par;
    u32* errw = (u32*)(ws + OFF_ERR);
    const rsrc_t rs = mk_rsrc(ws);
    bool dead = false;
    const bool isqk = type < 2;
    int wslot = 1;
    u32* sh = (u32*)(smem + L_MISC);

    {
        const u32 lbase = (u32)(uintptr_t)(__attribute__((address_space(3))) char*)smem;
        const u32x4 srd = mk_srd(ws);
        const u32 vl = lbase + (u32)lane * 16, xp = (u32)(((wave >> 1) & 1) * 20480 + (wave < 4 ? 4096 + (wave & 1) * 8192 : (wave & 1) * 2048)), xv = xp + (u32)lane * 16, ca = lbase + L_MISC + 64;
        const u32 fo = (u32)(OFF_FA + kind * 4096 + h * 128 + par * 4);
        auto xsrc = [&](int it) { return it < NSEG / 2 ? (u32)OFF_XNP + (u32)(it * 2 + par) * 40960u : (u32)OFF_XNP; };
        auto now = [&](u32 v) {
            DRAIN();
            if (lane_id() == 0 && __hip_atomic_fetch_add((u32*)(smem + L_MISC + 68), 1u, __ATOMIC_RELAXED, __HIP_MEMORY_SCOPE_WORKGROUP) == v * 4 - 1) { stx1(rs, fo, v); XSTAMP(wid, 224 + v); }
        };
        WSTAMP(4);
        STAMPS();
        u32 b0 = xbuf(0), b1 = xbuf(1), b2 = xbuf(2);
        auto rot = [&] { const u32 t = b0; b0 = b1; b1 = b2; b2 = t; };
        if (wave < 4) {
            const u32 oq = (u32)(wave * 64 + lane) * 16;
#pragma unroll 1
            for (int it = 0; it < NSEG / 2; ++it) {
                if (isqk) {
                    if (it == 4) wait_level(rs, tag, lane_id(), 2);
                    const u32 ob = far_off(wp ^ 1u, NEAR_QX + (u32)((it * 2 + par) * H_ + h) * 8192);
                    wx0(W, vl + b0, oq, srd, xv, xsrc(it + 2), lbase + b2 + xp, ob, ob + 4096 STA);
                } else wx1(W, vl + b0, lbase + L_VS + (u32)(it & 1) * 8192 + oq STA);
                rot();
                CFLUSH();
                if (it < 3 || (it & 7) == 7) WSTAMP(0x10 + it);
            }
            DRAIN();
            LBAR();
        } else if (isqk) {
            const u32 oq = (u32)(tid & 255) * 16;
            LBAR();
#pragma unroll 1
            for (int it = 0; it < NSEG / 2; ++it) {
                const int sg = it * 2 + par;
                if (it == 4) { wait_level(rs, tag, lane_id(), 2); sh[18] = wait_init(rs, ws, tag, lane_id()); }
                const u32 ob = far_off(wp ^ 1u, NEAR_KX + (u32)(sg * H_ + h) * 8192);
                wk(W, vl + b0, oq, srd, ob, ob + 4096, 0.08838834764831845f * 1.4426950408889634f, xv, xsrc(it + 2), lbase + b2 + xp,
                   ca, (u32)it * 4 + 3, (u32)it, fo STA);
                rot();
                CFLUSH();
                if (it < NOWV) now((u32)it + 1);
                if (MOEV && it >= 6 && (wave & 2) == 0) moe_ahead(h * 2 + par, (it - 6) * 2 + (wave & 1), wg, wu, wd, lbase + L_MISC + 256);
            }
        } else {
            const u32 ob = amap((u32)(h * 1024 + lane * 16)), yp = 4096u + (u32)(wave & 1) * 6144u;
            LBAR();
#pragma unroll
            for (int i = 0; i < 10; ++i) {
                const u32 o = i < 2 ? (u32)i * 1024 : yp + (u32)(i - 2) * 1024;
                dma16_sc1(ws + xsrc(2) + xv + o, __builtin_amdgcn_readfirstlane(lbase + b2 + xp + o));
            }
            DRAIN();
            LBAR();
#pragma unroll 1
            for (int it = 1; it <= NSEG / 2; ++it) {
                rot();
                if (it == 4) { wait_level(rs, tag, lane_id(), 2); sh[18] = wait_init(rs, ws, tag, lane_id()); }
                wb(W, vl + L_VS + (u32)(~it & 1) * 8192, ob, srd, FAR_BX + (u32)((it * 2 - 2 + par) * NRED + (wave & 3) * 2) * 32768, wp, (u32)it,
                   xv, xsrc(it + 2), lbase + b2 + xp, xsrc(it + 2) + yp, lbase + b2 + xp + yp, ca, (u32)it * 4 - 1, (u32)it - 1, fo STA);
                CFLUSH();
                if (it <= NOWV) { now((u32)it); LBAR(); }
            }
        }
        DRAIN();
        LBAR();
        stx1(rs, wave == 0 && lane_id() == 0 ? fo : OOB, NSEG / 2);
    }
    WSTAMP(2);

    {
    const int lane = lane_id(), tid = wave * 64 + lane;
    if (isqk) moe_part(h * 2 + par, tid, wg, wu, wd, ws, smem);
    WSTAMP(5);
    u32* nextp = (u32*)(smem + L_MISC + 96);
    u32* tctr = (u32*)(ws + OFF_CNT + 512);
    if (tid == 0) nextp[0] = __hip_atomic_fetch_add(tctr, 1u, __ATOMIC_RELAXED, __HIP_MEMORY_SCOPE_AGENT);
    wait_flags(rs, (u32)OFF_IDX + (lane & 1) * 16, true, 1u, dead, errw, 256u, lane);
    __syncthreads();
    int sel_idx[TOPC];
    {
        u32x4 i0 = ldx4(rs, (u32)OFF_IDX), i1 = ldx4(rs, (u32)OFF_IDX + 16);
#pragma unroll
        for (int c = 0; c < 4; ++c) { sel_idx[c] = (int)i0[c] - 1; sel_idx[4 + c] = (int)i1[c] - 1; }
    }
    u32 order;
    {
        bool hit = false;
#pragma unroll
        for (int c = 0; c < TOPC; ++c) hit |= (sel_idx[c] >> 5) == lane;
        const u64 m = __ballot(hit && lane < 40), below = ((u64)1 << lane) - 1;
        const int pos = lane >= 40 ? lane : ((m >> lane) & 1 ? 40 - __popcll(m) + __popcll(m & below) : lane - __popcll(m & below));
        order = (u32)__builtin_amdgcn_ds_permute(pos * 4, lane);
    }
    WSTAMP(3);
    const OutCtx cx = {rs, x, out, errw, to_sgpr(sh[18]), wid, ws};
    bool adone = false;
    u32 rdy = 0;
#pragma unroll 1
    for (int tc = 0;; ++tc) {
        const int task = __builtin_amdgcn_readfirstlane(nextp[tc & 1]);
        if (task >= NTASK) break;
        char* redp = smem + (tc & 1) * 65536;
        if (task < NBIG) {
            WSTAMP(0x100 + task / 10);
            out_task<4, 2>(cx, (int)tab(order, task / 10), (task % 10) * 4, 0, tid, sel_idx, adone, rdy, dead, redp, nextp + ((tc + 1) & 1), tctr);
        } else {
            WSTAMP(0x100 + NSEG - 1);
            out_task<1, 1>(cx, NSEG - 1, (task - NBIG) >> 1, (task - NBIG) & 1, tid, sel_idx, adone, rdy, dead, redp, nextp + ((tc + 1) & 1), tctr);
        }
        WSTAMP(0x300);
        if (task == NTASK - 1) {
            wait_word(rs, (u32)(OFF_CNT + 768), true, 4u, dead, errw, 64u, lane);
            if (tid < 256) stx2(rs, (u32)(OFF_INI + tid * 8), (u32x2){~(u32)tag, ~cal_tag(tag, ~(u32)tag)});
        }
    }
    }
}

constexpr int FUSED_LDS = L_MISC + 1024;
#define ORD_A 0x143210
#define ORD_C 0x101234
#define KERNEL_SYM "fused_kernel"
extern "C" __global__ __launch_bounds__(512, 2) void fused_kernel(const u16* __restrict__ x, const u16* __restrict__ mem0, const u16* __restrict__ w_r,
                                                        const u16* __restrict__ Wq, const u16* __restrict__ Wk,
                                                        const u16* __restrict__ Wv, const u16* __restrict__ Wo,
                                                        const u16* __restrict__ wg, const u16* __restrict__ wu,
                                                        const u16* __restrict__ wd, char* __restrict__ ws, u16* __restrict__ out) {
    extern __shared__ __attribute__((aligned(16))) char smem[];
    const int wave = __builtin_amdgcn_readfirstlane(threadIdx.x >> 6);
    u64 gen = 0, tag = 0, old = 0;
    u32 xcc, se;
    asm volatile("s_getreg_b32 %0, hwreg(HW_REG_XCC_ID, 0, 3)" : "=s"(xcc));
    asm volatile("s_getreg_b32 %0, hwreg(HW_REG_HW_ID, 13, 2)" : "=s"(se));
    u32 bid = blockIdx.x;
    asm volatile("" : "+s"(bid));
    const bool first = (int)bid >= 0;
    u32 bq = first ? (bid & ~31u) + se * 8u + xcc : bid & 255u;
    asm volatile("" : "+s"(bq));
    const int b = (int)bq, k = b >> 3, xg = b & 3;
    if (threadIdx.x >= 448 && threadIdx.x < 453) {
        const int lane = threadIdx.x & 63;
        const u32 pg = (bid >> 3 & 31u) + 32u * (u32)lane;
        const char* tp = lane < 4 ? ws + min(pg << 21, (u32)WS_TOTAL - 4u) : (const char*)out + (size_t)(bid >> 3 & 3u) * 655360;
        u32 sv;
        asm volatile("s_mov_b32 %0, m0\n\ts_mov_b32 m0, %2\n\ts_nop 0\n\tglobal_load_lds_dword %1, off\n\ts_mov_b32 m0, %0"
                     : "=&s"(sv) : "v"(tp), "s"((u32)(uintptr_t)(__attribute__((address_space(3))) char*)smem + L_MISC + 256u) : "memory");
    }
    u64* claim = (u64*)(ws + OFF_CLAIM);
    u32* sh = (u32*)(smem + L_MISC);
    auto try_claim = [&] {
        if (wave == 0 && first) {
            asm volatile("s_load_dwordx2 %0, %1, 0x0\n\ts_waitcnt lgkmcnt(0)" : "=s"(gen) : "s"(ws + OFF_GEN) : "memory");
            tag = (gen + 0x9E3779B97F4A7C15ull) * 0xBF58476D1CE4E5B9ull;
            tag ^= tag >> 31;
            tag *= 0x94D049BB133111EBull;
            tag ^= tag >> 29;
            if (lane_id() == 0) {
                old = __hip_atomic_exchange(claim + b * 8, tag, __ATOMIC_RELAXED, __HIP_MEMORY_SCOPE_AGENT);
                sh[1] = (u32)tag; sh[2] = (u32)(tag >> 32); sh[3] = (u32)gen; sh[4] = (u32)(gen >> 32);
            }
        }
    };
    auto settle = [&] {
        const int tid = wave * 64 + lane_id();
        if (tid == 0 && first) {
            u32 vb = (u32)b;
#pragma unroll 1
            for (int i = 0; i < 256 && old == tag; ++i) {
                vb = (vb + 1) & 255u;
                old = __hip_atomic_exchange(claim + vb * 8, tag, __ATOMIC_RELAXED, __HIP_MEMORY_SCOPE_AGENT);
            }
            sh[0] = vb;
        }
        LBAR();
        const u32 bc = __builtin_amdgcn_readfirstlane(sh[0]);
        asm volatile("s_cmp_eq_u32 %0, %1\n\ts_cbranch_scc1 Lgo%=\n\t"
                     "s_or_b32 s2, %0, 0x80000000\n\ts_mov_b64 s[0:1], %2\n\tv_mov_b32 v0, %3\n\ts_waitcnt vmcnt(0) lgkmcnt(0)\n\t"
                     "s_getpc_b64 s[4:5]\nLpc%=:\n\ts_sub_u32 s4, s4, Lpc%=-" KERNEL_SYM "\n\ts_subb_u32 s5, s5, 0\n\ts_setpc_b64 s[4:5]\nLgo%=:"
                     :: "s"(bc), "s"(b), "s"(__builtin_amdgcn_kernarg_segment_ptr()), "v"(tid) : "s2", "scc", "memory");
        tag = (u64)(u32)__builtin_amdgcn_readfirstlane(sh[1]) | ((u64)(u32)__builtin_amdgcn_readfirstlane(sh[2]) << 32);
    };
    if (b & 4) {
        const int wid = k * 4 + xg, h = (wid & 3) * 8 + (wid >> 4), par = (wid >> 2) & 1;
        const bool isqk = ((wid >> 3) & 1) == 0;
        const int lane = lane_id();
        u32x32 W[5];
        u32 wp = 0;
        u32x2 f0, f1;
        u64 tg2 = 0;
        bool got = false;
        auto xtile = [&](int t) {
#pragma unroll
            for (int i = 0; i < 5; ++i)
                dma16_sc1(ws + OFF_XNP + (size_t)(par + 2 * t) * 40960 + (wave * 5 + i) * 1024 + lane * 16,
                          __builtin_amdgcn_readfirstlane((u32)(uintptr_t)(__attribute__((address_space(3))) char*)smem + xbuf(t) + (u32)(wave * 5 + i) * 1024));
        };
        load_weights<ORD_A>([&](int st) {
            if (st == 0) try_claim();
            if (st == LOOKST) {
                asm volatile("global_load_dwordx2 %0, %2, off sc1\n\tglobal_load_dwordx2 %1, %2, off offset:512 sc1"
                             : "=&v"(f0), "=&v"(f1) : "v"(ws + OFF_INI + lane * 8) : "memory");
                tg2 = (u64)(u32)__builtin_amdgcn_readfirstlane(sh[1]) | ((u64)(u32)__builtin_amdgcn_readfirstlane(sh[2]) << 32);
            }
            if (st == 3) {
                asm volatile("" : "+v"(f0), "+v"(f1));
                got = __all((f1[0] == (u32)tg2) & (f1[1] == (u32)(tg2 >> 32)));
                if (got) { xtile(0); xtile(1); }
                if (__all(f0[1] == cal_tag(tg2, f0[0]))) wp = wp_of(ws, f0[0]);
            }
        }, (isqk ? Wq : Wv) + h * DH_, isqk ? Wk + h * DH_ : Wo + (size_t)h * DH_ * D_, !isqk, smem, wave, lane,
           wave >> 2, 2 * (wave & 3), wave >> 2, 2 * (wave & 3) + 1, W);
        settle();
        const rsrc_t rs = mk_rsrc(ws);
        if (!got) { wait_level(rs, tag, lane, 1); xtile(0); xtile(1); }
        if (wp == 0) wp = wait_init(rs, ws, tag, lane, false);
        sh[16] = sh[17] = 0u;
        DRAIN();
        LBAR();
        PIN_W(W);
        worker_role(wave, wid, tag, wp, x, W, wg, wu, wd, ws, out, smem);
        __builtin_amdgcn_endpgm();
    }
    const int hl = ((k >> 1) ^ (xg >> 1)) & 1, j = (k >> 2) * 2 + (k & 1);
    if (CFG ? xg < 2 : j < 8) {
        const int h = CFG ? xg * 16 + (j >> 3) * 8 + hl * 4 + ((j >> 1) & 3) : xg * 8 + hl * 4 + (j >> 1);
        u32x32 W[5];
        auto hook = [&](int st) {
            if (st == 0) try_claim();
        };
        if ((j & 1) == 0) {
            load_weights<ORD_C>(hook, Wq + h * DH_, Wk + h * DH_, false, smem, wave, lane_id(), 0, wave, 1, wave, W);
            settle();
            PIN_W(W);
            s_role(wave, h, xg, tag, mem0, W, ws, smem);
            __builtin_amdgcn_endpgm();
        }
        load_weights<ORD_C | (1 << 21)>(hook, Wv + h * DH_, Wo + (size_t)h * DH_ * D_, true, smem, wave, lane_id(), wave >> 2, 2 * (wave & 3), wave >> 2, 2 * (wave & 3) + 1, W);
        settle();
        PIN_W(W);
        v_role(wave, h, xg, tag, mem0, W, ws, smem);
        __builtin_amdgcn_endpgm();
    }
    constexpr int NR = CFG ? 10 : 5, NS = CFG ? 6 : 3, NP = CFG ? 4 : 2;
    const int jr = CFG ? j : j - 8, hi = (CFG ? xg - 2 : xg) * 2 + hl;
    try_claim();
    init_role([&] { settle(); return tag; }, wave, jr < NR + NS - NP ? hi * (NR + NS - NP) + jr : 48 + hi * NP + jr - (NR + NS - NP), xcc, ((se ^ xcc) >> 1) & 1u, x, w_r, ws, smem);
    gen = (u64)sh[3] | ((u64)sh[4] << 32);
    tag = (u64)(u32)__builtin_amdgcn_readfirstlane(sh[1]) | ((u64)(u32)__builtin_amdgcn_readfirstlane(sh[2]) << 32);
    const int wi = hl * NS + jr - NR;
    const bool aux = wi == 2 * NS - 1;
    if (jr < NR) { if (wave < 6) r_role(wave, tile_of(xg, hl, jr), xg, wait_init(mk_rsrc(ws), ws, tag, lane_id()), ws, smem); }
    else if (wi == 0) { if (xg == 0 && wave == 7) topk_role(tag, ws); }
    else if (!aux) {
        if (wave >= 4) return;
        const u32 wp = wait_init(mk_rsrc(ws), ws, tag, lane_id());
        LBAR();
        if (wave == 2) fill_role(wp, xg * 4 + wi - 1, ws);
        if (wave == 3 && xg == 3 && wi == 4) relay_role(wp, xg, ws);
        if (wave == 1) warm_role(wp, xg, wi - 1, ws, smem);
    }
    else if (wave == 1) {
        const u32 wp = wait_init(mk_rsrc(ws), ws, tag, lane_id());
        if (lane_id() == 0) __hip_atomic_fetch_add((u32*)(ws + OFF_CNT + 768), 1u, __ATOMIC_RELAXED, __HIP_MEMORY_SCOPE_AGENT);
        lead_role(wp, xg, ws, smem);
    }
    else if (aux && xg == 2 && wave == 0) {
        const rsrc_t rs = mk_rsrc(ws);
        const int lane = lane_id();
#pragma unroll 1
        for (u32 spins = 0; spins < SPIN_CAP; ++spins) {
            CFENCE();
            bool ok = true;
#pragma unroll
            for (int q = 0; q < 4; ++q) {
                u32x2 f = ldx2(rs, (u32)(OFF_CLAIM + (q * 64 + lane) * 64));
                ok = ok && f[0] == (u32)tag && f[1] == (u32)(tag >> 32);
            }
            if (__all(ok)) break;
            __builtin_amdgcn_s_sleep(32);
        }
        if (lane == 0) __hip_atomic_store((u64*)(ws + OFF_GEN), gen + 1ull, __ATOMIC_RELAXED, __HIP_MEMORY_SCOPE_AGENT);
#pragma unroll
        for (int q = 0; q < 4; ++q) stx2(rs, (u32)(OFF_CLAIM + (q * 64 + lane) * 64), (u32x2){~(u32)tag, 0u});
    }
}

extern "C" void kernel_launch(void* const* d_in, const int* in_sizes, int n_in, void* d_out, int out_size,
                              void* d_ws, size_t ws_size, hipStream_t stream) {
    const u16* x = (const u16*)d_in[0];
    const u16* mem0 = (const u16*)d_in[1];
    const u16* Wq = (const u16*)d_in[2];
    const u16* Wk = (const u16*)d_in[3];
    const u16* Wv = (const u16*)d_in[4];
    const u16* Wo = (const u16*)d_in[5];
    const u16* w_r = (const u16*)d_in[8];
    const u16* w_gate = (const u16*)d_in[9];
    const u16* w_up = (const u16*)d_in[10];
    const u16* w_down = (const u16*)d_in[11];
    char* ws = (char*)d_ws;
    u16* out = (u16*)d_out;

    hipFuncSetAttribute((const void*)fused_kernel, hipFuncAttributeMaxDynamicSharedMemorySize, FUSED_LDS);
    fused_kernel<<<256, 512, FUSED_LDS, stream>>>(x, mem0, w_r, Wq, Wk, Wv, Wo, w_gate, w_up, w_down, ws, out);
}
```

```cpp
#define CFG 0
#include <hip/hip_runtime.h>
#include <hip/hip_bf16.h>
#include <stdint.h>

constexpr int S_ = 2048, D_ = 640, H_ = 32, DH_ = 128, NSEG = 64, SEGL = 32, FF_ = 1728, TOPC = 8;
constexpr int HD_ = H_ * DH_;
constexpr int NRED = 40;

typedef unsigned short u16;
typedef unsigned int u32;
typedef unsigned long long u64;
typedef __attribute__((ext_vector_type(8))) __bf16 bf16x8;
typedef __attribute__((ext_vector_type(16))) __bf16 bf16x16;
typedef __attribute__((ext_vector_type(4))) float f32x4;
typedef __attribute__((ext_vector_type(4))) unsigned int u32x4;
typedef __attribute__((ext_vector_type(8))) unsigned int u32x8;
typedef __attribute__((ext_vector_type(2))) unsigned int u32x2;

__device__ __forceinline__ float bf2f(u16 h) { return __uint_as_float(((u32)h) << 16); }
typedef __bf16 bf16x2_t __attribute__((ext_vector_type(2)));
typedef float f32x2_t __attribute__((ext_vector_type(2)));
__device__ __forceinline__ u16 f2bf(float f) {
    __bf16 h = (__bf16)f;
    return __builtin_bit_cast(u16, h);
}
__device__ __forceinline__ float bfr(float f) { return (float)(__bf16)f; }
__device__ __forceinline__ u32 pack2(float a, float b) {
    f32x2_t f = {a, b};
    bf16x2_t h = __builtin_convertvector(f, bf16x2_t);
    return __builtin_bit_cast(u32, h);
}
__device__ __forceinline__ bf16x8 as_bf(u32x4 v) { return __builtin_bit_cast(bf16x8, v); }
__device__ __forceinline__ f32x4 mfma16(u32x4 a, u32x4 b, f32x4 c) {
    return __builtin_amdgcn_mfma_f32_16x16x32_bf16(as_bf(a), as_bf(b), c, 0, 0, 0);
}
__device__ __forceinline__ f32x4 smf(u32x4 a, u32x4 b0, u32x4 b1, f32x4 c, int idx) {
    u32x8 b = {b0[0], b0[1], b0[2], b0[3], b1[0], b1[1], b1[2], b1[3]};
    return __builtin_amdgcn_smfmac_f32_16x16x64_bf16(as_bf(a), __builtin_bit_cast(bf16x16, b), c, idx, 0, 0);
}
__device__ __forceinline__ int dmap(int p) {
    int q = p & 15;
    return (p & ~15) | (q & 1) | ((q & 2) << 2) | ((q & 4) >> 1) | ((q & 8) >> 1);
}

constexpr size_t al256(size_t x) { return (x + 255) & ~(size_t)255; }
constexpr size_t OFF_XN = 0;
constexpr size_t OFF_XNP = OFF_XN + al256((size_t)S_ * D_ * 2);
constexpr size_t OFF_R = OFF_XNP + al256((size_t)S_ * D_ * 2);
constexpr size_t OFF_PX = OFF_R + al256((size_t)S_ * 4);
constexpr size_t OFF_PXM = OFF_PX + al256((size_t)NSEG * H_ * 2048);
constexpr size_t OFF_HB = OFF_PXM + al256((size_t)NSEG * 8 * 2048);
constexpr size_t OFF_UPD = OFF_HB + al256((size_t)8 * FF_ * 2);
constexpr size_t OFF_GEN = OFF_UPD + al256((size_t)8 * D_ * 2);
constexpr size_t OFF_CLAIM = OFF_GEN + 256;
constexpr size_t OFF_INI = OFF_CLAIM + 16384;
constexpr size_t OFF_SUM = OFF_INI + 2048;
constexpr size_t OFF_ERR = OFF_SUM + 3072;
constexpr size_t OFF_FA = OFF_ERR + 256;
constexpr size_t OFF_FB = OFF_FA + 4096;
constexpr size_t OFF_FP = OFF_FB + 4096;
constexpr size_t OFF_FZ = OFF_FP + 1024;
constexpr size_t OFF_RDY = OFF_FZ + 2048;
constexpr size_t OFF_IDX = OFF_RDY + 1024;
constexpr size_t OFF_CNT = OFF_IDX + 256;
constexpr size_t ZERO_BEGIN = OFF_ERR, ZERO_END = OFF_CNT + 1024;
constexpr size_t OFF_WIN = (size_t)12 << 20;
constexpr size_t WS_TOTAL = OFF_WIN + ((size_t)188 << 20);
static_assert(ZERO_END <= OFF_WIN && WS_TOTAL <= ((size_t)256 << 20), "ws");

constexpr u32 SLOT0 = 52u << 20;
constexpr u32 SL_P = 4 * 32768, SL_TS = 2 * 32768 + 1024;
constexpr u32 FAR_BX = 1u << 20;
constexpr u32 NEAR_QX = 59u << 20;
constexpr u32 NEAR_KX = NEAR_QX + NSEG * H_ * 8192;
static_assert(2 * ((size_t)NEAR_KX + (size_t)NSEG * H_ * 8192) + ((size_t)2 << 20) <= WS_TOTAL - OFF_WIN && SLOT0 + (64u << 20) <= 2 * NEAR_QX
              && 2 * ((size_t)FAR_BX + (size_t)NSEG * NRED * 32768) + ((size_t)2 << 20) <= WS_TOTAL - OFF_WIN, "far");
__device__ __forceinline__ u32 slot_off(u32 wp, int s) {
    u32 r, t;
    asm("s_lshr_b32 %0, %3, 1\n\ts_xor_b32 %0, %0, %2\n\ts_and_b32 %0, %0, 1\n\ts_lshl_b32 %0, %0, 13\n\ts_lshl_b32 %1, %3, 20\n\t"
        "s_add_u32 %0, %0, %1\n\ts_andn2_b32 %1, %2, 15\n\ts_add_u32 %0, %0, %1\n\ts_add_u32 %0, %0, %4" : "=&s"(r), "=&s"(t) : "s"(wp), "s"(s), "n"(SLOT0) : "scc");
    return r;
}
__device__ __forceinline__ u32 slot_z(u32 wp, u32 s) { return (wp & ~15u) + SLOT0 + (s << 20) + (((wp ^ (s >> 1)) & 1u) << 13); }
__device__ __forceinline__ u32 slot_v(u32 wp, u32 s) { return slot_z(wp, s) + ((((wp >> 3) ^ (s >> 5)) & 1u) << 18); }
__device__ __forceinline__ u32 far_v(u32 wp, u32 L) { return (wp & ~15u) + 2 * L + ((~(wp ^ (L >> 20)) & 1u) << 13); }
__device__ __forceinline__ u32 tab(u32 t, int s) { return __builtin_amdgcn_readlane(t, s); }
__device__ __forceinline__ u32 copy_off(u32 c, u32 s) { return ((c & 1u) << 14) | ((((c >> 1) ^ s) & 1u) << 12); }
__device__ __forceinline__ u32 z_off(u32 wp, u32 ct) {
    const u32 zb = ct >> 1, pg = zb < 16 ? zb + 16 : 6 + (zb & 1u) + (zb & 2u) * 4;
    return pg * 32768 + (((ct ^ (wp >> 1) ^ (pg >> 4)) & 1u) << 14);
}
constexpr int NCP = CFG ? 2 : 4;
__device__ __forceinline__ u32 put_off(u32 wp, u32 x0, u32 s) { return CFG ? copy_off(x0 ^ (wp >> 1), s) & 4096u : 0u; }
__device__ __forceinline__ u32 cpy(u32 c) { return CFG ? (c & 1u) * 16384u : (c & 1u) * 4096u + (c >> 1) * 16384u; }
__device__ __forceinline__ u32 far_off(u32 wp, u32 L) {
    u32 r, t;
    asm("s_lshr_b32 %0, %3, 20\n\ts_xnor_b32 %0, %0, %2\n\ts_and_b32 %0, %0, 1\n\ts_lshl_b32 %0, %0, 13\n\ts_lshl_b32 %1, %3, 1\n\t"
        "s_add_u32 %0, %0, %1\n\ts_andn2_b32 %1, %2, 15\n\ts_add_u32 %0, %0, %1" : "=&s"(r), "=&s"(t) : "s"(wp), "s"(L) : "scc");
    return r;
}
constexpr u32 SENT = 0xffbfffbfu;
constexpr u32 MARK = 0xffbeffbeu;
constexpr u32 OOB = 0x80000000u;
__device__ __forceinline__ u32 amap(u32 L) { return ((L >> 13) << 14) | (L & 8191u); }
__device__ __forceinline__ u32 win_off(const char* ws) {
    u64 a = (u64)(uintptr_t)ws;
    u64 w = (a + OFF_WIN + ((u64)2 << 20) - 1) & ~(((u64)2 << 20) - 1);
    return (u32)(w - a);
}

constexpr int AUX_SC1 = 16;
typedef __amdgpu_buffer_rsrc_t rsrc_t;
__device__ __forceinline__ rsrc_t mk_rsrc(const void* p) {
    return __builtin_amdgcn_make_buffer_rsrc((void*)p, 0, 0x7fffffff, 0x00020000);
}
__device__ __forceinline__ u32x4 ldx4(rsrc_t r, u32 off, u32 so = 0) { return __builtin_amdgcn_raw_buffer_load_b128(r, off, so, AUX_SC1); }
__device__ __forceinline__ u32x2 ldx2(rsrc_t r, u32 off, u32 so = 0) { return __builtin_amdgcn_raw_buffer_load_b64(r, off, so, AUX_SC1); }
__device__ __forceinline__ u32 ldx1(rsrc_t r, u32 off, u32 so = 0) { return __builtin_amdgcn_raw_buffer_load_b32(r, off, so, AUX_SC1); }
__device__ __forceinline__ void stx4(rsrc_t r, u32 off, u32x4 v, u32 so = 0) { __builtin_amdgcn_raw_buffer_store_b128(v, r, off + so, 0, AUX_SC1); }
__device__ __forceinline__ void stx2(rsrc_t r, u32 off, u32x2 v, u32 so = 0) { __builtin_amdgcn_raw_buffer_store_b64(v, r, off, so, AUX_SC1); }
__device__ __forceinline__ void stx1(rsrc_t r, u32 off, u32 v, u32 so = 0) { __builtin_amdgcn_raw_buffer_store_b32(v, r, off, so, AUX_SC1); }
#define DRAIN() asm volatile("s_waitcnt vmcnt(0)" ::: "memory")
#define CFENCE() asm volatile("" ::: "memory")
#define LBAR() asm volatile("s_waitcnt lgkmcnt(0)\n\ts_barrier" ::: "memory")

constexpr u32 L_MISC = 147456;
constexpr u32 SPIN_CAP = 1u << 18;
__device__ __forceinline__ int lane_id() {
    int l;
    asm volatile("v_mbcnt_lo_u32_b32 %0, -1, 0\n\tv_mbcnt_hi_u32_b32 %0, -1, %0" : "=v"(l));
    __builtin_assume(l >= 0 && l < 64);
    return l;
}

__device__ __forceinline__ float xmax16(float x) {
    u32 a = __float_as_uint(x);
    auto r = __builtin_amdgcn_permlane16_swap(a, a, false, false);
    return fmaxf(__uint_as_float(r[0]), __uint_as_float(r[1]));
}
__device__ __forceinline__ float xmax32(float x) {
    u32 a = __float_as_uint(x);
    auto r = __builtin_amdgcn_permlane32_swap(a, a, false, false);
    return fmaxf(__uint_as_float(r[0]), __uint_as_float(r[1]));
}
__device__ __forceinline__ float xadd16(float x) {
    u32 a = __float_as_uint(x);
    auto r = __builtin_amdgcn_permlane16_swap(a, a, false, false);
    return __uint_as_float(r[0]) + __uint_as_float(r[1]);
}
__device__ __forceinline__ float xadd32(float x) {
    u32 a = __float_as_uint(x);
    auto r = __builtin_amdgcn_permlane32_swap(a, a, false, false);
    return __uint_as_float(r[0]) + __uint_as_float(r[1]);
}
__device__ __forceinline__ u32 umax3(u32 a, u32 b, u32 c) { return max(max(a, b), c); }

__device__ __forceinline__ void wait_flags(rsrc_t rs, u32 off, bool act, u32 target, bool& dead, u32* errw, u32 code, int lane) {
    u32 spins = 0;
#pragma unroll 1
    while (!dead) {
        CFENCE();
        u32x4 f = ldx4(rs, off);
        bool ok = !act || ((f[0] >= target) & (f[1] >= target) & (f[2] >= target) & (f[3] >= target));
        if (__all(ok)) break;
        __builtin_amdgcn_s_sleep(2);
        if (++spins > SPIN_CAP) { if (lane == 0) atomicOr(errw, code); dead = true; }
        if ((spins & 63) == 0 && ldx1(rs, (u32)OFF_ERR) != 0u) dead = true;
    }
    CFENCE();
}
__device__ __forceinline__ void wait_word(rsrc_t rs, u32 off, bool act, u32 target, bool& dead, u32* errw, u32 code, int lane) {
    u32 spins = 0;
#pragma unroll 1
    while (!dead) {
        CFENCE();
        u32 f = ldx1(rs, off);
        if (__all(!act || f >= target)) break;
        __builtin_amdgcn_s_sleep(4);
        if (++spins > SPIN_CAP) { if (lane == 0) atomicOr(errw, code); dead = true; }
        if ((spins & 63) == 0 && ldx1(rs, (u32)OFF_ERR) != 0u) dead = true;
    }
    CFENCE();
}

__device__ __forceinline__ u32 cal_tag(u64 tag, u32 v) { return ((u32)tag ^ (u32)(tag >> 32)) + v * 0x9E3779B1u; }
__device__ __forceinline__ u32 to_sgpr(u32 x) {
    u32 r;
    asm volatile("s_nop 0\n\tv_readfirstlane_b32 %0, %1\n\ts_nop 4" : "=s"(r) : "v"(x));
    return r;
}
__device__ __forceinline__ u32 wp_of(const char* ws, u32 v) {
    v += __builtin_amdgcn_update_dpp(0u, v, 0xb1, 0xf, 0xf, false);
    v += __builtin_amdgcn_update_dpp(0u, v, 0x4e, 0xf, 0xf, false);
    v += __builtin_amdgcn_update_dpp(0u, v, 0x124, 0xf, 0xf, false);
    v += __builtin_amdgcn_update_dpp(0u, v, 0x128, 0xf, 0xf, false);
    const u32 r = __builtin_amdgcn_readlane(v, 48) + 0x0f0f0f0fu;
    return to_sgpr(win_off(ws) | ((r >> 7) & 1u) | ((r >> 14) & 2u) | ((r >> 21) & 4u) | ((r >> 28) & 8u));
}
__device__ __forceinline__ u32 sum_off(int level) {
    u32 xcc;
    asm volatile("s_getreg_b32 %0, hwreg(HW_REG_XCC_ID, 0, 3)" : "=s"(xcc));
    return (u32)(OFF_SUM + level * 1024) + xcc * 128u;
}
__device__ __forceinline__ u32 wait_init(rsrc_t rs, u64 tag, int level) {
    const u32 so = sum_off(level);
    u32x2 c = {0u, 0u};
#pragma unroll 1
    for (u32 spins = 0; spins < SPIN_CAP; ++spins) {
        CFENCE();
        c = ldx2(rs, 0u, so);
        if (__all(c[1] == cal_tag(tag, c[0]))) break;
        __builtin_amdgcn_s_sleep(6);
    }
    CFENCE();
    return to_sgpr(min(c[0], (u32)OFF_WIN + 0x20000fu));
}

#define STAMPS() do { } while (0)
#define STAMP(k) do { } while (0)
#define RSTAMP(k) do { } while (0)
#define STAMP2(k, r) do { } while (0)
#define FLUSH() do { } while (0)
#define LOGS() do { } while (0)
#define LOG() do { } while (0)
#define LOGOUT(cu) do { } while (0)
#define CFLUSH() do { } while (0)
#define XSTAMP(w, sl) do { } while (0)
#define WSTAMP(id) do { } while (0)
#define CSTAMP(id) do { } while (0)
#define NPROBE 2
constexpr u32 INI_ZERO_CHUNKS = (u32)((ZERO_END - ZERO_BEGIN) / 16);
static_assert(INI_ZERO_CHUNKS <= 8 * 256, "zero");

__device__ __forceinline__ float rsum16(float x) {
    x += __uint_as_float(__builtin_amdgcn_update_dpp(0u, __float_as_uint(x), 0xb1, 0xf, 0xf, false));
    x += __uint_as_float(__builtin_amdgcn_update_dpp(0u, __float_as_uint(x), 0x4e, 0xf, 0xf, false));
    x += __uint_as_float(__builtin_amdgcn_update_dpp(0u, __float_as_uint(x), 0x124, 0xf, 0xf, false));
    x += __uint_as_float(__builtin_amdgcn_update_dpp(0u, __float_as_uint(x), 0x128, 0xf, 0xf, false));
    return x;
}

__device__ __forceinline__ void ln_regs(u32x4 (&xv)[5]) {
    float sum = 0.f;
#pragma unroll
    for (int i = 0; i < 5; ++i)
#pragma unroll
        for (int j = 0; j < 4; ++j) sum += __uint_as_float(xv[i][j] << 16) + __uint_as_float(xv[i][j] & 0xffff0000u);
    const float mean = rsum16(sum) * (1.0f / D_);
    float vs = 0.f;
#pragma unroll
    for (int i = 0; i < 5; ++i)
#pragma unroll
        for (int j = 0; j < 4; ++j) {
            float d0 = __uint_as_float(xv[i][j] << 16) - mean, d1 = __uint_as_float(xv[i][j] & 0xffff0000u) - mean;
            vs += d0 * d0 + d1 * d1;
        }
    const float var = bfr(rsum16(vs) * (1.0f / D_));
    const float m_bf = bfr(mean);
    const float rden = 1.0f / bfr(sqrtf(bfr(var + 1e-05f)));
#pragma unroll
    for (int i = 0; i < 5; ++i)
#pragma unroll
        for (int j = 0; j < 4; ++j)
            xv[i][j] = pack2(bfr(__uint_as_float(xv[i][j] << 16) - m_bf) * rden, bfr(__uint_as_float(xv[i][j] & 0xffff0000u) - m_bf) * rden);
}
__device__ __forceinline__ u32x4 ln_perm(const u32x4 o, const int n) {
    const u32 s0 = (n & 1) ? o[0] : o[2], s1 = (n & 1) ? o[1] : o[3];
    const u32 t0 = __builtin_amdgcn_update_dpp(0u, s0, 0xb1, 0xf, 0xf, false), t1 = __builtin_amdgcn_update_dpp(0u, s1, 0xb1, 0xf, 0xf, false);
    return (n & 1) ? (u32x4){t0, o[2], t1, o[3]} : (u32x4){o[0], t0, o[1], t1};
}
__device__ __forceinline__ u32 img_off(const int row, const int n, const int i) {
    return (u32)((row >> 4) * 20480 + (4 * i + (n >> 2)) * 1024 + (n & 3) * 256 + (row & 15) * 16);
}
__device__ __forceinline__ void ln_load(const int row0, const int lane, const u16* __restrict__ x, const u16* __restrict__ w_r, u32x4 (&xv)[5], u32x4 (&wv)[5]) {
    const int n = lane & 15, row = row0 + (lane >> 4);
#pragma unroll
    for (int i = 0; i < 5; ++i) xv[i] = *(const u32x4*)(x + (size_t)row * D_ + (n + 16 * i) * 8);
#pragma unroll
    for (int i = 0; i < 5; ++i) wv[i] = *(const u32x4*)(w_r + (n + 16 * i) * 8);
}
__device__ __forceinline__ void ln_store(const int row0, const int lane, const u32x4 (&xv)[5], const u32x4 (&wv)[5], rsrc_t rs, const bool img = true) {
    const int n = lane & 15, row = row0 + (lane >> 4);
#pragma unroll
    for (int i = 0; i < 5; ++i) if (img) stx4(rs, (u32)OFF_XNP + img_off(row, n, i), ln_perm(xv[i], n));
    CFENCE();
    float dot = 0.f;
#pragma unroll
    for (int i = 0; i < 5; ++i) {
        stx4(rs, (u32)OFF_XN + (u32)(row * D_ + (n + 16 * i) * 8) * 2, xv[i]);
#pragma unroll
        for (int j = 0; j < 4; ++j)
            dot += __uint_as_float(xv[i][j] << 16) * __uint_as_float(wv[i][j] << 16) + __uint_as_float(xv[i][j] & 0xffff0000u) * __uint_as_float(wv[i][j] & 0xffff0000u);
    }
    dot = rsum16(dot);
    stx1(rs, n == 0 ? (u32)(OFF_R + row * 4) : OOB, __float_as_uint(bfr(dot)));
}
__device__ __forceinline__ u32x4 mk_srd(const char* ws) {
    const u64 a = (u64)(uintptr_t)ws;
    return (u32x4){(u32)a, (u32)(a >> 32), 0x7fffffffu, 0x00020000u};
}

template <typename F>
__device__ __forceinline__ u32 probe(F&& early, const int pi, const u32 xcc, const u32 hl, char* __restrict__ ws) {
    const u32x4 srd = mk_srd(ws);
    const u32 base = win_off(ws) + SLOT0 + (8u << 20) + 6u * 32768u + (u32)pi * 128u;
    const int lane = lane_id();
    const u32 zero = 0;
    asm volatile("buffer_atomic_or %0, %1, %2, %3 offen sc1\n\ts_waitcnt vmcnt(0)" :: "v"(zero), "v"(lane < 16 ? (u32)lane * 2048u : OOB), "s"(srd), "s"(base) : "memory");
    const u32 one = lane == 0 ? 0u : OOB;
    auto lat = [&](u32 q) {
        u64 t0, t1;
        asm volatile("s_memtime %0\n\tbuffer_atomic_or %2, %3, %4, %5 offen sc1\n\ts_waitcnt vmcnt(0)\n\ts_memtime %1\n\ts_waitcnt lgkmcnt(0)"
                     : "=&s"(t0), "=&s"(t1) : "v"(zero), "v"(one), "s"(srd), "s"(base + q * 2048u) : "memory");
        return (u32)t1 - (u32)t0;
    };
    const u32 q0 = (u32)pi & 11u, a0 = lat(q0), a1 = lat(q0 | 4u), a2 = lat(q0 ^ 8u), a3 = lat((q0 ^ 8u) | 4u), a4 = lat(q0 ^ 2u), a5 = lat((q0 ^ 2u) | 4u);
    const int d13 = (int)min(min(a0, a2), a4) - (int)min(min(a1, a3), a5), v13 = min(max(d13 >> 4, -7), 7);
    const u32 b13 = d13 > 0 ? 4u : 0u;
    const u32 flip = (xcc >> 2) | ((xcc & 3u) << 1) | (hl << 3);
    u32 votes = (u32)(7 + ((flip & 1u) ? -v13 : v13));
    early(votes);
    u32 best = ~0u;
#pragma unroll 1
    for (u32 i = 0; i < 8 * NPROBE; ++i) {
        const u32 c = (i + (u32)pi) & 7u, t = lat((c & 3u) | b13 | (c & 4u) * 2u);
        if ((u32)lane == c) best = min(best, t);
    }
#pragma unroll
    for (u32 i = 1; i < 4; ++i) {
        const u32 bit = 8u >> i;
        const int d = (int)best - __shfl_xor((int)best, (int)bit);
        int v = 0;
#pragma unroll
        for (u32 c = 0; c < 8; ++c) if (!(c & bit)) v += min(max(__builtin_amdgcn_readlane(d, c), -28), 28);
        v = (v + 8) >> 4;
        if ((flip >> i) & 1u) v = -v;
        votes |= (u32)(xcc >= 4 ? 7 : 7 + v) << (8 * i);
    }
    return votes;
}

typedef __attribute__((ext_vector_type(32))) unsigned int u32x32;
__device__ __forceinline__ u32x4 wfrag(const u32x32 (&W)[5], const int i) {
    return (u32x4){W[i >> 3][(i & 7) * 4], W[i >> 3][(i & 7) * 4 + 1], W[i >> 3][(i & 7) * 4 + 2], W[i >> 3][(i & 7) * 4 + 3]};
}
#define PIN_W(W) asm volatile("" : "+{v[16:47]}"(W[0]), "+{v[48:79]}"(W[1]), "+{v[80:111]}"(W[2]), "+{v[112:143]}"(W[3]), "+{v[144:175]}"(W[4]))
typedef __attribute__((ext_vector_type(4))) short s16x4;
typedef __attribute__((address_space(3))) s16x4* lds_s16x4p;
__device__ __forceinline__ u32x2 trd(u32 ldsaddr) {
    return __builtin_bit_cast(u32x2, __builtin_amdgcn_ds_read_tr16_b64_v4i16((lds_s16x4p)ldsaddr));
}
template <bool NT>
__device__ __forceinline__ void dma16(const void* g, u32 lds_base) {
    u32 sv;
    if (NT)
        asm volatile("s_mov_b32 %0, m0\n\ts_mov_b32 m0, %2\n\ts_nop 0\n\tglobal_load_lds_dwordx4 %1, off nt\n\ts_mov_b32 m0, %0"
                     : "=&s"(sv) : "v"(g), "s"(lds_base) : "memory");
    else
        asm volatile("s_mov_b32 %0, m0\n\ts_mov_b32 m0, %2\n\ts_nop 0\n\tglobal_load_lds_dwordx4 %1, off\n\ts_mov_b32 m0, %0"
                     : "=&s"(sv) : "v"(g), "s"(lds_base) : "memory");
}
__device__ __forceinline__ void dma16_sc1(const void* g, u32 lds_base) {
    u32 sv;
    asm volatile("s_mov_b32 %0, m0\n\ts_mov_b32 m0, %2\n\ts_nop 0\n\tglobal_load_lds_dwordx4 %1, off sc1\n\ts_mov_b32 m0, %0"
                 : "=&s"(sv) : "v"(g), "s"(lds_base) : "memory");
}
template <int ORD, typename F>
__device__ __forceinline__ void load_weights(F&& after, const u16* __restrict__ p0, const u16* __restrict__ p1, const bool wo1, char* smem,
                                             const int wave, const int lane, const int sa, const int ta, const int sb, const int tb,
                                             u32x32 (&W)[5]) {
    const u32 lbase = (u32)(uintptr_t)(__attribute__((address_space(3))) char*)smem;
    const int strm = wave >> 2;
    const bool wo = wo1 && strm == 1;
    const u16* src = strm ? p1 : p0;
    const u32 sstep = wo ? 128u : 128u * HD_;
    u32 so[8];
#pragma unroll
    for (int i = 0; i < 8; ++i) {
        const int j = (wave & 3) * 8 + i, r = 4 * j + (lane >> 4);
        const int f = (lane >> 4) | (((j >> 1) & 1) << 2);
        const int ch = (lane & 15) ^ (f << 1);
        const int e = (r & ~31) | (((r >> 2) & 1) << 4) | (((r >> 3) & 3) << 2) | (r & 3);
        so[i] = wo ? (u32)(e * D_ + ch * 8) : (u32)(dmap(r) * HD_ + ch * 8);
    }
    auto issue = [&](int st) {
        const u32 dst = __builtin_amdgcn_readfirstlane(lbase + (u32)((st & 1) * 65536 + strm * 32768 + (wave & 3) * 8192));
#pragma unroll
        for (int i = 0; i < 8; ++i) dma16<((ORD >> 20) & 1) != 0>(src + so[i] + (size_t)((ORD >> (4 * st)) & 15) * sstep, dst + i * 1024);
    };
    const int g = lane >> 4, q = (lane >> 2) & 3, p = lane & 3;
    const int f = q | ((g & 1) << 2);
    const u32 ra = lbase + (u32)(sa * 32768 + (8 * g + q) * 256 + (((ta ^ f) << 1) | (p >> 1)) * 16 + (p & 1) * 8);
    const u32 rb = lbase + (u32)(sb * 32768 + (8 * g + q) * 256 + (((tb ^ f) << 1) | (p >> 1)) * 16 + (p & 1) * 8);
    issue(0);
    issue(1);
#pragma unroll
    for (int st = 0; st < 5; ++st) {
        if (st < 4) asm volatile("s_waitcnt vmcnt(8)" ::: "memory"); else asm volatile("s_waitcnt vmcnt(0)" ::: "memory");
        LBAR();
#pragma unroll
        for (int k = 0; k < 4; ++k) {
            const u32 o = (u32)((st & 1) * 65536 + k * 8192);
            u32x2 a0 = trd(ra + o), a1 = trd(ra + o + 1024), b0 = trd(rb + o), b1 = trd(rb + o + 1024);
            const int ia = (((ORD >> (4 * st)) & 15) * 4 + k) * 4, ib = 80 + ia;
            if ((ORD >> 21) & 1) {
                u32x32& w = W[(ORD >> (4 * st)) & 15];
                w[k * 8] = a0[0]; w[k * 8 + 1] = b0[0]; w[k * 8 + 2] = a0[1]; w[k * 8 + 3] = b0[1];
                w[k * 8 + 4] = a1[0]; w[k * 8 + 5] = b1[0]; w[k * 8 + 6] = a1[1]; w[k * 8 + 7] = b1[1];
            } else {
                W[ia >> 5][ia & 31] = a0[0]; W[ia >> 5][(ia & 31) + 1] = a0[1]; W[ia >> 5][(ia & 31) + 2] = a1[0]; W[ia >> 5][(ia & 31) + 3] = a1[1];
                W[ib >> 5][ib & 31] = b0[0]; W[ib >> 5][(ib & 31) + 1] = b0[1]; W[ib >> 5][(ib & 31) + 2] = b1[0]; W[ib >> 5][(ib & 31) + 3] = b1[1];
            }
        }
        LBAR();
        after(st + 16);
        if (st + 2 < 5) issue(st + 2);
        after(st);
    }
}

__device__ __forceinline__ void early_lines(const int t0, const u64 wq, const u64 wk, const u64 wv, const u32 lds) {
#pragma unroll
    for (int q = 0; q < 2; ++q) {
        const int t = t0 + q * 320, m = t / 180, r = (t - m * 180) * 64 + lane_id(), row = r / 18, c = r - row * 18;
        const u64 w = m == 0 ? wk : m == 1 ? wv : wq;
        const u32 blk = ((c < 16 ? (u32)(c >> 1) * 4u + 1u : 31u) - ((u32)w >> 8)) & 31u;
        u32 sv;
        if (t < 540)
            asm volatile("s_mov_b32 %0, m0\n\ts_mov_b32 m0, %3\n\ts_nop 0\n\tglobal_load_lds_dword %1, %2\n\ts_mov_b32 m0, %0"
                         : "=&s"(sv) : "v"((u32)row * 8192u + blk * 256u + (u32)(c & 1) * 128u), "s"(w), "s"(lds) : "memory");
    }
}
template <typename F>
__device__ __forceinline__ void init_role(F&& settle, const int wave, const int ii, const u32 xcc, const u32 hl, const u16* __restrict__ x, const u16* __restrict__ w_r,
                                          const u16* Wq, const u16* Wk, const u16* Wv, char* __restrict__ ws, char* smem) {
    const int lane = lane_id(), tid = wave * 64 + lane;
    const rsrc_t rs = mk_rsrc(ws);
    u32* cnt = (u32*)(smem + L_MISC + 128);
    if (tid < 3) cnt[tid] = 0u;
    u64 tag;
    auto arrive = [&](int level) {
        if (lane == 0 && __hip_atomic_fetch_add(cnt + level, 1u, __ATOMIC_RELAXED, __HIP_MEMORY_SCOPE_WORKGROUP) == 7u)
            stx2(rs, (u32)(OFF_INI + (level * 64 + ii) * 8), (u32x2){0u, cal_tag(tag, 0u)});
    };
    if (ii >= 48) {
        if (wave == 1 && ii < 63) {
            const u32 votes = probe([&](u32 v) {
                tag = settle();
                stx2(rs, lane == 0 ? (u32)(OFF_INI + ii * 8) : OOB, (u32x2){v, cal_tag(tag, v)});
                DRAIN();
                XSTAMP(192 + ii, 220);
            }, ii - 48, xcc, hl, ws);
            XSTAMP(192 + ii, 223);
            stx2(rs, lane == 0 ? (u32)(OFF_INI + (64 + ii) * 8) : OOB, (u32x2){votes, cal_tag(tag, votes)});
        } else tag = settle();
        if (wave == 2 && ii == 63) {
            u32 wp = 0;
#pragma unroll 1
            for (int level = 0; level < 3; ++level) {
                const bool act = level == 2 ? lane < 48 : lane < 63 && (lane >= 40 || level == 1);
                u32x2 c = {0u, 0u};
                u32 spins = 0;
#pragma unroll 1
                for (; spins < SPIN_CAP; ++spins) {
                    CFENCE();
                    c = ldx2(rs, (u32)(OFF_INI + level * 512 + lane * 8));
                    if (__all(!act || c[1] == cal_tag(tag, c[0]))) break;
                }
                if (spins == SPIN_CAP) break;
                if (level < 2) wp = wp_of(ws, lane < 63 ? c[0] : 0x07070707u);
                stx2(rs, lane < 8 ? (u32)(OFF_SUM + level * 1024 + lane * 128) : OOB, (u32x2){wp, cal_tag(tag, wp)});
                XSTAMP(192 + level, 229);
            }
        }
        if (ii < 63) LBAR();
        return;
    }
    u32x4 xv[5], wv[5];
    if (ii < 40) {
        early_lines(wave * 40 + ii, (u64)(uintptr_t)Wq, (u64)(uintptr_t)Wk, (u64)(uintptr_t)Wv, (u32)(uintptr_t)(__attribute__((address_space(3))) char*)smem + L_MISC + 512u);
        tag = settle();
        const u32 wp = wait_init(rs, tag, 0), st = (u32)(ii * 8 + wave), so = slot_off(wp, (int)(st >> 6)) + (st & 63u) * 16384u + (u32)lane * 16;
#pragma unroll
        for (int i = 0; i < 8; ++i) stx4(rs, so + i * 1024, (u32x4){SENT, SENT, SENT, SENT});
        DRAIN();
        XSTAMP(192 + ii, 225);
        arrive(1);
    } else {
        const int row = (ii - 40) * 32 + wave * 4;
        u32x4 xw[5];
        ln_load(row, lane, x, w_r, xv, wv);
#pragma unroll
        for (int i = 0; i < 5; ++i) xw[i] = *(const u32x4*)(x + (size_t)(row + 256 + (lane >> 4)) * D_ + ((lane & 15) + 16 * i) * 8);
        ln_regs(xv);
        tag = settle();
        const u32 i = (u32)((ii - 40) * 512 + tid);
        stx4(rs, i < INI_ZERO_CHUNKS ? (u32)ZERO_BEGIN + i * 16 : OOB, (u32x4){0u, 0u, 0u, 0u});
#pragma unroll
        for (int i = 0; i < 5; ++i) stx4(rs, (u32)OFF_XNP + img_off(row + (lane >> 4), lane & 15, i), ln_perm(xv[i], lane & 15));
        ln_regs(xw);
        DRAIN();
        XSTAMP(192 + ii, 220);
        arrive(0);
        ln_store(row + 256, lane, xw, wv, rs);
        asm volatile("s_waitcnt vmcnt(6)" ::: "memory");
        arrive(1);
        ln_store(row, lane, xv, wv, rs, false);
    }
    {
        const int row = 512 + (ii * 8 + wave) * 4;
        ln_load(row, lane, x, w_r, xv, wv);
        ln_regs(xv);
        ln_store(row, lane, xv, wv, rs);
    }
    DRAIN();
    XSTAMP(192 + ii, 222);
    arrive(2);
    LBAR();
}

#define NOWV 2
#define STP
#define STA
#define LGP
#define LGA
__device__ __forceinline__ void s_fetch(u32 vm, u32 xo, u32x4 rs, u32 pfo, u32 pfd, u32 xd) {
    asm volatile(
        "s_mov_b32 s92, m0\n\t"
        "s_mov_b32 m0, %[pfd]\n\t"
        "s_add_u32 s93, %[pfo], 0x1000\n\t"
        "s_nop 2\n\t"
        "buffer_load_dwordx4 %[vm], %[rs], %[pfo] offen offset:0 sc1 lds\n\t"
        "buffer_load_dwordx4 %[vm], %[rs], %[pfo] offen offset:1024 sc1 lds\n\t"
        "buffer_load_dwordx4 %[vm], %[rs], %[pfo] offen offset:2048 sc1 lds\n\t"
        "buffer_load_dwordx4 %[vm], %[rs], %[pfo] offen offset:3072 sc1 lds\n\t"
        "s_add_u32 m0, m0, 0x1000\n\t"
        "s_nop 0\n\t"
        "buffer_load_dwordx4 %[vm], %[rs], s93 offen offset:0 sc1 lds\n\t"
        "buffer_load_dwordx4 %[vm], %[rs], s93 offen offset:1024 sc1 lds\n\t"
        "buffer_load_dwordx4 %[vm], %[rs], s93 offen offset:2048 sc1 lds\n\t"
        "buffer_load_dwordx4 %[vm], %[rs], s93 offen offset:3072 sc1 lds\n\t"
        "s_mov_b32 m0, %[xd]\n\t"
        "s_nop 0\n\t"
        "buffer_load_dword %[xo], %[rs], 0 offen sc1 lds\n\t"
        "s_mov_b32 m0, s92\n\t"
        :
        : [vm] "v"(vm), [xo] "v"(xo), [rs] "s"(rs), [pfo] "s"(pfo), [pfd] "s"(pfd), [xd] "s"(xd)
        : "s92", "s93", "vcc", "scc", "memory");
}

__device__ __forceinline__ void s_k0(const u32x32 (&W)[5], u32& rnd, u32 va, u32 sidx, u32 ow, u32 lb STP) {
    asm volatile(
        "Ltop%=:\n\t"
        "v_mov_b64 v[248:249], 0\n\t"
        "v_mov_b64 v[250:251], 0\n\t"
        "v_mov_b64 v[252:253], 0\n\t"
        "v_mov_b64 v[254:255], 0\n\t"
        "v_mov_b32 v200, 0xffbfffbf\n\t"
        "v_mov_b32 v206, %[lb]\n\t"
        "ds_write_b32 %[ow], v200\n\t"
        "ds_write_b32 %[ow], v200 offset:3072\n\t"
        "s_add_u32 %[rnd], %[rnd], 1\n\t"
        "s_setprio 1\n\t"
        "s_waitcnt lgkmcnt(0)\n\t"
        "s_barrier\n\t"
        "s_nop 15\n\t"
        "s_nop 11\n\t"
        "ds_read_b128 v[208:211], %[va] offset:0\n\t"
        "ds_read_b128 v[212:215], %[va] offset:1024\n\t"
        "ds_read_b128 v[216:219], %[va] offset:2048\n\t"
        "ds_read_b128 v[220:223], %[va] offset:3072\n\t"
        "ds_read_b128 v[224:227], %[va] offset:4096\n\t"
        "ds_read_b128 v[228:231], %[va] offset:5120\n\t"
        "ds_read_b128 v[232:235], %[va] offset:6144\n\t"
        "ds_read_b128 v[236:239], %[va] offset:7168\n\t"
        "ds_read_b128 v[240:243], %[va] offset:8192\n\t"
        "ds_read_b128 v[244:247], %[va] offset:10240\n\t"
        "s_waitcnt lgkmcnt(0)\n\t"
        "v_smfmac_f32_16x16x64_bf16 v[248:251], v[208:211], v[16:23], %[sidx]\n\t"
        "v_smfmac_f32_16x16x64_bf16 v[248:251], v[212:215], v[24:31], %[sidx]\n\t"
        "v_smfmac_f32_16x16x64_bf16 v[248:251], v[216:219], v[32:39], %[sidx]\n\t"
        "v_smfmac_f32_16x16x64_bf16 v[248:251], v[220:223], v[40:47], %[sidx]\n\t"
        "v_smfmac_f32_16x16x64_bf16 v[248:251], v[224:227], v[48:55], %[sidx]\n\t"
        "v_smfmac_f32_16x16x64_bf16 v[248:251], v[228:231], v[56:63], %[sidx]\n\t"
        "v_smfmac_f32_16x16x64_bf16 v[248:251], v[232:235], v[64:71], %[sidx]\n\t"
        "v_smfmac_f32_16x16x64_bf16 v[248:251], v[236:239], v[72:79], %[sidx]\n\t"
        "v_smfmac_f32_16x16x64_bf16 v[248:251], v[240:243], v[80:87], %[sidx]\n\t"
        "v_smfmac_f32_16x16x64_bf16 v[248:251], v[244:247], v[88:95], %[sidx]\n\t"
        "v_smfmac_f32_16x16x64_bf16 v[252:255], v[208:211], v[96:103], %[sidx]\n\t"
        "v_smfmac_f32_16x16x64_bf16 v[252:255], v[212:215], v[104:111], %[sidx]\n\t"
        "v_smfmac_f32_16x16x64_bf16 v[252:255], v[216:219], v[112:119], %[sidx]\n\t"
        "v_add_f32 v248, v248, v250\n\t"
        "v_smfmac_f32_16x16x64_bf16 v[252:255], v[220:223], v[120:127], %[sidx]\n\t"
        "v_add_f32 v249, v249, v251\n\t"
        "v_smfmac_f32_16x16x64_bf16 v[252:255], v[224:227], v[128:135], %[sidx]\n\t"
        "v_cvt_pk_bf16_f32 v200, v248, v249\n\t"
        "v_smfmac_f32_16x16x64_bf16 v[252:255], v[228:231], v[136:143], %[sidx]\n\t"
        "ds_write_b32 %[ow], v200\n\t"
        "v_smfmac_f32_16x16x64_bf16 v[252:255], v[232:235], v[144:151], %[sidx]\n\t"
        "v_cmp_u_f32 vcc, v248, v249\n\t"
        "v_smfmac_f32_16x16x64_bf16 v[252:255], v[236:239], v[152:159], %[sidx]\n\t"
        "v_smfmac_f32_16x16x64_bf16 v[252:255], v[240:243], v[160:167], %[sidx]\n\t"
        "v_smfmac_f32_16x16x64_bf16 v[252:255], v[244:247], v[168:175], %[sidx]\n\t"
        "s_nop 6\n\t"
        "s_cbranch_vccnz Lnan%=\n\t"
        "Lok%=:\n\t"
        "v_add_f32 v252, v252, v254\n\t"
        "v_add_f32 v253, v253, v255\n\t"
        "v_cvt_pk_bf16_f32 v201, v252, v253\n\t"
        "ds_write_b32 %[ow], v201 offset:3072\n\t"
        "s_barrier\n\t"
        "s_setprio 0\n\t"
        "ds_read_b32 v207, v206 offset:25104\n\t"
        "s_waitcnt lgkmcnt(0)\n\t"
        "v_cmp_eq_u32 vcc, %[rnd], v207\n\t"
        "s_barrier\n\t"
        "s_cbranch_vccnz Ltop%=\n\t"
        "s_branch Lend%=\n\t"
        "Lnan%=:\n\t"
        "v_max_u32 v207, v208, v209\n\t"
        "v_max3_u32 v207, v207, v210, v211\n\t"
        "v_max3_u32 v207, v207, v212, v213\n\t"
        "v_max3_u32 v207, v207, v214, v215\n\t"
        "v_max3_u32 v207, v207, v216, v217\n\t"
        "v_max3_u32 v207, v207, v218, v219\n\t"
        "v_max3_u32 v207, v207, v220, v221\n\t"
        "v_max3_u32 v207, v207, v222, v223\n\t"
        "v_max3_u32 v207, v207, v224, v225\n\t"
        "v_max3_u32 v207, v207, v226, v227\n\t"
        "v_max3_u32 v207, v207, v228, v229\n\t"
        "v_max3_u32 v207, v207, v230, v231\n\t"
        "v_max3_u32 v207, v207, v232, v233\n\t"
        "v_max3_u32 v207, v207, v234, v235\n\t"
        "v_max3_u32 v207, v207, v236, v237\n\t"
        "v_max3_u32 v207, v207, v238, v239\n\t"
        "v_max3_u32 v207, v207, v240, v241\n\t"
        "v_max3_u32 v207, v207, v242, v243\n\t"
        "v_max3_u32 v207, v207, v244, v245\n\t"
        "v_max3_u32 v207, v207, v246, v247\n\t"
        "v_cmp_eq_u32 vcc, 0xffbfffbf, v207\n\t"
        "v_mov_b32 v207, %[rnd]\n\t"
        "s_cbranch_vccz Lok%=\n\t"
        "ds_write_b32 v206, v207 offset:25104\n\t"
        "s_waitcnt lgkmcnt(0)\n\t"
        "s_barrier\n\t"
        "s_barrier\n\t"
        "s_branch Ltop%=\n\t"
        "Lend%=:\n\t"
        : [rnd] "+&s"(rnd)
        : [w0] "{v[16:47]}"(W[0]), [w1] "{v[48:79]}"(W[1]), [w2] "{v[80:111]}"(W[2]), [w3] "{v[112:143]}"(W[3]), [w4] "{v[144:175]}"(W[4]), [va] "v"(va), [sidx] "v"(sidx), [ow] "v"(ow), [lb] "s"(lb)
        : "v200", "v201", "v202", "v203", "v204", "v205", "v206", "v207", "v208", "v209", "v210", "v211", "v212", "v213", "v214", "v215", "v216", "v217", "v218", "v219", "v220", "v221", "v222", "v223", "v224", "v225", "v226", "v227", "v228", "v229", "v230", "v231", "v232", "v233", "v234", "v235", "v236", "v237", "v238", "v239", "v240", "v241", "v242", "v243", "v244", "v245", "v246", "v247", "v248", "v249", "v250", "v251", "v252", "v253", "v254", "v255", "vcc", "scc", "memory");
}

__device__ __forceinline__ void s_k2(const u32x32 (&W)[5], u32& rnd, u32 va, u32 sidx, u32 ow, u32 lb) {
    asm volatile(
        "Ltop%=:\n\t"
        "v_mov_b64 v[248:249], 0\n\t"
        "v_mov_b64 v[250:251], 0\n\t"
        "v_mov_b64 v[252:253], 0\n\t"
        "v_mov_b64 v[254:255], 0\n\t"
        "v_mov_b32 v200, 0xffbfffbf\n\t"
        "v_mov_b32 v206, %[lb]\n\t"
        "ds_write_b32 %[ow], v200\n\t"
        "ds_write_b32 %[ow], v200 offset:3072\n\t"
        "s_add_u32 %[rnd], %[rnd], 1\n\t"
        "s_setprio 1\n\t"
        "s_waitcnt lgkmcnt(0)\n\t"
        "s_barrier\n\t"
        "ds_read_b128 v[208:211], %[va] offset:0\n\t"
        "ds_read_b128 v[212:215], %[va] offset:1024\n\t"
        "ds_read_b128 v[216:219], %[va] offset:2048\n\t"
        "ds_read_b128 v[220:223], %[va] offset:3072\n\t"
        "ds_read_b128 v[224:227], %[va] offset:4096\n\t"
        "ds_read_b128 v[228:231], %[va] offset:5120\n\t"
        "ds_read_b128 v[232:235], %[va] offset:6144\n\t"
        "ds_read_b128 v[236:239], %[va] offset:7168\n\t"
        "ds_read_b128 v[240:243], %[va] offset:8192\n\t"
        "ds_read_b128 v[244:247], %[va] offset:10240\n\t"
        "s_waitcnt lgkmcnt(9)\n\t"
        "v_smfmac_f32_16x16x64_bf16 v[248:251], v[208:211], v[16:23], %[sidx]\n\t"
        "s_waitcnt lgkmcnt(8)\n\t"
        "v_smfmac_f32_16x16x64_bf16 v[248:251], v[212:215], v[24:31], %[sidx]\n\t"
        "s_waitcnt lgkmcnt(7)\n\t"
        "v_smfmac_f32_16x16x64_bf16 v[248:251], v[216:219], v[32:39], %[sidx]\n\t"
        "s_waitcnt lgkmcnt(6)\n\t"
        "v_smfmac_f32_16x16x64_bf16 v[248:251], v[220:223], v[40:47], %[sidx]\n\t"
        "s_waitcnt lgkmcnt(5)\n\t"
        "v_smfmac_f32_16x16x64_bf16 v[248:251], v[224:227], v[48:55], %[sidx]\n\t"
        "s_waitcnt lgkmcnt(4)\n\t"
        "v_smfmac_f32_16x16x64_bf16 v[248:251], v[228:231], v[56:63], %[sidx]\n\t"
        "s_waitcnt lgkmcnt(3)\n\t"
        "v_smfmac_f32_16x16x64_bf16 v[248:251], v[232:235], v[64:71], %[sidx]\n\t"
        "s_waitcnt lgkmcnt(2)\n\t"
        "v_smfmac_f32_16x16x64_bf16 v[248:251], v[236:239], v[72:79], %[sidx]\n\t"
        "s_waitcnt lgkmcnt(1)\n\t"
        "v_smfmac_f32_16x16x64_bf16 v[248:251], v[240:243], v[80:87], %[sidx]\n\t"
        "s_waitcnt lgkmcnt(0)\n\t"
        "v_smfmac_f32_16x16x64_bf16 v[248:251], v[244:247], v[88:95], %[sidx]\n\t"
        "v_smfmac_f32_16x16x64_bf16 v[252:255], v[208:211], v[96:103], %[sidx]\n\t"
        "v_smfmac_f32_16x16x64_bf16 v[252:255], v[212:215], v[104:111], %[sidx]\n\t"
        "v_smfmac_f32_16x16x64_bf16 v[252:255], v[216:219], v[112:119], %[sidx]\n\t"
        "v_add_f32 v248, v248, v250\n\t"
        "v_smfmac_f32_16x16x64_bf16 v[252:255], v[220:223], v[120:127], %[sidx]\n\t"
        "v_add_f32 v249, v249, v251\n\t"
        "v_smfmac_f32_16x16x64_bf16 v[252:255], v[224:227], v[128:135], %[sidx]\n\t"
        "v_cvt_pk_bf16_f32 v200, v248, v249\n\t"
        "v_smfmac_f32_16x16x64_bf16 v[252:255], v[228:231], v[136:143], %[sidx]\n\t"
        "ds_write_b32 %[ow], v200\n\t"
        "v_smfmac_f32_16x16x64_bf16 v[252:255], v[232:235], v[144:151], %[sidx]\n\t"
        "v_cmp_u_f32 vcc, v248, v249\n\t"
        "v_smfmac_f32_16x16x64_bf16 v[252:255], v[236:239], v[152:159], %[sidx]\n\t"
        "v_smfmac_f32_16x16x64_bf16 v[252:255], v[240:243], v[160:167], %[sidx]\n\t"
        "v_smfmac_f32_16x16x64_bf16 v[252:255], v[244:247], v[168:175], %[sidx]\n\t"
        "s_nop 6\n\t"
        "s_cbranch_vccnz Lnan%=\n\t"
        "Lok%=:\n\t"
        "v_add_f32 v252, v252, v254\n\t"
        "v_add_f32 v253, v253, v255\n\t"
        "v_cvt_pk_bf16_f32 v201, v252, v253\n\t"
        "ds_write_b32 %[ow], v201 offset:3072\n\t"
        "s_barrier\n\t"
        "s_setprio 0\n\t"
        "ds_read_b32 v207, v206 offset:25104\n\t"
        "s_waitcnt lgkmcnt(0)\n\t"
        "v_cmp_eq_u32 vcc, %[rnd], v207\n\t"
        "s_barrier\n\t"
        "s_cbranch_vccnz Ltop%=\n\t"
        "s_branch Lend%=\n\t"
        "Lnan%=:\n\t"
        "v_max_u32 v207, v208, v209\n\t"
        "v_max3_u32 v207, v207, v210, v211\n\t"
        "v_max3_u32 v207, v207, v212, v213\n\t"
        "v_max3_u32 v207, v207, v214, v215\n\t"
        "v_max3_u32 v207, v207, v216, v217\n\t"
        "v_max3_u32 v207, v207, v218, v219\n\t"
        "v_max3_u32 v207, v207, v220, v221\n\t"
        "v_max3_u32 v207, v207, v222, v223\n\t"
        "v_max3_u32 v207, v207, v224, v225\n\t"
        "v_max3_u32 v207, v207, v226, v227\n\t"
        "v_max3_u32 v207, v207, v228, v229\n\t"
        "v_max3_u32 v207, v207, v230, v231\n\t"
        "v_max3_u32 v207, v207, v232, v233\n\t"
        "v_max3_u32 v207, v207, v234, v235\n\t"
        "v_max3_u32 v207, v207, v236, v237\n\t"
        "v_max3_u32 v207, v207, v238, v239\n\t"
        "v_max3_u32 v207, v207, v240, v241\n\t"
        "v_max3_u32 v207, v207, v242, v243\n\t"
        "v_max3_u32 v207, v207, v244, v245\n\t"
        "v_max3_u32 v207, v207, v246, v247\n\t"
        "v_cmp_eq_u32 vcc, 0xffbfffbf, v207\n\t"
        "v_mov_b32 v207, %[rnd]\n\t"
        "s_cbranch_vccz Lok%=\n\t"
        "ds_write_b32 v206, v207 offset:25104\n\t"
        "s_waitcnt lgkmcnt(0)\n\t"
        "s_barrier\n\t"
        "s_barrier\n\t"
        "s_branch Ltop%=\n\t"
        "Lend%=:\n\t"
        : [rnd] "+&s"(rnd)
        : [w0] "{v[16:47]}"(W[0]), [w1] "{v[48:79]}"(W[1]), [w2] "{v[80:111]}"(W[2]), [w3] "{v[112:143]}"(W[3]), [w4] "{v[144:175]}"(W[4]), [va] "v"(va), [sidx] "v"(sidx), [ow] "v"(ow), [lb] "s"(lb)
        : "v200", "v201", "v202", "v203", "v204", "v205", "v206", "v207", "v208", "v209", "v210", "v211", "v212", "v213", "v214", "v215", "v216", "v217", "v218", "v219", "v220", "v221", "v222", "v223", "v224", "v225", "v226", "v227", "v228", "v229", "v230", "v231", "v232", "v233", "v234", "v235", "v236", "v237", "v238", "v239", "v240", "v241", "v242", "v243", "v244", "v245", "v246", "v247", "v248", "v249", "v250", "v251", "v252", "v253", "v254", "v255", "vcc", "scc", "memory");
}

__device__ __forceinline__ void s_l(u32& rnd, u32& redo, u32 lb) {
    asm volatile(
        "v_mov_b32 v206, %[lb]\n\t"
        "s_add_u32 %[rnd], %[rnd], 1\n\t"
        "s_mov_b32 %[redo], 1\n\t"
        "s_barrier\n\t"
        "s_barrier\n\t"
        "s_setprio 0\n\t"
        "ds_read_b32 v207, v206 offset:25104\n\t"
        "s_waitcnt lgkmcnt(0)\n\t"
        "v_cmp_eq_u32 vcc, %[rnd], v207\n\t"
        "s_barrier\n\t"
        "s_cbranch_vccnz Lend%=\n\t"
        "s_mov_b32 %[redo], 0\n\t"
        "Lend%=:\n\t"
        : [rnd] "+&s"(rnd), [redo] "=&s"(redo)
        : [lb] "s"(lb)
        : "v206", "v207", "vcc", "scc", "memory");
}

__device__ __forceinline__ void s_e(const u32x32 (&W)[5], u32& rnd, u32 va, u32 sidx, u32 ow, u32 lb, u32 kxa, u32 qtr, u32 qo, u32 eo, u32 ew, float lim, u32x4 rs, u32 so STP) {
    asm volatile(
        "ds_read_b128 v[176:179], %[kxa] offset:0\n\t"
        "ds_read_b128 v[180:183], %[kxa] offset:1024\n\t"
        "ds_read_b128 v[184:187], %[kxa] offset:2048\n\t"
        "ds_read_b128 v[188:191], %[kxa] offset:3072\n\t"
        "s_add_u32 s92, %[so], 0x1000\n\t"
        "s_add_u32 s93, %[so], 0x4000\n\t"
        "s_add_u32 s94, %[so], 0x5000\n\t"
        "Ltop%=:\n\t"
        "v_mov_b64 v[248:249], 0\n\t"
        "v_mov_b64 v[250:251], 0\n\t"
        "v_mov_b64 v[252:253], 0\n\t"
        "v_mov_b64 v[254:255], 0\n\t"
        "v_mov_b32 v200, 0xffbfffbf\n\t"
        "v_mov_b32 v206, %[lb]\n\t"
        "ds_write_b32 %[ow], v200\n\t"
        "ds_write_b32 %[ow], v200 offset:3072\n\t"
        "s_add_u32 %[rnd], %[rnd], 1\n\t"
        "s_setprio 3\n\t"
        "s_mov_b32 s95, 1\n\t"
        "s_waitcnt lgkmcnt(0)\n\t"
        "s_barrier\n\t"
        "ds_read_b128 v[208:211], %[va] offset:0\n\t"
        "ds_read_b128 v[212:215], %[va] offset:1024\n\t"
        "ds_read_b128 v[216:219], %[va] offset:2048\n\t"
        "ds_read_b128 v[220:223], %[va] offset:3072\n\t"
        "ds_read_b128 v[224:227], %[va] offset:4096\n\t"
        "ds_read_b128 v[228:231], %[va] offset:5120\n\t"
        "s_waitcnt lgkmcnt(5)\n\t"
        "v_smfmac_f32_16x16x64_bf16 v[248:251], v[208:211], v[16:23], %[sidx]\n\t"
        "ds_read_b128 v[232:235], %[va] offset:6144\n\t"
        "v_smfmac_f32_16x16x64_bf16 v[252:255], v[208:211], v[96:103], %[sidx]\n\t"
        "s_waitcnt lgkmcnt(5)\n\t"
        "v_smfmac_f32_16x16x64_bf16 v[248:251], v[212:215], v[24:31], %[sidx]\n\t"
        "ds_read_b128 v[236:239], %[va] offset:7168\n\t"
        "v_smfmac_f32_16x16x64_bf16 v[252:255], v[212:215], v[104:111], %[sidx]\n\t"
        "s_waitcnt lgkmcnt(5)\n\t"
        "v_smfmac_f32_16x16x64_bf16 v[248:251], v[216:219], v[32:39], %[sidx]\n\t"
        "ds_read_b128 v[240:243], %[va] offset:8192\n\t"
        "v_smfmac_f32_16x16x64_bf16 v[252:255], v[216:219], v[112:119], %[sidx]\n\t"
        "s_waitcnt lgkmcnt(5)\n\t"
        "v_smfmac_f32_16x16x64_bf16 v[248:251], v[220:223], v[40:47], %[sidx]\n\t"
        "ds_read_b128 v[244:247], %[va] offset:10240\n\t"
        "v_smfmac_f32_16x16x64_bf16 v[252:255], v[220:223], v[120:127], %[sidx]\n\t"
        "s_waitcnt lgkmcnt(5)\n\t"
        "v_smfmac_f32_16x16x64_bf16 v[248:251], v[224:227], v[48:55], %[sidx]\n\t"
        "s_waitcnt lgkmcnt(4)\n\t"
        "v_smfmac_f32_16x16x64_bf16 v[248:251], v[228:231], v[56:63], %[sidx]\n\t"
        "s_waitcnt lgkmcnt(3)\n\t"
        "v_smfmac_f32_16x16x64_bf16 v[248:251], v[232:235], v[64:71], %[sidx]\n\t"
        "s_waitcnt lgkmcnt(2)\n\t"
        "v_smfmac_f32_16x16x64_bf16 v[248:251], v[236:239], v[72:79], %[sidx]\n\t"
        "s_waitcnt lgkmcnt(1)\n\t"
        "v_smfmac_f32_16x16x64_bf16 v[248:251], v[240:243], v[80:87], %[sidx]\n\t"
        "s_waitcnt lgkmcnt(0)\n\t"
        "v_smfmac_f32_16x16x64_bf16 v[248:251], v[244:247], v[88:95], %[sidx]\n\t"
        "v_smfmac_f32_16x16x64_bf16 v[252:255], v[224:227], v[128:135], %[sidx]\n\t"
        "v_smfmac_f32_16x16x64_bf16 v[252:255], v[228:231], v[136:143], %[sidx]\n\t"
        "v_add_f32 v248, v248, v250\n\t"
        "v_smfmac_f32_16x16x64_bf16 v[252:255], v[232:235], v[144:151], %[sidx]\n\t"
        "v_add_f32 v249, v249, v251\n\t"
        "v_smfmac_f32_16x16x64_bf16 v[252:255], v[236:239], v[152:159], %[sidx]\n\t"
        "v_cvt_pk_bf16_f32 v200, v248, v249\n\t"
        "v_smfmac_f32_16x16x64_bf16 v[252:255], v[240:243], v[160:167], %[sidx]\n\t"
        "ds_write_b32 %[ow], v200\n\t"
        "v_smfmac_f32_16x16x64_bf16 v[252:255], v[244:247], v[168:175], %[sidx]\n\t"
        "v_cmp_u_f32 vcc, v248, v249\n\t"
        "s_nop 2\n\t"
        "ds_read_b64_tr_b16 v[192:193], %[qtr] offset:0\n\t"
        "ds_read_b64_tr_b16 v[194:195], %[qtr] offset:768\n\t"
        "ds_read_b64_tr_b16 v[196:197], %[qtr] offset:1536\n\t"
        "ds_read_b64_tr_b16 v[198:199], %[qtr] offset:2304\n\t"
        "s_cbranch_vccnz Lnan%=\n\t"
        "Lok%=:\n\t"
        "v_add_f32 v252, v252, v254\n\t"
        "v_add_f32 v253, v253, v255\n\t"
        "v_cvt_pk_bf16_f32 v201, v252, v253\n\t"
        "ds_write_b32 %[ow], v201 offset:3072\n\t"
        "s_waitcnt lgkmcnt(3)\n\t"
        "v_mfma_f32_16x16x32_bf16 v[248:251], v[176:179], v[192:195], 0\n\t"
        "s_waitcnt lgkmcnt(1)\n\t"
        "v_mfma_f32_16x16x32_bf16 v[248:251], v[180:183], v[196:199], v[248:251]\n\t"
        "s_waitcnt lgkmcnt(0)\n\t"
        "s_nop 1\n\t"
        "Lre%=:\n\t"
        "ds_read_b64_tr_b16 v[208:209], %[qtr] offset:3072\n\t"
        "ds_read_b64_tr_b16 v[210:211], %[qtr] offset:3840\n\t"
        "ds_read_b64_tr_b16 v[212:213], %[qtr] offset:4608\n\t"
        "ds_read_b64_tr_b16 v[214:215], %[qtr] offset:5376\n\t"
        "s_waitcnt lgkmcnt(2)\n\t"
        "v_mfma_f32_16x16x32_bf16 v[248:251], v[184:187], v[208:211], v[248:251]\n\t"
        "s_waitcnt lgkmcnt(0)\n\t"
        "v_mfma_f32_16x16x32_bf16 v[248:251], v[188:191], v[212:215], v[248:251]\n\t"
        "s_nop 7\n\t"
        "v_mov_b32_dpp v248, v250 quad_perm:[0,1,2,3] row_mask:0xf bank_mask:0xc\n\t"
        "v_mov_b32_dpp v249, v251 quad_perm:[0,1,2,3] row_mask:0xf bank_mask:0xc\n\t"
        "v_exp_f32 v202, v248\n\t"
        "v_exp_f32 v203, v249\n\t"
        "v_max_f32_e64 v204, |v248|, |v249|\n\t"
        "v_cvt_pk_bf16_f32 v202, v202, v203\n\t"
        "v_cmpx_le_f32 vcc, v204, %[lim]\n\t"
        "buffer_store_dword v202, %[eo], %[rs], %[so] offen sc1\n\t"
        "buffer_store_dword v202, %[eo], %[rs], s92 offen sc1\n\t"
        "buffer_store_dword v202, %[eo], %[rs], s93 offen sc1\n\t"
        "buffer_store_dword v202, %[eo], %[rs], s94 offen sc1\n\t"
        "s_mov_b64 exec, -1\n\t"
        "v_cndmask_b32 v202, -1, v202, vcc\n\t"
        "s_cmp_lg_u64 vcc, -1\n\t"
        "ds_write_b32 %[ew], v202\n\t"
        "s_cbranch_scc1 Lag%=\n\t"
        "Lgo%=:\n\t"
        "s_waitcnt lgkmcnt(0)\n\t"
        "s_barrier\n\t"
        "s_setprio 0\n\t"
        "ds_read_b32 v207, v206 offset:25104\n\t"
        "s_waitcnt lgkmcnt(0)\n\t"
        "v_cmp_eq_u32 vcc, %[rnd], v207\n\t"
        "s_barrier\n\t"
        "s_cbranch_vccnz Ltop%=\n\t"
        "s_branch Lend%=\n\t"
        "Lag%=:\n\t"
        "s_sub_u32 s95, s95, 1\n\t"
        "s_cbranch_scc1 Lgo%=\n\t"
        "Lqw%=:\n\t"
        "v_subrev_u32 v205, %[qo], %[ow]\n\t"
        "ds_read_b32 v192, v205 offset:0\n\t"
        "ds_read_b32 v193, v205 offset:768\n\t"
        "ds_read_b32 v194, v205 offset:1536\n\t"
        "ds_read_b32 v195, v205 offset:2304\n\t"
        "ds_read_b32 v196, v205 offset:3072\n\t"
        "ds_read_b32 v197, v205 offset:3840\n\t"
        "ds_read_b32 v198, v205 offset:4608\n\t"
        "ds_read_b32 v199, v205 offset:5376\n\t"
        "ds_read_b32 v207, v206 offset:25104\n\t"
        "s_waitcnt lgkmcnt(0)\n\t"
        "v_cmp_eq_u32 vcc, %[rnd], v207\n\t"
        "s_cbranch_vccnz Lgo%=\n\t"
        "v_max_u32 v207, v192, v193\n\t"
        "v_max3_u32 v207, v207, v194, v195\n\t"
        "v_max3_u32 v207, v207, v196, v197\n\t"
        "v_max3_u32 v207, v207, v198, v199\n\t"
        "v_cmp_eq_u32 vcc, 0xffbfffbf, v207\n\t"
        "s_cbranch_vccnz Lqw%=\n\t"
        "ds_read_b64_tr_b16 v[192:193], %[qtr] offset:0\n\t"
        "ds_read_b64_tr_b16 v[194:195], %[qtr] offset:768\n\t"
        "ds_read_b64_tr_b16 v[196:197], %[qtr] offset:1536\n\t"
        "ds_read_b64_tr_b16 v[198:199], %[qtr] offset:2304\n\t"
        "s_waitcnt lgkmcnt(2)\n\t"
        "v_mfma_f32_16x16x32_bf16 v[248:251], v[176:179], v[192:195], 0\n\t"
        "s_waitcnt lgkmcnt(0)\n\t"
        "v_mfma_f32_16x16x32_bf16 v[248:251], v[180:183], v[196:199], v[248:251]\n\t"
        "s_branch Lre%=\n\t"
        "Lnan%=:\n\t"
        "v_max_u32 v207, v208, v209\n\t"
        "v_max3_u32 v207, v207, v210, v211\n\t"
        "v_max3_u32 v207, v207, v212, v213\n\t"
        "v_max3_u32 v207, v207, v214, v215\n\t"
        "v_max3_u32 v207, v207, v216, v217\n\t"
        "v_max3_u32 v207, v207, v218, v219\n\t"
        "v_max3_u32 v207, v207, v220, v221\n\t"
        "v_max3_u32 v207, v207, v222, v223\n\t"
        "v_max3_u32 v207, v207, v224, v225\n\t"
        "v_max3_u32 v207, v207, v226, v227\n\t"
        "v_max3_u32 v207, v207, v228, v229\n\t"
        "v_max3_u32 v207, v207, v230, v231\n\t"
        "v_max3_u32 v207, v207, v232, v233\n\t"
        "v_max3_u32 v207, v207, v234, v235\n\t"
        "v_max3_u32 v207, v207, v236, v237\n\t"
        "v_max3_u32 v207, v207, v238, v239\n\t"
        "v_max3_u32 v207, v207, v240, v241\n\t"
        "v_max3_u32 v207, v207, v242, v243\n\t"
        "v_max3_u32 v207, v207, v244, v245\n\t"
        "v_max3_u32 v207, v207, v246, v247\n\t"
        "v_cmp_eq_u32 vcc, 0xffbfffbf, v207\n\t"
        "v_mov_b32 v207, %[rnd]\n\t"
        "s_cbranch_vccz Lok%=\n\t"
        "ds_write_b32 v206, v207 offset:25104\n\t"
        "s_waitcnt lgkmcnt(0)\n\t"
        "s_barrier\n\t"
        "s_barrier\n\t"
        "s_branch Ltop%=\n\t"
        "Lend%=:\n\t"
        : [rnd] "+&s"(rnd)
        : [w0] "{v[16:47]}"(W[0]), [w1] "{v[48:79]}"(W[1]), [w2] "{v[80:111]}"(W[2]), [w3] "{v[112:143]}"(W[3]), [w4] "{v[144:175]}"(W[4]), [va] "v"(va), [sidx] "v"(sidx), [ow] "v"(ow), [lb] "s"(lb), [kxa] "v"(kxa), [qtr] "v"(qtr), [eo] "v"(eo), [ew] "v"(ew), [lim] "v"(lim), [rs] "s"(rs), [so] "s"(so), [qo] "s"(qo)
        : "s92", "s93", "s94", "s95", "v176", "v177", "v178", "v179", "v180", "v181", "v182", "v183", "v184", "v185", "v186", "v187", "v188", "v189", "v190", "v191", "v192", "v193", "v194", "v195", "v196", "v197", "v198", "v199", "v200", "v201", "v202", "v203", "v204", "v205", "v206", "v207", "v208", "v209", "v210", "v211", "v212", "v213", "v214", "v215", "v216", "v217", "v218", "v219", "v220", "v221", "v222", "v223", "v224", "v225", "v226", "v227", "v228", "v229", "v230", "v231", "v232", "v233", "v234", "v235", "v236", "v237", "v238", "v239", "v240", "v241", "v242", "v243", "v244", "v245", "v246", "v247", "v248", "v249", "v250", "v251", "v252", "v253", "v254", "v255", "vcc", "scc", "memory");
}

__device__ __forceinline__ void s_f(const u32x32 (&W)[5], u32& rnd, u32 va, u32 sidx, u32 ow, u32 lb, u32& bad, u32x4 ones, u32 qtr, u32 ktr, u32 er, u32 po, float c1, float ind0, float ind1, u32x4 rs, u32 so, float ce, float big STP) {
    asm volatile(
        "s_add_u32 s92, %[so], 0x1000\n\t"
        "s_add_u32 s93, %[so], 0x4000\n\t"
        "s_add_u32 s94, %[so], 0x5000\n\t"
        "Ltop%=:\n\t"
        "v_mov_b64 v[248:249], 0\n\t"
        "v_mov_b64 v[250:251], 0\n\t"
        "v_mov_b64 v[252:253], 0\n\t"
        "v_mov_b64 v[254:255], 0\n\t"
        "v_mov_b32 v200, 0xffbfffbf\n\t"
        "v_mov_b32 v206, %[lb]\n\t"
        "ds_write_b32 %[ow], v200\n\t"
        "ds_write_b32 %[ow], v200 offset:3072\n\t"
        "s_add_u32 %[rnd], %[rnd], 1\n\t"
        "s_setprio 3\n\t"
        "s_mov_b32 %[bad], 2\n\t"
        "v_mov_b32 v197, 0\n\t"
        "s_waitcnt lgkmcnt(0)\n\t"
        "s_barrier\n\t"
        "ds_read_b128 v[208:211], %[va] offset:0\n\t"
        "ds_read_b128 v[212:215], %[va] offset:1024\n\t"
        "ds_read_b128 v[216:219], %[va] offset:2048\n\t"
        "ds_read_b128 v[220:223], %[va] offset:3072\n\t"
        "ds_read_b128 v[224:227], %[va] offset:4096\n\t"
        "ds_read_b128 v[228:231], %[va] offset:5120\n\t"
        "s_waitcnt lgkmcnt(5)\n\t"
        "v_smfmac_f32_16x16x64_bf16 v[248:251], v[208:211], v[16:23], %[sidx]\n\t"
        "ds_read_b128 v[232:235], %[va] offset:6144\n\t"
        "v_smfmac_f32_16x16x64_bf16 v[252:255], v[208:211], v[96:103], %[sidx]\n\t"
        "s_waitcnt lgkmcnt(5)\n\t"
        "v_smfmac_f32_16x16x64_bf16 v[248:251], v[212:215], v[24:31], %[sidx]\n\t"
        "ds_read_b128 v[236:239], %[va] offset:7168\n\t"
        "v_smfmac_f32_16x16x64_bf16 v[252:255], v[212:215], v[104:111], %[sidx]\n\t"
        "s_waitcnt lgkmcnt(5)\n\t"
        "v_smfmac_f32_16x16x64_bf16 v[248:251], v[216:219], v[32:39], %[sidx]\n\t"
        "ds_read_b128 v[240:243], %[va] offset:8192\n\t"
        "v_smfmac_f32_16x16x64_bf16 v[252:255], v[216:219], v[112:119], %[sidx]\n\t"
        "s_waitcnt lgkmcnt(5)\n\t"
        "v_smfmac_f32_16x16x64_bf16 v[248:251], v[220:223], v[40:47], %[sidx]\n\t"
        "ds_read_b128 v[244:247], %[va] offset:10240\n\t"
        "v_smfmac_f32_16x16x64_bf16 v[252:255], v[220:223], v[120:127], %[sidx]\n\t"
        "s_waitcnt lgkmcnt(5)\n\t"
        "v_smfmac_f32_16x16x64_bf16 v[248:251], v[224:227], v[48:55], %[sidx]\n\t"
        "s_waitcnt lgkmcnt(4)\n\t"
        "v_smfmac_f32_16x16x64_bf16 v[248:251], v[228:231], v[56:63], %[sidx]\n\t"
        "s_waitcnt lgkmcnt(3)\n\t"
        "v_smfmac_f32_16x16x64_bf16 v[248:251], v[232:235], v[64:71], %[sidx]\n\t"
        "s_waitcnt lgkmcnt(2)\n\t"
        "v_smfmac_f32_16x16x64_bf16 v[248:251], v[236:239], v[72:79], %[sidx]\n\t"
        "s_waitcnt lgkmcnt(1)\n\t"
        "v_smfmac_f32_16x16x64_bf16 v[248:251], v[240:243], v[80:87], %[sidx]\n\t"
        "s_waitcnt lgkmcnt(0)\n\t"
        "v_smfmac_f32_16x16x64_bf16 v[248:251], v[244:247], v[88:95], %[sidx]\n\t"
        "v_smfmac_f32_16x16x64_bf16 v[252:255], v[224:227], v[128:135], %[sidx]\n\t"
        "v_smfmac_f32_16x16x64_bf16 v[252:255], v[228:231], v[136:143], %[sidx]\n\t"
        "v_add_f32 v248, v248, v250\n\t"
        "v_smfmac_f32_16x16x64_bf16 v[252:255], v[232:235], v[144:151], %[sidx]\n\t"
        "v_add_f32 v249, v249, v251\n\t"
        "v_smfmac_f32_16x16x64_bf16 v[252:255], v[236:239], v[152:159], %[sidx]\n\t"
        "v_cvt_pk_bf16_f32 v200, v248, v249\n\t"
        "v_smfmac_f32_16x16x64_bf16 v[252:255], v[240:243], v[160:167], %[sidx]\n\t"
        "ds_write_b32 %[ow], v200\n\t"
        "v_smfmac_f32_16x16x64_bf16 v[252:255], v[244:247], v[168:175], %[sidx]\n\t"
        "v_cmp_u_f32 vcc, v248, v249\n\t"
        "s_nop 6\n\t"
        "s_cbranch_vccnz Lnan%=\n\t"
        "Lok%=:\n\t"
        "v_add_f32 v252, v252, v254\n\t"
        "v_add_f32 v253, v253, v255\n\t"
        "v_cvt_pk_bf16_f32 v201, v252, v253\n\t"
        "ds_write_b32 %[ow], v201 offset:3072\n\t"
        "s_waitcnt lgkmcnt(0)\n\t"
        "s_nop 15\n\t"
        "ds_read_b64_tr_b16 v[180:181], %[qtr] offset:0\n\t"
        "ds_read_b64_tr_b16 v[182:183], %[qtr] offset:768\n\t"
        "ds_read_b64_tr_b16 v[184:185], %[qtr] offset:1536\n\t"
        "ds_read_b64_tr_b16 v[186:187], %[qtr] offset:2304\n\t"
        "ds_read_b64_tr_b16 v[188:189], %[qtr] offset:3072\n\t"
        "ds_read_b64_tr_b16 v[190:191], %[qtr] offset:3840\n\t"
        "ds_read_b64_tr_b16 v[192:193], %[qtr] offset:4608\n\t"
        "ds_read_b64_tr_b16 v[194:195], %[qtr] offset:5376\n\t"
        "s_nop 15\n\t"
        "s_nop 7\n\t"
        "ds_read_b64_tr_b16 v[224:225], %[ktr] offset:0\n\t"
        "ds_read_b64_tr_b16 v[226:227], %[ktr] offset:768\n\t"
        "ds_read_b64_tr_b16 v[228:229], %[ktr] offset:1536\n\t"
        "ds_read_b64_tr_b16 v[230:231], %[ktr] offset:2304\n\t"
        "s_nop 7\n\t"
        "ds_read_b64_tr_b16 v[232:233], %[ktr] offset:4608\n\t"
        "ds_read_b64_tr_b16 v[234:235], %[ktr] offset:5376\n\t"
        "s_waitcnt lgkmcnt(4)\n\t"
        "v_mfma_f32_16x16x32_bf16 v[248:251], v[224:227], v[180:183], 0\n\t"
        "s_waitcnt lgkmcnt(2)\n\t"
        "v_mfma_f32_16x16x32_bf16 v[248:251], v[228:231], v[184:187], v[248:251]\n\t"
        "s_waitcnt lgkmcnt(0)\n\t"
        "v_mfma_f32_16x16x32_bf16 v[248:251], v[232:235], v[192:195], v[248:251]\n\t"
        "s_barrier\n\t"
        "Lre%=:\n\t"
        "ds_read_b64_tr_b16 v[208:209], %[ktr] offset:3072\n\t"
        "ds_read_b64_tr_b16 v[210:211], %[ktr] offset:3840\n\t"
        "ds_read_b128 v[240:243], %[er]\n\t"
        "ds_read_b32 v207, v206 offset:25104\n\t"
        "s_waitcnt lgkmcnt(2)\n\t"
        "v_mfma_f32_16x16x32_bf16 v[248:251], v[208:211], v[188:191], v[248:251]\n\t"
        "s_waitcnt lgkmcnt(1)\n\t"
        "v_mfma_f32_16x16x32_bf16 v[244:247], v[176:179], v[240:243], 0\n\t"
        "s_nop 3\n\t"
        "v_mul_f32 v248, %[ce], v248\n\t"
        "v_mul_f32 v249, %[ce], v249\n\t"
        "v_exp_f32 v250, v248\n\t"
        "v_exp_f32 v251, v249\n\t"
        "s_nop 0\n\t"
        "v_cvt_pk_bf16_f32 v196, v250, v251\n\t"
        "v_fma_f32 v250, v250, %[c1], %[ind0]\n\t"
        "v_fma_f32 v251, v251, %[c1], %[ind1]\n\t"
        "v_mfma_f32_16x16x16_bf16 v[244:247], v[176:177], v[196:197], v[244:247]\n\t"
        "s_nop 7\n\t"
        "v_rcp_f32 v246, v244\n\t"
        "v_cmpx_gt_f32 vcc, %[big], v244\n\t"
        "v_pk_mul_f32 v[250:251], v[250:251], v[246:247] op_sel_hi:[1,0]\n\t"
        "v_cvt_pk_bf16_f32 v250, v250, v251\n\t"
        "buffer_store_dword v250, %[po], %[rs], %[so] offen sc1\n\t"
        "buffer_store_dword v250, %[po], %[rs], s92 offen sc1\n\t"
        "buffer_store_dword v250, %[po], %[rs], s93 offen sc1\n\t"
        "buffer_store_dword v250, %[po], %[rs], s94 offen sc1\n\t"
        "s_mov_b64 exec, -1\n\t"
        "s_cmp_eq_u32 %[bad], 1\n\t"
        "s_cbranch_scc1 Ltw%=\n\t"
        "s_cmp_lg_u64 vcc, -1\n\t"
        "s_cselect_b32 %[bad], 1, 0\n\t"
        "s_waitcnt lgkmcnt(0)\n\t"
        "v_cmp_eq_u32 vcc, %[rnd], v207\n\t"
        "s_barrier\n\t"
        "s_cbranch_vccnz Ltop%=\n\t"
        "s_cbranch_scc0 Lend%=\n\t"
        "ds_read_b64_tr_b16 v[180:181], %[qtr] offset:0\n\t"
        "ds_read_b64_tr_b16 v[182:183], %[qtr] offset:768\n\t"
        "ds_read_b64_tr_b16 v[184:185], %[qtr] offset:1536\n\t"
        "ds_read_b64_tr_b16 v[186:187], %[qtr] offset:2304\n\t"
        "ds_read_b64_tr_b16 v[188:189], %[qtr] offset:3072\n\t"
        "ds_read_b64_tr_b16 v[190:191], %[qtr] offset:3840\n\t"
        "ds_read_b64_tr_b16 v[192:193], %[qtr] offset:4608\n\t"
        "ds_read_b64_tr_b16 v[194:195], %[qtr] offset:5376\n\t"
        "ds_read_b64_tr_b16 v[224:225], %[ktr] offset:0\n\t"
        "ds_read_b64_tr_b16 v[226:227], %[ktr] offset:768\n\t"
        "ds_read_b64_tr_b16 v[228:229], %[ktr] offset:1536\n\t"
        "ds_read_b64_tr_b16 v[230:231], %[ktr] offset:2304\n\t"
        "ds_read_b64_tr_b16 v[232:233], %[ktr] offset:4608\n\t"
        "ds_read_b64_tr_b16 v[234:235], %[ktr] offset:5376\n\t"
        "s_waitcnt lgkmcnt(0)\n\t"
        "v_mfma_f32_16x16x32_bf16 v[248:251], v[224:227], v[180:183], 0\n\t"
        "v_mfma_f32_16x16x32_bf16 v[248:251], v[228:231], v[184:187], v[248:251]\n\t"
        "v_mfma_f32_16x16x32_bf16 v[248:251], v[232:235], v[192:195], v[248:251]\n\t"
        "s_branch Lre%=\n\t"
        "Lnan%=:\n\t"
        "v_max_u32 v207, v208, v209\n\t"
        "v_max3_u32 v207, v207, v210, v211\n\t"
        "v_max3_u32 v207, v207, v212, v213\n\t"
        "v_max3_u32 v207, v207, v214, v215\n\t"
        "v_max3_u32 v207, v207, v216, v217\n\t"
        "v_max3_u32 v207, v207, v218, v219\n\t"
        "v_max3_u32 v207, v207, v220, v221\n\t"
        "v_max3_u32 v207, v207, v222, v223\n\t"
        "v_max3_u32 v207, v207, v224, v225\n\t"
        "v_max3_u32 v207, v207, v226, v227\n\t"
        "v_max3_u32 v207, v207, v228, v229\n\t"
        "v_max3_u32 v207, v207, v230, v231\n\t"
        "v_max3_u32 v207, v207, v232, v233\n\t"
        "v_max3_u32 v207, v207, v234, v235\n\t"
        "v_max3_u32 v207, v207, v236, v237\n\t"
        "v_max3_u32 v207, v207, v238, v239\n\t"
        "v_max3_u32 v207, v207, v240, v241\n\t"
        "v_max3_u32 v207, v207, v242, v243\n\t"
        "v_max3_u32 v207, v207, v244, v245\n\t"
        "v_max3_u32 v207, v207, v246, v247\n\t"
        "v_cmp_eq_u32 vcc, 0xffbfffbf, v207\n\t"
        "v_mov_b32 v207, %[rnd]\n\t"
        "s_cbranch_vccz Lok%=\n\t"
        "ds_write_b32 v206, v207 offset:25104\n\t"
        "s_waitcnt lgkmcnt(0)\n\t"
        "s_barrier\n\t"
        "s_barrier\n\t"
        "s_branch Ltop%=\n\t"
        "Ltw%=:\n\t"
        "s_cmp_lg_u64 vcc, -1\n\t"
        "s_cselect_b32 %[bad], 1, 0\n\t"
        "s_waitcnt lgkmcnt(0)\n\t"
        "Lend%=:\n\t"
        "s_setprio 0\n\t"
        : [rnd] "+&s"(rnd), [bad] "=&s"(bad)
        : [w0] "{v[16:47]}"(W[0]), [w1] "{v[48:79]}"(W[1]), [w2] "{v[80:111]}"(W[2]), [w3] "{v[112:143]}"(W[3]), [w4] "{v[144:175]}"(W[4]), [va] "v"(va), [sidx] "v"(sidx), [ow] "v"(ow), [lb] "s"(lb), [ones] "{v[176:179]}"(ones), [qtr] "v"(qtr), [ktr] "v"(ktr), [er] "v"(er), [po] "v"(po), [c1] "v"(c1), [ind0] "v"(ind0), [ind1] "v"(ind1), [rs] "s"(rs), [so] "s"(so), [ce] "s"(ce), [big] "s"(big)
        : "s92", "s93", "s94", "v180", "v181", "v182", "v183", "v184", "v185", "v186", "v187", "v188", "v189", "v190", "v191", "v192", "v193", "v194", "v195", "v196", "v197", "v198", "v199", "v200", "v201", "v202", "v203", "v204", "v205", "v206", "v207", "v208", "v209", "v210", "v211", "v212", "v213", "v214", "v215", "v216", "v217", "v218", "v219", "v220", "v221", "v222", "v223", "v224", "v225", "v226", "v227", "v228", "v229", "v230", "v231", "v232", "v233", "v234", "v235", "v236", "v237", "v238", "v239", "v240", "v241", "v242", "v243", "v244", "v245", "v246", "v247", "v248", "v249", "v250", "v251", "v252", "v253", "v254", "v255", "vcc", "scc", "memory");
}

__device__ __forceinline__ void s_m(const u32x32 (&W)[5], u32& rnd, u32 va, u32 sidx, u32 ow, u32 lb, u32& left, u32& c0, u32& r0, u32& t1, u32& t3, u32 tg, u32 kr, u32 vm, u32 so, u32 md, u32x4 rs, u32 poll, u32 tv, u32 tsr STP) {
    asm volatile(
        "s_mov_b32 s94, m0\n\t"
        "s_add_u32 s92, %[so], 0x8000\n\t"
        "s_add_u32 s93, %[so], 0x10000\n\t"
        "s_mov_b32 m0, %[md]\n\t"
        "s_setprio 3\n\t"
        "s_mov_b32 s96, %[left]\n\t"
        "s_cmp_eq_u32 %[poll], 0\n\t"
        "s_cbranch_scc1 Ltop%=\n\t"
        "Lda_%=:\n\t"
        "s_memrealtime s[90:91]\n\t"
        "s_waitcnt lgkmcnt(0)\n\t"
        "s_mul_i32 s90, s90, 24\n\t"
        "s_sub_u32 s88, %[tg], s90\n\t"
        "s_sub_u32 s89, s88, 620\n\t"
        "s_cmp_gt_u32 s89, 0x4000\n\t"
        "s_cbranch_scc1 Lea_%=\n\t"
        "s_sleep 1\n\t"
        "s_branch Lda_%=\n\t"
        "Lea_%=:\n\t"
        "s_memrealtime s[64:65]\n\t"
        "s_nop 0\n\t"
        "s_memrealtime s[66:67]\n\t"
        "s_nop 0\n\t"
        "s_memrealtime s[68:69]\n\t"
        "s_nop 0\n\t"
        "s_memrealtime s[70:71]\n\t"
        "s_nop 0\n\t"
        "s_memrealtime s[72:73]\n\t"
        "s_nop 0\n\t"
        "s_memrealtime s[74:75]\n\t"
        "s_memtime s[90:91]\n\t"
        "s_memrealtime s[76:77]\n\t"
        "s_nop 0\n\t"
        "s_memrealtime s[78:79]\n\t"
        "s_nop 0\n\t"
        "s_memrealtime s[80:81]\n\t"
        "s_nop 0\n\t"
        "s_memrealtime s[82:83]\n\t"
        "s_nop 0\n\t"
        "s_memrealtime s[84:85]\n\t"
        "s_nop 0\n\t"
        "s_memrealtime s[86:87]\n\t"
        "s_nop 0\n\t"
        "s_nop 7\n\t"
        "s_waitcnt lgkmcnt(0)\n\t"
        "s_add_u32 s64, s64, s66\n\t"
        "s_add_u32 s64, s64, s68\n\t"
        "s_add_u32 s64, s64, s70\n\t"
        "s_add_u32 s64, s64, s72\n\t"
        "s_add_u32 s64, s64, s74\n\t"
        "s_add_u32 s64, s64, s76\n\t"
        "s_add_u32 s64, s64, s78\n\t"
        "s_add_u32 s64, s64, s80\n\t"
        "s_add_u32 s64, s64, s82\n\t"
        "s_add_u32 s64, s64, s84\n\t"
        "s_add_u32 s64, s64, s86\n\t"
        "s_lshl_b32 s64, s64, 1\n\t"
        "s_mov_b32 %[c0], s90\n\t"
        "s_mov_b32 %[r0], s64\n\t"
        "s_sub_u32 s88, %[tg], s64\n\t"
        "s_mul_i32 s88, s88, %[kr]\n\t"
        "s_ashr_i32 s88, s88, 12\n\t"
        "s_sub_u32 s88, s88, 244\n\t"
        "s_nop 1\n\t"
        "s_max_i32 s88, s88, 0\n\t"
        "s_min_i32 s88, s88, 384\n\t"
        "s_andn2_b32 s88, s88, 3\n\t"
        "s_getpc_b64 s[90:91]\n\t"
        "Lga_%=:\n\t"
        "s_sub_u32 s90, s90, s88\n\t"
        "s_subb_u32 s91, s91, 0\n\t"
        "s_add_u32 s90, s90, Lza_%=-Lga_%=\n\t"
        "s_addc_u32 s91, s91, 0\n\t"
        "s_setpc_b64 s[90:91]\n\t"
        "s_nop 0\n\ts_nop 0\n\ts_nop 0\n\ts_nop 0\n\t"
        "s_nop 0\n\ts_nop 0\n\ts_nop 0\n\ts_nop 0\n\t"
        "s_nop 0\n\ts_nop 0\n\ts_nop 0\n\ts_nop 0\n\t"
        "s_nop 0\n\ts_nop 0\n\ts_nop 0\n\ts_nop 0\n\t"
        "s_nop 0\n\ts_nop 0\n\ts_nop 0\n\ts_nop 0\n\t"
        "s_nop 0\n\ts_nop 0\n\ts_nop 0\n\ts_nop 0\n\t"
        "s_nop 0\n\ts_nop 0\n\ts_nop 0\n\ts_nop 0\n\t"
        "s_nop 0\n\ts_nop 0\n\ts_nop 0\n\ts_nop 0\n\t"
        "s_nop 0\n\ts_nop 0\n\ts_nop 0\n\ts_nop 0\n\t"
        "s_nop 0\n\ts_nop 0\n\ts_nop 0\n\ts_nop 0\n\t"
        "s_nop 0\n\ts_nop 0\n\ts_nop 0\n\ts_nop 0\n\t"
        "s_nop 0\n\ts_nop 0\n\ts_nop 0\n\ts_nop 0\n\t"
        "s_nop 0\n\ts_nop 0\n\ts_nop 0\n\ts_nop 0\n\t"
        "s_nop 0\n\ts_nop 0\n\ts_nop 0\n\ts_nop 0\n\t"
        "s_nop 0\n\ts_nop 0\n\ts_nop 0\n\ts_nop 0\n\t"
        "s_nop 0\n\ts_nop 0\n\ts_nop 0\n\ts_nop 0\n\t"
        "s_nop 0\n\ts_nop 0\n\ts_nop 0\n\ts_nop 0\n\t"
        "s_nop 0\n\ts_nop 0\n\ts_nop 0\n\ts_nop 0\n\t"
        "s_nop 0\n\ts_nop 0\n\ts_nop 0\n\ts_nop 0\n\t"
        "s_nop 0\n\ts_nop 0\n\ts_nop 0\n\ts_nop 0\n\t"
        "s_nop 0\n\ts_nop 0\n\ts_nop 0\n\ts_nop 0\n\t"
        "s_nop 0\n\ts_nop 0\n\ts_nop 0\n\ts_nop 0\n\t"
        "s_nop 0\n\ts_nop 0\n\ts_nop 0\n\ts_nop 0\n\t"
        "s_nop 0\n\ts_nop 0\n\ts_nop 0\n\ts_nop 0\n\t"
        "Lza_%=:\n\t"
        "buffer_load_dwordx4 %[vm], %[rs], %[so] offen offset:0 sc1 lds\n\t"
        "buffer_load_dwordx4 %[vm], %[rs], %[so] offen offset:1024 sc1 lds\n\t"
        "buffer_load_dwordx4 %[vm], %[rs], %[so] offen offset:2048 sc1 lds\n\t"
        "buffer_load_dwordx4 %[vm], %[rs], %[so] offen offset:3072 sc1 lds\n\t"
        "s_add_u32 m0, m0, 0x1000\n\t"
        "s_nop 0\n\t"
        "buffer_load_dwordx4 %[vm], %[rs], s92 offen offset:0 sc1 lds\n\t"
        "buffer_load_dwordx4 %[vm], %[rs], s92 offen offset:1024 sc1 lds\n\t"
        "buffer_load_dwordx4 %[vm], %[rs], s92 offen offset:2048 sc1 lds\n\t"
        "buffer_load_dwordx4 %[vm], %[rs], s92 offen offset:3072 sc1 lds\n\t"
        "s_add_u32 m0, m0, 0x1000\n\t"
        "s_nop 0\n\t"
        "buffer_load_dwordx4 %[vm], %[rs], s93 offen offset:0 sc1 lds\n\t"
        "buffer_load_dwordx4 %[vm], %[rs], s93 offen offset:2048 sc1 lds\n\t"
        "s_barrier\n\t"
        "Ltop%=:\n\t"
        "v_mov_b64 v[248:249], 0\n\t"
        "v_mov_b64 v[250:251], 0\n\t"
        "v_mov_b64 v[252:253], 0\n\t"
        "v_mov_b64 v[254:255], 0\n\t"
        "v_mov_b32 v200, 0xffbfffbf\n\t"
        "v_mov_b32 v206, %[lb]\n\t"
        "ds_write_b32 %[ow], v200\n\t"
        "ds_write_b32 %[ow], v200 offset:3072\n\t"
        "s_add_u32 %[rnd], %[rnd], 1\n\t"
        "s_setprio 3\n\t"
        "s_mov_b32 s95, 0\n\t"
        "s_waitcnt vmcnt(0)\n\t"
        "s_waitcnt lgkmcnt(0)\n\t"
        "s_barrier\n\t"
        "ds_read_b128 v[208:211], %[va] offset:0\n\t"
        "ds_read_b128 v[212:215], %[va] offset:1024\n\t"
        "ds_read_b128 v[216:219], %[va] offset:2048\n\t"
        "ds_read_b128 v[220:223], %[va] offset:3072\n\t"
        "ds_read_b128 v[224:227], %[va] offset:4096\n\t"
        "ds_read_b128 v[228:231], %[va] offset:5120\n\t"
        "s_waitcnt lgkmcnt(5)\n\t"
        "v_smfmac_f32_16x16x64_bf16 v[248:251], v[208:211], v[16:23], %[sidx]\n\t"
        "ds_read_b128 v[232:235], %[va] offset:6144\n\t"
        "v_smfmac_f32_16x16x64_bf16 v[252:255], v[208:211], v[96:103], %[sidx]\n\t"
        "s_waitcnt lgkmcnt(5)\n\t"
        "v_smfmac_f32_16x16x64_bf16 v[248:251], v[212:215], v[24:31], %[sidx]\n\t"
        "ds_read_b128 v[236:239], %[va] offset:7168\n\t"
        "v_smfmac_f32_16x16x64_bf16 v[252:255], v[212:215], v[104:111], %[sidx]\n\t"
        "s_waitcnt lgkmcnt(5)\n\t"
        "v_smfmac_f32_16x16x64_bf16 v[248:251], v[216:219], v[32:39], %[sidx]\n\t"
        "ds_read_b128 v[240:243], %[va] offset:8192\n\t"
        "v_smfmac_f32_16x16x64_bf16 v[252:255], v[216:219], v[112:119], %[sidx]\n\t"
        "s_waitcnt lgkmcnt(5)\n\t"
        "v_smfmac_f32_16x16x64_bf16 v[248:251], v[220:223], v[40:47], %[sidx]\n\t"
        "ds_read_b128 v[244:247], %[va] offset:10240\n\t"
        "v_smfmac_f32_16x16x64_bf16 v[252:255], v[220:223], v[120:127], %[sidx]\n\t"
        "s_waitcnt lgkmcnt(5)\n\t"
        "v_smfmac_f32_16x16x64_bf16 v[248:251], v[224:227], v[48:55], %[sidx]\n\t"
        "s_waitcnt lgkmcnt(4)\n\t"
        "v_smfmac_f32_16x16x64_bf16 v[248:251], v[228:231], v[56:63], %[sidx]\n\t"
        "s_waitcnt lgkmcnt(3)\n\t"
        "v_smfmac_f32_16x16x64_bf16 v[248:251], v[232:235], v[64:71], %[sidx]\n\t"
        "s_waitcnt lgkmcnt(2)\n\t"
        "v_smfmac_f32_16x16x64_bf16 v[248:251], v[236:239], v[72:79], %[sidx]\n\t"
        "s_waitcnt lgkmcnt(1)\n\t"
        "v_smfmac_f32_16x16x64_bf16 v[248:251], v[240:243], v[80:87], %[sidx]\n\t"
        "s_waitcnt lgkmcnt(0)\n\t"
        "v_smfmac_f32_16x16x64_bf16 v[248:251], v[244:247], v[88:95], %[sidx]\n\t"
        "v_smfmac_f32_16x16x64_bf16 v[252:255], v[224:227], v[128:135], %[sidx]\n\t"
        "v_smfmac_f32_16x16x64_bf16 v[252:255], v[228:231], v[136:143], %[sidx]\n\t"
        "v_add_f32 v248, v248, v250\n\t"
        "v_smfmac_f32_16x16x64_bf16 v[252:255], v[232:235], v[144:151], %[sidx]\n\t"
        "v_add_f32 v249, v249, v251\n\t"
        "v_smfmac_f32_16x16x64_bf16 v[252:255], v[236:239], v[152:159], %[sidx]\n\t"
        "v_cvt_pk_bf16_f32 v200, v248, v249\n\t"
        "v_smfmac_f32_16x16x64_bf16 v[252:255], v[240:243], v[160:167], %[sidx]\n\t"
        "ds_write_b32 %[ow], v200\n\t"
        "v_smfmac_f32_16x16x64_bf16 v[252:255], v[244:247], v[168:175], %[sidx]\n\t"
        "v_cmp_u_f32 vcc, v248, v249\n\t"
        "s_nop 6\n\t"
        "s_cbranch_vccnz Lnan%=\n\t"
        "Lok%=:\n\t"
        "v_add_f32 v252, v252, v254\n\t"
        "v_add_f32 v253, v253, v255\n\t"
        "v_cvt_pk_bf16_f32 v201, v252, v253\n\t"
        "ds_write_b32 %[ow], v201 offset:3072\n\t"
        "s_waitcnt lgkmcnt(0)\n\t"
        "s_cmp_lg_u32 s96, %[left]\n\t"
        "s_cbranch_scc1 Lv%=\n\t"
        "s_buffer_load_dwordx4 s[64:67], %[rs], %[tsr] offset:0x0\n\t"
        "s_buffer_load_dwordx4 s[68:71], %[rs], %[tsr] offset:0x80\n\t"
        "s_buffer_load_dwordx4 s[72:75], %[rs], %[tsr] offset:0x100\n\t"
        "s_buffer_load_dwordx4 s[76:79], %[rs], %[tsr] offset:0x180\n\t"
        "s_barrier\n\t"
        "s_setprio 0\n\t"
        "ds_read_b32 v207, v206 offset:25104\n\t"
        "s_waitcnt lgkmcnt(0)\n\t"
        "v_cmp_eq_u32 vcc, %[rnd], v207\n\t"
        "s_barrier\n\t"
        "s_cbranch_vccnz Lck%=\n\t"
        "s_xor_b32 s67, s67, %[t3]\n\t"
        "s_xor_b32 s71, s71, %[t3]\n\t"
        "s_xor_b32 s75, s75, %[t3]\n\t"
        "s_xor_b32 s79, s79, %[t3]\n\t"
        "s_sub_u32 s69, s69, s65\n\t"
        "s_sub_u32 s73, s73, s65\n\t"
        "s_sub_u32 s77, s77, s65\n\t"
        "s_max_i32 s69, s69, s73\n\t"
        "s_max_i32 s77, s77, 0\n\t"
        "s_max_i32 s69, s69, s77\n\t"
        "s_add_u32 s65, s65, s69\n\t"
        "s_or_b32 s66, s67, s71\n\t"
        "s_or_b32 s70, s75, s79\n\t"
        "s_or_b32 s70, s70, s66\n\t"
        "s_cmp_eq_u32 s70, 0\n\t"
        "s_cselect_b32 %[t3], 1, 0\n\t"
        "s_mov_b32 %[t1], s65\n\t"
        "s_branch Lend%=\n\t"
        "Lv%=:\n\t"
        "s_barrier\n\t"
        "buffer_load_dwordx4 v[196:199], %[tv], %[rs], %[tsr] offen sc1\n\t"
        "s_setprio 0\n\t"
        "ds_read_b32 v207, v206 offset:25104\n\t"
        "s_waitcnt lgkmcnt(0)\n\t"
        "v_cmp_eq_u32 vcc, %[rnd], v207\n\t"
        "s_barrier\n\t"
        "s_cbranch_vccnz Lck%=\n\t"
        "s_sub_u32 s97, %[r0], 0x100000\n\t"
        "s_waitcnt vmcnt(0)\n\t"
        "v_cmp_eq_u32 vcc, %[t3], v199\n\t"
        "v_subrev_u32 v196, s97, v196\n\t"
        "v_subrev_u32 v197, s97, v197\n\t"
        "v_cndmask_b32 v196, 0, v196, vcc\n\t"
        "v_cndmask_b32 v197, 0, v197, vcc\n\t"
        "v_cndmask_b32 v198, 0, v198, vcc\n\t"
        "v_max_u32_dpp v196, v196, v196 quad_perm:[1,0,3,2] row_mask:0xf bank_mask:0xf\n\t"
        "v_max_u32_dpp v197, v197, v197 quad_perm:[1,0,3,2] row_mask:0xf bank_mask:0xf\n\t"
        "v_max_u32_dpp v198, v198, v198 quad_perm:[1,0,3,2] row_mask:0xf bank_mask:0xf\n\t"
        "v_max_u32_dpp v196, v196, v196 quad_perm:[2,3,0,1] row_mask:0xf bank_mask:0xf\n\t"
        "v_max_u32_dpp v197, v197, v197 quad_perm:[2,3,0,1] row_mask:0xf bank_mask:0xf\n\t"
        "v_max_u32_dpp v198, v198, v198 quad_perm:[2,3,0,1] row_mask:0xf bank_mask:0xf\n\t"
        "v_max_u32_dpp v196, v196, v196 row_half_mirror row_mask:0xf bank_mask:0xf\n\t"
        "v_max_u32_dpp v197, v197, v197 row_half_mirror row_mask:0xf bank_mask:0xf\n\t"
        "v_max_u32_dpp v198, v198, v198 row_half_mirror row_mask:0xf bank_mask:0xf\n\t"
        "s_lshr_b32 s96, vcc_lo, 4\n\t"
        "v_readfirstlane_b32 %[t1], v197\n\t"
        "s_add_u32 %[t1], %[t1], s97\n\t"
        "s_or_b32 s96, s96, vcc_lo\n\t"
        "s_and_b32 s96, s96, 15\n\t"
        "s_cmp_eq_u32 s96, 15\n\t"
        "s_cselect_b32 %[t3], 1, 0\n\t"
        "s_branch Lend%=\n\t"
        "Lnan%=:\n\t"
        "s_cmp_lg_u32 %[poll], 1\n\t"
        "s_cbranch_scc1 Lok%=\n\t"
        "s_mov_b32 m0, %[lb]\n\t"
        "v_add_u32 v204, %[lb], %[vm]\n\t"
        "buffer_load_dwordx4 %[vm], %[rs], %[so] offen offset:0 sc1 lds\n\t"
        "buffer_load_dwordx4 %[vm], %[rs], %[so] offen offset:1024 sc1 lds\n\t"
        "buffer_load_dwordx4 %[vm], %[rs], %[so] offen offset:2048 sc1 lds\n\t"
        "buffer_load_dwordx4 %[vm], %[rs], %[so] offen offset:3072 sc1 lds\n\t"
        "s_add_u32 m0, m0, 0x1000\n\t"
        "s_nop 0\n\t"
        "buffer_load_dwordx4 %[vm], %[rs], s92 offen offset:0 sc1 lds\n\t"
        "buffer_load_dwordx4 %[vm], %[rs], s92 offen offset:1024 sc1 lds\n\t"
        "buffer_load_dwordx4 %[vm], %[rs], s92 offen offset:2048 sc1 lds\n\t"
        "buffer_load_dwordx4 %[vm], %[rs], s92 offen offset:3072 sc1 lds\n\t"
        "s_add_u32 m0, m0, 0x1000\n\t"
        "s_nop 0\n\t"
        "buffer_load_dwordx4 %[vm], %[rs], s93 offen offset:0 sc1 lds\n\t"
        "buffer_load_dwordx4 %[vm], %[rs], s93 offen offset:2048 sc1 lds\n\t"
        "v_max_u32 v207, v208, v209\n\t"
        "v_max3_u32 v207, v207, v210, v211\n\t"
        "v_max3_u32 v207, v207, v212, v213\n\t"
        "v_max3_u32 v207, v207, v214, v215\n\t"
        "v_max3_u32 v207, v207, v216, v217\n\t"
        "v_max3_u32 v207, v207, v218, v219\n\t"
        "v_max3_u32 v207, v207, v220, v221\n\t"
        "v_max3_u32 v207, v207, v222, v223\n\t"
        "v_max3_u32 v207, v207, v224, v225\n\t"
        "v_max3_u32 v207, v207, v226, v227\n\t"
        "v_max3_u32 v207, v207, v228, v229\n\t"
        "v_max3_u32 v207, v207, v230, v231\n\t"
        "v_max3_u32 v207, v207, v232, v233\n\t"
        "v_max3_u32 v207, v207, v234, v235\n\t"
        "v_max3_u32 v207, v207, v236, v237\n\t"
        "v_max3_u32 v207, v207, v238, v239\n\t"
        "v_max3_u32 v207, v207, v240, v241\n\t"
        "v_max3_u32 v207, v207, v242, v243\n\t"
        "v_max3_u32 v207, v207, v244, v245\n\t"
        "v_max3_u32 v207, v207, v246, v247\n\t"
        "v_cmp_eq_u32 vcc, 0xffbfffbf, v207\n\t"
        "v_mov_b32 v207, %[rnd]\n\t"
        "s_cbranch_vccz Lok%=\n\t"
        "ds_write_b32 v206, v207 offset:25104\n\t"
        "s_mov_b32 s95, 1\n\t"
        "s_waitcnt lgkmcnt(0)\n\t"
        "s_branch Lcv%=\n\t"
        "Lck%=:\n\t"
        "v_add_u32 v204, %[lb], %[vm]\n\t"
        "Lcl%=:\n\t"
        "s_mov_b32 m0, %[lb]\n\t"
        "s_nop 0\n\t"
        "buffer_load_dwordx4 %[vm], %[rs], %[so] offen offset:0 sc1 lds\n\t"
        "buffer_load_dwordx4 %[vm], %[rs], %[so] offen offset:1024 sc1 lds\n\t"
        "buffer_load_dwordx4 %[vm], %[rs], %[so] offen offset:2048 sc1 lds\n\t"
        "buffer_load_dwordx4 %[vm], %[rs], %[so] offen offset:3072 sc1 lds\n\t"
        "s_add_u32 m0, m0, 0x1000\n\t"
        "s_nop 0\n\t"
        "buffer_load_dwordx4 %[vm], %[rs], s92 offen offset:0 sc1 lds\n\t"
        "buffer_load_dwordx4 %[vm], %[rs], s92 offen offset:1024 sc1 lds\n\t"
        "buffer_load_dwordx4 %[vm], %[rs], s92 offen offset:2048 sc1 lds\n\t"
        "buffer_load_dwordx4 %[vm], %[rs], s92 offen offset:3072 sc1 lds\n\t"
        "s_add_u32 m0, m0, 0x1000\n\t"
        "s_nop 0\n\t"
        "buffer_load_dwordx4 %[vm], %[rs], s93 offen offset:0 sc1 lds\n\t"
        "buffer_load_dwordx4 %[vm], %[rs], s93 offen offset:2048 sc1 lds\n\t"
        "Lcv%=:\n\t"
        "s_sub_u32 %[left], %[left], 1\n\t"
        "s_waitcnt vmcnt(9)\n\t"
        "ds_read_b128 v[208:211], v204 offset:0\n\t"
        "s_waitcnt vmcnt(8)\n\t"
        "ds_read_b128 v[212:215], v204 offset:1024\n\t"
        "s_waitcnt vmcnt(7)\n\t"
        "ds_read_b128 v[216:219], v204 offset:2048\n\t"
        "s_waitcnt vmcnt(6)\n\t"
        "ds_read_b128 v[220:223], v204 offset:3072\n\t"
        "s_waitcnt vmcnt(5)\n\t"
        "ds_read_b128 v[224:227], v204 offset:4096\n\t"
        "s_waitcnt vmcnt(4)\n\t"
        "ds_read_b128 v[228:231], v204 offset:5120\n\t"
        "s_waitcnt lgkmcnt(5)\n\t"
        "v_max3_u32 v207, v208, v208, v209\n\t"
        "v_max3_u32 v207, v207, v210, v211\n\t"
        "s_waitcnt vmcnt(3)\n\t"
        "ds_read_b128 v[232:235], v204 offset:6144\n\t"
        "s_waitcnt lgkmcnt(5)\n\t"
        "v_max3_u32 v207, v207, v212, v213\n\t"
        "v_max3_u32 v207, v207, v214, v215\n\t"
        "s_waitcnt vmcnt(2)\n\t"
        "ds_read_b128 v[236:239], v204 offset:7168\n\t"
        "s_waitcnt lgkmcnt(5)\n\t"
        "v_max3_u32 v207, v207, v216, v217\n\t"
        "v_max3_u32 v207, v207, v218, v219\n\t"
        "s_waitcnt vmcnt(1)\n\t"
        "ds_read_b128 v[240:243], v204 offset:8192\n\t"
        "s_waitcnt lgkmcnt(5)\n\t"
        "v_max3_u32 v207, v207, v220, v221\n\t"
        "v_max3_u32 v207, v207, v222, v223\n\t"
        "s_waitcnt vmcnt(0)\n\t"
        "ds_read_b128 v[244:247], v204 offset:10240\n\t"
        "s_waitcnt lgkmcnt(5)\n\t"
        "v_max3_u32 v207, v207, v224, v225\n\t"
        "v_max3_u32 v207, v207, v226, v227\n\t"
        "s_waitcnt lgkmcnt(4)\n\t"
        "v_max3_u32 v207, v207, v228, v229\n\t"
        "v_max3_u32 v207, v207, v230, v231\n\t"
        "s_waitcnt lgkmcnt(3)\n\t"
        "v_max3_u32 v207, v207, v232, v233\n\t"
        "v_max3_u32 v207, v207, v234, v235\n\t"
        "s_waitcnt lgkmcnt(2)\n\t"
        "v_max3_u32 v207, v207, v236, v237\n\t"
        "v_max3_u32 v207, v207, v238, v239\n\t"
        "s_waitcnt lgkmcnt(1)\n\t"
        "v_max3_u32 v207, v207, v240, v241\n\t"
        "v_max3_u32 v207, v207, v242, v243\n\t"
        "s_waitcnt lgkmcnt(0)\n\t"
        "v_max3_u32 v207, v207, v244, v245\n\t"
        "v_max3_u32 v207, v207, v246, v247\n\t"
        "v_cmp_eq_u32 vcc, 0xffbfffbf, v207\n\t"
        "s_cbranch_vccz Lcd%=\n\t"
        "s_cbranch_scc0 Lcl%=\n\t"
        "Lcd%=:\n\t"
        "s_cmp_eq_u32 s95, 0\n\t"
        "s_cbranch_scc1 Lce%=\n\t"
        "s_barrier\n\t"
        "s_barrier\n\t"
        "Lce%=:\n\t"
        "s_cbranch_vccz Ltop%=\n\t"
        "Lend%=:\n\t"
        "s_mov_b32 m0, s94\n\t"
        "s_waitcnt vmcnt(0)\n\t"
        : [rnd] "+&s"(rnd), [c0] "+&s"(c0), [r0] "+&s"(r0), [t1] "+&s"(t1), [t3] "+&s"(t3), [left] "+&s"(left)
        : [w0] "{v[16:47]}"(W[0]), [w1] "{v[48:79]}"(W[1]), [w2] "{v[80:111]}"(W[2]), [w3] "{v[112:143]}"(W[3]), [w4] "{v[144:175]}"(W[4]), [va] "v"(va), [sidx] "v"(sidx), [ow] "v"(ow), [rs] "s"(rs), [poll] "s"(poll), [tg] "s"(tg), [kr] "s"(kr), [tsr] "s"(tsr), [vm] "v"(vm), [so] "s"(so), [lb] "s"(lb), [md] "s"(md), [tv] "v"(tv)
        : "s64", "s65", "s66", "s67", "s68", "s69", "s70", "s71", "s72", "s73", "s74", "s75", "s76", "s77", "s78", "s79", "s80", "s81", "s82", "s83", "s84", "s85", "s86", "s87", "s88", "s89", "s90", "s91", "s92", "s93", "s94", "s95", "s96", "s97", "v196", "v197", "v198", "v199", "v200", "v201", "v202", "v203", "v204", "v205", "v206", "v207", "v208", "v209", "v210", "v211", "v212", "v213", "v214", "v215", "v216", "v217", "v218", "v219", "v220", "v221", "v222", "v223", "v224", "v225", "v226", "v227", "v228", "v229", "v230", "v231", "v232", "v233", "v234", "v235", "v236", "v237", "v238", "v239", "v240", "v241", "v242", "v243", "v244", "v245", "v246", "v247", "v248", "v249", "v250", "v251", "v252", "v253", "v254", "v255", "vcc", "scc", "memory");
}

__device__ __forceinline__ void gather(u32 tg, u32 kr, u32 vm, u32 so, u32 lb, u32x4 rs) {
    asm volatile(
        "s_mov_b32 s94, m0\n\t"
        "s_add_u32 s92, %[so], 0x8000\n\t"
        "s_add_u32 s93, %[so], 0x10000\n\t"
        "s_mov_b32 m0, %[lb]\n\t"
        "Lda_%=:\n\t"
        "s_memrealtime s[90:91]\n\t"
        "s_waitcnt lgkmcnt(0)\n\t"
        "s_mul_i32 s90, s90, 24\n\t"
        "s_sub_u32 s88, %[tg], s90\n\t"
        "s_sub_u32 s89, s88, 620\n\t"
        "s_cmp_gt_u32 s89, 0x4000\n\t"
        "s_cbranch_scc1 Lea_%=\n\t"
        "s_sleep 1\n\t"
        "s_branch Lda_%=\n\t"
        "Lea_%=:\n\t"
        "s_cmp_lt_i32 s88, 120\n\t"
        "s_cbranch_scc1 Lza_%=\n\t"
        "s_memrealtime s[64:65]\n\t"
        "s_nop 0\n\t"
        "s_memrealtime s[66:67]\n\t"
        "s_nop 0\n\t"
        "s_memrealtime s[68:69]\n\t"
        "s_nop 0\n\t"
        "s_memrealtime s[70:71]\n\t"
        "s_nop 0\n\t"
        "s_memrealtime s[72:73]\n\t"
        "s_nop 0\n\t"
        "s_memrealtime s[74:75]\n\t"
        "s_memtime s[90:91]\n\t"
        "s_memrealtime s[76:77]\n\t"
        "s_nop 0\n\t"
        "s_memrealtime s[78:79]\n\t"
        "s_nop 0\n\t"
        "s_memrealtime s[80:81]\n\t"
        "s_nop 0\n\t"
        "s_memrealtime s[82:83]\n\t"
        "s_nop 0\n\t"
        "s_memrealtime s[84:85]\n\t"
        "s_nop 0\n\t"
        "s_memrealtime s[86:87]\n\t"
        "s_nop 0\n\t"
        "s_nop 7\n\t"
        "s_waitcnt lgkmcnt(0)\n\t"
        "s_add_u32 s64, s64, s66\n\t"
        "s_add_u32 s64, s64, s68\n\t"
        "s_add_u32 s64, s64, s70\n\t"
        "s_add_u32 s64, s64, s72\n\t"
        "s_add_u32 s64, s64, s74\n\t"
        "s_add_u32 s64, s64, s76\n\t"
        "s_add_u32 s64, s64, s78\n\t"
        "s_add_u32 s64, s64, s80\n\t"
        "s_add_u32 s64, s64, s82\n\t"
        "s_add_u32 s64, s64, s84\n\t"
        "s_add_u32 s64, s64, s86\n\t"
        "s_lshl_b32 s64, s64, 1\n\t"
        "s_nop 1\n\t"
        "s_sub_u32 s88, %[tg], s64\n\t"
        "s_mul_i32 s88, s88, %[kr]\n\t"
        "s_ashr_i32 s88, s88, 12\n\t"
        "s_sub_u32 s88, s88, 244\n\t"
        "s_nop 1\n\t"
        "s_max_i32 s88, s88, 0\n\t"
        "s_min_i32 s88, s88, 384\n\t"
        "s_andn2_b32 s88, s88, 3\n\t"
        "s_getpc_b64 s[90:91]\n\t"
        "Lga_%=:\n\t"
        "s_sub_u32 s90, s90, s88\n\t"
        "s_subb_u32 s91, s91, 0\n\t"
        "s_add_u32 s90, s90, Lza_%=-Lga_%=\n\t"
        "s_addc_u32 s91, s91, 0\n\t"
        "s_setpc_b64 s[90:91]\n\t"
        "s_nop 0\n\ts_nop 0\n\ts_nop 0\n\ts_nop 0\n\t"
        "s_nop 0\n\ts_nop 0\n\ts_nop 0\n\ts_nop 0\n\t"
        "s_nop 0\n\ts_nop 0\n\ts_nop 0\n\ts_nop 0\n\t"
        "s_nop 0\n\ts_nop 0\n\ts_nop 0\n\ts_nop 0\n\t"
        "s_nop 0\n\ts_nop 0\n\ts_nop 0\n\ts_nop 0\n\t"
        "s_nop 0\n\ts_nop 0\n\ts_nop 0\n\ts_nop 0\n\t"
        "s_nop 0\n\ts_nop 0\n\ts_nop 0\n\ts_nop 0\n\t"
        "s_nop 0\n\ts_nop 0\n\ts_nop 0\n\ts_nop 0\n\t"
        "s_nop 0\n\ts_nop 0\n\ts_nop 0\n\ts_nop 0\n\t"
        "s_nop 0\n\ts_nop 0\n\ts_nop 0\n\ts_nop 0\n\t"
        "s_nop 0\n\ts_nop 0\n\ts_nop 0\n\ts_nop 0\n\t"
        "s_nop 0\n\ts_nop 0\n\ts_nop 0\n\ts_nop 0\n\t"
        "s_nop 0\n\ts_nop 0\n\ts_nop 0\n\ts_nop 0\n\t"
        "s_nop 0\n\ts_nop 0\n\ts_nop 0\n\ts_nop 0\n\t"
        "s_nop 0\n\ts_nop 0\n\ts_nop 0\n\ts_nop 0\n\t"
        "s_nop 0\n\ts_nop 0\n\ts_nop 0\n\ts_nop 0\n\t"
        "s_nop 0\n\ts_nop 0\n\ts_nop 0\n\ts_nop 0\n\t"
        "s_nop 0\n\ts_nop 0\n\ts_nop 0\n\ts_nop 0\n\t"
        "s_nop 0\n\ts_nop 0\n\ts_nop 0\n\ts_nop 0\n\t"
        "s_nop 0\n\ts_nop 0\n\ts_nop 0\n\ts_nop 0\n\t"
        "s_nop 0\n\ts_nop 0\n\ts_nop 0\n\ts_nop 0\n\t"
        "s_nop 0\n\ts_nop 0\n\ts_nop 0\n\ts_nop 0\n\t"
        "s_nop 0\n\ts_nop 0\n\ts_nop 0\n\ts_nop 0\n\t"
        "s_nop 0\n\ts_nop 0\n\ts_nop 0\n\ts_nop 0\n\t"
        "Lza_%=:\n\t"
        "buffer_load_dwordx4 %[vm], %[rs], %[so] offen offset:0 sc1 lds\n\t"
        "buffer_load_dwordx4 %[vm], %[rs], %[so] offen offset:1024 sc1 lds\n\t"
        "buffer_load_dwordx4 %[vm], %[rs], %[so] offen offset:2048 sc1 lds\n\t"
        "buffer_load_dwordx4 %[vm], %[rs], %[so] offen offset:3072 sc1 lds\n\t"
        "s_add_u32 m0, m0, 0x1000\n\t"
        "s_nop 0\n\t"
        "buffer_load_dwordx4 %[vm], %[rs], s92 offen offset:0 sc1 lds\n\t"
        "buffer_load_dwordx4 %[vm], %[rs], s92 offen offset:1024 sc1 lds\n\t"
        "buffer_load_dwordx4 %[vm], %[rs], s92 offen offset:2048 sc1 lds\n\t"
        "buffer_load_dwordx4 %[vm], %[rs], s92 offen offset:3072 sc1 lds\n\t"
        "s_add_u32 m0, m0, 0x1000\n\t"
        "s_nop 0\n\t"
        "buffer_load_dwordx4 %[vm], %[rs], s93 offen offset:0 sc1 lds\n\t"
        "buffer_load_dwordx4 %[vm], %[rs], s93 offen offset:2048 sc1 lds\n\t"
        "s_mov_b32 m0, s94\n\t"
        :
        : [rs] "s"(rs), [tg] "s"(tg), [kr] "s"(kr), [vm] "v"(vm), [so] "s"(so), [lb] "s"(lb)
        : "s64", "s65", "s66", "s67", "s68", "s69", "s70", "s71", "s72", "s73", "s74", "s75", "s76", "s77", "s78", "s79", "s80", "s81", "s82", "s83", "s84", "s85", "s86", "s87", "s88", "s89", "s90", "s91", "s92", "s93", "s94", "vcc", "scc", "memory");
}

__device__ __forceinline__ void ts_look(u32& t1, u32& ok, u32x4 rs, u32 tsr, u32 want) {
    asm volatile(
        "s_nop 4\n\t"
        "s_buffer_load_dwordx4 s[64:67], %[rs], %[tsr] offset:0x0\n\t"
        "s_buffer_load_dwordx4 s[68:71], %[rs], %[tsr] offset:0x80\n\t"
        "s_buffer_load_dwordx4 s[72:75], %[rs], %[tsr] offset:0x100\n\t"
        "s_buffer_load_dwordx4 s[76:79], %[rs], %[tsr] offset:0x180\n\t"
        "s_waitcnt lgkmcnt(0)\n\t"
        "s_xor_b32 s67, s67, %[want]\n\t"
        "s_xor_b32 s71, s71, %[want]\n\t"
        "s_xor_b32 s75, s75, %[want]\n\t"
        "s_xor_b32 s79, s79, %[want]\n\t"
        "s_sub_u32 s69, s69, s65\n\t"
        "s_sub_u32 s73, s73, s65\n\t"
        "s_sub_u32 s77, s77, s65\n\t"
        "s_max_i32 s69, s69, s73\n\t"
        "s_max_i32 s77, s77, 0\n\t"
        "s_max_i32 s69, s69, s77\n\t"
        "s_add_u32 s65, s65, s69\n\t"
        "s_or_b32 s66, s67, s71\n\t"
        "s_or_b32 s70, s75, s79\n\t"
        "s_or_b32 s70, s70, s66\n\t"
        "s_cmp_eq_u32 s70, 0\n\t"
        "s_cselect_b32 %[ok], 15, 0\n\t"
        "s_mov_b32 %[t1], s65\n\t"
        : [t1] "=&s"(t1), [ok] "=&s"(ok)
        : [rs] "s"(rs), [tsr] "s"(tsr), [want] "s"(want)
        : "s64", "s65", "s66", "s67", "s68", "s69", "s70", "s71", "s72", "s73", "s74", "s75", "s76", "s77", "s78", "s79", "vcc", "scc", "memory");
}

__device__ __forceinline__ void pull(u32 vo, u32 tb, u32x4 rs, u32 n, u32 gap, u32 ld) {
    asm volatile(
        "s_mov_b32 s94, m0\n\t"
        "s_mov_b32 m0, %[ld]\n\t"
        "s_mov_b32 s92, 0\n\t"
        "Lp%=:\n\t"
        "v_readlane_b32 s93, %[tb], s92\n\t"
        "s_getpc_b64 s[90:91]\n\t"
        "Lgg_%=:\n\t"
        "s_sub_u32 s90, s90, %[gap]\n\t"
        "s_subb_u32 s91, s91, 0\n\t"
        "s_add_u32 s90, s90, Lzg_%=-Lgg_%=\n\t"
        "s_addc_u32 s91, s91, 0\n\t"
        "s_setpc_b64 s[90:91]\n\t"
        "s_nop 0\n\ts_nop 0\n\ts_nop 0\n\ts_nop 0\n\t"
        "s_nop 0\n\ts_nop 0\n\ts_nop 0\n\ts_nop 0\n\t"
        "s_nop 0\n\ts_nop 0\n\ts_nop 0\n\ts_nop 0\n\t"
        "s_nop 0\n\ts_nop 0\n\ts_nop 0\n\ts_nop 0\n\t"
        "s_nop 0\n\ts_nop 0\n\ts_nop 0\n\ts_nop 0\n\t"
        "s_nop 0\n\ts_nop 0\n\ts_nop 0\n\ts_nop 0\n\t"
        "s_nop 0\n\ts_nop 0\n\ts_nop 0\n\ts_nop 0\n\t"
        "s_nop 0\n\ts_nop 0\n\ts_nop 0\n\ts_nop 0\n\t"
        "s_nop 0\n\ts_nop 0\n\ts_nop 0\n\ts_nop 0\n\t"
        "s_nop 0\n\ts_nop 0\n\ts_nop 0\n\ts_nop 0\n\t"
        "s_nop 0\n\ts_nop 0\n\ts_nop 0\n\ts_nop 0\n\t"
        "s_nop 0\n\ts_nop 0\n\ts_nop 0\n\ts_nop 0\n\t"
        "s_nop 0\n\ts_nop 0\n\ts_nop 0\n\ts_nop 0\n\t"
        "s_nop 0\n\ts_nop 0\n\ts_nop 0\n\ts_nop 0\n\t"
        "s_nop 0\n\ts_nop 0\n\ts_nop 0\n\ts_nop 0\n\t"
        "s_nop 0\n\ts_nop 0\n\ts_nop 0\n\ts_nop 0\n\t"
        "Lzg_%=:\n\t"
        "buffer_load_dword %[vo], %[rs], s93 offen sc1 lds\n\t"
        "s_add_u32 s92, s92, 1\n\t"
        "s_cmp_lt_u32 s92, %[n]\n\t"
        "s_cbranch_scc1 Lp%=\n\t"
        "s_mov_b32 m0, s94\n\t"
        :
        : [vo] "v"(vo), [tb] "v"(tb), [rs] "s"(rs), [n] "s"(n), [gap] "s"(gap), [ld] "s"(ld)
        : "s88", "s89", "s90", "s91", "s92", "s93", "s94", "vcc", "scc", "memory");
}

__device__ __forceinline__ void calib(u32& c0, u32& r0) {
    asm volatile(
        "s_memrealtime s[64:65]\n\t"
        "s_nop 0\n\t"
        "s_memrealtime s[66:67]\n\t"
        "s_nop 0\n\t"
        "s_memrealtime s[68:69]\n\t"
        "s_nop 0\n\t"
        "s_memrealtime s[70:71]\n\t"
        "s_nop 0\n\t"
        "s_memrealtime s[72:73]\n\t"
        "s_nop 0\n\t"
        "s_memrealtime s[74:75]\n\t"
        "s_memtime s[90:91]\n\t"
        "s_memrealtime s[76:77]\n\t"
        "s_nop 0\n\t"
        "s_memrealtime s[78:79]\n\t"
        "s_nop 0\n\t"
        "s_memrealtime s[80:81]\n\t"
        "s_nop 0\n\t"
        "s_memrealtime s[82:83]\n\t"
        "s_nop 0\n\t"
        "s_memrealtime s[84:85]\n\t"
        "s_nop 0\n\t"
        "s_memrealtime s[86:87]\n\t"
        "s_nop 0\n\t"
        "s_nop 7\n\t"
        "s_waitcnt lgkmcnt(0)\n\t"
        "s_add_u32 s64, s64, s66\n\t"
        "s_add_u32 s64, s64, s68\n\t"
        "s_add_u32 s64, s64, s70\n\t"
        "s_add_u32 s64, s64, s72\n\t"
        "s_add_u32 s64, s64, s74\n\t"
        "s_add_u32 s64, s64, s76\n\t"
        "s_add_u32 s64, s64, s78\n\t"
        "s_add_u32 s64, s64, s80\n\t"
        "s_add_u32 s64, s64, s82\n\t"
        "s_add_u32 s64, s64, s84\n\t"
        "s_add_u32 s64, s64, s86\n\t"
        "s_lshl_b32 s64, s64, 1\n\t"
        "s_mov_b32 %[c0], s90\n\t"
        "s_mov_b32 %[r0], s64\n\t"
        : [c0] "=&s"(c0), [r0] "=&s"(r0)
        :
        : "s64", "s65", "s66", "s67", "s68", "s69", "s70", "s71", "s72", "s73", "s74", "s75", "s76", "s77", "s78", "s79", "s80", "s81", "s82", "s83", "s84", "s85", "s86", "s87", "s88", "s89", "s90", "s91", "vcc", "scc", "memory");
}

__device__ __forceinline__ void px_step(u32x4& p0, u32& p1, u32 ka, u32 qa, u32 ma, u32 ktr, float ce) {
    asm volatile(
        "ds_read_b128 v[240:243], %[ma] offset:0\n\t"
        "ds_read_b128 v[244:247], %[ma] offset:1024\n\t"
        "ds_read_b128 v[208:211], %[qa] offset:0\n\t"
        "ds_read_b128 v[224:227], %[ka] offset:0\n\t"
        "ds_read_b128 v[212:215], %[qa] offset:1024\n\t"
        "ds_read_b128 v[228:231], %[ka] offset:1024\n\t"
        "ds_read_b128 v[216:219], %[qa] offset:2048\n\t"
        "ds_read_b128 v[232:235], %[ka] offset:2048\n\t"
        "ds_read_b128 v[220:223], %[qa] offset:3072\n\t"
        "ds_read_b128 v[236:239], %[ka] offset:3072\n\t"
        "s_waitcnt lgkmcnt(6)\n\t"
        "v_mfma_f32_16x16x32_bf16 v[240:243], v[224:227], v[208:211], v[240:243]\n\t"
        "ds_read_b128 v[224:227], %[ka] offset:4096\n\t"
        "s_waitcnt lgkmcnt(5)\n\t"
        "v_mfma_f32_16x16x32_bf16 v[240:243], v[228:231], v[212:215], v[240:243]\n\t"
        "ds_read_b128 v[228:231], %[ka] offset:5120\n\t"
        "s_waitcnt lgkmcnt(4)\n\t"
        "v_mfma_f32_16x16x32_bf16 v[240:243], v[232:235], v[216:219], v[240:243]\n\t"
        "ds_read_b128 v[232:235], %[ka] offset:6144\n\t"
        "s_waitcnt lgkmcnt(3)\n\t"
        "v_mfma_f32_16x16x32_bf16 v[240:243], v[236:239], v[220:223], v[240:243]\n\t"
        "ds_read_b128 v[236:239], %[ka] offset:7168\n\t"
        "s_waitcnt lgkmcnt(3)\n\t"
        "v_mfma_f32_16x16x32_bf16 v[244:247], v[224:227], v[208:211], v[244:247]\n\t"
        "ds_read_b64_tr_b16 v[224:225], %[ktr] offset:0\n\t"
        "ds_read_b64_tr_b16 v[226:227], %[ktr] offset:768\n\t"
        "s_waitcnt lgkmcnt(4)\n\t"
        "v_mfma_f32_16x16x32_bf16 v[244:247], v[228:231], v[212:215], v[244:247]\n\t"
        "ds_read_b64_tr_b16 v[228:229], %[ktr] offset:1536\n\t"
        "ds_read_b64_tr_b16 v[230:231], %[ktr] offset:2304\n\t"
        "s_waitcnt lgkmcnt(5)\n\t"
        "v_mfma_f32_16x16x32_bf16 v[244:247], v[232:235], v[216:219], v[244:247]\n\t"
        "ds_read_b64_tr_b16 v[232:233], %[ktr] offset:3072\n\t"
        "ds_read_b64_tr_b16 v[234:235], %[ktr] offset:3840\n\t"
        "s_waitcnt lgkmcnt(6)\n\t"
        "v_mfma_f32_16x16x32_bf16 v[244:247], v[236:239], v[220:223], v[244:247]\n\t"
        "ds_read_b64_tr_b16 v[236:237], %[ktr] offset:4608\n\t"
        "ds_read_b64_tr_b16 v[238:239], %[ktr] offset:5376\n\t"
        "s_waitcnt lgkmcnt(6)\n\t"
        "v_mfma_f32_16x16x32_bf16 v[248:251], v[224:227], v[208:211], 0\n\t"
        "s_waitcnt lgkmcnt(4)\n\t"
        "v_mfma_f32_16x16x32_bf16 v[248:251], v[228:231], v[212:215], v[248:251]\n\t"
        "s_waitcnt lgkmcnt(2)\n\t"
        "v_mfma_f32_16x16x32_bf16 v[248:251], v[232:235], v[216:219], v[248:251]\n\t"
        "s_waitcnt lgkmcnt(0)\n\t"
        "v_mfma_f32_16x16x32_bf16 v[248:251], v[236:239], v[220:223], v[248:251]\n\t"
        "v_max3_f32 v253, v240, v241, v242\n\t"
        "v_max3_f32 v253, v253, v243, v244\n\t"
        "v_max3_f32 v253, v253, v245, v246\n\t"
        "v_max_f32 v253, v253, v247\n\t"
        "s_nop 3\n\t"
        "v_mul_f32 v248, %[ce], v248\n\t"
        "v_mul_f32 v249, %[ce], v249\n\t"
        "v_max3_f32 v253, v253, v248, v249\n\t"
        "v_mov_b32 v254, v253\n\t"
        "s_nop 1\n\t"
        "v_permlane16_swap_b32 v253, v254\n\t"
        "v_max_f32 v253, v253, v254\n\t"
        "v_mov_b32 v254, v253\n\t"
        "s_nop 1\n\t"
        "v_permlane32_swap_b32 v253, v254\n\t"
        "v_max_f32 v253, v253, v254\n\t"
        "v_sub_f32 v240, v240, v253\n\t"
        "v_sub_f32 v241, v241, v253\n\t"
        "v_sub_f32 v242, v242, v253\n\t"
        "v_sub_f32 v243, v243, v253\n\t"
        "v_sub_f32 v244, v244, v253\n\t"
        "v_sub_f32 v245, v245, v253\n\t"
        "v_sub_f32 v246, v246, v253\n\t"
        "v_sub_f32 v247, v247, v253\n\t"
        "v_sub_f32 v248, v248, v253\n\t"
        "v_sub_f32 v249, v249, v253\n\t"
        "v_exp_f32 v240, v240\n\t"
        "v_exp_f32 v241, v241\n\t"
        "v_exp_f32 v242, v242\n\t"
        "v_exp_f32 v243, v243\n\t"
        "v_exp_f32 v244, v244\n\t"
        "v_exp_f32 v245, v245\n\t"
        "v_exp_f32 v246, v246\n\t"
        "v_exp_f32 v247, v247\n\t"
        "v_exp_f32 v248, v248\n\t"
        "v_exp_f32 v249, v249\n\t"
        "v_pk_add_f32 v[252:253], v[240:241], v[242:243]\n\t"
        "v_pk_add_f32 v[254:255], v[244:245], v[246:247]\n\t"
        "v_pk_add_f32 v[252:253], v[252:253], v[248:249]\n\t"
        "v_pk_add_f32 v[252:253], v[252:253], v[254:255]\n\t"
        "v_add_f32 v252, v252, v253\n\t"
        "v_mov_b32 v253, v252\n\t"
        "s_nop 1\n\t"
        "v_permlane16_swap_b32 v252, v253\n\t"
        "v_add_f32 v252, v252, v253\n\t"
        "v_mov_b32 v253, v252\n\t"
        "s_nop 1\n\t"
        "v_permlane32_swap_b32 v252, v253\n\t"
        "v_add_f32 v252, v252, v253\n\t"
        "v_rcp_f32 v252, v252\n\t"
        "s_nop 0\n\t"
        "v_pk_mul_f32 v[240:241], v[240:241], v[252:253] op_sel_hi:[1,0]\n\t"
        "v_pk_mul_f32 v[242:243], v[242:243], v[252:253] op_sel_hi:[1,0]\n\t"
        "v_pk_mul_f32 v[244:245], v[244:245], v[252:253] op_sel_hi:[1,0]\n\t"
        "v_pk_mul_f32 v[246:247], v[246:247], v[252:253] op_sel_hi:[1,0]\n\t"
        "v_pk_mul_f32 v[248:249], v[248:249], v[252:253] op_sel_hi:[1,0]\n\t"
        "v_cvt_pk_bf16_f32 v192, v240, v241\n\t"
        "v_cvt_pk_bf16_f32 v193, v242, v243\n\t"
        "v_cvt_pk_bf16_f32 v194, v244, v245\n\t"
        "v_cvt_pk_bf16_f32 v195, v246, v247\n\t"
        "v_cvt_pk_bf16_f32 v196, v248, v249\n\t"
        : [p0] "=&{v[192:195]}"(p0), [p1] "=&{v196}"(p1)
        : [ka] "v"(ka), [qa] "v"(qa), [ma] "v"(ma), [ktr] "v"(ktr), [ce] "s"(ce)
        : "v200", "v201", "v202", "v203", "v204", "v205", "v206", "v207", "v208", "v209", "v210", "v211", "v212", "v213", "v214", "v215", "v216", "v217", "v218", "v219", "v220", "v221", "v222", "v223", "v224", "v225", "v226", "v227", "v228", "v229", "v230", "v231", "v232", "v233", "v234", "v235", "v236", "v237", "v238", "v239", "v240", "v241", "v242", "v243", "v244", "v245", "v246", "v247", "v248", "v249", "v250", "v251", "v252", "v253", "v254", "v255", "vcc", "scc", "memory");
}

__device__ __forceinline__ void v_a0(const u32x32 (&W)[5], u32& rnd, u32 va, u32 sidx, u32 vw, u32 lb STP) {
    asm volatile(
        "Ltop%=:\n\t"
        "v_mov_b64 v[248:249], 0\n\t"
        "v_mov_b64 v[250:251], 0\n\t"
        "v_mov_b64 v[252:253], 0\n\t"
        "v_mov_b64 v[254:255], 0\n\t"
        "v_mov_b32 v200, 0xffbfffbf\n\t"
        "v_mov_b32 v206, %[lb]\n\t"
        "ds_write_b32 %[vw], v200\n\t"
        "ds_write_b32 %[vw], v200 offset:1024\n\t"
        "s_add_u32 %[rnd], %[rnd], 1\n\t"
        "s_setprio 3\n\t"
        "s_waitcnt lgkmcnt(0)\n\t"
        "s_barrier\n\t"
        "ds_read_b128 v[208:211], %[va] offset:0\n\t"
        "ds_read_b128 v[212:215], %[va] offset:1024\n\t"
        "ds_read_b128 v[216:219], %[va] offset:2048\n\t"
        "ds_read_b128 v[220:223], %[va] offset:3072\n\t"
        "ds_read_b128 v[224:227], %[va] offset:4096\n\t"
        "ds_read_b128 v[228:231], %[va] offset:5120\n\t"
        "s_waitcnt lgkmcnt(5)\n\t"
        "v_smfmac_f32_16x16x64_bf16 v[248:251], v[208:211], v[16:23], %[sidx]\n\t"
        "ds_read_b128 v[232:235], %[va] offset:6144\n\t"
        "v_smfmac_f32_16x16x64_bf16 v[252:255], v[208:211], v[96:103], %[sidx]\n\t"
        "s_waitcnt lgkmcnt(5)\n\t"
        "v_smfmac_f32_16x16x64_bf16 v[248:251], v[212:215], v[24:31], %[sidx]\n\t"
        "ds_read_b128 v[236:239], %[va] offset:7168\n\t"
        "v_smfmac_f32_16x16x64_bf16 v[252:255], v[212:215], v[104:111], %[sidx]\n\t"
        "s_waitcnt lgkmcnt(5)\n\t"
        "v_smfmac_f32_16x16x64_bf16 v[248:251], v[216:219], v[32:39], %[sidx]\n\t"
        "ds_read_b128 v[240:243], %[va] offset:8192\n\t"
        "s_waitcnt lgkmcnt(5)\n\t"
        "v_smfmac_f32_16x16x64_bf16 v[248:251], v[220:223], v[40:47], %[sidx]\n\t"
        "ds_read_b128 v[244:247], %[va] offset:10240\n\t"
        "s_waitcnt lgkmcnt(5)\n\t"
        "v_smfmac_f32_16x16x64_bf16 v[248:251], v[224:227], v[48:55], %[sidx]\n\t"
        "s_waitcnt lgkmcnt(4)\n\t"
        "v_smfmac_f32_16x16x64_bf16 v[248:251], v[228:231], v[56:63], %[sidx]\n\t"
        "s_waitcnt lgkmcnt(3)\n\t"
        "v_smfmac_f32_16x16x64_bf16 v[248:251], v[232:235], v[64:71], %[sidx]\n\t"
        "s_waitcnt lgkmcnt(2)\n\t"
        "v_smfmac_f32_16x16x64_bf16 v[248:251], v[236:239], v[72:79], %[sidx]\n\t"
        "s_waitcnt lgkmcnt(1)\n\t"
        "v_smfmac_f32_16x16x64_bf16 v[248:251], v[240:243], v[80:87], %[sidx]\n\t"
        "s_waitcnt lgkmcnt(0)\n\t"
        "v_smfmac_f32_16x16x64_bf16 v[248:251], v[244:247], v[88:95], %[sidx]\n\t"
        "v_smfmac_f32_16x16x64_bf16 v[252:255], v[216:219], v[112:119], %[sidx]\n\t"
        "v_smfmac_f32_16x16x64_bf16 v[252:255], v[220:223], v[120:127], %[sidx]\n\t"
        "v_add_f32 v248, v248, v250\n\t"
        "v_smfmac_f32_16x16x64_bf16 v[252:255], v[224:227], v[128:135], %[sidx]\n\t"
        "v_add_f32 v249, v249, v251\n\t"
        "v_smfmac_f32_16x16x64_bf16 v[252:255], v[228:231], v[136:143], %[sidx]\n\t"
        "v_cvt_pk_bf16_f32 v200, v248, v249\n\t"
        "v_smfmac_f32_16x16x64_bf16 v[252:255], v[232:235], v[144:151], %[sidx]\n\t"
        "ds_write_b32 %[vw], v200\n\t"
        "v_smfmac_f32_16x16x64_bf16 v[252:255], v[236:239], v[152:159], %[sidx]\n\t"
        "v_cmp_u_f32 vcc, v248, v249\n\t"
        "v_smfmac_f32_16x16x64_bf16 v[252:255], v[240:243], v[160:167], %[sidx]\n\t"
        "v_smfmac_f32_16x16x64_bf16 v[252:255], v[244:247], v[168:175], %[sidx]\n\t"
        "s_nop 7\n\t"
        "v_add_f32 v252, v252, v254\n\t"
        "v_add_f32 v253, v253, v255\n\t"
        "v_cvt_pk_bf16_f32 v201, v252, v253\n\t"
        "ds_write_b32 %[vw], v201 offset:1024\n\t"
        "s_setprio 0\n\t"
        "s_cbranch_vccnz Lnan%=\n\t"
        "Lok%=:\n\t"
        "s_waitcnt lgkmcnt(0)\n\t"
        "s_barrier\n\t"
        "ds_read_b32 v207, v206 offset:14336\n\t"
        "s_waitcnt lgkmcnt(0)\n\t"
        "v_cmp_eq_u32 vcc, %[rnd], v207\n\t"
        "s_barrier\n\t"
        "s_cbranch_vccnz Ltop%=\n\t"
        "s_branch Lend%=\n\t"
        "Lnan%=:\n\t"
        "v_max_u32 v207, v208, v209\n\t"
        "v_max3_u32 v207, v207, v210, v211\n\t"
        "v_max3_u32 v207, v207, v212, v213\n\t"
        "v_max3_u32 v207, v207, v214, v215\n\t"
        "v_max3_u32 v207, v207, v216, v217\n\t"
        "v_max3_u32 v207, v207, v218, v219\n\t"
        "v_max3_u32 v207, v207, v220, v221\n\t"
        "v_max3_u32 v207, v207, v222, v223\n\t"
        "v_max3_u32 v207, v207, v224, v225\n\t"
        "v_max3_u32 v207, v207, v226, v227\n\t"
        "v_max3_u32 v207, v207, v228, v229\n\t"
        "v_max3_u32 v207, v207, v230, v231\n\t"
        "v_max3_u32 v207, v207, v232, v233\n\t"
        "v_max3_u32 v207, v207, v234, v235\n\t"
        "v_max3_u32 v207, v207, v236, v237\n\t"
        "v_max3_u32 v207, v207, v238, v239\n\t"
        "v_max3_u32 v207, v207, v240, v241\n\t"
        "v_max3_u32 v207, v207, v242, v243\n\t"
        "v_max3_u32 v207, v207, v244, v245\n\t"
        "v_max3_u32 v207, v207, v246, v247\n\t"
        "v_cmp_eq_u32 vcc, 0xffbfffbf, v207\n\t"
        "v_mov_b32 v207, %[rnd]\n\t"
        "s_cbranch_vccz Lok%=\n\t"
        "ds_write_b32 v206, v207 offset:14336\n\t"
        "s_branch Lok%=\n\t"
        "Lend%=:\n\t"
        : [rnd] "+&s"(rnd)
        : [w0] "{v[16:47]}"(W[0]), [w1] "{v[48:79]}"(W[1]), [w2] "{v[80:111]}"(W[2]), [w3] "{v[112:143]}"(W[3]), [w4] "{v[144:175]}"(W[4]), [va] "v"(va), [sidx] "v"(sidx), [vw] "v"(vw), [lb] "s"(lb)
        : "v200", "v201", "v202", "v203", "v204", "v205", "v206", "v207", "v208", "v209", "v210", "v211", "v212", "v213", "v214", "v215", "v216", "v217", "v218", "v219", "v220", "v221", "v222", "v223", "v224", "v225", "v226", "v227", "v228", "v229", "v230", "v231", "v232", "v233", "v234", "v235", "v236", "v237", "v238", "v239", "v240", "v241", "v242", "v243", "v244", "v245", "v246", "v247", "v248", "v249", "v250", "v251", "v252", "v253", "v254", "v255", "vcc", "scc", "memory");
}

__device__ __forceinline__ void v_a1(const u32x32 (&W)[5], u32& rnd, u32 va, u32 sidx, u32 vw, u32 lb, u32& left, u32& c0, u32& r0, u32 tg, u32 kr, u32 poll, u32 vm, u32 so, u32 md, u32x4 rs STP) {
    asm volatile(
        "s_mov_b32 s94, m0\n\t"
        "s_add_u32 s92, %[so], 0x8000\n\t"
        "s_add_u32 s93, %[so], 0x10000\n\t"
        "s_mov_b32 m0, %[md]\n\t"
        "s_setprio 3\n\t"
        "s_cmp_eq_u32 %[poll], 0\n\t"
        "s_cbranch_scc1 Ltop%=\n\t"
        "Lda_%=:\n\t"
        "s_memrealtime s[90:91]\n\t"
        "s_waitcnt lgkmcnt(0)\n\t"
        "s_mul_i32 s90, s90, 24\n\t"
        "s_sub_u32 s88, %[tg], s90\n\t"
        "s_sub_u32 s89, s88, 620\n\t"
        "s_cmp_gt_u32 s89, 0x4000\n\t"
        "s_cbranch_scc1 Lea_%=\n\t"
        "s_sleep 1\n\t"
        "s_branch Lda_%=\n\t"
        "Lea_%=:\n\t"
        "s_memrealtime s[64:65]\n\t"
        "s_nop 0\n\t"
        "s_memrealtime s[66:67]\n\t"
        "s_nop 0\n\t"
        "s_memrealtime s[68:69]\n\t"
        "s_nop 0\n\t"
        "s_memrealtime s[70:71]\n\t"
        "s_nop 0\n\t"
        "s_memrealtime s[72:73]\n\t"
        "s_nop 0\n\t"
        "s_memrealtime s[74:75]\n\t"
        "s_memtime s[90:91]\n\t"
        "s_memrealtime s[76:77]\n\t"
        "s_nop 0\n\t"
        "s_memrealtime s[78:79]\n\t"
        "s_nop 0\n\t"
        "s_memrealtime s[80:81]\n\t"
        "s_nop 0\n\t"
        "s_memrealtime s[82:83]\n\t"
        "s_nop 0\n\t"
        "s_memrealtime s[84:85]\n\t"
        "s_nop 0\n\t"
        "s_memrealtime s[86:87]\n\t"
        "s_nop 0\n\t"
        "s_nop 7\n\t"
        "s_waitcnt lgkmcnt(0)\n\t"
        "s_add_u32 s64, s64, s66\n\t"
        "s_add_u32 s64, s64, s68\n\t"
        "s_add_u32 s64, s64, s70\n\t"
        "s_add_u32 s64, s64, s72\n\t"
        "s_add_u32 s64, s64, s74\n\t"
        "s_add_u32 s64, s64, s76\n\t"
        "s_add_u32 s64, s64, s78\n\t"
        "s_add_u32 s64, s64, s80\n\t"
        "s_add_u32 s64, s64, s82\n\t"
        "s_add_u32 s64, s64, s84\n\t"
        "s_add_u32 s64, s64, s86\n\t"
        "s_lshl_b32 s64, s64, 1\n\t"
        "s_mov_b32 %[c0], s90\n\t"
        "s_mov_b32 %[r0], s64\n\t"
        "s_sub_u32 s88, %[tg], s64\n\t"
        "s_mul_i32 s88, s88, %[kr]\n\t"
        "s_ashr_i32 s88, s88, 12\n\t"
        "s_sub_u32 s88, s88, 244\n\t"
        "s_nop 1\n\t"
        "s_max_i32 s88, s88, 0\n\t"
        "s_min_i32 s88, s88, 384\n\t"
        "s_andn2_b32 s88, s88, 3\n\t"
        "s_getpc_b64 s[90:91]\n\t"
        "Lga_%=:\n\t"
        "s_sub_u32 s90, s90, s88\n\t"
        "s_subb_u32 s91, s91, 0\n\t"
        "s_add_u32 s90, s90, Lza_%=-Lga_%=\n\t"
        "s_addc_u32 s91, s91, 0\n\t"
        "s_setpc_b64 s[90:91]\n\t"
        "s_nop 0\n\ts_nop 0\n\ts_nop 0\n\ts_nop 0\n\t"
        "s_nop 0\n\ts_nop 0\n\ts_nop 0\n\ts_nop 0\n\t"
        "s_nop 0\n\ts_nop 0\n\ts_nop 0\n\ts_nop 0\n\t"
        "s_nop 0\n\ts_nop 0\n\ts_nop 0\n\ts_nop 0\n\t"
        "s_nop 0\n\ts_nop 0\n\ts_nop 0\n\ts_nop 0\n\t"
        "s_nop 0\n\ts_nop 0\n\ts_nop 0\n\ts_nop 0\n\t"
        "s_nop 0\n\ts_nop 0\n\ts_nop 0\n\ts_nop 0\n\t"
        "s_nop 0\n\ts_nop 0\n\ts_nop 0\n\ts_nop 0\n\t"
        "s_nop 0\n\ts_nop 0\n\ts_nop 0\n\ts_nop 0\n\t"
        "s_nop 0\n\ts_nop 0\n\ts_nop 0\n\ts_nop 0\n\t"
        "s_nop 0\n\ts_nop 0\n\ts_nop 0\n\ts_nop 0\n\t"
        "s_nop 0\n\ts_nop 0\n\ts_nop 0\n\ts_nop 0\n\t"
        "s_nop 0\n\ts_nop 0\n\ts_nop 0\n\ts_nop 0\n\t"
        "s_nop 0\n\ts_nop 0\n\ts_nop 0\n\ts_nop 0\n\t"
        "s_nop 0\n\ts_nop 0\n\ts_nop 0\n\ts_nop 0\n\t"
        "s_nop 0\n\ts_nop 0\n\ts_nop 0\n\ts_nop 0\n\t"
        "s_nop 0\n\ts_nop 0\n\ts_nop 0\n\ts_nop 0\n\t"
        "s_nop 0\n\ts_nop 0\n\ts_nop 0\n\ts_nop 0\n\t"
        "s_nop 0\n\ts_nop 0\n\ts_nop 0\n\ts_nop 0\n\t"
        "s_nop 0\n\ts_nop 0\n\ts_nop 0\n\ts_nop 0\n\t"
        "s_nop 0\n\ts_nop 0\n\ts_nop 0\n\ts_nop 0\n\t"
        "s_nop 0\n\ts_nop 0\n\ts_nop 0\n\ts_nop 0\n\t"
        "s_nop 0\n\ts_nop 0\n\ts_nop 0\n\ts_nop 0\n\t"
        "s_nop 0\n\ts_nop 0\n\ts_nop 0\n\ts_nop 0\n\t"
        "Lza_%=:\n\t"
        "buffer_load_dwordx4 %[vm], %[rs], %[so] offen offset:0 sc1 lds\n\t"
        "buffer_load_dwordx4 %[vm], %[rs], %[so] offen offset:1024 sc1 lds\n\t"
        "buffer_load_dwordx4 %[vm], %[rs], %[so] offen offset:2048 sc1 lds\n\t"
        "buffer_load_dwordx4 %[vm], %[rs], %[so] offen offset:3072 sc1 lds\n\t"
        "s_add_u32 m0, m0, 0x1000\n\t"
        "s_nop 0\n\t"
        "buffer_load_dwordx4 %[vm], %[rs], s92 offen offset:0 sc1 lds\n\t"
        "buffer_load_dwordx4 %[vm], %[rs], s92 offen offset:1024 sc1 lds\n\t"
        "buffer_load_dwordx4 %[vm], %[rs], s92 offen offset:2048 sc1 lds\n\t"
        "buffer_load_dwordx4 %[vm], %[rs], s92 offen offset:3072 sc1 lds\n\t"
        "s_add_u32 m0, m0, 0x1000\n\t"
        "s_nop 0\n\t"
        "buffer_load_dwordx4 %[vm], %[rs], s93 offen offset:0 sc1 lds\n\t"
        "buffer_load_dwordx4 %[vm], %[rs], s93 offen offset:2048 sc1 lds\n\t"
        "Ltop%=:\n\t"
        "v_mov_b64 v[248:249], 0\n\t"
        "v_mov_b64 v[250:251], 0\n\t"
        "v_mov_b64 v[252:253], 0\n\t"
        "v_mov_b64 v[254:255], 0\n\t"
        "v_mov_b32 v200, 0xffbfffbf\n\t"
        "v_mov_b32 v206, %[lb]\n\t"
        "ds_write_b32 %[vw], v200\n\t"
        "ds_write_b32 %[vw], v200 offset:1024\n\t"
        "s_add_u32 %[rnd], %[rnd], 1\n\t"
        "s_setprio 3\n\t"
        "s_mov_b32 s95, 0\n\t"
        "s_waitcnt vmcnt(0) lgkmcnt(0)\n\t"
        "s_barrier\n\t"
        "ds_read_b128 v[208:211], %[va] offset:0\n\t"
        "ds_read_b128 v[212:215], %[va] offset:1024\n\t"
        "ds_read_b128 v[216:219], %[va] offset:2048\n\t"
        "ds_read_b128 v[220:223], %[va] offset:3072\n\t"
        "ds_read_b128 v[224:227], %[va] offset:4096\n\t"
        "ds_read_b128 v[228:231], %[va] offset:5120\n\t"
        "s_waitcnt lgkmcnt(5)\n\t"
        "v_smfmac_f32_16x16x64_bf16 v[248:251], v[208:211], v[16:23], %[sidx]\n\t"
        "ds_read_b128 v[232:235], %[va] offset:6144\n\t"
        "v_smfmac_f32_16x16x64_bf16 v[252:255], v[208:211], v[96:103], %[sidx]\n\t"
        "s_waitcnt lgkmcnt(5)\n\t"
        "v_smfmac_f32_16x16x64_bf16 v[248:251], v[212:215], v[24:31], %[sidx]\n\t"
        "ds_read_b128 v[236:239], %[va] offset:7168\n\t"
        "v_smfmac_f32_16x16x64_bf16 v[252:255], v[212:215], v[104:111], %[sidx]\n\t"
        "s_waitcnt lgkmcnt(5)\n\t"
        "v_smfmac_f32_16x16x64_bf16 v[248:251], v[216:219], v[32:39], %[sidx]\n\t"
        "ds_read_b128 v[240:243], %[va] offset:8192\n\t"
        "s_waitcnt lgkmcnt(5)\n\t"
        "v_smfmac_f32_16x16x64_bf16 v[248:251], v[220:223], v[40:47], %[sidx]\n\t"
        "ds_read_b128 v[244:247], %[va] offset:10240\n\t"
        "s_waitcnt lgkmcnt(5)\n\t"
        "v_smfmac_f32_16x16x64_bf16 v[248:251], v[224:227], v[48:55], %[sidx]\n\t"
        "s_waitcnt lgkmcnt(4)\n\t"
        "v_smfmac_f32_16x16x64_bf16 v[248:251], v[228:231], v[56:63], %[sidx]\n\t"
        "s_waitcnt lgkmcnt(3)\n\t"
        "v_smfmac_f32_16x16x64_bf16 v[248:251], v[232:235], v[64:71], %[sidx]\n\t"
        "s_waitcnt lgkmcnt(2)\n\t"
        "v_smfmac_f32_16x16x64_bf16 v[248:251], v[236:239], v[72:79], %[sidx]\n\t"
        "s_waitcnt lgkmcnt(1)\n\t"
        "v_smfmac_f32_16x16x64_bf16 v[248:251], v[240:243], v[80:87], %[sidx]\n\t"
        "s_waitcnt lgkmcnt(0)\n\t"
        "v_smfmac_f32_16x16x64_bf16 v[248:251], v[244:247], v[88:95], %[sidx]\n\t"
        "v_smfmac_f32_16x16x64_bf16 v[252:255], v[216:219], v[112:119], %[sidx]\n\t"
        "v_smfmac_f32_16x16x64_bf16 v[252:255], v[220:223], v[120:127], %[sidx]\n\t"
        "v_add_f32 v248, v248, v250\n\t"
        "v_smfmac_f32_16x16x64_bf16 v[252:255], v[224:227], v[128:135], %[sidx]\n\t"
        "v_add_f32 v249, v249, v251\n\t"
        "v_smfmac_f32_16x16x64_bf16 v[252:255], v[228:231], v[136:143], %[sidx]\n\t"
        "v_cvt_pk_bf16_f32 v200, v248, v249\n\t"
        "v_smfmac_f32_16x16x64_bf16 v[252:255], v[232:235], v[144:151], %[sidx]\n\t"
        "ds_write_b32 %[vw], v200\n\t"
        "v_smfmac_f32_16x16x64_bf16 v[252:255], v[236:239], v[152:159], %[sidx]\n\t"
        "v_cmp_u_f32 vcc, v248, v249\n\t"
        "v_smfmac_f32_16x16x64_bf16 v[252:255], v[240:243], v[160:167], %[sidx]\n\t"
        "v_smfmac_f32_16x16x64_bf16 v[252:255], v[244:247], v[168:175], %[sidx]\n\t"
        "s_nop 7\n\t"
        "v_add_f32 v252, v252, v254\n\t"
        "v_add_f32 v253, v253, v255\n\t"
        "v_cvt_pk_bf16_f32 v201, v252, v253\n\t"
        "ds_write_b32 %[vw], v201 offset:1024\n\t"
        "s_setprio 0\n\t"
        "s_cbranch_vccnz Lnan%=\n\t"
        "Lok%=:\n\t"
        "s_waitcnt lgkmcnt(0)\n\t"
        "s_barrier\n\t"
        "ds_read_b32 v207, v206 offset:14336\n\t"
        "s_waitcnt lgkmcnt(0)\n\t"
        "v_cmp_eq_u32 vcc, %[rnd], v207\n\t"
        "s_barrier\n\t"
        "s_cbranch_vccnz Lck%=\n\t"
        "s_branch Lend%=\n\t"
        "Lnan%=:\n\t"
        "s_cmp_lg_u32 %[poll], 1\n\t"
        "s_cbranch_scc1 Lok%=\n\t"
        "s_mov_b32 m0, %[lb]\n\t"
        "v_add_u32 v204, %[lb], %[vm]\n\t"
        "buffer_load_dwordx4 %[vm], %[rs], %[so] offen offset:0 sc1 lds\n\t"
        "buffer_load_dwordx4 %[vm], %[rs], %[so] offen offset:1024 sc1 lds\n\t"
        "buffer_load_dwordx4 %[vm], %[rs], %[so] offen offset:2048 sc1 lds\n\t"
        "buffer_load_dwordx4 %[vm], %[rs], %[so] offen offset:3072 sc1 lds\n\t"
        "s_add_u32 m0, m0, 0x1000\n\t"
        "s_nop 0\n\t"
        "buffer_load_dwordx4 %[vm], %[rs], s92 offen offset:0 sc1 lds\n\t"
        "buffer_load_dwordx4 %[vm], %[rs], s92 offen offset:1024 sc1 lds\n\t"
        "buffer_load_dwordx4 %[vm], %[rs], s92 offen offset:2048 sc1 lds\n\t"
        "buffer_load_dwordx4 %[vm], %[rs], s92 offen offset:3072 sc1 lds\n\t"
        "s_add_u32 m0, m0, 0x1000\n\t"
        "s_nop 0\n\t"
        "buffer_load_dwordx4 %[vm], %[rs], s93 offen offset:0 sc1 lds\n\t"
        "buffer_load_dwordx4 %[vm], %[rs], s93 offen offset:2048 sc1 lds\n\t"
        "v_max_u32 v207, v208, v209\n\t"
        "v_max3_u32 v207, v207, v210, v211\n\t"
        "v_max3_u32 v207, v207, v212, v213\n\t"
        "v_max3_u32 v207, v207, v214, v215\n\t"
        "v_max3_u32 v207, v207, v216, v217\n\t"
        "v_max3_u32 v207, v207, v218, v219\n\t"
        "v_max3_u32 v207, v207, v220, v221\n\t"
        "v_max3_u32 v207, v207, v222, v223\n\t"
        "v_max3_u32 v207, v207, v224, v225\n\t"
        "v_max3_u32 v207, v207, v226, v227\n\t"
        "v_max3_u32 v207, v207, v228, v229\n\t"
        "v_max3_u32 v207, v207, v230, v231\n\t"
        "v_max3_u32 v207, v207, v232, v233\n\t"
        "v_max3_u32 v207, v207, v234, v235\n\t"
        "v_max3_u32 v207, v207, v236, v237\n\t"
        "v_max3_u32 v207, v207, v238, v239\n\t"
        "v_max3_u32 v207, v207, v240, v241\n\t"
        "v_max3_u32 v207, v207, v242, v243\n\t"
        "v_max3_u32 v207, v207, v244, v245\n\t"
        "v_max3_u32 v207, v207, v246, v247\n\t"
        "v_cmp_eq_u32 vcc, 0xffbfffbf, v207\n\t"
        "v_mov_b32 v207, %[rnd]\n\t"
        "s_cbranch_vccz Lok%=\n\t"
        "ds_write_b32 v206, v207 offset:14336\n\t"
        "s_mov_b32 s95, 1\n\t"
        "s_waitcnt lgkmcnt(0)\n\t"
        "s_branch Lcv%=\n\t"
        "Lck%=:\n\t"
        "v_add_u32 v204, %[lb], %[vm]\n\t"
        "Lcl%=:\n\t"
        "s_mov_b32 m0, %[lb]\n\t"
        "s_nop 0\n\t"
        "buffer_load_dwordx4 %[vm], %[rs], %[so] offen offset:0 sc1 lds\n\t"
        "buffer_load_dwordx4 %[vm], %[rs], %[so] offen offset:1024 sc1 lds\n\t"
        "buffer_load_dwordx4 %[vm], %[rs], %[so] offen offset:2048 sc1 lds\n\t"
        "buffer_load_dwordx4 %[vm], %[rs], %[so] offen offset:3072 sc1 lds\n\t"
        "s_add_u32 m0, m0, 0x1000\n\t"
        "s_nop 0\n\t"
        "buffer_load_dwordx4 %[vm], %[rs], s92 offen offset:0 sc1 lds\n\t"
        "buffer_load_dwordx4 %[vm], %[rs], s92 offen offset:1024 sc1 lds\n\t"
        "buffer_load_dwordx4 %[vm], %[rs], s92 offen offset:2048 sc1 lds\n\t"
        "buffer_load_dwordx4 %[vm], %[rs], s92 offen offset:3072 sc1 lds\n\t"
        "s_add_u32 m0, m0, 0x1000\n\t"
        "s_nop 0\n\t"
        "buffer_load_dwordx4 %[vm], %[rs], s93 offen offset:0 sc1 lds\n\t"
        "buffer_load_dwordx4 %[vm], %[rs], s93 offen offset:2048 sc1 lds\n\t"
        "Lcv%=:\n\t"
        "s_sub_u32 %[left], %[left], 1\n\t"
        "s_waitcnt vmcnt(9)\n\t"
        "ds_read_b128 v[208:211], v204 offset:0\n\t"
        "s_waitcnt vmcnt(8)\n\t"
        "ds_read_b128 v[212:215], v204 offset:1024\n\t"
        "s_waitcnt vmcnt(7)\n\t"
        "ds_read_b128 v[216:219], v204 offset:2048\n\t"
        "s_waitcnt vmcnt(6)\n\t"
        "ds_read_b128 v[220:223], v204 offset:3072\n\t"
        "s_waitcnt vmcnt(5)\n\t"
        "ds_read_b128 v[224:227], v204 offset:4096\n\t"
        "s_waitcnt vmcnt(4)\n\t"
        "ds_read_b128 v[228:231], v204 offset:5120\n\t"
        "s_waitcnt lgkmcnt(5)\n\t"
        "v_max3_u32 v207, v208, v208, v209\n\t"
        "v_max3_u32 v207, v207, v210, v211\n\t"
        "s_waitcnt vmcnt(3)\n\t"
        "ds_read_b128 v[232:235], v204 offset:6144\n\t"
        "s_waitcnt lgkmcnt(5)\n\t"
        "v_max3_u32 v207, v207, v212, v213\n\t"
        "v_max3_u32 v207, v207, v214, v215\n\t"
        "s_waitcnt vmcnt(2)\n\t"
        "ds_read_b128 v[236:239], v204 offset:7168\n\t"
        "s_waitcnt lgkmcnt(5)\n\t"
        "v_max3_u32 v207, v207, v216, v217\n\t"
        "v_max3_u32 v207, v207, v218, v219\n\t"
        "s_waitcnt vmcnt(1)\n\t"
        "ds_read_b128 v[240:243], v204 offset:8192\n\t"
        "s_waitcnt lgkmcnt(5)\n\t"
        "v_max3_u32 v207, v207, v220, v221\n\t"
        "v_max3_u32 v207, v207, v222, v223\n\t"
        "s_waitcnt vmcnt(0)\n\t"
        "ds_read_b128 v[244:247], v204 offset:10240\n\t"
        "s_waitcnt lgkmcnt(5)\n\t"
        "v_max3_u32 v207, v207, v224, v225\n\t"
        "v_max3_u32 v207, v207, v226, v227\n\t"
        "s_waitcnt lgkmcnt(4)\n\t"
        "v_max3_u32 v207, v207, v228, v229\n\t"
        "v_max3_u32 v207, v207, v230, v231\n\t"
        "s_waitcnt lgkmcnt(3)\n\t"
        "v_max3_u32 v207, v207, v232, v233\n\t"
        "v_max3_u32 v207, v207, v234, v235\n\t"
        "s_waitcnt lgkmcnt(2)\n\t"
        "v_max3_u32 v207, v207, v236, v237\n\t"
        "v_max3_u32 v207, v207, v238, v239\n\t"
        "s_waitcnt lgkmcnt(1)\n\t"
        "v_max3_u32 v207, v207, v240, v241\n\t"
        "v_max3_u32 v207, v207, v242, v243\n\t"
        "s_waitcnt lgkmcnt(0)\n\t"
        "v_max3_u32 v207, v207, v244, v245\n\t"
        "v_max3_u32 v207, v207, v246, v247\n\t"
        "v_cmp_eq_u32 vcc, 0xffbfffbf, v207\n\t"
        "s_cbranch_vccz Lcd%=\n\t"
        "s_cbranch_scc0 Lcl%=\n\t"
        "Lcd%=:\n\t"
        "s_cmp_eq_u32 s95, 0\n\t"
        "s_cbranch_scc1 Lce%=\n\t"
        "s_barrier\n\t"
        "s_barrier\n\t"
        "Lce%=:\n\t"
        "s_cbranch_vccz Ltop%=\n\t"
        "Lend%=:\n\t"
        "s_mov_b32 m0, s94\n\t"
        "s_waitcnt vmcnt(0)\n\t"
        : [rnd] "+&s"(rnd), [c0] "+&s"(c0), [r0] "+&s"(r0), [left] "+&s"(left)
        : [w0] "{v[16:47]}"(W[0]), [w1] "{v[48:79]}"(W[1]), [w2] "{v[80:111]}"(W[2]), [w3] "{v[112:143]}"(W[3]), [w4] "{v[144:175]}"(W[4]), [va] "v"(va), [sidx] "v"(sidx), [vw] "v"(vw), [rs] "s"(rs), [tg] "s"(tg), [kr] "s"(kr), [poll] "s"(poll), [vm] "v"(vm), [so] "s"(so), [lb] "s"(lb), [md] "s"(md)
        : "s64", "s65", "s66", "s67", "s68", "s69", "s70", "s71", "s72", "s73", "s74", "s75", "s76", "s77", "s78", "s79", "s80", "s81", "s82", "s83", "s84", "s85", "s86", "s87", "s88", "s89", "s90", "s91", "s92", "s93", "s94", "s95", "v200", "v201", "v202", "v203", "v204", "v205", "v206", "v207", "v208", "v209", "v210", "v211", "v212", "v213", "v214", "v215", "v216", "v217", "v218", "v219", "v220", "v221", "v222", "v223", "v224", "v225", "v226", "v227", "v228", "v229", "v230", "v231", "v232", "v233", "v234", "v235", "v236", "v237", "v238", "v239", "v240", "v241", "v242", "v243", "v244", "v245", "v246", "v247", "v248", "v249", "v250", "v251", "v252", "v253", "v254", "v255", "vcc", "scc", "memory");
}

__device__ __forceinline__ void v_b(const u32x32 (&W)[5], u32& rnd, u32 sidx, u32 vr, u32 z0, u32 z1, u32 z2, u32x4 rs, u32 lb STP) {
    asm volatile(
        "Ltop%=:\n\t"
        "v_mov_b64 v[224:225], 0\n\t"
        "v_mov_b64 v[226:227], 0\n\t"
        "v_mov_b64 v[228:229], 0\n\t"
        "v_mov_b64 v[230:231], 0\n\t"
        "v_mov_b64 v[232:233], 0\n\t"
        "v_mov_b64 v[234:235], 0\n\t"
        "v_mov_b64 v[236:237], 0\n\t"
        "v_mov_b64 v[238:239], 0\n\t"
        "v_mov_b64 v[240:241], 0\n\t"
        "v_mov_b64 v[242:243], 0\n\t"
        "v_mov_b32 v206, %[lb]\n\t"
        "s_add_u32 %[rnd], %[rnd], 1\n\t"
        "s_mov_b32 s92, 0\n\t"
        "s_setprio 2\n\t"
        "s_barrier\n\t"
        "s_nop 15\n\t"
        "s_nop 15\n\t"
        "s_nop 15\n\t"
        "s_nop 15\n\t"
        "s_nop 15\n\t"
        "s_nop 7\n\t"
        "Lre%=:\n\t"
        "ds_read_b64_tr_b16 v[208:209], %[vr] offset:0\n\t"
        "ds_read_b64_tr_b16 v[210:211], %[vr] offset:64\n\t"
        "ds_read_b64_tr_b16 v[212:213], %[vr] offset:512\n\t"
        "ds_read_b64_tr_b16 v[214:215], %[vr] offset:576\n\t"
        "s_waitcnt lgkmcnt(0)\n\t"
        "v_smfmac_f32_16x16x64_bf16 v[224:227], v[208:211], v[16:23], %[sidx]\n\t"
        "v_smfmac_f32_16x16x64_bf16 v[228:231], v[208:211], v[48:55], %[sidx]\n\t"
        "v_smfmac_f32_16x16x64_bf16 v[232:235], v[208:211], v[80:87], %[sidx]\n\t"
        "v_smfmac_f32_16x16x64_bf16 v[236:239], v[208:211], v[112:119], %[sidx]\n\t"
        "v_smfmac_f32_16x16x64_bf16 v[240:243], v[208:211], v[144:151], %[sidx]\n\t"
        "ds_read_b64_tr_b16 v[216:217], %[vr] offset:1024\n\t"
        "v_smfmac_f32_16x16x64_bf16 v[224:227], v[212:215], v[24:31], %[sidx]\n\t"
        "ds_read_b64_tr_b16 v[218:219], %[vr] offset:1088\n\t"
        "v_smfmac_f32_16x16x64_bf16 v[228:231], v[212:215], v[56:63], %[sidx]\n\t"
        "ds_read_b64_tr_b16 v[220:221], %[vr] offset:1536\n\t"
        "v_smfmac_f32_16x16x64_bf16 v[232:235], v[212:215], v[88:95], %[sidx]\n\t"
        "ds_read_b64_tr_b16 v[222:223], %[vr] offset:1600\n\t"
        "v_smfmac_f32_16x16x64_bf16 v[236:239], v[212:215], v[120:127], %[sidx]\n\t"
        "v_smfmac_f32_16x16x64_bf16 v[240:243], v[212:215], v[152:159], %[sidx]\n\t"
        "s_waitcnt lgkmcnt(0)\n\t"
        "v_smfmac_f32_16x16x64_bf16 v[224:227], v[216:219], v[32:39], %[sidx]\n\t"
        "v_smfmac_f32_16x16x64_bf16 v[224:227], v[220:223], v[40:47], %[sidx]\n\t"
        "v_smfmac_f32_16x16x64_bf16 v[228:231], v[216:219], v[64:71], %[sidx]\n\t"
        "v_smfmac_f32_16x16x64_bf16 v[228:231], v[220:223], v[72:79], %[sidx]\n\t"
        "v_fma_f32 v205, v224, v225, v226\n\t"
        "v_smfmac_f32_16x16x64_bf16 v[232:235], v[216:219], v[96:103], %[sidx]\n\t"
        "v_cmp_u_f32 vcc, v205, v227\n\t"
        "v_smfmac_f32_16x16x64_bf16 v[232:235], v[220:223], v[104:111], %[sidx]\n\t"
        "v_cvt_pk_bf16_f32 v244, v224, v225\n\t"
        "v_smfmac_f32_16x16x64_bf16 v[236:239], v[216:219], v[128:135], %[sidx]\n\t"
        "v_cvt_pk_bf16_f32 v245, v226, v227\n\t"
        "v_smfmac_f32_16x16x64_bf16 v[236:239], v[220:223], v[136:143], %[sidx]\n\t"
        "v_cvt_pk_bf16_f32 v246, v228, v229\n\t"
        "v_smfmac_f32_16x16x64_bf16 v[240:243], v[216:219], v[160:167], %[sidx]\n\t"
        "v_cvt_pk_bf16_f32 v247, v230, v231\n\t"
        "v_smfmac_f32_16x16x64_bf16 v[240:243], v[220:223], v[168:175], %[sidx]\n\t"
        "s_cbranch_vccnz Lnan%=\n\t"
        "Lst%=:\n\t"
        "v_permlane16_swap_b32 v244, v246\n\t"
        "v_permlane16_swap_b32 v245, v247\n\t"
        "buffer_store_dwordx4 v[244:247], %[z0], %[rs], 0 offen sc1\n\t"
        "v_cvt_pk_bf16_f32 v248, v232, v233\n\t"
        "v_cvt_pk_bf16_f32 v249, v234, v235\n\t"
        "v_cvt_pk_bf16_f32 v250, v236, v237\n\t"
        "v_cvt_pk_bf16_f32 v251, v238, v239\n\t"
        "s_nop 0\n\t"
        "v_permlane16_swap_b32 v248, v250\n\t"
        "v_permlane16_swap_b32 v249, v251\n\t"
        "buffer_store_dwordx4 v[248:251], %[z1], %[rs], 0 offen sc1\n\t"
        "v_cvt_pk_bf16_f32 v252, v240, v241\n\t"
        "v_cvt_pk_bf16_f32 v254, v240, v241\n\t"
        "v_cvt_pk_bf16_f32 v253, v242, v243\n\t"
        "v_cvt_pk_bf16_f32 v255, v242, v243\n\t"
        "s_nop 0\n\t"
        "v_permlane16_swap_b32 v252, v254\n\t"
        "v_permlane16_swap_b32 v253, v255\n\t"
        "buffer_store_dwordx4 v[252:255], %[z2], %[rs], 0 offen sc1\n\t"
        "s_setprio 0\n\t"
        "s_cmp_eq_u32 s92, 2\n\t"
        "s_cbranch_scc1 Ly%=\n\t"
        "Lx%=:\n\t"
        "s_waitcnt lgkmcnt(0)\n\t"
        "s_barrier\n\t"
        "ds_read_b32 v207, v206 offset:14336\n\t"
        "s_waitcnt lgkmcnt(0)\n\t"
        "v_cmp_eq_u32 vcc, %[rnd], v207\n\t"
        "s_cmp_eq_u32 s92, 1\n\t"
        "s_cbranch_vccnz Lz%=\n\t"
        "s_cbranch_scc0 Ly%=\n\t"
        "s_mov_b32 s92, 2\n\t"
        "v_mov_b64 v[224:225], 0\n\t"
        "v_mov_b64 v[226:227], 0\n\t"
        "v_mov_b64 v[228:229], 0\n\t"
        "v_mov_b64 v[230:231], 0\n\t"
        "v_mov_b64 v[232:233], 0\n\t"
        "v_mov_b64 v[234:235], 0\n\t"
        "v_mov_b64 v[236:237], 0\n\t"
        "v_mov_b64 v[238:239], 0\n\t"
        "v_mov_b64 v[240:241], 0\n\t"
        "v_mov_b64 v[242:243], 0\n\t"
        "s_branch Lre%=\n\t"
        "Lnan%=:\n\t"
        "s_cmp_eq_u32 s92, 2\n\t"
        "s_cbranch_scc1 Lst%=\n\t"
        "s_mov_b32 s92, 1\n\t"
        "s_branch Lx%=\n\t"
        "Lz%=:\n\t"
        "s_barrier\n\t"
        "s_branch Ltop%=\n\t"
        "Ly%=:\n\t"
        "s_barrier\n\t"
        : [rnd] "+&s"(rnd)
        : [w0] "{v[16:47]}"(W[0]), [w1] "{v[48:79]}"(W[1]), [w2] "{v[80:111]}"(W[2]), [w3] "{v[112:143]}"(W[3]), [w4] "{v[144:175]}"(W[4]), [sidx] "v"(sidx), [vr] "v"(vr), [z0] "v"(z0), [z1] "v"(z1), [z2] "v"(z2), [rs] "s"(rs), [lb] "s"(lb)
        : "s92", "v205", "v206", "v207", "v208", "v209", "v210", "v211", "v212", "v213", "v214", "v215", "v216", "v217", "v218", "v219", "v220", "v221", "v222", "v223", "v224", "v225", "v226", "v227", "v228", "v229", "v230", "v231", "v232", "v233", "v234", "v235", "v236", "v237", "v238", "v239", "v240", "v241", "v242", "v243", "v244", "v245", "v246", "v247", "v248", "v249", "v250", "v251", "v252", "v253", "v254", "v255", "vcc", "scc", "memory");
}

__device__ __forceinline__ void l_g(u32& miss, u32& c0, u32& r0, u64& tl, u32 tg, u32 kr, u32 m0o, u32 m1o, u32 so, u32x4 rs, u32x4 rec, u32 sr STP) {
    asm volatile(
        "s_mov_b32 %[miss], 1\n\t"
        "v_or_b32 v62, 0x10000, v59\n\t"
        "s_nop 1\n\t"
        "s_cmp_eq_u32 %[kr], 0\n\t"
        "s_cbranch_scc1 Lza_%=\n\t"
        "Lda_%=:\n\t"
        "s_memrealtime s[90:91]\n\t"
        "s_waitcnt lgkmcnt(0)\n\t"
        "s_mul_i32 s90, s90, 24\n\t"
        "s_sub_u32 s88, %[tg], s90\n\t"
        "s_sub_u32 s89, s88, 620\n\t"
        "s_cmp_gt_u32 s89, 0x4000\n\t"
        "s_cbranch_scc1 Lea_%=\n\t"
        "s_sleep 1\n\t"
        "s_branch Lda_%=\n\t"
        "Lea_%=:\n\t"
        "s_memrealtime s[64:65]\n\t"
        "s_nop 0\n\t"
        "s_memrealtime s[66:67]\n\t"
        "s_nop 0\n\t"
        "s_memrealtime s[68:69]\n\t"
        "s_nop 0\n\t"
        "s_memrealtime s[70:71]\n\t"
        "s_nop 0\n\t"
        "s_memrealtime s[72:73]\n\t"
        "s_nop 0\n\t"
        "s_memrealtime s[74:75]\n\t"
        "s_memtime s[90:91]\n\t"
        "s_memrealtime s[76:77]\n\t"
        "s_nop 0\n\t"
        "s_memrealtime s[78:79]\n\t"
        "s_nop 0\n\t"
        "s_memrealtime s[80:81]\n\t"
        "s_nop 0\n\t"
        "s_memrealtime s[82:83]\n\t"
        "s_nop 0\n\t"
        "s_memrealtime s[84:85]\n\t"
        "s_nop 0\n\t"
        "s_memrealtime s[86:87]\n\t"
        "s_nop 0\n\t"
        "s_nop 7\n\t"
        "s_waitcnt lgkmcnt(0)\n\t"
        "s_add_u32 s64, s64, s66\n\t"
        "s_add_u32 s64, s64, s68\n\t"
        "s_add_u32 s64, s64, s70\n\t"
        "s_add_u32 s64, s64, s72\n\t"
        "s_add_u32 s64, s64, s74\n\t"
        "s_add_u32 s64, s64, s76\n\t"
        "s_add_u32 s64, s64, s78\n\t"
        "s_add_u32 s64, s64, s80\n\t"
        "s_add_u32 s64, s64, s82\n\t"
        "s_add_u32 s64, s64, s84\n\t"
        "s_add_u32 s64, s64, s86\n\t"
        "s_lshl_b32 s64, s64, 1\n\t"
        "s_mov_b32 %[c0], s90\n\t"
        "s_mov_b32 %[r0], s64\n\t"
        "s_sub_u32 s88, %[tg], s64\n\t"
        "s_mul_i32 s88, s88, %[kr]\n\t"
        "s_ashr_i32 s88, s88, 12\n\t"
        "s_sub_u32 s88, s88, 244\n\t"
        "s_nop 1\n\t"
        "s_max_i32 s88, s88, 0\n\t"
        "s_min_i32 s88, s88, 384\n\t"
        "s_andn2_b32 s88, s88, 3\n\t"
        "s_getpc_b64 s[90:91]\n\t"
        "Lga_%=:\n\t"
        "s_sub_u32 s90, s90, s88\n\t"
        "s_subb_u32 s91, s91, 0\n\t"
        "s_add_u32 s90, s90, Lza_%=-Lga_%=\n\t"
        "s_addc_u32 s91, s91, 0\n\t"
        "s_setpc_b64 s[90:91]\n\t"
        "s_nop 0\n\ts_nop 0\n\ts_nop 0\n\ts_nop 0\n\t"
        "s_nop 0\n\ts_nop 0\n\ts_nop 0\n\ts_nop 0\n\t"
        "s_nop 0\n\ts_nop 0\n\ts_nop 0\n\ts_nop 0\n\t"
        "s_nop 0\n\ts_nop 0\n\ts_nop 0\n\ts_nop 0\n\t"
        "s_nop 0\n\ts_nop 0\n\ts_nop 0\n\ts_nop 0\n\t"
        "s_nop 0\n\ts_nop 0\n\ts_nop 0\n\ts_nop 0\n\t"
        "s_nop 0\n\ts_nop 0\n\ts_nop 0\n\ts_nop 0\n\t"
        "s_nop 0\n\ts_nop 0\n\ts_nop 0\n\ts_nop 0\n\t"
        "s_nop 0\n\ts_nop 0\n\ts_nop 0\n\ts_nop 0\n\t"
        "s_nop 0\n\ts_nop 0\n\ts_nop 0\n\ts_nop 0\n\t"
        "s_nop 0\n\ts_nop 0\n\ts_nop 0\n\ts_nop 0\n\t"
        "s_nop 0\n\ts_nop 0\n\ts_nop 0\n\ts_nop 0\n\t"
        "s_nop 0\n\ts_nop 0\n\ts_nop 0\n\ts_nop 0\n\t"
        "s_nop 0\n\ts_nop 0\n\ts_nop 0\n\ts_nop 0\n\t"
        "s_nop 0\n\ts_nop 0\n\ts_nop 0\n\ts_nop 0\n\t"
        "s_nop 0\n\ts_nop 0\n\ts_nop 0\n\ts_nop 0\n\t"
        "s_nop 0\n\ts_nop 0\n\ts_nop 0\n\ts_nop 0\n\t"
        "s_nop 0\n\ts_nop 0\n\ts_nop 0\n\ts_nop 0\n\t"
        "s_nop 0\n\ts_nop 0\n\ts_nop 0\n\ts_nop 0\n\t"
        "s_nop 0\n\ts_nop 0\n\ts_nop 0\n\ts_nop 0\n\t"
        "s_nop 0\n\ts_nop 0\n\ts_nop 0\n\ts_nop 0\n\t"
        "s_nop 0\n\ts_nop 0\n\ts_nop 0\n\ts_nop 0\n\t"
        "s_nop 0\n\ts_nop 0\n\ts_nop 0\n\ts_nop 0\n\t"
        "s_nop 0\n\ts_nop 0\n\ts_nop 0\n\ts_nop 0\n\t"
        "Lza_%=:\n\t"
        "buffer_load_dword v60, %[m0o], %[rs], %[so] offen sc1\n\t"
        "buffer_load_dword v61, %[m1o], %[rs], %[so] offen sc1\n\t"
        "s_memrealtime %[tl]\n\t"
        "s_waitcnt vmcnt(0)\n\t"
        "v_max_u32 v60, v60, v61\n\t"
        "v_cmp_eq_u32 vcc, 0xffbfffbf, v60\n\t"
        "s_cbranch_vccnz Lm%=\n\t"
        "buffer_store_dwordx4 v[56:59], %[sr], %[rs], 0 offen sc1\n\t"
        "s_mov_b32 %[miss], 0\n\t"
        "s_branch Lend%=\n\t"
        "Lm%=:\n\t"
        "buffer_store_dword v62, %[sr], %[rs], 0 offen offset:12 sc1\n\t"
        "Lend%=:\n\t"
        "s_waitcnt lgkmcnt(0)\n\t"
        : [c0] "+&s"(c0), [r0] "+&s"(r0), [tl] "=&s"(tl), [miss] "=&s"(miss)
        : [m0o] "v"(m0o), [m1o] "v"(m1o), [sr] "v"(sr), [rs] "s"(rs), [so] "s"(so), [tg] "s"(tg), [kr] "s"(kr), [rec] "{v[56:59]}"(rec)
        : "s64", "s65", "s66", "s67", "s68", "s69", "s70", "s71", "s72", "s73", "s74", "s75", "s76", "s77", "s78", "s79", "s80", "s81", "s82", "s83", "s84", "s85", "s86", "s87", "s88", "s89", "s90", "s91", "v60", "v61", "v62", "vcc", "scc", "memory");
}

__device__ __forceinline__ void r_t0(u32& c0, u32& r0, u32& t1, u32& t2, u32& ok, u32 tl, u32 t0, u32 kr, u32 fv, u32 lb, u32x4 rs, u32 so STP) {
    asm volatile(
        "s_setprio 3\n\t"
        "v_mov_b32 v20, %[lb]\n\t"
        "s_mov_b32 %[ok], 0\n\t"
        "s_or_b32 s92, %[fv], 0x10000\n\t"
        "s_lshr_b32 s95, %[t2], 16\n\t"
        "s_mul_i32 s95, s95, %[kr]\n\t"
        "s_lshr_b32 s95, s95, 12\n\t"
        "s_min_u32 s95, s95, 512\n\t"
        "s_andn2_b32 s95, s95, 3\n\t"
        "v_mov_b32 v21, s95\n\t"
        "ds_write_b32 v20, v21 offset:16384\n\t"
        "Ldl_%=:\n\t"
        "s_memrealtime s[90:91]\n\t"
        "s_waitcnt lgkmcnt(0)\n\t"
        "s_mul_i32 s90, s90, 24\n\t"
        "s_sub_u32 s88, %[tl], s90\n\t"
        "s_sub_u32 s89, s88, 620\n\t"
        "s_cmp_gt_u32 s89, 0x4000\n\t"
        "s_cbranch_scc1 Lel_%=\n\t"
        "s_sleep 1\n\t"
        "s_branch Ldl_%=\n\t"
        "Lel_%=:\n\t"
        "s_memrealtime s[64:65]\n\t"
        "s_nop 0\n\t"
        "s_memrealtime s[66:67]\n\t"
        "s_nop 0\n\t"
        "s_memrealtime s[68:69]\n\t"
        "s_nop 0\n\t"
        "s_memrealtime s[70:71]\n\t"
        "s_nop 0\n\t"
        "s_memrealtime s[72:73]\n\t"
        "s_nop 0\n\t"
        "s_memrealtime s[74:75]\n\t"
        "s_memtime s[90:91]\n\t"
        "s_memrealtime s[76:77]\n\t"
        "s_nop 0\n\t"
        "s_memrealtime s[78:79]\n\t"
        "s_nop 0\n\t"
        "s_memrealtime s[80:81]\n\t"
        "s_nop 0\n\t"
        "s_memrealtime s[82:83]\n\t"
        "s_nop 0\n\t"
        "s_memrealtime s[84:85]\n\t"
        "s_nop 0\n\t"
        "s_memrealtime s[86:87]\n\t"
        "s_nop 0\n\t"
        "s_nop 7\n\t"
        "s_waitcnt lgkmcnt(0)\n\t"
        "s_add_u32 s64, s64, s66\n\t"
        "s_add_u32 s64, s64, s68\n\t"
        "s_add_u32 s64, s64, s70\n\t"
        "s_add_u32 s64, s64, s72\n\t"
        "s_add_u32 s64, s64, s74\n\t"
        "s_add_u32 s64, s64, s76\n\t"
        "s_add_u32 s64, s64, s78\n\t"
        "s_add_u32 s64, s64, s80\n\t"
        "s_add_u32 s64, s64, s82\n\t"
        "s_add_u32 s64, s64, s84\n\t"
        "s_add_u32 s64, s64, s86\n\t"
        "s_lshl_b32 s64, s64, 1\n\t"
        "s_mov_b32 %[c0], s90\n\t"
        "s_mov_b32 %[r0], s64\n\t"
        "s_sub_u32 s88, %[tl], s64\n\t"
        "s_mul_i32 s88, s88, %[kr]\n\t"
        "s_ashr_i32 s88, s88, 12\n\t"
        "s_sub_u32 s88, s88, 244\n\t"
        "s_nop 1\n\t"
        "s_max_i32 s88, s88, 0\n\t"
        "s_min_i32 s88, s88, 384\n\t"
        "s_andn2_b32 s88, s88, 3\n\t"
        "s_getpc_b64 s[90:91]\n\t"
        "Lgl_%=:\n\t"
        "s_sub_u32 s90, s90, s88\n\t"
        "s_subb_u32 s91, s91, 0\n\t"
        "s_add_u32 s90, s90, Lzl_%=-Lgl_%=\n\t"
        "s_addc_u32 s91, s91, 0\n\t"
        "s_setpc_b64 s[90:91]\n\t"
        "s_nop 0\n\ts_nop 0\n\ts_nop 0\n\ts_nop 0\n\t"
        "s_nop 0\n\ts_nop 0\n\ts_nop 0\n\ts_nop 0\n\t"
        "s_nop 0\n\ts_nop 0\n\ts_nop 0\n\ts_nop 0\n\t"
        "s_nop 0\n\ts_nop 0\n\ts_nop 0\n\ts_nop 0\n\t"
        "s_nop 0\n\ts_nop 0\n\ts_nop 0\n\ts_nop 0\n\t"
        "s_nop 0\n\ts_nop 0\n\ts_nop 0\n\ts_nop 0\n\t"
        "s_nop 0\n\ts_nop 0\n\ts_nop 0\n\ts_nop 0\n\t"
        "s_nop 0\n\ts_nop 0\n\ts_nop 0\n\ts_nop 0\n\t"
        "s_nop 0\n\ts_nop 0\n\ts_nop 0\n\ts_nop 0\n\t"
        "s_nop 0\n\ts_nop 0\n\ts_nop 0\n\ts_nop 0\n\t"
        "s_nop 0\n\ts_nop 0\n\ts_nop 0\n\ts_nop 0\n\t"
        "s_nop 0\n\ts_nop 0\n\ts_nop 0\n\ts_nop 0\n\t"
        "s_nop 0\n\ts_nop 0\n\ts_nop 0\n\ts_nop 0\n\t"
        "s_nop 0\n\ts_nop 0\n\ts_nop 0\n\ts_nop 0\n\t"
        "s_nop 0\n\ts_nop 0\n\ts_nop 0\n\ts_nop 0\n\t"
        "s_nop 0\n\ts_nop 0\n\ts_nop 0\n\ts_nop 0\n\t"
        "s_nop 0\n\ts_nop 0\n\ts_nop 0\n\ts_nop 0\n\t"
        "s_nop 0\n\ts_nop 0\n\ts_nop 0\n\ts_nop 0\n\t"
        "s_nop 0\n\ts_nop 0\n\ts_nop 0\n\ts_nop 0\n\t"
        "s_nop 0\n\ts_nop 0\n\ts_nop 0\n\ts_nop 0\n\t"
        "s_nop 0\n\ts_nop 0\n\ts_nop 0\n\ts_nop 0\n\t"
        "s_nop 0\n\ts_nop 0\n\ts_nop 0\n\ts_nop 0\n\t"
        "s_nop 0\n\ts_nop 0\n\ts_nop 0\n\ts_nop 0\n\t"
        "s_nop 0\n\ts_nop 0\n\ts_nop 0\n\ts_nop 0\n\t"
        "Lzl_%=:\n\t"
        "s_buffer_load_dwordx4 s[48:51], %[rs], %[so] offset:0x0\n\t"
        "s_buffer_load_dwordx4 s[52:55], %[rs], %[so] offset:0x80\n\t"
        "s_buffer_load_dwordx4 s[56:59], %[rs], %[so] offset:0x100\n\t"
        "s_buffer_load_dwordx4 s[60:63], %[rs], %[so] offset:0x180\n\t"
        "s_sub_u32 s95, %[t0], %[r0]\n\t"
        "s_mul_i32 s95, s95, %[kr]\n\t"
        "s_ashr_i32 s95, s95, 12\n\t"
        "s_add_u32 s95, s95, %[c0]\n\t"
        "s_waitcnt lgkmcnt(0)\n\t"
        "s_cmp_eq_u32 s51, s92\n\t"
        "s_cbranch_scc1 Lend%=\n\t"
        "s_cmp_eq_u32 s55, s92\n\t"
        "s_cbranch_scc1 Lend%=\n\t"
        "s_cmp_eq_u32 s59, s92\n\t"
        "s_cbranch_scc1 Lend%=\n\t"
        "s_cmp_eq_u32 s63, s92\n\t"
        "s_cbranch_scc1 Lend%=\n\t"
        "Lda_%=:\n\t"
        "s_memtime s[90:91]\n\t"
        "s_waitcnt lgkmcnt(0)\n\t"
        "s_sub_u32 s88, s95, s90\n\t"
        "s_sub_u32 s89, s88, 336\n\t"
        "s_cmp_gt_u32 s89, 0x4000\n\t"
        "s_cbranch_scc1 Lea_%=\n\t"
        "s_sleep 1\n\t"
        "s_branch Lda_%=\n\t"
        "Lea_%=:\n\t"
        "s_sub_u32 s88, s88, 112\n\t"
        "s_max_i32 s88, s88, 0\n\t"
        "s_min_i32 s88, s88, 224\n\t"
        "s_andn2_b32 s88, s88, 3\n\t"
        "s_getpc_b64 s[90:91]\n\t"
        "Lga_%=:\n\t"
        "s_sub_u32 s90, s90, s88\n\t"
        "s_subb_u32 s91, s91, 0\n\t"
        "s_add_u32 s90, s90, Lza_%=-Lga_%=\n\t"
        "s_addc_u32 s91, s91, 0\n\t"
        "s_setpc_b64 s[90:91]\n\t"
        "s_nop 0\n\ts_nop 0\n\ts_nop 0\n\ts_nop 0\n\t"
        "s_nop 0\n\ts_nop 0\n\ts_nop 0\n\ts_nop 0\n\t"
        "s_nop 0\n\ts_nop 0\n\ts_nop 0\n\ts_nop 0\n\t"
        "s_nop 0\n\ts_nop 0\n\ts_nop 0\n\ts_nop 0\n\t"
        "s_nop 0\n\ts_nop 0\n\ts_nop 0\n\ts_nop 0\n\t"
        "s_nop 0\n\ts_nop 0\n\ts_nop 0\n\ts_nop 0\n\t"
        "s_nop 0\n\ts_nop 0\n\ts_nop 0\n\ts_nop 0\n\t"
        "s_nop 0\n\ts_nop 0\n\ts_nop 0\n\ts_nop 0\n\t"
        "s_nop 0\n\ts_nop 0\n\ts_nop 0\n\ts_nop 0\n\t"
        "s_nop 0\n\ts_nop 0\n\ts_nop 0\n\ts_nop 0\n\t"
        "s_nop 0\n\ts_nop 0\n\ts_nop 0\n\ts_nop 0\n\t"
        "s_nop 0\n\ts_nop 0\n\ts_nop 0\n\ts_nop 0\n\t"
        "s_nop 0\n\ts_nop 0\n\ts_nop 0\n\ts_nop 0\n\t"
        "s_nop 0\n\ts_nop 0\n\ts_nop 0\n\ts_nop 0\n\t"
        "Lza_%=:\n\t"
        "s_barrier\n\t"
        "s_setprio 0\n\t"
        "s_xor_b32 s51, s51, %[fv]\n\t"
        "s_xor_b32 s55, s55, %[fv]\n\t"
        "s_xor_b32 s59, s59, %[fv]\n\t"
        "s_xor_b32 s63, s63, %[fv]\n\t"
        "s_sub_u32 s53, s53, s49\n\t"
        "s_sub_u32 s57, s57, s49\n\t"
        "s_sub_u32 s61, s61, s49\n\t"
        "s_max_i32 s53, s53, s57\n\t"
        "s_max_i32 s61, s61, 0\n\t"
        "s_max_i32 s53, s53, s61\n\t"
        "s_add_u32 s49, s49, s53\n\t"
        "s_max_u32 s50, s50, s54\n\t"
        "s_max_u32 s50, s50, s58\n\t"
        "s_max_u32 s50, s50, s62\n\t"
        "s_or_b32 s58, s51, s55\n\t"
        "s_or_b32 s54, s59, s63\n\t"
        "s_or_b32 s54, s54, s58\n\t"
        "s_cmp_eq_u32 s54, 0\n\t"
        "s_cselect_b32 %[ok], 1, 2\n\t"
        "s_cselect_b32 %[t1], s49, %[t1]\n\t"
        "s_cselect_b32 %[t2], s50, %[t2]\n\t"
        "s_barrier\n\t"
        "Lend%=:\n\t"
        : [c0] "+&s"(c0), [r0] "+&s"(r0), [t1] "+&s"(t1), [t2] "+&s"(t2), [ok] "=&s"(ok)
        : [rs] "s"(rs), [so] "s"(so), [kr] "s"(kr), [fv] "s"(fv), [lb] "s"(lb), [tl] "s"(tl), [t0] "s"(t0)
        : "s48", "s49", "s50", "s51", "s52", "s53", "s54", "s55", "s56", "s57", "s58", "s59", "s60", "s61", "s62", "s63", "s64", "s65", "s66", "s67", "s68", "s69", "s70", "s71", "s72", "s73", "s74", "s75", "s76", "s77", "s78", "s79", "s80", "s81", "s82", "s83", "s84", "s85", "s86", "s87", "s88", "s89", "s90", "s91", "s92", "s93", "s94", "s95", "v20", "v21", "vcc", "scc", "memory");
}

__device__ __forceinline__ void r_t1(u32& c0, u32& r0, u32& t1, u32& t2, u32 tv, u32 kr, u32 fv, u32 lb, u32x4 rs, u32 so STP) {
    asm volatile(
        "s_setprio 3\n\t"
        "s_mov_b32 s92, 0x4000\n\t"
        "v_mov_b32 v20, %[lb]\n\t"
        "s_nop 2\n\t"
        "Lr%=:\n\t"
        "buffer_load_dwordx4 v[16:19], %[tv], %[rs], %[so] offen sc1\n\t"
        "s_memrealtime s[64:65]\n\t"
        "s_nop 0\n\t"
        "s_memrealtime s[66:67]\n\t"
        "s_nop 0\n\t"
        "s_memrealtime s[68:69]\n\t"
        "s_nop 0\n\t"
        "s_memrealtime s[70:71]\n\t"
        "s_nop 0\n\t"
        "s_memrealtime s[72:73]\n\t"
        "s_nop 0\n\t"
        "s_memrealtime s[74:75]\n\t"
        "s_memtime s[90:91]\n\t"
        "s_memrealtime s[76:77]\n\t"
        "s_nop 0\n\t"
        "s_memrealtime s[78:79]\n\t"
        "s_nop 0\n\t"
        "s_memrealtime s[80:81]\n\t"
        "s_nop 0\n\t"
        "s_memrealtime s[82:83]\n\t"
        "s_nop 0\n\t"
        "s_memrealtime s[84:85]\n\t"
        "s_nop 0\n\t"
        "s_memrealtime s[86:87]\n\t"
        "s_nop 0\n\t"
        "s_nop 7\n\t"
        "s_waitcnt lgkmcnt(0)\n\t"
        "s_add_u32 s64, s64, s66\n\t"
        "s_add_u32 s64, s64, s68\n\t"
        "s_add_u32 s64, s64, s70\n\t"
        "s_add_u32 s64, s64, s72\n\t"
        "s_add_u32 s64, s64, s74\n\t"
        "s_add_u32 s64, s64, s76\n\t"
        "s_add_u32 s64, s64, s78\n\t"
        "s_add_u32 s64, s64, s80\n\t"
        "s_add_u32 s64, s64, s82\n\t"
        "s_add_u32 s64, s64, s84\n\t"
        "s_add_u32 s64, s64, s86\n\t"
        "s_lshl_b32 s64, s64, 1\n\t"
        "s_mov_b32 %[c0], s90\n\t"
        "s_mov_b32 %[r0], s64\n\t"
        "s_sub_u32 s96, %[r0], 0x100000\n\t"
        "s_waitcnt vmcnt(0)\n\t"
        "v_cmp_eq_u32 vcc, %[fv], v19\n\t"
        "v_subrev_u32 v16, s96, v16\n\t"
        "v_subrev_u32 v17, s96, v17\n\t"
        "v_cndmask_b32 v16, 0, v16, vcc\n\t"
        "v_cndmask_b32 v17, 0, v17, vcc\n\t"
        "v_cndmask_b32 v18, 0, v18, vcc\n\t"
        "v_max_u32_dpp v16, v16, v16 quad_perm:[1,0,3,2] row_mask:0xf bank_mask:0xf\n\t"
        "v_max_u32_dpp v17, v17, v17 quad_perm:[1,0,3,2] row_mask:0xf bank_mask:0xf\n\t"
        "v_max_u32_dpp v18, v18, v18 quad_perm:[1,0,3,2] row_mask:0xf bank_mask:0xf\n\t"
        "v_max_u32_dpp v16, v16, v16 quad_perm:[2,3,0,1] row_mask:0xf bank_mask:0xf\n\t"
        "v_max_u32_dpp v17, v17, v17 quad_perm:[2,3,0,1] row_mask:0xf bank_mask:0xf\n\t"
        "v_max_u32_dpp v18, v18, v18 quad_perm:[2,3,0,1] row_mask:0xf bank_mask:0xf\n\t"
        "v_max_u32_dpp v16, v16, v16 row_half_mirror row_mask:0xf bank_mask:0xf\n\t"
        "v_max_u32_dpp v17, v17, v17 row_half_mirror row_mask:0xf bank_mask:0xf\n\t"
        "v_max_u32_dpp v18, v18, v18 row_half_mirror row_mask:0xf bank_mask:0xf\n\t"
        "s_lshr_b32 s94, vcc_lo, 4\n\t"
        "v_readfirstlane_b32 s93, v16\n\t"
        "v_readfirstlane_b32 %[t1], v17\n\t"
        "v_readfirstlane_b32 %[t2], v18\n\t"
        "s_add_u32 s93, s93, s96\n\t"
        "s_add_u32 %[t1], %[t1], s96\n\t"
        "s_or_b32 s94, s94, vcc_lo\n\t"
        "s_and_b32 s94, s94, 15\n\t"
        "s_cmp_eq_u32 s94, 15\n\t"
        "s_cbranch_scc0 Lx%=\n\t"
        "Lgo%=:\n\t"
        "s_lshr_b32 s95, %[t2], 16\n\t"
        "s_mul_i32 s95, s95, %[kr]\n\t"
        "s_lshr_b32 s95, s95, 12\n\t"
        "s_min_u32 s95, s95, 512\n\t"
        "s_andn2_b32 s95, s95, 3\n\t"
        "v_mov_b32 v21, s95\n\t"
        "ds_write_b32 v20, v21 offset:16384\n\t"
        "s_sub_u32 s95, s93, %[r0]\n\t"
        "s_mul_i32 s95, s95, %[kr]\n\t"
        "s_ashr_i32 s95, s95, 12\n\t"
        "s_add_u32 s95, s95, %[c0]\n\t"
        "Lda_%=:\n\t"
        "s_memtime s[90:91]\n\t"
        "s_waitcnt lgkmcnt(0)\n\t"
        "s_sub_u32 s88, s95, s90\n\t"
        "s_sub_u32 s89, s88, 336\n\t"
        "s_cmp_gt_u32 s89, 0x4000\n\t"
        "s_cbranch_scc1 Lea_%=\n\t"
        "s_sleep 1\n\t"
        "s_branch Lda_%=\n\t"
        "Lea_%=:\n\t"
        "s_sub_u32 s88, s88, 112\n\t"
        "s_max_i32 s88, s88, 0\n\t"
        "s_min_i32 s88, s88, 224\n\t"
        "s_andn2_b32 s88, s88, 3\n\t"
        "s_getpc_b64 s[90:91]\n\t"
        "Lga_%=:\n\t"
        "s_sub_u32 s90, s90, s88\n\t"
        "s_subb_u32 s91, s91, 0\n\t"
        "s_add_u32 s90, s90, Lza_%=-Lga_%=\n\t"
        "s_addc_u32 s91, s91, 0\n\t"
        "s_setpc_b64 s[90:91]\n\t"
        "s_nop 0\n\ts_nop 0\n\ts_nop 0\n\ts_nop 0\n\t"
        "s_nop 0\n\ts_nop 0\n\ts_nop 0\n\ts_nop 0\n\t"
        "s_nop 0\n\ts_nop 0\n\ts_nop 0\n\ts_nop 0\n\t"
        "s_nop 0\n\ts_nop 0\n\ts_nop 0\n\ts_nop 0\n\t"
        "s_nop 0\n\ts_nop 0\n\ts_nop 0\n\ts_nop 0\n\t"
        "s_nop 0\n\ts_nop 0\n\ts_nop 0\n\ts_nop 0\n\t"
        "s_nop 0\n\ts_nop 0\n\ts_nop 0\n\ts_nop 0\n\t"
        "s_nop 0\n\ts_nop 0\n\ts_nop 0\n\ts_nop 0\n\t"
        "s_nop 0\n\ts_nop 0\n\ts_nop 0\n\ts_nop 0\n\t"
        "s_nop 0\n\ts_nop 0\n\ts_nop 0\n\ts_nop 0\n\t"
        "s_nop 0\n\ts_nop 0\n\ts_nop 0\n\ts_nop 0\n\t"
        "s_nop 0\n\ts_nop 0\n\ts_nop 0\n\ts_nop 0\n\t"
        "s_nop 0\n\ts_nop 0\n\ts_nop 0\n\ts_nop 0\n\t"
        "s_nop 0\n\ts_nop 0\n\ts_nop 0\n\ts_nop 0\n\t"
        "Lza_%=:\n\t"
        "s_barrier\n\t"
        "s_setprio 0\n\t"
        "s_barrier\n\t"
        "s_branch Lend%=\n\t"
        "Lx%=:\n\t"
        "s_sub_u32 s92, s92, 1\n\t"
        "s_cbranch_scc1 Lgo%=\n\t"
        "s_branch Lr%=\n\t"
        "Lend%=:\n\t"
        : [c0] "+&s"(c0), [r0] "+&s"(r0), [t1] "+&s"(t1), [t2] "+&s"(t2)
        : [tv] "v"(tv), [rs] "s"(rs), [so] "s"(so), [kr] "s"(kr), [fv] "s"(fv), [lb] "s"(lb)
        : "s64", "s65", "s66", "s67", "s68", "s69", "s70", "s71", "s72", "s73", "s74", "s75", "s76", "s77", "s78", "s79", "s80", "s81", "s82", "s83", "s84", "s85", "s86", "s87", "s88", "s89", "s90", "s91", "s92", "s93", "s94", "s95", "s96", "v16", "v17", "v18", "v19", "v20", "v21", "vcc", "scc", "memory");
}

__device__ __forceinline__ void r_u(u32x32& bn, u32& s, u32& have, u32& left, u32& why, u32 sidx, u32 eo, u32 uw, u32 fa, u32 vm, u32 fo, u32 te, u32 tb, u32x4 rs, u32 lag, u32 lb STP LGP) {
    asm volatile(
        "v_mov_b32 v125, %[lb]\n\t"
        "v_mov_b32 v80, 0xffbfffbf\n\t"
        "v_mov_b32 v81, v80\n\t"
        "ds_write_b64 %[uw], v[80:81] offset:0\n\t"
        "ds_write_b64 %[uw], v[80:81] offset:512\n\t"
        "ds_write_b64 %[uw], v[80:81] offset:1024\n\t"
        "ds_write_b64 %[uw], v[80:81] offset:1536\n\t"
        "Lp%=:\n\t"
        "v_readlane_b32 s82, %[te], %[s]\n\t"
        "v_readlane_b32 s83, %[tb], %[s]\n\t"
        "s_add_u32 s92, %[s], 1\n\t"
        "s_mul_i32 s93, s92, 0x10001\n\t"
        "s_mov_b32 s95, %[left]\n\t"
        "s_mov_b32 s94, 0\n\t"
        "v_mov_b32 v124, s93\n\t"
        "s_add_u32 s97, %[s], 4\n\t"
        "s_cmp_le_u32 %[have], s97\n\t"
        "s_cselect_b32 s96, 0, 0x80000000\n\t"
        "v_or_b32 v126, s96, %[fo]\n\t"
        "s_add_u32 s84, s83, 0x1000\n\t"
        "s_setprio 2\n\t"
        "s_waitcnt lgkmcnt(0)\n\t"
        "s_getpc_b64 s[90:91]\n\t"
        "Lj%=:\n\t"
        "s_sub_u32 s90, s90, %[lag]\n\t"
        "s_subb_u32 s91, s91, 0\n\t"
        "s_add_u32 s90, s90, Lg%=-Lj%=\n\t"
        "s_addc_u32 s91, s91, 0\n\t"
        "s_barrier\n\t"
        "s_setpc_b64 s[90:91]\n\t"
        "s_nop 10\n\t"
        "s_nop 10\n\t"
        "s_nop 10\n\t"
        "Lg%=:\n\t"
        "buffer_load_dwordx4 v[48:51], %[eo], %[rs], s82 offen offset:0 sc1\n\t"
        "buffer_load_dwordx4 v[52:55], %[eo], %[rs], s82 offen offset:1024 sc1\n\t"
        "buffer_load_dwordx4 v[56:59], %[eo], %[rs], s82 offen offset:2048 sc1\n\t"
        "buffer_load_dwordx4 v[60:63], %[eo], %[rs], s82 offen offset:3072 sc1\n\t"
        "v_mov_b64 v[64:65], 0\n\t"
        "v_mov_b64 v[66:67], 0\n\t"
        "v_mov_b64 v[68:69], 0\n\t"
        "v_mov_b64 v[70:71], 0\n\t"
        "v_mov_b64 v[72:73], 0\n\t"
        "v_mov_b64 v[74:75], 0\n\t"
        "v_mov_b64 v[76:77], 0\n\t"
        "v_mov_b64 v[78:79], 0\n\t"
        "s_waitcnt vmcnt(3)\n\t"
        "v_smfmac_f32_16x16x64_bf16 v[64:67], v[48:51], v[16:23], %[sidx]\n\t"
        "s_waitcnt vmcnt(2)\n\t"
        "v_smfmac_f32_16x16x64_bf16 v[68:71], v[52:55], v[24:31], %[sidx]\n\t"
        "s_waitcnt vmcnt(1)\n\t"
        "v_smfmac_f32_16x16x64_bf16 v[72:75], v[56:59], v[32:39], %[sidx]\n\t"
        "v_cvt_pk_bf16_f32 v80, v64, v65\n\t"
        "v_cvt_pk_bf16_f32 v81, v66, v67\n\t"
        "ds_write_b64 %[uw], v[80:81] offset:0\n\t"
        "s_waitcnt vmcnt(0)\n\t"
        "v_smfmac_f32_16x16x64_bf16 v[76:79], v[60:63], v[40:47], %[sidx]\n\t"
        "v_cvt_pk_bf16_f32 v82, v68, v69\n\t"
        "v_cvt_pk_bf16_f32 v83, v70, v71\n\t"
        "ds_write_b64 %[uw], v[82:83] offset:512\n\t"
        "v_cvt_pk_bf16_f32 v84, v72, v73\n\t"
        "v_cvt_pk_bf16_f32 v85, v74, v75\n\t"
        "ds_write_b64 %[uw], v[84:85] offset:1024\n\t"
        "s_nop 1\n\t"
        "v_cvt_pk_bf16_f32 v86, v76, v77\n\t"
        "v_cvt_pk_bf16_f32 v87, v78, v79\n\t"
        "ds_write_b64 %[uw], v[86:87] offset:1536\n\t"
        "s_setprio 0\n\t"
        "s_cmp_lg_u32 s94, 0\n\t"
        "s_cbranch_scc1 Lc%=\n\t"
        "s_waitcnt lgkmcnt(0)\n\t"
        "s_barrier\n\t"
        "ds_read_b32 v88, v125 offset:16392\n\t"
        "s_waitcnt lgkmcnt(0)\n\t"
        "v_cmp_eq_u32 vcc, s93, v88\n\t"
        "s_cbranch_vccnz Lc%=\n\t"
        "Lok%=:\n\t"
        "s_cmp_gt_u32 %[have], s92\n\t"
        "s_cselect_b32 %[why], 0, 2\n\t"
        "buffer_load_dwordx2 v[122:123], v126, %[rs], 0 offen sc1\n\t"
        "s_cbranch_scc0 Ln%=\n\t"
        "buffer_load_dwordx4 v[90:93], %[vm], %[rs], s83 offen offset:0 sc1\n\t"
        "buffer_load_dwordx4 v[94:97], %[vm], %[rs], s83 offen offset:1024 sc1\n\t"
        "buffer_load_dwordx4 v[98:101], %[vm], %[rs], s83 offen offset:2048 sc1\n\t"
        "buffer_load_dwordx4 v[102:105], %[vm], %[rs], s83 offen offset:3072 sc1\n\t"
        "buffer_load_dwordx4 v[106:109], %[vm], %[rs], s84 offen offset:0 sc1\n\t"
        "buffer_load_dwordx4 v[110:113], %[vm], %[rs], s84 offen offset:1024 sc1\n\t"
        "buffer_load_dwordx4 v[114:117], %[vm], %[rs], s84 offen offset:2048 sc1\n\t"
        "buffer_load_dwordx4 v[118:121], %[vm], %[rs], s84 offen offset:3072 sc1\n\t"
        "Ln%=:\n\t"
        "ds_read_b32 v88, v125 offset:16396\n\t"
        "v_mov_b32 v80, 0xffbfffbf\n\t"
        "v_mov_b32 v81, v80\n\t"
        "s_sub_u32 s95, s95, %[left]\n\t"
        "s_mov_b32 %[s], s92\n\t"
        "s_waitcnt lgkmcnt(0)\n\t"
        "v_cmp_eq_u32 vcc, s93, v88\n\t"
        "s_cmp_eq_u32 %[s], 63\n\t"
        "s_cbranch_vccnz Lr%=\n\t"
        "s_cbranch_scc1 Lz%=\n\t"
        "s_cmp_lg_u32 %[why], 0\n\t"
        "s_cbranch_scc1 Lend%=\n\t"
        "ds_write_b64 %[uw], v[80:81] offset:0\n\t"
        "ds_write_b64 %[uw], v[80:81] offset:512\n\t"
        "ds_write_b64 %[uw], v[80:81] offset:1024\n\t"
        "ds_write_b64 %[uw], v[80:81] offset:1536\n\t"
        "s_waitcnt vmcnt(8)\n\t"
        "v_lshlrev_b32 v122, 1, v122\n\t"
        "v_lshl_or_b32 v123, v123, 1, 1\n\t"
        "v_min_u32 v122, v122, v123\n\t"
        "s_waitcnt vmcnt(6)\n\t"
        "v_mov_b32 v16, v90\n\t"
        "v_mov_b32 v17, v94\n\t"
        "v_min_u32_dpp v122, v122, v122 quad_perm:[1,0,3,2] row_mask:0xf bank_mask:0xf\n\t"
        "v_mov_b32 v18, v91\n\t"
        "v_mov_b32 v19, v95\n\t"
        "v_min_u32_dpp v122, v122, v122 quad_perm:[2,3,0,1] row_mask:0xf bank_mask:0xf\n\t"
        "v_mov_b32 v20, v92\n\t"
        "v_mov_b32 v21, v96\n\t"
        "v_min_u32_dpp v122, v122, v122 row_ror:4 row_mask:0xf bank_mask:0xf\n\t"
        "v_mov_b32 v22, v93\n\t"
        "v_mov_b32 v23, v97\n\t"
        "v_readfirstlane_b32 s96, v122\n\t"
        "s_waitcnt vmcnt(4)\n\t"
        "v_mov_b32 v24, v98\n\t"
        "v_mov_b32 v25, v102\n\t"
        "v_mov_b32 v26, v99\n\t"
        "v_mov_b32 v27, v103\n\t"
        "v_mov_b32 v28, v100\n\t"
        "v_mov_b32 v29, v104\n\t"
        "v_mov_b32 v30, v101\n\t"
        "v_mov_b32 v31, v105\n\t"
        "s_waitcnt vmcnt(2)\n\t"
        "v_mov_b32 v32, v106\n\t"
        "v_mov_b32 v33, v110\n\t"
        "v_mov_b32 v34, v107\n\t"
        "v_mov_b32 v35, v111\n\t"
        "v_mov_b32 v36, v108\n\t"
        "v_mov_b32 v37, v112\n\t"
        "v_mov_b32 v38, v109\n\t"
        "v_mov_b32 v39, v113\n\t"
        "s_max_u32 %[have], %[have], s96\n\t"
        "s_waitcnt vmcnt(0)\n\t"
        "v_mov_b32 v40, v114\n\t"
        "v_mov_b32 v41, v118\n\t"
        "v_mov_b32 v42, v115\n\t"
        "v_mov_b32 v43, v119\n\t"
        "v_mov_b32 v44, v116\n\t"
        "v_mov_b32 v45, v120\n\t"
        "v_mov_b32 v46, v117\n\t"
        "v_mov_b32 v47, v121\n\t"
        "s_branch Lp%=\n\t"
        "Lc%=:\n\t"
        "v_max_u32 v88, v48, v49\n\t"
        "v_max3_u32 v88, v88, v50, v51\n\t"
        "v_max3_u32 v88, v88, v52, v53\n\t"
        "v_max3_u32 v88, v88, v54, v55\n\t"
        "v_max3_u32 v88, v88, v56, v57\n\t"
        "v_max3_u32 v88, v88, v58, v59\n\t"
        "v_max3_u32 v88, v88, v60, v61\n\t"
        "v_max3_u32 v88, v88, v62, v63\n\t"
        "v_cmp_eq_u32 vcc, 0xffbfffbf, v88\n\t"
        "s_mov_b32 s94, 1\n\t"
        "s_cbranch_vccz Ld%=\n\t"
        "s_sub_u32 %[left], %[left], 1\n\t"
        "s_cbranch_scc0 Lg%=\n\t"
        "s_mov_b32 %[left], 0\n\t"
        "Ld%=:\n\t"
        "ds_write_b32 %[fa], v124\n\t"
        "s_waitcnt lgkmcnt(0)\n\t"
        "s_barrier\n\t"
        "s_branch Lok%=\n\t"
        "Lr%=:\n\t"
        "s_mov_b32 %[why], 3\n\t"
        "s_branch Lend%=\n\t"
        "Lz%=:\n\t"
        "s_mov_b32 %[why], 0\n\t"
        "Lend%=:\n\t"
        "s_waitcnt vmcnt(0)\n\t"
        : [bn] "+{v[16:47]}"(bn), [s] "+&s"(s), [have] "+&s"(have), [left] "+&s"(left), [why] "=&s"(why)
        : [sidx] "v"(sidx), [eo] "v"(eo), [uw] "v"(uw), [fa] "v"(fa), [vm] "v"(vm), [fo] "v"(fo), [te] "v"(te), [tb] "v"(tb), [rs] "s"(rs), [lag] "s"(lag), [lb] "s"(lb)
        : "s82", "s83", "s84", "s85", "s86", "s87", "s88", "s89", "s90", "s91", "s92", "s93", "s94", "s95", "s96", "s97", "s98", "s99", "v48", "v49", "v50", "v51", "v52", "v53", "v54", "v55", "v56", "v57", "v58", "v59", "v60", "v61", "v62", "v63", "v64", "v65", "v66", "v67", "v68", "v69", "v70", "v71", "v72", "v73", "v74", "v75", "v76", "v77", "v78", "v79", "v80", "v81", "v82", "v83", "v84", "v85", "v86", "v87", "v88", "v89", "v90", "v91", "v92", "v93", "v94", "v95", "v96", "v97", "v98", "v99", "v100", "v101", "v102", "v103", "v104", "v105", "v106", "v107", "v108", "v109", "v110", "v111", "v112", "v113", "v114", "v115", "v116", "v117", "v118", "v119", "v120", "v121", "v122", "v123", "v124", "v125", "v126", "vcc", "scc", "memory");
}

__device__ __forceinline__ void r_f(u32& bad, u32& pass, u32 sidx, u32 pv, u32 zv, u32 ra, u32 rb, u32 mo, u32 uf, u32x4 rs, u32 so, u32 sz, u32 sm, u32 lb, u32 fv, u32 cap, u32 zp STP) {
    asm volatile(
        "v_mov_b32 v120, %[lb]\n\t"
        "s_add_u32 s92, %[sm], 0x1000\n\t"
        "s_add_u32 s93, %[sm], 0x4000\n\t"
        "s_add_u32 s94, %[sm], 0x5000\n\t"
        "s_add_u32 s95, %[so], 0x8000\n\t"
        "s_add_u32 s96, %[sz], 0x1000\n\t"
        "s_mov_b32 %[pass], 0\n\t"
        "s_mov_b32 s97, 0\n\t"
        "s_mov_b32 %[bad], 0\n\t"
        "s_setprio 3\n\t"
        "s_barrier\n\t"
        "ds_read_b32 v116, v120 offset:16384\n\t"
        "s_waitcnt lgkmcnt(0)\n\t"
        "v_readfirstlane_b32 s98, v116\n\t"
        "s_nop 3\n\t"
        "s_getpc_b64 s[90:91]\n\t"
        "Lgp_%=:\n\t"
        "s_sub_u32 s90, s90, s98\n\t"
        "s_subb_u32 s91, s91, 0\n\t"
        "s_add_u32 s90, s90, Lzp_%=-Lgp_%=\n\t"
        "s_addc_u32 s91, s91, 0\n\t"
        "s_setpc_b64 s[90:91]\n\t"
        "s_nop 0\n\ts_nop 0\n\ts_nop 0\n\ts_nop 0\n\t"
        "s_nop 0\n\ts_nop 0\n\ts_nop 0\n\ts_nop 0\n\t"
        "s_nop 0\n\ts_nop 0\n\ts_nop 0\n\ts_nop 0\n\t"
        "s_nop 0\n\ts_nop 0\n\ts_nop 0\n\ts_nop 0\n\t"
        "s_nop 0\n\ts_nop 0\n\ts_nop 0\n\ts_nop 0\n\t"
        "s_nop 0\n\ts_nop 0\n\ts_nop 0\n\ts_nop 0\n\t"
        "s_nop 0\n\ts_nop 0\n\ts_nop 0\n\ts_nop 0\n\t"
        "s_nop 0\n\ts_nop 0\n\ts_nop 0\n\ts_nop 0\n\t"
        "s_nop 0\n\ts_nop 0\n\ts_nop 0\n\ts_nop 0\n\t"
        "s_nop 0\n\ts_nop 0\n\ts_nop 0\n\ts_nop 0\n\t"
        "s_nop 0\n\ts_nop 0\n\ts_nop 0\n\ts_nop 0\n\t"
        "s_nop 0\n\ts_nop 0\n\ts_nop 0\n\ts_nop 0\n\t"
        "s_nop 0\n\ts_nop 0\n\ts_nop 0\n\ts_nop 0\n\t"
        "s_nop 0\n\ts_nop 0\n\ts_nop 0\n\ts_nop 0\n\t"
        "s_nop 0\n\ts_nop 0\n\ts_nop 0\n\ts_nop 0\n\t"
        "s_nop 0\n\ts_nop 0\n\ts_nop 0\n\ts_nop 0\n\t"
        "s_nop 0\n\ts_nop 0\n\ts_nop 0\n\ts_nop 0\n\t"
        "s_nop 0\n\ts_nop 0\n\ts_nop 0\n\ts_nop 0\n\t"
        "s_nop 0\n\ts_nop 0\n\ts_nop 0\n\ts_nop 0\n\t"
        "s_nop 0\n\ts_nop 0\n\ts_nop 0\n\ts_nop 0\n\t"
        "s_nop 0\n\ts_nop 0\n\ts_nop 0\n\ts_nop 0\n\t"
        "s_nop 0\n\ts_nop 0\n\ts_nop 0\n\ts_nop 0\n\t"
        "s_nop 0\n\ts_nop 0\n\ts_nop 0\n\ts_nop 0\n\t"
        "s_nop 0\n\ts_nop 0\n\ts_nop 0\n\ts_nop 0\n\t"
        "s_nop 0\n\ts_nop 0\n\ts_nop 0\n\ts_nop 0\n\t"
        "s_nop 0\n\ts_nop 0\n\ts_nop 0\n\ts_nop 0\n\t"
        "s_nop 0\n\ts_nop 0\n\ts_nop 0\n\ts_nop 0\n\t"
        "s_nop 0\n\ts_nop 0\n\ts_nop 0\n\ts_nop 0\n\t"
        "s_nop 0\n\ts_nop 0\n\ts_nop 0\n\ts_nop 0\n\t"
        "s_nop 0\n\ts_nop 0\n\ts_nop 0\n\ts_nop 0\n\t"
        "s_nop 0\n\ts_nop 0\n\ts_nop 0\n\ts_nop 0\n\t"
        "s_nop 0\n\ts_nop 0\n\ts_nop 0\n\ts_nop 0\n\t"
        "Lzp_%=:\n\t"
        "buffer_load_dwordx4 v[48:51], %[zv], %[rs], %[sz] offen offset:0 sc1\n\t"
        "buffer_load_dwordx4 v[52:55], %[zv], %[rs], %[sz] offen offset:1024 sc1\n\t"
        "buffer_load_dwordx4 v[56:59], %[zv], %[rs], %[sz] offen offset:2048 sc1\n\t"
        "s_getpc_b64 s[90:91]\n\t"
        "Lgq_%=:\n\t"
        "s_sub_u32 s90, s90, %[zp]\n\t"
        "s_subb_u32 s91, s91, 0\n\t"
        "s_add_u32 s90, s90, Lzq_%=-Lgq_%=\n\t"
        "s_addc_u32 s91, s91, 0\n\t"
        "s_setpc_b64 s[90:91]\n\t"
        "s_nop 0\n\ts_nop 0\n\ts_nop 0\n\ts_nop 0\n\t"
        "s_nop 0\n\ts_nop 0\n\ts_nop 0\n\ts_nop 0\n\t"
        "s_nop 0\n\ts_nop 0\n\ts_nop 0\n\ts_nop 0\n\t"
        "s_nop 0\n\ts_nop 0\n\ts_nop 0\n\ts_nop 0\n\t"
        "s_nop 0\n\ts_nop 0\n\ts_nop 0\n\ts_nop 0\n\t"
        "s_nop 0\n\ts_nop 0\n\ts_nop 0\n\ts_nop 0\n\t"
        "s_nop 0\n\ts_nop 0\n\ts_nop 0\n\ts_nop 0\n\t"
        "s_nop 0\n\ts_nop 0\n\ts_nop 0\n\ts_nop 0\n\t"
        "s_nop 0\n\ts_nop 0\n\ts_nop 0\n\ts_nop 0\n\t"
        "s_nop 0\n\ts_nop 0\n\ts_nop 0\n\ts_nop 0\n\t"
        "s_nop 0\n\ts_nop 0\n\ts_nop 0\n\ts_nop 0\n\t"
        "s_nop 0\n\ts_nop 0\n\ts_nop 0\n\ts_nop 0\n\t"
        "s_nop 0\n\ts_nop 0\n\ts_nop 0\n\ts_nop 0\n\t"
        "s_nop 0\n\ts_nop 0\n\ts_nop 0\n\ts_nop 0\n\t"
        "s_nop 0\n\ts_nop 0\n\ts_nop 0\n\ts_nop 0\n\t"
        "s_nop 0\n\ts_nop 0\n\ts_nop 0\n\ts_nop 0\n\t"
        "Lzq_%=:\n\t"
        "buffer_load_dwordx4 v[14:17], %[pv], %[rs], %[so] offen offset:0 sc1\n\t"
        "buffer_load_dwordx4 v[60:63], %[zv], %[rs], %[sz] offen offset:3072 sc1\n\t"
        "buffer_load_dwordx4 v[18:21], %[pv], %[rs], %[so] offen offset:256 sc1\n\t"
        "buffer_load_dwordx4 v[64:67], %[zv], %[rs], s96 offen offset:0 sc1\n\t"
        "buffer_load_dwordx4 v[22:25], %[pv], %[rs], %[so] offen offset:1024 sc1\n\t"
        "buffer_load_dwordx4 v[68:71], %[zv], %[rs], s96 offen offset:1024 sc1\n\t"
        "buffer_load_dwordx4 v[26:29], %[pv], %[rs], %[so] offen offset:1280 sc1\n\t"
        "buffer_load_dwordx4 v[72:75], %[zv], %[rs], s96 offen offset:2048 sc1\n\t"
        "buffer_load_dwordx4 v[30:33], %[pv], %[rs], s95 offen offset:0 sc1\n\t"
        "buffer_load_dwordx4 v[76:79], %[zv], %[rs], s96 offen offset:3072 sc1\n\t"
        "buffer_load_dwordx4 v[34:37], %[pv], %[rs], s95 offen offset:256 sc1\n\t"
        "buffer_load_dwordx4 v[38:41], %[pv], %[rs], s95 offen offset:1024 sc1\n\t"
        "buffer_load_dwordx4 v[42:45], %[pv], %[rs], s95 offen offset:1280 sc1\n\t"
        "Lrd%=:\n\t"
        "v_mov_b64 v[112:113], 0\n\t"
        "v_mov_b64 v[114:115], 0\n\t"
        "ds_read_b32 v121, %[uf]\n\t"
        "s_waitcnt vmcnt(13)\n\t"
        "ds_read_b128 v[80:83], %[ra] offset:0\n\t"
        "ds_read_b128 v[84:87], %[ra] offset:1024\n\t"
        "s_waitcnt vmcnt(12)\n\t"
        "v_mov_b64 v[46:47], v[14:15]\n\t"
        "s_waitcnt vmcnt(10)\n\t"
        "v_smfmac_f32_16x16x64_bf16 v[112:115], v[16:19], v[48:55], %[sidx]\n\t"
        "s_waitcnt vmcnt(9)\n\t"
        "ds_read_b128 v[88:91], %[ra] offset:2048\n\t"
        "ds_read_b128 v[92:95], %[ra] offset:3072\n\t"
        "s_waitcnt vmcnt(8) lgkmcnt(2)\n\t"
        "v_smfmac_f32_16x16x64_bf16 v[112:115], v[20:23], v[80:87], %[sidx]\n\t"
        "s_waitcnt vmcnt(6)\n\t"
        "v_smfmac_f32_16x16x64_bf16 v[112:115], v[24:27], v[56:63], %[sidx]\n\t"
        "s_waitcnt vmcnt(5)\n\t"
        "ds_read_b128 v[96:99], %[ra] offset:4096\n\t"
        "ds_read_b128 v[100:103], %[ra] offset:5120\n\t"
        "ds_read_b128 v[104:107], %[rb] offset:6144\n\t"
        "ds_read_b128 v[108:111], %[rb] offset:7168\n\t"
        "s_waitcnt vmcnt(4) lgkmcnt(4)\n\t"
        "v_smfmac_f32_16x16x64_bf16 v[112:115], v[28:31], v[88:95], %[sidx]\n\t"
        "s_waitcnt vmcnt(2)\n\t"
        "v_smfmac_f32_16x16x64_bf16 v[112:115], v[32:35], v[64:71], %[sidx]\n\t"
        "s_waitcnt vmcnt(1) lgkmcnt(2)\n\t"
        "v_smfmac_f32_16x16x64_bf16 v[112:115], v[36:39], v[96:103], %[sidx]\n\t"
        "s_waitcnt vmcnt(0)\n\t"
        "v_smfmac_f32_16x16x64_bf16 v[112:115], v[40:43], v[72:79], %[sidx]\n\t"
        "s_waitcnt vmcnt(0) lgkmcnt(0)\n\t"
        "v_smfmac_f32_16x16x64_bf16 v[112:115], v[44:47], v[104:111], %[sidx]\n\t"
        "s_nop 7\n\t"
        "v_add_f32 v116, v112, v114\n\t"
        "v_add_f32 v117, v113, v115\n\t"
        "v_add_f32 v118, v113, v115\n\t"
        "v_mov_b32_dpp v118, v116 row_ror:8 row_mask:0xf bank_mask:0x3\n\t"
        "v_mov_b32_dpp v116, v117 row_ror:8 row_mask:0xf bank_mask:0xc\n\t"
        "v_cvt_pk_bf16_f32 v119, v116, v118\n\t"
        "v_cmpx_o_f32 vcc, v116, v118\n\t"
        "buffer_store_dword v119, %[mo], %[rs], %[sm] offen sc1\n\t"
        "buffer_store_dword v119, %[mo], %[rs], s92 offen sc1\n\t"
        "buffer_store_dword v119, %[mo], %[rs], s93 offen sc1\n\t"
        "buffer_store_dword v119, %[mo], %[rs], s94 offen sc1\n\t"
        "s_nop 2\n\t"
        "s_cmp_eq_u64 exec, -1\n\t"
        "s_mov_b64 exec, -1\n\t"
        "s_cbranch_scc0 Lag%=\n\t"
        "s_barrier\n\t"
        "s_branch Lend%=\n\t"
        "Lag%=:\n\t"
        "v_cmp_ne_u32 vcc, %[fv], v121\n\t"
        "s_add_u32 %[pass], %[pass], 1\n\t"
        "s_cmp_eq_u32 s97, 0\n\t"
        "s_cbranch_scc1 Lfi%=\n\t"
        "s_cbranch_vccnz Lzp_%=\n\t"
        "v_max_u32 v116, v16, v17\n\t"
        "v_max3_u32 v116, v116, v18, v19\n\t"
        "v_max3_u32 v116, v116, v20, v21\n\t"
        "v_max3_u32 v116, v116, v22, v23\n\t"
        "v_max3_u32 v116, v116, v24, v25\n\t"
        "v_max3_u32 v116, v116, v26, v27\n\t"
        "v_max3_u32 v116, v116, v28, v29\n\t"
        "v_max3_u32 v116, v116, v30, v31\n\t"
        "v_max3_u32 v116, v116, v32, v33\n\t"
        "v_max3_u32 v116, v116, v34, v35\n\t"
        "v_max3_u32 v116, v116, v36, v37\n\t"
        "v_max3_u32 v116, v116, v38, v39\n\t"
        "v_max3_u32 v116, v116, v40, v41\n\t"
        "v_max3_u32 v116, v116, v42, v43\n\t"
        "v_max3_u32 v116, v116, v44, v45\n\t"
        "v_max3_u32 v116, v116, v46, v47\n\t"
        "v_max3_u32 v116, v116, v48, v49\n\t"
        "v_max3_u32 v116, v116, v50, v51\n\t"
        "v_max3_u32 v116, v116, v52, v53\n\t"
        "v_max3_u32 v116, v116, v54, v55\n\t"
        "v_max3_u32 v116, v116, v56, v57\n\t"
        "v_max3_u32 v116, v116, v58, v59\n\t"
        "v_max3_u32 v116, v116, v60, v61\n\t"
        "v_max3_u32 v116, v116, v62, v63\n\t"
        "v_max3_u32 v116, v116, v64, v65\n\t"
        "v_max3_u32 v116, v116, v66, v67\n\t"
        "v_max3_u32 v116, v116, v68, v69\n\t"
        "v_max3_u32 v116, v116, v70, v71\n\t"
        "v_max3_u32 v116, v116, v72, v73\n\t"
        "v_max3_u32 v116, v116, v74, v75\n\t"
        "v_max3_u32 v116, v116, v76, v77\n\t"
        "v_max3_u32 v116, v116, v78, v79\n\t"
        "v_max3_u32 v116, v116, v80, v81\n\t"
        "v_max3_u32 v116, v116, v82, v83\n\t"
        "v_max3_u32 v116, v116, v84, v85\n\t"
        "v_max3_u32 v116, v116, v86, v87\n\t"
        "v_max3_u32 v116, v116, v88, v89\n\t"
        "v_max3_u32 v116, v116, v90, v91\n\t"
        "v_max3_u32 v116, v116, v92, v93\n\t"
        "v_max3_u32 v116, v116, v94, v95\n\t"
        "v_max3_u32 v116, v116, v96, v97\n\t"
        "v_max3_u32 v116, v116, v98, v99\n\t"
        "v_max3_u32 v116, v116, v100, v101\n\t"
        "v_max3_u32 v116, v116, v102, v103\n\t"
        "v_max3_u32 v116, v116, v104, v105\n\t"
        "v_max3_u32 v116, v116, v106, v107\n\t"
        "v_max3_u32 v116, v116, v108, v109\n\t"
        "v_max3_u32 v116, v116, v110, v111\n\t"
        "v_cmp_eq_u32 vcc, 0xffbfffbf, v116\n\t"
        "s_cmp_lt_u32 %[pass], %[cap]\n\t"
        "s_cbranch_vccz Lbad%=\n\t"
        "s_cbranch_scc1 Lzp_%=\n\t"
        "Lbad%=:\n\t"
        "s_mov_b32 %[bad], 1\n\t"
        "s_branch Lend%=\n\t"
        "Lfi%=:\n\t"
        "s_mov_b32 s97, 1\n\t"
        "v_mov_b32 v116, %[fv]\n\t"
        "ds_write_b32 v120, v116 offset:16392\n\t"
        "s_waitcnt lgkmcnt(0)\n\t"
        "s_barrier\n\t"
        "s_branch Lrd%=\n\t"
        "Lend%=:\n\t"
        "s_setprio 0\n\t"
        : [bad] "=&s"(bad), [pass] "=&s"(pass)
        : [sidx] "v"(sidx), [pv] "v"(pv), [zv] "v"(zv), [ra] "v"(ra), [rb] "v"(rb), [mo] "v"(mo), [uf] "v"(uf), [rs] "s"(rs), [so] "s"(so), [sz] "s"(sz), [sm] "s"(sm), [lb] "s"(lb), [fv] "s"(fv), [cap] "s"(cap), [zp] "s"(zp)
        : "s88", "s89", "s90", "s91", "s92", "s93", "s94", "s95", "s96", "s97", "s98", "v14", "v15", "v16", "v17", "v18", "v19", "v20", "v21", "v22", "v23", "v24", "v25", "v26", "v27", "v28", "v29", "v30", "v31", "v32", "v33", "v34", "v35", "v36", "v37", "v38", "v39", "v40", "v41", "v42", "v43", "v44", "v45", "v46", "v47", "v48", "v49", "v50", "v51", "v52", "v53", "v54", "v55", "v56", "v57", "v58", "v59", "v60", "v61", "v62", "v63", "v64", "v65", "v66", "v67", "v68", "v69", "v70", "v71", "v72", "v73", "v74", "v75", "v76", "v77", "v78", "v79", "v80", "v81", "v82", "v83", "v84", "v85", "v86", "v87", "v88", "v89", "v90", "v91", "v92", "v93", "v94", "v95", "v96", "v97", "v98", "v99", "v100", "v101", "v102", "v103", "v104", "v105", "v106", "v107", "v108", "v109", "v110", "v111", "v112", "v113", "v114", "v115", "v116", "v117", "v118", "v119", "v120", "v121", "vcc", "scc", "memory");
}

__device__ __forceinline__ void wx0(const u32x32 (&W)[5], u32 xa, u32 oa, u32x4 rs, u32 xv, u32 xs, u32 xd, u32 o0, u32 o1 STP) {
    asm volatile(
        "ds_read_b128 v[192:195], %[xa] offset:0\n\t"
        "ds_read_b128 v[196:199], %[xa] offset:20480\n\t"
        "ds_read_b128 v[200:203], %[xa] offset:1024\n\t"
        "ds_read_b128 v[204:207], %[xa] offset:21504\n\t"
        "ds_read_b128 v[208:211], %[xa] offset:2048\n\t"
        "ds_read_b128 v[212:215], %[xa] offset:22528\n\t"
        "s_waitcnt vmcnt(8)\n\t"
        "s_barrier\n\t"
        "s_waitcnt lgkmcnt(4)\n\t"
        "v_mfma_f32_16x16x32_bf16 v[176:179], v[16:19], v[192:195], 0\n\t"
        "ds_read_b128 v[216:219], %[xa] offset:3072\n\t"
        "v_mfma_f32_16x16x32_bf16 v[184:187], v[16:19], v[196:199], 0\n\t"
        "ds_read_b128 v[220:223], %[xa] offset:23552\n\t"
        "v_mfma_f32_16x16x32_bf16 v[180:183], v[96:99], v[192:195], 0\n\t"
        "v_mfma_f32_16x16x32_bf16 v[188:191], v[96:99], v[196:199], 0\n\t"
        "s_waitcnt lgkmcnt(4)\n\t"
        "v_mfma_f32_16x16x32_bf16 v[176:179], v[20:23], v[200:203], v[176:179]\n\t"
        "ds_read_b128 v[192:195], %[xa] offset:4096\n\t"
        "v_mfma_f32_16x16x32_bf16 v[184:187], v[20:23], v[204:207], v[184:187]\n\t"
        "ds_read_b128 v[196:199], %[xa] offset:24576\n\t"
        "v_mfma_f32_16x16x32_bf16 v[180:183], v[100:103], v[200:203], v[180:183]\n\t"
        "v_mfma_f32_16x16x32_bf16 v[188:191], v[100:103], v[204:207], v[188:191]\n\t"
        "s_waitcnt lgkmcnt(4)\n\t"
        "v_mfma_f32_16x16x32_bf16 v[176:179], v[24:27], v[208:211], v[176:179]\n\t"
        "ds_read_b128 v[200:203], %[xa] offset:5120\n\t"
        "v_mfma_f32_16x16x32_bf16 v[184:187], v[24:27], v[212:215], v[184:187]\n\t"
        "ds_read_b128 v[204:207], %[xa] offset:25600\n\t"
        "v_mfma_f32_16x16x32_bf16 v[180:183], v[104:107], v[208:211], v[180:183]\n\t"
        "v_mfma_f32_16x16x32_bf16 v[188:191], v[104:107], v[212:215], v[188:191]\n\t"
        "s_waitcnt lgkmcnt(4)\n\t"
        "v_mfma_f32_16x16x32_bf16 v[176:179], v[28:31], v[216:219], v[176:179]\n\t"
        "ds_read_b128 v[208:211], %[xa] offset:6144\n\t"
        "v_mfma_f32_16x16x32_bf16 v[184:187], v[28:31], v[220:223], v[184:187]\n\t"
        "ds_read_b128 v[212:215], %[xa] offset:26624\n\t"
        "v_mfma_f32_16x16x32_bf16 v[180:183], v[108:111], v[216:219], v[180:183]\n\t"
        "v_mfma_f32_16x16x32_bf16 v[188:191], v[108:111], v[220:223], v[188:191]\n\t"
        "s_waitcnt lgkmcnt(4)\n\t"
        "v_mfma_f32_16x16x32_bf16 v[176:179], v[32:35], v[192:195], v[176:179]\n\t"
        "ds_read_b128 v[216:219], %[xa] offset:7168\n\t"
        "v_mfma_f32_16x16x32_bf16 v[184:187], v[32:35], v[196:199], v[184:187]\n\t"
        "ds_read_b128 v[220:223], %[xa] offset:27648\n\t"
        "v_mfma_f32_16x16x32_bf16 v[180:183], v[112:115], v[192:195], v[180:183]\n\t"
        "v_mfma_f32_16x16x32_bf16 v[188:191], v[112:115], v[196:199], v[188:191]\n\t"
        "s_waitcnt lgkmcnt(4)\n\t"
        "v_mfma_f32_16x16x32_bf16 v[176:179], v[36:39], v[200:203], v[176:179]\n\t"
        "ds_read_b128 v[192:195], %[xa] offset:8192\n\t"
        "v_mfma_f32_16x16x32_bf16 v[184:187], v[36:39], v[204:207], v[184:187]\n\t"
        "ds_read_b128 v[196:199], %[xa] offset:28672\n\t"
        "v_mfma_f32_16x16x32_bf16 v[180:183], v[116:119], v[200:203], v[180:183]\n\t"
        "v_mfma_f32_16x16x32_bf16 v[188:191], v[116:119], v[204:207], v[188:191]\n\t"
        "s_waitcnt lgkmcnt(4)\n\t"
        "v_mfma_f32_16x16x32_bf16 v[176:179], v[40:43], v[208:211], v[176:179]\n\t"
        "ds_read_b128 v[200:203], %[xa] offset:9216\n\t"
        "v_mfma_f32_16x16x32_bf16 v[184:187], v[40:43], v[212:215], v[184:187]\n\t"
        "ds_read_b128 v[204:207], %[xa] offset:29696\n\t"
        "v_mfma_f32_16x16x32_bf16 v[180:183], v[120:123], v[208:211], v[180:183]\n\t"
        "v_mfma_f32_16x16x32_bf16 v[188:191], v[120:123], v[212:215], v[188:191]\n\t"
        "s_waitcnt lgkmcnt(4)\n\t"
        "v_mfma_f32_16x16x32_bf16 v[176:179], v[44:47], v[216:219], v[176:179]\n\t"
        "ds_read_b128 v[208:211], %[xa] offset:10240\n\t"
        "v_mfma_f32_16x16x32_bf16 v[184:187], v[44:47], v[220:223], v[184:187]\n\t"
        "ds_read_b128 v[212:215], %[xa] offset:30720\n\t"
        "v_mfma_f32_16x16x32_bf16 v[180:183], v[124:127], v[216:219], v[180:183]\n\t"
        "v_mfma_f32_16x16x32_bf16 v[188:191], v[124:127], v[220:223], v[188:191]\n\t"
        "s_waitcnt lgkmcnt(4)\n\t"
        "v_mfma_f32_16x16x32_bf16 v[176:179], v[48:51], v[192:195], v[176:179]\n\t"
        "ds_read_b128 v[216:219], %[xa] offset:11264\n\t"
        "v_mfma_f32_16x16x32_bf16 v[184:187], v[48:51], v[196:199], v[184:187]\n\t"
        "ds_read_b128 v[220:223], %[xa] offset:31744\n\t"
        "v_mfma_f32_16x16x32_bf16 v[180:183], v[128:131], v[192:195], v[180:183]\n\t"
        "v_mfma_f32_16x16x32_bf16 v[188:191], v[128:131], v[196:199], v[188:191]\n\t"
        "s_waitcnt lgkmcnt(4)\n\t"
        "v_mfma_f32_16x16x32_bf16 v[176:179], v[52:55], v[200:203], v[176:179]\n\t"
        "ds_read_b128 v[192:195], %[xa] offset:12288\n\t"
        "v_mfma_f32_16x16x32_bf16 v[184:187], v[52:55], v[204:207], v[184:187]\n\t"
        "ds_read_b128 v[196:199], %[xa] offset:32768\n\t"
        "v_mfma_f32_16x16x32_bf16 v[180:183], v[132:135], v[200:203], v[180:183]\n\t"
        "v_mfma_f32_16x16x32_bf16 v[188:191], v[132:135], v[204:207], v[188:191]\n\t"
        "s_waitcnt lgkmcnt(4)\n\t"
        "v_mfma_f32_16x16x32_bf16 v[176:179], v[56:59], v[208:211], v[176:179]\n\t"
        "ds_read_b128 v[200:203], %[xa] offset:13312\n\t"
        "v_mfma_f32_16x16x32_bf16 v[184:187], v[56:59], v[212:215], v[184:187]\n\t"
        "ds_read_b128 v[204:207], %[xa] offset:33792\n\t"
        "v_mfma_f32_16x16x32_bf16 v[180:183], v[136:139], v[208:211], v[180:183]\n\t"
        "v_mfma_f32_16x16x32_bf16 v[188:191], v[136:139], v[212:215], v[188:191]\n\t"
        "s_waitcnt lgkmcnt(4)\n\t"
        "v_mfma_f32_16x16x32_bf16 v[176:179], v[60:63], v[216:219], v[176:179]\n\t"
        "ds_read_b128 v[208:211], %[xa] offset:14336\n\t"
        "v_mfma_f32_16x16x32_bf16 v[184:187], v[60:63], v[220:223], v[184:187]\n\t"
        "ds_read_b128 v[212:215], %[xa] offset:34816\n\t"
        "v_mfma_f32_16x16x32_bf16 v[180:183], v[140:143], v[216:219], v[180:183]\n\t"
        "v_mfma_f32_16x16x32_bf16 v[188:191], v[140:143], v[220:223], v[188:191]\n\t"
        "s_waitcnt lgkmcnt(4)\n\t"
        "v_mfma_f32_16x16x32_bf16 v[176:179], v[64:67], v[192:195], v[176:179]\n\t"
        "ds_read_b128 v[216:219], %[xa] offset:15360\n\t"
        "v_mfma_f32_16x16x32_bf16 v[184:187], v[64:67], v[196:199], v[184:187]\n\t"
        "ds_read_b128 v[220:223], %[xa] offset:35840\n\t"
        "v_mfma_f32_16x16x32_bf16 v[180:183], v[144:147], v[192:195], v[180:183]\n\t"
        "v_mfma_f32_16x16x32_bf16 v[188:191], v[144:147], v[196:199], v[188:191]\n\t"
        "s_waitcnt lgkmcnt(4)\n\t"
        "v_mfma_f32_16x16x32_bf16 v[176:179], v[68:71], v[200:203], v[176:179]\n\t"
        "ds_read_b128 v[192:195], %[xa] offset:16384\n\t"
        "v_mfma_f32_16x16x32_bf16 v[184:187], v[68:71], v[204:207], v[184:187]\n\t"
        "ds_read_b128 v[196:199], %[xa] offset:36864\n\t"
        "v_mfma_f32_16x16x32_bf16 v[180:183], v[148:151], v[200:203], v[180:183]\n\t"
        "v_mfma_f32_16x16x32_bf16 v[188:191], v[148:151], v[204:207], v[188:191]\n\t"
        "s_waitcnt lgkmcnt(4)\n\t"
        "v_mfma_f32_16x16x32_bf16 v[176:179], v[72:75], v[208:211], v[176:179]\n\t"
        "ds_read_b128 v[200:203], %[xa] offset:17408\n\t"
        "v_mfma_f32_16x16x32_bf16 v[184:187], v[72:75], v[212:215], v[184:187]\n\t"
        "ds_read_b128 v[204:207], %[xa] offset:37888\n\t"
        "v_mfma_f32_16x16x32_bf16 v[180:183], v[152:155], v[208:211], v[180:183]\n\t"
        "v_mfma_f32_16x16x32_bf16 v[188:191], v[152:155], v[212:215], v[188:191]\n\t"
        "s_waitcnt lgkmcnt(4)\n\t"
        "v_mfma_f32_16x16x32_bf16 v[176:179], v[76:79], v[216:219], v[176:179]\n\t"
        "ds_read_b128 v[208:211], %[xa] offset:18432\n\t"
        "v_mfma_f32_16x16x32_bf16 v[184:187], v[76:79], v[220:223], v[184:187]\n\t"
        "ds_read_b128 v[212:215], %[xa] offset:38912\n\t"
        "v_mfma_f32_16x16x32_bf16 v[180:183], v[156:159], v[216:219], v[180:183]\n\t"
        "v_mfma_f32_16x16x32_bf16 v[188:191], v[156:159], v[220:223], v[188:191]\n\t"
        "s_waitcnt lgkmcnt(4)\n\t"
        "v_mfma_f32_16x16x32_bf16 v[176:179], v[80:83], v[192:195], v[176:179]\n\t"
        "ds_read_b128 v[216:219], %[xa] offset:19456\n\t"
        "v_mfma_f32_16x16x32_bf16 v[184:187], v[80:83], v[196:199], v[184:187]\n\t"
        "ds_read_b128 v[220:223], %[xa] offset:39936\n\t"
        "v_mfma_f32_16x16x32_bf16 v[180:183], v[160:163], v[192:195], v[180:183]\n\t"
        "v_mfma_f32_16x16x32_bf16 v[188:191], v[160:163], v[196:199], v[188:191]\n\t"
        "s_waitcnt lgkmcnt(4)\n\t"
        "v_mfma_f32_16x16x32_bf16 v[176:179], v[84:87], v[200:203], v[176:179]\n\t"
        "v_mfma_f32_16x16x32_bf16 v[184:187], v[84:87], v[204:207], v[184:187]\n\t"
        "v_mfma_f32_16x16x32_bf16 v[180:183], v[164:167], v[200:203], v[180:183]\n\t"
        "v_mfma_f32_16x16x32_bf16 v[188:191], v[164:167], v[204:207], v[188:191]\n\t"
        "s_waitcnt lgkmcnt(2)\n\t"
        "v_mfma_f32_16x16x32_bf16 v[176:179], v[88:91], v[208:211], v[176:179]\n\t"
        "v_mfma_f32_16x16x32_bf16 v[184:187], v[88:91], v[212:215], v[184:187]\n\t"
        "v_mfma_f32_16x16x32_bf16 v[180:183], v[168:171], v[208:211], v[180:183]\n\t"
        "v_mfma_f32_16x16x32_bf16 v[188:191], v[168:171], v[212:215], v[188:191]\n\t"
        "s_waitcnt lgkmcnt(0)\n\t"
        "v_mfma_f32_16x16x32_bf16 v[176:179], v[92:95], v[216:219], v[176:179]\n\t"
        "v_mfma_f32_16x16x32_bf16 v[184:187], v[92:95], v[220:223], v[184:187]\n\t"
        "v_mfma_f32_16x16x32_bf16 v[180:183], v[172:175], v[216:219], v[180:183]\n\t"
        "v_mfma_f32_16x16x32_bf16 v[188:191], v[172:175], v[220:223], v[188:191]\n\t"
        "s_nop 4\n\t"
        "v_cvt_pk_bf16_f32 v224, v176, v177\n\t"
        "v_cvt_pk_bf16_f32 v225, v178, v179\n\t"
        "v_cvt_pk_bf16_f32 v226, v180, v181\n\t"
        "v_cvt_pk_bf16_f32 v227, v182, v183\n\t"
        "buffer_store_dwordx4 v[224:227], %[oa], %[rs], %[o0] offen sc1\n\t"
        "v_cvt_pk_bf16_f32 v228, v184, v185\n\t"
        "v_cvt_pk_bf16_f32 v229, v186, v187\n\t"
        "v_cvt_pk_bf16_f32 v230, v188, v189\n\t"
        "v_cvt_pk_bf16_f32 v231, v190, v191\n\t"
        "buffer_store_dwordx4 v[228:231], %[oa], %[rs], %[o1] offen sc1\n\t"
        "s_mov_b32 s88, m0\n\t"
        "s_mov_b32 m0, %[xd]\n\t"
        "s_add_u32 s89, %[xs], 0x1000\n\t"
        "buffer_load_dwordx4 %[xv], %[rs], %[xs] offen offset:0 sc1 lds\n\t"
        "buffer_load_dwordx4 %[xv], %[rs], %[xs] offen offset:1024 sc1 lds\n\t"
        "buffer_load_dwordx4 %[xv], %[rs], %[xs] offen offset:2048 sc1 lds\n\t"
        "buffer_load_dwordx4 %[xv], %[rs], %[xs] offen offset:3072 sc1 lds\n\t"
        "s_add_u32 m0, m0, 0x1000\n\t"
        "s_nop 0\n\t"
        "buffer_load_dwordx4 %[xv], %[rs], s89 offen offset:0 sc1 lds\n\t"
        "buffer_load_dwordx4 %[xv], %[rs], s89 offen offset:1024 sc1 lds\n\t"
        "buffer_load_dwordx4 %[xv], %[rs], s89 offen offset:2048 sc1 lds\n\t"
        "buffer_load_dwordx4 %[xv], %[rs], s89 offen offset:3072 sc1 lds\n\t"
        "s_mov_b32 m0, s88\n\t"
        :
        : [w0] "{v[16:47]}"(W[0]), [w1] "{v[48:79]}"(W[1]), [w2] "{v[80:111]}"(W[2]), [w3] "{v[112:143]}"(W[3]), [w4] "{v[144:175]}"(W[4]), [xa] "v"(xa), [oa] "v"(oa), [xv] "v"(xv), [xs] "s"(xs), [xd] "s"(xd), [rs] "s"(rs), [o0] "s"(o0), [o1] "s"(o1)
        : "s88", "s89", "v176", "v177", "v178", "v179", "v180", "v181", "v182", "v183", "v184", "v185", "v186", "v187", "v188", "v189", "v190", "v191", "v192", "v193", "v194", "v195", "v196", "v197", "v198", "v199", "v200", "v201", "v202", "v203", "v204", "v205", "v206", "v207", "v208", "v209", "v210", "v211", "v212", "v213", "v214", "v215", "v216", "v217", "v218", "v219", "v220", "v221", "v222", "v223", "v224", "v225", "v226", "v227", "v228", "v229", "v230", "v231", "vcc", "scc", "memory");
}

__device__ __forceinline__ void wx1(const u32x32 (&W)[5], u32 xa, u32 oa STP) {
    asm volatile(
        "ds_read_b128 v[192:195], %[xa] offset:0\n\t"
        "ds_read_b128 v[196:199], %[xa] offset:20480\n\t"
        "ds_read_b128 v[200:203], %[xa] offset:1024\n\t"
        "ds_read_b128 v[204:207], %[xa] offset:21504\n\t"
        "ds_read_b128 v[208:211], %[xa] offset:2048\n\t"
        "ds_read_b128 v[212:215], %[xa] offset:22528\n\t"
        "s_waitcnt lgkmcnt(6)\n\t"
        "s_barrier\n\t"
        "s_waitcnt lgkmcnt(4)\n\t"
        "v_mfma_f32_16x16x32_bf16 v[176:179], v[16:19], v[192:195], 0\n\t"
        "ds_read_b128 v[216:219], %[xa] offset:3072\n\t"
        "v_mfma_f32_16x16x32_bf16 v[184:187], v[16:19], v[196:199], 0\n\t"
        "ds_read_b128 v[220:223], %[xa] offset:23552\n\t"
        "v_mfma_f32_16x16x32_bf16 v[180:183], v[96:99], v[192:195], 0\n\t"
        "v_mfma_f32_16x16x32_bf16 v[188:191], v[96:99], v[196:199], 0\n\t"
        "s_waitcnt lgkmcnt(4)\n\t"
        "v_mfma_f32_16x16x32_bf16 v[176:179], v[20:23], v[200:203], v[176:179]\n\t"
        "ds_read_b128 v[192:195], %[xa] offset:4096\n\t"
        "v_mfma_f32_16x16x32_bf16 v[184:187], v[20:23], v[204:207], v[184:187]\n\t"
        "ds_read_b128 v[196:199], %[xa] offset:24576\n\t"
        "v_mfma_f32_16x16x32_bf16 v[180:183], v[100:103], v[200:203], v[180:183]\n\t"
        "v_mfma_f32_16x16x32_bf16 v[188:191], v[100:103], v[204:207], v[188:191]\n\t"
        "s_waitcnt lgkmcnt(4)\n\t"
        "v_mfma_f32_16x16x32_bf16 v[176:179], v[24:27], v[208:211], v[176:179]\n\t"
        "ds_read_b128 v[200:203], %[xa] offset:5120\n\t"
        "v_mfma_f32_16x16x32_bf16 v[184:187], v[24:27], v[212:215], v[184:187]\n\t"
        "ds_read_b128 v[204:207], %[xa] offset:25600\n\t"
        "v_mfma_f32_16x16x32_bf16 v[180:183], v[104:107], v[208:211], v[180:183]\n\t"
        "v_mfma_f32_16x16x32_bf16 v[188:191], v[104:107], v[212:215], v[188:191]\n\t"
        "s_waitcnt lgkmcnt(4)\n\t"
        "v_mfma_f32_16x16x32_bf16 v[176:179], v[28:31], v[216:219], v[176:179]\n\t"
        "ds_read_b128 v[208:211], %[xa] offset:6144\n\t"
        "v_mfma_f32_16x16x32_bf16 v[184:187], v[28:31], v[220:223], v[184:187]\n\t"
        "ds_read_b128 v[212:215], %[xa] offset:26624\n\t"
        "v_mfma_f32_16x16x32_bf16 v[180:183], v[108:111], v[216:219], v[180:183]\n\t"
        "v_mfma_f32_16x16x32_bf16 v[188:191], v[108:111], v[220:223], v[188:191]\n\t"
        "s_waitcnt lgkmcnt(4)\n\t"
        "v_mfma_f32_16x16x32_bf16 v[176:179], v[32:35], v[192:195], v[176:179]\n\t"
        "ds_read_b128 v[216:219], %[xa] offset:7168\n\t"
        "v_mfma_f32_16x16x32_bf16 v[184:187], v[32:35], v[196:199], v[184:187]\n\t"
        "ds_read_b128 v[220:223], %[xa] offset:27648\n\t"
        "v_mfma_f32_16x16x32_bf16 v[180:183], v[112:115], v[192:195], v[180:183]\n\t"
        "v_mfma_f32_16x16x32_bf16 v[188:191], v[112:115], v[196:199], v[188:191]\n\t"
        "s_waitcnt lgkmcnt(4)\n\t"
        "v_mfma_f32_16x16x32_bf16 v[176:179], v[36:39], v[200:203], v[176:179]\n\t"
        "ds_read_b128 v[192:195], %[xa] offset:8192\n\t"
        "v_mfma_f32_16x16x32_bf16 v[184:187], v[36:39], v[204:207], v[184:187]\n\t"
        "ds_read_b128 v[196:199], %[xa] offset:28672\n\t"
        "v_mfma_f32_16x16x32_bf16 v[180:183], v[116:119], v[200:203], v[180:183]\n\t"
        "v_mfma_f32_16x16x32_bf16 v[188:191], v[116:119], v[204:207], v[188:191]\n\t"
        "s_waitcnt lgkmcnt(4)\n\t"
        "v_mfma_f32_16x16x32_bf16 v[176:179], v[40:43], v[208:211], v[176:179]\n\t"
        "ds_read_b128 v[200:203], %[xa] offset:9216\n\t"
        "v_mfma_f32_16x16x32_bf16 v[184:187], v[40:43], v[212:215], v[184:187]\n\t"
        "ds_read_b128 v[204:207], %[xa] offset:29696\n\t"
        "v_mfma_f32_16x16x32_bf16 v[180:183], v[120:123], v[208:211], v[180:183]\n\t"
        "v_mfma_f32_16x16x32_bf16 v[188:191], v[120:123], v[212:215], v[188:191]\n\t"
        "s_waitcnt lgkmcnt(4)\n\t"
        "v_mfma_f32_16x16x32_bf16 v[176:179], v[44:47], v[216:219], v[176:179]\n\t"
        "ds_read_b128 v[208:211], %[xa] offset:10240\n\t"
        "v_mfma_f32_16x16x32_bf16 v[184:187], v[44:47], v[220:223], v[184:187]\n\t"
        "ds_read_b128 v[212:215], %[xa] offset:30720\n\t"
        "v_mfma_f32_16x16x32_bf16 v[180:183], v[124:127], v[216:219], v[180:183]\n\t"
        "v_mfma_f32_16x16x32_bf16 v[188:191], v[124:127], v[220:223], v[188:191]\n\t"
        "s_waitcnt lgkmcnt(4)\n\t"
        "v_mfma_f32_16x16x32_bf16 v[176:179], v[48:51], v[192:195], v[176:179]\n\t"
        "ds_read_b128 v[216:219], %[xa] offset:11264\n\t"
        "v_mfma_f32_16x16x32_bf16 v[184:187], v[48:51], v[196:199], v[184:187]\n\t"
        "ds_read_b128 v[220:223], %[xa] offset:31744\n\t"
        "v_mfma_f32_16x16x32_bf16 v[180:183], v[128:131], v[192:195], v[180:183]\n\t"
        "v_mfma_f32_16x16x32_bf16 v[188:191], v[128:131], v[196:199], v[188:191]\n\t"
        "s_waitcnt lgkmcnt(4)\n\t"
        "v_mfma_f32_16x16x32_bf16 v[176:179], v[52:55], v[200:203], v[176:179]\n\t"
        "ds_read_b128 v[192:195], %[xa] offset:12288\n\t"
        "v_mfma_f32_16x16x32_bf16 v[184:187], v[52:55], v[204:207], v[184:187]\n\t"
        "ds_read_b128 v[196:199], %[xa] offset:32768\n\t"
        "v_mfma_f32_16x16x32_bf16 v[180:183], v[132:135], v[200:203], v[180:183]\n\t"
        "v_mfma_f32_16x16x32_bf16 v[188:191], v[132:135], v[204:207], v[188:191]\n\t"
        "s_waitcnt lgkmcnt(4)\n\t"
        "v_mfma_f32_16x16x32_bf16 v[176:179], v[56:59], v[208:211], v[176:179]\n\t"
        "ds_read_b128 v[200:203], %[xa] offset:13312\n\t"
        "v_mfma_f32_16x16x32_bf16 v[184:187], v[56:59], v[212:215], v[184:187]\n\t"
        "ds_read_b128 v[204:207], %[xa] offset:33792\n\t"
        "v_mfma_f32_16x16x32_bf16 v[180:183], v[136:139], v[208:211], v[180:183]\n\t"
        "v_mfma_f32_16x16x32_bf16 v[188:191], v[136:139], v[212:215], v[188:191]\n\t"
        "s_waitcnt lgkmcnt(4)\n\t"
        "v_mfma_f32_16x16x32_bf16 v[176:179], v[60:63], v[216:219], v[176:179]\n\t"
        "ds_read_b128 v[208:211], %[xa] offset:14336\n\t"
        "v_mfma_f32_16x16x32_bf16 v[184:187], v[60:63], v[220:223], v[184:187]\n\t"
        "ds_read_b128 v[212:215], %[xa] offset:34816\n\t"
        "v_mfma_f32_16x16x32_bf16 v[180:183], v[140:143], v[216:219], v[180:183]\n\t"
        "v_mfma_f32_16x16x32_bf16 v[188:191], v[140:143], v[220:223], v[188:191]\n\t"
        "s_waitcnt lgkmcnt(4)\n\t"
        "v_mfma_f32_16x16x32_bf16 v[176:179], v[64:67], v[192:195], v[176:179]\n\t"
        "ds_read_b128 v[216:219], %[xa] offset:15360\n\t"
        "v_mfma_f32_16x16x32_bf16 v[184:187], v[64:67], v[196:199], v[184:187]\n\t"
        "ds_read_b128 v[220:223], %[xa] offset:35840\n\t"
        "v_mfma_f32_16x16x32_bf16 v[180:183], v[144:147], v[192:195], v[180:183]\n\t"
        "v_mfma_f32_16x16x32_bf16 v[188:191], v[144:147], v[196:199], v[188:191]\n\t"
        "s_waitcnt lgkmcnt(4)\n\t"
        "v_mfma_f32_16x16x32_bf16 v[176:179], v[68:71], v[200:203], v[176:179]\n\t"
        "ds_read_b128 v[192:195], %[xa] offset:16384\n\t"
        "v_mfma_f32_16x16x32_bf16 v[184:187], v[68:71], v[204:207], v[184:187]\n\t"
        "ds_read_b128 v[196:199], %[xa] offset:36864\n\t"
        "v_mfma_f32_16x16x32_bf16 v[180:183], v[148:151], v[200:203], v[180:183]\n\t"
        "v_mfma_f32_16x16x32_bf16 v[188:191], v[148:151], v[204:207], v[188:191]\n\t"
        "s_waitcnt lgkmcnt(4)\n\t"
        "v_mfma_f32_16x16x32_bf16 v[176:179], v[72:75], v[208:211], v[176:179]\n\t"
        "ds_read_b128 v[200:203], %[xa] offset:17408\n\t"
        "v_mfma_f32_16x16x32_bf16 v[184:187], v[72:75], v[212:215], v[184:187]\n\t"
        "ds_read_b128 v[204:207], %[xa] offset:37888\n\t"
        "v_mfma_f32_16x16x32_bf16 v[180:183], v[152:155], v[208:211], v[180:183]\n\t"
        "v_mfma_f32_16x16x32_bf16 v[188:191], v[152:155], v[212:215], v[188:191]\n\t"
        "s_waitcnt lgkmcnt(4)\n\t"
        "v_mfma_f32_16x16x32_bf16 v[176:179], v[76:79], v[216:219], v[176:179]\n\t"
        "ds_read_b128 v[208:211], %[xa] offset:18432\n\t"
        "v_mfma_f32_16x16x32_bf16 v[184:187], v[76:79], v[220:223], v[184:187]\n\t"
        "ds_read_b128 v[212:215], %[xa] offset:38912\n\t"
        "v_mfma_f32_16x16x32_bf16 v[180:183], v[156:159], v[216:219], v[180:183]\n\t"
        "v_mfma_f32_16x16x32_bf16 v[188:191], v[156:159], v[220:223], v[188:191]\n\t"
        "s_waitcnt lgkmcnt(4)\n\t"
        "v_mfma_f32_16x16x32_bf16 v[176:179], v[80:83], v[192:195], v[176:179]\n\t"
        "ds_read_b128 v[216:219], %[xa] offset:19456\n\t"
        "v_mfma_f32_16x16x32_bf16 v[184:187], v[80:83], v[196:199], v[184:187]\n\t"
        "ds_read_b128 v[220:223], %[xa] offset:39936\n\t"
        "v_mfma_f32_16x16x32_bf16 v[180:183], v[160:163], v[192:195], v[180:183]\n\t"
        "v_mfma_f32_16x16x32_bf16 v[188:191], v[160:163], v[196:199], v[188:191]\n\t"
        "s_waitcnt lgkmcnt(4)\n\t"
        "v_mfma_f32_16x16x32_bf16 v[176:179], v[84:87], v[200:203], v[176:179]\n\t"
        "v_mfma_f32_16x16x32_bf16 v[184:187], v[84:87], v[204:207], v[184:187]\n\t"
        "v_mfma_f32_16x16x32_bf16 v[180:183], v[164:167], v[200:203], v[180:183]\n\t"
        "v_mfma_f32_16x16x32_bf16 v[188:191], v[164:167], v[204:207], v[188:191]\n\t"
        "s_waitcnt lgkmcnt(2)\n\t"
        "v_mfma_f32_16x16x32_bf16 v[176:179], v[88:91], v[208:211], v[176:179]\n\t"
        "v_mfma_f32_16x16x32_bf16 v[184:187], v[88:91], v[212:215], v[184:187]\n\t"
        "v_mfma_f32_16x16x32_bf16 v[180:183], v[168:171], v[208:211], v[180:183]\n\t"
        "v_mfma_f32_16x16x32_bf16 v[188:191], v[168:171], v[212:215], v[188:191]\n\t"
        "s_waitcnt lgkmcnt(0)\n\t"
        "v_mfma_f32_16x16x32_bf16 v[176:179], v[92:95], v[216:219], v[176:179]\n\t"
        "v_mfma_f32_16x16x32_bf16 v[184:187], v[92:95], v[220:223], v[184:187]\n\t"
        "v_mfma_f32_16x16x32_bf16 v[180:183], v[172:175], v[216:219], v[180:183]\n\t"
        "v_mfma_f32_16x16x32_bf16 v[188:191], v[172:175], v[220:223], v[188:191]\n\t"
        "s_nop 4\n\t"
        "v_cvt_pk_bf16_f32 v224, v176, v177\n\t"
        "v_cvt_pk_bf16_f32 v225, v178, v179\n\t"
        "v_cvt_pk_bf16_f32 v226, v180, v181\n\t"
        "v_cvt_pk_bf16_f32 v227, v182, v183\n\t"
        "ds_write_b128 %[oa], v[224:227] offset:0\n\t"
        "v_cvt_pk_bf16_f32 v228, v184, v185\n\t"
        "v_cvt_pk_bf16_f32 v229, v186, v187\n\t"
        "v_cvt_pk_bf16_f32 v230, v188, v189\n\t"
        "v_cvt_pk_bf16_f32 v231, v190, v191\n\t"
        "ds_write_b128 %[oa], v[228:231] offset:4096\n\t"
        :
        : [w0] "{v[16:47]}"(W[0]), [w1] "{v[48:79]}"(W[1]), [w2] "{v[80:111]}"(W[2]), [w3] "{v[112:143]}"(W[3]), [w4] "{v[144:175]}"(W[4]), [xa] "v"(xa), [oa] "v"(oa)
        : "v176", "v177", "v178", "v179", "v180", "v181", "v182", "v183", "v184", "v185", "v186", "v187", "v188", "v189", "v190", "v191", "v192", "v193", "v194", "v195", "v196", "v197", "v198", "v199", "v200", "v201", "v202", "v203", "v204", "v205", "v206", "v207", "v208", "v209", "v210", "v211", "v212", "v213", "v214", "v215", "v216", "v217", "v218", "v219", "v220", "v221", "v222", "v223", "v224", "v225", "v226", "v227", "v228", "v229", "v230", "v231", "vcc", "scc", "memory");
}

__device__ __forceinline__ void wk(const u32x32 (&W)[5], u32 xa, u32 oa, u32x4 rs, u32 o0, u32 o1, float sc, u32 xv, u32 xs, u32 xd, u32 ca, u32 fc, u32 fw, u32 fo STP) {
    asm volatile(
        "ds_read_b128 v[192:195], %[xa] offset:0\n\t"
        "ds_read_b128 v[196:199], %[xa] offset:20480\n\t"
        "ds_read_b128 v[200:203], %[xa] offset:1024\n\t"
        "ds_read_b128 v[204:207], %[xa] offset:21504\n\t"
        "ds_read_b128 v[208:211], %[xa] offset:2048\n\t"
        "ds_read_b128 v[212:215], %[xa] offset:22528\n\t"
        "v_mov_b32 v232, %[ca]\n\t"
        "v_mov_b32 v233, 1\n\t"
        "v_mov_b32 v234, %[fw]\n\t"
        "s_mov_b32 s88, m0\n\t"
        "s_mov_b32 m0, %[xd]\n\t"
        "s_add_u32 s89, %[xs], 0x1000\n\t"
        "buffer_load_dwordx4 %[xv], %[rs], %[xs] offen offset:0 sc1 lds\n\t"
        "buffer_load_dwordx4 %[xv], %[rs], %[xs] offen offset:1024 sc1 lds\n\t"
        "s_mov_b32 m0, s88\n\t"
        "s_waitcnt lgkmcnt(4)\n\t"
        "v_mfma_f32_16x16x32_bf16 v[176:179], v[16:19], v[192:195], 0\n\t"
        "ds_read_b128 v[216:219], %[xa] offset:3072\n\t"
        "v_mfma_f32_16x16x32_bf16 v[184:187], v[16:19], v[196:199], 0\n\t"
        "ds_read_b128 v[220:223], %[xa] offset:23552\n\t"
        "v_mfma_f32_16x16x32_bf16 v[180:183], v[96:99], v[192:195], 0\n\t"
        "v_mfma_f32_16x16x32_bf16 v[188:191], v[96:99], v[196:199], 0\n\t"
        "s_waitcnt lgkmcnt(4)\n\t"
        "v_mfma_f32_16x16x32_bf16 v[176:179], v[20:23], v[200:203], v[176:179]\n\t"
        "ds_read_b128 v[192:195], %[xa] offset:4096\n\t"
        "v_mfma_f32_16x16x32_bf16 v[184:187], v[20:23], v[204:207], v[184:187]\n\t"
        "ds_read_b128 v[196:199], %[xa] offset:24576\n\t"
        "v_mfma_f32_16x16x32_bf16 v[180:183], v[100:103], v[200:203], v[180:183]\n\t"
        "v_mfma_f32_16x16x32_bf16 v[188:191], v[100:103], v[204:207], v[188:191]\n\t"
        "s_waitcnt lgkmcnt(4)\n\t"
        "v_mfma_f32_16x16x32_bf16 v[176:179], v[24:27], v[208:211], v[176:179]\n\t"
        "ds_read_b128 v[200:203], %[xa] offset:5120\n\t"
        "v_mfma_f32_16x16x32_bf16 v[184:187], v[24:27], v[212:215], v[184:187]\n\t"
        "ds_read_b128 v[204:207], %[xa] offset:25600\n\t"
        "v_mfma_f32_16x16x32_bf16 v[180:183], v[104:107], v[208:211], v[180:183]\n\t"
        "v_mfma_f32_16x16x32_bf16 v[188:191], v[104:107], v[212:215], v[188:191]\n\t"
        "s_waitcnt lgkmcnt(4)\n\t"
        "v_mfma_f32_16x16x32_bf16 v[176:179], v[28:31], v[216:219], v[176:179]\n\t"
        "ds_read_b128 v[208:211], %[xa] offset:6144\n\t"
        "v_mfma_f32_16x16x32_bf16 v[184:187], v[28:31], v[220:223], v[184:187]\n\t"
        "ds_read_b128 v[212:215], %[xa] offset:26624\n\t"
        "v_mfma_f32_16x16x32_bf16 v[180:183], v[108:111], v[216:219], v[180:183]\n\t"
        "v_mfma_f32_16x16x32_bf16 v[188:191], v[108:111], v[220:223], v[188:191]\n\t"
        "s_waitcnt lgkmcnt(4)\n\t"
        "v_mfma_f32_16x16x32_bf16 v[176:179], v[32:35], v[192:195], v[176:179]\n\t"
        "ds_read_b128 v[216:219], %[xa] offset:7168\n\t"
        "v_mfma_f32_16x16x32_bf16 v[184:187], v[32:35], v[196:199], v[184:187]\n\t"
        "ds_read_b128 v[220:223], %[xa] offset:27648\n\t"
        "v_mfma_f32_16x16x32_bf16 v[180:183], v[112:115], v[192:195], v[180:183]\n\t"
        "v_mfma_f32_16x16x32_bf16 v[188:191], v[112:115], v[196:199], v[188:191]\n\t"
        "s_waitcnt lgkmcnt(4)\n\t"
        "v_mfma_f32_16x16x32_bf16 v[176:179], v[36:39], v[200:203], v[176:179]\n\t"
        "ds_read_b128 v[192:195], %[xa] offset:8192\n\t"
        "v_mfma_f32_16x16x32_bf16 v[184:187], v[36:39], v[204:207], v[184:187]\n\t"
        "ds_read_b128 v[196:199], %[xa] offset:28672\n\t"
        "v_mfma_f32_16x16x32_bf16 v[180:183], v[116:119], v[200:203], v[180:183]\n\t"
        "v_mfma_f32_16x16x32_bf16 v[188:191], v[116:119], v[204:207], v[188:191]\n\t"
        "s_waitcnt lgkmcnt(4)\n\t"
        "v_mfma_f32_16x16x32_bf16 v[176:179], v[40:43], v[208:211], v[176:179]\n\t"
        "ds_read_b128 v[200:203], %[xa] offset:9216\n\t"
        "v_mfma_f32_16x16x32_bf16 v[184:187], v[40:43], v[212:215], v[184:187]\n\t"
        "ds_read_b128 v[204:207], %[xa] offset:29696\n\t"
        "v_mfma_f32_16x16x32_bf16 v[180:183], v[120:123], v[208:211], v[180:183]\n\t"
        "s_waitcnt vmcnt(0)\n\t"
        "s_mov_b64 exec, 1\n\t"
        "ds_add_rtn_u32 v233, v232, v233\n\t"
        "s_mov_b64 exec, -1\n\t"
        "v_mfma_f32_16x16x32_bf16 v[188:191], v[120:123], v[212:215], v[188:191]\n\t"
        "s_waitcnt lgkmcnt(5)\n\t"
        "v_mfma_f32_16x16x32_bf16 v[176:179], v[44:47], v[216:219], v[176:179]\n\t"
        "ds_read_b128 v[208:211], %[xa] offset:10240\n\t"
        "v_mfma_f32_16x16x32_bf16 v[184:187], v[44:47], v[220:223], v[184:187]\n\t"
        "ds_read_b128 v[212:215], %[xa] offset:30720\n\t"
        "v_mfma_f32_16x16x32_bf16 v[180:183], v[124:127], v[216:219], v[180:183]\n\t"
        "v_mfma_f32_16x16x32_bf16 v[188:191], v[124:127], v[220:223], v[188:191]\n\t"
        "s_waitcnt lgkmcnt(5)\n\t"
        "v_mfma_f32_16x16x32_bf16 v[176:179], v[48:51], v[192:195], v[176:179]\n\t"
        "ds_read_b128 v[216:219], %[xa] offset:11264\n\t"
        "v_mfma_f32_16x16x32_bf16 v[184:187], v[48:51], v[196:199], v[184:187]\n\t"
        "ds_read_b128 v[220:223], %[xa] offset:31744\n\t"
        "v_mfma_f32_16x16x32_bf16 v[180:183], v[128:131], v[192:195], v[180:183]\n\t"
        "s_waitcnt lgkmcnt(4)\n\t"
        "s_mov_b64 exec, 1\n\t"
        "v_cmpx_eq_u32 vcc, %[fc], v233\n\t"
        "buffer_store_dword v234, off, %[rs], %[fo] sc1\n\t"
        "s_mov_b64 exec, -1\n\t"
        "v_mfma_f32_16x16x32_bf16 v[188:191], v[128:131], v[196:199], v[188:191]\n\t"
        "s_waitcnt lgkmcnt(5)\n\t"
        "v_mfma_f32_16x16x32_bf16 v[176:179], v[52:55], v[200:203], v[176:179]\n\t"
        "ds_read_b128 v[192:195], %[xa] offset:12288\n\t"
        "v_mfma_f32_16x16x32_bf16 v[184:187], v[52:55], v[204:207], v[184:187]\n\t"
        "ds_read_b128 v[196:199], %[xa] offset:32768\n\t"
        "v_mfma_f32_16x16x32_bf16 v[180:183], v[132:135], v[200:203], v[180:183]\n\t"
        "v_mfma_f32_16x16x32_bf16 v[188:191], v[132:135], v[204:207], v[188:191]\n\t"
        "s_waitcnt lgkmcnt(4)\n\t"
        "v_mfma_f32_16x16x32_bf16 v[176:179], v[56:59], v[208:211], v[176:179]\n\t"
        "ds_read_b128 v[200:203], %[xa] offset:13312\n\t"
        "v_mfma_f32_16x16x32_bf16 v[184:187], v[56:59], v[212:215], v[184:187]\n\t"
        "ds_read_b128 v[204:207], %[xa] offset:33792\n\t"
        "v_mfma_f32_16x16x32_bf16 v[180:183], v[136:139], v[208:211], v[180:183]\n\t"
        "v_mfma_f32_16x16x32_bf16 v[188:191], v[136:139], v[212:215], v[188:191]\n\t"
        "s_waitcnt lgkmcnt(4)\n\t"
        "v_mfma_f32_16x16x32_bf16 v[176:179], v[60:63], v[216:219], v[176:179]\n\t"
        "ds_read_b128 v[208:211], %[xa] offset:14336\n\t"
        "v_mfma_f32_16x16x32_bf16 v[184:187], v[60:63], v[220:223], v[184:187]\n\t"
        "ds_read_b128 v[212:215], %[xa] offset:34816\n\t"
        "v_mfma_f32_16x16x32_bf16 v[180:183], v[140:143], v[216:219], v[180:183]\n\t"
        "v_mfma_f32_16x16x32_bf16 v[188:191], v[140:143], v[220:223], v[188:191]\n\t"
        "s_waitcnt lgkmcnt(4)\n\t"
        "v_mfma_f32_16x16x32_bf16 v[176:179], v[64:67], v[192:195], v[176:179]\n\t"
        "ds_read_b128 v[216:219], %[xa] offset:15360\n\t"
        "v_mfma_f32_16x16x32_bf16 v[184:187], v[64:67], v[196:199], v[184:187]\n\t"
        "ds_read_b128 v[220:223], %[xa] offset:35840\n\t"
        "v_mfma_f32_16x16x32_bf16 v[180:183], v[144:147], v[192:195], v[180:183]\n\t"
        "v_mfma_f32_16x16x32_bf16 v[188:191], v[144:147], v[196:199], v[188:191]\n\t"
        "s_waitcnt lgkmcnt(4)\n\t"
        "v_mfma_f32_16x16x32_bf16 v[176:179], v[68:71], v[200:203], v[176:179]\n\t"
        "ds_read_b128 v[192:195], %[xa] offset:16384\n\t"
        "v_mfma_f32_16x16x32_bf16 v[184:187], v[68:71], v[204:207], v[184:187]\n\t"
        "ds_read_b128 v[196:199], %[xa] offset:36864\n\t"
        "v_mfma_f32_16x16x32_bf16 v[180:183], v[148:151], v[200:203], v[180:183]\n\t"
        "v_mfma_f32_16x16x32_bf16 v[188:191], v[148:151], v[204:207], v[188:191]\n\t"
        "s_waitcnt lgkmcnt(4)\n\t"
        "v_mfma_f32_16x16x32_bf16 v[176:179], v[72:75], v[208:211], v[176:179]\n\t"
        "ds_read_b128 v[200:203], %[xa] offset:17408\n\t"
        "v_mfma_f32_16x16x32_bf16 v[184:187], v[72:75], v[212:215], v[184:187]\n\t"
        "ds_read_b128 v[204:207], %[xa] offset:37888\n\t"
        "v_mfma_f32_16x16x32_bf16 v[180:183], v[152:155], v[208:211], v[180:183]\n\t"
        "v_mfma_f32_16x16x32_bf16 v[188:191], v[152:155], v[212:215], v[188:191]\n\t"
        "s_waitcnt lgkmcnt(4)\n\t"
        "v_mfma_f32_16x16x32_bf16 v[176:179], v[76:79], v[216:219], v[176:179]\n\t"
        "ds_read_b128 v[208:211], %[xa] offset:18432\n\t"
        "v_mfma_f32_16x16x32_bf16 v[184:187], v[76:79], v[220:223], v[184:187]\n\t"
        "ds_read_b128 v[212:215], %[xa] offset:38912\n\t"
        "v_mfma_f32_16x16x32_bf16 v[180:183], v[156:159], v[216:219], v[180:183]\n\t"
        "v_mfma_f32_16x16x32_bf16 v[188:191], v[156:159], v[220:223], v[188:191]\n\t"
        "s_waitcnt lgkmcnt(4)\n\t"
        "v_mfma_f32_16x16x32_bf16 v[176:179], v[80:83], v[192:195], v[176:179]\n\t"
        "ds_read_b128 v[216:219], %[xa] offset:19456\n\t"
        "v_mfma_f32_16x16x32_bf16 v[184:187], v[80:83], v[196:199], v[184:187]\n\t"
        "ds_read_b128 v[220:223], %[xa] offset:39936\n\t"
        "v_mfma_f32_16x16x32_bf16 v[180:183], v[160:163], v[192:195], v[180:183]\n\t"
        "v_mfma_f32_16x16x32_bf16 v[188:191], v[160:163], v[196:199], v[188:191]\n\t"
        "s_waitcnt lgkmcnt(4)\n\t"
        "v_mfma_f32_16x16x32_bf16 v[176:179], v[84:87], v[200:203], v[176:179]\n\t"
        "v_mfma_f32_16x16x32_bf16 v[184:187], v[84:87], v[204:207], v[184:187]\n\t"
        "v_mfma_f32_16x16x32_bf16 v[180:183], v[164:167], v[200:203], v[180:183]\n\t"
        "v_mfma_f32_16x16x32_bf16 v[188:191], v[164:167], v[204:207], v[188:191]\n\t"
        "s_waitcnt lgkmcnt(2)\n\t"
        "v_mfma_f32_16x16x32_bf16 v[176:179], v[88:91], v[208:211], v[176:179]\n\t"
        "v_mfma_f32_16x16x32_bf16 v[184:187], v[88:91], v[212:215], v[184:187]\n\t"
        "v_mfma_f32_16x16x32_bf16 v[180:183], v[168:171], v[208:211], v[180:183]\n\t"
        "v_mfma_f32_16x16x32_bf16 v[188:191], v[168:171], v[212:215], v[188:191]\n\t"
        "s_waitcnt lgkmcnt(0)\n\t"
        "v_mfma_f32_16x16x32_bf16 v[176:179], v[92:95], v[216:219], v[176:179]\n\t"
        "v_mfma_f32_16x16x32_bf16 v[184:187], v[92:95], v[220:223], v[184:187]\n\t"
        "v_mfma_f32_16x16x32_bf16 v[180:183], v[172:175], v[216:219], v[180:183]\n\t"
        "v_mfma_f32_16x16x32_bf16 v[188:191], v[172:175], v[220:223], v[188:191]\n\t"
        "s_nop 0\n\t"
        "s_barrier\n\t"
        "s_nop 4\n\t"
        "v_mul_f32 v176, %[sc], v176\n\t"
        "v_mul_f32 v177, %[sc], v177\n\t"
        "v_mul_f32 v178, %[sc], v178\n\t"
        "v_mul_f32 v179, %[sc], v179\n\t"
        "v_mul_f32 v180, %[sc], v180\n\t"
        "v_mul_f32 v181, %[sc], v181\n\t"
        "v_mul_f32 v182, %[sc], v182\n\t"
        "v_mul_f32 v183, %[sc], v183\n\t"
        "v_cvt_pk_bf16_f32 v224, v176, v177\n\t"
        "v_cvt_pk_bf16_f32 v225, v178, v179\n\t"
        "v_cvt_pk_bf16_f32 v226, v180, v181\n\t"
        "v_cvt_pk_bf16_f32 v227, v182, v183\n\t"
        "buffer_store_dwordx4 v[224:227], %[oa], %[rs], %[o0] offen sc1\n\t"
        "v_mul_f32 v184, %[sc], v184\n\t"
        "v_mul_f32 v185, %[sc], v185\n\t"
        "v_mul_f32 v186, %[sc], v186\n\t"
        "v_mul_f32 v187, %[sc], v187\n\t"
        "v_mul_f32 v188, %[sc], v188\n\t"
        "v_mul_f32 v189, %[sc], v189\n\t"
        "v_mul_f32 v190, %[sc], v190\n\t"
        "v_mul_f32 v191, %[sc], v191\n\t"
        "v_cvt_pk_bf16_f32 v228, v184, v185\n\t"
        "v_cvt_pk_bf16_f32 v229, v186, v187\n\t"
        "v_cvt_pk_bf16_f32 v230, v188, v189\n\t"
        "v_cvt_pk_bf16_f32 v231, v190, v191\n\t"
        "buffer_store_dwordx4 v[228:231], %[oa], %[rs], %[o1] offen sc1\n\t"
        "s_nop 1\n\t"
        :
        : [w0] "{v[16:47]}"(W[0]), [w1] "{v[48:79]}"(W[1]), [w2] "{v[80:111]}"(W[2]), [w3] "{v[112:143]}"(W[3]), [w4] "{v[144:175]}"(W[4]), [xa] "v"(xa), [oa] "v"(oa), [rs] "s"(rs), [o0] "s"(o0), [o1] "s"(o1), [sc] "s"(sc), [ca] "s"(ca), [fc] "s"(fc), [fw] "s"(fw), [fo] "s"(fo), [xv] "v"(xv), [xs] "s"(xs), [xd] "s"(xd)
        : "s88", "s89", "v176", "v177", "v178", "v179", "v180", "v181", "v182", "v183", "v184", "v185", "v186", "v187", "v188", "v189", "v190", "v191", "v192", "v193", "v194", "v195", "v196", "v197", "v198", "v199", "v200", "v201", "v202", "v203", "v204", "v205", "v206", "v207", "v208", "v209", "v210", "v211", "v212", "v213", "v214", "v215", "v216", "v217", "v218", "v219", "v220", "v221", "v222", "v223", "v224", "v225", "v226", "v227", "v228", "v229", "v230", "v231", "v232", "v233", "v234", "vcc", "scc", "memory");
}

__device__ __forceinline__ void wb(const u32x32 (&W)[5], u32 xa, u32 oa, u32x4 rs, u32 bl, u32 wp, u32 it, u32 xv, u32 xs, u32 xd, u32 ys, u32 yd, u32 ca, u32 fc, u32 fw, u32 fo STP) {
    asm volatile(
        "ds_read_b128 v[192:195], %[xa] offset:0\n\t"
        "ds_read_b128 v[196:199], %[xa] offset:1024\n\t"
        "ds_read_b128 v[200:203], %[xa] offset:2048\n\t"
        "ds_read_b128 v[204:207], %[xa] offset:3072\n\t"
        "ds_read_b128 v[208:211], %[xa] offset:4096\n\t"
        "ds_read_b128 v[212:215], %[xa] offset:5120\n\t"
        "ds_read_b128 v[216:219], %[xa] offset:6144\n\t"
        "ds_read_b128 v[220:223], %[xa] offset:7168\n\t"
        "v_mov_b32 v232, %[ca]\n\t"
        "v_mov_b32 v233, 1\n\t"
        "v_mov_b32 v234, %[fw]\n\t"
        "s_andn2_b32 s91, %[wp], 15\n\t"
        "s_mov_b32 s92, %[bl]\n\t"
        "s_mov_b32 s88, m0\n\t"
        "s_mov_b32 m0, %[xd]\n\t"
        "s_add_u32 s89, %[xs], 0x1000\n\t"
        "buffer_load_dwordx4 %[xv], %[rs], %[xs] offen offset:0 sc1 lds\n\t"
        "buffer_load_dwordx4 %[xv], %[rs], %[xs] offen offset:1024 sc1 lds\n\t"
        "s_mov_b32 m0, s88\n\t"
        "s_setprio 1\n\t"
        "s_waitcnt lgkmcnt(0)\n\t"
        "v_mfma_f32_16x16x32_bf16 v[176:179], v[192:195], v[16:19], 0\n\t"
        "s_lshr_b32 s95, s92, 20\n\t"
        "s_xnor_b32 s95, s95, %[wp]\n\t"
        "v_mfma_f32_16x16x32_bf16 v[180:183], v[208:211], v[16:19], 0\n\t"
        "s_and_b32 s95, s95, 1\n\t"
        "s_lshl_b32 s95, s95, 13\n\t"
        "v_mfma_f32_16x16x32_bf16 v[176:179], v[196:199], v[20:23], v[176:179]\n\t"
        "v_mfma_f32_16x16x32_bf16 v[180:183], v[212:215], v[20:23], v[180:183]\n\t"
        "v_mfma_f32_16x16x32_bf16 v[176:179], v[200:203], v[24:27], v[176:179]\n\t"
        "v_mfma_f32_16x16x32_bf16 v[180:183], v[216:219], v[24:27], v[180:183]\n\t"
        "v_mfma_f32_16x16x32_bf16 v[176:179], v[204:207], v[28:31], v[176:179]\n\t"
        "v_mfma_f32_16x16x32_bf16 v[180:183], v[220:223], v[28:31], v[180:183]\n\t"
        "s_lshl1_add_u32 s93, s92, s91\n\t"
        "s_add_u32 s93, s93, s95\n\t"
        "v_mfma_f32_16x16x32_bf16 v[184:187], v[192:195], v[96:99], 0\n\t"
        "s_add_u32 s92, s92, 0x40000\n\t"
        "v_mfma_f32_16x16x32_bf16 v[188:191], v[208:211], v[96:99], 0\n\t"
        "s_add_u32 s94, s93, 0x10000\n\t"
        "v_mfma_f32_16x16x32_bf16 v[184:187], v[196:199], v[100:103], v[184:187]\n\t"
        "v_cvt_pk_bf16_f32 v224, v176, v177\n\t"
        "v_mfma_f32_16x16x32_bf16 v[188:191], v[212:215], v[100:103], v[188:191]\n\t"
        "v_cvt_pk_bf16_f32 v225, v178, v179\n\t"
        "v_mfma_f32_16x16x32_bf16 v[184:187], v[200:203], v[104:107], v[184:187]\n\t"
        "v_cvt_pk_bf16_f32 v226, v180, v181\n\t"
        "v_mfma_f32_16x16x32_bf16 v[188:191], v[216:219], v[104:107], v[188:191]\n\t"
        "v_cvt_pk_bf16_f32 v227, v182, v183\n\t"
        "v_mfma_f32_16x16x32_bf16 v[184:187], v[204:207], v[108:111], v[184:187]\n\t"
        "buffer_store_dwordx4 v[224:227], %[oa], %[rs], s93 offen sc1\n\t"
        "v_mfma_f32_16x16x32_bf16 v[188:191], v[220:223], v[108:111], v[188:191]\n\t"
        "v_mfma_f32_16x16x32_bf16 v[176:179], v[192:195], v[32:35], 0\n\t"
        "s_lshr_b32 s95, s92, 20\n\t"
        "s_xnor_b32 s95, s95, %[wp]\n\t"
        "v_mfma_f32_16x16x32_bf16 v[180:183], v[208:211], v[32:35], 0\n\t"
        "s_and_b32 s95, s95, 1\n\t"
        "s_lshl_b32 s95, s95, 13\n\t"
        "v_mfma_f32_16x16x32_bf16 v[176:179], v[196:199], v[36:39], v[176:179]\n\t"
        "v_cvt_pk_bf16_f32 v228, v184, v185\n\t"
        "v_mfma_f32_16x16x32_bf16 v[180:183], v[212:215], v[36:39], v[180:183]\n\t"
        "v_cvt_pk_bf16_f32 v229, v186, v187\n\t"
        "v_mfma_f32_16x16x32_bf16 v[176:179], v[200:203], v[40:43], v[176:179]\n\t"
        "v_cvt_pk_bf16_f32 v230, v188, v189\n\t"
        "v_mfma_f32_16x16x32_bf16 v[180:183], v[216:219], v[40:43], v[180:183]\n\t"
        "v_cvt_pk_bf16_f32 v231, v190, v191\n\t"
        "v_mfma_f32_16x16x32_bf16 v[176:179], v[204:207], v[44:47], v[176:179]\n\t"
        "buffer_store_dwordx4 v[228:231], %[oa], %[rs], s94 offen sc1\n\t"
        "v_mfma_f32_16x16x32_bf16 v[180:183], v[220:223], v[44:47], v[180:183]\n\t"
        "s_lshl1_add_u32 s93, s92, s91\n\t"
        "s_add_u32 s93, s93, s95\n\t"
        "v_mfma_f32_16x16x32_bf16 v[184:187], v[192:195], v[112:115], 0\n\t"
        "s_add_u32 s92, s92, 0x40000\n\t"
        "v_mfma_f32_16x16x32_bf16 v[188:191], v[208:211], v[112:115], 0\n\t"
        "s_add_u32 s94, s93, 0x10000\n\t"
        "v_mfma_f32_16x16x32_bf16 v[184:187], v[196:199], v[116:119], v[184:187]\n\t"
        "v_cvt_pk_bf16_f32 v224, v176, v177\n\t"
        "v_mfma_f32_16x16x32_bf16 v[188:191], v[212:215], v[116:119], v[188:191]\n\t"
        "v_cvt_pk_bf16_f32 v225, v178, v179\n\t"
        "v_mfma_f32_16x16x32_bf16 v[184:187], v[200:203], v[120:123], v[184:187]\n\t"
        "v_cvt_pk_bf16_f32 v226, v180, v181\n\t"
        "v_mfma_f32_16x16x32_bf16 v[188:191], v[216:219], v[120:123], v[188:191]\n\t"
        "v_cvt_pk_bf16_f32 v227, v182, v183\n\t"
        "v_mfma_f32_16x16x32_bf16 v[184:187], v[204:207], v[124:127], v[184:187]\n\t"
        "buffer_store_dwordx4 v[224:227], %[oa], %[rs], s93 offen sc1\n\t"
        "v_mfma_f32_16x16x32_bf16 v[188:191], v[220:223], v[124:127], v[188:191]\n\t"
        "s_waitcnt vmcnt(5)\n\t"
        "s_mov_b64 exec, 1\n\t"
        "ds_add_rtn_u32 v233, v232, v233\n\t"
        "s_mov_b64 exec, -1\n\t"
        "v_mfma_f32_16x16x32_bf16 v[176:179], v[192:195], v[48:51], 0\n\t"
        "s_lshr_b32 s95, s92, 20\n\t"
        "s_xnor_b32 s95, s95, %[wp]\n\t"
        "v_mfma_f32_16x16x32_bf16 v[180:183], v[208:211], v[48:51], 0\n\t"
        "s_and_b32 s95, s95, 1\n\t"
        "s_lshl_b32 s95, s95, 13\n\t"
        "v_mfma_f32_16x16x32_bf16 v[176:179], v[196:199], v[52:55], v[176:179]\n\t"
        "v_cvt_pk_bf16_f32 v228, v184, v185\n\t"
        "v_mfma_f32_16x16x32_bf16 v[180:183], v[212:215], v[52:55], v[180:183]\n\t"
        "v_cvt_pk_bf16_f32 v229, v186, v187\n\t"
        "v_mfma_f32_16x16x32_bf16 v[176:179], v[200:203], v[56:59], v[176:179]\n\t"
        "v_cvt_pk_bf16_f32 v230, v188, v189\n\t"
        "v_mfma_f32_16x16x32_bf16 v[180:183], v[216:219], v[56:59], v[180:183]\n\t"
        "v_cvt_pk_bf16_f32 v231, v190, v191\n\t"
        "v_mfma_f32_16x16x32_bf16 v[176:179], v[204:207], v[60:63], v[176:179]\n\t"
        "buffer_store_dwordx4 v[228:231], %[oa], %[rs], s94 offen sc1\n\t"
        "v_mfma_f32_16x16x32_bf16 v[180:183], v[220:223], v[60:63], v[180:183]\n\t"
        "s_lshl1_add_u32 s93, s92, s91\n\t"
        "s_add_u32 s93, s93, s95\n\t"
        "v_mfma_f32_16x16x32_bf16 v[184:187], v[192:195], v[128:131], 0\n\t"
        "s_add_u32 s92, s92, 0x40000\n\t"
        "v_mfma_f32_16x16x32_bf16 v[188:191], v[208:211], v[128:131], 0\n\t"
        "s_add_u32 s94, s93, 0x10000\n\t"
        "v_mfma_f32_16x16x32_bf16 v[184:187], v[196:199], v[132:135], v[184:187]\n\t"
        "v_cvt_pk_bf16_f32 v224, v176, v177\n\t"
        "v_mfma_f32_16x16x32_bf16 v[188:191], v[212:215], v[132:135], v[188:191]\n\t"
        "v_cvt_pk_bf16_f32 v225, v178, v179\n\t"
        "v_mfma_f32_16x16x32_bf16 v[184:187], v[200:203], v[136:139], v[184:187]\n\t"
        "v_cvt_pk_bf16_f32 v226, v180, v181\n\t"
        "v_mfma_f32_16x16x32_bf16 v[188:191], v[216:219], v[136:139], v[188:191]\n\t"
        "v_cvt_pk_bf16_f32 v227, v182, v183\n\t"
        "v_mfma_f32_16x16x32_bf16 v[184:187], v[204:207], v[140:143], v[184:187]\n\t"
        "buffer_store_dwordx4 v[224:227], %[oa], %[rs], s93 offen sc1\n\t"
        "v_mfma_f32_16x16x32_bf16 v[188:191], v[220:223], v[140:143], v[188:191]\n\t"
        "s_waitcnt lgkmcnt(0)\n\t"
        "s_mov_b64 exec, 1\n\t"
        "v_cmpx_eq_u32 vcc, %[fc], v233\n\t"
        "buffer_store_dword v234, off, %[rs], %[fo] sc1\n\t"
        "s_mov_b64 exec, -1\n\t"
        "s_cmp_le_u32 %[it], %[nowv]\n\t"
        "s_cbranch_scc1 Lp%=\n\t"
        "s_setprio 0\n\t"
        "Lp%=:\n\t"
        "s_mov_b32 s88, m0\n\t"
        "s_mov_b32 m0, %[yd]\n\t"
        "s_add_u32 s89, %[ys], 0x1000\n\t"
        "buffer_load_dwordx4 %[xv], %[rs], %[ys] offen offset:0 sc1 lds\n\t"
        "buffer_load_dwordx4 %[xv], %[rs], %[ys] offen offset:1024 sc1 lds\n\t"
        "buffer_load_dwordx4 %[xv], %[rs], %[ys] offen offset:2048 sc1 lds\n\t"
        "buffer_load_dwordx4 %[xv], %[rs], %[ys] offen offset:3072 sc1 lds\n\t"
        "s_add_u32 m0, m0, 0x1000\n\t"
        "s_nop 0\n\t"
        "buffer_load_dwordx4 %[xv], %[rs], s89 offen offset:0 sc1 lds\n\t"
        "buffer_load_dwordx4 %[xv], %[rs], s89 offen offset:1024 sc1 lds\n\t"
        "buffer_load_dwordx4 %[xv], %[rs], s89 offen offset:2048 sc1 lds\n\t"
        "buffer_load_dwordx4 %[xv], %[rs], s89 offen offset:3072 sc1 lds\n\t"
        "s_mov_b32 m0, s88\n\t"
        "v_mfma_f32_16x16x32_bf16 v[176:179], v[192:195], v[64:67], 0\n\t"
        "s_lshr_b32 s95, s92, 20\n\t"
        "s_xnor_b32 s95, s95, %[wp]\n\t"
        "v_mfma_f32_16x16x32_bf16 v[180:183], v[208:211], v[64:67], 0\n\t"
        "s_and_b32 s95, s95, 1\n\t"
        "s_lshl_b32 s95, s95, 13\n\t"
        "v_mfma_f32_16x16x32_bf16 v[176:179], v[196:199], v[68:71], v[176:179]\n\t"
        "v_cvt_pk_bf16_f32 v228, v184, v185\n\t"
        "v_mfma_f32_16x16x32_bf16 v[180:183], v[212:215], v[68:71], v[180:183]\n\t"
        "v_cvt_pk_bf16_f32 v229, v186, v187\n\t"
        "v_mfma_f32_16x16x32_bf16 v[176:179], v[200:203], v[72:75], v[176:179]\n\t"
        "v_cvt_pk_bf16_f32 v230, v188, v189\n\t"
        "v_mfma_f32_16x16x32_bf16 v[180:183], v[216:219], v[72:75], v[180:183]\n\t"
        "v_cvt_pk_bf16_f32 v231, v190, v191\n\t"
        "v_mfma_f32_16x16x32_bf16 v[176:179], v[204:207], v[76:79], v[176:179]\n\t"
        "buffer_store_dwordx4 v[228:231], %[oa], %[rs], s94 offen sc1\n\t"
        "v_mfma_f32_16x16x32_bf16 v[180:183], v[220:223], v[76:79], v[180:183]\n\t"
        "s_lshl1_add_u32 s93, s92, s91\n\t"
        "s_add_u32 s93, s93, s95\n\t"
        "v_mfma_f32_16x16x32_bf16 v[184:187], v[192:195], v[144:147], 0\n\t"
        "s_add_u32 s92, s92, 0x40000\n\t"
        "v_mfma_f32_16x16x32_bf16 v[188:191], v[208:211], v[144:147], 0\n\t"
        "s_add_u32 s94, s93, 0x10000\n\t"
        "v_mfma_f32_16x16x32_bf16 v[184:187], v[196:199], v[148:151], v[184:187]\n\t"
        "v_cvt_pk_bf16_f32 v224, v176, v177\n\t"
        "v_mfma_f32_16x16x32_bf16 v[188:191], v[212:215], v[148:151], v[188:191]\n\t"
        "v_cvt_pk_bf16_f32 v225, v178, v179\n\t"
        "v_mfma_f32_16x16x32_bf16 v[184:187], v[200:203], v[152:155], v[184:187]\n\t"
        "v_cvt_pk_bf16_f32 v226, v180, v181\n\t"
        "v_mfma_f32_16x16x32_bf16 v[188:191], v[216:219], v[152:155], v[188:191]\n\t"
        "v_cvt_pk_bf16_f32 v227, v182, v183\n\t"
        "v_mfma_f32_16x16x32_bf16 v[184:187], v[204:207], v[156:159], v[184:187]\n\t"
        "buffer_store_dwordx4 v[224:227], %[oa], %[rs], s93 offen sc1\n\t"
        "v_mfma_f32_16x16x32_bf16 v[188:191], v[220:223], v[156:159], v[188:191]\n\t"
        "v_mfma_f32_16x16x32_bf16 v[176:179], v[192:195], v[80:83], 0\n\t"
        "s_lshr_b32 s95, s92, 20\n\t"
        "s_xnor_b32 s95, s95, %[wp]\n\t"
        "v_mfma_f32_16x16x32_bf16 v[180:183], v[208:211], v[80:83], 0\n\t"
        "s_and_b32 s95, s95, 1\n\t"
        "s_lshl_b32 s95, s95, 13\n\t"
        "v_mfma_f32_16x16x32_bf16 v[176:179], v[196:199], v[84:87], v[176:179]\n\t"
        "v_cvt_pk_bf16_f32 v228, v184, v185\n\t"
        "v_mfma_f32_16x16x32_bf16 v[180:183], v[212:215], v[84:87], v[180:183]\n\t"
        "v_cvt_pk_bf16_f32 v229, v186, v187\n\t"
        "v_mfma_f32_16x16x32_bf16 v[176:179], v[200:203], v[88:91], v[176:179]\n\t"
        "v_cvt_pk_bf16_f32 v230, v188, v189\n\t"
        "v_mfma_f32_16x16x32_bf16 v[180:183], v[216:219], v[88:91], v[180:183]\n\t"
        "v_cvt_pk_bf16_f32 v231, v190, v191\n\t"
        "v_mfma_f32_16x16x32_bf16 v[176:179], v[204:207], v[92:95], v[176:179]\n\t"
        "buffer_store_dwordx4 v[228:231], %[oa], %[rs], s94 offen sc1\n\t"
        "v_mfma_f32_16x16x32_bf16 v[180:183], v[220:223], v[92:95], v[180:183]\n\t"
        "s_lshl1_add_u32 s93, s92, s91\n\t"
        "s_add_u32 s93, s93, s95\n\t"
        "v_mfma_f32_16x16x32_bf16 v[184:187], v[192:195], v[160:163], 0\n\t"
        "s_add_u32 s92, s92, 0x40000\n\t"
        "v_mfma_f32_16x16x32_bf16 v[188:191], v[208:211], v[160:163], 0\n\t"
        "s_add_u32 s94, s93, 0x10000\n\t"
        "v_mfma_f32_16x16x32_bf16 v[184:187], v[196:199], v[164:167], v[184:187]\n\t"
        "v_cvt_pk_bf16_f32 v224, v176, v177\n\t"
        "v_mfma_f32_16x16x32_bf16 v[188:191], v[212:215], v[164:167], v[188:191]\n\t"
        "v_cvt_pk_bf16_f32 v225, v178, v179\n\t"
        "v_mfma_f32_16x16x32_bf16 v[184:187], v[200:203], v[168:171], v[184:187]\n\t"
        "v_cvt_pk_bf16_f32 v226, v180, v181\n\t"
        "v_mfma_f32_16x16x32_bf16 v[188:191], v[216:219], v[168:171], v[188:191]\n\t"
        "v_cvt_pk_bf16_f32 v227, v182, v183\n\t"
        "v_mfma_f32_16x16x32_bf16 v[184:187], v[204:207], v[172:175], v[184:187]\n\t"
        "buffer_store_dwordx4 v[224:227], %[oa], %[rs], s93 offen sc1\n\t"
        "v_mfma_f32_16x16x32_bf16 v[188:191], v[220:223], v[172:175], v[188:191]\n\t"
        "s_and_b32 s95, %[it], 31\n\t"
        "s_cmp_le_u32 s95, %[nowv]\n\t"
        "s_cbranch_scc1 Lnb%=\n\t"
        "s_waitcnt vmcnt(17)\n\t"
        "s_barrier\n\t"
        "Lnb%=:\n\t"
        "s_setprio 0\n\t"
        "s_nop 3\n\t"
        "v_cvt_pk_bf16_f32 v228, v184, v185\n\t"
        "v_cvt_pk_bf16_f32 v229, v186, v187\n\t"
        "v_cvt_pk_bf16_f32 v230, v188, v189\n\t"
        "v_cvt_pk_bf16_f32 v231, v190, v191\n\t"
        "buffer_store_dwordx4 v[228:231], %[oa], %[rs], s94 offen sc1\n\t"
        "s_nop 1\n\t"
        :
        : [w0] "{v[16:47]}"(W[0]), [w1] "{v[48:79]}"(W[1]), [w2] "{v[80:111]}"(W[2]), [w3] "{v[112:143]}"(W[3]), [w4] "{v[144:175]}"(W[4]), [xa] "v"(xa), [oa] "v"(oa), [rs] "s"(rs), [bl] "s"(bl), [wp] "s"(wp), [it] "s"(it), [nowv] "n"(NOWV), [ca] "s"(ca), [fc] "s"(fc), [fw] "s"(fw), [fo] "s"(fo), [xv] "v"(xv), [xs] "s"(xs), [xd] "s"(xd), [ys] "s"(ys), [yd] "s"(yd)
        : "s88", "s89", "s91", "s92", "s93", "s94", "s95", "v176", "v177", "v178", "v179", "v180", "v181", "v182", "v183", "v184", "v185", "v186", "v187", "v188", "v189", "v190", "v191", "v192", "v193", "v194", "v195", "v196", "v197", "v198", "v199", "v200", "v201", "v202", "v203", "v204", "v205", "v206", "v207", "v208", "v209", "v210", "v211", "v212", "v213", "v214", "v215", "v216", "v217", "v218", "v219", "v220", "v221", "v222", "v223", "v224", "v225", "v226", "v227", "v228", "v229", "v230", "v231", "v232", "v233", "v234", "vcc", "scc", "memory");
}

#define TP0 200
#define TP1 16
#define TP2 181
#define TP3 30
#define TP4 0
#define TP5 130
#define TP6 112
#define TP7 208
#define TP8 4
#define TP9 24
#define TP10 4
#define TP11 3
#define TP12 2
#define TP13 150
#define TP14 70
#define TP15 4
#define WAHEAD 3
#define FILLG 4
#define WFIRST 2
#define EARLY_N 35
#define BUMP_N 3
#define BUMP_WIN 0xffu
#define BUMP_S0 8
#define BUMP_MAX 3
#define TV(idx) ((u32)(TP##idx) * 8u)
#define TLOAD() do { } while (0)

__device__ __forceinline__ void nap(u32 n) {
    u32 hi = n >> 6, lo = (n >> 4) & 3u;
    asm volatile("s_cmp_eq_u32 %0, 0\n\ts_cbranch_scc1 Ln%=\nLm%=:\n\ts_sleep 1\n\ts_sub_u32 %0, %0, 1\n\ts_cmp_lg_u32 %0, 0\n\ts_cbranch_scc1 Lm%=\nLn%=:\n\t"
                 "s_cmp_eq_u32 %1, 0\n\ts_cbranch_scc1 Lp%=\nLo%=:\n\ts_sub_u32 %1, %1, 1\n\ts_cmp_lg_u32 %1, 0\n\ts_cbranch_scc1 Lo%=\nLp%=:"
                 : "+s"(hi), "+s"(lo) :: "scc", "memory");
}
typedef __attribute__((address_space(3))) u32* lds_u32p;
typedef __attribute__((address_space(3))) volatile u32* lds_vu32p;
typedef __attribute__((address_space(3))) u16* lds_u16p;
typedef __attribute__((address_space(3))) u32x2* lds_u32x2p;
typedef __attribute__((address_space(3))) u32x4* lds_u32x4p;
__device__ __forceinline__ u32 rfl(u32 x) { return __builtin_amdgcn_readfirstlane(x); }
__device__ __forceinline__ bool has_sent(u32x4 v) { return (v[0] == SENT) | (v[1] == SENT) | (v[2] == SENT) | (v[3] == SENT); }
__device__ __forceinline__ u32 now32() { return (u32)__builtin_amdgcn_s_memtime(); }
__device__ __forceinline__ u32 step_tag(int s) { return (u32)(s + 1) * 0x10001u; }

template <int S0 = 0>
__device__ __forceinline__ u32 unless_neg(u32 v, int s) {
    u32 r;
    asm("s_cmp_lt_i32 %2, %3\n\ts_cselect_b32 %0, 0, %1" : "=s"(r) : "s"(v), "s"(s), "n"(S0) : "scc");
    return r;
}
template <int S0 = 0>
__device__ __forceinline__ u32x4 live(u32x4 srd, int s) { srd[2] = unless_neg<S0>(srd[2], s); return srd; }
__device__ __forceinline__ float shx(float v, int m, int lane) { return __int_as_float(__builtin_amdgcn_ds_bpermute((lane ^ m) << 2, __float_as_int(v))); }
__device__ __forceinline__ void mem0_image(const u16* __restrict__ mem0, char* smem, const int tid) {
    for (int c = tid; c < 640; c += 512) {
        u32x4 v = *(const u32x4*)(mem0 + (c & 7) * D_ + (c >> 3) * 8);
#pragma unroll
        for (int i = 0; i < 4; ++i) v[i] = v[i] == SENT ? ~0u : v[i];
        ((u32x4*)smem)[c + (c >= 576 ? 64 : 0)] = v;
    }
}
__device__ __forceinline__ u32 once_more(u32& left, u32 lbase, u32* errw) {
    if (__builtin_expect(left != 0, 1)) { --left; return 2u; }
    atomicOr(errw, 1u);
    const int lane = lane_id();
    for (int k = 0; k < 11; ++k) *(lds_u32x4p)(lbase + (u32)(k * 1024 + lane * 16)) = (u32x4){0u, 0u, 0u, 0u};
    asm volatile("s_waitcnt lgkmcnt(0)" ::: "memory");
    return 0u;
}
constexpr u32 TS_MISS = 0x10000u;
__device__ __forceinline__ u32 read_ts(rsrc_t rs, u32 off, int s, u32& m) {
    const int lane = lane_id();
    const u32 vo = lane < 8 ? (u32)lane * 128u : OOB, base = (u32)__builtin_amdgcn_s_memrealtime() * 24u - 0x100000u;
    u32x4 t;
#pragma unroll 1
    for (u32 spins = 1;; ++spins) {
        CFENCE();
        t = ldx4(rs, vo, off);
        m = (u32)__ballot(t[3] == (u32)s + 1u);
        if (((m | (m >> 4)) & 15u) == 15u) break;
        m = 0;
        if (spins >= 0x4000u) break;
        __builtin_amdgcn_s_sleep(4);
    }
    u32 v = t[3] == (u32)s + 1u ? t[1] - base : 0u;
    v = max(v, (u32)__builtin_amdgcn_update_dpp(0u, v, 0xb1, 0xf, 0xf, false));
    v = max(v, (u32)__builtin_amdgcn_update_dpp(0u, v, 0x4e, 0xf, 0xf, false));
    v = max(v, (u32)__builtin_amdgcn_update_dpp(0u, v, 0x141, 0xf, 0xf, false));
    return to_sgpr(v + base);
}
__device__ __forceinline__ u32 read_ts_late(rsrc_t rs, u32x4 srd, u32 off, int s, u32& m, u32 gap = 1024) {
    u32 t;
    ts_look(t, m, srd, off, (u32)s + 1u);
    if (m) return t;
    nap(gap);
    return read_ts(rs, off, s, m);
}
__device__ __forceinline__ void until(u32 t) { for (int q = 0; q < 256 && (int)(t - (u32)__builtin_amdgcn_s_memrealtime() * 24u) > 0; ++q) __builtin_amdgcn_s_sleep(1); }
__device__ __forceinline__ u32 umin3(u32 a, u32 b, u32 c) { return min(min(a, b), c); }
__device__ __forceinline__ u32 dist(u32 d, u32x4 v, u32 k) { return umin3(umin3(d, v[0] ^ k, v[1] ^ k), v[2] ^ k, v[3] ^ k); }
#define COUNT(i) do { } while (0)
#define COUNT0() do { } while (0)
#define COUNT1(i) do { } while (0)
__device__ __forceinline__ void touch(u32x4 srd, u32 wp, int s, u32 lds) {
    u32 sv;
    const u32 off = slot_off(wp, s) + 3u * 32768u + ((~((wp >> 3) ^ ((u32)s >> 5)) & 1u) << 18);
    asm volatile("s_mov_b32 %0, m0\n\ts_mov_b32 m0, %3\n\ts_nop 3\n\tbuffer_load_dword off, %1, %2 lds\n\ts_mov_b32 m0, %0" : "=&s"(sv) : "s"(srd), "s"(off), "s"(lds) : "memory");
}
__device__ __forceinline__ void track_rate(u32 c0, u32 r0, u32& pc, u32& pr, u32& kr) {
    const u32 dr = r0 - pr;
    if (dr >= 6000u) {
        if (dr < 40000u) kr = to_sgpr(min(max((u32)(4096.f * (float)(c0 - pc) * __builtin_amdgcn_rcpf((float)dr) + 0.5f), 2560u), 4352u));
        pc = c0; pr = r0;
    }
}

#define LANES() const int lane = lane_id(), n = lane & 15, g = lane >> 4; (void)n; (void)g
constexpr u32 L_Q = 12288, L_K = 18432, L_E = 24576, L_DR = 25088, L_MSK = 25600, L_KQ = 32768;
constexpr float LIM_X = 32.f, LIM_D = 1.0995116e12f;
__device__ __forceinline__ u32 kq_buf(int sg) { return L_KQ + (u32)(sg % 3) * 16384u; }

__device__ __forceinline__ u32 p_pos(int row, int g, int dg) { return (u32)((row >> 1) * 64 + (g & 1) * 32 + (row & 1) * 16 + dg * 8 + (g >> 1) * 4); }
__device__ __forceinline__ void s_slow(const int h, const int s, rsrc_t rs, u32 so, u32 lbase) {
    const int lane = lane_id(), row = lane & 7, j = lane >> 3;
    const float ce = 0.08838834764831845f * 1.4426950408889634f;
    float sc[5] = {0.f, 0.f, 0.f, 0.f, 0.f};
    const u32 kx = lbase + kq_buf(s) + (u32)((j >> 2) * 4096 + (j & 3) * 64);
#pragma unroll 1
    for (int e = 0; e < DH_; ++e) {
        const u32 qa = lbase + L_Q + (u32)((e >> 4) * 768 + (row >> 2) * 192 + (e & 15) * 8 + (row & 3) * 2);
        const float q = bf2f(*(lds_u16p)qa);
        const u32 ka = kx + (u32)((e >> 5) * 1024 + ((e >> 2) & 3) * 256 + ((e >> 4) & 1) * 8 + (e & 3) * 2);
#pragma unroll
        for (int i = 0; i < 4; ++i) sc[i] += q * bf2f(*(lds_u16p)(ka + i * 16));
        sc[4] += q * bf2f(*(lds_u16p)(lbase + L_K + (u32)((e >> 4) * 768 + (j >> 1) * 192 + (e & 15) * 8 + (j & 1) * 2)));
    }
    sc[4] *= ce;
    const u32x2 ev = *(lds_u32x2p)(lbase + L_E + (u32)((j >> 1) * 128 + row * 16 + (j & 1) * 8));
    float se = (bf2f((u16)ev[0]) + bf2f((u16)(ev[0] >> 16))) + (bf2f((u16)ev[1]) + bf2f((u16)(ev[1] >> 16)));
    float mx = fmaxf(fmaxf(fmaxf(sc[0], sc[1]), fmaxf(sc[2], sc[3])), sc[4]);
#pragma unroll
    for (int o = 8; o < 64; o <<= 1) { se += shx(se, o, lane); mx = fmaxf(mx, shx(mx, o, lane)); }
    const bool rew = ev[0] == ~0u || ev[1] == ~0u;
    u64 rw = __ballot(rew);
    rw |= rw >> 32; rw |= rw >> 16; rw |= rw >> 8;
    const bool again = (rw >> row) & 1u;
    float al = exp2f(-mx);
    if (again) {
        const u32x2 en = {pack2(exp2f(sc[0] - mx), exp2f(sc[1] - mx)), pack2(exp2f(sc[2] - mx), exp2f(sc[3] - mx))};
        se = (bf2f((u16)en[0]) + bf2f((u16)(en[0] >> 16))) + (bf2f((u16)en[1]) + bf2f((u16)(en[1] >> 16)));
        al = 1.f;
    }
    float sn = se;
#pragma unroll
    for (int o = 8; o < 64; o <<= 1) sn += shx(sn, o, lane);
    if (again) se = sn;
    const float em = exp2f(sc[4] - mx);
    float sm = em;
#pragma unroll
    for (int o = 8; o < 64; o <<= 1) sm += shx(sm, o, lane);
    const float rd = 1.f / (sm + al * se);
    if (rw & 255u) {
#pragma unroll 1
        for (u32 spins = 0; spins < SPIN_CAP && !__all(*(lds_vu32p)(lbase + L_DR + (u32)(lane & 1) * 4) == step_tag(s)); ++spins) __builtin_amdgcn_s_sleep(1);
        const u32x2 en = {pack2(exp2f(sc[0] - mx), exp2f(sc[1] - mx)), pack2(exp2f(sc[2] - mx), exp2f(sc[3] - mx))};
        const u32 eo = again ? (u32)((h >> 3) * 32768 + ((h >> 1) & 3) * 1024 + (j >> 1) * 256 + (h & 1) * 128 + row * 16 + (j & 1) * 8) : OOB;
#pragma unroll
        for (int c = 0; c < NCP; ++c) stx2(rs, eo, en, so + cpy(c));
    }
    DRAIN();
    const u32 pm = pack2(em * rd, shx(em * rd, 8, lane));
    const int g = j >> 1;
    u32 dg = pack2(row == 2 * g ? al * rd : 0.f, row == 2 * g + 1 ? al * rd : 0.f);
    if (again && (row >> 1) == (g ^ 1)) dg = MARK;
    const int gj = h >> 2;
    const u32 po = SL_P + (u32)((gj >> 2) * 32768 + (gj & 1) * 2048 + ((gj >> 1) & 1) * 1024 + (h & 3) * 256) + p_pos(row, g, (j ^ ~h) & 1);
    const bool done = ldx1(rs, po, so) != SENT;
#pragma unroll
    for (int c = 0; c < NCP; ++c) stx1(rs, done ? OOB : po, (j & 1) ? dg : pm, so + cpy(c));
}

__device__ __forceinline__ void s_role(const int wave, const int h, const int xg, const u64 tag, const u16* __restrict__ mem0, const u32x32 (&W)[5],
                                       char* __restrict__ ws, char* smem) {
    const int lane = lane_id(), tid = wave * 64 + lane, n = lane & 15, g = lane >> 4;
    u32* errw = (u32*)(ws + OFF_ERR);
    const rsrc_t rs = mk_rsrc(ws);
    const u32x4 srd = mk_srd(ws);
    const u32 lbase = (u32)(uintptr_t)(__attribute__((address_space(3))) char*)smem;
    TLOAD();
    int wslot = 0; const int wid = 128 + h;
    WSTAMP(2);
    mem0_image(mem0, smem, tid);
    if (tid < 448) *(u32x4*)(smem + L_K + tid * 16) = (u32x4){0u, 0u, 0u, 0u};
    const u32 wp = wait_init(rs, tag, 1);
    if (wave == 2) XSTAMP(wid, 224);
    const u32 sidx = (n & 2) ? 0xEEEE : 0x4444;
    const u32 va = lbase + (u32)(g * 256 + (n & 2) * 64 + (n >> 2) * 32 + (n & 1) * 16);
    const u32 ow = lbase + (u32)((wave & 3) * 768 + n * 8) + (wave >= 4 ? L_Q + (u32)((g >> 1) * 192 + (g & 1) * 4) : L_K + (u32)g * 192);
    u32 rnd = 0;
    const float ce = 0.08838834764831845f * 1.4426950408889634f;
    STAMPS();
    COUNT0();
#define BAR0() asm volatile("s_waitcnt vmcnt(0) lgkmcnt(0)\n\ts_barrier" ::: "memory")
#define Q_LAST() do { u32 redo; do s_l(rnd, redo, lbase); while (__builtin_expect(redo, 0)); } while (0)

    if ((wave >> 1) == 2) {
        LANES();
        const int kt = wave & 1;
        const u32 kxa = lbase + (u32)(kt * 4096 + lane * 16), qtr = lbase + L_Q + (u32)((lane & 1) * 192 + (lane >> 2) * 8);
        const u32 ep = (u32)((n & 7) * 16 + (g & 1) * 8 + (n >> 3) * 4);
        const u32 eo = (u32)((h >> 3) * 32768 + ((h >> 1) & 3) * 1024 + (2 * kt + (g >> 1)) * 256 + (h & 1) * 128) + ep;
        const u32 ew = lbase + L_E + (u32)((2 * kt + (g >> 1)) * 128) + ep;
        const u32 ts = slot_v(wp, lane) + put_off(wp, 2u, lane), tk = kq_buf(lane);
#pragma unroll 1
        for (int s = -1; s + 1 < NSEG; ++s) {
            BAR0();
            s_e(W, rnd, va, sidx, ow, lbase, kxa + tab(tk, s & 63), qtr, (u32)kt * 768u, eo, ew, LIM_X, live(srd, s), tab(ts, s & 63) STA);
            DRAIN();
            *(lds_u32p)(lbase + L_DR + (u32)kt * 4) = step_tag(s);
            FLUSH();
        }
        BAR0();
        Q_LAST();
    } else if (wave == 6) {
        LANES();
        const int gj = h >> 2;
        const u32 po = SL_P + (u32)((gj >> 2) * 32768 + (gj & 1) * 2048 + ((gj >> 1) & 1) * 1024 + (h & 3) * 256) + p_pos(n & 7, g, ((n >> 3) ^ ~h) & 1);
        const u32 er = lbase + L_E + (u32)(g * 128 + (n & 7) * 16);
        const u32 qtr = lbase + L_Q + (u32)((lane & 1) * 192 + (lane >> 2) * 8), ktr = lbase + L_K + (u32)((lane & 3) * 192 + (lane >> 2) * 8);
        const u32x4 ones = {0x3f803f80u, 0x3f803f80u, 0x3f803f80u, 0x3f803f80u};
        const u32 ts = slot_v(wp, lane) + put_off(wp, 2u, lane);
#pragma unroll 1
        for (int s = -1; s + 1 < NSEG; ++s) {
            const u32 so = tab(ts, s & 63);
            u32 bad;
            BAR0();
            s_f(W, rnd, va, sidx, ow, lbase, bad, ones, qtr, ktr, er, po, n < 8 ? 1.f : 0.f, n == 8 + 2 * g ? 1.f : 0.f, n == 9 + 2 * g ? 1.f : 0.f, live(srd, s), so, ce, LIM_D STA);
            if (__builtin_expect(bad && s >= 0, 0)) s_slow(h, s, rs, so, lbase);
            FLUSH();
        }
        BAR0();
        Q_LAST();
    } else if (wave == 7) {
        LANES();
        const u32 vm = (u32)lane * 16, cp = (u32)xg ^ (wp >> 1);
        u32 c0, r0, kr = 3950;
        calib(c0, r0);
        const u32 tc = slot_v(wp ^ 8u, lane) + copy_off(cp, lane);
        u32 pc = c0, pr = r0, tg = r0, sc = 0, left = SPIN_CAP;
        asm volatile("s_waitcnt lgkmcnt(0)" ::: "memory");
#pragma unroll 1
        for (int s = -1; s + 1 < NSEG; ++s) {
            const u32 so = sc;
            sc = tab(tc, s & 63);
            u32 poll = (0x12u >> (min(s + 1, 2) * 2)) & 3u;
            if (s == 0) BAR0();
            const u32 rn0 = rnd;
            u32 t1 = 0, tm = (u32)s + 1u;
            for (;;) {
                s_m(W, rnd, va, sidx, ow, lbase, left, c0, r0, t1, tm, tg, kr, vm, so, lbase + (poll & 2u) * 40960u, live(srd, s), poll, lane < 8 ? vm * 8u : OOB, sc + SL_TS STA);
                if (__builtin_expect(left != ~0u, 1)) break;
                left = 0;
                once_more(left, lbase, errw);
                poll = 0;
            }
            if (rnd - rn0 > 1u) COUNT(0);
            tg = t1;
            if (__builtin_expect(!tm && s >= 0, 0)) tg = read_ts(rs, sc + SL_TS, s, tm);
            track_rate(c0, r0, pc, pr, kr);
            FLUSH();
        }
        const u32 so = sc;
        for (u32 poll = 1;;) {
            if (poll) gather(tg, kr, vm, so, lbase, srd);
            if (poll == 1) LBAR();
            DRAIN();
            u32 redo;
            s_l(rnd, redo, lbase);
            if (__builtin_expect(!redo, 1)) break;
            poll = once_more(left, lbase, errw);
        }
    } else if (wave < 2) {
        LANES();
        const int nt = wave & 1;
        const u32 ktr = lbase + L_K + (u32)((lane & 3) * 192 + (lane >> 2) * 8);
        f32x4* msk = (f32x4*)(smem + L_MSK) + nt * 128 + lane;
#pragma unroll
        for (int q = 0; q < 2; ++q) {
            f32x4 m;
#pragma unroll
            for (int i = 0; i < 4; ++i) m[i] = q * 16 + 4 * g + i <= nt * 16 + n ? 0.f : -__builtin_huge_valf();
            msk[q * 64] = m;
        }
        const u32 vl = lbase + (u32)lane * 16;
        const u32 o0 = (u32)OFF_PX + (u32)((h * 2 + nt) * 1024 + lane * 16);
        const u32 o1 = (u32)OFF_PXM + (u32)(((h >> 2) * 2 + nt) * 1024 + ((h & 3) * 16 + n) * 16 + g * 4);
        u32x4 p0 = {0u, 0u, 0u, 0u};
        u32 p1 = 0;
#pragma unroll 1
        for (int s = -1; s < NSEG; ++s) {
            const u32 off = s > 0 ? 0u : OOB;
            BAR0();
            stx4(rs, (o0 + (u32)s * 65536 - 65536) | off, p0);
            stx1(rs, (o1 + (u32)s * 16384 - 16384) | off, p1);
            if (s + 1 < NSEG) s_k0(W, rnd, va, sidx, ow, lbase STA);
            else {
                s_k2(W, rnd, va, sidx, ow, lbase);
                DRAIN();
                stx1(rs, lane == 0 ? (u32)(OFF_FP + (h >> 2) * 128 + ((h & 3) * 2 + nt) * 4) : OOB, 1u);
            }
            px_step(p0, p1, vl + kq_buf(s + 3), vl + kq_buf(s + 3) + 8192 + (u32)nt * 4096, vl + L_MSK + (u32)nt * 2048, ktr, ce);
            STAMP(3);
            FLUSH();
        }
        stx4(rs, o0 + (NSEG - 1) * 65536, p0);
        stx1(rs, o1 + (NSEG - 1) * 16384, p1);
        DRAIN();
        stx1(rs, lane == 0 ? (u32)(OFF_FZ + ((h >> 2) * 2 + nt) * 128 + (h & 3) * 4) : OOB, 1u);
    } else if (wave == 2) {
        LANES();
        u32 cap = SPIN_CAP;
        u32 have = 0;
        auto seen = [&](u32x4 f) {
            u32 a = min(2 * rfl(f[0]), 2 * rfl(f[1]) + 1);
            if (h < 16) a = min(a, max(rfl(f[2]), 4u) - 1u);
            have = max(have, a);
        };
        const u32 tt = slot_v(wp ^ 8u, lane) + 3u * 32768u;
        *(lds_u32p)(lbase + 11520u + (u32)lane * 4) = 0u;
#pragma unroll 1
        for (int s = -1; s + 1 < NSEG; ++s) {
            const u32 pfo = far_off(wp ^ 1u, NEAR_KX + (u32)((s + 1) * H_ + h) * 8192);
            BAR0();
            seen(*(lds_u32x4p)(lbase + 11520u));
            s_k0(W, rnd, va, sidx, ow, lbase STA);
            u32 spins = 0;
#pragma unroll 1
            for (; have <= (u32)(s + 1) && spins < cap; ++spins) {
                CFENCE();
                seen(ldx4(rs, 0u, (u32)(OFF_FA + h * 128)));
                if (spins) __builtin_amdgcn_s_sleep(2);
            }
            if (spins >= cap) { atomicOr(errw, 8u); cap = 1; }
            CFENCE();
            if (s < 0) XSTAMP(wid, 226);
            s_fetch((u32)lane * 16, lane == s + 1 ? tt : OOB, srd, pfo, lbase + kq_buf(s + 1), lbase + 11264u);
            s_fetch((u32)lane * 16, have <= (u32)(s + 4) && lane < 4 ? (u32)(OFF_FA + h * 128 + lane * 4) : OOB, srd, pfo - 2u * (NEAR_KX - NEAR_QX),
                    lbase + kq_buf(s + 1) + 8192, lbase + 11520u);
            FLUSH();
        }
        BAR0();
        s_k2(W, rnd, va, sidx, ow, lbase);
    } else {
#pragma unroll 1
        for (int s = -1; s + 1 < NSEG; ++s) {
            BAR0();
            s_k0(W, rnd, va, sidx, ow, lbase STA);
            FLUSH();
        }
        BAR0();
        s_k2(W, rnd, va, sidx, ow, lbase);
    }
    COUNT1(0);
}

__device__ __forceinline__ void v_role(const int wave, const int h, const int xg, const u64 tag, const u16* __restrict__ mem0, const u32x32 (&W)[5],
                                       char* __restrict__ ws, char* smem) {
    const int lane = lane_id(), tid = wave * 64 + lane, n = lane & 15, g = lane >> 4;
    u32* errw = (u32*)(ws + OFF_ERR);
    const rsrc_t rs = mk_rsrc(ws);
    const u32x4 srd = mk_srd(ws);
    const u32 lbase = (u32)(uintptr_t)(__attribute__((address_space(3))) char*)smem;
    TLOAD();
    int wslot = 0; const int wid = 160 + h;
    WSTAMP(2);
    mem0_image(mem0, smem, tid);
    *(lds_u32p)(lbase + 14336) = 0u;
    const u32 wp = wait_init(rs, tag, 1);
    const u32 sidx = (n & (wave < 4 ? 2 : 8)) ? 0xEEEE : 0x4444;
    asm volatile("s_waitcnt vmcnt(0) lgkmcnt(0)" ::: "memory");
    STAMPS();
    COUNT0();
    u32 rnd = 0;
    if (wave < 4) {
        const u32 va = lbase + (u32)(g * 256 + (n & 2) * 64 + (n >> 2) * 32 + (n & 1) * 16), vw = lbase + 12288 + (u32)((wave * 16 + n) * 16 + g * 4);
        if (wave) {
#pragma unroll 1
            for (int s = 0; s < NSEG; ++s) {
                v_a0(W, rnd, va, sidx, vw, lbase STA);
                FLUSH();
                if (wave == 1 && s + 1 < NSEG) touch(srd, wp, s + 1, lbase + 15360u);
            }
        } else {
            const u32 tc = slot_v(wp ^ 8u, lane) + copy_off((u32)xg ^ (wp >> 1), lane);
            u32 c0, r0, kr = 3950;
            calib(c0, r0);
            u32 pc = c0, pr = r0, tg = r0, sc = 0, left = SPIN_CAP;
#pragma unroll 1
            for (int s = 0; s < NSEG; ++s) {
                const u32 so = sc, rn0 = rnd;
                sc = tab(tc, s);
                for (u32 poll = 2u - (((u32)s + 63u) >> 6);; poll = 0) {
                    v_a1(W, rnd, va, sidx, vw, lbase, left, c0, r0, tg + TV(15), kr, poll, (u32)lane * 16, so, lbase + (poll & 2u) * 16384u, live<1>(srd, s) STA);
                    if (__builtin_expect(left != ~0u, 1)) break;
                    left = 0;
                    once_more(left, lbase, errw);
                }
                const u32 look = rnd - rn0;
                if (look > 1) COUNT(3);
                u32 tm;
                if (s + 1 < NSEG) tg = look > 1 ? read_ts(rs, sc + SL_TS, s, tm) : read_ts_late(rs, srd, sc + SL_TS, s, tm, 0u);
                track_rate(c0, r0, pc, pr, kr);
                FLUSH();
            }
        }
    } else {
        const u32 vr = lbase + 12288 + (u32)((g * 8 + (n >> 2)) * 16 + (n & 1) * 8);
        u32 z[3];
#pragma unroll
        for (int p = 0; p < 3; ++p) z[p] = p == 2 && (g & 1) ? OOB : z_off(wp, (u32)((p * 2 + (g & 1)) * 8 + (wave & 3) * 2 + (g >> 1))) + (u32)(h * 256 + n * 16);
#pragma unroll 1
        for (int s = 0; s < NSEG; ++s) {
            const u32 sz = slot_off(wp, s);
            v_b(W, rnd, sidx, vr, z[0] + sz, z[1] + sz, z[2] + sz, srd, lbase STA);
            FLUSH();
        }
    }
    DRAIN();
    LBAR();
    stx1(rs, wave == 0 && lane_id() < 2 ? (u32)(OFF_FZ + ((h >> 2) * 2 + lane_id()) * 128 + 16 + (h & 3) * 4) : OOB, 1u);
    COUNT1(3);
}

constexpr u32 L_U = 8192, L_T = 16384;
__device__ __forceinline__ u32x4 sub4(const u32x32& v, const int i) { return (u32x4){v[i], v[i + 1], v[i + 2], v[i + 3]}; }
__device__ __forceinline__ void r_slow(const int s, rsrc_t rs, u32 so, u32 sz, u32 sm, u32 pv, u32 zv, u32 ra, u32 rb, u32 mo, u32 sidx, u32 lbase, u32& cap, u32* errw) {
    const int lane = lane_id(), n = lane & 15;
    u32 spins = 0;
    u32x4 p[8], z[8];
#pragma unroll 1
    for (; spins < cap; ++spins) {
        CFENCE();
        u32 d = 1;
#pragma unroll
        for (int j = 0; j < 8; ++j) p[j] = ldx4(rs, pv + (u32)((j & 1) * 256 + ((j >> 1) & 1) * 1024), so + (u32)(j >> 2) * 32768u);
#pragma unroll
        for (int j = 0; j < 8; ++j) z[j] = ldx4(rs, zv + (u32)j * 1024, sz);
#pragma unroll
        for (int j = 0; j < 8; ++j) d = dist(dist(d, p[j], SENT), z[j], SENT);
        if (!__any(d == 0)) break;
    }
    if (spins >= cap) { atomicOr(errw, 2u); cap = 2; }
#pragma unroll
    for (int j = 0; j < 8; ++j) {
#pragma unroll
        for (int i = 0; i < 4; ++i) p[j][i] = p[j][i] == MARK ? 0u : p[j][i];
    }
    *(lds_vu32p)(lbase + L_T + 12) = step_tag(s);
    LBAR();
    LBAR();
    f32x4 acc = {0.f, 0.f, 0.f, 0.f};
#pragma unroll
    for (int j = 0; j < 8; j += 2) {
        const u32 ua = (j == 6 ? rb : ra) + (u32)j * 1024;
        acc = smf((u32x4){p[j][2], p[j][3], p[j + 1][0], p[j + 1][1]}, z[j], z[j + 1], acc, (int)sidx);
        acc = smf((u32x4){p[j + 1][2], p[j + 1][3], p[(j + 2) & 7][0], p[(j + 2) & 7][1]}, *(lds_u32x4p)ua, *(lds_u32x4p)(ua + 1024), acc, (int)sidx);
    }
    const float x0 = acc[0] + acc[2], x1 = acc[1] + acc[3], o = __shfl_xor(n < 8 ? x1 : x0, 8);
    const u32 r = n < 8 ? pack2(x0, o) : pack2(o, x1);
#pragma unroll
    for (int c = 0; c < NCP; ++c) stx1(rs, mo, r, sm + cpy(c));
    LBAR();
}

__device__ __forceinline__ void r_role(const int wave, const int ct, const int xg, const u32 wp, char* __restrict__ ws, char* smem) {
    const int lane = lane_id(), tid = wave * 64 + lane, n = lane & 15, g = lane >> 4;
    u32* errw = (u32*)(ws + OFF_ERR);
    const rsrc_t rs = mk_rsrc(ws);
    const u32x4 srd = mk_srd(ws);
    const u32 lbase = (u32)(uintptr_t)(__attribute__((address_space(3))) char*)smem;
    if (tid < 6) *(u32*)(smem + L_T + 8 + tid * 4) = ~0u;
    asm volatile("s_waitcnt lgkmcnt(0)" ::: "memory");
    int wslot = 0; const int wid = 192 + ct;
    WSTAMP(1);
    TLOAD();
    const u32 cp = (u32)xg ^ (wp >> 1);
    u32 cap = SPIN_CAP;
    STAMPS();
    LOGS();
    COUNT0();
    if (wave == 0) {
        const int kp = ct >= 36 ? 10 : ct >> 2;
        const u32 mb = (u32)((kp >> 2) * 32768 + (kp & 3) * 1024 + (ct & 3) * 256);
        const u32 mo = mb + (u32)(((n & 7) >> 2) * 128 + (2 * g + (n >> 3)) * 16 + (n & 3) * 4);
        const u32 pv = (u32)((g >> 1) * 2048 + (g & 1) * 512 + n * 16), zv = (u32)(g * 256 + (n & 7) * 32 + (n >> 3) * 16), sidx = (n & 2) ? 0xEEEE : 0x4444;
        const u32 ra = lbase + L_U + (u32)(((g & 1) ? 2048 : 256) + (g >> 1) * 512 + (n & 7) * 32 + (n >> 3) * 16), rb = ra - (u32)(g & 1) * 8192;
        const u32 te = slot_v(wp, lane) + copy_off(cp, lane) + SL_P, tm = slot_v(wp ^ 8u, lane) + put_off(wp, 0u, lane), tz = slot_z(wp, lane) + z_off(wp, (u32)ct);
#pragma unroll 1
        for (int s = -1; s + 1 < NSEG; ++s) {
            const u32 so = tab(te, s & 63), sm = tab(tm, s & 63), sz = tab(tz, s & 63);
            u32 bad, pass;
            r_f(bad, pass, sidx, pv, zv, ra, rb, mo, lbase + L_T + 16 + (u32)(lane & 3) * 4, live(srd, s), so, sz, sm, lbase, step_tag(s), unless_neg(cap, s), min(TV(8), 256u) STA);
            if (__builtin_expect(bad, 0)) { if (s < 0) LBAR(); else r_slow(s, rs, so, sz, sm, pv, zv, ra, rb, mo, sidx, lbase, cap, errw); }
            if (pass) COUNT(2);
            touch(srd, wp, s + 1, lbase + 17408u);
            LOG();
        }
    } else if (wave == 4) {
        u32 c0, r0, kr = 3950;
        calib(c0, r0);
        const u32 tc = slot_v(wp ^ 8u, lane) + copy_off(cp, lane) + SL_TS;
        u32 pc = c0, pr = r0, tg = r0 - 0x10000u, t2 = 0;
#pragma unroll 1
        for (int s = -1; s + 1 < NSEG; ++s) {
            u32 t1 = tg, ok = 0;
            if (s) r_t0(c0, r0, t1, t2, ok, tg + TV(6), tg + (t2 & 0xffffu), kr, (u32)(s & 127) + 1u, lbase, live(srd, s), tab(tc, s & 63) STA);
            if (s < 0) { if (!ok) { LBAR(); LBAR(); } }
            else if (__builtin_expect(!ok, 0)) {
                until(tg + TV(6) + TV(7) - 256u);
                r_t1(c0, r0, t1, t2, lane < 8 ? (u32)lane * 128u : OOB, kr, (u32)s + 1u, lbase, srd, tab(tc, s) STA);
            }
            if (__builtin_expect(ok == 2u && s >= 0, 0)) t1 = read_ts(rs, tab(tc, s), s, ok);
            tg = t1;
            if (__builtin_expect(rfl(*(lds_vu32p)(lbase + L_T + 8)) == step_tag(s), 0)) {
                LBAR();
                if (rfl(*(lds_vu32p)(lbase + L_T + 12)) == step_tag(s)) { LBAR(); LBAR(); }
            }
            track_rate(c0, r0, pc, pr, kr);
            LOG();
        }
    } else {
        const int q = wave == 5 ? 3 : wave - 1;
        u32 have = 0;
        u32x32 bn = {};
        auto seen = [&](u32x2 f) {
            u32 a = min(2 * f[0], 2 * f[1] + 1);
            a = min(a, (u32)__builtin_amdgcn_update_dpp(0u, a, 0xb1, 0xf, 0xf, false));
            a = min(a, (u32)__builtin_amdgcn_update_dpp(0u, a, 0x4e, 0xf, 0xf, false));
            a = min(a, (u32)__builtin_amdgcn_update_dpp(0u, a, 0x124, 0xf, 0xf, false));
            have = max(have, rfl(a));
        };
        const u32 fo = (u32)(OFF_FB + (q * 8 + (lane & 7)) * 128);
        auto load_b = [&](int sg, u32x32& bx) {
            u32 spins = 0;
#pragma unroll 1
            for (; have <= (u32)sg && spins < cap; ++spins) {
                CFENCE();
                seen(ldx2(rs, fo));
                if (spins) __builtin_amdgcn_s_sleep(4);
            }
            if (spins >= cap) { atomicOr(errw, 16u); cap = 1; }
            CFENCE();
            const u32 bs = far_off(wp, FAR_BX + (u32)(sg * NRED + ct) * 32768 + (u32)q * 8192);
            pull((u32)lane * 128, bs, srd, 1u, 0u, lbase + 17664u);
            u32x4 t[8];
#pragma unroll
            for (int i = 0; i < 8; ++i) t[i] = ldx4(rs, (u32)(lane * 16 + i * 1024), bs);
#pragma unroll
            for (int i = 0; i < 8; ++i)
#pragma unroll
                for (int j = 0; j < 4; ++j) bx[(i >> 1) * 8 + j * 2 + (i & 1)] = t[i][j];
        };
        const u32 eo = (u32)(q * 32768 + lane * 16), te = slot_v(wp, lane) + copy_off(cp, lane), sidx = n >= 8 ? 0xEEEE : 0x4444;
        const u32 uw = lbase + L_U + (u32)(q * 2048 + (g >> 1) * 256 + n * 16 + (g & 1) * 8);
        const u32 tb = far_v(wp, FAR_BX + (u32)(min(lane + 1, NSEG - 1) * NRED + ct) * 32768 + (u32)q * 8192);
        u32 s = ~0u, why = 0, left = 0;
#pragma unroll 1
        for (;;) {
            if (why & 1u) {
#pragma unroll
                for (int i = 0; i < 4; ++i) {
                    const f32x4 u = smf(ldx4(rs, eo + (u32)i * 1024, tab(te, (int)s - 1)), sub4(bn, i * 8), sub4(bn, i * 8 + 4), (f32x4){0.f, 0.f, 0.f, 0.f}, (int)sidx);
                    *(lds_u32x2p)(uw + (u32)i * 512) = (u32x2){pack2(u[0], u[1]), pack2(u[2], u[3])};
                }
                LBAR();
                LBAR();
            }
            if (s + 1 == NSEG) break;
            if (s == 0) left = SPIN_CAP;
            if ((int)s >= 0) load_b((int)s, bn);
            have = to_sgpr(have); left = to_sgpr(left);
            r_u(bn, s, have, left, why, sidx, eo, uw, lbase + L_T + 16 + (u32)q * 4, (u32)lane * 16, fo, te, tb, live(srd, (int)s), (u32)q * 4u, lbase STA LGA);
            s = to_sgpr(s); have = to_sgpr(have); left = to_sgpr(left); why = to_sgpr(why);
        }
        if (left == 0) atomicOr(errw, 4u);
    }
    COUNT1(wave ? 1 : 2);
    LOGOUT(32 + ct);
}

__device__ __forceinline__ void relay_role(const u32 wp, const int xg, char* __restrict__ ws) {
    const int lane = lane_id();
    const rsrc_t rs = mk_rsrc(ws);
    const u32 tc = slot_v(wp ^ 8u, lane) + copy_off((u32)xg ^ (wp >> 1), lane) + SL_TS;
#pragma unroll 1
    for (int k = 0; k + 1 < NSEG; ++k) {
        u32 t;
#pragma unroll 1
        for (u32 spins = 0;; ++spins) {
            u32 ok;
            t = read_ts(rs, tab(tc, k), k, ok);
            if (ok) break;
            if (spins >= 16u || ldx1(rs, (u32)OFF_ERR) != 0u) return;
        }
        if (k >= 3) stx1(rs, lane < 8 ? (u32)(OFF_RDY + lane * 128) : OOB, (u32)k - 2u);
        for (int q = 0; q < 64 && (int)((u32)__builtin_amdgcn_s_memrealtime() * 24u - t) < 2400; ++q) __builtin_amdgcn_s_sleep(2);
    }
}

__device__ __forceinline__ void lead_role(const u32 wp, const int xg, char* __restrict__ ws, char* smem) {
    const int lane = lane_id();
    u32* errw = (u32*)(ws + OFF_ERR);
    const rsrc_t rs = mk_rsrc(ws);
    const u32x4 srd = mk_srd(ws);
    TLOAD();
    STAMPS();
    const u32 tsw = lane < 4 ? SL_TS + (u32)((lane & 1) * 4096 + (lane >> 1) * 16384 + xg * 128) : OOB;
    const u32 tm = slot_v(wp ^ 8u, lane), tc = tm + copy_off((u32)xg ^ (wp >> 1), lane);
    u32 c0, r0, kr = 3950, ki = 265, bump = 0, hist = 0, dp = 0;
    calib(c0, r0);
    u32 pc = c0, pr = r0, tg = r0, cap = SPIN_CAP;
#pragma unroll 1
    for (int s = 0; s + 1 < NSEG; ++s) {
        u32 sr = tsw + tab(tm, s);
        const u32 mg = bump + (s < EARLY_N ? TV(10) : 0u) + (s < 4 ? TV(3) : 0u);
        const u32 de = (TV(0) * ki >> 8) + mg, dz = TV(1) * ki >> 8, per = max(de, dp) + (TV(2) * ki >> 8) + mg;
        dp = de;
        u32 look = 0;
        DRAIN();
        {
            const u32 so = tab(tc, (s + 63) & 63);
#pragma unroll 1
            for (;; ++look) {
                u64 tl;
                u32 miss;
                l_g(miss, c0, r0, tl, tg + TV(15), look ? 0u : kr, (u32)((lane >> 5) * 32768 + (lane & 31) * 128), lane < 16 ? (u32)(65536 + (lane >> 3) * 2048 + (lane & 7) * 128) : OOB, so, live<1>(srd, s), (u32x4){tg + de, tg + per, de | (dz << 16), (u32)s + 1u}, look || !s ? OOB : sr STA);
                if (!s) break;
                if (look && (int)((u32)tl * 24u + TV(14) - tg) > 0) tg = (u32)tl * 24u + TV(14);
                if (__builtin_expect(!miss, 1)) break;
                if (!look) {
                    sr += 512u;
                    tg += TV(7);
                }
                if (look >= cap) { atomicOr(errw, 4u); cap = 1; break; }
            }
        }
        if (!s) {
            bool dead = false;
            wait_word(rs, (u32)(OFF_FA + lane * 128), true, 1u, dead, errw, 8u, lane);
            XSTAMP(192 + xg, 227);
            tg = (u32)__builtin_amdgcn_s_memrealtime() * 24u + TV(13) - 512u;
        }
        if (!s || look) stx4(rs, sr, (u32x4){tg + de, tg + per, de | (dz << 16), (u32)s + 1u});
        nap(384);
        u32 m;
        tg = look ? read_ts(rs, tab(tc, s) + SL_TS, s, m) : read_ts_late(rs, srd, tab(tc, s) + SL_TS, s, m);
        touch(srd, wp, s + 1, (u32)(uintptr_t)(__attribute__((address_space(3))) char*)smem);
        track_rate(c0, r0, pc, pr, kr);
        hist = ((hist << 1) | (s >= BUMP_S0 && m != 15u ? 1u : 0u)) & BUMP_WIN;
        if (__builtin_popcount(hist) >= BUMP_N && bump < BUMP_MAX * TV(11)) { bump += TV(11); hist = 0; }
        if (pc == c0) ki = to_sgpr((u32)(1048576.f * __builtin_amdgcn_rcpf((float)kr) + 0.5f));
        FLUSH();
    }
}

__device__ __forceinline__ void fill_role(const u32 wp, const int fi, char* __restrict__ ws) {
    const int lane = lane_id();
    const rsrc_t rs = mk_rsrc(ws);
    TLOAD();
    const u32 oz = (u32)(fi * 65536 + lane * 16);
    nap(TV(4) * 64u);
#pragma unroll 1
    for (int s = 5; s < NSEG; ++s) {
        const u32 un = 0x38u << ((~((wp >> 3) ^ ((u32)s >> 5)) & 1u) * 8u), a = oz + slot_off(wp, s);
        const u32 a0 = (un >> (2 * fi)) & 1u ? OOB : a, a1 = (un >> (2 * fi + 1)) & 1u ? OOB : a + 32768u;
#pragma unroll
        for (u32 i = 0; i < 32; ++i) {
            stx4(rs, (i < 16 ? a0 : a1) + ((i >> 3) & 1u) * 16384 + (i & 7) * 1024, (u32x4){SENT, SENT, SENT, SENT});
            if (i == 15) {
                asm volatile("s_waitcnt vmcnt(16)" ::: "memory");
                stx1(rs, lane == 0 ? (u32)(OFF_FA + fi * 128 + 8) : OOB, (u32)s - 1u);
            }
            if (i % FILLG == FILLG - 1) nap(TV(9));
        }
    }
    DRAIN();
    stx1(rs, lane == 0 ? (u32)(OFF_FA + fi * 128 + 8) : OOB, (u32)NSEG + 1u);
}

__device__ __forceinline__ int tile_of(const int xg, const int hl, const int i) {
    const u32 pieces = hl ? 0x85410u : 0x97632u;
    return CFG ? (int)((pieces >> ((i >> 1) * 4)) & 15u) * 4 + (i & 1) * 2 + (xg & 1) : (int)((pieces >> (i * 4)) & 15u) * 4 + xg;
}
__device__ __forceinline__ void warm_role(const u32 wp, const int xg, const int wi, char* __restrict__ ws, char* smem) {
    const int lane = lane_id();
    const rsrc_t rs = mk_rsrc(ws);
    const u32x4 srd = mk_srd(ws);
    const u32 lbase = (u32)(uintptr_t)(__attribute__((address_space(3))) char*)smem;
    TLOAD();
    u32 have = 0, ta = 0;
    int next = 1;
    const u32 tc = slot_v(wp ^ 8u, lane) + copy_off((u32)xg ^ (wp >> 1), lane) + SL_TS;
    const u32 wq = wi < 0 ? wp ^ 1u : wp, ls = wi < 0 ? H_ * 8192u : NRED * 32768u;
    const u32 l0 = wi < 0 ? (wi & 1 ? NEAR_KX : NEAR_QX) + (u32)(xg * 8 + (lane & 7)) * 8192 : FAR_BX + (u32)(min(lane, 9) * 4 + xg) * 32768 + (u32)wi * 8192;
    auto look = [&] {
        const u32x2 f = ldx2(rs, wi < 0 ? (u32)(OFF_FA + (xg * 8 + (lane & 7)) * 128) : (u32)(OFF_FB + (wi * 8 + (lane & 7)) * 128));
        u32 a = min(2 * f[0], 2 * f[1] + 1);
#pragma unroll
        for (int q = 1; q < 8; q <<= 1) a = min(a, (u32)__shfl_xor((int)a, q));
        have = rfl(a);
    };
#pragma unroll 1
    for (u32 spins = 0; next < WFIRST && spins < 0x4000u; ++spins) {
        CFENCE();
        if (have <= (u32)next) { __builtin_amdgcn_s_sleep(4); look(); }
        else { pull((u32)lane * 128, far_v(wq, l0 + (u32)next * ls), srd, wi < 0 ? 8u : 10u, 64u, lbase + 69632u); ++next; }
    }
#pragma unroll 1
    for (int s = 0; s + 2 < NSEG; ++s) {
        if (s) until(ta + 1000u);
        u32 tm;
        ta = read_ts_late(rs, srd, tab(tc, s), s, tm);
        if (have <= (u32)s + WAHEAD) look();
        if (next < s + 2) next = s + 2;
#pragma unroll 1
        for (; next <= s + WAHEAD && next < NSEG && have > (u32)next; ++next) {
            const u32 tb = far_v(wq, l0 + (u32)next * ls);
            until(ta + TV(5) - 1024u);
            pull((u32)lane * 128, tb, srd, wi < 0 ? 8u : 10u, min(TV(12), 256u), lbase + 69632u);
        }
    }
    DRAIN();
}

__device__ __forceinline__ void topk_role(const u64 tag, char* __restrict__ ws) {
    const int lane = lane_id();
    const rsrc_t rs = mk_rsrc(ws);
    wait_init(rs, tag, 2);
    u32 v[32];
#pragma unroll
    for (int i = 0; i < 8; ++i) {
        const u32x4 t = ldx4(rs, (u32)lane * 16, (u32)(OFF_R + i * 1024));
#pragma unroll
        for (int j = 0; j < 4; ++j) v[i * 4 + j] = ((t[j] ^ ((u32)((int)t[j] >> 31) | 0x80000000u)) & 0xffff0000u) | (u32)(2047 - i * 256 - j - lane * 4);
    }
    u32 top = ~0u, res = 0;
#pragma unroll 1
    for (int c = 0; c < TOPC; ++c) {
        u32 d = ~0u;
#pragma unroll
        for (int i = 0; i < 32; ++i) d = min(d, top - v[i]);
        d = min(d, (u32)__builtin_amdgcn_update_dpp(0u, d, 0xb1, 0xf, 0xf, false));
        d = min(d, (u32)__builtin_amdgcn_update_dpp(0u, d, 0x4e, 0xf, 0xf, false));
        d = min(d, (u32)__builtin_amdgcn_update_dpp(0u, d, 0x124, 0xf, 0xf, false));
        d = min(d, (u32)__builtin_amdgcn_update_dpp(0u, d, 0x128, 0xf, 0xf, false));
        const u32 dm = min(min((u32)__builtin_amdgcn_readlane(d, 0), (u32)__builtin_amdgcn_readlane(d, 16)), min((u32)__builtin_amdgcn_readlane(d, 32), (u32)__builtin_amdgcn_readlane(d, 48)));
        top -= dm;
        if (lane == c) res = 2048u - (top & 2047u);
        top -= 1u;
    }
    stx1(rs, lane < TOPC ? (u32)(OFF_IDX + lane * 4) : OOB, res);
    XSTAMP(192, 224);
}

#define MOEV 0
#define MOE_LD(p) __builtin_nontemporal_load(p)
#define MOE_NT " nt"
template <int NK>
__device__ __forceinline__ void moe_ld(const u16* __restrict__ wp, const int ldw, const int nvalid, u32 (&r)[NK][8]) {
#pragma unroll
    for (int k = 0; k < NK; ++k)
#pragma unroll
        for (int i = 0; i < 8; ++i) r[k][i] = k < nvalid ? MOE_LD((const u32*)(wp + (size_t)(k * 32 + i) * ldw)) : 0u;
}
template <int NK>
__device__ __forceinline__ void moe_mma(const u32 (&r)[NK][8], const u32x4 (&af)[NK], f32x4& ce, f32x4& co) {
#pragma unroll
    for (int k = 0; k < NK; ++k) {
        u32x4 be, bo;
#pragma unroll
        for (int j = 0; j < 4; ++j) {
            be[j] = __builtin_amdgcn_perm(r[k][2 * j + 1], r[k][2 * j], 0x05040100u);
            bo[j] = __builtin_amdgcn_perm(r[k][2 * j + 1], r[k][2 * j], 0x07060302u);
        }
        ce = mfma16(af[k], be, ce);
        co = mfma16(af[k], bo, co);
    }
}
constexpr int NGU = FF_ / 32, NDN = D_ / 32;
__device__ __forceinline__ void moe_part(const int w, const int tid, const u16* __restrict__ wg, const u16* __restrict__ wu,
                                         const u16* __restrict__ wd, char* __restrict__ ws, char* smem) {
    const int wave = __builtin_amdgcn_readfirstlane(tid >> 6), lane = tid & 63;
    const int n = lane & 15, g = lane >> 4;
    u32* errw = (u32*)(ws + OFF_ERR);
    const rsrc_t rs = mk_rsrc(ws);
    bool dead = false;
    f32x4 (*red)[2][64] = (f32x4(*)[2][64])smem;
    const f32x4 z = {0.f, 0.f, 0.f, 0.f};
    wait_word(rs, (u32)(OFF_IDX + (n & 7) * 4), true, 1u, dead, errw, 16u, lane);
    const u32 row = (ldx1(rs, (u32)(OFF_IDX + (n & 7) * 4)) - 1u) & (S_ - 1);
    u32 r1[5][8], r2[7][8];
    const int kq = wave >> 1, dg = w - (64 - NDN), k0 = wave * 7, nk = wave == 7 ? 5 : 7;
    if (w < NGU) moe_ld<5>(((wave & 1) ? wu : wg) + (size_t)(kq * 160 + g * 8) * FF_ + w * 32 + n * 2, FF_, 5, r1);
    if (dg >= 0) moe_ld<7>(wd + (size_t)(k0 * 32 + g * 8) * D_ + dg * 32 + n * 2, D_, nk, r2);
    if (w < NGU) {
        u32x4 af[5];
#pragma unroll
        for (int k = 0; k < 5; ++k) af[k] = ldx4(rs, (u32)OFF_XN + (row * D_ + (u32)((kq * 5 + k) * 32 + g * 8)) * 2);
        f32x4 ce = z, co = z;
        moe_mma<5>(r1, af, ce, co);
        red[wave][0][lane] = ce; red[wave][1][lane] = co;
        __syncthreads();
        if (wave == 0 && g < 2) {
            f32x4 t[4];
#pragma unroll
            for (int q = 0; q < 4; ++q) t[q] = (red[q >> 1][q & 1][lane] + red[2 + (q >> 1)][q & 1][lane]) + (red[4 + (q >> 1)][q & 1][lane] + red[6 + (q >> 1)][q & 1][lane]);
#pragma unroll
            for (int i = 0; i < 4; ++i) {
                float res[2];
#pragma unroll
                for (int q = 0; q < 2; ++q) {
                    float gg = bfr(t[q][i]), uu = bfr(t[2 + q][i]);
                    float sg = bfr(gg / (1.0f + __expf(-gg)));
                    res[q] = sg * uu;
                }
                stx1(rs, (u32)(OFF_HB + ((size_t)(4 * g + i) * FF_ + w * 32 + n * 2) * 2), pack2(res[0], res[1]));
            }
        }
        if (wave == 0) {
            DRAIN();
            if (lane == 0) __hip_atomic_fetch_add((u32*)(ws + OFF_CNT), 1u, __ATOMIC_RELAXED, __HIP_MEMORY_SCOPE_AGENT);
        }
        __syncthreads();
    }
    if (dg >= 0) {
        const float rv = __uint_as_float(ldx1(rs, (u32)OFF_R + row * 4));
        wait_word(rs, (u32)OFF_CNT, true, (u32)NGU, dead, errw, 16u, lane);
        u32x4 af[7];
#pragma unroll
        for (int k = 0; k < 7; ++k) af[k] = ldx4(rs, (u32)(OFF_HB + ((size_t)(n & 7) * FF_ + (k0 + (k < nk ? k : 0)) * 32 + g * 8) * 2));
        f32x4 ye = z, yo = z;
        moe_mma<7>(r2, af, ye, yo);
        red[wave][0][lane] = ye; red[wave][1][lane] = yo;
        __syncthreads();
        if (wave == 0) {
            f32x4 t[2];
#pragma unroll
            for (int q = 0; q < 2; ++q)
                t[q] = ((red[0][q][lane] + red[1][q][lane]) + (red[2][q][lane] + red[3][q][lane])) + ((red[4][q][lane] + red[5][q][lane]) + (red[6][q][lane] + red[7][q][lane]));
            const float rs1 = bfr(1.0f / (1.0f + __expf(-rv)));
#pragma unroll
            for (int i = 0; i < 4; ++i) {
                const float rsg = __shfl(rs1, (4 * g + i) & 7);
                stx1(rs, g < 2 ? (u32)(OFF_UPD + ((size_t)(4 * g + i) * D_ + dg * 32 + n * 2) * 2) : OOB, pack2(bfr(t[0][i]) * rsg, bfr(t[1][i]) * rsg));
            }
            DRAIN();
            if (lane == 0) __hip_atomic_fetch_add((u32*)(ws + OFF_CNT + 256), 1u, __ATOMIC_RELAXED, __HIP_MEMORY_SCOPE_AGENT);
        }
        __syncthreads();
    }
}

__device__ __forceinline__ void moe_ahead(const int w, const int t, const u16* __restrict__ wg, const u16* __restrict__ wu, const u16* __restrict__ wd, u32 lds) {
    const int lane = lane_id(), t2 = w < NGU ? t - 20 : t;
    if (t2 >= (w >= 64 - NDN ? 27 : 0)) return;
    const u16* p = t2 < 0 ? (t < 10 ? wg : wu - 640 * FF_) + (size_t)(t * 64 + lane) * FF_ + w * 32 : wd + (size_t)(t2 * 64 + lane) * D_ + (w - (64 - NDN)) * 32;
    u32 sv;
    asm volatile("s_mov_b32 %0, m0\n\ts_mov_b32 m0, %2\n\ts_nop 0\n\tglobal_load_lds_dword %1, off" MOE_NT "\n\ts_mov_b32 m0, %0" : "=&s"(sv) : "v"(p), "s"(lds) : "memory");
}

#define LOOKST 18
constexpr int NBIG = (NSEG - 1) * 10, NTASK = NBIG + 80;

struct OutCtx { rsrc_t rs; const u16* x; u16* out; u32* errw; u32 wp; int wid; char* ws; };
template <int OT, int NT>
__device__ __forceinline__ void out_task(const OutCtx& cx, const int sg, const int c0, const int n0, const int tid, const int (&sel_idx)[TOPC], bool& adone, u32& rdy,
                                         bool& dead, char* redp, u32* nextp, u32* tctr) {
    const int wave = __builtin_amdgcn_readfirstlane(tid >> 6), lane = tid & 63, n = lane & 15, g = lane >> 4;
    const rsrc_t rs = cx.rs;
    if (!adone && rdy <= (u32)sg) {
        wait_word(rs, (u32)(OFF_FB + (wave * 4 + (lane & 3)) * 128 + (sg & 1) * 4), true, (u32)(sg >> 1) + 1, dead, cx.errw, 32u, lane);
        u32x2 f = ldx2(rs, (u32)(OFF_FB + (wave * 4 + (lane & 3)) * 128));
        adone = __all(f[0] >= NSEG / 2 && f[1] >= NSEG / 2);
    }
    u32x4 af[OT][5], bq[NT][5];
    const u32 bo = amap((u32)(wave * 4096 + lane * 16));
#pragma unroll
    for (int c = 0; c < OT; ++c) {
        const u32 bb = far_off(cx.wp, FAR_BX + (u32)(sg * 40 + c0 + c) * 32768);
#pragma unroll
        for (int hh = 0; hh < 4; ++hh) af[c][hh] = ldx4(rs, bo + hh * 1024, bb);
    }
    const int ti = (tid >> 6) & (OT * NT - 1);
    const int token = sg * SEGL + (n0 + (ti & (NT - 1))) * 16 + n;
    const int col = (c0 + ti / NT) * 16 + 4 * g;
    int sel = -1;
#pragma unroll
    for (int c = 0; c < TOPC; ++c) if (sel_idx[c] == token) sel = c;
    const u32x2 xv = *(const u32x2*)(cx.x + (size_t)token * D_ + col);
    const u32x2 nv = ldx2(rs, (u32)(OFF_XN + ((size_t)token * D_ + col) * 2));
    if (__any(sel >= 0)) wait_word(rs, (u32)(OFF_CNT + 256), true, (u32)NDN, dead, cx.errw, 256u, lane);
    const u32x2 uv = ldx2(rs, sel >= 0 ? (u32)(OFF_UPD + ((size_t)sel * D_ + col) * 2) : OOB);
    __builtin_amdgcn_sched_barrier(0);
    const bool late = sg + 4 >= NSEG;
    if (late) wait_flags(rs, (u32)((OT == 1 ? OFF_FZ + (wave * 2 + n0) * 128 : OFF_FP + wave * 128) + (lane & 1) * 16), true, 1u, dead, cx.errw, 64u, lane);
    else if (rdy <= (u32)sg) {
#pragma unroll 1
        for (u32 spins = 0; !dead; ++spins) {
            CFENCE();
            rdy = __builtin_amdgcn_readfirstlane(ldx1(rs, (u32)(OFF_RDY + (cx.wid & 7) * 128)));
            if (rdy > (u32)sg) break;
            __builtin_amdgcn_s_sleep(8);
            if (spins > SPIN_CAP) { if (lane == 0) atomicOr(cx.errw, 64u); dead = true; }
            if ((spins & 63) == 63 && ldx1(rs, (u32)OFF_ERR) != 0u) dead = true;
        }
        CFENCE();
    }
#pragma unroll
    for (int c = 0; c < OT; ++c) af[c][4] = ldx4(rs, (u32)(wave * 64 + lane) * 16, slot_off(cx.wp, sg) + z_off(cx.wp, (u32)(c0 + c)));
#pragma unroll
    for (int nt = 0; nt < NT; ++nt) {
#pragma unroll
        for (int hh = 0; hh < 4; ++hh) bq[nt][hh] = ldx4(rs, (u32)lane * 16, (u32)OFF_PX + (u32)((sg * H_ + wave * 4 + hh) * 2 + n0 + nt) * 1024);
        bq[nt][4] = ldx4(rs, (u32)lane * 16, (u32)OFF_PXM + (u32)((sg * 8 + wave) * 2 + n0 + nt) * 1024);
    }
    CFENCE();
    u32 next = 0;
    if (tid == 0) next = !late ? __hip_atomic_fetch_add(tctr, 1u, __ATOMIC_RELAXED, __HIP_MEMORY_SCOPE_AGENT)
                                 : __hip_atomic_load(tctr, __ATOMIC_RELAXED, __HIP_MEMORY_SCOPE_AGENT);
    f32x4 (*red)[OT * NT][64] = (f32x4(*)[OT * NT][64])redp;
#pragma unroll
    for (int c = 0; c < OT; ++c)
#pragma unroll
        for (int nt = 0; nt < NT; ++nt) {
            f32x4 acc = {0.f, 0.f, 0.f, 0.f};
#pragma unroll
            for (int k = 0; k < 5; ++k) acc = mfma16(af[c][k], bq[nt][k], acc);
            red[wave][c * NT + nt][lane] = acc;
        }
    if (tid == 0) {
        if (late && next < (u32)NTASK) next = __hip_atomic_fetch_add(tctr, 1u, __ATOMIC_RELAXED, __HIP_MEMORY_SCOPE_AGENT);
        *nextp = next;
    }
    LBAR();
    if (tid < OT * NT * 64) {
        f32x4 sum = red[0][ti][lane];
#pragma unroll
        for (int wv = 1; wv < 8; ++wv) sum += red[wv][ti][lane];
        float res[4];
#pragma unroll
        for (int q = 0; q < 4; ++q) {
            float xx = bf2f((u16)((xv[q >> 1] >> ((q & 1) * 16)) & 0xffff));
            float nn = bf2f((u16)((nv[q >> 1] >> ((q & 1) * 16)) & 0xffff));
            float bo2 = nn;
            if (sel >= 0) bo2 = bfr(nn + bf2f((u16)((uv[q >> 1] >> ((q & 1) * 16)) & 0xffff)));
            float gg = bfr(bo2 / (1.0f + __expf(-bo2)));
            float sv = bfr(sum[q]);
            res[q] = bfr(xx + bfr(sv * gg));
        }
        *(u32x2*)(cx.out + (size_t)token * D_ + col) = (u32x2){pack2(res[0], res[1]), pack2(res[2], res[3])};
    }
}

constexpr u32 L_VS = 40960;
__device__ __forceinline__ u32 xbuf(int b) { return b == 0 ? 65536u : (b == 1 ? 106496u : 0u); }
__device__ __forceinline__ void worker_role(const int wave, const int wid, const u64 tag, const u32 wp, const u16* __restrict__ x, const u32x32 (&W)[5],
                                            const u16* __restrict__ wg, const u16* __restrict__ wu,
                                            const u16* __restrict__ wd, char* __restrict__ ws, u16* __restrict__ out, char* smem) {
    const int lane = lane_id(), tid = wave * 64 + lane;
    const int n = lane & 15, g = lane >> 4;
    const int par = (wid >> 2) & 1, kind = (wid >> 3) & 1, h = (wid & 3) * 8 + (wid >> 4), type = kind * 2 + par;
    u32* errw = (u32*)(ws + OFF_ERR);
    const rsrc_t rs = mk_rsrc(ws);
    bool dead = false;
    const bool isqk = type < 2;
    int wslot = 1;
    u32* sh = (u32*)(smem + L_MISC);

    {
        const u32 lbase = (u32)(uintptr_t)(__attribute__((address_space(3))) char*)smem;
        const u32x4 srd = mk_srd(ws);
        const u32 vl = lbase + (u32)lane * 16, xp = (u32)(((wave >> 1) & 1) * 20480 + (wave < 4 ? 4096 + (wave & 1) * 8192 : (wave & 1) * 2048)), xv = xp + (u32)lane * 16, ca = lbase + L_MISC + 64;
        const u32 fo = (u32)(OFF_FA + kind * 4096 + h * 128 + par * 4);
        auto xsrc = [&](int it) { return it < NSEG / 2 ? (u32)OFF_XNP + (u32)(it * 2 + par) * 40960u : (u32)OFF_XNP; };
        auto now = [&](u32 v) {
            DRAIN();
            if (lane_id() == 0 && __hip_atomic_fetch_add((u32*)(smem + L_MISC + 68), 1u, __ATOMIC_RELAXED, __HIP_MEMORY_SCOPE_WORKGROUP) == v * 4 - 1) { stx1(rs, fo, v); XSTAMP(wid, 224 + v); }
        };
        WSTAMP(4);
        STAMPS();
        u32 b0 = xbuf(0), b1 = xbuf(1), b2 = xbuf(2);
        auto rot = [&] { const u32 t = b0; b0 = b1; b1 = b2; b2 = t; };
        if (wave < 4) {
            const u32 oq = (u32)(wave * 64 + lane) * 16;
#pragma unroll 1
            for (int it = 0; it < NSEG / 2; ++it) {
                if (isqk) {
                    if ((it & ~4) == 2) wait_init(rs, tag, 1 + (it >> 2));
                    const u32 ob = far_off(wp ^ 1u, NEAR_QX + (u32)((it * 2 + par) * H_ + h) * 8192);
                    wx0(W, vl + b0, oq, srd, xv, xsrc(it + 2), lbase + b2 + xp, ob, ob + 4096 STA);
                } else wx1(W, vl + b0, lbase + L_VS + (u32)(it & 1) * 8192 + oq STA);
                rot();
                CFLUSH();
                if (it < 3 || (it & 7) == 7) WSTAMP(0x10 + it);
            }
            DRAIN();
            LBAR();
        } else if (isqk) {
            const u32 oq = (u32)(tid & 255) * 16;
            LBAR();
#pragma unroll 1
            for (int it = 0; it < NSEG / 2; ++it) {
                const int sg = it * 2 + par;
                if ((it & ~4) == 2) sh[18] = wait_init(rs, tag, 1 + (it >> 2));
                const u32 ob = far_off(wp ^ 1u, NEAR_KX + (u32)(sg * H_ + h) * 8192);
                wk(W, vl + b0, oq, srd, ob, ob + 4096, 0.08838834764831845f * 1.4426950408889634f, xv, xsrc(it + 2), lbase + b2 + xp,
                   ca, (u32)it * 4 + 3, (u32)it, fo STA);
                rot();
                CFLUSH();
                if (it < NOWV) now((u32)it + 1);
                if (MOEV && it >= 6 && (wave & 2) == 0) moe_ahead(h * 2 + par, (it - 6) * 2 + (wave & 1), wg, wu, wd, lbase + L_MISC + 256);
            }
        } else {
            const u32 ob = amap((u32)(h * 1024 + lane * 16)), yp = 4096u + (u32)(wave & 1) * 6144u;
            LBAR();
#pragma unroll
            for (int i = 0; i < 10; ++i) {
                const u32 o = i < 2 ? (u32)i * 1024 : yp + (u32)(i - 2) * 1024;
                dma16_sc1(ws + xsrc(2) + xv + o, __builtin_amdgcn_readfirstlane(lbase + b2 + xp + o));
            }
            DRAIN();
            LBAR();
#pragma unroll 1
            for (int it = 1; it <= NSEG / 2; ++it) {
                rot();
                if ((it & ~4) == 2) sh[18] = wait_init(rs, tag, 1 + (it >> 2));
                wb(W, vl + L_VS + (u32)(~it & 1) * 8192, ob, srd, FAR_BX + (u32)((it * 2 - 2 + par) * NRED + (wave & 3) * 2) * 32768, wp, (u32)it,
                   xv, xsrc(it + 2), lbase + b2 + xp, xsrc(it + 2) + yp, lbase + b2 + xp + yp, ca, (u32)it * 4 - 1, (u32)it - 1, fo STA);
                CFLUSH();
                if (it <= NOWV) { now((u32)it); LBAR(); }
            }
        }
        DRAIN();
        LBAR();
        stx1(rs, wave == 0 && lane_id() == 0 ? fo : OOB, NSEG / 2);
    }
    WSTAMP(2);

    {
    const int lane = lane_id(), tid = wave * 64 + lane;
    if (isqk) moe_part(h * 2 + par, tid, wg, wu, wd, ws, smem);
    WSTAMP(5);
    u32* nextp = (u32*)(smem + L_MISC + 96);
    u32* tctr = (u32*)(ws + OFF_CNT + 512);
    if (tid == 0) nextp[0] = __hip_atomic_fetch_add(tctr, 1u, __ATOMIC_RELAXED, __HIP_MEMORY_SCOPE_AGENT);
    wait_flags(rs, (u32)OFF_IDX + (lane & 1) * 16, true, 1u, dead, errw, 256u, lane);
    __syncthreads();
    int sel_idx[TOPC];
    {
        u32x4 i0 = ldx4(rs, (u32)OFF_IDX), i1 = ldx4(rs, (u32)OFF_IDX + 16);
#pragma unroll
        for (int c = 0; c < 4; ++c) { sel_idx[c] = (int)i0[c] - 1; sel_idx[4 + c] = (int)i1[c] - 1; }
    }
    u32 order;
    {
        bool hit = false;
#pragma unroll
        for (int c = 0; c < TOPC; ++c) hit |= (sel_idx[c] >> 5) == lane;
        const u64 m = __ballot(hit && lane < 40), below = ((u64)1 << lane) - 1;
        const int pos = lane >= 40 ? lane : ((m >> lane) & 1 ? 40 - __popcll(m) + __popcll(m & below) : lane - __popcll(m & below));
        order = (u32)__builtin_amdgcn_ds_permute(pos * 4, lane);
    }
    WSTAMP(3);
    const OutCtx cx = {rs, x, out, errw, to_sgpr(sh[18]), wid, ws};
    bool adone = false;
    u32 rdy = 0;
#pragma unroll 1
    for (int tc = 0;; ++tc) {
        const int task = __builtin_amdgcn_readfirstlane(nextp[tc & 1]);
        if (task >= NTASK) break;
        char* redp = smem + (tc & 1) * 65536;
        if (task < NBIG) {
            WSTAMP(0x100 + task / 10);
            out_task<4, 2>(cx, (int)tab(order, task / 10), (task % 10) * 4, 0, tid, sel_idx, adone, rdy, dead, redp, nextp + ((tc + 1) & 1), tctr);
        } else {
            WSTAMP(0x100 + NSEG - 1);
            out_task<1, 1>(cx, NSEG - 1, (task - NBIG) >> 1, (task - NBIG) & 1, tid, sel_idx, adone, rdy, dead, redp, nextp + ((tc + 1) & 1), tctr);
        }
        WSTAMP(0x300);
        if (task == NTASK - 1) {
            wait_word(rs, (u32)(OFF_CNT + 768), true, 8u, dead, errw, 64u, lane);
            if (tid < 216) stx2(rs, tid < 192 ? (u32)(OFF_INI + tid * 8) : (u32)(OFF_SUM + (tid - 192) * 128), (u32x2){~(u32)tag, ~cal_tag(tag, ~(u32)tag)});
        }
    }
    }
}

constexpr int FUSED_LDS = L_MISC + 1024;
#define ORD_A 0x143210
#define ORD_C 0x101234
#define KERNEL_SYM "fused_kernel"
#define CODE_KB 176
extern "C" __global__ __launch_bounds__(512, 2) void fused_kernel(const u16* __restrict__ x, const u16* __restrict__ mem0, const u16* __restrict__ w_r,
                                                        const u16* __restrict__ Wq, const u16* __restrict__ Wk,
                                                        const u16* __restrict__ Wv, const u16* __restrict__ Wo,
                                                        const u16* __restrict__ wg, const u16* __restrict__ wu,
                                                        const u16* __restrict__ wd, char* __restrict__ ws, u16* __restrict__ out) {
    extern __shared__ __attribute__((aligned(16))) char smem[];
    const int wave = __builtin_amdgcn_readfirstlane(threadIdx.x >> 6);
    u64 gen = 0, tag = 0, old = 0;
    u32 xcc, se;
    asm volatile("s_getreg_b32 %0, hwreg(HW_REG_XCC_ID, 0, 3)" : "=s"(xcc));
    asm volatile("s_getreg_b32 %0, hwreg(HW_REG_HW_ID, 13, 2)" : "=s"(se));
    u32 bid = blockIdx.x;
    asm volatile("" : "+s"(bid));
    const bool first = (int)bid >= 0;
    u32 bq = first ? (bid & ~31u) + se * 8u + xcc : bid & 255u;
    asm volatile("" : "+s"(bq));
    const int b = (int)bq, k = b >> 3, xg = b & 3;
    if (threadIdx.x >= 448 && threadIdx.x < 453) {
        const int lane = threadIdx.x & 63;
        const u32 pg = (bid >> 3 & 31u) + 32u * (u32)lane;
        const char* tp = lane < 4 ? ws + min(pg << 21, (u32)WS_TOTAL - 4u) : (const char*)out + (size_t)(bid >> 3 & 3u) * 655360;
        u32 sv;
        asm volatile("s_mov_b32 %0, m0\n\ts_mov_b32 m0, %2\n\ts_nop 0\n\tglobal_load_lds_dword %1, off\n\ts_mov_b32 m0, %0"
                     : "=&s"(sv) : "v"(tp), "s"((u32)(uintptr_t)(__attribute__((address_space(3))) char*)smem + L_MISC + 256u) : "memory");
    }
    if (wave == 6 && (bid >> 3 & 31u) < CODE_KB / 8) {
        u32 sv;
        asm volatile("s_getpc_b64 s[98:99]\nLc%=:\n\ts_sub_u32 s98, s98, Lc%=-" KERNEL_SYM "\n\ts_subb_u32 s99, s99, 0\n\t"
                     "s_mov_b32 %0, m0\n\ts_mov_b32 m0, %2\n\ts_nop 0\n\tglobal_load_lds_dword %1, s[98:99]\n\ts_mov_b32 m0, %0"
                     : "=&s"(sv) : "v"((bid >> 3 & 31u) * 8192u + (threadIdx.x & 63u) * 128u),
                       "s"((u32)(uintptr_t)(__attribute__((address_space(3))) char*)smem + L_MISC + 768u) : "s98", "s99", "scc", "memory");
    }
    u64* claim = (u64*)(ws + OFF_CLAIM);
    u32* sh = (u32*)(smem + L_MISC);
    auto try_claim = [&] {
        if (wave == 0 && first) {
            asm volatile("s_load_dwordx2 %0, %1, 0x0\n\ts_waitcnt lgkmcnt(0)" : "=s"(gen) : "s"(ws + OFF_GEN) : "memory");
            tag = (gen + 0x9E3779B97F4A7C15ull) * 0xBF58476D1CE4E5B9ull;
            tag ^= tag >> 31;
            tag *= 0x94D049BB133111EBull;
            tag ^= tag >> 29;
            if (lane_id() == 0) {
                old = __hip_atomic_exchange(claim + b * 8, tag, __ATOMIC_RELAXED, __HIP_MEMORY_SCOPE_AGENT);
                sh[1] = (u32)tag; sh[2] = (u32)(tag >> 32); sh[3] = (u32)gen; sh[4] = (u32)(gen >> 32);
            }
        }
    };
    auto settle = [&] {
        const int tid = wave * 64 + lane_id();
        if (tid == 0 && first) {
            u32 vb = (u32)b;
#pragma unroll 1
            for (int i = 0; i < 256 && old == tag; ++i) {
                vb = (vb + 1) & 255u;
                old = __hip_atomic_exchange(claim + vb * 8, tag, __ATOMIC_RELAXED, __HIP_MEMORY_SCOPE_AGENT);
            }
            sh[0] = vb;
        }
        LBAR();
        const u32 bc = __builtin_amdgcn_readfirstlane(sh[0]);
        asm volatile("s_cmp_eq_u32 %0, %1\n\ts_cbranch_scc1 Lgo%=\n\t"
                     "s_or_b32 s2, %0, 0x80000000\n\ts_mov_b64 s[0:1], %2\n\tv_mov_b32 v0, %3\n\ts_waitcnt vmcnt(0) lgkmcnt(0)\n\t"
                     "s_getpc_b64 s[4:5]\nLpc%=:\n\ts_sub_u32 s4, s4, Lpc%=-" KERNEL_SYM "\n\ts_subb_u32 s5, s5, 0\n\ts_setpc_b64 s[4:5]\nLgo%=:"
                     :: "s"(bc), "s"(b), "s"(__builtin_amdgcn_kernarg_segment_ptr()), "v"(tid) : "s2", "scc", "memory");
        tag = (u64)(u32)__builtin_amdgcn_readfirstlane(sh[1]) | ((u64)(u32)__builtin_amdgcn_readfirstlane(sh[2]) << 32);
    };
    if (b & 4) {
        const int wid = k * 4 + xg, h = (wid & 3) * 8 + (wid >> 4), par = (wid >> 2) & 1;
        const bool isqk = ((wid >> 3) & 1) == 0;
        const int lane = lane_id();
        u32x32 W[5];
        u32 wp = 0;
        u32x2 f0;
        u64 tg2 = 0;
        bool got = false;
        auto xtile = [&](int t) {
#pragma unroll
            for (int i = 0; i < 5; ++i)
                dma16_sc1(ws + OFF_XNP + (size_t)(par + 2 * t) * 40960 + (wave * 5 + i) * 1024 + lane * 16,
                          __builtin_amdgcn_readfirstlane((u32)(uintptr_t)(__attribute__((address_space(3))) char*)smem + xbuf(t) + (u32)(wave * 5 + i) * 1024));
        };
        load_weights<ORD_A>([&](int st) {
            if (st == 0) try_claim();
            if (st == LOOKST) {
                asm volatile("global_load_dwordx2 %0, %1, off sc1" : "=&v"(f0) : "v"(ws + OFF_SUM + xcc * 128u) : "memory");
                tg2 = (u64)(u32)__builtin_amdgcn_readfirstlane(sh[1]) | ((u64)(u32)__builtin_amdgcn_readfirstlane(sh[2]) << 32);
            }
            if (st == 3) {
                asm volatile("" : "+v"(f0));
                got = __all(f0[1] == cal_tag(tg2, f0[0]));
                if (got) { xtile(0); xtile(1); }
                wp = to_sgpr(f0[0]);
            }
        }, (isqk ? Wq : Wv) + h * DH_, isqk ? Wk + h * DH_ : Wo + (size_t)h * DH_ * D_, !isqk, smem, wave, lane,
           wave >> 2, 2 * (wave & 3), wave >> 2, 2 * (wave & 3) + 1, W);
        settle();
        const rsrc_t rs = mk_rsrc(ws);
        if (!got) { wp = wait_init(rs, tag, 0); xtile(0); xtile(1); }
        sh[16] = sh[17] = 0u;
        DRAIN();
        LBAR();
        PIN_W(W);
        worker_role(wave, wid, tag, wp, x, W, wg, wu, wd, ws, out, smem);
        __builtin_amdgcn_endpgm();
    }
    const int hl = ((k >> 1) ^ (xg >> 1)) & 1, j = (k >> 2) * 2 + (k & 1);
    if (CFG ? xg < 2 : j < 8) {
        const int h = CFG ? xg * 16 + (j >> 3) * 8 + hl * 4 + ((j >> 1) & 3) : xg * 8 + hl * 4 + (j >> 1);
        u32x32 W[5];
        auto hook = [&](int st) {
            if (st == 0) try_claim();
        };
        if ((j & 1) == 0) {
            load_weights<ORD_C>(hook, Wq + h * DH_, Wk + h * DH_, false, smem, wave, lane_id(), wave < 4, wave & 3, wave < 4, 4 + (wave & 3), W);
            settle();
            PIN_W(W);
            s_role(wave, h, xg, tag, mem0, W, ws, smem);
            __builtin_amdgcn_endpgm();
        }
        if (wave < 4) { load_weights<ORD_C>(hook, Wv + h * DH_, Wo + (size_t)h * DH_ * D_, true, smem, wave, lane_id(), 0, wave, 0, 4 + wave, W); PIN_W(W); }
        else { load_weights<ORD_C | (1 << 21)>(hook, Wv + h * DH_, Wo + (size_t)h * DH_ * D_, true, smem, wave, lane_id(), 1, 2 * (wave & 3), 1, 2 * (wave & 3) + 1, W); PIN_W(W); }
        settle();
        PIN_W(W);
        v_role(wave, h, xg, tag, mem0, W, ws, smem);
        __builtin_amdgcn_endpgm();
    }
    constexpr int NR = CFG ? 10 : 5, NS = CFG ? 6 : 3, NP = CFG ? 4 : 2;
    const int jr = CFG ? j : j - 8, hi = (CFG ? xg - 2 : xg) * 2 + hl;
    try_claim();
    init_role([&] { settle(); return tag; }, wave, jr < NR ? hi * NR + jr : jr == NR ? 40 + hi : 48 + hi * NP + jr - (NR + NS - NP), xcc, ((se ^ xcc) >> 1) & 1u, x, w_r, Wq, Wk, Wv, ws, smem);
    gen = (u64)sh[3] | ((u64)sh[4] << 32);
    tag = (u64)(u32)__builtin_amdgcn_readfirstlane(sh[1]) | ((u64)(u32)__builtin_amdgcn_readfirstlane(sh[2]) << 32);
    const int wi = hl * NS + jr - NR;
    const bool aux = wi == 2 * NS - 1;
    if (jr < NR) { if (wave < 6) r_role(wave, tile_of(xg, hl, jr), xg, wait_init(mk_rsrc(ws), tag, 1), ws, smem); }
    else if (!aux) {
        if (wave >= 4) { if (wi == 0 && xg == 0 && wave == 7) topk_role(tag, ws); return; }
        const u32 wp = wait_init(mk_rsrc(ws), tag, 1);
        LBAR();
        if (wave == 0 && wi == 0 && lane_id() == 0) __hip_atomic_fetch_add((u32*)(ws + OFF_CNT + 768), 1u, __ATOMIC_RELAXED, __HIP_MEMORY_SCOPE_AGENT);
        if (wave == 2 && wi) fill_role(wp, xg * 4 + wi - 1, ws);
        if (wave == 3 && xg == 3 && wi == 4) relay_role(wp, xg, ws);
        if (wave == 1 || (wave == 2 && !wi)) warm_role(wp, xg, wi - wave, ws, smem);
    }
    else if (wave == 1) {
        const u32 wp = wait_init(mk_rsrc(ws), tag, 1);
        if (lane_id() == 0) __hip_atomic_fetch_add((u32*)(ws + OFF_CNT + 768), 1u, __ATOMIC_RELAXED, __HIP_MEMORY_SCOPE_AGENT);
        lead_role(wp, xg, ws, smem);
    }
    else if (aux && xg == 2 && wave == 0) {
        const rsrc_t rs = mk_rsrc(ws);
        const int lane = lane_id();
#pragma unroll 1
        for (u32 spins = 0; spins < SPIN_CAP; ++spins) {
            CFENCE();
            bool ok = true;
#pragma unroll
            for (int q = 0; q < 4; ++q) {
                u32x2 f = ldx2(rs, (u32)(OFF_CLAIM + (q * 64 + lane) * 64));
                ok = ok && f[0] == (u32)tag && f[1] == (u32)(tag >> 32);
            }
            if (__all(ok)) break;
            __builtin_amdgcn_s_sleep(32);
        }
        if (lane == 0) __hip_atomic_store((u64*)(ws + OFF_GEN), gen + 1ull, __ATOMIC_RELAXED, __HIP_MEMORY_SCOPE_AGENT);
#pragma unroll
        for (int q = 0; q < 4; ++q) stx2(rs, (u32)(OFF_CLAIM + (q * 64 + lane) * 64), (u32x2){~(u32)tag, 0u});
    }
}

extern "C" void kernel_launch(void* const* d_in, const int* in_sizes, int n_in, void* d_out, int out_size,
                              void* d_ws, size_t ws_size, hipStream_t stream) {
    const u16* x = (const u16*)d_in[0];
    const u16* mem0 = (const u16*)d_in[1];
    const u16* Wq = (const u16*)d_in[2];
    const u16* Wk = (const u16*)d_in[3];
    const u16* Wv = (const u16*)d_in[4];
    const u16* Wo = (const u16*)d_in[5];
    const u16* w_r = (const u16*)d_in[8];
    const u16* w_gate = (const u16*)d_in[9];
    const u16* w_up = (const u16*)d_in[10];
    const u16* w_down = (const u16*)d_in[11];
    char* ws = (char*)d_ws;
    u16* out = (u16*)d_out;

    hipFuncSetAttribute((const void*)fused_kernel, hipFuncAttributeMaxDynamicSharedMemorySize, FUSED_LDS);
    fused_kernel<<<256, 512, FUSED_LDS, stream>>>(x, mem0, w_r, Wq, Wk, Wv, Wo, w_gate, w_up, w_down, ws, out);
}
```

```cpp
#define CFG 0
#include <hip/hip_runtime.h>
#include <hip/hip_bf16.h>
#include <stdint.h>

constexpr int S_ = 2048, D_ = 640, H_ = 32, DH_ = 128, NSEG = 64, SEGL = 32, FF_ = 1728, TOPC = 8;
constexpr int HD_ = H_ * DH_;
constexpr int NRED = 40;

typedef unsigned short u16;
typedef unsigned int u32;
typedef unsigned long long u64;
typedef __attribute__((ext_vector_type(8))) __bf16 bf16x8;
typedef __attribute__((ext_vector_type(16))) __bf16 bf16x16;
typedef __attribute__((ext_vector_type(4))) float f32x4;
typedef __attribute__((ext_vector_type(4))) unsigned int u32x4;
typedef __attribute__((ext_vector_type(8))) unsigned int u32x8;
typedef __attribute__((ext_vector_type(2))) unsigned int u32x2;

__device__ __forceinline__ float bf2f(u16 h) { return __uint_as_float(((u32)h) << 16); }
typedef __bf16 bf16x2_t __attribute__((ext_vector_type(2)));
typedef float f32x2_t __attribute__((ext_vector_type(2)));
__device__ __forceinline__ u16 f2bf(float f) {
    __bf16 h = (__bf16)f;
    return __builtin_bit_cast(u16, h);
}
__device__ __forceinline__ float bfr(float f) { return (float)(__bf16)f; }
__device__ __forceinline__ u32 pack2(float a, float b) {
    f32x2_t f = {a, b};
    bf16x2_t h = __builtin_convertvector(f, bf16x2_t);
    return __builtin_bit_cast(u32, h);
}
__device__ __forceinline__ bf16x8 as_bf(u32x4 v) { return __builtin_bit_cast(bf16x8, v); }
__device__ __forceinline__ f32x4 mfma16(u32x4 a, u32x4 b, f32x4 c) {
    return __builtin_amdgcn_mfma_f32_16x16x32_bf16(as_bf(a), as_bf(b), c, 0, 0, 0);
}
__device__ __forceinline__ f32x4 smf(u32x4 a, u32x4 b0, u32x4 b1, f32x4 c, int idx) {
    u32x8 b = {b0[0], b0[1], b0[2], b0[3], b1[0], b1[1], b1[2], b1[3]};
    return __builtin_amdgcn_smfmac_f32_16x16x64_bf16(as_bf(a), __builtin_bit_cast(bf16x16, b), c, idx, 0, 0);
}
__device__ __forceinline__ int dmap(int p) {
    int q = p & 15;
    return (p & ~15) | (q & 1) | ((q & 2) << 2) | ((q & 4) >> 1) | ((q & 8) >> 1);
}

constexpr size_t al256(size_t x) { return (x + 255) & ~(size_t)255; }
constexpr size_t OFF_XN = 0;
constexpr size_t OFF_XNP = OFF_XN + al256((size_t)S_ * D_ * 2);
constexpr size_t OFF_R = OFF_XNP + al256((size_t)S_ * D_ * 2);
constexpr size_t OFF_PX = OFF_R + al256((size_t)S_ * 4);
constexpr size_t OFF_PXM = OFF_PX + al256((size_t)NSEG * H_ * 2048);
constexpr size_t OFF_HB = OFF_PXM + al256((size_t)NSEG * 8 * 2048);
constexpr size_t OFF_UPD = OFF_HB + al256((size_t)8 * FF_ * 2);
constexpr size_t OFF_GEN = OFF_UPD + al256((size_t)8 * D_ * 2);
constexpr size_t OFF_CLAIM = OFF_GEN + 256;
constexpr size_t OFF_INI = OFF_CLAIM + 16384;
constexpr size_t OFF_SUM = OFF_INI + 2048;
constexpr size_t OFF_ERR = OFF_SUM + 3072;
constexpr size_t OFF_FA = OFF_ERR + 256;
constexpr size_t OFF_FB = OFF_FA + 4096;
constexpr size_t OFF_FP = OFF_FB + 4096;
constexpr size_t OFF_FZ = OFF_FP + 1024;
constexpr size_t OFF_RDY = OFF_FZ + 2048;
constexpr size_t OFF_IDX = OFF_RDY + 1024;
constexpr size_t OFF_CNT = OFF_IDX + 256;
constexpr size_t ZERO_BEGIN = OFF_ERR, ZERO_END = OFF_CNT + 1024;
constexpr size_t OFF_WIN = (size_t)12 << 20;
constexpr size_t WS_TOTAL = OFF_WIN + ((size_t)188 << 20);
static_assert(ZERO_END <= OFF_WIN && WS_TOTAL <= ((size_t)256 << 20), "ws");

constexpr u32 SLOT0 = 52u << 20;
constexpr u32 SL_P = 4 * 32768, SL_TS = 2 * 32768 + 1024;
constexpr u32 FAR_BX = 1u << 20;
constexpr u32 NEAR_QX = 59u << 20;
constexpr u32 NEAR_KX = NEAR_QX + NSEG * H_ * 8192;
static_assert(2 * ((size_t)NEAR_KX + (size_t)NSEG * H_ * 8192) + ((size_t)2 << 20) <= WS_TOTAL - OFF_WIN && SLOT0 + (64u << 20) <= 2 * NEAR_QX
              && 2 * ((size_t)FAR_BX + (size_t)NSEG * NRED * 32768) + ((size_t)2 << 20) <= WS_TOTAL - OFF_WIN, "far");
__device__ __forceinline__ u32 slot_off(u32 wp, int s) {
    u32 r, t;
    asm("s_lshr_b32 %0, %3, 1\n\ts_xor_b32 %0, %0, %2\n\ts_and_b32 %0, %0, 1\n\ts_lshl_b32 %0, %0, 13\n\ts_lshl_b32 %1, %3, 20\n\t"
        "s_add_u32 %0, %0, %1\n\ts_andn2_b32 %1, %2, 15\n\ts_add_u32 %0, %0, %1\n\ts_add_u32 %0, %0, %4" : "=&s"(r), "=&s"(t) : "s"(wp), "s"(s), "n"(SLOT0) : "scc");
    return r;
}
__device__ __forceinline__ u32 slot_z(u32 wp, u32 s) { return (wp & ~15u) + SLOT0 + (s << 20) + (((wp ^ (s >> 1)) & 1u) << 13); }
__device__ __forceinline__ u32 slot_v(u32 wp, u32 s) { return slot_z(wp, s) + ((((wp >> 3) ^ (s >> 5)) & 1u) << 18); }
__device__ __forceinline__ u32 far_v(u32 wp, u32 L) { return (wp & ~15u) + 2 * L + ((~(wp ^ (L >> 20)) & 1u) << 13); }
__device__ __forceinline__ u32 tab(u32 t, int s) { return __builtin_amdgcn_readlane(t, s); }
__device__ __forceinline__ u32 copy_off(u32 c, u32 s) { return ((c & 1u) << 14) | ((((c >> 1) ^ s) & 1u) << 12); }
__device__ __forceinline__ u32 z_off(u32 wp, u32 ct) {
    const u32 zb = ct >> 1, pg = zb < 16 ? zb + 16 : 6 + (zb & 1u) + (zb & 2u) * 4;
    return pg * 32768 + (((ct ^ (wp >> 1) ^ (pg >> 4)) & 1u) << 14);
}
constexpr int NCP = CFG ? 2 : 4;
__device__ __forceinline__ u32 put_off(u32 wp, u32 x0, u32 s) { return CFG ? copy_off(x0 ^ (wp >> 1), s) & 4096u : 0u; }
__device__ __forceinline__ u32 cpy(u32 c) { return CFG ? (c & 1u) * 16384u : (c & 1u) * 4096u + (c >> 1) * 16384u; }
__device__ __forceinline__ u32 far_off(u32 wp, u32 L) {
    u32 r, t;
    asm("s_lshr_b32 %0, %3, 20\n\ts_xnor_b32 %0, %0, %2\n\ts_and_b32 %0, %0, 1\n\ts_lshl_b32 %0, %0, 13\n\ts_lshl_b32 %1, %3, 1\n\t"
        "s_add_u32 %0, %0, %1\n\ts_andn2_b32 %1, %2, 15\n\ts_add_u32 %0, %0, %1" : "=&s"(r), "=&s"(t) : "s"(wp), "s"(L) : "scc");
    return r;
}
constexpr u32 SENT = 0xffbfffbfu;
constexpr u32 MARK = 0xffbeffbeu;
constexpr u32 OOB = 0x80000000u;
__device__ __forceinline__ u32 amap(u32 L) { return ((L >> 13) << 14) | (L & 8191u); }
__device__ __forceinline__ u32 win_off(const char* ws) {
    u64 a = (u64)(uintptr_t)ws;
    u64 w = (a + OFF_WIN + ((u64)2 << 20) - 1) & ~(((u64)2 << 20) - 1);
    return (u32)(w - a);
}

constexpr int AUX_SC1 = 16;
typedef __amdgpu_buffer_rsrc_t rsrc_t;
__device__ __forceinline__ rsrc_t mk_rsrc(const void* p) {
    return __builtin_amdgcn_make_buffer_rsrc((void*)p, 0, 0x7fffffff, 0x00020000);
}
__device__ __forceinline__ u32x4 ldx4(rsrc_t r, u32 off, u32 so = 0) { return __builtin_amdgcn_raw_buffer_load_b128(r, off, so, AUX_SC1); }
__device__ __forceinline__ u32x2 ldx2(rsrc_t r, u32 off, u32 so = 0) { return __builtin_amdgcn_raw_buffer_load_b64(r, off, so, AUX_SC1); }
__device__ __forceinline__ u32 ldx1(rsrc_t r, u32 off, u32 so = 0) { return __builtin_amdgcn_raw_buffer_load_b32(r, off, so, AUX_SC1); }
__device__ __forceinline__ void stx4(rsrc_t r, u32 off, u32x4 v, u32 so = 0) { __builtin_amdgcn_raw_buffer_store_b128(v, r, off + so, 0, AUX_SC1); }
__device__ __forceinline__ void stx2(rsrc_t r, u32 off, u32x2 v, u32 so = 0) { __builtin_amdgcn_raw_buffer_store_b64(v, r, off, so, AUX_SC1); }
__device__ __forceinline__ void stx1(rsrc_t r, u32 off, u32 v, u32 so = 0) { __builtin_amdgcn_raw_buffer_store_b32(v, r, off, so, AUX_SC1); }
#define DRAIN() asm volatile("s_waitcnt vmcnt(0)" ::: "memory")
#define CFENCE() asm volatile("" ::: "memory")
#define LBAR() asm volatile("s_waitcnt lgkmcnt(0)\n\ts_barrier" ::: "memory")

constexpr u32 L_MISC = 147456;
constexpr u32 SPIN_CAP = 1u << 18;
__device__ __forceinline__ int lane_id() {
    int l;
    asm volatile("v_mbcnt_lo_u32_b32 %0, -1, 0\n\tv_mbcnt_hi_u32_b32 %0, -1, %0" : "=v"(l));
    __builtin_assume(l >= 0 && l < 64);
    return l;
}

__device__ __forceinline__ float xmax16(float x) {
    u32 a = __float_as_uint(x);
    auto r = __builtin_amdgcn_permlane16_swap(a, a, false, false);
    return fmaxf(__uint_as_float(r[0]), __uint_as_float(r[1]));
}
__device__ __forceinline__ float xmax32(float x) {
    u32 a = __float_as_uint(x);
    auto r = __builtin_amdgcn_permlane32_swap(a, a, false, false);
    return fmaxf(__uint_as_float(r[0]), __uint_as_float(r[1]));
}
__device__ __forceinline__ float xadd16(float x) {
    u32 a = __float_as_uint(x);
    auto r = __builtin_amdgcn_permlane16_swap(a, a, false, false);
    return __uint_as_float(r[0]) + __uint_as_float(r[1]);
}
__device__ __forceinline__ float xadd32(float x) {
    u32 a = __float_as_uint(x);
    auto r = __builtin_amdgcn_permlane32_swap(a, a, false, false);
    return __uint_as_float(r[0]) + __uint_as_float(r[1]);
}
__device__ __forceinline__ u32 umax3(u32 a, u32 b, u32 c) { return max(max(a, b), c); }

__device__ __forceinline__ void wait_flags(rsrc_t rs, u32 off, bool act, u32 target, bool& dead, u32* errw, u32 code, int lane) {
    u32 spins = 0;
#pragma unroll 1
    while (!dead) {
        CFENCE();
        u32x4 f = ldx4(rs, off);
        bool ok = !act || ((f[0] >= target) & (f[1] >= target) & (f[2] >= target) & (f[3] >= target));
        if (__all(ok)) break;
        __builtin_amdgcn_s_sleep(2);
        if (++spins > SPIN_CAP) { if (lane == 0) atomicOr(errw, code); dead = true; }
        if ((spins & 63) == 0 && ldx1(rs, (u32)OFF_ERR) != 0u) dead = true;
    }
    CFENCE();
}
__device__ __forceinline__ void wait_word(rsrc_t rs, u32 off, bool act, u32 target, bool& dead, u32* errw, u32 code, int lane) {
    u32 spins = 0;
#pragma unroll 1
    while (!dead) {
        CFENCE();
        u32 f = ldx1(rs, off);
        if (__all(!act || f >= target)) break;
        __builtin_amdgcn_s_sleep(4);
        if (++spins > SPIN_CAP) { if (lane == 0) atomicOr(errw, code); dead = true; }
        if ((spins & 63) == 0 && ldx1(rs, (u32)OFF_ERR) != 0u) dead = true;
    }
    CFENCE();
}

__device__ __forceinline__ u32 cal_tag(u64 tag, u32 v) { return ((u32)tag ^ (u32)(tag >> 32)) + v * 0x9E3779B1u; }
__device__ __forceinline__ u32 to_sgpr(u32 x) {
    u32 r;
    asm volatile("s_nop 0\n\tv_readfirstlane_b32 %0, %1\n\ts_nop 4" : "=s"(r) : "v"(x));
    return r;
}
__device__ __forceinline__ u32 wp_of(const char* ws, u32 v) {
    v += __builtin_amdgcn_update_dpp(0u, v, 0xb1, 0xf, 0xf, false);
    v += __builtin_amdgcn_update_dpp(0u, v, 0x4e, 0xf, 0xf, false);
    v += __builtin_amdgcn_update_dpp(0u, v, 0x124, 0xf, 0xf, false);
    v += __builtin_amdgcn_update_dpp(0u, v, 0x128, 0xf, 0xf, false);
    const u32 r = __builtin_amdgcn_readlane(v, 48) + 0x0f0f0f0fu;
    return to_sgpr(win_off(ws) | ((r >> 7) & 1u) | ((r >> 14) & 2u) | ((r >> 21) & 4u) | ((r >> 28) & 8u));
}
__device__ __forceinline__ u32 sum_off(int level) {
    u32 xcc;
    asm volatile("s_getreg_b32 %0, hwreg(HW_REG_XCC_ID, 0, 3)" : "=s"(xcc));
    return (u32)(OFF_SUM + level * 1024) + xcc * 128u;
}
__device__ __forceinline__ u32 wait_init(rsrc_t rs, u64 tag, int level) {
    const u32 so = sum_off(level);
    u32x2 c = {0u, 0u};
#pragma unroll 1
    for (u32 spins = 0; spins < SPIN_CAP; ++spins) {
        CFENCE();
        c = ldx2(rs, 0u, so);
        if (__all(c[1] == cal_tag(tag, c[0]))) break;
        __builtin_amdgcn_s_sleep(6);
    }
    CFENCE();
    return to_sgpr(min(c[0], (u32)OFF_WIN + 0x20000fu));
}

#define STAMPS() do { } while (0)
#define STAMP(k) do { } while (0)
#define RSTAMP(k) do { } while (0)
#define STAMP2(k, r) do { } while (0)
#define FLUSH() do { } while (0)
#define LOGS() do { } while (0)
#define LOG() do { } while (0)
#define LOGOUT(cu) do { } while (0)
#define CFLUSH() do { } while (0)
#define XSTAMP(w, sl) do { } while (0)
#define WSTAMP(id) do { } while (0)
#define CSTAMP(id) do { } while (0)
#define NPROBE 2
constexpr u32 INI_ZERO_CHUNKS = (u32)((ZERO_END - ZERO_BEGIN) / 16);
static_assert(INI_ZERO_CHUNKS <= 8 * 256, "zero");

__device__ __forceinline__ float rsum16(float x) {
    x += __uint_as_float(__builtin_amdgcn_update_dpp(0u, __float_as_uint(x), 0xb1, 0xf, 0xf, false));
    x += __uint_as_float(__builtin_amdgcn_update_dpp(0u, __float_as_uint(x), 0x4e, 0xf, 0xf, false));
    x += __uint_as_float(__builtin_amdgcn_update_dpp(0u, __float_as_uint(x), 0x124, 0xf, 0xf, false));
    x += __uint_as_float(__builtin_amdgcn_update_dpp(0u, __float_as_uint(x), 0x128, 0xf, 0xf, false));
    return x;
}

__device__ __forceinline__ void ln_regs(u32x4 (&xv)[5]) {
    float sum = 0.f;
#pragma unroll
    for (int i = 0; i < 5; ++i)
#pragma unroll
        for (int j = 0; j < 4; ++j) sum += __uint_as_float(xv[i][j] << 16) + __uint_as_float(xv[i][j] & 0xffff0000u);
    const float mean = rsum16(sum) * (1.0f / D_);
    float vs = 0.f;
#pragma unroll
    for (int i = 0; i < 5; ++i)
#pragma unroll
        for (int j = 0; j < 4; ++j) {
            float d0 = __uint_as_float(xv[i][j] << 16) - mean, d1 = __uint_as_float(xv[i][j] & 0xffff0000u) - mean;
            vs += d0 * d0 + d1 * d1;
        }
    const float var = bfr(rsum16(vs) * (1.0f / D_));
    const float m_bf = bfr(mean);
    const float rden = 1.0f / bfr(sqrtf(bfr(var + 1e-05f)));
#pragma unroll
    for (int i = 0; i < 5; ++i)
#pragma unroll
        for (int j = 0; j < 4; ++j)
            xv[i][j] = pack2(bfr(__uint_as_float(xv[i][j] << 16) - m_bf) * rden, bfr(__uint_as_float(xv[i][j] & 0xffff0000u) - m_bf) * rden);
}
__device__ __forceinline__ u32x4 ln_perm(const u32x4 o, const int n) {
    const u32 s0 = (n & 1) ? o[0] : o[2], s1 = (n & 1) ? o[1] : o[3];
    const u32 t0 = __builtin_amdgcn_update_dpp(0u, s0, 0xb1, 0xf, 0xf, false), t1 = __builtin_amdgcn_update_dpp(0u, s1, 0xb1, 0xf, 0xf, false);
    return (n & 1) ? (u32x4){t0, o[2], t1, o[3]} : (u32x4){o[0], t0, o[1], t1};
}
__device__ __forceinline__ u32 img_off(const int row, const int n, const int i) {
    return (u32)((row >> 4) * 20480 + (4 * i + (n >> 2)) * 1024 + (n & 3) * 256 + (row & 15) * 16);
}
__device__ __forceinline__ void ln_load(const int row0, const int lane, const u16* __restrict__ x, const u16* __restrict__ w_r, u32x4 (&xv)[5], u32x4 (&wv)[5]) {
    const int n = lane & 15, row = row0 + (lane >> 4);
#pragma unroll
    for (int i = 0; i < 5; ++i) xv[i] = *(const u32x4*)(x + (size_t)row * D_ + (n + 16 * i) * 8);
#pragma unroll
    for (int i = 0; i < 5; ++i) wv[i] = *(const u32x4*)(w_r + (n + 16 * i) * 8);
}
__device__ __forceinline__ void ln_store(const int row0, const int lane, const u32x4 (&xv)[5], const u32x4 (&wv)[5], rsrc_t rs, const bool img = true) {
    const int n = lane & 15, row = row0 + (lane >> 4);
#pragma unroll
    for (int i = 0; i < 5; ++i) if (img) stx4(rs, (u32)OFF_XNP + img_off(row, n, i), ln_perm(xv[i], n));
    CFENCE();
    float dot = 0.f;
#pragma unroll
    for (int i = 0; i < 5; ++i) {
        stx4(rs, (u32)OFF_XN + (u32)(row * D_ + (n + 16 * i) * 8) * 2, xv[i]);
#pragma unroll
        for (int j = 0; j < 4; ++j)
            dot += __uint_as_float(xv[i][j] << 16) * __uint_as_float(wv[i][j] << 16) + __uint_as_float(xv[i][j] & 0xffff0000u) * __uint_as_float(wv[i][j] & 0xffff0000u);
    }
    dot = rsum16(dot);
    stx1(rs, n == 0 ? (u32)(OFF_R + row * 4) : OOB, __float_as_uint(bfr(dot)));
}
__device__ __forceinline__ u32x4 mk_srd(const char* ws) {
    const u64 a = (u64)(uintptr_t)ws;
    return (u32x4){(u32)a, (u32)(a >> 32), 0x7fffffffu, 0x00020000u};
}

template <typename F>
__device__ __forceinline__ u32 probe(F&& early, const int pi, const u32 xcc, const u32 hl, char* __restrict__ ws) {
    const u32x4 srd = mk_srd(ws);
    const u32 base = win_off(ws) + SLOT0 + (8u << 20) + 6u * 32768u + (u32)pi * 128u;
    const int lane = lane_id();
    const u32 zero = 0;
    asm volatile("buffer_atomic_or %0, %1, %2, %3 offen sc1\n\ts_waitcnt vmcnt(0)" :: "v"(zero), "v"(lane < 16 ? (u32)lane * 2048u : OOB), "s"(srd), "s"(base) : "memory");
    const u32 one = lane == 0 ? 0u : OOB;
    auto lat = [&](u32 q) {
        u64 t0, t1;
        asm volatile("s_memtime %0\n\tbuffer_atomic_or %2, %3, %4, %5 offen sc1\n\ts_waitcnt vmcnt(0)\n\ts_memtime %1\n\ts_waitcnt lgkmcnt(0)"
                     : "=&s"(t0), "=&s"(t1) : "v"(zero), "v"(one), "s"(srd), "s"(base + q * 2048u) : "memory");
        return (u32)t1 - (u32)t0;
    };
    const u32 q0 = (u32)pi & 11u, a0 = lat(q0), a1 = lat(q0 | 4u), a2 = lat(q0 ^ 8u), a3 = lat((q0 ^ 8u) | 4u), a4 = lat(q0 ^ 2u), a5 = lat((q0 ^ 2u) | 4u);
    const int d13 = (int)min(min(a0, a2), a4) - (int)min(min(a1, a3), a5), v13 = min(max(d13 >> 4, -7), 7);
    const u32 b13 = d13 > 0 ? 4u : 0u;
    const u32 flip = (xcc >> 2) | ((xcc & 3u) << 1) | (hl << 3);
    u32 votes = (u32)(7 + ((flip & 1u) ? -v13 : v13));
    early(votes);
    u32 best = ~0u;
#pragma unroll 1
    for (u32 i = 0; i < 8 * NPROBE; ++i) {
        const u32 c = (i + (u32)pi) & 7u, t = lat((c & 3u) | b13 | (c & 4u) * 2u);
        if ((u32)lane == c) best = min(best, t);
    }
#pragma unroll
    for (u32 i = 1; i < 4; ++i) {
        const u32 bit = 8u >> i;
        const int d = (int)best - __shfl_xor((int)best, (int)bit);
        int v = 0;
#pragma unroll
        for (u32 c = 0; c < 8; ++c) if (!(c & bit)) v += min(max(__builtin_amdgcn_readlane(d, c), -28), 28);
        v = (v + 8) >> 4;
        if ((flip >> i) & 1u) v = -v;
        votes |= (u32)(xcc >= 4 ? 7 : 7 + v) << (8 * i);
    }
    return votes;
}

typedef __attribute__((ext_vector_type(32))) unsigned int u32x32;
__device__ __forceinline__ u32x4 wfrag(const u32x32 (&W)[5], const int i) {
    return (u32x4){W[i >> 3][(i & 7) * 4], W[i >> 3][(i & 7) * 4 + 1], W[i >> 3][(i & 7) * 4 + 2], W[i >> 3][(i & 7) * 4 + 3]};
}
#define PIN_W(W) asm volatile("" : "+{v[16:47]}"(W[0]), "+{v[48:79]}"(W[1]), "+{v[80:111]}"(W[2]), "+{v[112:143]}"(W[3]), "+{v[144:175]}"(W[4]))
typedef __attribute__((ext_vector_type(4))) short s16x4;
typedef __attribute__((address_space(3))) s16x4* lds_s16x4p;
__device__ __forceinline__ u32x2 trd(u32 ldsaddr) {
    return __builtin_bit_cast(u32x2, __builtin_amdgcn_ds_read_tr16_b64_v4i16((lds_s16x4p)ldsaddr));
}
template <bool NT>
__device__ __forceinline__ void dma16(const void* g, u32 lds_base) {
    u32 sv;
    if (NT)
        asm volatile("s_mov_b32 %0, m0\n\ts_mov_b32 m0, %2\n\ts_nop 0\n\tglobal_load_lds_dwordx4 %1, off nt\n\ts_mov_b32 m0, %0"
                     : "=&s"(sv) : "v"(g), "s"(lds_base) : "memory");
    else
        asm volatile("s_mov_b32 %0, m0\n\ts_mov_b32 m0, %2\n\ts_nop 0\n\tglobal_load_lds_dwordx4 %1, off\n\ts_mov_b32 m0, %0"
                     : "=&s"(sv) : "v"(g), "s"(lds_base) : "memory");
}
__device__ __forceinline__ void dma16_sc1(const void* g, u32 lds_base) {
    u32 sv;
    asm volatile("s_mov_b32 %0, m0\n\ts_mov_b32 m0, %2\n\ts_nop 0\n\tglobal_load_lds_dwordx4 %1, off sc1\n\ts_mov_b32 m0, %0"
                 : "=&s"(sv) : "v"(g), "s"(lds_base) : "memory");
}
template <int ORD, typename F>
__device__ __forceinline__ void load_weights(F&& after, const u16* __restrict__ p0, const u16* __restrict__ p1, const bool wo1, char* smem,
                                             const int wave, const int lane, const int sa, const int ta, const int sb, const int tb,
                                             u32x32 (&W)[5]) {
    const u32 lbase = (u32)(uintptr_t)(__attribute__((address_space(3))) char*)smem;
    const int strm = wave >> 2;
    const bool wo = wo1 && strm == 1;
    const u16* src = strm ? p1 : p0;
    const u32 sstep = wo ? 128u : 128u * HD_;
    u32 so[8];
#pragma unroll
    for (int i = 0; i < 8; ++i) {
        const int j = (wave & 3) * 8 + i, r = 4 * j + (lane >> 4);
        const int f = (lane >> 4) | (((j >> 1) & 1) << 2);
        const int ch = (lane & 15) ^ (f << 1);
        const int e = (r & ~31) | (((r >> 2) & 1) << 4) | (((r >> 3) & 3) << 2) | (r & 3);
        so[i] = wo ? (u32)(e * D_ + ch * 8) : (u32)(dmap(r) * HD_ + ch * 8);
    }
    auto issue = [&](int st) {
        const u32 dst = __builtin_amdgcn_readfirstlane(lbase + (u32)((st & 1) * 65536 + strm * 32768 + (wave & 3) * 8192));
#pragma unroll
        for (int i = 0; i < 8; ++i) dma16<((ORD >> 20) & 1) != 0>(src + so[i] + (size_t)((ORD >> (4 * st)) & 15) * sstep, dst + i * 1024);
    };
    const int g = lane >> 4, q = (lane >> 2) & 3, p = lane & 3;
    const int f = q | ((g & 1) << 2);
    const u32 ra = lbase + (u32)(sa * 32768 + (8 * g + q) * 256 + (((ta ^ f) << 1) | (p >> 1)) * 16 + (p & 1) * 8);
    const u32 rb = lbase + (u32)(sb * 32768 + (8 * g + q) * 256 + (((tb ^ f) << 1) | (p >> 1)) * 16 + (p & 1) * 8);
    issue(0);
    issue(1);
#pragma unroll
    for (int st = 0; st < 5; ++st) {
        if (st < 4) asm volatile("s_waitcnt vmcnt(8)" ::: "memory"); else asm volatile("s_waitcnt vmcnt(0)" ::: "memory");
        LBAR();
#pragma unroll
        for (int k = 0; k < 4; ++k) {
            const u32 o = (u32)((st & 1) * 65536 + k * 8192);
            u32x2 a0 = trd(ra + o), a1 = trd(ra + o + 1024), b0 = trd(rb + o), b1 = trd(rb + o + 1024);
            const int ia = (((ORD >> (4 * st)) & 15) * 4 + k) * 4, ib = 80 + ia;
            if ((ORD >> 21) & 1) {
                u32x32& w = W[(ORD >> (4 * st)) & 15];
                w[k * 8] = a0[0]; w[k * 8 + 1] = b0[0]; w[k * 8 + 2] = a0[1]; w[k * 8 + 3] = b0[1];
                w[k * 8 + 4] = a1[0]; w[k * 8 + 5] = b1[0]; w[k * 8 + 6] = a1[1]; w[k * 8 + 7] = b1[1];
            } else {
                W[ia >> 5][ia & 31] = a0[0]; W[ia >> 5][(ia & 31) + 1] = a0[1]; W[ia >> 5][(ia & 31) + 2] = a1[0]; W[ia >> 5][(ia & 31) + 3] = a1[1];
                W[ib >> 5][ib & 31] = b0[0]; W[ib >> 5][(ib & 31) + 1] = b0[1]; W[ib >> 5][(ib & 31) + 2] = b1[0]; W[ib >> 5][(ib & 31) + 3] = b1[1];
            }
        }
        LBAR();
        after(st + 16);
        if (st + 2 < 5) issue(st + 2);
        after(st);
    }
}

__device__ __forceinline__ void early_lines(const int t0, const u64 wq, const u64 wk, const u64 wv, const u64 wo, const u32 lds) {
#pragma unroll
    for (int q = 0; q < 3; ++q) {
        const int t = t0 + q * 320, m = t / 180, r = (t - m * 180) * 64 + lane_id(), row = r / 18, c = r - row * 18;
        const u64 w = m == 0 ? wo : m == 1 ? wv : m == 2 ? wk : wq;
        const u32 blk = ((c < 16 ? (u32)(c >> 1) * 4u + 1u : 31u) - ((u32)w >> 8)) & 31u;
        u32 sv;
        if (t < 720)
            asm volatile("s_mov_b32 %0, m0\n\ts_mov_b32 m0, %3\n\ts_nop 0\n\tglobal_load_lds_dword %1, %2\n\ts_mov_b32 m0, %0"
                         : "=&s"(sv) : "v"((u32)row * 8192u + blk * 256u + (u32)(c & 1) * 128u), "s"(w), "s"(lds) : "memory");
    }
}
template <typename F>
__device__ __forceinline__ void init_role(F&& settle, const int wave, const int ii, const u32 xcc, const u32 hl, const u16* __restrict__ x, const u16* __restrict__ w_r,
                                          const u16* Wq, const u16* Wk, const u16* Wv, const u16* Wo, char* __restrict__ ws, char* smem) {
    const int lane = lane_id(), tid = wave * 64 + lane;
    const rsrc_t rs = mk_rsrc(ws);
    u32* cnt = (u32*)(smem + L_MISC + 128);
    if (tid < 3) cnt[tid] = 0u;
    u64 tag;
    auto arrive = [&](int level) {
        if (lane == 0 && __hip_atomic_fetch_add(cnt + level, 1u, __ATOMIC_RELAXED, __HIP_MEMORY_SCOPE_WORKGROUP) == 7u)
            stx2(rs, (u32)(OFF_INI + (level * 64 + ii) * 8), (u32x2){0u, cal_tag(tag, 0u)});
    };
    if (ii >= 48) {
        if (wave == 1 && ii < 63) {
            const u32 votes = probe([&](u32 v) {
                tag = settle();
                stx2(rs, lane == 0 ? (u32)(OFF_INI + ii * 8) : OOB, (u32x2){v, cal_tag(tag, v)});
                DRAIN();
                XSTAMP(192 + ii, 220);
            }, ii - 48, xcc, hl, ws);
            XSTAMP(192 + ii, 223);
            stx2(rs, lane == 0 ? (u32)(OFF_INI + (64 + ii) * 8) : OOB, (u32x2){votes, cal_tag(tag, votes)});
        } else tag = settle();
        if (wave == 2 && ii == 63) {
            u32 wp = 0;
#pragma unroll 1
            for (int level = 0; level < 3; ++level) {
                const bool act = level == 2 ? lane < 48 : lane < 63 && (lane >= 40 || level == 1);
                u32x2 c = {0u, 0u};
                u32 spins = 0;
#pragma unroll 1
                for (; spins < SPIN_CAP; ++spins) {
                    CFENCE();
                    c = ldx2(rs, (u32)(OFF_INI + level * 512 + lane * 8));
                    if (__all(!act || c[1] == cal_tag(tag, c[0]))) break;
                }
                if (spins == SPIN_CAP) break;
                if (level < 2) wp = wp_of(ws, lane < 63 ? c[0] : 0x07070707u);
                stx2(rs, lane < 8 ? (u32)(OFF_SUM + level * 1024 + lane * 128) : OOB, (u32x2){wp, cal_tag(tag, wp)});
                XSTAMP(192 + level, 229);
            }
        }
        if (ii < 63) LBAR();
        return;
    }
    u32x4 xv[5], wv[5];
    if (ii < 40) {
        early_lines(wave * 40 + ii, (u64)(uintptr_t)Wq, (u64)(uintptr_t)Wk, (u64)(uintptr_t)Wv, (u64)(uintptr_t)Wo, (u32)(uintptr_t)(__attribute__((address_space(3))) char*)smem + L_MISC + 512u);
        tag = settle();
        const u32 wp = wait_init(rs, tag, 0), st = (u32)(ii * 8 + wave), so = slot_off(wp, (int)(st >> 6)) + (st & 63u) * 16384u + (u32)lane * 16;
#pragma unroll
        for (int i = 0; i < 8; ++i) stx4(rs, so + i * 1024, (u32x4){SENT, SENT, SENT, SENT});
        DRAIN();
        XSTAMP(192 + ii, 225);
        arrive(1);
    } else {
        const int row = (ii - 40) * 32 + wave * 4;
        u32x4 xw[5];
        ln_load(row, lane, x, w_r, xv, wv);
#pragma unroll
        for (int i = 0; i < 5; ++i) xw[i] = *(const u32x4*)(x + (size_t)(row + 256 + (lane >> 4)) * D_ + ((lane & 15) + 16 * i) * 8);
        ln_regs(xv);
        tag = settle();
        const u32 i = (u32)((ii - 40) * 512 + tid);
        stx4(rs, i < INI_ZERO_CHUNKS ? (u32)ZERO_BEGIN + i * 16 : OOB, (u32x4){0u, 0u, 0u, 0u});
#pragma unroll
        for (int i = 0; i < 5; ++i) stx4(rs, (u32)OFF_XNP + img_off(row + (lane >> 4), lane & 15, i), ln_perm(xv[i], lane & 15));
        ln_regs(xw);
        DRAIN();
        XSTAMP(192 + ii, 220);
        arrive(0);
        ln_store(row + 256, lane, xw, wv, rs);
        asm volatile("s_waitcnt vmcnt(6)" ::: "memory");
        arrive(1);
        ln_store(row, lane, xv, wv, rs, false);
    }
    {
        const int row = 512 + (ii * 8 + wave) * 4;
        ln_load(row, lane, x, w_r, xv, wv);
        ln_regs(xv);
        ln_store(row, lane, xv, wv, rs);
    }
    DRAIN();
    XSTAMP(192 + ii, 222);
    arrive(2);
    LBAR();
}

#define NOWV 2
#define STP
#define STA
#define LGP
#define LGA
__device__ __forceinline__ void s_fetch(u32 vm, u32 xo, u32x4 rs, u32 pfo, u32 pfd, u32 xd) {
    asm volatile(
        "s_mov_b32 s92, m0\n\t"
        "s_mov_b32 m0, %[pfd]\n\t"
        "s_add_u32 s93, %[pfo], 0x1000\n\t"
        "s_nop 2\n\t"
        "buffer_load_dwordx4 %[vm], %[rs], %[pfo] offen offset:0 sc1 lds\n\t"
        "buffer_load_dwordx4 %[vm], %[rs], %[pfo] offen offset:1024 sc1 lds\n\t"
        "buffer_load_dwordx4 %[vm], %[rs], %[pfo] offen offset:2048 sc1 lds\n\t"
        "buffer_load_dwordx4 %[vm], %[rs], %[pfo] offen offset:3072 sc1 lds\n\t"
        "s_add_u32 m0, m0, 0x1000\n\t"
        "s_nop 0\n\t"
        "buffer_load_dwordx4 %[vm], %[rs], s93 offen offset:0 sc1 lds\n\t"
        "buffer_load_dwordx4 %[vm], %[rs], s93 offen offset:1024 sc1 lds\n\t"
        "buffer_load_dwordx4 %[vm], %[rs], s93 offen offset:2048 sc1 lds\n\t"
        "buffer_load_dwordx4 %[vm], %[rs], s93 offen offset:3072 sc1 lds\n\t"
        "s_mov_b32 m0, %[xd]\n\t"
        "s_nop 0\n\t"
        "buffer_load_dword %[xo], %[rs], 0 offen sc1 lds\n\t"
        "s_mov_b32 m0, s92\n\t"
        :
        : [vm] "v"(vm), [xo] "v"(xo), [rs] "s"(rs), [pfo] "s"(pfo), [pfd] "s"(pfd), [xd] "s"(xd)
        : "s92", "s93", "vcc", "scc", "memory");
}

__device__ __forceinline__ void s_k0(const u32x32 (&W)[5], u32& rnd, u32 va, u32 sidx, u32 ow, u32 lb STP) {
    asm volatile(
        "Ltop%=:\n\t"
        "v_mov_b64 v[248:249], 0\n\t"
        "v_mov_b64 v[250:251], 0\n\t"
        "v_mov_b64 v[252:253], 0\n\t"
        "v_mov_b64 v[254:255], 0\n\t"
        "v_mov_b32 v200, 0xffbfffbf\n\t"
        "v_mov_b32 v206, %[lb]\n\t"
        "ds_write_b32 %[ow], v200\n\t"
        "ds_write_b32 %[ow], v200 offset:3072\n\t"
        "s_add_u32 %[rnd], %[rnd], 1\n\t"
        "s_setprio 1\n\t"
        "s_waitcnt lgkmcnt(0)\n\t"
        "s_barrier\n\t"
        "s_nop 15\n\t"
        "s_nop 11\n\t"
        "ds_read_b128 v[208:211], %[va] offset:0\n\t"
        "ds_read_b128 v[212:215], %[va] offset:1024\n\t"
        "ds_read_b128 v[216:219], %[va] offset:2048\n\t"
        "ds_read_b128 v[220:223], %[va] offset:3072\n\t"
        "ds_read_b128 v[224:227], %[va] offset:4096\n\t"
        "ds_read_b128 v[228:231], %[va] offset:5120\n\t"
        "ds_read_b128 v[232:235], %[va] offset:6144\n\t"
        "ds_read_b128 v[236:239], %[va] offset:7168\n\t"
        "ds_read_b128 v[240:243], %[va] offset:8192\n\t"
        "ds_read_b128 v[244:247], %[va] offset:10240\n\t"
        "s_waitcnt lgkmcnt(0)\n\t"
        "v_smfmac_f32_16x16x64_bf16 v[248:251], v[208:211], v[16:23], %[sidx]\n\t"
        "v_smfmac_f32_16x16x64_bf16 v[248:251], v[212:215], v[24:31], %[sidx]\n\t"
        "v_smfmac_f32_16x16x64_bf16 v[248:251], v[216:219], v[32:39], %[sidx]\n\t"
        "v_smfmac_f32_16x16x64_bf16 v[248:251], v[220:223], v[40:47], %[sidx]\n\t"
        "v_smfmac_f32_16x16x64_bf16 v[248:251], v[224:227], v[48:55], %[sidx]\n\t"
        "v_smfmac_f32_16x16x64_bf16 v[248:251], v[228:231], v[56:63], %[sidx]\n\t"
        "v_smfmac_f32_16x16x64_bf16 v[248:251], v[232:235], v[64:71], %[sidx]\n\t"
        "v_smfmac_f32_16x16x64_bf16 v[248:251], v[236:239], v[72:79], %[sidx]\n\t"
        "v_smfmac_f32_16x16x64_bf16 v[248:251], v[240:243], v[80:87], %[sidx]\n\t"
        "v_smfmac_f32_16x16x64_bf16 v[248:251], v[244:247], v[88:95], %[sidx]\n\t"
        "v_smfmac_f32_16x16x64_bf16 v[252:255], v[208:211], v[96:103], %[sidx]\n\t"
        "v_smfmac_f32_16x16x64_bf16 v[252:255], v[212:215], v[104:111], %[sidx]\n\t"
        "v_smfmac_f32_16x16x64_bf16 v[252:255], v[216:219], v[112:119], %[sidx]\n\t"
        "v_add_f32 v248, v248, v250\n\t"
        "v_smfmac_f32_16x16x64_bf16 v[252:255], v[220:223], v[120:127], %[sidx]\n\t"
        "v_add_f32 v249, v249, v251\n\t"
        "v_smfmac_f32_16x16x64_bf16 v[252:255], v[224:227], v[128:135], %[sidx]\n\t"
        "v_cvt_pk_bf16_f32 v200, v248, v249\n\t"
        "v_smfmac_f32_16x16x64_bf16 v[252:255], v[228:231], v[136:143], %[sidx]\n\t"
        "ds_write_b32 %[ow], v200\n\t"
        "v_smfmac_f32_16x16x64_bf16 v[252:255], v[232:235], v[144:151], %[sidx]\n\t"
        "v_cmp_u_f32 vcc, v248, v249\n\t"
        "v_smfmac_f32_16x16x64_bf16 v[252:255], v[236:239], v[152:159], %[sidx]\n\t"
        "v_smfmac_f32_16x16x64_bf16 v[252:255], v[240:243], v[160:167], %[sidx]\n\t"
        "v_smfmac_f32_16x16x64_bf16 v[252:255], v[244:247], v[168:175], %[sidx]\n\t"
        "s_nop 6\n\t"
        "s_cbranch_vccnz Lnan%=\n\t"
        "Lok%=:\n\t"
        "v_add_f32 v252, v252, v254\n\t"
        "v_add_f32 v253, v253, v255\n\t"
        "v_cvt_pk_bf16_f32 v201, v252, v253\n\t"
        "ds_write_b32 %[ow], v201 offset:3072\n\t"
        "s_barrier\n\t"
        "s_setprio 0\n\t"
        "ds_read_b32 v207, v206 offset:25104\n\t"
        "s_waitcnt lgkmcnt(0)\n\t"
        "v_cmp_eq_u32 vcc, %[rnd], v207\n\t"
        "s_barrier\n\t"
        "s_cbranch_vccnz Ltop%=\n\t"
        "s_branch Lend%=\n\t"
        "Lnan%=:\n\t"
        "v_max_u32 v207, v208, v209\n\t"
        "v_max3_u32 v207, v207, v210, v211\n\t"
        "v_max3_u32 v207, v207, v212, v213\n\t"
        "v_max3_u32 v207, v207, v214, v215\n\t"
        "v_max3_u32 v207, v207, v216, v217\n\t"
        "v_max3_u32 v207, v207, v218, v219\n\t"
        "v_max3_u32 v207, v207, v220, v221\n\t"
        "v_max3_u32 v207, v207, v222, v223\n\t"
        "v_max3_u32 v207, v207, v224, v225\n\t"
        "v_max3_u32 v207, v207, v226, v227\n\t"
        "v_max3_u32 v207, v207, v228, v229\n\t"
        "v_max3_u32 v207, v207, v230, v231\n\t"
        "v_max3_u32 v207, v207, v232, v233\n\t"
        "v_max3_u32 v207, v207, v234, v235\n\t"
        "v_max3_u32 v207, v207, v236, v237\n\t"
        "v_max3_u32 v207, v207, v238, v239\n\t"
        "v_max3_u32 v207, v207, v240, v241\n\t"
        "v_max3_u32 v207, v207, v242, v243\n\t"
        "v_max3_u32 v207, v207, v244, v245\n\t"
        "v_max3_u32 v207, v207, v246, v247\n\t"
        "v_cmp_eq_u32 vcc, 0xffbfffbf, v207\n\t"
        "v_mov_b32 v207, %[rnd]\n\t"
        "s_cbranch_vccz Lok%=\n\t"
        "ds_write_b32 v206, v207 offset:25104\n\t"
        "s_waitcnt lgkmcnt(0)\n\t"
        "s_barrier\n\t"
        "s_barrier\n\t"
        "s_branch Ltop%=\n\t"
        "Lend%=:\n\t"
        : [rnd] "+&s"(rnd)
        : [w0] "{v[16:47]}"(W[0]), [w1] "{v[48:79]}"(W[1]), [w2] "{v[80:111]}"(W[2]), [w3] "{v[112:143]}"(W[3]), [w4] "{v[144:175]}"(W[4]), [va] "v"(va), [sidx] "v"(sidx), [ow] "v"(ow), [lb] "s"(lb)
        : "v200", "v201", "v202", "v203", "v204", "v205", "v206", "v207", "v208", "v209", "v210", "v211", "v212", "v213", "v214", "v215", "v216", "v217", "v218", "v219", "v220", "v221", "v222", "v223", "v224", "v225", "v226", "v227", "v228", "v229", "v230", "v231", "v232", "v233", "v234", "v235", "v236", "v237", "v238", "v239", "v240", "v241", "v242", "v243", "v244", "v245", "v246", "v247", "v248", "v249", "v250", "v251", "v252", "v253", "v254", "v255", "vcc", "scc", "memory");
}

__device__ __forceinline__ void s_k2(const u32x32 (&W)[5], u32& rnd, u32 va, u32 sidx, u32 ow, u32 lb) {
    asm volatile(
        "Ltop%=:\n\t"
        "v_mov_b64 v[248:249], 0\n\t"
        "v_mov_b64 v[250:251], 0\n\t"
        "v_mov_b64 v[252:253], 0\n\t"
        "v_mov_b64 v[254:255], 0\n\t"
        "v_mov_b32 v200, 0xffbfffbf\n\t"
        "v_mov_b32 v206, %[lb]\n\t"
        "ds_write_b32 %[ow], v200\n\t"
        "ds_write_b32 %[ow], v200 offset:3072\n\t"
        "s_add_u32 %[rnd], %[rnd], 1\n\t"
        "s_setprio 1\n\t"
        "s_waitcnt lgkmcnt(0)\n\t"
        "s_barrier\n\t"
        "ds_read_b128 v[208:211], %[va] offset:0\n\t"
        "ds_read_b128 v[212:215], %[va] offset:1024\n\t"
        "ds_read_b128 v[216:219], %[va] offset:2048\n\t"
        "ds_read_b128 v[220:223], %[va] offset:3072\n\t"
        "ds_read_b128 v[224:227], %[va] offset:4096\n\t"
        "ds_read_b128 v[228:231], %[va] offset:5120\n\t"
        "ds_read_b128 v[232:235], %[va] offset:6144\n\t"
        "ds_read_b128 v[236:239], %[va] offset:7168\n\t"
        "ds_read_b128 v[240:243], %[va] offset:8192\n\t"
        "ds_read_b128 v[244:247], %[va] offset:10240\n\t"
        "s_waitcnt lgkmcnt(9)\n\t"
        "v_smfmac_f32_16x16x64_bf16 v[248:251], v[208:211], v[16:23], %[sidx]\n\t"
        "s_waitcnt lgkmcnt(8)\n\t"
        "v_smfmac_f32_16x16x64_bf16 v[248:251], v[212:215], v[24:31], %[sidx]\n\t"
        "s_waitcnt lgkmcnt(7)\n\t"
        "v_smfmac_f32_16x16x64_bf16 v[248:251], v[216:219], v[32:39], %[sidx]\n\t"
        "s_waitcnt lgkmcnt(6)\n\t"
        "v_smfmac_f32_16x16x64_bf16 v[248:251], v[220:223], v[40:47], %[sidx]\n\t"
        "s_waitcnt lgkmcnt(5)\n\t"
        "v_smfmac_f32_16x16x64_bf16 v[248:251], v[224:227], v[48:55], %[sidx]\n\t"
        "s_waitcnt lgkmcnt(4)\n\t"
        "v_smfmac_f32_16x16x64_bf16 v[248:251], v[228:231], v[56:63], %[sidx]\n\t"
        "s_waitcnt lgkmcnt(3)\n\t"
        "v_smfmac_f32_16x16x64_bf16 v[248:251], v[232:235], v[64:71], %[sidx]\n\t"
        "s_waitcnt lgkmcnt(2)\n\t"
        "v_smfmac_f32_16x16x64_bf16 v[248:251], v[236:239], v[72:79], %[sidx]\n\t"
        "s_waitcnt lgkmcnt(1)\n\t"
        "v_smfmac_f32_16x16x64_bf16 v[248:251], v[240:243], v[80:87], %[sidx]\n\t"
        "s_waitcnt lgkmcnt(0)\n\t"
        "v_smfmac_f32_16x16x64_bf16 v[248:251], v[244:247], v[88:95], %[sidx]\n\t"
        "v_smfmac_f32_16x16x64_bf16 v[252:255], v[208:211], v[96:103], %[sidx]\n\t"
        "v_smfmac_f32_16x16x64_bf16 v[252:255], v[212:215], v[104:111], %[sidx]\n\t"
        "v_smfmac_f32_16x16x64_bf16 v[252:255], v[216:219], v[112:119], %[sidx]\n\t"
        "v_add_f32 v248, v248, v250\n\t"
        "v_smfmac_f32_16x16x64_bf16 v[252:255], v[220:223], v[120:127], %[sidx]\n\t"
        "v_add_f32 v249, v249, v251\n\t"
        "v_smfmac_f32_16x16x64_bf16 v[252:255], v[224:227], v[128:135], %[sidx]\n\t"
        "v_cvt_pk_bf16_f32 v200, v248, v249\n\t"
        "v_smfmac_f32_16x16x64_bf16 v[252:255], v[228:231], v[136:143], %[sidx]\n\t"
        "ds_write_b32 %[ow], v200\n\t"
        "v_smfmac_f32_16x16x64_bf16 v[252:255], v[232:235], v[144:151], %[sidx]\n\t"
        "v_cmp_u_f32 vcc, v248, v249\n\t"
        "v_smfmac_f32_16x16x64_bf16 v[252:255], v[236:239], v[152:159], %[sidx]\n\t"
        "v_smfmac_f32_16x16x64_bf16 v[252:255], v[240:243], v[160:167], %[sidx]\n\t"
        "v_smfmac_f32_16x16x64_bf16 v[252:255], v[244:247], v[168:175], %[sidx]\n\t"
        "s_nop 6\n\t"
        "s_cbranch_vccnz Lnan%=\n\t"
        "Lok%=:\n\t"
        "v_add_f32 v252, v252, v254\n\t"
        "v_add_f32 v253, v253, v255\n\t"
        "v_cvt_pk_bf16_f32 v201, v252, v253\n\t"
        "ds_write_b32 %[ow], v201 offset:3072\n\t"
        "s_barrier\n\t"
        "s_setprio 0\n\t"
        "ds_read_b32 v207, v206 offset:25104\n\t"
        "s_waitcnt lgkmcnt(0)\n\t"
        "v_cmp_eq_u32 vcc, %[rnd], v207\n\t"
        "s_barrier\n\t"
        "s_cbranch_vccnz Ltop%=\n\t"
        "s_branch Lend%=\n\t"
        "Lnan%=:\n\t"
        "v_max_u32 v207, v208, v209\n\t"
        "v_max3_u32 v207, v207, v210, v211\n\t"
        "v_max3_u32 v207, v207, v212, v213\n\t"
        "v_max3_u32 v207, v207, v214, v215\n\t"
        "v_max3_u32 v207, v207, v216, v217\n\t"
        "v_max3_u32 v207, v207, v218, v219\n\t"
        "v_max3_u32 v207, v207, v220, v221\n\t"
        "v_max3_u32 v207, v207, v222, v223\n\t"
        "v_max3_u32 v207, v207, v224, v225\n\t"
        "v_max3_u32 v207, v207, v226, v227\n\t"
        "v_max3_u32 v207, v207, v228, v229\n\t"
        "v_max3_u32 v207, v207, v230, v231\n\t"
        "v_max3_u32 v207, v207, v232, v233\n\t"
        "v_max3_u32 v207, v207, v234, v235\n\t"
        "v_max3_u32 v207, v207, v236, v237\n\t"
        "v_max3_u32 v207, v207, v238, v239\n\t"
        "v_max3_u32 v207, v207, v240, v241\n\t"
        "v_max3_u32 v207, v207, v242, v243\n\t"
        "v_max3_u32 v207, v207, v244, v245\n\t"
        "v_max3_u32 v207, v207, v246, v247\n\t"
        "v_cmp_eq_u32 vcc, 0xffbfffbf, v207\n\t"
        "v_mov_b32 v207, %[rnd]\n\t"
        "s_cbranch_vccz Lok%=\n\t"
        "ds_write_b32 v206, v207 offset:25104\n\t"
        "s_waitcnt lgkmcnt(0)\n\t"
        "s_barrier\n\t"
        "s_barrier\n\t"
        "s_branch Ltop%=\n\t"
        "Lend%=:\n\t"
        : [rnd] "+&s"(rnd)
        : [w0] "{v[16:47]}"(W[0]), [w1] "{v[48:79]}"(W[1]), [w2] "{v[80:111]}"(W[2]), [w3] "{v[112:143]}"(W[3]), [w4] "{v[144:175]}"(W[4]), [va] "v"(va), [sidx] "v"(sidx), [ow] "v"(ow), [lb] "s"(lb)
        : "v200", "v201", "v202", "v203", "v204", "v205", "v206", "v207", "v208", "v209", "v210", "v211", "v212", "v213", "v214", "v215", "v216", "v217", "v218", "v219", "v220", "v221", "v222", "v223", "v224", "v225", "v226", "v227", "v228", "v229", "v230", "v231", "v232", "v233", "v234", "v235", "v236", "v237", "v238", "v239", "v240", "v241", "v242", "v243", "v244", "v245", "v246", "v247", "v248", "v249", "v250", "v251", "v252", "v253", "v254", "v255", "vcc", "scc", "memory");
}

__device__ __forceinline__ void s_l(u32& rnd, u32& redo, u32 lb) {
    asm volatile(
        "v_mov_b32 v206, %[lb]\n\t"
        "s_add_u32 %[rnd], %[rnd], 1\n\t"
        "s_mov_b32 %[redo], 1\n\t"
        "s_barrier\n\t"
        "s_barrier\n\t"
        "s_setprio 0\n\t"
        "ds_read_b32 v207, v206 offset:25104\n\t"
        "s_waitcnt lgkmcnt(0)\n\t"
        "v_cmp_eq_u32 vcc, %[rnd], v207\n\t"
        "s_barrier\n\t"
        "s_cbranch_vccnz Lend%=\n\t"
        "s_mov_b32 %[redo], 0\n\t"
        "Lend%=:\n\t"
        : [rnd] "+&s"(rnd), [redo] "=&s"(redo)
        : [lb] "s"(lb)
        : "v206", "v207", "vcc", "scc", "memory");
}

__device__ __forceinline__ void s_e(const u32x32 (&W)[5], u32& rnd, u32 va, u32 sidx, u32 ow, u32 lb, u32 kxa, u32 qtr, u32 qo, u32 eo, u32 ew, float lim, u32x4 rs, u32 so STP) {
    asm volatile(
        "ds_read_b128 v[176:179], %[kxa] offset:0\n\t"
        "ds_read_b128 v[180:183], %[kxa] offset:1024\n\t"
        "ds_read_b128 v[184:187], %[kxa] offset:2048\n\t"
        "ds_read_b128 v[188:191], %[kxa] offset:3072\n\t"
        "s_add_u32 s92, %[so], 0x1000\n\t"
        "s_add_u32 s93, %[so], 0x4000\n\t"
        "s_add_u32 s94, %[so], 0x5000\n\t"
        "Ltop%=:\n\t"
        "v_mov_b64 v[248:249], 0\n\t"
        "v_mov_b64 v[250:251], 0\n\t"
        "v_mov_b64 v[252:253], 0\n\t"
        "v_mov_b64 v[254:255], 0\n\t"
        "v_mov_b32 v200, 0xffbfffbf\n\t"
        "v_mov_b32 v206, %[lb]\n\t"
        "ds_write_b32 %[ow], v200\n\t"
        "ds_write_b32 %[ow], v200 offset:3072\n\t"
        "s_add_u32 %[rnd], %[rnd], 1\n\t"
        "s_setprio 3\n\t"
        "s_mov_b32 s95, 1\n\t"
        "s_waitcnt lgkmcnt(0)\n\t"
        "s_barrier\n\t"
        "ds_read_b128 v[208:211], %[va] offset:0\n\t"
        "ds_read_b128 v[212:215], %[va] offset:1024\n\t"
        "ds_read_b128 v[216:219], %[va] offset:2048\n\t"
        "ds_read_b128 v[220:223], %[va] offset:3072\n\t"
        "ds_read_b128 v[224:227], %[va] offset:4096\n\t"
        "ds_read_b128 v[228:231], %[va] offset:5120\n\t"
        "s_waitcnt lgkmcnt(5)\n\t"
        "v_smfmac_f32_16x16x64_bf16 v[248:251], v[208:211], v[16:23], %[sidx]\n\t"
        "ds_read_b128 v[232:235], %[va] offset:6144\n\t"
        "v_smfmac_f32_16x16x64_bf16 v[252:255], v[208:211], v[96:103], %[sidx]\n\t"
        "s_waitcnt lgkmcnt(5)\n\t"
        "v_smfmac_f32_16x16x64_bf16 v[248:251], v[212:215], v[24:31], %[sidx]\n\t"
        "ds_read_b128 v[236:239], %[va] offset:7168\n\t"
        "v_smfmac_f32_16x16x64_bf16 v[252:255], v[212:215], v[104:111], %[sidx]\n\t"
        "s_waitcnt lgkmcnt(5)\n\t"
        "v_smfmac_f32_16x16x64_bf16 v[248:251], v[216:219], v[32:39], %[sidx]\n\t"
        "ds_read_b128 v[240:243], %[va] offset:8192\n\t"
        "v_smfmac_f32_16x16x64_bf16 v[252:255], v[216:219], v[112:119], %[sidx]\n\t"
        "s_waitcnt lgkmcnt(5)\n\t"
        "v_smfmac_f32_16x16x64_bf16 v[248:251], v[220:223], v[40:47], %[sidx]\n\t"
        "ds_read_b128 v[244:247], %[va] offset:10240\n\t"
        "v_smfmac_f32_16x16x64_bf16 v[252:255], v[220:223], v[120:127], %[sidx]\n\t"
        "s_waitcnt lgkmcnt(5)\n\t"
        "v_smfmac_f32_16x16x64_bf16 v[248:251], v[224:227], v[48:55], %[sidx]\n\t"
        "s_waitcnt lgkmcnt(4)\n\t"
        "v_smfmac_f32_16x16x64_bf16 v[248:251], v[228:231], v[56:63], %[sidx]\n\t"
        "s_waitcnt lgkmcnt(3)\n\t"
        "v_smfmac_f32_16x16x64_bf16 v[248:251], v[232:235], v[64:71], %[sidx]\n\t"
        "s_waitcnt lgkmcnt(2)\n\t"
        "v_smfmac_f32_16x16x64_bf16 v[248:251], v[236:239], v[72:79], %[sidx]\n\t"
        "s_waitcnt lgkmcnt(1)\n\t"
        "v_smfmac_f32_16x16x64_bf16 v[248:251], v[240:243], v[80:87], %[sidx]\n\t"
        "s_waitcnt lgkmcnt(0)\n\t"
        "v_smfmac_f32_16x16x64_bf16 v[248:251], v[244:247], v[88:95], %[sidx]\n\t"
        "v_smfmac_f32_16x16x64_bf16 v[252:255], v[224:227], v[128:135], %[sidx]\n\t"
        "v_smfmac_f32_16x16x64_bf16 v[252:255], v[228:231], v[136:143], %[sidx]\n\t"
        "v_add_f32 v248, v248, v250\n\t"
        "v_smfmac_f32_16x16x64_bf16 v[252:255], v[232:235], v[144:151], %[sidx]\n\t"
        "v_add_f32 v249, v249, v251\n\t"
        "v_smfmac_f32_16x16x64_bf16 v[252:255], v[236:239], v[152:159], %[sidx]\n\t"
        "v_cvt_pk_bf16_f32 v200, v248, v249\n\t"
        "v_smfmac_f32_16x16x64_bf16 v[252:255], v[240:243], v[160:167], %[sidx]\n\t"
        "ds_write_b32 %[ow], v200\n\t"
        "v_smfmac_f32_16x16x64_bf16 v[252:255], v[244:247], v[168:175], %[sidx]\n\t"
        "v_cmp_u_f32 vcc, v248, v249\n\t"
        "s_nop 2\n\t"
        "ds_read_b64_tr_b16 v[192:193], %[qtr] offset:0\n\t"
        "ds_read_b64_tr_b16 v[194:195], %[qtr] offset:768\n\t"
        "ds_read_b64_tr_b16 v[196:197], %[qtr] offset:1536\n\t"
        "ds_read_b64_tr_b16 v[198:199], %[qtr] offset:2304\n\t"
        "s_cbranch_vccnz Lnan%=\n\t"
        "Lok%=:\n\t"
        "v_add_f32 v252, v252, v254\n\t"
        "v_add_f32 v253, v253, v255\n\t"
        "v_cvt_pk_bf16_f32 v201, v252, v253\n\t"
        "ds_write_b32 %[ow], v201 offset:3072\n\t"
        "s_waitcnt lgkmcnt(3)\n\t"
        "v_mfma_f32_16x16x32_bf16 v[248:251], v[176:179], v[192:195], 0\n\t"
        "s_waitcnt lgkmcnt(1)\n\t"
        "v_mfma_f32_16x16x32_bf16 v[248:251], v[180:183], v[196:199], v[248:251]\n\t"
        "s_waitcnt lgkmcnt(0)\n\t"
        "s_nop 1\n\t"
        "Lre%=:\n\t"
        "ds_read_b64_tr_b16 v[208:209], %[qtr] offset:3072\n\t"
        "ds_read_b64_tr_b16 v[210:211], %[qtr] offset:3840\n\t"
        "ds_read_b64_tr_b16 v[212:213], %[qtr] offset:4608\n\t"
        "ds_read_b64_tr_b16 v[214:215], %[qtr] offset:5376\n\t"
        "s_waitcnt lgkmcnt(2)\n\t"
        "v_mfma_f32_16x16x32_bf16 v[248:251], v[184:187], v[208:211], v[248:251]\n\t"
        "s_waitcnt lgkmcnt(0)\n\t"
        "v_mfma_f32_16x16x32_bf16 v[248:251], v[188:191], v[212:215], v[248:251]\n\t"
        "s_nop 7\n\t"
        "v_mov_b32_dpp v248, v250 quad_perm:[0,1,2,3] row_mask:0xf bank_mask:0xc\n\t"
        "v_mov_b32_dpp v249, v251 quad_perm:[0,1,2,3] row_mask:0xf bank_mask:0xc\n\t"
        "v_exp_f32 v202, v248\n\t"
        "v_exp_f32 v203, v249\n\t"
        "v_max_f32_e64 v204, |v248|, |v249|\n\t"
        "v_cvt_pk_bf16_f32 v202, v202, v203\n\t"
        "v_cmpx_le_f32 vcc, v204, %[lim]\n\t"
        "buffer_store_dword v202, %[eo], %[rs], %[so] offen sc1\n\t"
        "buffer_store_dword v202, %[eo], %[rs], s92 offen sc1\n\t"
        "buffer_store_dword v202, %[eo], %[rs], s93 offen sc1\n\t"
        "buffer_store_dword v202, %[eo], %[rs], s94 offen sc1\n\t"
        "s_mov_b64 exec, -1\n\t"
        "v_cndmask_b32 v202, -1, v202, vcc\n\t"
        "s_cmp_lg_u64 vcc, -1\n\t"
        "ds_write_b32 %[ew], v202\n\t"
        "s_cbranch_scc1 Lag%=\n\t"
        "Lgo%=:\n\t"
        "s_waitcnt lgkmcnt(0)\n\t"
        "s_barrier\n\t"
        "s_setprio 0\n\t"
        "ds_read_b32 v207, v206 offset:25104\n\t"
        "s_waitcnt lgkmcnt(0)\n\t"
        "v_cmp_eq_u32 vcc, %[rnd], v207\n\t"
        "s_barrier\n\t"
        "s_cbranch_vccnz Ltop%=\n\t"
        "s_branch Lend%=\n\t"
        "Lag%=:\n\t"
        "s_sub_u32 s95, s95, 1\n\t"
        "s_cbranch_scc1 Lgo%=\n\t"
        "Lqw%=:\n\t"
        "v_subrev_u32 v205, %[qo], %[ow]\n\t"
        "ds_read_b32 v192, v205 offset:0\n\t"
        "ds_read_b32 v193, v205 offset:768\n\t"
        "ds_read_b32 v194, v205 offset:1536\n\t"
        "ds_read_b32 v195, v205 offset:2304\n\t"
        "ds_read_b32 v196, v205 offset:3072\n\t"
        "ds_read_b32 v197, v205 offset:3840\n\t"
        "ds_read_b32 v198, v205 offset:4608\n\t"
        "ds_read_b32 v199, v205 offset:5376\n\t"
        "ds_read_b32 v207, v206 offset:25104\n\t"
        "s_waitcnt lgkmcnt(0)\n\t"
        "v_cmp_eq_u32 vcc, %[rnd], v207\n\t"
        "s_cbranch_vccnz Lgo%=\n\t"
        "v_max_u32 v207, v192, v193\n\t"
        "v_max3_u32 v207, v207, v194, v195\n\t"
        "v_max3_u32 v207, v207, v196, v197\n\t"
        "v_max3_u32 v207, v207, v198, v199\n\t"
        "v_cmp_eq_u32 vcc, 0xffbfffbf, v207\n\t"
        "s_cbranch_vccnz Lqw%=\n\t"
        "ds_read_b64_tr_b16 v[192:193], %[qtr] offset:0\n\t"
        "ds_read_b64_tr_b16 v[194:195], %[qtr] offset:768\n\t"
        "ds_read_b64_tr_b16 v[196:197], %[qtr] offset:1536\n\t"
        "ds_read_b64_tr_b16 v[198:199], %[qtr] offset:2304\n\t"
        "s_waitcnt lgkmcnt(2)\n\t"
        "v_mfma_f32_16x16x32_bf16 v[248:251], v[176:179], v[192:195], 0\n\t"
        "s_waitcnt lgkmcnt(0)\n\t"
        "v_mfma_f32_16x16x32_bf16 v[248:251], v[180:183], v[196:199], v[248:251]\n\t"
        "s_branch Lre%=\n\t"
        "Lnan%=:\n\t"
        "v_max_u32 v207, v208, v209\n\t"
        "v_max3_u32 v207, v207, v210, v211\n\t"
        "v_max3_u32 v207, v207, v212, v213\n\t"
        "v_max3_u32 v207, v207, v214, v215\n\t"
        "v_max3_u32 v207, v207, v216, v217\n\t"
        "v_max3_u32 v207, v207, v218, v219\n\t"
        "v_max3_u32 v207, v207, v220, v221\n\t"
        "v_max3_u32 v207, v207, v222, v223\n\t"
        "v_max3_u32 v207, v207, v224, v225\n\t"
        "v_max3_u32 v207, v207, v226, v227\n\t"
        "v_max3_u32 v207, v207, v228, v229\n\t"
        "v_max3_u32 v207, v207, v230, v231\n\t"
        "v_max3_u32 v207, v207, v232, v233\n\t"
        "v_max3_u32 v207, v207, v234, v235\n\t"
        "v_max3_u32 v207, v207, v236, v237\n\t"
        "v_max3_u32 v207, v207, v238, v239\n\t"
        "v_max3_u32 v207, v207, v240, v241\n\t"
        "v_max3_u32 v207, v207, v242, v243\n\t"
        "v_max3_u32 v207, v207, v244, v245\n\t"
        "v_max3_u32 v207, v207, v246, v247\n\t"
        "v_cmp_eq_u32 vcc, 0xffbfffbf, v207\n\t"
        "v_mov_b32 v207, %[rnd]\n\t"
        "s_cbranch_vccz Lok%=\n\t"
        "ds_write_b32 v206, v207 offset:25104\n\t"
        "s_waitcnt lgkmcnt(0)\n\t"
        "s_barrier\n\t"
        "s_barrier\n\t"
        "s_branch Ltop%=\n\t"
        "Lend%=:\n\t"
        : [rnd] "+&s"(rnd)
        : [w0] "{v[16:47]}"(W[0]), [w1] "{v[48:79]}"(W[1]), [w2] "{v[80:111]}"(W[2]), [w3] "{v[112:143]}"(W[3]), [w4] "{v[144:175]}"(W[4]), [va] "v"(va), [sidx] "v"(sidx), [ow] "v"(ow), [lb] "s"(lb), [kxa] "v"(kxa), [qtr] "v"(qtr), [eo] "v"(eo), [ew] "v"(ew), [lim] "v"(lim), [rs] "s"(rs), [so] "s"(so), [qo] "s"(qo)
        : "s92", "s93", "s94", "s95", "v176", "v177", "v178", "v179", "v180", "v181", "v182", "v183", "v184", "v185", "v186", "v187", "v188", "v189", "v190", "v191", "v192", "v193", "v194", "v195", "v196", "v197", "v198", "v199", "v200", "v201", "v202", "v203", "v204", "v205", "v206", "v207", "v208", "v209", "v210", "v211", "v212", "v213", "v214", "v215", "v216", "v217", "v218", "v219", "v220", "v221", "v222", "v223", "v224", "v225", "v226", "v227", "v228", "v229", "v230", "v231", "v232", "v233", "v234", "v235", "v236", "v237", "v238", "v239", "v240", "v241", "v242", "v243", "v244", "v245", "v246", "v247", "v248", "v249", "v250", "v251", "v252", "v253", "v254", "v255", "vcc", "scc", "memory");
}

__device__ __forceinline__ void s_f(const u32x32 (&W)[5], u32& rnd, u32 va, u32 sidx, u32 ow, u32 lb, u32& bad, u32x4 ones, u32 qtr, u32 ktr, u32 er, u32 po, float c1, float ind0, float ind1, u32x4 rs, u32 so, float ce, float big STP) {
    asm volatile(
        "s_add_u32 s92, %[so], 0x1000\n\t"
        "s_add_u32 s93, %[so], 0x4000\n\t"
        "s_add_u32 s94, %[so], 0x5000\n\t"
        "Ltop%=:\n\t"
        "v_mov_b64 v[248:249], 0\n\t"
        "v_mov_b64 v[250:251], 0\n\t"
        "v_mov_b64 v[252:253], 0\n\t"
        "v_mov_b64 v[254:255], 0\n\t"
        "v_mov_b32 v200, 0xffbfffbf\n\t"
        "v_mov_b32 v206, %[lb]\n\t"
        "ds_write_b32 %[ow], v200\n\t"
        "ds_write_b32 %[ow], v200 offset:3072\n\t"
        "s_add_u32 %[rnd], %[rnd], 1\n\t"
        "s_setprio 3\n\t"
        "s_mov_b32 %[bad], 2\n\t"
        "v_mov_b32 v197, 0\n\t"
        "s_waitcnt lgkmcnt(0)\n\t"
        "s_barrier\n\t"
        "ds_read_b128 v[208:211], %[va] offset:0\n\t"
        "ds_read_b128 v[212:215], %[va] offset:1024\n\t"
        "ds_read_b128 v[216:219], %[va] offset:2048\n\t"
        "ds_read_b128 v[220:223], %[va] offset:3072\n\t"
        "ds_read_b128 v[224:227], %[va] offset:4096\n\t"
        "ds_read_b128 v[228:231], %[va] offset:5120\n\t"
        "s_waitcnt lgkmcnt(5)\n\t"
        "v_smfmac_f32_16x16x64_bf16 v[248:251], v[208:211], v[16:23], %[sidx]\n\t"
        "ds_read_b128 v[232:235], %[va] offset:6144\n\t"
        "v_smfmac_f32_16x16x64_bf16 v[252:255], v[208:211], v[96:103], %[sidx]\n\t"
        "s_waitcnt lgkmcnt(5)\n\t"
        "v_smfmac_f32_16x16x64_bf16 v[248:251], v[212:215], v[24:31], %[sidx]\n\t"
        "ds_read_b128 v[236:239], %[va] offset:7168\n\t"
        "v_smfmac_f32_16x16x64_bf16 v[252:255], v[212:215], v[104:111], %[sidx]\n\t"
        "s_waitcnt lgkmcnt(5)\n\t"
        "v_smfmac_f32_16x16x64_bf16 v[248:251], v[216:219], v[32:39], %[sidx]\n\t"
        "ds_read_b128 v[240:243], %[va] offset:8192\n\t"
        "v_smfmac_f32_16x16x64_bf16 v[252:255], v[216:219], v[112:119], %[sidx]\n\t"
        "s_waitcnt lgkmcnt(5)\n\t"
        "v_smfmac_f32_16x16x64_bf16 v[248:251], v[220:223], v[40:47], %[sidx]\n\t"
        "ds_read_b128 v[244:247], %[va] offset:10240\n\t"
        "v_smfmac_f32_16x16x64_bf16 v[252:255], v[220:223], v[120:127], %[sidx]\n\t"
        "s_waitcnt lgkmcnt(5)\n\t"
        "v_smfmac_f32_16x16x64_bf16 v[248:251], v[224:227], v[48:55], %[sidx]\n\t"
        "s_waitcnt lgkmcnt(4)\n\t"
        "v_smfmac_f32_16x16x64_bf16 v[248:251], v[228:231], v[56:63], %[sidx]\n\t"
        "s_waitcnt lgkmcnt(3)\n\t"
        "v_smfmac_f32_16x16x64_bf16 v[248:251], v[232:235], v[64:71], %[sidx]\n\t"
        "s_waitcnt lgkmcnt(2)\n\t"
        "v_smfmac_f32_16x16x64_bf16 v[248:251], v[236:239], v[72:79], %[sidx]\n\t"
        "s_waitcnt lgkmcnt(1)\n\t"
        "v_smfmac_f32_16x16x64_bf16 v[248:251], v[240:243], v[80:87], %[sidx]\n\t"
        "s_waitcnt lgkmcnt(0)\n\t"
        "v_smfmac_f32_16x16x64_bf16 v[248:251], v[244:247], v[88:95], %[sidx]\n\t"
        "v_smfmac_f32_16x16x64_bf16 v[252:255], v[224:227], v[128:135], %[sidx]\n\t"
        "v_smfmac_f32_16x16x64_bf16 v[252:255], v[228:231], v[136:143], %[sidx]\n\t"
        "v_add_f32 v248, v248, v250\n\t"
        "v_smfmac_f32_16x16x64_bf16 v[252:255], v[232:235], v[144:151], %[sidx]\n\t"
        "v_add_f32 v249, v249, v251\n\t"
        "v_smfmac_f32_16x16x64_bf16 v[252:255], v[236:239], v[152:159], %[sidx]\n\t"
        "v_cvt_pk_bf16_f32 v200, v248, v249\n\t"
        "v_smfmac_f32_16x16x64_bf16 v[252:255], v[240:243], v[160:167], %[sidx]\n\t"
        "ds_write_b32 %[ow], v200\n\t"
        "v_smfmac_f32_16x16x64_bf16 v[252:255], v[244:247], v[168:175], %[sidx]\n\t"
        "v_cmp_u_f32 vcc, v248, v249\n\t"
        "s_nop 6\n\t"
        "s_cbranch_vccnz Lnan%=\n\t"
        "Lok%=:\n\t"
        "v_add_f32 v252, v252, v254\n\t"
        "v_add_f32 v253, v253, v255\n\t"
        "v_cvt_pk_bf16_f32 v201, v252, v253\n\t"
        "ds_write_b32 %[ow], v201 offset:3072\n\t"
        "s_waitcnt lgkmcnt(0)\n\t"
        "s_nop 15\n\t"
        "ds_read_b64_tr_b16 v[180:181], %[qtr] offset:0\n\t"
        "ds_read_b64_tr_b16 v[182:183], %[qtr] offset:768\n\t"
        "ds_read_b64_tr_b16 v[184:185], %[qtr] offset:1536\n\t"
        "ds_read_b64_tr_b16 v[186:187], %[qtr] offset:2304\n\t"
        "ds_read_b64_tr_b16 v[188:189], %[qtr] offset:3072\n\t"
        "ds_read_b64_tr_b16 v[190:191], %[qtr] offset:3840\n\t"
        "ds_read_b64_tr_b16 v[192:193], %[qtr] offset:4608\n\t"
        "ds_read_b64_tr_b16 v[194:195], %[qtr] offset:5376\n\t"
        "s_nop 15\n\t"
        "s_nop 7\n\t"
        "ds_read_b64_tr_b16 v[224:225], %[ktr] offset:0\n\t"
        "ds_read_b64_tr_b16 v[226:227], %[ktr] offset:768\n\t"
        "ds_read_b64_tr_b16 v[228:229], %[ktr] offset:1536\n\t"
        "ds_read_b64_tr_b16 v[230:231], %[ktr] offset:2304\n\t"
        "s_nop 7\n\t"
        "ds_read_b64_tr_b16 v[232:233], %[ktr] offset:4608\n\t"
        "ds_read_b64_tr_b16 v[234:235], %[ktr] offset:5376\n\t"
        "s_waitcnt lgkmcnt(4)\n\t"
        "v_mfma_f32_16x16x32_bf16 v[248:251], v[224:227], v[180:183], 0\n\t"
        "s_waitcnt lgkmcnt(2)\n\t"
        "v_mfma_f32_16x16x32_bf16 v[248:251], v[228:231], v[184:187], v[248:251]\n\t"
        "s_waitcnt lgkmcnt(0)\n\t"
        "v_mfma_f32_16x16x32_bf16 v[248:251], v[232:235], v[192:195], v[248:251]\n\t"
        "s_barrier\n\t"
        "Lre%=:\n\t"
        "ds_read_b64_tr_b16 v[208:209], %[ktr] offset:3072\n\t"
        "ds_read_b64_tr_b16 v[210:211], %[ktr] offset:3840\n\t"
        "ds_read_b128 v[240:243], %[er]\n\t"
        "ds_read_b32 v207, v206 offset:25104\n\t"
        "s_waitcnt lgkmcnt(2)\n\t"
        "v_mfma_f32_16x16x32_bf16 v[248:251], v[208:211], v[188:191], v[248:251]\n\t"
        "s_waitcnt lgkmcnt(1)\n\t"
        "v_mfma_f32_16x16x32_bf16 v[244:247], v[176:179], v[240:243], 0\n\t"
        "s_nop 3\n\t"
        "v_mul_f32 v248, %[ce], v248\n\t"
        "v_mul_f32 v249, %[ce], v249\n\t"
        "v_exp_f32 v250, v248\n\t"
        "v_exp_f32 v251, v249\n\t"
        "s_nop 0\n\t"
        "v_cvt_pk_bf16_f32 v196, v250, v251\n\t"
        "v_fma_f32 v250, v250, %[c1], %[ind0]\n\t"
        "v_fma_f32 v251, v251, %[c1], %[ind1]\n\t"
        "v_mfma_f32_16x16x16_bf16 v[244:247], v[176:177], v[196:197], v[244:247]\n\t"
        "s_nop 7\n\t"
        "v_rcp_f32 v246, v244\n\t"
        "v_cmpx_gt_f32 vcc, %[big], v244\n\t"
        "v_pk_mul_f32 v[250:251], v[250:251], v[246:247] op_sel_hi:[1,0]\n\t"
        "v_cvt_pk_bf16_f32 v250, v250, v251\n\t"
        "buffer_store_dword v250, %[po], %[rs], %[so] offen sc1\n\t"
        "buffer_store_dword v250, %[po], %[rs], s92 offen sc1\n\t"
        "buffer_store_dword v250, %[po], %[rs], s93 offen sc1\n\t"
        "buffer_store_dword v250, %[po], %[rs], s94 offen sc1\n\t"
        "s_mov_b64 exec, -1\n\t"
        "s_cmp_eq_u32 %[bad], 1\n\t"
        "s_cbranch_scc1 Ltw%=\n\t"
        "s_cmp_lg_u64 vcc, -1\n\t"
        "s_cselect_b32 %[bad], 1, 0\n\t"
        "s_waitcnt lgkmcnt(0)\n\t"
        "v_cmp_eq_u32 vcc, %[rnd], v207\n\t"
        "s_barrier\n\t"
        "s_cbranch_vccnz Ltop%=\n\t"
        "s_cbranch_scc0 Lend%=\n\t"
        "ds_read_b64_tr_b16 v[180:181], %[qtr] offset:0\n\t"
        "ds_read_b64_tr_b16 v[182:183], %[qtr] offset:768\n\t"
        "ds_read_b64_tr_b16 v[184:185], %[qtr] offset:1536\n\t"
        "ds_read_b64_tr_b16 v[186:187], %[qtr] offset:2304\n\t"
        "ds_read_b64_tr_b16 v[188:189], %[qtr] offset:3072\n\t"
        "ds_read_b64_tr_b16 v[190:191], %[qtr] offset:3840\n\t"
        "ds_read_b64_tr_b16 v[192:193], %[qtr] offset:4608\n\t"
        "ds_read_b64_tr_b16 v[194:195], %[qtr] offset:5376\n\t"
        "ds_read_b64_tr_b16 v[224:225], %[ktr] offset:0\n\t"
        "ds_read_b64_tr_b16 v[226:227], %[ktr] offset:768\n\t"
        "ds_read_b64_tr_b16 v[228:229], %[ktr] offset:1536\n\t"
        "ds_read_b64_tr_b16 v[230:231], %[ktr] offset:2304\n\t"
        "ds_read_b64_tr_b16 v[232:233], %[ktr] offset:4608\n\t"
        "ds_read_b64_tr_b16 v[234:235], %[ktr] offset:5376\n\t"
        "s_waitcnt lgkmcnt(0)\n\t"
        "v_mfma_f32_16x16x32_bf16 v[248:251], v[224:227], v[180:183], 0\n\t"
        "v_mfma_f32_16x16x32_bf16 v[248:251], v[228:231], v[184:187], v[248:251]\n\t"
        "v_mfma_f32_16x16x32_bf16 v[248:251], v[232:235], v[192:195], v[248:251]\n\t"
        "s_branch Lre%=\n\t"
        "Lnan%=:\n\t"
        "v_max_u32 v207, v208, v209\n\t"
        "v_max3_u32 v207, v207, v210, v211\n\t"
        "v_max3_u32 v207, v207, v212, v213\n\t"
        "v_max3_u32 v207, v207, v214, v215\n\t"
        "v_max3_u32 v207, v207, v216, v217\n\t"
        "v_max3_u32 v207, v207, v218, v219\n\t"
        "v_max3_u32 v207, v207, v220, v221\n\t"
        "v_max3_u32 v207, v207, v222, v223\n\t"
        "v_max3_u32 v207, v207, v224, v225\n\t"
        "v_max3_u32 v207, v207, v226, v227\n\t"
        "v_max3_u32 v207, v207, v228, v229\n\t"
        "v_max3_u32 v207, v207, v230, v231\n\t"
        "v_max3_u32 v207, v207, v232, v233\n\t"
        "v_max3_u32 v207, v207, v234, v235\n\t"
        "v_max3_u32 v207, v207, v236, v237\n\t"
        "v_max3_u32 v207, v207, v238, v239\n\t"
        "v_max3_u32 v207, v207, v240, v241\n\t"
        "v_max3_u32 v207, v207, v242, v243\n\t"
        "v_max3_u32 v207, v207, v244, v245\n\t"
        "v_max3_u32 v207, v207, v246, v247\n\t"
        "v_cmp_eq_u32 vcc, 0xffbfffbf, v207\n\t"
        "v_mov_b32 v207, %[rnd]\n\t"
        "s_cbranch_vccz Lok%=\n\t"
        "ds_write_b32 v206, v207 offset:25104\n\t"
        "s_waitcnt lgkmcnt(0)\n\t"
        "s_barrier\n\t"
        "s_barrier\n\t"
        "s_branch Ltop%=\n\t"
        "Ltw%=:\n\t"
        "s_cmp_lg_u64 vcc, -1\n\t"
        "s_cselect_b32 %[bad], 1, 0\n\t"
        "s_waitcnt lgkmcnt(0)\n\t"
        "Lend%=:\n\t"
        "s_setprio 0\n\t"
        : [rnd] "+&s"(rnd), [bad] "=&s"(bad)
        : [w0] "{v[16:47]}"(W[0]), [w1] "{v[48:79]}"(W[1]), [w2] "{v[80:111]}"(W[2]), [w3] "{v[112:143]}"(W[3]), [w4] "{v[144:175]}"(W[4]), [va] "v"(va), [sidx] "v"(sidx), [ow] "v"(ow), [lb] "s"(lb), [ones] "{v[176:179]}"(ones), [qtr] "v"(qtr), [ktr] "v"(ktr), [er] "v"(er), [po] "v"(po), [c1] "v"(c1), [ind0] "v"(ind0), [ind1] "v"(ind1), [rs] "s"(rs), [so] "s"(so), [ce] "s"(ce), [big] "s"(big)
        : "s92", "s93", "s94", "v180", "v181", "v182", "v183", "v184", "v185", "v186", "v187", "v188", "v189", "v190", "v191", "v192", "v193", "v194", "v195", "v196", "v197", "v198", "v199", "v200", "v201", "v202", "v203", "v204", "v205", "v206", "v207", "v208", "v209", "v210", "v211", "v212", "v213", "v214", "v215", "v216", "v217", "v218", "v219", "v220", "v221", "v222", "v223", "v224", "v225", "v226", "v227", "v228", "v229", "v230", "v231", "v232", "v233", "v234", "v235", "v236", "v237", "v238", "v239", "v240", "v241", "v242", "v243", "v244", "v245", "v246", "v247", "v248", "v249", "v250", "v251", "v252", "v253", "v254", "v255", "vcc", "scc", "memory");
}

__device__ __forceinline__ void s_m(const u32x32 (&W)[5], u32& rnd, u32 va, u32 sidx, u32 ow, u32 lb, u32& left, u32& c0, u32& r0, u32& t1, u32& t3, u32 tg, u32 kr, u32 vm, u32 so, u32 md, u32x4 rs, u32 poll, u32 tv, u32 tsr STP) {
    asm volatile(
        "s_mov_b32 s94, m0\n\t"
        "s_add_u32 s92, %[so], 0x8000\n\t"
        "s_add_u32 s93, %[so], 0x10000\n\t"
        "s_mov_b32 m0, %[md]\n\t"
        "s_setprio 3\n\t"
        "s_mov_b32 s96, %[left]\n\t"
        "s_cmp_eq_u32 %[poll], 0\n\t"
        "s_cbranch_scc1 Ltop%=\n\t"
        "Lda_%=:\n\t"
        "s_memrealtime s[90:91]\n\t"
        "s_waitcnt lgkmcnt(0)\n\t"
        "s_mul_i32 s90, s90, 24\n\t"
        "s_sub_u32 s88, %[tg], s90\n\t"
        "s_sub_u32 s89, s88, 620\n\t"
        "s_cmp_gt_u32 s89, 0x4000\n\t"
        "s_cbranch_scc1 Lea_%=\n\t"
        "s_sleep 1\n\t"
        "s_branch Lda_%=\n\t"
        "Lea_%=:\n\t"
        "s_memrealtime s[64:65]\n\t"
        "s_nop 0\n\t"
        "s_memrealtime s[66:67]\n\t"
        "s_nop 0\n\t"
        "s_memrealtime s[68:69]\n\t"
        "s_nop 0\n\t"
        "s_memrealtime s[70:71]\n\t"
        "s_nop 0\n\t"
        "s_memrealtime s[72:73]\n\t"
        "s_nop 0\n\t"
        "s_memrealtime s[74:75]\n\t"
        "s_memtime s[90:91]\n\t"
        "s_memrealtime s[76:77]\n\t"
        "s_nop 0\n\t"
        "s_memrealtime s[78:79]\n\t"
        "s_nop 0\n\t"
        "s_memrealtime s[80:81]\n\t"
        "s_nop 0\n\t"
        "s_memrealtime s[82:83]\n\t"
        "s_nop 0\n\t"
        "s_memrealtime s[84:85]\n\t"
        "s_nop 0\n\t"
        "s_memrealtime s[86:87]\n\t"
        "s_nop 0\n\t"
        "s_nop 7\n\t"
        "s_waitcnt lgkmcnt(0)\n\t"
        "s_add_u32 s64, s64, s66\n\t"
        "s_add_u32 s64, s64, s68\n\t"
        "s_add_u32 s64, s64, s70\n\t"
        "s_add_u32 s64, s64, s72\n\t"
        "s_add_u32 s64, s64, s74\n\t"
        "s_add_u32 s64, s64, s76\n\t"
        "s_add_u32 s64, s64, s78\n\t"
        "s_add_u32 s64, s64, s80\n\t"
        "s_add_u32 s64, s64, s82\n\t"
        "s_add_u32 s64, s64, s84\n\t"
        "s_add_u32 s64, s64, s86\n\t"
        "s_lshl_b32 s64, s64, 1\n\t"
        "s_mov_b32 %[c0], s90\n\t"
        "s_mov_b32 %[r0], s64\n\t"
        "s_sub_u32 s88, %[tg], s64\n\t"
        "s_mul_i32 s88, s88, %[kr]\n\t"
        "s_ashr_i32 s88, s88, 12\n\t"
        "s_sub_u32 s88, s88, 244\n\t"
        "s_nop 1\n\t"
        "s_max_i32 s88, s88, 0\n\t"
        "s_min_i32 s88, s88, 384\n\t"
        "s_andn2_b32 s88, s88, 3\n\t"
        "s_getpc_b64 s[90:91]\n\t"
        "Lga_%=:\n\t"
        "s_sub_u32 s90, s90, s88\n\t"
        "s_subb_u32 s91, s91, 0\n\t"
        "s_add_u32 s90, s90, Lza_%=-Lga_%=\n\t"
        "s_addc_u32 s91, s91, 0\n\t"
        "s_setpc_b64 s[90:91]\n\t"
        "s_nop 0\n\ts_nop 0\n\ts_nop 0\n\ts_nop 0\n\t"
        "s_nop 0\n\ts_nop 0\n\ts_nop 0\n\ts_nop 0\n\t"
        "s_nop 0\n\ts_nop 0\n\ts_nop 0\n\ts_nop 0\n\t"
        "s_nop 0\n\ts_nop 0\n\ts_nop 0\n\ts_nop 0\n\t"
        "s_nop 0\n\ts_nop 0\n\ts_nop 0\n\ts_nop 0\n\t"
        "s_nop 0\n\ts_nop 0\n\ts_nop 0\n\ts_nop 0\n\t"
        "s_nop 0\n\ts_nop 0\n\ts_nop 0\n\ts_nop 0\n\t"
        "s_nop 0\n\ts_nop 0\n\ts_nop 0\n\ts_nop 0\n\t"
        "s_nop 0\n\ts_nop 0\n\ts_nop 0\n\ts_nop 0\n\t"
        "s_nop 0\n\ts_nop 0\n\ts_nop 0\n\ts_nop 0\n\t"
        "s_nop 0\n\ts_nop 0\n\ts_nop 0\n\ts_nop 0\n\t"
        "s_nop 0\n\ts_nop 0\n\ts_nop 0\n\ts_nop 0\n\t"
        "s_nop 0\n\ts_nop 0\n\ts_nop 0\n\ts_nop 0\n\t"
        "s_nop 0\n\ts_nop 0\n\ts_nop 0\n\ts_nop 0\n\t"
        "s_nop 0\n\ts_nop 0\n\ts_nop 0\n\ts_nop 0\n\t"
        "s_nop 0\n\ts_nop 0\n\ts_nop 0\n\ts_nop 0\n\t"
        "s_nop 0\n\ts_nop 0\n\ts_nop 0\n\ts_nop 0\n\t"
        "s_nop 0\n\ts_nop 0\n\ts_nop 0\n\ts_nop 0\n\t"
        "s_nop 0\n\ts_nop 0\n\ts_nop 0\n\ts_nop 0\n\t"
        "s_nop 0\n\ts_nop 0\n\ts_nop 0\n\ts_nop 0\n\t"
        "s_nop 0\n\ts_nop 0\n\ts_nop 0\n\ts_nop 0\n\t"
        "s_nop 0\n\ts_nop 0\n\ts_nop 0\n\ts_nop 0\n\t"
        "s_nop 0\n\ts_nop 0\n\ts_nop 0\n\ts_nop 0\n\t"
        "s_nop 0\n\ts_nop 0\n\ts_nop 0\n\ts_nop 0\n\t"
        "Lza_%=:\n\t"
        "buffer_load_dwordx4 %[vm], %[rs], %[so] offen offset:0 sc1 lds\n\t"
        "buffer_load_dwordx4 %[vm], %[rs], %[so] offen offset:1024 sc1 lds\n\t"
        "buffer_load_dwordx4 %[vm], %[rs], %[so] offen offset:2048 sc1 lds\n\t"
        "buffer_load_dwordx4 %[vm], %[rs], %[so] offen offset:3072 sc1 lds\n\t"
        "s_add_u32 m0, m0, 0x1000\n\t"
        "s_nop 0\n\t"
        "buffer_load_dwordx4 %[vm], %[rs], s92 offen offset:0 sc1 lds\n\t"
        "buffer_load_dwordx4 %[vm], %[rs], s92 offen offset:1024 sc1 lds\n\t"
        "buffer_load_dwordx4 %[vm], %[rs], s92 offen offset:2048 sc1 lds\n\t"
        "buffer_load_dwordx4 %[vm], %[rs], s92 offen offset:3072 sc1 lds\n\t"
        "s_add_u32 m0, m0, 0x1000\n\t"
        "s_nop 0\n\t"
        "buffer_load_dwordx4 %[vm], %[rs], s93 offen offset:0 sc1 lds\n\t"
        "buffer_load_dwordx4 %[vm], %[rs], s93 offen offset:2048 sc1 lds\n\t"
        "s_barrier\n\t"
        "Ltop%=:\n\t"
        "v_mov_b64 v[248:249], 0\n\t"
        "v_mov_b64 v[250:251], 0\n\t"
        "v_mov_b64 v[252:253], 0\n\t"
        "v_mov_b64 v[254:255], 0\n\t"
        "v_mov_b32 v200, 0xffbfffbf\n\t"
        "v_mov_b32 v206, %[lb]\n\t"
        "ds_write_b32 %[ow], v200\n\t"
        "ds_write_b32 %[ow], v200 offset:3072\n\t"
        "s_add_u32 %[rnd], %[rnd], 1\n\t"
        "s_setprio 3\n\t"
        "s_mov_b32 s95, 0\n\t"
        "s_waitcnt vmcnt(0)\n\t"
        "s_waitcnt lgkmcnt(0)\n\t"
        "s_barrier\n\t"
        "ds_read_b128 v[208:211], %[va] offset:0\n\t"
        "ds_read_b128 v[212:215], %[va] offset:1024\n\t"
        "ds_read_b128 v[216:219], %[va] offset:2048\n\t"
        "ds_read_b128 v[220:223], %[va] offset:3072\n\t"
        "ds_read_b128 v[224:227], %[va] offset:4096\n\t"
        "ds_read_b128 v[228:231], %[va] offset:5120\n\t"
        "s_waitcnt lgkmcnt(5)\n\t"
        "v_smfmac_f32_16x16x64_bf16 v[248:251], v[208:211], v[16:23], %[sidx]\n\t"
        "ds_read_b128 v[232:235], %[va] offset:6144\n\t"
        "v_smfmac_f32_16x16x64_bf16 v[252:255], v[208:211], v[96:103], %[sidx]\n\t"
        "s_waitcnt lgkmcnt(5)\n\t"
        "v_smfmac_f32_16x16x64_bf16 v[248:251], v[212:215], v[24:31], %[sidx]\n\t"
        "ds_read_b128 v[236:239], %[va] offset:7168\n\t"
        "v_smfmac_f32_16x16x64_bf16 v[252:255], v[212:215], v[104:111], %[sidx]\n\t"
        "s_waitcnt lgkmcnt(5)\n\t"
        "v_smfmac_f32_16x16x64_bf16 v[248:251], v[216:219], v[32:39], %[sidx]\n\t"
        "ds_read_b128 v[240:243], %[va] offset:8192\n\t"
        "v_smfmac_f32_16x16x64_bf16 v[252:255], v[216:219], v[112:119], %[sidx]\n\t"
        "s_waitcnt lgkmcnt(5)\n\t"
        "v_smfmac_f32_16x16x64_bf16 v[248:251], v[220:223], v[40:47], %[sidx]\n\t"
        "ds_read_b128 v[244:247], %[va] offset:10240\n\t"
        "v_smfmac_f32_16x16x64_bf16 v[252:255], v[220:223], v[120:127], %[sidx]\n\t"
        "s_waitcnt lgkmcnt(5)\n\t"
        "v_smfmac_f32_16x16x64_bf16 v[248:251], v[224:227], v[48:55], %[sidx]\n\t"
        "s_waitcnt lgkmcnt(4)\n\t"
        "v_smfmac_f32_16x16x64_bf16 v[248:251], v[228:231], v[56:63], %[sidx]\n\t"
        "s_waitcnt lgkmcnt(3)\n\t"
        "v_smfmac_f32_16x16x64_bf16 v[248:251], v[232:235], v[64:71], %[sidx]\n\t"
        "s_waitcnt lgkmcnt(2)\n\t"
        "v_smfmac_f32_16x16x64_bf16 v[248:251], v[236:239], v[72:79], %[sidx]\n\t"
        "s_waitcnt lgkmcnt(1)\n\t"
        "v_smfmac_f32_16x16x64_bf16 v[248:251], v[240:243], v[80:87], %[sidx]\n\t"
        "s_waitcnt lgkmcnt(0)\n\t"
        "v_smfmac_f32_16x16x64_bf16 v[248:251], v[244:247], v[88:95], %[sidx]\n\t"
        "v_smfmac_f32_16x16x64_bf16 v[252:255], v[224:227], v[128:135], %[sidx]\n\t"
        "v_smfmac_f32_16x16x64_bf16 v[252:255], v[228:231], v[136:143], %[sidx]\n\t"
        "v_add_f32 v248, v248, v250\n\t"
        "v_smfmac_f32_16x16x64_bf16 v[252:255], v[232:235], v[144:151], %[sidx]\n\t"
        "v_add_f32 v249, v249, v251\n\t"
        "v_smfmac_f32_16x16x64_bf16 v[252:255], v[236:239], v[152:159], %[sidx]\n\t"
        "v_cvt_pk_bf16_f32 v200, v248, v249\n\t"
        "v_smfmac_f32_16x16x64_bf16 v[252:255], v[240:243], v[160:167], %[sidx]\n\t"
        "ds_write_b32 %[ow], v200\n\t"
        "v_smfmac_f32_16x16x64_bf16 v[252:255], v[244:247], v[168:175], %[sidx]\n\t"
        "v_cmp_u_f32 vcc, v248, v249\n\t"
        "s_nop 6\n\t"
        "s_cbranch_vccnz Lnan%=\n\t"
        "Lok%=:\n\t"
        "v_add_f32 v252, v252, v254\n\t"
        "v_add_f32 v253, v253, v255\n\t"
        "v_cvt_pk_bf16_f32 v201, v252, v253\n\t"
        "ds_write_b32 %[ow], v201 offset:3072\n\t"
        "s_waitcnt lgkmcnt(0)\n\t"
        "s_cmp_lg_u32 s96, %[left]\n\t"
        "s_cbranch_scc1 Lv%=\n\t"
        "s_buffer_load_dwordx4 s[64:67], %[rs], %[tsr] offset:0x0\n\t"
        "s_buffer_load_dwordx4 s[68:71], %[rs], %[tsr] offset:0x80\n\t"
        "s_buffer_load_dwordx4 s[72:75], %[rs], %[tsr] offset:0x100\n\t"
        "s_buffer_load_dwordx4 s[76:79], %[rs], %[tsr] offset:0x180\n\t"
        "s_barrier\n\t"
        "s_setprio 0\n\t"
        "ds_read_b32 v207, v206 offset:25104\n\t"
        "s_waitcnt lgkmcnt(0)\n\t"
        "v_cmp_eq_u32 vcc, %[rnd], v207\n\t"
        "s_barrier\n\t"
        "s_cbranch_vccnz Lck%=\n\t"
        "s_xor_b32 s67, s67, %[t3]\n\t"
        "s_xor_b32 s71, s71, %[t3]\n\t"
        "s_xor_b32 s75, s75, %[t3]\n\t"
        "s_xor_b32 s79, s79, %[t3]\n\t"
        "s_sub_u32 s69, s69, s65\n\t"
        "s_sub_u32 s73, s73, s65\n\t"
        "s_sub_u32 s77, s77, s65\n\t"
        "s_max_i32 s69, s69, s73\n\t"
        "s_max_i32 s77, s77, 0\n\t"
        "s_max_i32 s69, s69, s77\n\t"
        "s_add_u32 s65, s65, s69\n\t"
        "s_or_b32 s66, s67, s71\n\t"
        "s_or_b32 s70, s75, s79\n\t"
        "s_or_b32 s70, s70, s66\n\t"
        "s_cmp_eq_u32 s70, 0\n\t"
        "s_cselect_b32 %[t3], 1, 0\n\t"
        "s_mov_b32 %[t1], s65\n\t"
        "s_branch Lend%=\n\t"
        "Lv%=:\n\t"
        "s_barrier\n\t"
        "buffer_load_dwordx4 v[196:199], %[tv], %[rs], %[tsr] offen sc1\n\t"
        "s_setprio 0\n\t"
        "ds_read_b32 v207, v206 offset:25104\n\t"
        "s_waitcnt lgkmcnt(0)\n\t"
        "v_cmp_eq_u32 vcc, %[rnd], v207\n\t"
        "s_barrier\n\t"
        "s_cbranch_vccnz Lck%=\n\t"
        "s_sub_u32 s97, %[r0], 0x100000\n\t"
        "s_waitcnt vmcnt(0)\n\t"
        "v_cmp_eq_u32 vcc, %[t3], v199\n\t"
        "v_subrev_u32 v196, s97, v196\n\t"
        "v_subrev_u32 v197, s97, v197\n\t"
        "v_cndmask_b32 v196, 0, v196, vcc\n\t"
        "v_cndmask_b32 v197, 0, v197, vcc\n\t"
        "v_cndmask_b32 v198, 0, v198, vcc\n\t"
        "v_max_u32_dpp v196, v196, v196 quad_perm:[1,0,3,2] row_mask:0xf bank_mask:0xf\n\t"
        "v_max_u32_dpp v197, v197, v197 quad_perm:[1,0,3,2] row_mask:0xf bank_mask:0xf\n\t"
        "v_max_u32_dpp v198, v198, v198 quad_perm:[1,0,3,2] row_mask:0xf bank_mask:0xf\n\t"
        "v_max_u32_dpp v196, v196, v196 quad_perm:[2,3,0,1] row_mask:0xf bank_mask:0xf\n\t"
        "v_max_u32_dpp v197, v197, v197 quad_perm:[2,3,0,1] row_mask:0xf bank_mask:0xf\n\t"
        "v_max_u32_dpp v198, v198, v198 quad_perm:[2,3,0,1] row_mask:0xf bank_mask:0xf\n\t"
        "v_max_u32_dpp v196, v196, v196 row_half_mirror row_mask:0xf bank_mask:0xf\n\t"
        "v_max_u32_dpp v197, v197, v197 row_half_mirror row_mask:0xf bank_mask:0xf\n\t"
        "v_max_u32_dpp v198, v198, v198 row_half_mirror row_mask:0xf bank_mask:0xf\n\t"
        "s_lshr_b32 s96, vcc_lo, 4\n\t"
        "v_readfirstlane_b32 %[t1], v197\n\t"
        "s_add_u32 %[t1], %[t1], s97\n\t"
        "s_or_b32 s96, s96, vcc_lo\n\t"
        "s_and_b32 s96, s96, 15\n\t"
        "s_cmp_eq_u32 s96, 15\n\t"
        "s_cselect_b32 %[t3], 1, 0\n\t"
        "s_branch Lend%=\n\t"
        "Lnan%=:\n\t"
        "s_cmp_lg_u32 %[poll], 1\n\t"
        "s_cbranch_scc1 Lok%=\n\t"
        "s_mov_b32 m0, %[lb]\n\t"
        "v_add_u32 v204, %[lb], %[vm]\n\t"
        "buffer_load_dwordx4 %[vm], %[rs], %[so] offen offset:0 sc1 lds\n\t"
        "buffer_load_dwordx4 %[vm], %[rs], %[so] offen offset:1024 sc1 lds\n\t"
        "buffer_load_dwordx4 %[vm], %[rs], %[so] offen offset:2048 sc1 lds\n\t"
        "buffer_load_dwordx4 %[vm], %[rs], %[so] offen offset:3072 sc1 lds\n\t"
        "s_add_u32 m0, m0, 0x1000\n\t"
        "s_nop 0\n\t"
        "buffer_load_dwordx4 %[vm], %[rs], s92 offen offset:0 sc1 lds\n\t"
        "buffer_load_dwordx4 %[vm], %[rs], s92 offen offset:1024 sc1 lds\n\t"
        "buffer_load_dwordx4 %[vm], %[rs], s92 offen offset:2048 sc1 lds\n\t"
        "buffer_load_dwordx4 %[vm], %[rs], s92 offen offset:3072 sc1 lds\n\t"
        "s_add_u32 m0, m0, 0x1000\n\t"
        "s_nop 0\n\t"
        "buffer_load_dwordx4 %[vm], %[rs], s93 offen offset:0 sc1 lds\n\t"
        "buffer_load_dwordx4 %[vm], %[rs], s93 offen offset:2048 sc1 lds\n\t"
        "v_max_u32 v207, v208, v209\n\t"
        "v_max3_u32 v207, v207, v210, v211\n\t"
        "v_max3_u32 v207, v207, v212, v213\n\t"
        "v_max3_u32 v207, v207, v214, v215\n\t"
        "v_max3_u32 v207, v207, v216, v217\n\t"
        "v_max3_u32 v207, v207, v218, v219\n\t"
        "v_max3_u32 v207, v207, v220, v221\n\t"
        "v_max3_u32 v207, v207, v222, v223\n\t"
        "v_max3_u32 v207, v207, v224, v225\n\t"
        "v_max3_u32 v207, v207, v226, v227\n\t"
        "v_max3_u32 v207, v207, v228, v229\n\t"
        "v_max3_u32 v207, v207, v230, v231\n\t"
        "v_max3_u32 v207, v207, v232, v233\n\t"
        "v_max3_u32 v207, v207, v234, v235\n\t"
        "v_max3_u32 v207, v207, v236, v237\n\t"
        "v_max3_u32 v207, v207, v238, v239\n\t"
        "v_max3_u32 v207, v207, v240, v241\n\t"
        "v_max3_u32 v207, v207, v242, v243\n\t"
        "v_max3_u32 v207, v207, v244, v245\n\t"
        "v_max3_u32 v207, v207, v246, v247\n\t"
        "v_cmp_eq_u32 vcc, 0xffbfffbf, v207\n\t"
        "v_mov_b32 v207, %[rnd]\n\t"
        "s_cbranch_vccz Lok%=\n\t"
        "ds_write_b32 v206, v207 offset:25104\n\t"
        "s_mov_b32 s95, 1\n\t"
        "s_waitcnt lgkmcnt(0)\n\t"
        "s_branch Lcv%=\n\t"
        "Lck%=:\n\t"
        "v_add_u32 v204, %[lb], %[vm]\n\t"
        "Lcl%=:\n\t"
        "s_mov_b32 m0, %[lb]\n\t"
        "s_nop 0\n\t"
        "buffer_load_dwordx4 %[vm], %[rs], %[so] offen offset:0 sc1 lds\n\t"
        "buffer_load_dwordx4 %[vm], %[rs], %[so] offen offset:1024 sc1 lds\n\t"
        "buffer_load_dwordx4 %[vm], %[rs], %[so] offen offset:2048 sc1 lds\n\t"
        "buffer_load_dwordx4 %[vm], %[rs], %[so] offen offset:3072 sc1 lds\n\t"
        "s_add_u32 m0, m0, 0x1000\n\t"
        "s_nop 0\n\t"
        "buffer_load_dwordx4 %[vm], %[rs], s92 offen offset:0 sc1 lds\n\t"
        "buffer_load_dwordx4 %[vm], %[rs], s92 offen offset:1024 sc1 lds\n\t"
        "buffer_load_dwordx4 %[vm], %[rs], s92 offen offset:2048 sc1 lds\n\t"
        "buffer_load_dwordx4 %[vm], %[rs], s92 offen offset:3072 sc1 lds\n\t"
        "s_add_u32 m0, m0, 0x1000\n\t"
        "s_nop 0\n\t"
        "buffer_load_dwordx4 %[vm], %[rs], s93 offen offset:0 sc1 lds\n\t"
        "buffer_load_dwordx4 %[vm], %[rs], s93 offen offset:2048 sc1 lds\n\t"
        "Lcv%=:\n\t"
        "s_sub_u32 %[left], %[left], 1\n\t"
        "s_waitcnt vmcnt(9)\n\t"
        "ds_read_b128 v[208:211], v204 offset:0\n\t"
        "s_waitcnt vmcnt(8)\n\t"
        "ds_read_b128 v[212:215], v204 offset:1024\n\t"
        "s_waitcnt vmcnt(7)\n\t"
        "ds_read_b128 v[216:219], v204 offset:2048\n\t"
        "s_waitcnt vmcnt(6)\n\t"
        "ds_read_b128 v[220:223], v204 offset:3072\n\t"
        "s_waitcnt vmcnt(5)\n\t"
        "ds_read_b128 v[224:227], v204 offset:4096\n\t"
        "s_waitcnt vmcnt(4)\n\t"
        "ds_read_b128 v[228:231], v204 offset:5120\n\t"
        "s_waitcnt lgkmcnt(5)\n\t"
        "v_max3_u32 v207, v208, v208, v209\n\t"
        "v_max3_u32 v207, v207, v210, v211\n\t"
        "s_waitcnt vmcnt(3)\n\t"
        "ds_read_b128 v[232:235], v204 offset:6144\n\t"
        "s_waitcnt lgkmcnt(5)\n\t"
        "v_max3_u32 v207, v207, v212, v213\n\t"
        "v_max3_u32 v207, v207, v214, v215\n\t"
        "s_waitcnt vmcnt(2)\n\t"
        "ds_read_b128 v[236:239], v204 offset:7168\n\t"
        "s_waitcnt lgkmcnt(5)\n\t"
        "v_max3_u32 v207, v207, v216, v217\n\t"
        "v_max3_u32 v207, v207, v218, v219\n\t"
        "s_waitcnt vmcnt(1)\n\t"
        "ds_read_b128 v[240:243], v204 offset:8192\n\t"
        "s_waitcnt lgkmcnt(5)\n\t"
        "v_max3_u32 v207, v207, v220, v221\n\t"
        "v_max3_u32 v207, v207, v222, v223\n\t"
        "s_waitcnt vmcnt(0)\n\t"
        "ds_read_b128 v[244:247], v204 offset:10240\n\t"
        "s_waitcnt lgkmcnt(5)\n\t"
        "v_max3_u32 v207, v207, v224, v225\n\t"
        "v_max3_u32 v207, v207, v226, v227\n\t"
        "s_waitcnt lgkmcnt(4)\n\t"
        "v_max3_u32 v207, v207, v228, v229\n\t"
        "v_max3_u32 v207, v207, v230, v231\n\t"
        "s_waitcnt lgkmcnt(3)\n\t"
        "v_max3_u32 v207, v207, v232, v233\n\t"
        "v_max3_u32 v207, v207, v234, v235\n\t"
        "s_waitcnt lgkmcnt(2)\n\t"
        "v_max3_u32 v207, v207, v236, v237\n\t"
        "v_max3_u32 v207, v207, v238, v239\n\t"
        "s_waitcnt lgkmcnt(1)\n\t"
        "v_max3_u32 v207, v207, v240, v241\n\t"
        "v_max3_u32 v207, v207, v242, v243\n\t"
        "s_waitcnt lgkmcnt(0)\n\t"
        "v_max3_u32 v207, v207, v244, v245\n\t"
        "v_max3_u32 v207, v207, v246, v247\n\t"
        "v_cmp_eq_u32 vcc, 0xffbfffbf, v207\n\t"
        "s_cbranch_vccz Lcd%=\n\t"
        "s_cbranch_scc0 Lcl%=\n\t"
        "Lcd%=:\n\t"
        "s_cmp_eq_u32 s95, 0\n\t"
        "s_cbranch_scc1 Lce%=\n\t"
        "s_barrier\n\t"
        "s_barrier\n\t"
        "Lce%=:\n\t"
        "s_cbranch_vccz Ltop%=\n\t"
        "Lend%=:\n\t"
        "s_mov_b32 m0, s94\n\t"
        "s_waitcnt vmcnt(0)\n\t"
        : [rnd] "+&s"(rnd), [c0] "+&s"(c0), [r0] "+&s"(r0), [t1] "+&s"(t1), [t3] "+&s"(t3), [left] "+&s"(left)
        : [w0] "{v[16:47]}"(W[0]), [w1] "{v[48:79]}"(W[1]), [w2] "{v[80:111]}"(W[2]), [w3] "{v[112:143]}"(W[3]), [w4] "{v[144:175]}"(W[4]), [va] "v"(va), [sidx] "v"(sidx), [ow] "v"(ow), [rs] "s"(rs), [poll] "s"(poll), [tg] "s"(tg), [kr] "s"(kr), [tsr] "s"(tsr), [vm] "v"(vm), [so] "s"(so), [lb] "s"(lb), [md] "s"(md), [tv] "v"(tv)
        : "s64", "s65", "s66", "s67", "s68", "s69", "s70", "s71", "s72", "s73", "s74", "s75", "s76", "s77", "s78", "s79", "s80", "s81", "s82", "s83", "s84", "s85", "s86", "s87", "s88", "s89", "s90", "s91", "s92", "s93", "s94", "s95", "s96", "s97", "v196", "v197", "v198", "v199", "v200", "v201", "v202", "v203", "v204", "v205", "v206", "v207", "v208", "v209", "v210", "v211", "v212", "v213", "v214", "v215", "v216", "v217", "v218", "v219", "v220", "v221", "v222", "v223", "v224", "v225", "v226", "v227", "v228", "v229", "v230", "v231", "v232", "v233", "v234", "v235", "v236", "v237", "v238", "v239", "v240", "v241", "v242", "v243", "v244", "v245", "v246", "v247", "v248", "v249", "v250", "v251", "v252", "v253", "v254", "v255", "vcc", "scc", "memory");
}

__device__ __forceinline__ void gather(u32 tg, u32 kr, u32 vm, u32 so, u32 lb, u32x4 rs) {
    asm volatile(
        "s_mov_b32 s94, m0\n\t"
        "s_add_u32 s92, %[so], 0x8000\n\t"
        "s_add_u32 s93, %[so], 0x10000\n\t"
        "s_mov_b32 m0, %[lb]\n\t"
        "Lda_%=:\n\t"
        "s_memrealtime s[90:91]\n\t"
        "s_waitcnt lgkmcnt(0)\n\t"
        "s_mul_i32 s90, s90, 24\n\t"
        "s_sub_u32 s88, %[tg], s90\n\t"
        "s_sub_u32 s89, s88, 620\n\t"
        "s_cmp_gt_u32 s89, 0x4000\n\t"
        "s_cbranch_scc1 Lea_%=\n\t"
        "s_sleep 1\n\t"
        "s_branch Lda_%=\n\t"
        "Lea_%=:\n\t"
        "s_cmp_lt_i32 s88, 120\n\t"
        "s_cbranch_scc1 Lza_%=\n\t"
        "s_memrealtime s[64:65]\n\t"
        "s_nop 0\n\t"
        "s_memrealtime s[66:67]\n\t"
        "s_nop 0\n\t"
        "s_memrealtime s[68:69]\n\t"
        "s_nop 0\n\t"
        "s_memrealtime s[70:71]\n\t"
        "s_nop 0\n\t"
        "s_memrealtime s[72:73]\n\t"
        "s_nop 0\n\t"
        "s_memrealtime s[74:75]\n\t"
        "s_memtime s[90:91]\n\t"
        "s_memrealtime s[76:77]\n\t"
        "s_nop 0\n\t"
        "s_memrealtime s[78:79]\n\t"
        "s_nop 0\n\t"
        "s_memrealtime s[80:81]\n\t"
        "s_nop 0\n\t"
        "s_memrealtime s[82:83]\n\t"
        "s_nop 0\n\t"
        "s_memrealtime s[84:85]\n\t"
        "s_nop 0\n\t"
        "s_memrealtime s[86:87]\n\t"
        "s_nop 0\n\t"
        "s_nop 7\n\t"
        "s_waitcnt lgkmcnt(0)\n\t"
        "s_add_u32 s64, s64, s66\n\t"
        "s_add_u32 s64, s64, s68\n\t"
        "s_add_u32 s64, s64, s70\n\t"
        "s_add_u32 s64, s64, s72\n\t"
        "s_add_u32 s64, s64, s74\n\t"
        "s_add_u32 s64, s64, s76\n\t"
        "s_add_u32 s64, s64, s78\n\t"
        "s_add_u32 s64, s64, s80\n\t"
        "s_add_u32 s64, s64, s82\n\t"
        "s_add_u32 s64, s64, s84\n\t"
        "s_add_u32 s64, s64, s86\n\t"
        "s_lshl_b32 s64, s64, 1\n\t"
        "s_nop 1\n\t"
        "s_sub_u32 s88, %[tg], s64\n\t"
        "s_mul_i32 s88, s88, %[kr]\n\t"
        "s_ashr_i32 s88, s88, 12\n\t"
        "s_sub_u32 s88, s88, 244\n\t"
        "s_nop 1\n\t"
        "s_max_i32 s88, s88, 0\n\t"
        "s_min_i32 s88, s88, 384\n\t"
        "s_andn2_b32 s88, s88, 3\n\t"
        "s_getpc_b64 s[90:91]\n\t"
        "Lga_%=:\n\t"
        "s_sub_u32 s90, s90, s88\n\t"
        "s_subb_u32 s91, s91, 0\n\t"
        "s_add_u32 s90, s90, Lza_%=-Lga_%=\n\t"
        "s_addc_u32 s91, s91, 0\n\t"
        "s_setpc_b64 s[90:91]\n\t"
        "s_nop 0\n\ts_nop 0\n\ts_nop 0\n\ts_nop 0\n\t"
        "s_nop 0\n\ts_nop 0\n\ts_nop 0\n\ts_nop 0\n\t"
        "s_nop 0\n\ts_nop 0\n\ts_nop 0\n\ts_nop 0\n\t"
        "s_nop 0\n\ts_nop 0\n\ts_nop 0\n\ts_nop 0\n\t"
        "s_nop 0\n\ts_nop 0\n\ts_nop 0\n\ts_nop 0\n\t"
        "s_nop 0\n\ts_nop 0\n\ts_nop 0\n\ts_nop 0\n\t"
        "s_nop 0\n\ts_nop 0\n\ts_nop 0\n\ts_nop 0\n\t"
        "s_nop 0\n\ts_nop 0\n\ts_nop 0\n\ts_nop 0\n\t"
        "s_nop 0\n\ts_nop 0\n\ts_nop 0\n\ts_nop 0\n\t"
        "s_nop 0\n\ts_nop 0\n\ts_nop 0\n\ts_nop 0\n\t"
        "s_nop 0\n\ts_nop 0\n\ts_nop 0\n\ts_nop 0\n\t"
        "s_nop 0\n\ts_nop 0\n\ts_nop 0\n\ts_nop 0\n\t"
        "s_nop 0\n\ts_nop 0\n\ts_nop 0\n\ts_nop 0\n\t"
        "s_nop 0\n\ts_nop 0\n\ts_nop 0\n\ts_nop 0\n\t"
        "s_nop 0\n\ts_nop 0\n\ts_nop 0\n\ts_nop 0\n\t"
        "s_nop 0\n\ts_nop 0\n\ts_nop 0\n\ts_nop 0\n\t"
        "s_nop 0\n\ts_nop 0\n\ts_nop 0\n\ts_nop 0\n\t"
        "s_nop 0\n\ts_nop 0\n\ts_nop 0\n\ts_nop 0\n\t"
        "s_nop 0\n\ts_nop 0\n\ts_nop 0\n\ts_nop 0\n\t"
        "s_nop 0\n\ts_nop 0\n\ts_nop 0\n\ts_nop 0\n\t"
        "s_nop 0\n\ts_nop 0\n\ts_nop 0\n\ts_nop 0\n\t"
        "s_nop 0\n\ts_nop 0\n\ts_nop 0\n\ts_nop 0\n\t"
        "s_nop 0\n\ts_nop 0\n\ts_nop 0\n\ts_nop 0\n\t"
        "s_nop 0\n\ts_nop 0\n\ts_nop 0\n\ts_nop 0\n\t"
        "Lza_%=:\n\t"
        "buffer_load_dwordx4 %[vm], %[rs], %[so] offen offset:0 sc1 lds\n\t"
        "buffer_load_dwordx4 %[vm], %[rs], %[so] offen offset:1024 sc1 lds\n\t"
        "buffer_load_dwordx4 %[vm], %[rs], %[so] offen offset:2048 sc1 lds\n\t"
        "buffer_load_dwordx4 %[vm], %[rs], %[so] offen offset:3072 sc1 lds\n\t"
        "s_add_u32 m0, m0, 0x1000\n\t"
        "s_nop 0\n\t"
        "buffer_load_dwordx4 %[vm], %[rs], s92 offen offset:0 sc1 lds\n\t"
        "buffer_load_dwordx4 %[vm], %[rs], s92 offen offset:1024 sc1 lds\n\t"
        "buffer_load_dwordx4 %[vm], %[rs], s92 offen offset:2048 sc1 lds\n\t"
        "buffer_load_dwordx4 %[vm], %[rs], s92 offen offset:3072 sc1 lds\n\t"
        "s_add_u32 m0, m0, 0x1000\n\t"
        "s_nop 0\n\t"
        "buffer_load_dwordx4 %[vm], %[rs], s93 offen offset:0 sc1 lds\n\t"
        "buffer_load_dwordx4 %[vm], %[rs], s93 offen offset:2048 sc1 lds\n\t"
        "s_mov_b32 m0, s94\n\t"
        :
        : [rs] "s"(rs), [tg] "s"(tg), [kr] "s"(kr), [vm] "v"(vm), [so] "s"(so), [lb] "s"(lb)
        : "s64", "s65", "s66", "s67", "s68", "s69", "s70", "s71", "s72", "s73", "s74", "s75", "s76", "s77", "s78", "s79", "s80", "s81", "s82", "s83", "s84", "s85", "s86", "s87", "s88", "s89", "s90", "s91", "s92", "s93", "s94", "vcc", "scc", "memory");
}

__device__ __forceinline__ void ts_look(u32& t1, u32& ok, u32x4 rs, u32 tsr, u32 want) {
    asm volatile(
        "s_nop 4\n\t"
        "s_buffer_load_dwordx4 s[64:67], %[rs], %[tsr] offset:0x0\n\t"
        "s_buffer_load_dwordx4 s[68:71], %[rs], %[tsr] offset:0x80\n\t"
        "s_buffer_load_dwordx4 s[72:75], %[rs], %[tsr] offset:0x100\n\t"
        "s_buffer_load_dwordx4 s[76:79], %[rs], %[tsr] offset:0x180\n\t"
        "s_waitcnt lgkmcnt(0)\n\t"
        "s_xor_b32 s67, s67, %[want]\n\t"
        "s_xor_b32 s71, s71, %[want]\n\t"
        "s_xor_b32 s75, s75, %[want]\n\t"
        "s_xor_b32 s79, s79, %[want]\n\t"
        "s_sub_u32 s69, s69, s65\n\t"
        "s_sub_u32 s73, s73, s65\n\t"
        "s_sub_u32 s77, s77, s65\n\t"
        "s_max_i32 s69, s69, s73\n\t"
        "s_max_i32 s77, s77, 0\n\t"
        "s_max_i32 s69, s69, s77\n\t"
        "s_add_u32 s65, s65, s69\n\t"
        "s_or_b32 s66, s67, s71\n\t"
        "s_or_b32 s70, s75, s79\n\t"
        "s_or_b32 s70, s70, s66\n\t"
        "s_cmp_eq_u32 s70, 0\n\t"
        "s_cselect_b32 %[ok], 15, 0\n\t"
        "s_mov_b32 %[t1], s65\n\t"
        : [t1] "=&s"(t1), [ok] "=&s"(ok)
        : [rs] "s"(rs), [tsr] "s"(tsr), [want] "s"(want)
        : "s64", "s65", "s66", "s67", "s68", "s69", "s70", "s71", "s72", "s73", "s74", "s75", "s76", "s77", "s78", "s79", "vcc", "scc", "memory");
}

__device__ __forceinline__ void pull(u32 vo, u32 tb, u32x4 rs, u32 n, u32 gap, u32 ld) {
    asm volatile(
        "s_mov_b32 s94, m0\n\t"
        "s_mov_b32 m0, %[ld]\n\t"
        "s_mov_b32 s92, 0\n\t"
        "Lp%=:\n\t"
        "v_readlane_b32 s93, %[tb], s92\n\t"
        "s_getpc_b64 s[90:91]\n\t"
        "Lgg_%=:\n\t"
        "s_sub_u32 s90, s90, %[gap]\n\t"
        "s_subb_u32 s91, s91, 0\n\t"
        "s_add_u32 s90, s90, Lzg_%=-Lgg_%=\n\t"
        "s_addc_u32 s91, s91, 0\n\t"
        "s_setpc_b64 s[90:91]\n\t"
        "s_nop 0\n\ts_nop 0\n\ts_nop 0\n\ts_nop 0\n\t"
        "s_nop 0\n\ts_nop 0\n\ts_nop 0\n\ts_nop 0\n\t"
        "s_nop 0\n\ts_nop 0\n\ts_nop 0\n\ts_nop 0\n\t"
        "s_nop 0\n\ts_nop 0\n\ts_nop 0\n\ts_nop 0\n\t"
        "s_nop 0\n\ts_nop 0\n\ts_nop 0\n\ts_nop 0\n\t"
        "s_nop 0\n\ts_nop 0\n\ts_nop 0\n\ts_nop 0\n\t"
        "s_nop 0\n\ts_nop 0\n\ts_nop 0\n\ts_nop 0\n\t"
        "s_nop 0\n\ts_nop 0\n\ts_nop 0\n\ts_nop 0\n\t"
        "s_nop 0\n\ts_nop 0\n\ts_nop 0\n\ts_nop 0\n\t"
        "s_nop 0\n\ts_nop 0\n\ts_nop 0\n\ts_nop 0\n\t"
        "s_nop 0\n\ts_nop 0\n\ts_nop 0\n\ts_nop 0\n\t"
        "s_nop 0\n\ts_nop 0\n\ts_nop 0\n\ts_nop 0\n\t"
        "s_nop 0\n\ts_nop 0\n\ts_nop 0\n\ts_nop 0\n\t"
        "s_nop 0\n\ts_nop 0\n\ts_nop 0\n\ts_nop 0\n\t"
        "s_nop 0\n\ts_nop 0\n\ts_nop 0\n\ts_nop 0\n\t"
        "s_nop 0\n\ts_nop 0\n\ts_nop 0\n\ts_nop 0\n\t"
        "Lzg_%=:\n\t"
        "buffer_load_dword %[vo], %[rs], s93 offen sc1 lds\n\t"
        "s_add_u32 s92, s92, 1\n\t"
        "s_cmp_lt_u32 s92, %[n]\n\t"
        "s_cbranch_scc1 Lp%=\n\t"
        "s_mov_b32 m0, s94\n\t"
        :
        : [vo] "v"(vo), [tb] "v"(tb), [rs] "s"(rs), [n] "s"(n), [gap] "s"(gap), [ld] "s"(ld)
        : "s88", "s89", "s90", "s91", "s92", "s93", "s94", "vcc", "scc", "memory");
}

__device__ __forceinline__ void calib(u32& c0, u32& r0) {
    asm volatile(
        "s_memrealtime s[64:65]\n\t"
        "s_nop 0\n\t"
        "s_memrealtime s[66:67]\n\t"
        "s_nop 0\n\t"
        "s_memrealtime s[68:69]\n\t"
        "s_nop 0\n\t"
        "s_memrealtime s[70:71]\n\t"
        "s_nop 0\n\t"
        "s_memrealtime s[72:73]\n\t"
        "s_nop 0\n\t"
        "s_memrealtime s[74:75]\n\t"
        "s_memtime s[90:91]\n\t"
        "s_memrealtime s[76:77]\n\t"
        "s_nop 0\n\t"
        "s_memrealtime s[78:79]\n\t"
        "s_nop 0\n\t"
        "s_memrealtime s[80:81]\n\t"
        "s_nop 0\n\t"
        "s_memrealtime s[82:83]\n\t"
        "s_nop 0\n\t"
        "s_memrealtime s[84:85]\n\t"
        "s_nop 0\n\t"
        "s_memrealtime s[86:87]\n\t"
        "s_nop 0\n\t"
        "s_nop 7\n\t"
        "s_waitcnt lgkmcnt(0)\n\t"
        "s_add_u32 s64, s64, s66\n\t"
        "s_add_u32 s64, s64, s68\n\t"
        "s_add_u32 s64, s64, s70\n\t"
        "s_add_u32 s64, s64, s72\n\t"
        "s_add_u32 s64, s64, s74\n\t"
        "s_add_u32 s64, s64, s76\n\t"
        "s_add_u32 s64, s64, s78\n\t"
        "s_add_u32 s64, s64, s80\n\t"
        "s_add_u32 s64, s64, s82\n\t"
        "s_add_u32 s64, s64, s84\n\t"
        "s_add_u32 s64, s64, s86\n\t"
        "s_lshl_b32 s64, s64, 1\n\t"
        "s_mov_b32 %[c0], s90\n\t"
        "s_mov_b32 %[r0], s64\n\t"
        : [c0] "=&s"(c0), [r0] "=&s"(r0)
        :
        : "s64", "s65", "s66", "s67", "s68", "s69", "s70", "s71", "s72", "s73", "s74", "s75", "s76", "s77", "s78", "s79", "s80", "s81", "s82", "s83", "s84", "s85", "s86", "s87", "s88", "s89", "s90", "s91", "vcc", "scc", "memory");
}

__device__ __forceinline__ void px_step(u32x4& p0, u32& p1, u32 ka, u32 qa, u32 ma, u32 ktr, float ce) {
    asm volatile(
        "ds_read_b128 v[240:243], %[ma] offset:0\n\t"
        "ds_read_b128 v[244:247], %[ma] offset:1024\n\t"
        "ds_read_b128 v[208:211], %[qa] offset:0\n\t"
        "ds_read_b128 v[224:227], %[ka] offset:0\n\t"
        "ds_read_b128 v[212:215], %[qa] offset:1024\n\t"
        "ds_read_b128 v[228:231], %[ka] offset:1024\n\t"
        "ds_read_b128 v[216:219], %[qa] offset:2048\n\t"
        "ds_read_b128 v[232:235], %[ka] offset:2048\n\t"
        "ds_read_b128 v[220:223], %[qa] offset:3072\n\t"
        "ds_read_b128 v[236:239], %[ka] offset:3072\n\t"
        "s_waitcnt lgkmcnt(6)\n\t"
        "v_mfma_f32_16x16x32_bf16 v[240:243], v[224:227], v[208:211], v[240:243]\n\t"
        "ds_read_b128 v[224:227], %[ka] offset:4096\n\t"
        "s_waitcnt lgkmcnt(5)\n\t"
        "v_mfma_f32_16x16x32_bf16 v[240:243], v[228:231], v[212:215], v[240:243]\n\t"
        "ds_read_b128 v[228:231], %[ka] offset:5120\n\t"
        "s_waitcnt lgkmcnt(4)\n\t"
        "v_mfma_f32_16x16x32_bf16 v[240:243], v[232:235], v[216:219], v[240:243]\n\t"
        "ds_read_b128 v[232:235], %[ka] offset:6144\n\t"
        "s_waitcnt lgkmcnt(3)\n\t"
        "v_mfma_f32_16x16x32_bf16 v[240:243], v[236:239], v[220:223], v[240:243]\n\t"
        "ds_read_b128 v[236:239], %[ka] offset:7168\n\t"
        "s_waitcnt lgkmcnt(3)\n\t"
        "v_mfma_f32_16x16x32_bf16 v[244:247], v[224:227], v[208:211], v[244:247]\n\t"
        "ds_read_b64_tr_b16 v[224:225], %[ktr] offset:0\n\t"
        "ds_read_b64_tr_b16 v[226:227], %[ktr] offset:768\n\t"
        "s_waitcnt lgkmcnt(4)\n\t"
        "v_mfma_f32_16x16x32_bf16 v[244:247], v[228:231], v[212:215], v[244:247]\n\t"
        "ds_read_b64_tr_b16 v[228:229], %[ktr] offset:1536\n\t"
        "ds_read_b64_tr_b16 v[230:231], %[ktr] offset:2304\n\t"
        "s_waitcnt lgkmcnt(5)\n\t"
        "v_mfma_f32_16x16x32_bf16 v[244:247], v[232:235], v[216:219], v[244:247]\n\t"
        "ds_read_b64_tr_b16 v[232:233], %[ktr] offset:3072\n\t"
        "ds_read_b64_tr_b16 v[234:235], %[ktr] offset:3840\n\t"
        "s_waitcnt lgkmcnt(6)\n\t"
        "v_mfma_f32_16x16x32_bf16 v[244:247], v[236:239], v[220:223], v[244:247]\n\t"
        "ds_read_b64_tr_b16 v[236:237], %[ktr] offset:4608\n\t"
        "ds_read_b64_tr_b16 v[238:239], %[ktr] offset:5376\n\t"
        "s_waitcnt lgkmcnt(6)\n\t"
        "v_mfma_f32_16x16x32_bf16 v[248:251], v[224:227], v[208:211], 0\n\t"
        "s_waitcnt lgkmcnt(4)\n\t"
        "v_mfma_f32_16x16x32_bf16 v[248:251], v[228:231], v[212:215], v[248:251]\n\t"
        "s_waitcnt lgkmcnt(2)\n\t"
        "v_mfma_f32_16x16x32_bf16 v[248:251], v[232:235], v[216:219], v[248:251]\n\t"
        "s_waitcnt lgkmcnt(0)\n\t"
        "v_mfma_f32_16x16x32_bf16 v[248:251], v[236:239], v[220:223], v[248:251]\n\t"
        "v_max3_f32 v253, v240, v241, v242\n\t"
        "v_max3_f32 v253, v253, v243, v244\n\t"
        "v_max3_f32 v253, v253, v245, v246\n\t"
        "v_max_f32 v253, v253, v247\n\t"
        "s_nop 3\n\t"
        "v_mul_f32 v248, %[ce], v248\n\t"
        "v_mul_f32 v249, %[ce], v249\n\t"
        "v_max3_f32 v253, v253, v248, v249\n\t"
        "v_mov_b32 v254, v253\n\t"
        "s_nop 1\n\t"
        "v_permlane16_swap_b32 v253, v254\n\t"
        "v_max_f32 v253, v253, v254\n\t"
        "v_mov_b32 v254, v253\n\t"
        "s_nop 1\n\t"
        "v_permlane32_swap_b32 v253, v254\n\t"
        "v_max_f32 v253, v253, v254\n\t"
        "v_sub_f32 v240, v240, v253\n\t"
        "v_sub_f32 v241, v241, v253\n\t"
        "v_sub_f32 v242, v242, v253\n\t"
        "v_sub_f32 v243, v243, v253\n\t"
        "v_sub_f32 v244, v244, v253\n\t"
        "v_sub_f32 v245, v245, v253\n\t"
        "v_sub_f32 v246, v246, v253\n\t"
        "v_sub_f32 v247, v247, v253\n\t"
        "v_sub_f32 v248, v248, v253\n\t"
        "v_sub_f32 v249, v249, v253\n\t"
        "v_exp_f32 v240, v240\n\t"
        "v_exp_f32 v241, v241\n\t"
        "v_exp_f32 v242, v242\n\t"
        "v_exp_f32 v243, v243\n\t"
        "v_exp_f32 v244, v244\n\t"
        "v_exp_f32 v245, v245\n\t"
        "v_exp_f32 v246, v246\n\t"
        "v_exp_f32 v247, v247\n\t"
        "v_exp_f32 v248, v248\n\t"
        "v_exp_f32 v249, v249\n\t"
        "v_pk_add_f32 v[252:253], v[240:241], v[242:243]\n\t"
        "v_pk_add_f32 v[254:255], v[244:245], v[246:247]\n\t"
        "v_pk_add_f32 v[252:253], v[252:253], v[248:249]\n\t"
        "v_pk_add_f32 v[252:253], v[252:253], v[254:255]\n\t"
        "v_add_f32 v252, v252, v253\n\t"
        "v_mov_b32 v253, v252\n\t"
        "s_nop 1\n\t"
        "v_permlane16_swap_b32 v252, v253\n\t"
        "v_add_f32 v252, v252, v253\n\t"
        "v_mov_b32 v253, v252\n\t"
        "s_nop 1\n\t"
        "v_permlane32_swap_b32 v252, v253\n\t"
        "v_add_f32 v252, v252, v253\n\t"
        "v_rcp_f32 v252, v252\n\t"
        "s_nop 0\n\t"
        "v_pk_mul_f32 v[240:241], v[240:241], v[252:253] op_sel_hi:[1,0]\n\t"
        "v_pk_mul_f32 v[242:243], v[242:243], v[252:253] op_sel_hi:[1,0]\n\t"
        "v_pk_mul_f32 v[244:245], v[244:245], v[252:253] op_sel_hi:[1,0]\n\t"
        "v_pk_mul_f32 v[246:247], v[246:247], v[252:253] op_sel_hi:[1,0]\n\t"
        "v_pk_mul_f32 v[248:249], v[248:249], v[252:253] op_sel_hi:[1,0]\n\t"
        "v_cvt_pk_bf16_f32 v192, v240, v241\n\t"
        "v_cvt_pk_bf16_f32 v193, v242, v243\n\t"
        "v_cvt_pk_bf16_f32 v194, v244, v245\n\t"
        "v_cvt_pk_bf16_f32 v195, v246, v247\n\t"
        "v_cvt_pk_bf16_f32 v196, v248, v249\n\t"
        : [p0] "=&{v[192:195]}"(p0), [p1] "=&{v196}"(p1)
        : [ka] "v"(ka), [qa] "v"(qa), [ma] "v"(ma), [ktr] "v"(ktr), [ce] "s"(ce)
        : "v200", "v201", "v202", "v203", "v204", "v205", "v206", "v207", "v208", "v209", "v210", "v211", "v212", "v213", "v214", "v215", "v216", "v217", "v218", "v219", "v220", "v221", "v222", "v223", "v224", "v225", "v226", "v227", "v228", "v229", "v230", "v231", "v232", "v233", "v234", "v235", "v236", "v237", "v238", "v239", "v240", "v241", "v242", "v243", "v244", "v245", "v246", "v247", "v248", "v249", "v250", "v251", "v252", "v253", "v254", "v255", "vcc", "scc", "memory");
}

__device__ __forceinline__ void v_a0(const u32x32 (&W)[5], u32& rnd, u32 va, u32 sidx, u32 vw, u32 lb STP) {
    asm volatile(
        "Ltop%=:\n\t"
        "v_mov_b64 v[248:249], 0\n\t"
        "v_mov_b64 v[250:251], 0\n\t"
        "v_mov_b64 v[252:253], 0\n\t"
        "v_mov_b64 v[254:255], 0\n\t"
        "v_mov_b32 v200, 0xffbfffbf\n\t"
        "v_mov_b32 v206, %[lb]\n\t"
        "ds_write_b32 %[vw], v200\n\t"
        "ds_write_b32 %[vw], v200 offset:1024\n\t"
        "s_add_u32 %[rnd], %[rnd], 1\n\t"
        "s_setprio 3\n\t"
        "s_waitcnt lgkmcnt(0)\n\t"
        "s_barrier\n\t"
        "ds_read_b128 v[208:211], %[va] offset:0\n\t"
        "ds_read_b128 v[212:215], %[va] offset:1024\n\t"
        "ds_read_b128 v[216:219], %[va] offset:2048\n\t"
        "ds_read_b128 v[220:223], %[va] offset:3072\n\t"
        "ds_read_b128 v[224:227], %[va] offset:4096\n\t"
        "ds_read_b128 v[228:231], %[va] offset:5120\n\t"
        "s_waitcnt lgkmcnt(5)\n\t"
        "v_smfmac_f32_16x16x64_bf16 v[248:251], v[208:211], v[16:23], %[sidx]\n\t"
        "ds_read_b128 v[232:235], %[va] offset:6144\n\t"
        "v_smfmac_f32_16x16x64_bf16 v[252:255], v[208:211], v[96:103], %[sidx]\n\t"
        "s_waitcnt lgkmcnt(5)\n\t"
        "v_smfmac_f32_16x16x64_bf16 v[248:251], v[212:215], v[24:31], %[sidx]\n\t"
        "ds_read_b128 v[236:239], %[va] offset:7168\n\t"
        "v_smfmac_f32_16x16x64_bf16 v[252:255], v[212:215], v[104:111], %[sidx]\n\t"
        "s_waitcnt lgkmcnt(5)\n\t"
        "v_smfmac_f32_16x16x64_bf16 v[248:251], v[216:219], v[32:39], %[sidx]\n\t"
        "ds_read_b128 v[240:243], %[va] offset:8192\n\t"
        "s_waitcnt lgkmcnt(5)\n\t"
        "v_smfmac_f32_16x16x64_bf16 v[248:251], v[220:223], v[40:47], %[sidx]\n\t"
        "ds_read_b128 v[244:247], %[va] offset:10240\n\t"
        "s_waitcnt lgkmcnt(5)\n\t"
        "v_smfmac_f32_16x16x64_bf16 v[248:251], v[224:227], v[48:55], %[sidx]\n\t"
        "s_waitcnt lgkmcnt(4)\n\t"
        "v_smfmac_f32_16x16x64_bf16 v[248:251], v[228:231], v[56:63], %[sidx]\n\t"
        "s_waitcnt lgkmcnt(3)\n\t"
        "v_smfmac_f32_16x16x64_bf16 v[248:251], v[232:235], v[64:71], %[sidx]\n\t"
        "s_waitcnt lgkmcnt(2)\n\t"
        "v_smfmac_f32_16x16x64_bf16 v[248:251], v[236:239], v[72:79], %[sidx]\n\t"
        "s_waitcnt lgkmcnt(1)\n\t"
        "v_smfmac_f32_16x16x64_bf16 v[248:251], v[240:243], v[80:87], %[sidx]\n\t"
        "s_waitcnt lgkmcnt(0)\n\t"
        "v_smfmac_f32_16x16x64_bf16 v[248:251], v[244:247], v[88:95], %[sidx]\n\t"
        "v_smfmac_f32_16x16x64_bf16 v[252:255], v[216:219], v[112:119], %[sidx]\n\t"
        "v_smfmac_f32_16x16x64_bf16 v[252:255], v[220:223], v[120:127], %[sidx]\n\t"
        "v_add_f32 v248, v248, v250\n\t"
        "v_smfmac_f32_16x16x64_bf16 v[252:255], v[224:227], v[128:135], %[sidx]\n\t"
        "v_add_f32 v249, v249, v251\n\t"
        "v_smfmac_f32_16x16x64_bf16 v[252:255], v[228:231], v[136:143], %[sidx]\n\t"
        "v_cvt_pk_bf16_f32 v200, v248, v249\n\t"
        "v_smfmac_f32_16x16x64_bf16 v[252:255], v[232:235], v[144:151], %[sidx]\n\t"
        "ds_write_b32 %[vw], v200\n\t"
        "v_smfmac_f32_16x16x64_bf16 v[252:255], v[236:239], v[152:159], %[sidx]\n\t"
        "v_cmp_u_f32 vcc, v248, v249\n\t"
        "v_smfmac_f32_16x16x64_bf16 v[252:255], v[240:243], v[160:167], %[sidx]\n\t"
        "v_smfmac_f32_16x16x64_bf16 v[252:255], v[244:247], v[168:175], %[sidx]\n\t"
        "s_nop 7\n\t"
        "v_add_f32 v252, v252, v254\n\t"
        "v_add_f32 v253, v253, v255\n\t"
        "v_cvt_pk_bf16_f32 v201, v252, v253\n\t"
        "ds_write_b32 %[vw], v201 offset:1024\n\t"
        "s_setprio 0\n\t"
        "s_cbranch_vccnz Lnan%=\n\t"
        "Lok%=:\n\t"
        "s_waitcnt lgkmcnt(0)\n\t"
        "s_barrier\n\t"
        "ds_read_b32 v207, v206 offset:14336\n\t"
        "s_waitcnt lgkmcnt(0)\n\t"
        "v_cmp_eq_u32 vcc, %[rnd], v207\n\t"
        "s_barrier\n\t"
        "s_cbranch_vccnz Ltop%=\n\t"
        "s_branch Lend%=\n\t"
        "Lnan%=:\n\t"
        "v_max_u32 v207, v208, v209\n\t"
        "v_max3_u32 v207, v207, v210, v211\n\t"
        "v_max3_u32 v207, v207, v212, v213\n\t"
        "v_max3_u32 v207, v207, v214, v215\n\t"
        "v_max3_u32 v207, v207, v216, v217\n\t"
        "v_max3_u32 v207, v207, v218, v219\n\t"
        "v_max3_u32 v207, v207, v220, v221\n\t"
        "v_max3_u32 v207, v207, v222, v223\n\t"
        "v_max3_u32 v207, v207, v224, v225\n\t"
        "v_max3_u32 v207, v207, v226, v227\n\t"
        "v_max3_u32 v207, v207, v228, v229\n\t"
        "v_max3_u32 v207, v207, v230, v231\n\t"
        "v_max3_u32 v207, v207, v232, v233\n\t"
        "v_max3_u32 v207, v207, v234, v235\n\t"
        "v_max3_u32 v207, v207, v236, v237\n\t"
        "v_max3_u32 v207, v207, v238, v239\n\t"
        "v_max3_u32 v207, v207, v240, v241\n\t"
        "v_max3_u32 v207, v207, v242, v243\n\t"
        "v_max3_u32 v207, v207, v244, v245\n\t"
        "v_max3_u32 v207, v207, v246, v247\n\t"
        "v_cmp_eq_u32 vcc, 0xffbfffbf, v207\n\t"
        "v_mov_b32 v207, %[rnd]\n\t"
        "s_cbranch_vccz Lok%=\n\t"
        "ds_write_b32 v206, v207 offset:14336\n\t"
        "s_branch Lok%=\n\t"
        "Lend%=:\n\t"
        : [rnd] "+&s"(rnd)
        : [w0] "{v[16:47]}"(W[0]), [w1] "{v[48:79]}"(W[1]), [w2] "{v[80:111]}"(W[2]), [w3] "{v[112:143]}"(W[3]), [w4] "{v[144:175]}"(W[4]), [va] "v"(va), [sidx] "v"(sidx), [vw] "v"(vw), [lb] "s"(lb)
        : "v200", "v201", "v202", "v203", "v204", "v205", "v206", "v207", "v208", "v209", "v210", "v211", "v212", "v213", "v214", "v215", "v216", "v217", "v218", "v219", "v220", "v221", "v222", "v223", "v224", "v225", "v226", "v227", "v228", "v229", "v230", "v231", "v232", "v233", "v234", "v235", "v236", "v237", "v238", "v239", "v240", "v241", "v242", "v243", "v244", "v245", "v246", "v247", "v248", "v249", "v250", "v251", "v252", "v253", "v254", "v255", "vcc", "scc", "memory");
}

__device__ __forceinline__ void v_a1(const u32x32 (&W)[5], u32& rnd, u32 va, u32 sidx, u32 vw, u32 lb, u32& left, u32& c0, u32& r0, u32 tg, u32 kr, u32 poll, u32 vm, u32 so, u32 md, u32x4 rs STP) {
    asm volatile(
        "s_mov_b32 s94, m0\n\t"
        "s_add_u32 s92, %[so], 0x8000\n\t"
        "s_add_u32 s93, %[so], 0x10000\n\t"
        "s_mov_b32 m0, %[md]\n\t"
        "s_setprio 3\n\t"
        "s_cmp_eq_u32 %[poll], 0\n\t"
        "s_cbranch_scc1 Ltop%=\n\t"
        "Lda_%=:\n\t"
        "s_memrealtime s[90:91]\n\t"
        "s_waitcnt lgkmcnt(0)\n\t"
        "s_mul_i32 s90, s90, 24\n\t"
        "s_sub_u32 s88, %[tg], s90\n\t"
        "s_sub_u32 s89, s88, 620\n\t"
        "s_cmp_gt_u32 s89, 0x4000\n\t"
        "s_cbranch_scc1 Lea_%=\n\t"
        "s_sleep 1\n\t"
        "s_branch Lda_%=\n\t"
        "Lea_%=:\n\t"
        "s_memrealtime s[64:65]\n\t"
        "s_nop 0\n\t"
        "s_memrealtime s[66:67]\n\t"
        "s_nop 0\n\t"
        "s_memrealtime s[68:69]\n\t"
        "s_nop 0\n\t"
        "s_memrealtime s[70:71]\n\t"
        "s_nop 0\n\t"
        "s_memrealtime s[72:73]\n\t"
        "s_nop 0\n\t"
        "s_memrealtime s[74:75]\n\t"
        "s_memtime s[90:91]\n\t"
        "s_memrealtime s[76:77]\n\t"
        "s_nop 0\n\t"
        "s_memrealtime s[78:79]\n\t"
        "s_nop 0\n\t"
        "s_memrealtime s[80:81]\n\t"
        "s_nop 0\n\t"
        "s_memrealtime s[82:83]\n\t"
        "s_nop 0\n\t"
        "s_memrealtime s[84:85]\n\t"
        "s_nop 0\n\t"
        "s_memrealtime s[86:87]\n\t"
        "s_nop 0\n\t"
        "s_nop 7\n\t"
        "s_waitcnt lgkmcnt(0)\n\t"
        "s_add_u32 s64, s64, s66\n\t"
        "s_add_u32 s64, s64, s68\n\t"
        "s_add_u32 s64, s64, s70\n\t"
        "s_add_u32 s64, s64, s72\n\t"
        "s_add_u32 s64, s64, s74\n\t"
        "s_add_u32 s64, s64, s76\n\t"
        "s_add_u32 s64, s64, s78\n\t"
        "s_add_u32 s64, s64, s80\n\t"
        "s_add_u32 s64, s64, s82\n\t"
        "s_add_u32 s64, s64, s84\n\t"
        "s_add_u32 s64, s64, s86\n\t"
        "s_lshl_b32 s64, s64, 1\n\t"
        "s_mov_b32 %[c0], s90\n\t"
        "s_mov_b32 %[r0], s64\n\t"
        "s_sub_u32 s88, %[tg], s64\n\t"
        "s_mul_i32 s88, s88, %[kr]\n\t"
        "s_ashr_i32 s88, s88, 12\n\t"
        "s_sub_u32 s88, s88, 244\n\t"
        "s_nop 1\n\t"
        "s_max_i32 s88, s88, 0\n\t"
        "s_min_i32 s88, s88, 384\n\t"
        "s_andn2_b32 s88, s88, 3\n\t"
        "s_getpc_b64 s[90:91]\n\t"
        "Lga_%=:\n\t"
        "s_sub_u32 s90, s90, s88\n\t"
        "s_subb_u32 s91, s91, 0\n\t"
        "s_add_u32 s90, s90, Lza_%=-Lga_%=\n\t"
        "s_addc_u32 s91, s91, 0\n\t"
        "s_setpc_b64 s[90:91]\n\t"
        "s_nop 0\n\ts_nop 0\n\ts_nop 0\n\ts_nop 0\n\t"
        "s_nop 0\n\ts_nop 0\n\ts_nop 0\n\ts_nop 0\n\t"
        "s_nop 0\n\ts_nop 0\n\ts_nop 0\n\ts_nop 0\n\t"
        "s_nop 0\n\ts_nop 0\n\ts_nop 0\n\ts_nop 0\n\t"
        "s_nop 0\n\ts_nop 0\n\ts_nop 0\n\ts_nop 0\n\t"
        "s_nop 0\n\ts_nop 0\n\ts_nop 0\n\ts_nop 0\n\t"
        "s_nop 0\n\ts_nop 0\n\ts_nop 0\n\ts_nop 0\n\t"
        "s_nop 0\n\ts_nop 0\n\ts_nop 0\n\ts_nop 0\n\t"
        "s_nop 0\n\ts_nop 0\n\ts_nop 0\n\ts_nop 0\n\t"
        "s_nop 0\n\ts_nop 0\n\ts_nop 0\n\ts_nop 0\n\t"
        "s_nop 0\n\ts_nop 0\n\ts_nop 0\n\ts_nop 0\n\t"
        "s_nop 0\n\ts_nop 0\n\ts_nop 0\n\ts_nop 0\n\t"
        "s_nop 0\n\ts_nop 0\n\ts_nop 0\n\ts_nop 0\n\t"
        "s_nop 0\n\ts_nop 0\n\ts_nop 0\n\ts_nop 0\n\t"
        "s_nop 0\n\ts_nop 0\n\ts_nop 0\n\ts_nop 0\n\t"
        "s_nop 0\n\ts_nop 0\n\ts_nop 0\n\ts_nop 0\n\t"
        "s_nop 0\n\ts_nop 0\n\ts_nop 0\n\ts_nop 0\n\t"
        "s_nop 0\n\ts_nop 0\n\ts_nop 0\n\ts_nop 0\n\t"
        "s_nop 0\n\ts_nop 0\n\ts_nop 0\n\ts_nop 0\n\t"
        "s_nop 0\n\ts_nop 0\n\ts_nop 0\n\ts_nop 0\n\t"
        "s_nop 0\n\ts_nop 0\n\ts_nop 0\n\ts_nop 0\n\t"
        "s_nop 0\n\ts_nop 0\n\ts_nop 0\n\ts_nop 0\n\t"
        "s_nop 0\n\ts_nop 0\n\ts_nop 0\n\ts_nop 0\n\t"
        "s_nop 0\n\ts_nop 0\n\ts_nop 0\n\ts_nop 0\n\t"
        "Lza_%=:\n\t"
        "buffer_load_dwordx4 %[vm], %[rs], %[so] offen offset:0 sc1 lds\n\t"
        "buffer_load_dwordx4 %[vm], %[rs], %[so] offen offset:1024 sc1 lds\n\t"
        "buffer_load_dwordx4 %[vm], %[rs], %[so] offen offset:2048 sc1 lds\n\t"
        "buffer_load_dwordx4 %[vm], %[rs], %[so] offen offset:3072 sc1 lds\n\t"
        "s_add_u32 m0, m0, 0x1000\n\t"
        "s_nop 0\n\t"
        "buffer_load_dwordx4 %[vm], %[rs], s92 offen offset:0 sc1 lds\n\t"
        "buffer_load_dwordx4 %[vm], %[rs], s92 offen offset:1024 sc1 lds\n\t"
        "buffer_load_dwordx4 %[vm], %[rs], s92 offen offset:2048 sc1 lds\n\t"
        "buffer_load_dwordx4 %[vm], %[rs], s92 offen offset:3072 sc1 lds\n\t"
        "s_add_u32 m0, m0, 0x1000\n\t"
        "s_nop 0\n\t"
        "buffer_load_dwordx4 %[vm], %[rs], s93 offen offset:0 sc1 lds\n\t"
        "buffer_load_dwordx4 %[vm], %[rs], s93 offen offset:2048 sc1 lds\n\t"
        "Ltop%=:\n\t"
        "v_mov_b64 v[248:249], 0\n\t"
        "v_mov_b64 v[250:251], 0\n\t"
        "v_mov_b64 v[252:253], 0\n\t"
        "v_mov_b64 v[254:255], 0\n\t"
        "v_mov_b32 v200, 0xffbfffbf\n\t"
        "v_mov_b32 v206, %[lb]\n\t"
        "ds_write_b32 %[vw], v200\n\t"
        "ds_write_b32 %[vw], v200 offset:1024\n\t"
        "s_add_u32 %[rnd], %[rnd], 1\n\t"
        "s_setprio 3\n\t"
        "s_mov_b32 s95, 0\n\t"
        "s_waitcnt vmcnt(0) lgkmcnt(0)\n\t"
        "s_barrier\n\t"
        "ds_read_b128 v[208:211], %[va] offset:0\n\t"
        "ds_read_b128 v[212:215], %[va] offset:1024\n\t"
        "ds_read_b128 v[216:219], %[va] offset:2048\n\t"
        "ds_read_b128 v[220:223], %[va] offset:3072\n\t"
        "ds_read_b128 v[224:227], %[va] offset:4096\n\t"
        "ds_read_b128 v[228:231], %[va] offset:5120\n\t"
        "s_waitcnt lgkmcnt(5)\n\t"
        "v_smfmac_f32_16x16x64_bf16 v[248:251], v[208:211], v[16:23], %[sidx]\n\t"
        "ds_read_b128 v[232:235], %[va] offset:6144\n\t"
        "v_smfmac_f32_16x16x64_bf16 v[252:255], v[208:211], v[96:103], %[sidx]\n\t"
        "s_waitcnt lgkmcnt(5)\n\t"
        "v_smfmac_f32_16x16x64_bf16 v[248:251], v[212:215], v[24:31], %[sidx]\n\t"
        "ds_read_b128 v[236:239], %[va] offset:7168\n\t"
        "v_smfmac_f32_16x16x64_bf16 v[252:255], v[212:215], v[104:111], %[sidx]\n\t"
        "s_waitcnt lgkmcnt(5)\n\t"
        "v_smfmac_f32_16x16x64_bf16 v[248:251], v[216:219], v[32:39], %[sidx]\n\t"
        "ds_read_b128 v[240:243], %[va] offset:8192\n\t"
        "s_waitcnt lgkmcnt(5)\n\t"
        "v_smfmac_f32_16x16x64_bf16 v[248:251], v[220:223], v[40:47], %[sidx]\n\t"
        "ds_read_b128 v[244:247], %[va] offset:10240\n\t"
        "s_waitcnt lgkmcnt(5)\n\t"
        "v_smfmac_f32_16x16x64_bf16 v[248:251], v[224:227], v[48:55], %[sidx]\n\t"
        "s_waitcnt lgkmcnt(4)\n\t"
        "v_smfmac_f32_16x16x64_bf16 v[248:251], v[228:231], v[56:63], %[sidx]\n\t"
        "s_waitcnt lgkmcnt(3)\n\t"
        "v_smfmac_f32_16x16x64_bf16 v[248:251], v[232:235], v[64:71], %[sidx]\n\t"
        "s_waitcnt lgkmcnt(2)\n\t"
        "v_smfmac_f32_16x16x64_bf16 v[248:251], v[236:239], v[72:79], %[sidx]\n\t"
        "s_waitcnt lgkmcnt(1)\n\t"
        "v_smfmac_f32_16x16x64_bf16 v[248:251], v[240:243], v[80:87], %[sidx]\n\t"
        "s_waitcnt lgkmcnt(0)\n\t"
        "v_smfmac_f32_16x16x64_bf16 v[248:251], v[244:247], v[88:95], %[sidx]\n\t"
        "v_smfmac_f32_16x16x64_bf16 v[252:255], v[216:219], v[112:119], %[sidx]\n\t"
        "v_smfmac_f32_16x16x64_bf16 v[252:255], v[220:223], v[120:127], %[sidx]\n\t"
        "v_add_f32 v248, v248, v250\n\t"
        "v_smfmac_f32_16x16x64_bf16 v[252:255], v[224:227], v[128:135], %[sidx]\n\t"
        "v_add_f32 v249, v249, v251\n\t"
        "v_smfmac_f32_16x16x64_bf16 v[252:255], v[228:231], v[136:143], %[sidx]\n\t"
        "v_cvt_pk_bf16_f32 v200, v248, v249\n\t"
        "v_smfmac_f32_16x16x64_bf16 v[252:255], v[232:235], v[144:151], %[sidx]\n\t"
        "ds_write_b32 %[vw], v200\n\t"
        "v_smfmac_f32_16x16x64_bf16 v[252:255], v[236:239], v[152:159], %[sidx]\n\t"
        "v_cmp_u_f32 vcc, v248, v249\n\t"
        "v_smfmac_f32_16x16x64_bf16 v[252:255], v[240:243], v[160:167], %[sidx]\n\t"
        "v_smfmac_f32_16x16x64_bf16 v[252:255], v[244:247], v[168:175], %[sidx]\n\t"
        "s_nop 7\n\t"
        "v_add_f32 v252, v252, v254\n\t"
        "v_add_f32 v253, v253, v255\n\t"
        "v_cvt_pk_bf16_f32 v201, v252, v253\n\t"
        "ds_write_b32 %[vw], v201 offset:1024\n\t"
        "s_setprio 0\n\t"
        "s_cbranch_vccnz Lnan%=\n\t"
        "Lok%=:\n\t"
        "s_waitcnt lgkmcnt(0)\n\t"
        "s_barrier\n\t"
        "ds_read_b32 v207, v206 offset:14336\n\t"
        "s_waitcnt lgkmcnt(0)\n\t"
        "v_cmp_eq_u32 vcc, %[rnd], v207\n\t"
        "s_barrier\n\t"
        "s_cbranch_vccnz Lck%=\n\t"
        "s_branch Lend%=\n\t"
        "Lnan%=:\n\t"
        "s_cmp_lg_u32 %[poll], 1\n\t"
        "s_cbranch_scc1 Lok%=\n\t"
        "s_mov_b32 m0, %[lb]\n\t"
        "v_add_u32 v204, %[lb], %[vm]\n\t"
        "buffer_load_dwordx4 %[vm], %[rs], %[so] offen offset:0 sc1 lds\n\t"
        "buffer_load_dwordx4 %[vm], %[rs], %[so] offen offset:1024 sc1 lds\n\t"
        "buffer_load_dwordx4 %[vm], %[rs], %[so] offen offset:2048 sc1 lds\n\t"
        "buffer_load_dwordx4 %[vm], %[rs], %[so] offen offset:3072 sc1 lds\n\t"
        "s_add_u32 m0, m0, 0x1000\n\t"
        "s_nop 0\n\t"
        "buffer_load_dwordx4 %[vm], %[rs], s92 offen offset:0 sc1 lds\n\t"
        "buffer_load_dwordx4 %[vm], %[rs], s92 offen offset:1024 sc1 lds\n\t"
        "buffer_load_dwordx4 %[vm], %[rs], s92 offen offset:2048 sc1 lds\n\t"
        "buffer_load_dwordx4 %[vm], %[rs], s92 offen offset:3072 sc1 lds\n\t"
        "s_add_u32 m0, m0, 0x1000\n\t"
        "s_nop 0\n\t"
        "buffer_load_dwordx4 %[vm], %[rs], s93 offen offset:0 sc1 lds\n\t"
        "buffer_load_dwordx4 %[vm], %[rs], s93 offen offset:2048 sc1 lds\n\t"
        "v_max_u32 v207, v208, v209\n\t"
        "v_max3_u32 v207, v207, v210, v211\n\t"
        "v_max3_u32 v207, v207, v212, v213\n\t"
        "v_max3_u32 v207, v207, v214, v215\n\t"
        "v_max3_u32 v207, v207, v216, v217\n\t"
        "v_max3_u32 v207, v207, v218, v219\n\t"
        "v_max3_u32 v207, v207, v220, v221\n\t"
        "v_max3_u32 v207, v207, v222, v223\n\t"
        "v_max3_u32 v207, v207, v224, v225\n\t"
        "v_max3_u32 v207, v207, v226, v227\n\t"
        "v_max3_u32 v207, v207, v228, v229\n\t"
        "v_max3_u32 v207, v207, v230, v231\n\t"
        "v_max3_u32 v207, v207, v232, v233\n\t"
        "v_max3_u32 v207, v207, v234, v235\n\t"
        "v_max3_u32 v207, v207, v236, v237\n\t"
        "v_max3_u32 v207, v207, v238, v239\n\t"
        "v_max3_u32 v207, v207, v240, v241\n\t"
        "v_max3_u32 v207, v207, v242, v243\n\t"
        "v_max3_u32 v207, v207, v244, v245\n\t"
        "v_max3_u32 v207, v207, v246, v247\n\t"
        "v_cmp_eq_u32 vcc, 0xffbfffbf, v207\n\t"
        "v_mov_b32 v207, %[rnd]\n\t"
        "s_cbranch_vccz Lok%=\n\t"
        "ds_write_b32 v206, v207 offset:14336\n\t"
        "s_mov_b32 s95, 1\n\t"
        "s_waitcnt lgkmcnt(0)\n\t"
        "s_branch Lcv%=\n\t"
        "Lck%=:\n\t"
        "v_add_u32 v204, %[lb], %[vm]\n\t"
        "Lcl%=:\n\t"
        "s_mov_b32 m0, %[lb]\n\t"
        "s_nop 0\n\t"
        "buffer_load_dwordx4 %[vm], %[rs], %[so] offen offset:0 sc1 lds\n\t"
        "buffer_load_dwordx4 %[vm], %[rs], %[so] offen offset:1024 sc1 lds\n\t"
        "buffer_load_dwordx4 %[vm], %[rs], %[so] offen offset:2048 sc1 lds\n\t"
        "buffer_load_dwordx4 %[vm], %[rs], %[so] offen offset:3072 sc1 lds\n\t"
        "s_add_u32 m0, m0, 0x1000\n\t"
        "s_nop 0\n\t"
        "buffer_load_dwordx4 %[vm], %[rs], s92 offen offset:0 sc1 lds\n\t"
        "buffer_load_dwordx4 %[vm], %[rs], s92 offen offset:1024 sc1 lds\n\t"
        "buffer_load_dwordx4 %[vm], %[rs], s92 offen offset:2048 sc1 lds\n\t"
        "buffer_load_dwordx4 %[vm], %[rs], s92 offen offset:3072 sc1 lds\n\t"
        "s_add_u32 m0, m0, 0x1000\n\t"
        "s_nop 0\n\t"
        "buffer_load_dwordx4 %[vm], %[rs], s93 offen offset:0 sc1 lds\n\t"
        "buffer_load_dwordx4 %[vm], %[rs], s93 offen offset:2048 sc1 lds\n\t"
        "Lcv%=:\n\t"
        "s_sub_u32 %[left], %[left], 1\n\t"
        "s_waitcnt vmcnt(9)\n\t"
        "ds_read_b128 v[208:211], v204 offset:0\n\t"
        "s_waitcnt vmcnt(8)\n\t"
        "ds_read_b128 v[212:215], v204 offset:1024\n\t"
        "s_waitcnt vmcnt(7)\n\t"
        "ds_read_b128 v[216:219], v204 offset:2048\n\t"
        "s_waitcnt vmcnt(6)\n\t"
        "ds_read_b128 v[220:223], v204 offset:3072\n\t"
        "s_waitcnt vmcnt(5)\n\t"
        "ds_read_b128 v[224:227], v204 offset:4096\n\t"
        "s_waitcnt vmcnt(4)\n\t"
        "ds_read_b128 v[228:231], v204 offset:5120\n\t"
        "s_waitcnt lgkmcnt(5)\n\t"
        "v_max3_u32 v207, v208, v208, v209\n\t"
        "v_max3_u32 v207, v207, v210, v211\n\t"
        "s_waitcnt vmcnt(3)\n\t"
        "ds_read_b128 v[232:235], v204 offset:6144\n\t"
        "s_waitcnt lgkmcnt(5)\n\t"
        "v_max3_u32 v207, v207, v212, v213\n\t"
        "v_max3_u32 v207, v207, v214, v215\n\t"
        "s_waitcnt vmcnt(2)\n\t"
        "ds_read_b128 v[236:239], v204 offset:7168\n\t"
        "s_waitcnt lgkmcnt(5)\n\t"
        "v_max3_u32 v207, v207, v216, v217\n\t"
        "v_max3_u32 v207, v207, v218, v219\n\t"
        "s_waitcnt vmcnt(1)\n\t"
        "ds_read_b128 v[240:243], v204 offset:8192\n\t"
        "s_waitcnt lgkmcnt(5)\n\t"
        "v_max3_u32 v207, v207, v220, v221\n\t"
        "v_max3_u32 v207, v207, v222, v223\n\t"
        "s_waitcnt vmcnt(0)\n\t"
        "ds_read_b128 v[244:247], v204 offset:10240\n\t"
        "s_waitcnt lgkmcnt(5)\n\t"
        "v_max3_u32 v207, v207, v224, v225\n\t"
        "v_max3_u32 v207, v207, v226, v227\n\t"
        "s_waitcnt lgkmcnt(4)\n\t"
        "v_max3_u32 v207, v207, v228, v229\n\t"
        "v_max3_u32 v207, v207, v230, v231\n\t"
        "s_waitcnt lgkmcnt(3)\n\t"
        "v_max3_u32 v207, v207, v232, v233\n\t"
        "v_max3_u32 v207, v207, v234, v235\n\t"
        "s_waitcnt lgkmcnt(2)\n\t"
        "v_max3_u32 v207, v207, v236, v237\n\t"
        "v_max3_u32 v207, v207, v238, v239\n\t"
        "s_waitcnt lgkmcnt(1)\n\t"
        "v_max3_u32 v207, v207, v240, v241\n\t"
        "v_max3_u32 v207, v207, v242, v243\n\t"
        "s_waitcnt lgkmcnt(0)\n\t"
        "v_max3_u32 v207, v207, v244, v245\n\t"
        "v_max3_u32 v207, v207, v246, v247\n\t"
        "v_cmp_eq_u32 vcc, 0xffbfffbf, v207\n\t"
        "s_cbranch_vccz Lcd%=\n\t"
        "s_cbranch_scc0 Lcl%=\n\t"
        "Lcd%=:\n\t"
        "s_cmp_eq_u32 s95, 0\n\t"
        "s_cbranch_scc1 Lce%=\n\t"
        "s_barrier\n\t"
        "s_barrier\n\t"
        "Lce%=:\n\t"
        "s_cbranch_vccz Ltop%=\n\t"
        "Lend%=:\n\t"
        "s_mov_b32 m0, s94\n\t"
        "s_waitcnt vmcnt(0)\n\t"
        : [rnd] "+&s"(rnd), [c0] "+&s"(c0), [r0] "+&s"(r0), [left] "+&s"(left)
        : [w0] "{v[16:47]}"(W[0]), [w1] "{v[48:79]}"(W[1]), [w2] "{v[80:111]}"(W[2]), [w3] "{v[112:143]}"(W[3]), [w4] "{v[144:175]}"(W[4]), [va] "v"(va), [sidx] "v"(sidx), [vw] "v"(vw), [rs] "s"(rs), [tg] "s"(tg), [kr] "s"(kr), [poll] "s"(poll), [vm] "v"(vm), [so] "s"(so), [lb] "s"(lb), [md] "s"(md)
        : "s64", "s65", "s66", "s67", "s68", "s69", "s70", "s71", "s72", "s73", "s74", "s75", "s76", "s77", "s78", "s79", "s80", "s81", "s82", "s83", "s84", "s85", "s86", "s87", "s88", "s89", "s90", "s91", "s92", "s93", "s94", "s95", "v200", "v201", "v202", "v203", "v204", "v205", "v206", "v207", "v208", "v209", "v210", "v211", "v212", "v213", "v214", "v215", "v216", "v217", "v218", "v219", "v220", "v221", "v222", "v223", "v224", "v225", "v226", "v227", "v228", "v229", "v230", "v231", "v232", "v233", "v234", "v235", "v236", "v237", "v238", "v239", "v240", "v241", "v242", "v243", "v244", "v245", "v246", "v247", "v248", "v249", "v250", "v251", "v252", "v253", "v254", "v255", "vcc", "scc", "memory");
}

__device__ __forceinline__ void v_b(const u32x32 (&W)[5], u32& rnd, u32 sidx, u32 vr, u32 z0, u32 z1, u32 z2, u32x4 rs, u32 lb STP) {
    asm volatile(
        "Ltop%=:\n\t"
        "v_mov_b64 v[224:225], 0\n\t"
        "v_mov_b64 v[226:227], 0\n\t"
        "v_mov_b64 v[228:229], 0\n\t"
        "v_mov_b64 v[230:231], 0\n\t"
        "v_mov_b64 v[232:233], 0\n\t"
        "v_mov_b64 v[234:235], 0\n\t"
        "v_mov_b64 v[236:237], 0\n\t"
        "v_mov_b64 v[238:239], 0\n\t"
        "v_mov_b64 v[240:241], 0\n\t"
        "v_mov_b64 v[242:243], 0\n\t"
        "v_mov_b32 v206, %[lb]\n\t"
        "s_add_u32 %[rnd], %[rnd], 1\n\t"
        "s_mov_b32 s92, 0\n\t"
        "s_setprio 2\n\t"
        "s_barrier\n\t"
        "s_nop 15\n\t"
        "s_nop 15\n\t"
        "s_nop 15\n\t"
        "s_nop 15\n\t"
        "s_nop 15\n\t"
        "s_nop 7\n\t"
        "Lre%=:\n\t"
        "ds_read_b64_tr_b16 v[208:209], %[vr] offset:0\n\t"
        "ds_read_b64_tr_b16 v[210:211], %[vr] offset:64\n\t"
        "ds_read_b64_tr_b16 v[212:213], %[vr] offset:512\n\t"
        "ds_read_b64_tr_b16 v[214:215], %[vr] offset:576\n\t"
        "s_waitcnt lgkmcnt(0)\n\t"
        "v_smfmac_f32_16x16x64_bf16 v[224:227], v[208:211], v[16:23], %[sidx]\n\t"
        "v_smfmac_f32_16x16x64_bf16 v[228:231], v[208:211], v[48:55], %[sidx]\n\t"
        "v_smfmac_f32_16x16x64_bf16 v[232:235], v[208:211], v[80:87], %[sidx]\n\t"
        "v_smfmac_f32_16x16x64_bf16 v[236:239], v[208:211], v[112:119], %[sidx]\n\t"
        "v_smfmac_f32_16x16x64_bf16 v[240:243], v[208:211], v[144:151], %[sidx]\n\t"
        "ds_read_b64_tr_b16 v[216:217], %[vr] offset:1024\n\t"
        "v_smfmac_f32_16x16x64_bf16 v[224:227], v[212:215], v[24:31], %[sidx]\n\t"
        "ds_read_b64_tr_b16 v[218:219], %[vr] offset:1088\n\t"
        "v_smfmac_f32_16x16x64_bf16 v[228:231], v[212:215], v[56:63], %[sidx]\n\t"
        "ds_read_b64_tr_b16 v[220:221], %[vr] offset:1536\n\t"
        "v_smfmac_f32_16x16x64_bf16 v[232:235], v[212:215], v[88:95], %[sidx]\n\t"
        "ds_read_b64_tr_b16 v[222:223], %[vr] offset:1600\n\t"
        "v_smfmac_f32_16x16x64_bf16 v[236:239], v[212:215], v[120:127], %[sidx]\n\t"
        "v_smfmac_f32_16x16x64_bf16 v[240:243], v[212:215], v[152:159], %[sidx]\n\t"
        "s_waitcnt lgkmcnt(0)\n\t"
        "v_smfmac_f32_16x16x64_bf16 v[224:227], v[216:219], v[32:39], %[sidx]\n\t"
        "v_smfmac_f32_16x16x64_bf16 v[224:227], v[220:223], v[40:47], %[sidx]\n\t"
        "v_smfmac_f32_16x16x64_bf16 v[228:231], v[216:219], v[64:71], %[sidx]\n\t"
        "v_smfmac_f32_16x16x64_bf16 v[228:231], v[220:223], v[72:79], %[sidx]\n\t"
        "v_fma_f32 v205, v224, v225, v226\n\t"
        "v_smfmac_f32_16x16x64_bf16 v[232:235], v[216:219], v[96:103], %[sidx]\n\t"
        "v_cmp_u_f32 vcc, v205, v227\n\t"
        "v_smfmac_f32_16x16x64_bf16 v[232:235], v[220:223], v[104:111], %[sidx]\n\t"
        "v_cvt_pk_bf16_f32 v244, v224, v225\n\t"
        "v_smfmac_f32_16x16x64_bf16 v[236:239], v[216:219], v[128:135], %[sidx]\n\t"
        "v_cvt_pk_bf16_f32 v245, v226, v227\n\t"
        "v_smfmac_f32_16x16x64_bf16 v[236:239], v[220:223], v[136:143], %[sidx]\n\t"
        "v_cvt_pk_bf16_f32 v246, v228, v229\n\t"
        "v_smfmac_f32_16x16x64_bf16 v[240:243], v[216:219], v[160:167], %[sidx]\n\t"
        "v_cvt_pk_bf16_f32 v247, v230, v231\n\t"
        "v_smfmac_f32_16x16x64_bf16 v[240:243], v[220:223], v[168:175], %[sidx]\n\t"
        "s_cbranch_vccnz Lnan%=\n\t"
        "Lst%=:\n\t"
        "v_permlane16_swap_b32 v244, v246\n\t"
        "v_permlane16_swap_b32 v245, v247\n\t"
        "buffer_store_dwordx4 v[244:247], %[z0], %[rs], 0 offen sc1\n\t"
        "v_cvt_pk_bf16_f32 v248, v232, v233\n\t"
        "v_cvt_pk_bf16_f32 v249, v234, v235\n\t"
        "v_cvt_pk_bf16_f32 v250, v236, v237\n\t"
        "v_cvt_pk_bf16_f32 v251, v238, v239\n\t"
        "s_nop 0\n\t"
        "v_permlane16_swap_b32 v248, v250\n\t"
        "v_permlane16_swap_b32 v249, v251\n\t"
        "buffer_store_dwordx4 v[248:251], %[z1], %[rs], 0 offen sc1\n\t"
        "v_cvt_pk_bf16_f32 v252, v240, v241\n\t"
        "v_cvt_pk_bf16_f32 v254, v240, v241\n\t"
        "v_cvt_pk_bf16_f32 v253, v242, v243\n\t"
        "v_cvt_pk_bf16_f32 v255, v242, v243\n\t"
        "s_nop 0\n\t"
        "v_permlane16_swap_b32 v252, v254\n\t"
        "v_permlane16_swap_b32 v253, v255\n\t"
        "buffer_store_dwordx4 v[252:255], %[z2], %[rs], 0 offen sc1\n\t"
        "s_setprio 0\n\t"
        "s_cmp_eq_u32 s92, 2\n\t"
        "s_cbranch_scc1 Ly%=\n\t"
        "Lx%=:\n\t"
        "s_waitcnt lgkmcnt(0)\n\t"
        "s_barrier\n\t"
        "ds_read_b32 v207, v206 offset:14336\n\t"
        "s_waitcnt lgkmcnt(0)\n\t"
        "v_cmp_eq_u32 vcc, %[rnd], v207\n\t"
        "s_cmp_eq_u32 s92, 1\n\t"
        "s_cbranch_vccnz Lz%=\n\t"
        "s_cbranch_scc0 Ly%=\n\t"
        "s_mov_b32 s92, 2\n\t"
        "v_mov_b64 v[224:225], 0\n\t"
        "v_mov_b64 v[226:227], 0\n\t"
        "v_mov_b64 v[228:229], 0\n\t"
        "v_mov_b64 v[230:231], 0\n\t"
        "v_mov_b64 v[232:233], 0\n\t"
        "v_mov_b64 v[234:235], 0\n\t"
        "v_mov_b64 v[236:237], 0\n\t"
        "v_mov_b64 v[238:239], 0\n\t"
        "v_mov_b64 v[240:241], 0\n\t"
        "v_mov_b64 v[242:243], 0\n\t"
        "s_branch Lre%=\n\t"
        "Lnan%=:\n\t"
        "s_cmp_eq_u32 s92, 2\n\t"
        "s_cbranch_scc1 Lst%=\n\t"
        "s_mov_b32 s92, 1\n\t"
        "s_branch Lx%=\n\t"
        "Lz%=:\n\t"
        "s_barrier\n\t"
        "s_branch Ltop%=\n\t"
        "Ly%=:\n\t"
        "s_barrier\n\t"
        : [rnd] "+&s"(rnd)
        : [w0] "{v[16:47]}"(W[0]), [w1] "{v[48:79]}"(W[1]), [w2] "{v[80:111]}"(W[2]), [w3] "{v[112:143]}"(W[3]), [w4] "{v[144:175]}"(W[4]), [sidx] "v"(sidx), [vr] "v"(vr), [z0] "v"(z0), [z1] "v"(z1), [z2] "v"(z2), [rs] "s"(rs), [lb] "s"(lb)
        : "s92", "v205", "v206", "v207", "v208", "v209", "v210", "v211", "v212", "v213", "v214", "v215", "v216", "v217", "v218", "v219", "v220", "v221", "v222", "v223", "v224", "v225", "v226", "v227", "v228", "v229", "v230", "v231", "v232", "v233", "v234", "v235", "v236", "v237", "v238", "v239", "v240", "v241", "v242", "v243", "v244", "v245", "v246", "v247", "v248", "v249", "v250", "v251", "v252", "v253", "v254", "v255", "vcc", "scc", "memory");
}

__device__ __forceinline__ void l_g(u32& miss, u32& c0, u32& r0, u64& tl, u32 tg, u32 kr, u32 m0o, u32 m1o, u32 so, u32x4 rs, u32x4 rec, u32 sr STP) {
    asm volatile(
        "s_mov_b32 %[miss], 1\n\t"
        "v_or_b32 v62, 0x10000, v59\n\t"
        "s_nop 1\n\t"
        "s_cmp_eq_u32 %[kr], 0\n\t"
        "s_cbranch_scc1 Lza_%=\n\t"
        "Lda_%=:\n\t"
        "s_memrealtime s[90:91]\n\t"
        "s_waitcnt lgkmcnt(0)\n\t"
        "s_mul_i32 s90, s90, 24\n\t"
        "s_sub_u32 s88, %[tg], s90\n\t"
        "s_sub_u32 s89, s88, 620\n\t"
        "s_cmp_gt_u32 s89, 0x4000\n\t"
        "s_cbranch_scc1 Lea_%=\n\t"
        "s_sleep 1\n\t"
        "s_branch Lda_%=\n\t"
        "Lea_%=:\n\t"
        "s_memrealtime s[64:65]\n\t"
        "s_nop 0\n\t"
        "s_memrealtime s[66:67]\n\t"
        "s_nop 0\n\t"
        "s_memrealtime s[68:69]\n\t"
        "s_nop 0\n\t"
        "s_memrealtime s[70:71]\n\t"
        "s_nop 0\n\t"
        "s_memrealtime s[72:73]\n\t"
        "s_nop 0\n\t"
        "s_memrealtime s[74:75]\n\t"
        "s_memtime s[90:91]\n\t"
        "s_memrealtime s[76:77]\n\t"
        "s_nop 0\n\t"
        "s_memrealtime s[78:79]\n\t"
        "s_nop 0\n\t"
        "s_memrealtime s[80:81]\n\t"
        "s_nop 0\n\t"
        "s_memrealtime s[82:83]\n\t"
        "s_nop 0\n\t"
        "s_memrealtime s[84:85]\n\t"
        "s_nop 0\n\t"
        "s_memrealtime s[86:87]\n\t"
        "s_nop 0\n\t"
        "s_nop 7\n\t"
        "s_waitcnt lgkmcnt(0)\n\t"
        "s_add_u32 s64, s64, s66\n\t"
        "s_add_u32 s64, s64, s68\n\t"
        "s_add_u32 s64, s64, s70\n\t"
        "s_add_u32 s64, s64, s72\n\t"
        "s_add_u32 s64, s64, s74\n\t"
        "s_add_u32 s64, s64, s76\n\t"
        "s_add_u32 s64, s64, s78\n\t"
        "s_add_u32 s64, s64, s80\n\t"
        "s_add_u32 s64, s64, s82\n\t"
        "s_add_u32 s64, s64, s84\n\t"
        "s_add_u32 s64, s64, s86\n\t"
        "s_lshl_b32 s64, s64, 1\n\t"
        "s_mov_b32 %[c0], s90\n\t"
        "s_mov_b32 %[r0], s64\n\t"
        "s_sub_u32 s88, %[tg], s64\n\t"
        "s_mul_i32 s88, s88, %[kr]\n\t"
        "s_ashr_i32 s88, s88, 12\n\t"
        "s_sub_u32 s88, s88, 244\n\t"
        "s_nop 1\n\t"
        "s_max_i32 s88, s88, 0\n\t"
        "s_min_i32 s88, s88, 384\n\t"
        "s_andn2_b32 s88, s88, 3\n\t"
        "s_getpc_b64 s[90:91]\n\t"
        "Lga_%=:\n\t"
        "s_sub_u32 s90, s90, s88\n\t"
        "s_subb_u32 s91, s91, 0\n\t"
        "s_add_u32 s90, s90, Lza_%=-Lga_%=\n\t"
        "s_addc_u32 s91, s91, 0\n\t"
        "s_setpc_b64 s[90:91]\n\t"
        "s_nop 0\n\ts_nop 0\n\ts_nop 0\n\ts_nop 0\n\t"
        "s_nop 0\n\ts_nop 0\n\ts_nop 0\n\ts_nop 0\n\t"
        "s_nop 0\n\ts_nop 0\n\ts_nop 0\n\ts_nop 0\n\t"
        "s_nop 0\n\ts_nop 0\n\ts_nop 0\n\ts_nop 0\n\t"
        "s_nop 0\n\ts_nop 0\n\ts_nop 0\n\ts_nop 0\n\t"
        "s_nop 0\n\ts_nop 0\n\ts_nop 0\n\ts_nop 0\n\t"
        "s_nop 0\n\ts_nop 0\n\ts_nop 0\n\ts_nop 0\n\t"
        "s_nop 0\n\ts_nop 0\n\ts_nop 0\n\ts_nop 0\n\t"
        "s_nop 0\n\ts_nop 0\n\ts_nop 0\n\ts_nop 0\n\t"
        "s_nop 0\n\ts_nop 0\n\ts_nop 0\n\ts_nop 0\n\t"
        "s_nop 0\n\ts_nop 0\n\ts_nop 0\n\ts_nop 0\n\t"
        "s_nop 0\n\ts_nop 0\n\ts_nop 0\n\ts_nop 0\n\t"
        "s_nop 0\n\ts_nop 0\n\ts_nop 0\n\ts_nop 0\n\t"
        "s_nop 0\n\ts_nop 0\n\ts_nop 0\n\ts_nop 0\n\t"
        "s_nop 0\n\ts_nop 0\n\ts_nop 0\n\ts_nop 0\n\t"
        "s_nop 0\n\ts_nop 0\n\ts_nop 0\n\ts_nop 0\n\t"
        "s_nop 0\n\ts_nop 0\n\ts_nop 0\n\ts_nop 0\n\t"
        "s_nop 0\n\ts_nop 0\n\ts_nop 0\n\ts_nop 0\n\t"
        "s_nop 0\n\ts_nop 0\n\ts_nop 0\n\ts_nop 0\n\t"
        "s_nop 0\n\ts_nop 0\n\ts_nop 0\n\ts_nop 0\n\t"
        "s_nop 0\n\ts_nop 0\n\ts_nop 0\n\ts_nop 0\n\t"
        "s_nop 0\n\ts_nop 0\n\ts_nop 0\n\ts_nop 0\n\t"
        "s_nop 0\n\ts_nop 0\n\ts_nop 0\n\ts_nop 0\n\t"
        "s_nop 0\n\ts_nop 0\n\ts_nop 0\n\ts_nop 0\n\t"
        "Lza_%=:\n\t"
        "buffer_load_dword v60, %[m0o], %[rs], %[so] offen sc1\n\t"
        "buffer_load_dword v61, %[m1o], %[rs], %[so] offen sc1\n\t"
        "s_memrealtime %[tl]\n\t"
        "s_waitcnt vmcnt(0)\n\t"
        "v_max_u32 v60, v60, v61\n\t"
        "v_cmp_eq_u32 vcc, 0xffbfffbf, v60\n\t"
        "s_cbranch_vccnz Lm%=\n\t"
        "buffer_store_dwordx4 v[56:59], %[sr], %[rs], 0 offen sc1\n\t"
        "s_mov_b32 %[miss], 0\n\t"
        "s_branch Lend%=\n\t"
        "Lm%=:\n\t"
        "buffer_store_dword v62, %[sr], %[rs], 0 offen offset:12 sc1\n\t"
        "Lend%=:\n\t"
        "s_waitcnt lgkmcnt(0)\n\t"
        : [c0] "+&s"(c0), [r0] "+&s"(r0), [tl] "=&s"(tl), [miss] "=&s"(miss)
        : [m0o] "v"(m0o), [m1o] "v"(m1o), [sr] "v"(sr), [rs] "s"(rs), [so] "s"(so), [tg] "s"(tg), [kr] "s"(kr), [rec] "{v[56:59]}"(rec)
        : "s64", "s65", "s66", "s67", "s68", "s69", "s70", "s71", "s72", "s73", "s74", "s75", "s76", "s77", "s78", "s79", "s80", "s81", "s82", "s83", "s84", "s85", "s86", "s87", "s88", "s89", "s90", "s91", "v60", "v61", "v62", "vcc", "scc", "memory");
}

__device__ __forceinline__ void r_t0(u32& c0, u32& r0, u32& t1, u32& t2, u32& ok, u32 tl, u32 t0, u32 kr, u32 fv, u32 lb, u32x4 rs, u32 so STP) {
    asm volatile(
        "s_setprio 3\n\t"
        "v_mov_b32 v20, %[lb]\n\t"
        "s_mov_b32 %[ok], 0\n\t"
        "s_or_b32 s92, %[fv], 0x10000\n\t"
        "s_lshr_b32 s95, %[t2], 16\n\t"
        "s_mul_i32 s95, s95, %[kr]\n\t"
        "s_lshr_b32 s95, s95, 12\n\t"
        "s_min_u32 s95, s95, 512\n\t"
        "s_andn2_b32 s95, s95, 3\n\t"
        "v_mov_b32 v21, s95\n\t"
        "ds_write_b32 v20, v21 offset:16384\n\t"
        "Ldl_%=:\n\t"
        "s_memrealtime s[90:91]\n\t"
        "s_waitcnt lgkmcnt(0)\n\t"
        "s_mul_i32 s90, s90, 24\n\t"
        "s_sub_u32 s88, %[tl], s90\n\t"
        "s_sub_u32 s89, s88, 620\n\t"
        "s_cmp_gt_u32 s89, 0x4000\n\t"
        "s_cbranch_scc1 Lel_%=\n\t"
        "s_sleep 1\n\t"
        "s_branch Ldl_%=\n\t"
        "Lel_%=:\n\t"
        "s_memrealtime s[64:65]\n\t"
        "s_nop 0\n\t"
        "s_memrealtime s[66:67]\n\t"
        "s_nop 0\n\t"
        "s_memrealtime s[68:69]\n\t"
        "s_nop 0\n\t"
        "s_memrealtime s[70:71]\n\t"
        "s_nop 0\n\t"
        "s_memrealtime s[72:73]\n\t"
        "s_nop 0\n\t"
        "s_memrealtime s[74:75]\n\t"
        "s_memtime s[90:91]\n\t"
        "s_memrealtime s[76:77]\n\t"
        "s_nop 0\n\t"
        "s_memrealtime s[78:79]\n\t"
        "s_nop 0\n\t"
        "s_memrealtime s[80:81]\n\t"
        "s_nop 0\n\t"
        "s_memrealtime s[82:83]\n\t"
        "s_nop 0\n\t"
        "s_memrealtime s[84:85]\n\t"
        "s_nop 0\n\t"
        "s_memrealtime s[86:87]\n\t"
        "s_nop 0\n\t"
        "s_nop 7\n\t"
        "s_waitcnt lgkmcnt(0)\n\t"
        "s_add_u32 s64, s64, s66\n\t"
        "s_add_u32 s64, s64, s68\n\t"
        "s_add_u32 s64, s64, s70\n\t"
        "s_add_u32 s64, s64, s72\n\t"
        "s_add_u32 s64, s64, s74\n\t"
        "s_add_u32 s64, s64, s76\n\t"
        "s_add_u32 s64, s64, s78\n\t"
        "s_add_u32 s64, s64, s80\n\t"
        "s_add_u32 s64, s64, s82\n\t"
        "s_add_u32 s64, s64, s84\n\t"
        "s_add_u32 s64, s64, s86\n\t"
        "s_lshl_b32 s64, s64, 1\n\t"
        "s_mov_b32 %[c0], s90\n\t"
        "s_mov_b32 %[r0], s64\n\t"
        "s_sub_u32 s88, %[tl], s64\n\t"
        "s_mul_i32 s88, s88, %[kr]\n\t"
        "s_ashr_i32 s88, s88, 12\n\t"
        "s_sub_u32 s88, s88, 244\n\t"
        "s_nop 1\n\t"
        "s_max_i32 s88, s88, 0\n\t"
        "s_min_i32 s88, s88, 384\n\t"
        "s_andn2_b32 s88, s88, 3\n\t"
        "s_getpc_b64 s[90:91]\n\t"
        "Lgl_%=:\n\t"
        "s_sub_u32 s90, s90, s88\n\t"
        "s_subb_u32 s91, s91, 0\n\t"
        "s_add_u32 s90, s90, Lzl_%=-Lgl_%=\n\t"
        "s_addc_u32 s91, s91, 0\n\t"
        "s_setpc_b64 s[90:91]\n\t"
        "s_nop 0\n\ts_nop 0\n\ts_nop 0\n\ts_nop 0\n\t"
        "s_nop 0\n\ts_nop 0\n\ts_nop 0\n\ts_nop 0\n\t"
        "s_nop 0\n\ts_nop 0\n\ts_nop 0\n\ts_nop 0\n\t"
        "s_nop 0\n\ts_nop 0\n\ts_nop 0\n\ts_nop 0\n\t"
        "s_nop 0\n\ts_nop 0\n\ts_nop 0\n\ts_nop 0\n\t"
        "s_nop 0\n\ts_nop 0\n\ts_nop 0\n\ts_nop 0\n\t"
        "s_nop 0\n\ts_nop 0\n\ts_nop 0\n\ts_nop 0\n\t"
        "s_nop 0\n\ts_nop 0\n\ts_nop 0\n\ts_nop 0\n\t"
        "s_nop 0\n\ts_nop 0\n\ts_nop 0\n\ts_nop 0\n\t"
        "s_nop 0\n\ts_nop 0\n\ts_nop 0\n\ts_nop 0\n\t"
        "s_nop 0\n\ts_nop 0\n\ts_nop 0\n\ts_nop 0\n\t"
        "s_nop 0\n\ts_nop 0\n\ts_nop 0\n\ts_nop 0\n\t"
        "s_nop 0\n\ts_nop 0\n\ts_nop 0\n\ts_nop 0\n\t"
        "s_nop 0\n\ts_nop 0\n\ts_nop 0\n\ts_nop 0\n\t"
        "s_nop 0\n\ts_nop 0\n\ts_nop 0\n\ts_nop 0\n\t"
        "s_nop 0\n\ts_nop 0\n\ts_nop 0\n\ts_nop 0\n\t"
        "s_nop 0\n\ts_nop 0\n\ts_nop 0\n\ts_nop 0\n\t"
        "s_nop 0\n\ts_nop 0\n\ts_nop 0\n\ts_nop 0\n\t"
        "s_nop 0\n\ts_nop 0\n\ts_nop 0\n\ts_nop 0\n\t"
        "s_nop 0\n\ts_nop 0\n\ts_nop 0\n\ts_nop 0\n\t"
        "s_nop 0\n\ts_nop 0\n\ts_nop 0\n\ts_nop 0\n\t"
        "s_nop 0\n\ts_nop 0\n\ts_nop 0\n\ts_nop 0\n\t"
        "s_nop 0\n\ts_nop 0\n\ts_nop 0\n\ts_nop 0\n\t"
        "s_nop 0\n\ts_nop 0\n\ts_nop 0\n\ts_nop 0\n\t"
        "Lzl_%=:\n\t"
        "s_buffer_load_dwordx4 s[48:51], %[rs], %[so] offset:0x0\n\t"
        "s_buffer_load_dwordx4 s[52:55], %[rs], %[so] offset:0x80\n\t"
        "s_buffer_load_dwordx4 s[56:59], %[rs], %[so] offset:0x100\n\t"
        "s_buffer_load_dwordx4 s[60:63], %[rs], %[so] offset:0x180\n\t"
        "s_sub_u32 s95, %[t0], %[r0]\n\t"
        "s_mul_i32 s95, s95, %[kr]\n\t"
        "s_ashr_i32 s95, s95, 12\n\t"
        "s_add_u32 s95, s95, %[c0]\n\t"
        "s_waitcnt lgkmcnt(0)\n\t"
        "s_cmp_eq_u32 s51, s92\n\t"
        "s_cbranch_scc1 Lend%=\n\t"
        "s_cmp_eq_u32 s55, s92\n\t"
        "s_cbranch_scc1 Lend%=\n\t"
        "s_cmp_eq_u32 s59, s92\n\t"
        "s_cbranch_scc1 Lend%=\n\t"
        "s_cmp_eq_u32 s63, s92\n\t"
        "s_cbranch_scc1 Lend%=\n\t"
        "Lda_%=:\n\t"
        "s_memtime s[90:91]\n\t"
        "s_waitcnt lgkmcnt(0)\n\t"
        "s_sub_u32 s88, s95, s90\n\t"
        "s_sub_u32 s89, s88, 336\n\t"
        "s_cmp_gt_u32 s89, 0x4000\n\t"
        "s_cbranch_scc1 Lea_%=\n\t"
        "s_sleep 1\n\t"
        "s_branch Lda_%=\n\t"
        "Lea_%=:\n\t"
        "s_sub_u32 s88, s88, 112\n\t"
        "s_max_i32 s88, s88, 0\n\t"
        "s_min_i32 s88, s88, 224\n\t"
        "s_andn2_b32 s88, s88, 3\n\t"
        "s_getpc_b64 s[90:91]\n\t"
        "Lga_%=:\n\t"
        "s_sub_u32 s90, s90, s88\n\t"
        "s_subb_u32 s91, s91, 0\n\t"
        "s_add_u32 s90, s90, Lza_%=-Lga_%=\n\t"
        "s_addc_u32 s91, s91, 0\n\t"
        "s_setpc_b64 s[90:91]\n\t"
        "s_nop 0\n\ts_nop 0\n\ts_nop 0\n\ts_nop 0\n\t"
        "s_nop 0\n\ts_nop 0\n\ts_nop 0\n\ts_nop 0\n\t"
        "s_nop 0\n\ts_nop 0\n\ts_nop 0\n\ts_nop 0\n\t"
        "s_nop 0\n\ts_nop 0\n\ts_nop 0\n\ts_nop 0\n\t"
        "s_nop 0\n\ts_nop 0\n\ts_nop 0\n\ts_nop 0\n\t"
        "s_nop 0\n\ts_nop 0\n\ts_nop 0\n\ts_nop 0\n\t"
        "s_nop 0\n\ts_nop 0\n\ts_nop 0\n\ts_nop 0\n\t"
        "s_nop 0\n\ts_nop 0\n\ts_nop 0\n\ts_nop 0\n\t"
        "s_nop 0\n\ts_nop 0\n\ts_nop 0\n\ts_nop 0\n\t"
        "s_nop 0\n\ts_nop 0\n\ts_nop 0\n\ts_nop 0\n\t"
        "s_nop 0\n\ts_nop 0\n\ts_nop 0\n\ts_nop 0\n\t"
        "s_nop 0\n\ts_nop 0\n\ts_nop 0\n\ts_nop 0\n\t"
        "s_nop 0\n\ts_nop 0\n\ts_nop 0\n\ts_nop 0\n\t"
        "s_nop 0\n\ts_nop 0\n\ts_nop 0\n\ts_nop 0\n\t"
        "Lza_%=:\n\t"
        "s_barrier\n\t"
        "s_setprio 0\n\t"
        "s_xor_b32 s51, s51, %[fv]\n\t"
        "s_xor_b32 s55, s55, %[fv]\n\t"
        "s_xor_b32 s59, s59, %[fv]\n\t"
        "s_xor_b32 s63, s63, %[fv]\n\t"
        "s_sub_u32 s53, s53, s49\n\t"
        "s_sub_u32 s57, s57, s49\n\t"
        "s_sub_u32 s61, s61, s49\n\t"
        "s_max_i32 s53, s53, s57\n\t"
        "s_max_i32 s61, s61, 0\n\t"
        "s_max_i32 s53, s53, s61\n\t"
        "s_add_u32 s49, s49, s53\n\t"
        "s_max_u32 s50, s50, s54\n\t"
        "s_max_u32 s50, s50, s58\n\t"
        "s_max_u32 s50, s50, s62\n\t"
        "s_or_b32 s58, s51, s55\n\t"
        "s_or_b32 s54, s59, s63\n\t"
        "s_or_b32 s54, s54, s58\n\t"
        "s_cmp_eq_u32 s54, 0\n\t"
        "s_cselect_b32 %[ok], 1, 2\n\t"
        "s_cselect_b32 %[t1], s49, %[t1]\n\t"
        "s_cselect_b32 %[t2], s50, %[t2]\n\t"
        "s_barrier\n\t"
        "Lend%=:\n\t"
        : [c0] "+&s"(c0), [r0] "+&s"(r0), [t1] "+&s"(t1), [t2] "+&s"(t2), [ok] "=&s"(ok)
        : [rs] "s"(rs), [so] "s"(so), [kr] "s"(kr), [fv] "s"(fv), [lb] "s"(lb), [tl] "s"(tl), [t0] "s"(t0)
        : "s48", "s49", "s50", "s51", "s52", "s53", "s54", "s55", "s56", "s57", "s58", "s59", "s60", "s61", "s62", "s63", "s64", "s65", "s66", "s67", "s68", "s69", "s70", "s71", "s72", "s73", "s74", "s75", "s76", "s77", "s78", "s79", "s80", "s81", "s82", "s83", "s84", "s85", "s86", "s87", "s88", "s89", "s90", "s91", "s92", "s93", "s94", "s95", "v20", "v21", "vcc", "scc", "memory");
}

__device__ __forceinline__ void r_t1(u32& c0, u32& r0, u32& t1, u32& t2, u32 tv, u32 kr, u32 fv, u32 lb, u32x4 rs, u32 so STP) {
    asm volatile(
        "s_setprio 3\n\t"
        "s_mov_b32 s92, 0x4000\n\t"
        "v_mov_b32 v20, %[lb]\n\t"
        "s_nop 2\n\t"
        "Lr%=:\n\t"
        "buffer_load_dwordx4 v[16:19], %[tv], %[rs], %[so] offen sc1\n\t"
        "s_memrealtime s[64:65]\n\t"
        "s_nop 0\n\t"
        "s_memrealtime s[66:67]\n\t"
        "s_nop 0\n\t"
        "s_memrealtime s[68:69]\n\t"
        "s_nop 0\n\t"
        "s_memrealtime s[70:71]\n\t"
        "s_nop 0\n\t"
        "s_memrealtime s[72:73]\n\t"
        "s_nop 0\n\t"
        "s_memrealtime s[74:75]\n\t"
        "s_memtime s[90:91]\n\t"
        "s_memrealtime s[76:77]\n\t"
        "s_nop 0\n\t"
        "s_memrealtime s[78:79]\n\t"
        "s_nop 0\n\t"
        "s_memrealtime s[80:81]\n\t"
        "s_nop 0\n\t"
        "s_memrealtime s[82:83]\n\t"
        "s_nop 0\n\t"
        "s_memrealtime s[84:85]\n\t"
        "s_nop 0\n\t"
        "s_memrealtime s[86:87]\n\t"
        "s_nop 0\n\t"
        "s_nop 7\n\t"
        "s_waitcnt lgkmcnt(0)\n\t"
        "s_add_u32 s64, s64, s66\n\t"
        "s_add_u32 s64, s64, s68\n\t"
        "s_add_u32 s64, s64, s70\n\t"
        "s_add_u32 s64, s64, s72\n\t"
        "s_add_u32 s64, s64, s74\n\t"
        "s_add_u32 s64, s64, s76\n\t"
        "s_add_u32 s64, s64, s78\n\t"
        "s_add_u32 s64, s64, s80\n\t"
        "s_add_u32 s64, s64, s82\n\t"
        "s_add_u32 s64, s64, s84\n\t"
        "s_add_u32 s64, s64, s86\n\t"
        "s_lshl_b32 s64, s64, 1\n\t"
        "s_mov_b32 %[c0], s90\n\t"
        "s_mov_b32 %[r0], s64\n\t"
        "s_sub_u32 s96, %[r0], 0x100000\n\t"
        "s_waitcnt vmcnt(0)\n\t"
        "v_cmp_eq_u32 vcc, %[fv], v19\n\t"
        "v_subrev_u32 v16, s96, v16\n\t"
        "v_subrev_u32 v17, s96, v17\n\t"
        "v_cndmask_b32 v16, 0, v16, vcc\n\t"
        "v_cndmask_b32 v17, 0, v17, vcc\n\t"
        "v_cndmask_b32 v18, 0, v18, vcc\n\t"
        "v_max_u32_dpp v16, v16, v16 quad_perm:[1,0,3,2] row_mask:0xf bank_mask:0xf\n\t"
        "v_max_u32_dpp v17, v17, v17 quad_perm:[1,0,3,2] row_mask:0xf bank_mask:0xf\n\t"
        "v_max_u32_dpp v18, v18, v18 quad_perm:[1,0,3,2] row_mask:0xf bank_mask:0xf\n\t"
        "v_max_u32_dpp v16, v16, v16 quad_perm:[2,3,0,1] row_mask:0xf bank_mask:0xf\n\t"
        "v_max_u32_dpp v17, v17, v17 quad_perm:[2,3,0,1] row_mask:0xf bank_mask:0xf\n\t"
        "v_max_u32_dpp v18, v18, v18 quad_perm:[2,3,0,1] row_mask:0xf bank_mask:0xf\n\t"
        "v_max_u32_dpp v16, v16, v16 row_half_mirror row_mask:0xf bank_mask:0xf\n\t"
        "v_max_u32_dpp v17, v17, v17 row_half_mirror row_mask:0xf bank_mask:0xf\n\t"
        "v_max_u32_dpp v18, v18, v18 row_half_mirror row_mask:0xf bank_mask:0xf\n\t"
        "s_lshr_b32 s94, vcc_lo, 4\n\t"
        "v_readfirstlane_b32 s93, v16\n\t"
        "v_readfirstlane_b32 %[t1], v17\n\t"
        "v_readfirstlane_b32 %[t2], v18\n\t"
        "s_add_u32 s93, s93, s96\n\t"
        "s_add_u32 %[t1], %[t1], s96\n\t"
        "s_or_b32 s94, s94, vcc_lo\n\t"
        "s_and_b32 s94, s94, 15\n\t"
        "s_cmp_eq_u32 s94, 15\n\t"
        "s_cbranch_scc0 Lx%=\n\t"
        "Lgo%=:\n\t"
        "s_lshr_b32 s95, %[t2], 16\n\t"
        "s_mul_i32 s95, s95, %[kr]\n\t"
        "s_lshr_b32 s95, s95, 12\n\t"
        "s_min_u32 s95, s95, 512\n\t"
        "s_andn2_b32 s95, s95, 3\n\t"
        "v_mov_b32 v21, s95\n\t"
        "ds_write_b32 v20, v21 offset:16384\n\t"
        "s_sub_u32 s95, s93, %[r0]\n\t"
        "s_mul_i32 s95, s95, %[kr]\n\t"
        "s_ashr_i32 s95, s95, 12\n\t"
        "s_add_u32 s95, s95, %[c0]\n\t"
        "Lda_%=:\n\t"
        "s_memtime s[90:91]\n\t"
        "s_waitcnt lgkmcnt(0)\n\t"
        "s_sub_u32 s88, s95, s90\n\t"
        "s_sub_u32 s89, s88, 336\n\t"
        "s_cmp_gt_u32 s89, 0x4000\n\t"
        "s_cbranch_scc1 Lea_%=\n\t"
        "s_sleep 1\n\t"
        "s_branch Lda_%=\n\t"
        "Lea_%=:\n\t"
        "s_sub_u32 s88, s88, 112\n\t"
        "s_max_i32 s88, s88, 0\n\t"
        "s_min_i32 s88, s88, 224\n\t"
        "s_andn2_b32 s88, s88, 3\n\t"
        "s_getpc_b64 s[90:91]\n\t"
        "Lga_%=:\n\t"
        "s_sub_u32 s90, s90, s88\n\t"
        "s_subb_u32 s91, s91, 0\n\t"
        "s_add_u32 s90, s90, Lza_%=-Lga_%=\n\t"
        "s_addc_u32 s91, s91, 0\n\t"
        "s_setpc_b64 s[90:91]\n\t"
        "s_nop 0\n\ts_nop 0\n\ts_nop 0\n\ts_nop 0\n\t"
        "s_nop 0\n\ts_nop 0\n\ts_nop 0\n\ts_nop 0\n\t"
        "s_nop 0\n\ts_nop 0\n\ts_nop 0\n\ts_nop 0\n\t"
        "s_nop 0\n\ts_nop 0\n\ts_nop 0\n\ts_nop 0\n\t"
        "s_nop 0\n\ts_nop 0\n\ts_nop 0\n\ts_nop 0\n\t"
        "s_nop 0\n\ts_nop 0\n\ts_nop 0\n\ts_nop 0\n\t"
        "s_nop 0\n\ts_nop 0\n\ts_nop 0\n\ts_nop 0\n\t"
        "s_nop 0\n\ts_nop 0\n\ts_nop 0\n\ts_nop 0\n\t"
        "s_nop 0\n\ts_nop 0\n\ts_nop 0\n\ts_nop 0\n\t"
        "s_nop 0\n\ts_nop 0\n\ts_nop 0\n\ts_nop 0\n\t"
        "s_nop 0\n\ts_nop 0\n\ts_nop 0\n\ts_nop 0\n\t"
        "s_nop 0\n\ts_nop 0\n\ts_nop 0\n\ts_nop 0\n\t"
        "s_nop 0\n\ts_nop 0\n\ts_nop 0\n\ts_nop 0\n\t"
        "s_nop 0\n\ts_nop 0\n\ts_nop 0\n\ts_nop 0\n\t"
        "Lza_%=:\n\t"
        "s_barrier\n\t"
        "s_setprio 0\n\t"
        "s_barrier\n\t"
        "s_branch Lend%=\n\t"
        "Lx%=:\n\t"
        "s_sub_u32 s92, s92, 1\n\t"
        "s_cbranch_scc1 Lgo%=\n\t"
        "s_branch Lr%=\n\t"
        "Lend%=:\n\t"
        : [c0] "+&s"(c0), [r0] "+&s"(r0), [t1] "+&s"(t1), [t2] "+&s"(t2)
        : [tv] "v"(tv), [rs] "s"(rs), [so] "s"(so), [kr] "s"(kr), [fv] "s"(fv), [lb] "s"(lb)
        : "s64", "s65", "s66", "s67", "s68", "s69", "s70", "s71", "s72", "s73", "s74", "s75", "s76", "s77", "s78", "s79", "s80", "s81", "s82", "s83", "s84", "s85", "s86", "s87", "s88", "s89", "s90", "s91", "s92", "s93", "s94", "s95", "s96", "v16", "v17", "v18", "v19", "v20", "v21", "vcc", "scc", "memory");
}

__device__ __forceinline__ void r_u(u32x32& bn, u32& s, u32& have, u32& left, u32& why, u32 sidx, u32 eo, u32 uw, u32 fa, u32 vm, u32 fo, u32 te, u32 tb, u32x4 rs, u32 lag, u32 lb STP LGP) {
    asm volatile(
        "v_mov_b32 v125, %[lb]\n\t"
        "v_mov_b32 v80, 0xffbfffbf\n\t"
        "v_mov_b32 v81, v80\n\t"
        "ds_write_b64 %[uw], v[80:81] offset:0\n\t"
        "ds_write_b64 %[uw], v[80:81] offset:512\n\t"
        "ds_write_b64 %[uw], v[80:81] offset:1024\n\t"
        "ds_write_b64 %[uw], v[80:81] offset:1536\n\t"
        "Lp%=:\n\t"
        "v_readlane_b32 s82, %[te], %[s]\n\t"
        "v_readlane_b32 s83, %[tb], %[s]\n\t"
        "s_add_u32 s92, %[s], 1\n\t"
        "s_mul_i32 s93, s92, 0x10001\n\t"
        "s_mov_b32 s95, %[left]\n\t"
        "s_mov_b32 s94, 0\n\t"
        "v_mov_b32 v124, s93\n\t"
        "s_add_u32 s97, %[s], 4\n\t"
        "s_cmp_le_u32 %[have], s97\n\t"
        "s_cselect_b32 s96, 0, 0x80000000\n\t"
        "v_or_b32 v126, s96, %[fo]\n\t"
        "s_add_u32 s84, s83, 0x1000\n\t"
        "s_setprio 2\n\t"
        "s_waitcnt lgkmcnt(0)\n\t"
        "s_getpc_b64 s[90:91]\n\t"
        "Lj%=:\n\t"
        "s_sub_u32 s90, s90, %[lag]\n\t"
        "s_subb_u32 s91, s91, 0\n\t"
        "s_add_u32 s90, s90, Lg%=-Lj%=\n\t"
        "s_addc_u32 s91, s91, 0\n\t"
        "s_barrier\n\t"
        "s_setpc_b64 s[90:91]\n\t"
        "s_nop 10\n\t"
        "s_nop 10\n\t"
        "s_nop 10\n\t"
        "Lg%=:\n\t"
        "buffer_load_dwordx4 v[48:51], %[eo], %[rs], s82 offen offset:0 sc1\n\t"
        "buffer_load_dwordx4 v[52:55], %[eo], %[rs], s82 offen offset:1024 sc1\n\t"
        "buffer_load_dwordx4 v[56:59], %[eo], %[rs], s82 offen offset:2048 sc1\n\t"
        "buffer_load_dwordx4 v[60:63], %[eo], %[rs], s82 offen offset:3072 sc1\n\t"
        "v_mov_b64 v[64:65], 0\n\t"
        "v_mov_b64 v[66:67], 0\n\t"
        "v_mov_b64 v[68:69], 0\n\t"
        "v_mov_b64 v[70:71], 0\n\t"
        "v_mov_b64 v[72:73], 0\n\t"
        "v_mov_b64 v[74:75], 0\n\t"
        "v_mov_b64 v[76:77], 0\n\t"
        "v_mov_b64 v[78:79], 0\n\t"
        "s_waitcnt vmcnt(3)\n\t"
        "v_smfmac_f32_16x16x64_bf16 v[64:67], v[48:51], v[16:23], %[sidx]\n\t"
        "s_waitcnt vmcnt(2)\n\t"
        "v_smfmac_f32_16x16x64_bf16 v[68:71], v[52:55], v[24:31], %[sidx]\n\t"
        "s_waitcnt vmcnt(1)\n\t"
        "v_smfmac_f32_16x16x64_bf16 v[72:75], v[56:59], v[32:39], %[sidx]\n\t"
        "v_cvt_pk_bf16_f32 v80, v64, v65\n\t"
        "v_cvt_pk_bf16_f32 v81, v66, v67\n\t"
        "ds_write_b64 %[uw], v[80:81] offset:0\n\t"
        "s_waitcnt vmcnt(0)\n\t"
        "v_smfmac_f32_16x16x64_bf16 v[76:79], v[60:63], v[40:47], %[sidx]\n\t"
        "v_cvt_pk_bf16_f32 v82, v68, v69\n\t"
        "v_cvt_pk_bf16_f32 v83, v70, v71\n\t"
        "ds_write_b64 %[uw], v[82:83] offset:512\n\t"
        "v_cvt_pk_bf16_f32 v84, v72, v73\n\t"
        "v_cvt_pk_bf16_f32 v85, v74, v75\n\t"
        "ds_write_b64 %[uw], v[84:85] offset:1024\n\t"
        "s_nop 1\n\t"
        "v_cvt_pk_bf16_f32 v86, v76, v77\n\t"
        "v_cvt_pk_bf16_f32 v87, v78, v79\n\t"
        "ds_write_b64 %[uw], v[86:87] offset:1536\n\t"
        "s_setprio 0\n\t"
        "s_cmp_lg_u32 s94, 0\n\t"
        "s_cbranch_scc1 Lc%=\n\t"
        "s_waitcnt lgkmcnt(0)\n\t"
        "s_barrier\n\t"
        "ds_read_b32 v88, v125 offset:16392\n\t"
        "s_waitcnt lgkmcnt(0)\n\t"
        "v_cmp_eq_u32 vcc, s93, v88\n\t"
        "s_cbranch_vccnz Lc%=\n\t"
        "Lok%=:\n\t"
        "s_cmp_gt_u32 %[have], s92\n\t"
        "s_cselect_b32 %[why], 0, 2\n\t"
        "buffer_load_dwordx2 v[122:123], v126, %[rs], 0 offen sc1\n\t"
        "s_cbranch_scc0 Ln%=\n\t"
        "buffer_load_dwordx4 v[90:93], %[vm], %[rs], s83 offen offset:0 sc1\n\t"
        "buffer_load_dwordx4 v[94:97], %[vm], %[rs], s83 offen offset:1024 sc1\n\t"
        "buffer_load_dwordx4 v[98:101], %[vm], %[rs], s83 offen offset:2048 sc1\n\t"
        "buffer_load_dwordx4 v[102:105], %[vm], %[rs], s83 offen offset:3072 sc1\n\t"
        "buffer_load_dwordx4 v[106:109], %[vm], %[rs], s84 offen offset:0 sc1\n\t"
        "buffer_load_dwordx4 v[110:113], %[vm], %[rs], s84 offen offset:1024 sc1\n\t"
        "buffer_load_dwordx4 v[114:117], %[vm], %[rs], s84 offen offset:2048 sc1\n\t"
        "buffer_load_dwordx4 v[118:121], %[vm], %[rs], s84 offen offset:3072 sc1\n\t"
        "Ln%=:\n\t"
        "ds_read_b32 v88, v125 offset:16396\n\t"
        "v_mov_b32 v80, 0xffbfffbf\n\t"
        "v_mov_b32 v81, v80\n\t"
        "s_sub_u32 s95, s95, %[left]\n\t"
        "s_mov_b32 %[s], s92\n\t"
        "s_waitcnt lgkmcnt(0)\n\t"
        "v_cmp_eq_u32 vcc, s93, v88\n\t"
        "s_cmp_eq_u32 %[s], 63\n\t"
        "s_cbranch_vccnz Lr%=\n\t"
        "s_cbranch_scc1 Lz%=\n\t"
        "s_cmp_lg_u32 %[why], 0\n\t"
        "s_cbranch_scc1 Lend%=\n\t"
        "ds_write_b64 %[uw], v[80:81] offset:0\n\t"
        "ds_write_b64 %[uw], v[80:81] offset:512\n\t"
        "ds_write_b64 %[uw], v[80:81] offset:1024\n\t"
        "ds_write_b64 %[uw], v[80:81] offset:1536\n\t"
        "s_waitcnt vmcnt(8)\n\t"
        "v_lshlrev_b32 v122, 1, v122\n\t"
        "v_lshl_or_b32 v123, v123, 1, 1\n\t"
        "v_min_u32 v122, v122, v123\n\t"
        "s_waitcnt vmcnt(6)\n\t"
        "v_mov_b32 v16, v90\n\t"
        "v_mov_b32 v17, v94\n\t"
        "v_min_u32_dpp v122, v122, v122 quad_perm:[1,0,3,2] row_mask:0xf bank_mask:0xf\n\t"
        "v_mov_b32 v18, v91\n\t"
        "v_mov_b32 v19, v95\n\t"
        "v_min_u32_dpp v122, v122, v122 quad_perm:[2,3,0,1] row_mask:0xf bank_mask:0xf\n\t"
        "v_mov_b32 v20, v92\n\t"
        "v_mov_b32 v21, v96\n\t"
        "v_min_u32_dpp v122, v122, v122 row_ror:4 row_mask:0xf bank_mask:0xf\n\t"
        "v_mov_b32 v22, v93\n\t"
        "v_mov_b32 v23, v97\n\t"
        "v_readfirstlane_b32 s96, v122\n\t"
        "s_waitcnt vmcnt(4)\n\t"
        "v_mov_b32 v24, v98\n\t"
        "v_mov_b32 v25, v102\n\t"
        "v_mov_b32 v26, v99\n\t"
        "v_mov_b32 v27, v103\n\t"
        "v_mov_b32 v28, v100\n\t"
        "v_mov_b32 v29, v104\n\t"
        "v_mov_b32 v30, v101\n\t"
        "v_mov_b32 v31, v105\n\t"
        "s_waitcnt vmcnt(2)\n\t"
        "v_mov_b32 v32, v106\n\t"
        "v_mov_b32 v33, v110\n\t"
        "v_mov_b32 v34, v107\n\t"
        "v_mov_b32 v35, v111\n\t"
        "v_mov_b32 v36, v108\n\t"
        "v_mov_b32 v37, v112\n\t"
        "v_mov_b32 v38, v109\n\t"
        "v_mov_b32 v39, v113\n\t"
        "s_max_u32 %[have], %[have], s96\n\t"
        "s_waitcnt vmcnt(0)\n\t"
        "v_mov_b32 v40, v114\n\t"
        "v_mov_b32 v41, v118\n\t"
        "v_mov_b32 v42, v115\n\t"
        "v_mov_b32 v43, v119\n\t"
        "v_mov_b32 v44, v116\n\t"
        "v_mov_b32 v45, v120\n\t"
        "v_mov_b32 v46, v117\n\t"
        "v_mov_b32 v47, v121\n\t"
        "s_branch Lp%=\n\t"
        "Lc%=:\n\t"
        "v_max_u32 v88, v48, v49\n\t"
        "v_max3_u32 v88, v88, v50, v51\n\t"
        "v_max3_u32 v88, v88, v52, v53\n\t"
        "v_max3_u32 v88, v88, v54, v55\n\t"
        "v_max3_u32 v88, v88, v56, v57\n\t"
        "v_max3_u32 v88, v88, v58, v59\n\t"
        "v_max3_u32 v88, v88, v60, v61\n\t"
        "v_max3_u32 v88, v88, v62, v63\n\t"
        "v_cmp_eq_u32 vcc, 0xffbfffbf, v88\n\t"
        "s_mov_b32 s94, 1\n\t"
        "s_cbranch_vccz Ld%=\n\t"
        "s_sub_u32 %[left], %[left], 1\n\t"
        "s_cbranch_scc0 Lg%=\n\t"
        "s_mov_b32 %[left], 0\n\t"
        "Ld%=:\n\t"
        "ds_write_b32 %[fa], v124\n\t"
        "s_waitcnt lgkmcnt(0)\n\t"
        "s_barrier\n\t"
        "s_branch Lok%=\n\t"
        "Lr%=:\n\t"
        "s_mov_b32 %[why], 3\n\t"
        "s_branch Lend%=\n\t"
        "Lz%=:\n\t"
        "s_mov_b32 %[why], 0\n\t"
        "Lend%=:\n\t"
        "s_waitcnt vmcnt(0)\n\t"
        : [bn] "+{v[16:47]}"(bn), [s] "+&s"(s), [have] "+&s"(have), [left] "+&s"(left), [why] "=&s"(why)
        : [sidx] "v"(sidx), [eo] "v"(eo), [uw] "v"(uw), [fa] "v"(fa), [vm] "v"(vm), [fo] "v"(fo), [te] "v"(te), [tb] "v"(tb), [rs] "s"(rs), [lag] "s"(lag), [lb] "s"(lb)
        : "s82", "s83", "s84", "s85", "s86", "s87", "s88", "s89", "s90", "s91", "s92", "s93", "s94", "s95", "s96", "s97", "s98", "s99", "v48", "v49", "v50", "v51", "v52", "v53", "v54", "v55", "v56", "v57", "v58", "v59", "v60", "v61", "v62", "v63", "v64", "v65", "v66", "v67", "v68", "v69", "v70", "v71", "v72", "v73", "v74", "v75", "v76", "v77", "v78", "v79", "v80", "v81", "v82", "v83", "v84", "v85", "v86", "v87", "v88", "v89", "v90", "v91", "v92", "v93", "v94", "v95", "v96", "v97", "v98", "v99", "v100", "v101", "v102", "v103", "v104", "v105", "v106", "v107", "v108", "v109", "v110", "v111", "v112", "v113", "v114", "v115", "v116", "v117", "v118", "v119", "v120", "v121", "v122", "v123", "v124", "v125", "v126", "vcc", "scc", "memory");
}

__device__ __forceinline__ void r_f(u32& bad, u32& pass, u32 sidx, u32 pv, u32 zv, u32 ra, u32 rb, u32 mo, u32 uf, u32x4 rs, u32 so, u32 sz, u32 sm, u32 lb, u32 fv, u32 cap, u32 zp STP) {
    asm volatile(
        "v_mov_b32 v120, %[lb]\n\t"
        "s_add_u32 s92, %[sm], 0x1000\n\t"
        "s_add_u32 s93, %[sm], 0x4000\n\t"
        "s_add_u32 s94, %[sm], 0x5000\n\t"
        "s_add_u32 s95, %[so], 0x8000\n\t"
        "s_add_u32 s96, %[sz], 0x1000\n\t"
        "s_mov_b32 %[pass], 0\n\t"
        "s_mov_b32 s97, 0\n\t"
        "s_mov_b32 %[bad], 0\n\t"
        "s_setprio 3\n\t"
        "s_barrier\n\t"
        "ds_read_b32 v116, v120 offset:16384\n\t"
        "s_waitcnt lgkmcnt(0)\n\t"
        "v_readfirstlane_b32 s98, v116\n\t"
        "s_nop 3\n\t"
        "s_getpc_b64 s[90:91]\n\t"
        "Lgp_%=:\n\t"
        "s_sub_u32 s90, s90, s98\n\t"
        "s_subb_u32 s91, s91, 0\n\t"
        "s_add_u32 s90, s90, Lzp_%=-Lgp_%=\n\t"
        "s_addc_u32 s91, s91, 0\n\t"
        "s_setpc_b64 s[90:91]\n\t"
        "s_nop 0\n\ts_nop 0\n\ts_nop 0\n\ts_nop 0\n\t"
        "s_nop 0\n\ts_nop 0\n\ts_nop 0\n\ts_nop 0\n\t"
        "s_nop 0\n\ts_nop 0\n\ts_nop 0\n\ts_nop 0\n\t"
        "s_nop 0\n\ts_nop 0\n\ts_nop 0\n\ts_nop 0\n\t"
        "s_nop 0\n\ts_nop 0\n\ts_nop 0\n\ts_nop 0\n\t"
        "s_nop 0\n\ts_nop 0\n\ts_nop 0\n\ts_nop 0\n\t"
        "s_nop 0\n\ts_nop 0\n\ts_nop 0\n\ts_nop 0\n\t"
        "s_nop 0\n\ts_nop 0\n\ts_nop 0\n\ts_nop 0\n\t"
        "s_nop 0\n\ts_nop 0\n\ts_nop 0\n\ts_nop 0\n\t"
        "s_nop 0\n\ts_nop 0\n\ts_nop 0\n\ts_nop 0\n\t"
        "s_nop 0\n\ts_nop 0\n\ts_nop 0\n\ts_nop 0\n\t"
        "s_nop 0\n\ts_nop 0\n\ts_nop 0\n\ts_nop 0\n\t"
        "s_nop 0\n\ts_nop 0\n\ts_nop 0\n\ts_nop 0\n\t"
        "s_nop 0\n\ts_nop 0\n\ts_nop 0\n\ts_nop 0\n\t"
        "s_nop 0\n\ts_nop 0\n\ts_nop 0\n\ts_nop 0\n\t"
        "s_nop 0\n\ts_nop 0\n\ts_nop 0\n\ts_nop 0\n\t"
        "s_nop 0\n\ts_nop 0\n\ts_nop 0\n\ts_nop 0\n\t"
        "s_nop 0\n\ts_nop 0\n\ts_nop 0\n\ts_nop 0\n\t"
        "s_nop 0\n\ts_nop 0\n\ts_nop 0\n\ts_nop 0\n\t"
        "s_nop 0\n\ts_nop 0\n\ts_nop 0\n\ts_nop 0\n\t"
        "s_nop 0\n\ts_nop 0\n\ts_nop 0\n\ts_nop 0\n\t"
        "s_nop 0\n\ts_nop 0\n\ts_nop 0\n\ts_nop 0\n\t"
        "s_nop 0\n\ts_nop 0\n\ts_nop 0\n\ts_nop 0\n\t"
        "s_nop 0\n\ts_nop 0\n\ts_nop 0\n\ts_nop 0\n\t"
        "s_nop 0\n\ts_nop 0\n\ts_nop 0\n\ts_nop 0\n\t"
        "s_nop 0\n\ts_nop 0\n\ts_nop 0\n\ts_nop 0\n\t"
        "s_nop 0\n\ts_nop 0\n\ts_nop 0\n\ts_nop 0\n\t"
        "s_nop 0\n\ts_nop 0\n\ts_nop 0\n\ts_nop 0\n\t"
        "s_nop 0\n\ts_nop 0\n\ts_nop 0\n\ts_nop 0\n\t"
        "s_nop 0\n\ts_nop 0\n\ts_nop 0\n\ts_nop 0\n\t"
        "s_nop 0\n\ts_nop 0\n\ts_nop 0\n\ts_nop 0\n\t"
        "s_nop 0\n\ts_nop 0\n\ts_nop 0\n\ts_nop 0\n\t"
        "Lzp_%=:\n\t"
        "buffer_load_dwordx4 v[48:51], %[zv], %[rs], %[sz] offen offset:0 sc1\n\t"
        "buffer_load_dwordx4 v[52:55], %[zv], %[rs], %[sz] offen offset:1024 sc1\n\t"
        "buffer_load_dwordx4 v[56:59], %[zv], %[rs], %[sz] offen offset:2048 sc1\n\t"
        "s_getpc_b64 s[90:91]\n\t"
        "Lgq_%=:\n\t"
        "s_sub_u32 s90, s90, %[zp]\n\t"
        "s_subb_u32 s91, s91, 0\n\t"
        "s_add_u32 s90, s90, Lzq_%=-Lgq_%=\n\t"
        "s_addc_u32 s91, s91, 0\n\t"
        "s_setpc_b64 s[90:91]\n\t"
        "s_nop 0\n\ts_nop 0\n\ts_nop 0\n\ts_nop 0\n\t"
        "s_nop 0\n\ts_nop 0\n\ts_nop 0\n\ts_nop 0\n\t"
        "s_nop 0\n\ts_nop 0\n\ts_nop 0\n\ts_nop 0\n\t"
        "s_nop 0\n\ts_nop 0\n\ts_nop 0\n\ts_nop 0\n\t"
        "s_nop 0\n\ts_nop 0\n\ts_nop 0\n\ts_nop 0\n\t"
        "s_nop 0\n\ts_nop 0\n\ts_nop 0\n\ts_nop 0\n\t"
        "s_nop 0\n\ts_nop 0\n\ts_nop 0\n\ts_nop 0\n\t"
        "s_nop 0\n\ts_nop 0\n\ts_nop 0\n\ts_nop 0\n\t"
        "s_nop 0\n\ts_nop 0\n\ts_nop 0\n\ts_nop 0\n\t"
        "s_nop 0\n\ts_nop 0\n\ts_nop 0\n\ts_nop 0\n\t"
        "s_nop 0\n\ts_nop 0\n\ts_nop 0\n\ts_nop 0\n\t"
        "s_nop 0\n\ts_nop 0\n\ts_nop 0\n\ts_nop 0\n\t"
        "s_nop 0\n\ts_nop 0\n\ts_nop 0\n\ts_nop 0\n\t"
        "s_nop 0\n\ts_nop 0\n\ts_nop 0\n\ts_nop 0\n\t"
        "s_nop 0\n\ts_nop 0\n\ts_nop 0\n\ts_nop 0\n\t"
        "s_nop 0\n\ts_nop 0\n\ts_nop 0\n\ts_nop 0\n\t"
        "Lzq_%=:\n\t"
        "buffer_load_dwordx4 v[14:17], %[pv], %[rs], %[so] offen offset:0 sc1\n\t"
        "buffer_load_dwordx4 v[60:63], %[zv], %[rs], %[sz] offen offset:3072 sc1\n\t"
        "buffer_load_dwordx4 v[18:21], %[pv], %[rs], %[so] offen offset:256 sc1\n\t"
        "buffer_load_dwordx4 v[64:67], %[zv], %[rs], s96 offen offset:0 sc1\n\t"
        "buffer_load_dwordx4 v[22:25], %[pv], %[rs], %[so] offen offset:1024 sc1\n\t"
        "buffer_load_dwordx4 v[68:71], %[zv], %[rs], s96 offen offset:1024 sc1\n\t"
        "buffer_load_dwordx4 v[26:29], %[pv], %[rs], %[so] offen offset:1280 sc1\n\t"
        "buffer_load_dwordx4 v[72:75], %[zv], %[rs], s96 offen offset:2048 sc1\n\t"
        "buffer_load_dwordx4 v[30:33], %[pv], %[rs], s95 offen offset:0 sc1\n\t"
        "buffer_load_dwordx4 v[76:79], %[zv], %[rs], s96 offen offset:3072 sc1\n\t"
        "buffer_load_dwordx4 v[34:37], %[pv], %[rs], s95 offen offset:256 sc1\n\t"
        "buffer_load_dwordx4 v[38:41], %[pv], %[rs], s95 offen offset:1024 sc1\n\t"
        "buffer_load_dwordx4 v[42:45], %[pv], %[rs], s95 offen offset:1280 sc1\n\t"
        "Lrd%=:\n\t"
        "v_mov_b64 v[112:113], 0\n\t"
        "v_mov_b64 v[114:115], 0\n\t"
        "ds_read_b32 v121, %[uf]\n\t"
        "s_waitcnt vmcnt(13)\n\t"
        "ds_read_b128 v[80:83], %[ra] offset:0\n\t"
        "ds_read_b128 v[84:87], %[ra] offset:1024\n\t"
        "s_waitcnt vmcnt(12)\n\t"
        "v_mov_b64 v[46:47], v[14:15]\n\t"
        "s_waitcnt vmcnt(10)\n\t"
        "v_smfmac_f32_16x16x64_bf16 v[112:115], v[16:19], v[48:55], %[sidx]\n\t"
        "s_waitcnt vmcnt(9)\n\t"
        "ds_read_b128 v[88:91], %[ra] offset:2048\n\t"
        "ds_read_b128 v[92:95], %[ra] offset:3072\n\t"
        "s_waitcnt vmcnt(8) lgkmcnt(2)\n\t"
        "v_smfmac_f32_16x16x64_bf16 v[112:115], v[20:23], v[80:87], %[sidx]\n\t"
        "s_waitcnt vmcnt(6)\n\t"
        "v_smfmac_f32_16x16x64_bf16 v[112:115], v[24:27], v[56:63], %[sidx]\n\t"
        "s_waitcnt vmcnt(5)\n\t"
        "ds_read_b128 v[96:99], %[ra] offset:4096\n\t"
        "ds_read_b128 v[100:103], %[ra] offset:5120\n\t"
        "ds_read_b128 v[104:107], %[rb] offset:6144\n\t"
        "ds_read_b128 v[108:111], %[rb] offset:7168\n\t"
        "s_waitcnt vmcnt(4) lgkmcnt(4)\n\t"
        "v_smfmac_f32_16x16x64_bf16 v[112:115], v[28:31], v[88:95], %[sidx]\n\t"
        "s_waitcnt vmcnt(2)\n\t"
        "v_smfmac_f32_16x16x64_bf16 v[112:115], v[32:35], v[64:71], %[sidx]\n\t"
        "s_waitcnt vmcnt(1) lgkmcnt(2)\n\t"
        "v_smfmac_f32_16x16x64_bf16 v[112:115], v[36:39], v[96:103], %[sidx]\n\t"
        "s_waitcnt vmcnt(0)\n\t"
        "v_smfmac_f32_16x16x64_bf16 v[112:115], v[40:43], v[72:79], %[sidx]\n\t"
        "s_waitcnt vmcnt(0) lgkmcnt(0)\n\t"
        "v_smfmac_f32_16x16x64_bf16 v[112:115], v[44:47], v[104:111], %[sidx]\n\t"
        "s_nop 7\n\t"
        "v_add_f32 v116, v112, v114\n\t"
        "v_add_f32 v117, v113, v115\n\t"
        "v_add_f32 v118, v113, v115\n\t"
        "v_mov_b32_dpp v118, v116 row_ror:8 row_mask:0xf bank_mask:0x3\n\t"
        "v_mov_b32_dpp v116, v117 row_ror:8 row_mask:0xf bank_mask:0xc\n\t"
        "v_cvt_pk_bf16_f32 v119, v116, v118\n\t"
        "v_cmpx_o_f32 vcc, v116, v118\n\t"
        "buffer_store_dword v119, %[mo], %[rs], %[sm] offen sc1\n\t"
        "buffer_store_dword v119, %[mo], %[rs], s92 offen sc1\n\t"
        "buffer_store_dword v119, %[mo], %[rs], s93 offen sc1\n\t"
        "buffer_store_dword v119, %[mo], %[rs], s94 offen sc1\n\t"
        "s_nop 2\n\t"
        "s_cmp_eq_u64 exec, -1\n\t"
        "s_mov_b64 exec, -1\n\t"
        "s_cbranch_scc0 Lag%=\n\t"
        "s_barrier\n\t"
        "s_branch Lend%=\n\t"
        "Lag%=:\n\t"
        "v_cmp_ne_u32 vcc, %[fv], v121\n\t"
        "s_add_u32 %[pass], %[pass], 1\n\t"
        "s_cmp_eq_u32 s97, 0\n\t"
        "s_cbranch_scc1 Lfi%=\n\t"
        "s_cbranch_vccnz Lzp_%=\n\t"
        "v_max_u32 v116, v16, v17\n\t"
        "v_max3_u32 v116, v116, v18, v19\n\t"
        "v_max3_u32 v116, v116, v20, v21\n\t"
        "v_max3_u32 v116, v116, v22, v23\n\t"
        "v_max3_u32 v116, v116, v24, v25\n\t"
        "v_max3_u32 v116, v116, v26, v27\n\t"
        "v_max3_u32 v116, v116, v28, v29\n\t"
        "v_max3_u32 v116, v116, v30, v31\n\t"
        "v_max3_u32 v116, v116, v32, v33\n\t"
        "v_max3_u32 v116, v116, v34, v35\n\t"
        "v_max3_u32 v116, v116, v36, v37\n\t"
        "v_max3_u32 v116, v116, v38, v39\n\t"
        "v_max3_u32 v116, v116, v40, v41\n\t"
        "v_max3_u32 v116, v116, v42, v43\n\t"
        "v_max3_u32 v116, v116, v44, v45\n\t"
        "v_max3_u32 v116, v116, v46, v47\n\t"
        "v_max3_u32 v116, v116, v48, v49\n\t"
        "v_max3_u32 v116, v116, v50, v51\n\t"
        "v_max3_u32 v116, v116, v52, v53\n\t"
        "v_max3_u32 v116, v116, v54, v55\n\t"
        "v_max3_u32 v116, v116, v56, v57\n\t"
        "v_max3_u32 v116, v116, v58, v59\n\t"
        "v_max3_u32 v116, v116, v60, v61\n\t"
        "v_max3_u32 v116, v116, v62, v63\n\t"
        "v_max3_u32 v116, v116, v64, v65\n\t"
        "v_max3_u32 v116, v116, v66, v67\n\t"
        "v_max3_u32 v116, v116, v68, v69\n\t"
        "v_max3_u32 v116, v116, v70, v71\n\t"
        "v_max3_u32 v116, v116, v72, v73\n\t"
        "v_max3_u32 v116, v116, v74, v75\n\t"
        "v_max3_u32 v116, v116, v76, v77\n\t"
        "v_max3_u32 v116, v116, v78, v79\n\t"
        "v_max3_u32 v116, v116, v80, v81\n\t"
        "v_max3_u32 v116, v116, v82, v83\n\t"
        "v_max3_u32 v116, v116, v84, v85\n\t"
        "v_max3_u32 v116, v116, v86, v87\n\t"
        "v_max3_u32 v116, v116, v88, v89\n\t"
        "v_max3_u32 v116, v116, v90, v91\n\t"
        "v_max3_u32 v116, v116, v92, v93\n\t"
        "v_max3_u32 v116, v116, v94, v95\n\t"
        "v_max3_u32 v116, v116, v96, v97\n\t"
        "v_max3_u32 v116, v116, v98, v99\n\t"
        "v_max3_u32 v116, v116, v100, v101\n\t"
        "v_max3_u32 v116, v116, v102, v103\n\t"
        "v_max3_u32 v116, v116, v104, v105\n\t"
        "v_max3_u32 v116, v116, v106, v107\n\t"
        "v_max3_u32 v116, v116, v108, v109\n\t"
        "v_max3_u32 v116, v116, v110, v111\n\t"
        "v_cmp_eq_u32 vcc, 0xffbfffbf, v116\n\t"
        "s_cmp_lt_u32 %[pass], %[cap]\n\t"
        "s_cbranch_vccz Lbad%=\n\t"
        "s_cbranch_scc1 Lzp_%=\n\t"
        "Lbad%=:\n\t"
        "s_mov_b32 %[bad], 1\n\t"
        "s_branch Lend%=\n\t"
        "Lfi%=:\n\t"
        "s_mov_b32 s97, 1\n\t"
        "v_mov_b32 v116, %[fv]\n\t"
        "ds_write_b32 v120, v116 offset:16392\n\t"
        "s_waitcnt lgkmcnt(0)\n\t"
        "s_barrier\n\t"
        "s_branch Lrd%=\n\t"
        "Lend%=:\n\t"
        "s_setprio 0\n\t"
        : [bad] "=&s"(bad), [pass] "=&s"(pass)
        : [sidx] "v"(sidx), [pv] "v"(pv), [zv] "v"(zv), [ra] "v"(ra), [rb] "v"(rb), [mo] "v"(mo), [uf] "v"(uf), [rs] "s"(rs), [so] "s"(so), [sz] "s"(sz), [sm] "s"(sm), [lb] "s"(lb), [fv] "s"(fv), [cap] "s"(cap), [zp] "s"(zp)
        : "s88", "s89", "s90", "s91", "s92", "s93", "s94", "s95", "s96", "s97", "s98", "v14", "v15", "v16", "v17", "v18", "v19", "v20", "v21", "v22", "v23", "v24", "v25", "v26", "v27", "v28", "v29", "v30", "v31", "v32", "v33", "v34", "v35", "v36", "v37", "v38", "v39", "v40", "v41", "v42", "v43", "v44", "v45", "v46", "v47", "v48", "v49", "v50", "v51", "v52", "v53", "v54", "v55", "v56", "v57", "v58", "v59", "v60", "v61", "v62", "v63", "v64", "v65", "v66", "v67", "v68", "v69", "v70", "v71", "v72", "v73", "v74", "v75", "v76", "v77", "v78", "v79", "v80", "v81", "v82", "v83", "v84", "v85", "v86", "v87", "v88", "v89", "v90", "v91", "v92", "v93", "v94", "v95", "v96", "v97", "v98", "v99", "v100", "v101", "v102", "v103", "v104", "v105", "v106", "v107", "v108", "v109", "v110", "v111", "v112", "v113", "v114", "v115", "v116", "v117", "v118", "v119", "v120", "v121", "vcc", "scc", "memory");
}

__device__ __forceinline__ void wx0(const u32x32 (&W)[5], u32 xa, u32 oa, u32x4 rs, u32 xv, u32 xs, u32 xd, u32 o0, u32 o1 STP) {
    asm volatile(
        "ds_read_b128 v[192:195], %[xa] offset:0\n\t"
        "ds_read_b128 v[196:199], %[xa] offset:20480\n\t"
        "ds_read_b128 v[200:203], %[xa] offset:1024\n\t"
        "ds_read_b128 v[204:207], %[xa] offset:21504\n\t"
        "ds_read_b128 v[208:211], %[xa] offset:2048\n\t"
        "ds_read_b128 v[212:215], %[xa] offset:22528\n\t"
        "s_waitcnt vmcnt(8)\n\t"
        "s_barrier\n\t"
        "s_waitcnt lgkmcnt(4)\n\t"
        "v_mfma_f32_16x16x32_bf16 v[176:179], v[16:19], v[192:195], 0\n\t"
        "ds_read_b128 v[216:219], %[xa] offset:3072\n\t"
        "v_mfma_f32_16x16x32_bf16 v[184:187], v[16:19], v[196:199], 0\n\t"
        "ds_read_b128 v[220:223], %[xa] offset:23552\n\t"
        "v_mfma_f32_16x16x32_bf16 v[180:183], v[96:99], v[192:195], 0\n\t"
        "v_mfma_f32_16x16x32_bf16 v[188:191], v[96:99], v[196:199], 0\n\t"
        "s_waitcnt lgkmcnt(4)\n\t"
        "v_mfma_f32_16x16x32_bf16 v[176:179], v[20:23], v[200:203], v[176:179]\n\t"
        "ds_read_b128 v[192:195], %[xa] offset:4096\n\t"
        "v_mfma_f32_16x16x32_bf16 v[184:187], v[20:23], v[204:207], v[184:187]\n\t"
        "ds_read_b128 v[196:199], %[xa] offset:24576\n\t"
        "v_mfma_f32_16x16x32_bf16 v[180:183], v[100:103], v[200:203], v[180:183]\n\t"
        "v_mfma_f32_16x16x32_bf16 v[188:191], v[100:103], v[204:207], v[188:191]\n\t"
        "s_waitcnt lgkmcnt(4)\n\t"
        "v_mfma_f32_16x16x32_bf16 v[176:179], v[24:27], v[208:211], v[176:179]\n\t"
        "ds_read_b128 v[200:203], %[xa] offset:5120\n\t"
        "v_mfma_f32_16x16x32_bf16 v[184:187], v[24:27], v[212:215], v[184:187]\n\t"
        "ds_read_b128 v[204:207], %[xa] offset:25600\n\t"
        "v_mfma_f32_16x16x32_bf16 v[180:183], v[104:107], v[208:211], v[180:183]\n\t"
        "v_mfma_f32_16x16x32_bf16 v[188:191], v[104:107], v[212:215], v[188:191]\n\t"
        "s_waitcnt lgkmcnt(4)\n\t"
        "v_mfma_f32_16x16x32_bf16 v[176:179], v[28:31], v[216:219], v[176:179]\n\t"
        "ds_read_b128 v[208:211], %[xa] offset:6144\n\t"
        "v_mfma_f32_16x16x32_bf16 v[184:187], v[28:31], v[220:223], v[184:187]\n\t"
        "ds_read_b128 v[212:215], %[xa] offset:26624\n\t"
        "v_mfma_f32_16x16x32_bf16 v[180:183], v[108:111], v[216:219], v[180:183]\n\t"
        "v_mfma_f32_16x16x32_bf16 v[188:191], v[108:111], v[220:223], v[188:191]\n\t"
        "s_waitcnt lgkmcnt(4)\n\t"
        "v_mfma_f32_16x16x32_bf16 v[176:179], v[32:35], v[192:195], v[176:179]\n\t"
        "ds_read_b128 v[216:219], %[xa] offset:7168\n\t"
        "v_mfma_f32_16x16x32_bf16 v[184:187], v[32:35], v[196:199], v[184:187]\n\t"
        "ds_read_b128 v[220:223], %[xa] offset:27648\n\t"
        "v_mfma_f32_16x16x32_bf16 v[180:183], v[112:115], v[192:195], v[180:183]\n\t"
        "v_mfma_f32_16x16x32_bf16 v[188:191], v[112:115], v[196:199], v[188:191]\n\t"
        "s_waitcnt lgkmcnt(4)\n\t"
        "v_mfma_f32_16x16x32_bf16 v[176:179], v[36:39], v[200:203], v[176:179]\n\t"
        "ds_read_b128 v[192:195], %[xa] offset:8192\n\t"
        "v_mfma_f32_16x16x32_bf16 v[184:187], v[36:39], v[204:207], v[184:187]\n\t"
        "ds_read_b128 v[196:199], %[xa] offset:28672\n\t"
        "v_mfma_f32_16x16x32_bf16 v[180:183], v[116:119], v[200:203], v[180:183]\n\t"
        "v_mfma_f32_16x16x32_bf16 v[188:191], v[116:119], v[204:207], v[188:191]\n\t"
        "s_waitcnt lgkmcnt(4)\n\t"
        "v_mfma_f32_16x16x32_bf16 v[176:179], v[40:43], v[208:211], v[176:179]\n\t"
        "ds_read_b128 v[200:203], %[xa] offset:9216\n\t"
        "v_mfma_f32_16x16x32_bf16 v[184:187], v[40:43], v[212:215], v[184:187]\n\t"
        "ds_read_b128 v[204:207], %[xa] offset:29696\n\t"
        "v_mfma_f32_16x16x32_bf16 v[180:183], v[120:123], v[208:211], v[180:183]\n\t"
        "v_mfma_f32_16x16x32_bf16 v[188:191], v[120:123], v[212:215], v[188:191]\n\t"
        "s_waitcnt lgkmcnt(4)\n\t"
        "v_mfma_f32_16x16x32_bf16 v[176:179], v[44:47], v[216:219], v[176:179]\n\t"
        "ds_read_b128 v[208:211], %[xa] offset:10240\n\t"
        "v_mfma_f32_16x16x32_bf16 v[184:187], v[44:47], v[220:223], v[184:187]\n\t"
        "ds_read_b128 v[212:215], %[xa] offset:30720\n\t"
        "v_mfma_f32_16x16x32_bf16 v[180:183], v[124:127], v[216:219], v[180:183]\n\t"
        "v_mfma_f32_16x16x32_bf16 v[188:191], v[124:127], v[220:223], v[188:191]\n\t"
        "s_waitcnt lgkmcnt(4)\n\t"
        "v_mfma_f32_16x16x32_bf16 v[176:179], v[48:51], v[192:195], v[176:179]\n\t"
        "ds_read_b128 v[216:219], %[xa] offset:11264\n\t"
        "v_mfma_f32_16x16x32_bf16 v[184:187], v[48:51], v[196:199], v[184:187]\n\t"
        "ds_read_b128 v[220:223], %[xa] offset:31744\n\t"
        "v_mfma_f32_16x16x32_bf16 v[180:183], v[128:131], v[192:195], v[180:183]\n\t"
        "v_mfma_f32_16x16x32_bf16 v[188:191], v[128:131], v[196:199], v[188:191]\n\t"
        "s_waitcnt lgkmcnt(4)\n\t"
        "v_mfma_f32_16x16x32_bf16 v[176:179], v[52:55], v[200:203], v[176:179]\n\t"
        "ds_read_b128 v[192:195], %[xa] offset:12288\n\t"
        "v_mfma_f32_16x16x32_bf16 v[184:187], v[52:55], v[204:207], v[184:187]\n\t"
        "ds_read_b128 v[196:199], %[xa] offset:32768\n\t"
        "v_mfma_f32_16x16x32_bf16 v[180:183], v[132:135], v[200:203], v[180:183]\n\t"
        "v_mfma_f32_16x16x32_bf16 v[188:191], v[132:135], v[204:207], v[188:191]\n\t"
        "s_waitcnt lgkmcnt(4)\n\t"
        "v_mfma_f32_16x16x32_bf16 v[176:179], v[56:59], v[208:211], v[176:179]\n\t"
        "ds_read_b128 v[200:203], %[xa] offset:13312\n\t"
        "v_mfma_f32_16x16x32_bf16 v[184:187], v[56:59], v[212:215], v[184:187]\n\t"
        "ds_read_b128 v[204:207], %[xa] offset:33792\n\t"
        "v_mfma_f32_16x16x32_bf16 v[180:183], v[136:139], v[208:211], v[180:183]\n\t"
        "v_mfma_f32_16x16x32_bf16 v[188:191], v[136:139], v[212:215], v[188:191]\n\t"
        "s_waitcnt lgkmcnt(4)\n\t"
        "v_mfma_f32_16x16x32_bf16 v[176:179], v[60:63], v[216:219], v[176:179]\n\t"
        "ds_read_b128 v[208:211], %[xa] offset:14336\n\t"
        "v_mfma_f32_16x16x32_bf16 v[184:187], v[60:63], v[220:223], v[184:187]\n\t"
        "ds_read_b128 v[212:215], %[xa] offset:34816\n\t"
        "v_mfma_f32_16x16x32_bf16 v[180:183], v[140:143], v[216:219], v[180:183]\n\t"
        "v_mfma_f32_16x16x32_bf16 v[188:191], v[140:143], v[220:223], v[188:191]\n\t"
        "s_waitcnt lgkmcnt(4)\n\t"
        "v_mfma_f32_16x16x32_bf16 v[176:179], v[64:67], v[192:195], v[176:179]\n\t"
        "ds_read_b128 v[216:219], %[xa] offset:15360\n\t"
        "v_mfma_f32_16x16x32_bf16 v[184:187], v[64:67], v[196:199], v[184:187]\n\t"
        "ds_read_b128 v[220:223], %[xa] offset:35840\n\t"
        "v_mfma_f32_16x16x32_bf16 v[180:183], v[144:147], v[192:195], v[180:183]\n\t"
        "v_mfma_f32_16x16x32_bf16 v[188:191], v[144:147], v[196:199], v[188:191]\n\t"
        "s_waitcnt lgkmcnt(4)\n\t"
        "v_mfma_f32_16x16x32_bf16 v[176:179], v[68:71], v[200:203], v[176:179]\n\t"
        "ds_read_b128 v[192:195], %[xa] offset:16384\n\t"
        "v_mfma_f32_16x16x32_bf16 v[184:187], v[68:71], v[204:207], v[184:187]\n\t"
        "ds_read_b128 v[196:199], %[xa] offset:36864\n\t"
        "v_mfma_f32_16x16x32_bf16 v[180:183], v[148:151], v[200:203], v[180:183]\n\t"
        "v_mfma_f32_16x16x32_bf16 v[188:191], v[148:151], v[204:207], v[188:191]\n\t"
        "s_waitcnt lgkmcnt(4)\n\t"
        "v_mfma_f32_16x16x32_bf16 v[176:179], v[72:75], v[208:211], v[176:179]\n\t"
        "ds_read_b128 v[200:203], %[xa] offset:17408\n\t"
        "v_mfma_f32_16x16x32_bf16 v[184:187], v[72:75], v[212:215], v[184:187]\n\t"
        "ds_read_b128 v[204:207], %[xa] offset:37888\n\t"
        "v_mfma_f32_16x16x32_bf16 v[180:183], v[152:155], v[208:211], v[180:183]\n\t"
        "v_mfma_f32_16x16x32_bf16 v[188:191], v[152:155], v[212:215], v[188:191]\n\t"
        "s_waitcnt lgkmcnt(4)\n\t"
        "v_mfma_f32_16x16x32_bf16 v[176:179], v[76:79], v[216:219], v[176:179]\n\t"
        "ds_read_b128 v[208:211], %[xa] offset:18432\n\t"
        "v_mfma_f32_16x16x32_bf16 v[184:187], v[76:79], v[220:223], v[184:187]\n\t"
        "ds_read_b128 v[212:215], %[xa] offset:38912\n\t"
        "v_mfma_f32_16x16x32_bf16 v[180:183], v[156:159], v[216:219], v[180:183]\n\t"
        "v_mfma_f32_16x16x32_bf16 v[188:191], v[156:159], v[220:223], v[188:191]\n\t"
        "s_waitcnt lgkmcnt(4)\n\t"
        "v_mfma_f32_16x16x32_bf16 v[176:179], v[80:83], v[192:195], v[176:179]\n\t"
        "ds_read_b128 v[216:219], %[xa] offset:19456\n\t"
        "v_mfma_f32_16x16x32_bf16 v[184:187], v[80:83], v[196:199], v[184:187]\n\t"
        "ds_read_b128 v[220:223], %[xa] offset:39936\n\t"
        "v_mfma_f32_16x16x32_bf16 v[180:183], v[160:163], v[192:195], v[180:183]\n\t"
        "v_mfma_f32_16x16x32_bf16 v[188:191], v[160:163], v[196:199], v[188:191]\n\t"
        "s_waitcnt lgkmcnt(4)\n\t"
        "v_mfma_f32_16x16x32_bf16 v[176:179], v[84:87], v[200:203], v[176:179]\n\t"
        "v_mfma_f32_16x16x32_bf16 v[184:187], v[84:87], v[204:207], v[184:187]\n\t"
        "v_mfma_f32_16x16x32_bf16 v[180:183], v[164:167], v[200:203], v[180:183]\n\t"
        "v_mfma_f32_16x16x32_bf16 v[188:191], v[164:167], v[204:207], v[188:191]\n\t"
        "s_waitcnt lgkmcnt(2)\n\t"
        "v_mfma_f32_16x16x32_bf16 v[176:179], v[88:91], v[208:211], v[176:179]\n\t"
        "v_mfma_f32_16x16x32_bf16 v[184:187], v[88:91], v[212:215], v[184:187]\n\t"
        "v_mfma_f32_16x16x32_bf16 v[180:183], v[168:171], v[208:211], v[180:183]\n\t"
        "v_mfma_f32_16x16x32_bf16 v[188:191], v[168:171], v[212:215], v[188:191]\n\t"
        "s_waitcnt lgkmcnt(0)\n\t"
        "v_mfma_f32_16x16x32_bf16 v[176:179], v[92:95], v[216:219], v[176:179]\n\t"
        "v_mfma_f32_16x16x32_bf16 v[184:187], v[92:95], v[220:223], v[184:187]\n\t"
        "v_mfma_f32_16x16x32_bf16 v[180:183], v[172:175], v[216:219], v[180:183]\n\t"
        "v_mfma_f32_16x16x32_bf16 v[188:191], v[172:175], v[220:223], v[188:191]\n\t"
        "s_nop 4\n\t"
        "v_cvt_pk_bf16_f32 v224, v176, v177\n\t"
        "v_cvt_pk_bf16_f32 v225, v178, v179\n\t"
        "v_cvt_pk_bf16_f32 v226, v180, v181\n\t"
        "v_cvt_pk_bf16_f32 v227, v182, v183\n\t"
        "buffer_store_dwordx4 v[224:227], %[oa], %[rs], %[o0] offen sc1\n\t"
        "v_cvt_pk_bf16_f32 v228, v184, v185\n\t"
        "v_cvt_pk_bf16_f32 v229, v186, v187\n\t"
        "v_cvt_pk_bf16_f32 v230, v188, v189\n\t"
        "v_cvt_pk_bf16_f32 v231, v190, v191\n\t"
        "buffer_store_dwordx4 v[228:231], %[oa], %[rs], %[o1] offen sc1\n\t"
        "s_mov_b32 s88, m0\n\t"
        "s_mov_b32 m0, %[xd]\n\t"
        "s_add_u32 s89, %[xs], 0x1000\n\t"
        "buffer_load_dwordx4 %[xv], %[rs], %[xs] offen offset:0 sc1 lds\n\t"
        "buffer_load_dwordx4 %[xv], %[rs], %[xs] offen offset:1024 sc1 lds\n\t"
        "buffer_load_dwordx4 %[xv], %[rs], %[xs] offen offset:2048 sc1 lds\n\t"
        "buffer_load_dwordx4 %[xv], %[rs], %[xs] offen offset:3072 sc1 lds\n\t"
        "s_add_u32 m0, m0, 0x1000\n\t"
        "s_nop 0\n\t"
        "buffer_load_dwordx4 %[xv], %[rs], s89 offen offset:0 sc1 lds\n\t"
        "buffer_load_dwordx4 %[xv], %[rs], s89 offen offset:1024 sc1 lds\n\t"
        "buffer_load_dwordx4 %[xv], %[rs], s89 offen offset:2048 sc1 lds\n\t"
        "buffer_load_dwordx4 %[xv], %[rs], s89 offen offset:3072 sc1 lds\n\t"
        "s_mov_b32 m0, s88\n\t"
        :
        : [w0] "{v[16:47]}"(W[0]), [w1] "{v[48:79]}"(W[1]), [w2] "{v[80:111]}"(W[2]), [w3] "{v[112:143]}"(W[3]), [w4] "{v[144:175]}"(W[4]), [xa] "v"(xa), [oa] "v"(oa), [xv] "v"(xv), [xs] "s"(xs), [xd] "s"(xd), [rs] "s"(rs), [o0] "s"(o0), [o1] "s"(o1)
        : "s88", "s89", "v176", "v177", "v178", "v179", "v180", "v181", "v182", "v183", "v184", "v185", "v186", "v187", "v188", "v189", "v190", "v191", "v192", "v193", "v194", "v195", "v196", "v197", "v198", "v199", "v200", "v201", "v202", "v203", "v204", "v205", "v206", "v207", "v208", "v209", "v210", "v211", "v212", "v213", "v214", "v215", "v216", "v217", "v218", "v219", "v220", "v221", "v222", "v223", "v224", "v225", "v226", "v227", "v228", "v229", "v230", "v231", "vcc", "scc", "memory");
}

__device__ __forceinline__ void wx1(const u32x32 (&W)[5], u32 xa, u32 oa STP) {
    asm volatile(
        "ds_read_b128 v[192:195], %[xa] offset:0\n\t"
        "ds_read_b128 v[196:199], %[xa] offset:20480\n\t"
        "ds_read_b128 v[200:203], %[xa] offset:1024\n\t"
        "ds_read_b128 v[204:207], %[xa] offset:21504\n\t"
        "ds_read_b128 v[208:211], %[xa] offset:2048\n\t"
        "ds_read_b128 v[212:215], %[xa] offset:22528\n\t"
        "s_waitcnt lgkmcnt(6)\n\t"
        "s_barrier\n\t"
        "s_waitcnt lgkmcnt(4)\n\t"
        "v_mfma_f32_16x16x32_bf16 v[176:179], v[16:19], v[192:195], 0\n\t"
        "ds_read_b128 v[216:219], %[xa] offset:3072\n\t"
        "v_mfma_f32_16x16x32_bf16 v[184:187], v[16:19], v[196:199], 0\n\t"
        "ds_read_b128 v[220:223], %[xa] offset:23552\n\t"
        "v_mfma_f32_16x16x32_bf16 v[180:183], v[96:99], v[192:195], 0\n\t"
        "v_mfma_f32_16x16x32_bf16 v[188:191], v[96:99], v[196:199], 0\n\t"
        "s_waitcnt lgkmcnt(4)\n\t"
        "v_mfma_f32_16x16x32_bf16 v[176:179], v[20:23], v[200:203], v[176:179]\n\t"
        "ds_read_b128 v[192:195], %[xa] offset:4096\n\t"
        "v_mfma_f32_16x16x32_bf16 v[184:187], v[20:23], v[204:207], v[184:187]\n\t"
        "ds_read_b128 v[196:199], %[xa] offset:24576\n\t"
        "v_mfma_f32_16x16x32_bf16 v[180:183], v[100:103], v[200:203], v[180:183]\n\t"
        "v_mfma_f32_16x16x32_bf16 v[188:191], v[100:103], v[204:207], v[188:191]\n\t"
        "s_waitcnt lgkmcnt(4)\n\t"
        "v_mfma_f32_16x16x32_bf16 v[176:179], v[24:27], v[208:211], v[176:179]\n\t"
        "ds_read_b128 v[200:203], %[xa] offset:5120\n\t"
        "v_mfma_f32_16x16x32_bf16 v[184:187], v[24:27], v[212:215], v[184:187]\n\t"
        "ds_read_b128 v[204:207], %[xa] offset:25600\n\t"
        "v_mfma_f32_16x16x32_bf16 v[180:183], v[104:107], v[208:211], v[180:183]\n\t"
        "v_mfma_f32_16x16x32_bf16 v[188:191], v[104:107], v[212:215], v[188:191]\n\t"
        "s_waitcnt lgkmcnt(4)\n\t"
        "v_mfma_f32_16x16x32_bf16 v[176:179], v[28:31], v[216:219], v[176:179]\n\t"
        "ds_read_b128 v[208:211], %[xa] offset:6144\n\t"
        "v_mfma_f32_16x16x32_bf16 v[184:187], v[28:31], v[220:223], v[184:187]\n\t"
        "ds_read_b128 v[212:215], %[xa] offset:26624\n\t"
        "v_mfma_f32_16x16x32_bf16 v[180:183], v[108:111], v[216:219], v[180:183]\n\t"
        "v_mfma_f32_16x16x32_bf16 v[188:191], v[108:111], v[220:223], v[188:191]\n\t"
        "s_waitcnt lgkmcnt(4)\n\t"
        "v_mfma_f32_16x16x32_bf16 v[176:179], v[32:35], v[192:195], v[176:179]\n\t"
        "ds_read_b128 v[216:219], %[xa] offset:7168\n\t"
        "v_mfma_f32_16x16x32_bf16 v[184:187], v[32:35], v[196:199], v[184:187]\n\t"
        "ds_read_b128 v[220:223], %[xa] offset:27648\n\t"
        "v_mfma_f32_16x16x32_bf16 v[180:183], v[112:115], v[192:195], v[180:183]\n\t"
        "v_mfma_f32_16x16x32_bf16 v[188:191], v[112:115], v[196:199], v[188:191]\n\t"
        "s_waitcnt lgkmcnt(4)\n\t"
        "v_mfma_f32_16x16x32_bf16 v[176:179], v[36:39], v[200:203], v[176:179]\n\t"
        "ds_read_b128 v[192:195], %[xa] offset:8192\n\t"
        "v_mfma_f32_16x16x32_bf16 v[184:187], v[36:39], v[204:207], v[184:187]\n\t"
        "ds_read_b128 v[196:199], %[xa] offset:28672\n\t"
        "v_mfma_f32_16x16x32_bf16 v[180:183], v[116:119], v[200:203], v[180:183]\n\t"
        "v_mfma_f32_16x16x32_bf16 v[188:191], v[116:119], v[204:207], v[188:191]\n\t"
        "s_waitcnt lgkmcnt(4)\n\t"
        "v_mfma_f32_16x16x32_bf16 v[176:179], v[40:43], v[208:211], v[176:179]\n\t"
        "ds_read_b128 v[200:203], %[xa] offset:9216\n\t"
        "v_mfma_f32_16x16x32_bf16 v[184:187], v[40:43], v[212:215], v[184:187]\n\t"
        "ds_read_b128 v[204:207], %[xa] offset:29696\n\t"
        "v_mfma_f32_16x16x32_bf16 v[180:183], v[120:123], v[208:211], v[180:183]\n\t"
        "v_mfma_f32_16x16x32_bf16 v[188:191], v[120:123], v[212:215], v[188:191]\n\t"
        "s_waitcnt lgkmcnt(4)\n\t"
        "v_mfma_f32_16x16x32_bf16 v[176:179], v[44:47], v[216:219], v[176:179]\n\t"
        "ds_read_b128 v[208:211], %[xa] offset:10240\n\t"
        "v_mfma_f32_16x16x32_bf16 v[184:187], v[44:47], v[220:223], v[184:187]\n\t"
        "ds_read_b128 v[212:215], %[xa] offset:30720\n\t"
        "v_mfma_f32_16x16x32_bf16 v[180:183], v[124:127], v[216:219], v[180:183]\n\t"
        "v_mfma_f32_16x16x32_bf16 v[188:191], v[124:127], v[220:223], v[188:191]\n\t"
        "s_waitcnt lgkmcnt(4)\n\t"
        "v_mfma_f32_16x16x32_bf16 v[176:179], v[48:51], v[192:195], v[176:179]\n\t"
        "ds_read_b128 v[216:219], %[xa] offset:11264\n\t"
        "v_mfma_f32_16x16x32_bf16 v[184:187], v[48:51], v[196:199], v[184:187]\n\t"
        "ds_read_b128 v[220:223], %[xa] offset:31744\n\t"
        "v_mfma_f32_16x16x32_bf16 v[180:183], v[128:131], v[192:195], v[180:183]\n\t"
        "v_mfma_f32_16x16x32_bf16 v[188:191], v[128:131], v[196:199], v[188:191]\n\t"
        "s_waitcnt lgkmcnt(4)\n\t"
        "v_mfma_f32_16x16x32_bf16 v[176:179], v[52:55], v[200:203], v[176:179]\n\t"
        "ds_read_b128 v[192:195], %[xa] offset:12288\n\t"
        "v_mfma_f32_16x16x32_bf16 v[184:187], v[52:55], v[204:207], v[184:187]\n\t"
        "ds_read_b128 v[196:199], %[xa] offset:32768\n\t"
        "v_mfma_f32_16x16x32_bf16 v[180:183], v[132:135], v[200:203], v[180:183]\n\t"
        "v_mfma_f32_16x16x32_bf16 v[188:191], v[132:135], v[204:207], v[188:191]\n\t"
        "s_waitcnt lgkmcnt(4)\n\t"
        "v_mfma_f32_16x16x32_bf16 v[176:179], v[56:59], v[208:211], v[176:179]\n\t"
        "ds_read_b128 v[200:203], %[xa] offset:13312\n\t"
        "v_mfma_f32_16x16x32_bf16 v[184:187], v[56:59], v[212:215], v[184:187]\n\t"
        "ds_read_b128 v[204:207], %[xa] offset:33792\n\t"
        "v_mfma_f32_16x16x32_bf16 v[180:183], v[136:139], v[208:211], v[180:183]\n\t"
        "v_mfma_f32_16x16x32_bf16 v[188:191], v[136:139], v[212:215], v[188:191]\n\t"
        "s_waitcnt lgkmcnt(4)\n\t"
        "v_mfma_f32_16x16x32_bf16 v[176:179], v[60:63], v[216:219], v[176:179]\n\t"
        "ds_read_b128 v[208:211], %[xa] offset:14336\n\t"
        "v_mfma_f32_16x16x32_bf16 v[184:187], v[60:63], v[220:223], v[184:187]\n\t"
        "ds_read_b128 v[212:215], %[xa] offset:34816\n\t"
        "v_mfma_f32_16x16x32_bf16 v[180:183], v[140:143], v[216:219], v[180:183]\n\t"
        "v_mfma_f32_16x16x32_bf16 v[188:191], v[140:143], v[220:223], v[188:191]\n\t"
        "s_waitcnt lgkmcnt(4)\n\t"
        "v_mfma_f32_16x16x32_bf16 v[176:179], v[64:67], v[192:195], v[176:179]\n\t"
        "ds_read_b128 v[216:219], %[xa] offset:15360\n\t"
        "v_mfma_f32_16x16x32_bf16 v[184:187], v[64:67], v[196:199], v[184:187]\n\t"
        "ds_read_b128 v[220:223], %[xa] offset:35840\n\t"
        "v_mfma_f32_16x16x32_bf16 v[180:183], v[144:147], v[192:195], v[180:183]\n\t"
        "v_mfma_f32_16x16x32_bf16 v[188:191], v[144:147], v[196:199], v[188:191]\n\t"
        "s_waitcnt lgkmcnt(4)\n\t"
        "v_mfma_f32_16x16x32_bf16 v[176:179], v[68:71], v[200:203], v[176:179]\n\t"
        "ds_read_b128 v[192:195], %[xa] offset:16384\n\t"
        "v_mfma_f32_16x16x32_bf16 v[184:187], v[68:71], v[204:207], v[184:187]\n\t"
        "ds_read_b128 v[196:199], %[xa] offset:36864\n\t"
        "v_mfma_f32_16x16x32_bf16 v[180:183], v[148:151], v[200:203], v[180:183]\n\t"
        "v_mfma_f32_16x16x32_bf16 v[188:191], v[148:151], v[204:207], v[188:191]\n\t"
        "s_waitcnt lgkmcnt(4)\n\t"
        "v_mfma_f32_16x16x32_bf16 v[176:179], v[72:75], v[208:211], v[176:179]\n\t"
        "ds_read_b128 v[200:203], %[xa] offset:17408\n\t"
        "v_mfma_f32_16x16x32_bf16 v[184:187], v[72:75], v[212:215], v[184:187]\n\t"
        "ds_read_b128 v[204:207], %[xa] offset:37888\n\t"
        "v_mfma_f32_16x16x32_bf16 v[180:183], v[152:155], v[208:211], v[180:183]\n\t"
        "v_mfma_f32_16x16x32_bf16 v[188:191], v[152:155], v[212:215], v[188:191]\n\t"
        "s_waitcnt lgkmcnt(4)\n\t"
        "v_mfma_f32_16x16x32_bf16 v[176:179], v[76:79], v[216:219], v[176:179]\n\t"
        "ds_read_b128 v[208:211], %[xa] offset:18432\n\t"
        "v_mfma_f32_16x16x32_bf16 v[184:187], v[76:79], v[220:223], v[184:187]\n\t"
        "ds_read_b128 v[212:215], %[xa] offset:38912\n\t"
        "v_mfma_f32_16x16x32_bf16 v[180:183], v[156:159], v[216:219], v[180:183]\n\t"
        "v_mfma_f32_16x16x32_bf16 v[188:191], v[156:159], v[220:223], v[188:191]\n\t"
        "s_waitcnt lgkmcnt(4)\n\t"
        "v_mfma_f32_16x16x32_bf16 v[176:179], v[80:83], v[192:195], v[176:179]\n\t"
        "ds_read_b128 v[216:219], %[xa] offset:19456\n\t"
        "v_mfma_f32_16x16x32_bf16 v[184:187], v[80:83], v[196:199], v[184:187]\n\t"
        "ds_read_b128 v[220:223], %[xa] offset:39936\n\t"
        "v_mfma_f32_16x16x32_bf16 v[180:183], v[160:163], v[192:195], v[180:183]\n\t"
        "v_mfma_f32_16x16x32_bf16 v[188:191], v[160:163], v[196:199], v[188:191]\n\t"
        "s_waitcnt lgkmcnt(4)\n\t"
        "v_mfma_f32_16x16x32_bf16 v[176:179], v[84:87], v[200:203], v[176:179]\n\t"
        "v_mfma_f32_16x16x32_bf16 v[184:187], v[84:87], v[204:207], v[184:187]\n\t"
        "v_mfma_f32_16x16x32_bf16 v[180:183], v[164:167], v[200:203], v[180:183]\n\t"
        "v_mfma_f32_16x16x32_bf16 v[188:191], v[164:167], v[204:207], v[188:191]\n\t"
        "s_waitcnt lgkmcnt(2)\n\t"
        "v_mfma_f32_16x16x32_bf16 v[176:179], v[88:91], v[208:211], v[176:179]\n\t"
        "v_mfma_f32_16x16x32_bf16 v[184:187], v[88:91], v[212:215], v[184:187]\n\t"
        "v_mfma_f32_16x16x32_bf16 v[180:183], v[168:171], v[208:211], v[180:183]\n\t"
        "v_mfma_f32_16x16x32_bf16 v[188:191], v[168:171], v[212:215], v[188:191]\n\t"
        "s_waitcnt lgkmcnt(0)\n\t"
        "v_mfma_f32_16x16x32_bf16 v[176:179], v[92:95], v[216:219], v[176:179]\n\t"
        "v_mfma_f32_16x16x32_bf16 v[184:187], v[92:95], v[220:223], v[184:187]\n\t"
        "v_mfma_f32_16x16x32_bf16 v[180:183], v[172:175], v[216:219], v[180:183]\n\t"
        "v_mfma_f32_16x16x32_bf16 v[188:191], v[172:175], v[220:223], v[188:191]\n\t"
        "s_nop 4\n\t"
        "v_cvt_pk_bf16_f32 v224, v176, v177\n\t"
        "v_cvt_pk_bf16_f32 v225, v178, v179\n\t"
        "v_cvt_pk_bf16_f32 v226, v180, v181\n\t"
        "v_cvt_pk_bf16_f32 v227, v182, v183\n\t"
        "ds_write_b128 %[oa], v[224:227] offset:0\n\t"
        "v_cvt_pk_bf16_f32 v228, v184, v185\n\t"
        "v_cvt_pk_bf16_f32 v229, v186, v187\n\t"
        "v_cvt_pk_bf16_f32 v230, v188, v189\n\t"
        "v_cvt_pk_bf16_f32 v231, v190, v191\n\t"
        "ds_write_b128 %[oa], v[228:231] offset:4096\n\t"
        :
        : [w0] "{v[16:47]}"(W[0]), [w1] "{v[48:79]}"(W[1]), [w2] "{v[80:111]}"(W[2]), [w3] "{v[112:143]}"(W[3]), [w4] "{v[144:175]}"(W[4]), [xa] "v"(xa), [oa] "v"(oa)
        : "v176", "v177", "v178", "v179", "v180", "v181", "v182", "v183", "v184", "v185", "v186", "v187", "v188", "v189", "v190", "v191", "v192", "v193", "v194", "v195", "v196", "v197", "v198", "v199", "v200", "v201", "v202", "v203", "v204", "v205", "v206", "v207", "v208", "v209", "v210", "v211", "v212", "v213", "v214", "v215", "v216", "v217", "v218", "v219", "v220", "v221", "v222", "v223", "v224", "v225", "v226", "v227", "v228", "v229", "v230", "v231", "vcc", "scc", "memory");
}

__device__ __forceinline__ void wk(const u32x32 (&W)[5], u32 xa, u32 oa, u32x4 rs, u32 o0, u32 o1, float sc, u32 xv, u32 xs, u32 xd, u32 ca, u32 fc, u32 fw, u32 fo STP) {
    asm volatile(
        "ds_read_b128 v[192:195], %[xa] offset:0\n\t"
        "ds_read_b128 v[196:199], %[xa] offset:20480\n\t"
        "ds_read_b128 v[200:203], %[xa] offset:1024\n\t"
        "ds_read_b128 v[204:207], %[xa] offset:21504\n\t"
        "ds_read_b128 v[208:211], %[xa] offset:2048\n\t"
        "ds_read_b128 v[212:215], %[xa] offset:22528\n\t"
        "v_mov_b32 v232, %[ca]\n\t"
        "v_mov_b32 v233, 1\n\t"
        "v_mov_b32 v234, %[fw]\n\t"
        "s_mov_b32 s88, m0\n\t"
        "s_mov_b32 m0, %[xd]\n\t"
        "s_add_u32 s89, %[xs], 0x1000\n\t"
        "buffer_load_dwordx4 %[xv], %[rs], %[xs] offen offset:0 sc1 lds\n\t"
        "buffer_load_dwordx4 %[xv], %[rs], %[xs] offen offset:1024 sc1 lds\n\t"
        "s_mov_b32 m0, s88\n\t"
        "s_waitcnt lgkmcnt(4)\n\t"
        "v_mfma_f32_16x16x32_bf16 v[176:179], v[16:19], v[192:195], 0\n\t"
        "ds_read_b128 v[216:219], %[xa] offset:3072\n\t"
        "v_mfma_f32_16x16x32_bf16 v[184:187], v[16:19], v[196:199], 0\n\t"
        "ds_read_b128 v[220:223], %[xa] offset:23552\n\t"
        "v_mfma_f32_16x16x32_bf16 v[180:183], v[96:99], v[192:195], 0\n\t"
        "v_mfma_f32_16x16x32_bf16 v[188:191], v[96:99], v[196:199], 0\n\t"
        "s_waitcnt lgkmcnt(4)\n\t"
        "v_mfma_f32_16x16x32_bf16 v[176:179], v[20:23], v[200:203], v[176:179]\n\t"
        "ds_read_b128 v[192:195], %[xa] offset:4096\n\t"
        "v_mfma_f32_16x16x32_bf16 v[184:187], v[20:23], v[204:207], v[184:187]\n\t"
        "ds_read_b128 v[196:199], %[xa] offset:24576\n\t"
        "v_mfma_f32_16x16x32_bf16 v[180:183], v[100:103], v[200:203], v[180:183]\n\t"
        "v_mfma_f32_16x16x32_bf16 v[188:191], v[100:103], v[204:207], v[188:191]\n\t"
        "s_waitcnt lgkmcnt(4)\n\t"
        "v_mfma_f32_16x16x32_bf16 v[176:179], v[24:27], v[208:211], v[176:179]\n\t"
        "ds_read_b128 v[200:203], %[xa] offset:5120\n\t"
        "v_mfma_f32_16x16x32_bf16 v[184:187], v[24:27], v[212:215], v[184:187]\n\t"
        "ds_read_b128 v[204:207], %[xa] offset:25600\n\t"
        "v_mfma_f32_16x16x32_bf16 v[180:183], v[104:107], v[208:211], v[180:183]\n\t"
        "v_mfma_f32_16x16x32_bf16 v[188:191], v[104:107], v[212:215], v[188:191]\n\t"
        "s_waitcnt lgkmcnt(4)\n\t"
        "v_mfma_f32_16x16x32_bf16 v[176:179], v[28:31], v[216:219], v[176:179]\n\t"
        "ds_read_b128 v[208:211], %[xa] offset:6144\n\t"
        "v_mfma_f32_16x16x32_bf16 v[184:187], v[28:31], v[220:223], v[184:187]\n\t"
        "ds_read_b128 v[212:215], %[xa] offset:26624\n\t"
        "v_mfma_f32_16x16x32_bf16 v[180:183], v[108:111], v[216:219], v[180:183]\n\t"
        "v_mfma_f32_16x16x32_bf16 v[188:191], v[108:111], v[220:223], v[188:191]\n\t"
        "s_waitcnt lgkmcnt(4)\n\t"
        "v_mfma_f32_16x16x32_bf16 v[176:179], v[32:35], v[192:195], v[176:179]\n\t"
        "ds_read_b128 v[216:219], %[xa] offset:7168\n\t"
        "v_mfma_f32_16x16x32_bf16 v[184:187], v[32:35], v[196:199], v[184:187]\n\t"
        "ds_read_b128 v[220:223], %[xa] offset:27648\n\t"
        "v_mfma_f32_16x16x32_bf16 v[180:183], v[112:115], v[192:195], v[180:183]\n\t"
        "v_mfma_f32_16x16x32_bf16 v[188:191], v[112:115], v[196:199], v[188:191]\n\t"
        "s_waitcnt lgkmcnt(4)\n\t"
        "v_mfma_f32_16x16x32_bf16 v[176:179], v[36:39], v[200:203], v[176:179]\n\t"
        "ds_read_b128 v[192:195], %[xa] offset:8192\n\t"
        "v_mfma_f32_16x16x32_bf16 v[184:187], v[36:39], v[204:207], v[184:187]\n\t"
        "ds_read_b128 v[196:199], %[xa] offset:28672\n\t"
        "v_mfma_f32_16x16x32_bf16 v[180:183], v[116:119], v[200:203], v[180:183]\n\t"
        "v_mfma_f32_16x16x32_bf16 v[188:191], v[116:119], v[204:207], v[188:191]\n\t"
        "s_waitcnt lgkmcnt(4)\n\t"
        "v_mfma_f32_16x16x32_bf16 v[176:179], v[40:43], v[208:211], v[176:179]\n\t"
        "ds_read_b128 v[200:203], %[xa] offset:9216\n\t"
        "v_mfma_f32_16x16x32_bf16 v[184:187], v[40:43], v[212:215], v[184:187]\n\t"
        "ds_read_b128 v[204:207], %[xa] offset:29696\n\t"
        "v_mfma_f32_16x16x32_bf16 v[180:183], v[120:123], v[208:211], v[180:183]\n\t"
        "s_waitcnt vmcnt(0)\n\t"
        "s_mov_b64 exec, 1\n\t"
        "ds_add_rtn_u32 v233, v232, v233\n\t"
        "s_mov_b64 exec, -1\n\t"
        "v_mfma_f32_16x16x32_bf16 v[188:191], v[120:123], v[212:215], v[188:191]\n\t"
        "s_waitcnt lgkmcnt(5)\n\t"
        "v_mfma_f32_16x16x32_bf16 v[176:179], v[44:47], v[216:219], v[176:179]\n\t"
        "ds_read_b128 v[208:211], %[xa] offset:10240\n\t"
        "v_mfma_f32_16x16x32_bf16 v[184:187], v[44:47], v[220:223], v[184:187]\n\t"
        "ds_read_b128 v[212:215], %[xa] offset:30720\n\t"
        "v_mfma_f32_16x16x32_bf16 v[180:183], v[124:127], v[216:219], v[180:183]\n\t"
        "v_mfma_f32_16x16x32_bf16 v[188:191], v[124:127], v[220:223], v[188:191]\n\t"
        "s_waitcnt lgkmcnt(5)\n\t"
        "v_mfma_f32_16x16x32_bf16 v[176:179], v[48:51], v[192:195], v[176:179]\n\t"
        "ds_read_b128 v[216:219], %[xa] offset:11264\n\t"
        "v_mfma_f32_16x16x32_bf16 v[184:187], v[48:51], v[196:199], v[184:187]\n\t"
        "ds_read_b128 v[220:223], %[xa] offset:31744\n\t"
        "v_mfma_f32_16x16x32_bf16 v[180:183], v[128:131], v[192:195], v[180:183]\n\t"
        "s_waitcnt lgkmcnt(4)\n\t"
        "s_mov_b64 exec, 1\n\t"
        "v_cmpx_eq_u32 vcc, %[fc], v233\n\t"
        "buffer_store_dword v234, off, %[rs], %[fo] sc1\n\t"
        "s_mov_b64 exec, -1\n\t"
        "v_mfma_f32_16x16x32_bf16 v[188:191], v[128:131], v[196:199], v[188:191]\n\t"
        "s_waitcnt lgkmcnt(5)\n\t"
        "v_mfma_f32_16x16x32_bf16 v[176:179], v[52:55], v[200:203], v[176:179]\n\t"
        "ds_read_b128 v[192:195], %[xa] offset:12288\n\t"
        "v_mfma_f32_16x16x32_bf16 v[184:187], v[52:55], v[204:207], v[184:187]\n\t"
        "ds_read_b128 v[196:199], %[xa] offset:32768\n\t"
        "v_mfma_f32_16x16x32_bf16 v[180:183], v[132:135], v[200:203], v[180:183]\n\t"
        "v_mfma_f32_16x16x32_bf16 v[188:191], v[132:135], v[204:207], v[188:191]\n\t"
        "s_waitcnt lgkmcnt(4)\n\t"
        "v_mfma_f32_16x16x32_bf16 v[176:179], v[56:59], v[208:211], v[176:179]\n\t"
        "ds_read_b128 v[200:203], %[xa] offset:13312\n\t"
        "v_mfma_f32_16x16x32_bf16 v[184:187], v[56:59], v[212:215], v[184:187]\n\t"
        "ds_read_b128 v[204:207], %[xa] offset:33792\n\t"
        "v_mfma_f32_16x16x32_bf16 v[180:183], v[136:139], v[208:211], v[180:183]\n\t"
        "v_mfma_f32_16x16x32_bf16 v[188:191], v[136:139], v[212:215], v[188:191]\n\t"
        "s_waitcnt lgkmcnt(4)\n\t"
        "v_mfma_f32_16x16x32_bf16 v[176:179], v[60:63], v[216:219], v[176:179]\n\t"
        "ds_read_b128 v[208:211], %[xa] offset:14336\n\t"
        "v_mfma_f32_16x16x32_bf16 v[184:187], v[60:63], v[220:223], v[184:187]\n\t"
        "ds_read_b128 v[212:215], %[xa] offset:34816\n\t"
        "v_mfma_f32_16x16x32_bf16 v[180:183], v[140:143], v[216:219], v[180:183]\n\t"
        "v_mfma_f32_16x16x32_bf16 v[188:191], v[140:143], v[220:223], v[188:191]\n\t"
        "s_waitcnt lgkmcnt(4)\n\t"
        "v_mfma_f32_16x16x32_bf16 v[176:179], v[64:67], v[192:195], v[176:179]\n\t"
        "ds_read_b128 v[216:219], %[xa] offset:15360\n\t"
        "v_mfma_f32_16x16x32_bf16 v[184:187], v[64:67], v[196:199], v[184:187]\n\t"
        "ds_read_b128 v[220:223], %[xa] offset:35840\n\t"
        "v_mfma_f32_16x16x32_bf16 v[180:183], v[144:147], v[192:195], v[180:183]\n\t"
        "v_mfma_f32_16x16x32_bf16 v[188:191], v[144:147], v[196:199], v[188:191]\n\t"
        "s_waitcnt lgkmcnt(4)\n\t"
        "v_mfma_f32_16x16x32_bf16 v[176:179], v[68:71], v[200:203], v[176:179]\n\t"
        "ds_read_b128 v[192:195], %[xa] offset:16384\n\t"
        "v_mfma_f32_16x16x32_bf16 v[184:187], v[68:71], v[204:207], v[184:187]\n\t"
        "ds_read_b128 v[196:199], %[xa] offset:36864\n\t"
        "v_mfma_f32_16x16x32_bf16 v[180:183], v[148:151], v[200:203], v[180:183]\n\t"
        "v_mfma_f32_16x16x32_bf16 v[188:191], v[148:151], v[204:207], v[188:191]\n\t"
        "s_waitcnt lgkmcnt(4)\n\t"
        "v_mfma_f32_16x16x32_bf16 v[176:179], v[72:75], v[208:211], v[176:179]\n\t"
        "ds_read_b128 v[200:203], %[xa] offset:17408\n\t"
        "v_mfma_f32_16x16x32_bf16 v[184:187], v[72:75], v[212:215], v[184:187]\n\t"
        "ds_read_b128 v[204:207], %[xa] offset:37888\n\t"
        "v_mfma_f32_16x16x32_bf16 v[180:183], v[152:155], v[208:211], v[180:183]\n\t"
        "v_mfma_f32_16x16x32_bf16 v[188:191], v[152:155], v[212:215], v[188:191]\n\t"
        "s_waitcnt lgkmcnt(4)\n\t"
        "v_mfma_f32_16x16x32_bf16 v[176:179], v[76:79], v[216:219], v[176:179]\n\t"
        "ds_read_b128 v[208:211], %[xa] offset:18432\n\t"
        "v_mfma_f32_16x16x32_bf16 v[184:187], v[76:79], v[220:223], v[184:187]\n\t"
        "ds_read_b128 v[212:215], %[xa] offset:38912\n\t"
        "v_mfma_f32_16x16x32_bf16 v[180:183], v[156:159], v[216:219], v[180:183]\n\t"
        "v_mfma_f32_16x16x32_bf16 v[188:191], v[156:159], v[220:223], v[188:191]\n\t"
        "s_waitcnt lgkmcnt(4)\n\t"
        "v_mfma_f32_16x16x32_bf16 v[176:179], v[80:83], v[192:195], v[176:179]\n\t"
        "ds_read_b128 v[216:219], %[xa] offset:19456\n\t"
        "v_mfma_f32_16x16x32_bf16 v[184:187], v[80:83], v[196:199], v[184:187]\n\t"
        "ds_read_b128 v[220:223], %[xa] offset:39936\n\t"
        "v_mfma_f32_16x16x32_bf16 v[180:183], v[160:163], v[192:195], v[180:183]\n\t"
        "v_mfma_f32_16x16x32_bf16 v[188:191], v[160:163], v[196:199], v[188:191]\n\t"
        "s_waitcnt lgkmcnt(4)\n\t"
        "v_mfma_f32_16x16x32_bf16 v[176:179], v[84:87], v[200:203], v[176:179]\n\t"
        "v_mfma_f32_16x16x32_bf16 v[184:187], v[84:87], v[204:207], v[184:187]\n\t"
        "v_mfma_f32_16x16x32_bf16 v[180:183], v[164:167], v[200:203], v[180:183]\n\t"
        "v_mfma_f32_16x16x32_bf16 v[188:191], v[164:167], v[204:207], v[188:191]\n\t"
        "s_waitcnt lgkmcnt(2)\n\t"
        "v_mfma_f32_16x16x32_bf16 v[176:179], v[88:91], v[208:211], v[176:179]\n\t"
        "v_mfma_f32_16x16x32_bf16 v[184:187], v[88:91], v[212:215], v[184:187]\n\t"
        "v_mfma_f32_16x16x32_bf16 v[180:183], v[168:171], v[208:211], v[180:183]\n\t"
        "v_mfma_f32_16x16x32_bf16 v[188:191], v[168:171], v[212:215], v[188:191]\n\t"
        "s_waitcnt lgkmcnt(0)\n\t"
        "v_mfma_f32_16x16x32_bf16 v[176:179], v[92:95], v[216:219], v[176:179]\n\t"
        "v_mfma_f32_16x16x32_bf16 v[184:187], v[92:95], v[220:223], v[184:187]\n\t"
        "v_mfma_f32_16x16x32_bf16 v[180:183], v[172:175], v[216:219], v[180:183]\n\t"
        "v_mfma_f32_16x16x32_bf16 v[188:191], v[172:175], v[220:223], v[188:191]\n\t"
        "s_nop 0\n\t"
        "s_barrier\n\t"
        "s_nop 4\n\t"
        "v_mul_f32 v176, %[sc], v176\n\t"
        "v_mul_f32 v177, %[sc], v177\n\t"
        "v_mul_f32 v178, %[sc], v178\n\t"
        "v_mul_f32 v179, %[sc], v179\n\t"
        "v_mul_f32 v180, %[sc], v180\n\t"
        "v_mul_f32 v181, %[sc], v181\n\t"
        "v_mul_f32 v182, %[sc], v182\n\t"
        "v_mul_f32 v183, %[sc], v183\n\t"
        "v_cvt_pk_bf16_f32 v224, v176, v177\n\t"
        "v_cvt_pk_bf16_f32 v225, v178, v179\n\t"
        "v_cvt_pk_bf16_f32 v226, v180, v181\n\t"
        "v_cvt_pk_bf16_f32 v227, v182, v183\n\t"
        "buffer_store_dwordx4 v[224:227], %[oa], %[rs], %[o0] offen sc1\n\t"
        "v_mul_f32 v184, %[sc], v184\n\t"
        "v_mul_f32 v185, %[sc], v185\n\t"
        "v_mul_f32 v186, %[sc], v186\n\t"
        "v_mul_f32 v187, %[sc], v187\n\t"
        "v_mul_f32 v188, %[sc], v188\n\t"
        "v_mul_f32 v189, %[sc], v189\n\t"
        "v_mul_f32 v190, %[sc], v190\n\t"
        "v_mul_f32 v191, %[sc], v191\n\t"
        "v_cvt_pk_bf16_f32 v228, v184, v185\n\t"
        "v_cvt_pk_bf16_f32 v229, v186, v187\n\t"
        "v_cvt_pk_bf16_f32 v230, v188, v189\n\t"
        "v_cvt_pk_bf16_f32 v231, v190, v191\n\t"
        "buffer_store_dwordx4 v[228:231], %[oa], %[rs], %[o1] offen sc1\n\t"
        "s_nop 1\n\t"
        :
        : [w0] "{v[16:47]}"(W[0]), [w1] "{v[48:79]}"(W[1]), [w2] "{v[80:111]}"(W[2]), [w3] "{v[112:143]}"(W[3]), [w4] "{v[144:175]}"(W[4]), [xa] "v"(xa), [oa] "v"(oa), [rs] "s"(rs), [o0] "s"(o0), [o1] "s"(o1), [sc] "s"(sc), [ca] "s"(ca), [fc] "s"(fc), [fw] "s"(fw), [fo] "s"(fo), [xv] "v"(xv), [xs] "s"(xs), [xd] "s"(xd)
        : "s88", "s89", "v176", "v177", "v178", "v179", "v180", "v181", "v182", "v183", "v184", "v185", "v186", "v187", "v188", "v189", "v190", "v191", "v192", "v193", "v194", "v195", "v196", "v197", "v198", "v199", "v200", "v201", "v202", "v203", "v204", "v205", "v206", "v207", "v208", "v209", "v210", "v211", "v212", "v213", "v214", "v215", "v216", "v217", "v218", "v219", "v220", "v221", "v222", "v223", "v224", "v225", "v226", "v227", "v228", "v229", "v230", "v231", "v232", "v233", "v234", "vcc", "scc", "memory");
}

__device__ __forceinline__ void wb(const u32x32 (&W)[5], u32 xa, u32 oa, u32x4 rs, u32 bl, u32 wp, u32 it, u32 xv, u32 xs, u32 xd, u32 ys, u32 yd, u32 ca, u32 fc, u32 fw, u32 fo STP) {
    asm volatile(
        "ds_read_b128 v[192:195], %[xa] offset:0\n\t"
        "ds_read_b128 v[196:199], %[xa] offset:1024\n\t"
        "ds_read_b128 v[200:203], %[xa] offset:2048\n\t"
        "ds_read_b128 v[204:207], %[xa] offset:3072\n\t"
        "ds_read_b128 v[208:211], %[xa] offset:4096\n\t"
        "ds_read_b128 v[212:215], %[xa] offset:5120\n\t"
        "ds_read_b128 v[216:219], %[xa] offset:6144\n\t"
        "ds_read_b128 v[220:223], %[xa] offset:7168\n\t"
        "v_mov_b32 v232, %[ca]\n\t"
        "v_mov_b32 v233, 1\n\t"
        "v_mov_b32 v234, %[fw]\n\t"
        "s_andn2_b32 s91, %[wp], 15\n\t"
        "s_mov_b32 s92, %[bl]\n\t"
        "s_mov_b32 s88, m0\n\t"
        "s_mov_b32 m0, %[xd]\n\t"
        "s_add_u32 s89, %[xs], 0x1000\n\t"
        "buffer_load_dwordx4 %[xv], %[rs], %[xs] offen offset:0 sc1 lds\n\t"
        "buffer_load_dwordx4 %[xv], %[rs], %[xs] offen offset:1024 sc1 lds\n\t"
        "s_mov_b32 m0, s88\n\t"
        "s_setprio 1\n\t"
        "s_waitcnt lgkmcnt(0)\n\t"
        "v_mfma_f32_16x16x32_bf16 v[176:179], v[192:195], v[16:19], 0\n\t"
        "s_lshr_b32 s95, s92, 20\n\t"
        "s_xnor_b32 s95, s95, %[wp]\n\t"
        "v_mfma_f32_16x16x32_bf16 v[180:183], v[208:211], v[16:19], 0\n\t"
        "s_and_b32 s95, s95, 1\n\t"
        "s_lshl_b32 s95, s95, 13\n\t"
        "v_mfma_f32_16x16x32_bf16 v[176:179], v[196:199], v[20:23], v[176:179]\n\t"
        "v_mfma_f32_16x16x32_bf16 v[180:183], v[212:215], v[20:23], v[180:183]\n\t"
        "v_mfma_f32_16x16x32_bf16 v[176:179], v[200:203], v[24:27], v[176:179]\n\t"
        "v_mfma_f32_16x16x32_bf16 v[180:183], v[216:219], v[24:27], v[180:183]\n\t"
        "v_mfma_f32_16x16x32_bf16 v[176:179], v[204:207], v[28:31], v[176:179]\n\t"
        "v_mfma_f32_16x16x32_bf16 v[180:183], v[220:223], v[28:31], v[180:183]\n\t"
        "s_lshl1_add_u32 s93, s92, s91\n\t"
        "s_add_u32 s93, s93, s95\n\t"
        "v_mfma_f32_16x16x32_bf16 v[184:187], v[192:195], v[96:99], 0\n\t"
        "s_add_u32 s92, s92, 0x40000\n\t"
        "v_mfma_f32_16x16x32_bf16 v[188:191], v[208:211], v[96:99], 0\n\t"
        "s_add_u32 s94, s93, 0x10000\n\t"
        "v_mfma_f32_16x16x32_bf16 v[184:187], v[196:199], v[100:103], v[184:187]\n\t"
        "v_cvt_pk_bf16_f32 v224, v176, v177\n\t"
        "v_mfma_f32_16x16x32_bf16 v[188:191], v[212:215], v[100:103], v[188:191]\n\t"
        "v_cvt_pk_bf16_f32 v225, v178, v179\n\t"
        "v_mfma_f32_16x16x32_bf16 v[184:187], v[200:203], v[104:107], v[184:187]\n\t"
        "v_cvt_pk_bf16_f32 v226, v180, v181\n\t"
        "v_mfma_f32_16x16x32_bf16 v[188:191], v[216:219], v[104:107], v[188:191]\n\t"
        "v_cvt_pk_bf16_f32 v227, v182, v183\n\t"
        "v_mfma_f32_16x16x32_bf16 v[184:187], v[204:207], v[108:111], v[184:187]\n\t"
        "buffer_store_dwordx4 v[224:227], %[oa], %[rs], s93 offen sc1\n\t"
        "v_mfma_f32_16x16x32_bf16 v[188:191], v[220:223], v[108:111], v[188:191]\n\t"
        "v_mfma_f32_16x16x32_bf16 v[176:179], v[192:195], v[32:35], 0\n\t"
        "s_lshr_b32 s95, s92, 20\n\t"
        "s_xnor_b32 s95, s95, %[wp]\n\t"
        "v_mfma_f32_16x16x32_bf16 v[180:183], v[208:211], v[32:35], 0\n\t"
        "s_and_b32 s95, s95, 1\n\t"
        "s_lshl_b32 s95, s95, 13\n\t"
        "v_mfma_f32_16x16x32_bf16 v[176:179], v[196:199], v[36:39], v[176:179]\n\t"
        "v_cvt_pk_bf16_f32 v228, v184, v185\n\t"
        "v_mfma_f32_16x16x32_bf16 v[180:183], v[212:215], v[36:39], v[180:183]\n\t"
        "v_cvt_pk_bf16_f32 v229, v186, v187\n\t"
        "v_mfma_f32_16x16x32_bf16 v[176:179], v[200:203], v[40:43], v[176:179]\n\t"
        "v_cvt_pk_bf16_f32 v230, v188, v189\n\t"
        "v_mfma_f32_16x16x32_bf16 v[180:183], v[216:219], v[40:43], v[180:183]\n\t"
        "v_cvt_pk_bf16_f32 v231, v190, v191\n\t"
        "v_mfma_f32_16x16x32_bf16 v[176:179], v[204:207], v[44:47], v[176:179]\n\t"
        "buffer_store_dwordx4 v[228:231], %[oa], %[rs], s94 offen sc1\n\t"
        "v_mfma_f32_16x16x32_bf16 v[180:183], v[220:223], v[44:47], v[180:183]\n\t"
        "s_lshl1_add_u32 s93, s92, s91\n\t"
        "s_add_u32 s93, s93, s95\n\t"
        "v_mfma_f32_16x16x32_bf16 v[184:187], v[192:195], v[112:115], 0\n\t"
        "s_add_u32 s92, s92, 0x40000\n\t"
        "v_mfma_f32_16x16x32_bf16 v[188:191], v[208:211], v[112:115], 0\n\t"
        "s_add_u32 s94, s93, 0x10000\n\t"
        "v_mfma_f32_16x16x32_bf16 v[184:187], v[196:199], v[116:119], v[184:187]\n\t"
        "v_cvt_pk_bf16_f32 v224, v176, v177\n\t"
        "v_mfma_f32_16x16x32_bf16 v[188:191], v[212:215], v[116:119], v[188:191]\n\t"
        "v_cvt_pk_bf16_f32 v225, v178, v179\n\t"
        "v_mfma_f32_16x16x32_bf16 v[184:187], v[200:203], v[120:123], v[184:187]\n\t"
        "v_cvt_pk_bf16_f32 v226, v180, v181\n\t"
        "v_mfma_f32_16x16x32_bf16 v[188:191], v[216:219], v[120:123], v[188:191]\n\t"
        "v_cvt_pk_bf16_f32 v227, v182, v183\n\t"
        "v_mfma_f32_16x16x32_bf16 v[184:187], v[204:207], v[124:127], v[184:187]\n\t"
        "buffer_store_dwordx4 v[224:227], %[oa], %[rs], s93 offen sc1\n\t"
        "v_mfma_f32_16x16x32_bf16 v[188:191], v[220:223], v[124:127], v[188:191]\n\t"
        "s_waitcnt vmcnt(5)\n\t"
        "s_mov_b64 exec, 1\n\t"
        "ds_add_rtn_u32 v233, v232, v233\n\t"
        "s_mov_b64 exec, -1\n\t"
        "v_mfma_f32_16x16x32_bf16 v[176:179], v[192:195], v[48:51], 0\n\t"
        "s_lshr_b32 s95, s92, 20\n\t"
        "s_xnor_b32 s95, s95, %[wp]\n\t"
        "v_mfma_f32_16x16x32_bf16 v[180:183], v[208:211], v[48:51], 0\n\t"
        "s_and_b32 s95, s95, 1\n\t"
        "s_lshl_b32 s95, s95, 13\n\t"
        "v_mfma_f32_16x16x32_bf16 v[176:179], v[196:199], v[52:55], v[176:179]\n\t"
        "v_cvt_pk_bf16_f32 v228, v184, v185\n\t"
        "v_mfma_f32_16x16x32_bf16 v[180:183], v[212:215], v[52:55], v[180:183]\n\t"
        "v_cvt_pk_bf16_f32 v229, v186, v187\n\t"
        "v_mfma_f32_16x16x32_bf16 v[176:179], v[200:203], v[56:59], v[176:179]\n\t"
        "v_cvt_pk_bf16_f32 v230, v188, v189\n\t"
        "v_mfma_f32_16x16x32_bf16 v[180:183], v[216:219], v[56:59], v[180:183]\n\t"
        "v_cvt_pk_bf16_f32 v231, v190, v191\n\t"
        "v_mfma_f32_16x16x32_bf16 v[176:179], v[204:207], v[60:63], v[176:179]\n\t"
        "buffer_store_dwordx4 v[228:231], %[oa], %[rs], s94 offen sc1\n\t"
        "v_mfma_f32_16x16x32_bf16 v[180:183], v[220:223], v[60:63], v[180:183]\n\t"
        "s_lshl1_add_u32 s93, s92, s91\n\t"
        "s_add_u32 s93, s93, s95\n\t"
        "v_mfma_f32_16x16x32_bf16 v[184:187], v[192:195], v[128:131], 0\n\t"
        "s_add_u32 s92, s92, 0x40000\n\t"
        "v_mfma_f32_16x16x32_bf16 v[188:191], v[208:211], v[128:131], 0\n\t"
        "s_add_u32 s94, s93, 0x10000\n\t"
        "v_mfma_f32_16x16x32_bf16 v[184:187], v[196:199], v[132:135], v[184:187]\n\t"
        "v_cvt_pk_bf16_f32 v224, v176, v177\n\t"
        "v_mfma_f32_16x16x32_bf16 v[188:191], v[212:215], v[132:135], v[188:191]\n\t"
        "v_cvt_pk_bf16_f32 v225, v178, v179\n\t"
        "v_mfma_f32_16x16x32_bf16 v[184:187], v[200:203], v[136:139], v[184:187]\n\t"
        "v_cvt_pk_bf16_f32 v226, v180, v181\n\t"
        "v_mfma_f32_16x16x32_bf16 v[188:191], v[216:219], v[136:139], v[188:191]\n\t"
        "v_cvt_pk_bf16_f32 v227, v182, v183\n\t"
        "v_mfma_f32_16x16x32_bf16 v[184:187], v[204:207], v[140:143], v[184:187]\n\t"
        "buffer_store_dwordx4 v[224:227], %[oa], %[rs], s93 offen sc1\n\t"
        "v_mfma_f32_16x16x32_bf16 v[188:191], v[220:223], v[140:143], v[188:191]\n\t"
        "s_waitcnt lgkmcnt(0)\n\t"
        "s_mov_b64 exec, 1\n\t"
        "v_cmpx_eq_u32 vcc, %[fc], v233\n\t"
        "buffer_store_dword v234, off, %[rs], %[fo] sc1\n\t"
        "s_mov_b64 exec, -1\n\t"
        "s_cmp_le_u32 %[it], %[nowv]\n\t"
        "s_cbranch_scc1 Lp%=\n\t"
        "s_setprio 0\n\t"
        "Lp%=:\n\t"
        "s_mov_b32 s88, m0\n\t"
        "s_mov_b32 m0, %[yd]\n\t"
        "s_add_u32 s89, %[ys], 0x1000\n\t"
        "buffer_load_dwordx4 %[xv], %[rs], %[ys] offen offset:0 sc1 lds\n\t"
        "buffer_load_dwordx4 %[xv], %[rs], %[ys] offen offset:1024 sc1 lds\n\t"
        "buffer_load_dwordx4 %[xv], %[rs], %[ys] offen offset:2048 sc1 lds\n\t"
        "buffer_load_dwordx4 %[xv], %[rs], %[ys] offen offset:3072 sc1 lds\n\t"
        "s_add_u32 m0, m0, 0x1000\n\t"
        "s_nop 0\n\t"
        "buffer_load_dwordx4 %[xv], %[rs], s89 offen offset:0 sc1 lds\n\t"
        "buffer_load_dwordx4 %[xv], %[rs], s89 offen offset:1024 sc1 lds\n\t"
        "buffer_load_dwordx4 %[xv], %[rs], s89 offen offset:2048 sc1 lds\n\t"
        "buffer_load_dwordx4 %[xv], %[rs], s89 offen offset:3072 sc1 lds\n\t"
        "s_mov_b32 m0, s88\n\t"
        "v_mfma_f32_16x16x32_bf16 v[176:179], v[192:195], v[64:67], 0\n\t"
        "s_lshr_b32 s95, s92, 20\n\t"
        "s_xnor_b32 s95, s95, %[wp]\n\t"
        "v_mfma_f32_16x16x32_bf16 v[180:183], v[208:211], v[64:67], 0\n\t"
        "s_and_b32 s95, s95, 1\n\t"
        "s_lshl_b32 s95, s95, 13\n\t"
        "v_mfma_f32_16x16x32_bf16 v[176:179], v[196:199], v[68:71], v[176:179]\n\t"
        "v_cvt_pk_bf16_f32 v228, v184, v185\n\t"
        "v_mfma_f32_16x16x32_bf16 v[180:183], v[212:215], v[68:71], v[180:183]\n\t"
        "v_cvt_pk_bf16_f32 v229, v186, v187\n\t"
        "v_mfma_f32_16x16x32_bf16 v[176:179], v[200:203], v[72:75], v[176:179]\n\t"
        "v_cvt_pk_bf16_f32 v230, v188, v189\n\t"
        "v_mfma_f32_16x16x32_bf16 v[180:183], v[216:219], v[72:75], v[180:183]\n\t"
        "v_cvt_pk_bf16_f32 v231, v190, v191\n\t"
        "v_mfma_f32_16x16x32_bf16 v[176:179], v[204:207], v[76:79], v[176:179]\n\t"
        "buffer_store_dwordx4 v[228:231], %[oa], %[rs], s94 offen sc1\n\t"
        "v_mfma_f32_16x16x32_bf16 v[180:183], v[220:223], v[76:79], v[180:183]\n\t"
        "s_lshl1_add_u32 s93, s92, s91\n\t"
        "s_add_u32 s93, s93, s95\n\t"
        "v_mfma_f32_16x16x32_bf16 v[184:187], v[192:195], v[144:147], 0\n\t"
        "s_add_u32 s92, s92, 0x40000\n\t"
        "v_mfma_f32_16x16x32_bf16 v[188:191], v[208:211], v[144:147], 0\n\t"
        "s_add_u32 s94, s93, 0x10000\n\t"
        "v_mfma_f32_16x16x32_bf16 v[184:187], v[196:199], v[148:151], v[184:187]\n\t"
        "v_cvt_pk_bf16_f32 v224, v176, v177\n\t"
        "v_mfma_f32_16x16x32_bf16 v[188:191], v[212:215], v[148:151], v[188:191]\n\t"
        "v_cvt_pk_bf16_f32 v225, v178, v179\n\t"
        "v_mfma_f32_16x16x32_bf16 v[184:187], v[200:203], v[152:155], v[184:187]\n\t"
        "v_cvt_pk_bf16_f32 v226, v180, v181\n\t"
        "v_mfma_f32_16x16x32_bf16 v[188:191], v[216:219], v[152:155], v[188:191]\n\t"
        "v_cvt_pk_bf16_f32 v227, v182, v183\n\t"
        "v_mfma_f32_16x16x32_bf16 v[184:187], v[204:207], v[156:159], v[184:187]\n\t"
        "buffer_store_dwordx4 v[224:227], %[oa], %[rs], s93 offen sc1\n\t"
        "v_mfma_f32_16x16x32_bf16 v[188:191], v[220:223], v[156:159], v[188:191]\n\t"
        "v_mfma_f32_16x16x32_bf16 v[176:179], v[192:195], v[80:83], 0\n\t"
        "s_lshr_b32 s95, s92, 20\n\t"
        "s_xnor_b32 s95, s95, %[wp]\n\t"
        "v_mfma_f32_16x16x32_bf16 v[180:183], v[208:211], v[80:83], 0\n\t"
        "s_and_b32 s95, s95, 1\n\t"
        "s_lshl_b32 s95, s95, 13\n\t"
        "v_mfma_f32_16x16x32_bf16 v[176:179], v[196:199], v[84:87], v[176:179]\n\t"
        "v_cvt_pk_bf16_f32 v228, v184, v185\n\t"
        "v_mfma_f32_16x16x32_bf16 v[180:183], v[212:215], v[84:87], v[180:183]\n\t"
        "v_cvt_pk_bf16_f32 v229, v186, v187\n\t"
        "v_mfma_f32_16x16x32_bf16 v[176:179], v[200:203], v[88:91], v[176:179]\n\t"
        "v_cvt_pk_bf16_f32 v230, v188, v189\n\t"
        "v_mfma_f32_16x16x32_bf16 v[180:183], v[216:219], v[88:91], v[180:183]\n\t"
        "v_cvt_pk_bf16_f32 v231, v190, v191\n\t"
        "v_mfma_f32_16x16x32_bf16 v[176:179], v[204:207], v[92:95], v[176:179]\n\t"
        "buffer_store_dwordx4 v[228:231], %[oa], %[rs], s94 offen sc1\n\t"
        "v_mfma_f32_16x16x32_bf16 v[180:183], v[220:223], v[92:95], v[180:183]\n\t"
        "s_lshl1_add_u32 s93, s92, s91\n\t"
        "s_add_u32 s93, s93, s95\n\t"
        "v_mfma_f32_16x16x32_bf16 v[184:187], v[192:195], v[160:163], 0\n\t"
        "s_add_u32 s92, s92, 0x40000\n\t"
        "v_mfma_f32_16x16x32_bf16 v[188:191], v[208:211], v[160:163], 0\n\t"
        "s_add_u32 s94, s93, 0x10000\n\t"
        "v_mfma_f32_16x16x32_bf16 v[184:187], v[196:199], v[164:167], v[184:187]\n\t"
        "v_cvt_pk_bf16_f32 v224, v176, v177\n\t"
        "v_mfma_f32_16x16x32_bf16 v[188:191], v[212:215], v[164:167], v[188:191]\n\t"
        "v_cvt_pk_bf16_f32 v225, v178, v179\n\t"
        "v_mfma_f32_16x16x32_bf16 v[184:187], v[200:203], v[168:171], v[184:187]\n\t"
        "v_cvt_pk_bf16_f32 v226, v180, v181\n\t"
        "v_mfma_f32_16x16x32_bf16 v[188:191], v[216:219], v[168:171], v[188:191]\n\t"
        "v_cvt_pk_bf16_f32 v227, v182, v183\n\t"
        "v_mfma_f32_16x16x32_bf16 v[184:187], v[204:207], v[172:175], v[184:187]\n\t"
        "buffer_store_dwordx4 v[224:227], %[oa], %[rs], s93 offen sc1\n\t"
        "v_mfma_f32_16x16x32_bf16 v[188:191], v[220:223], v[172:175], v[188:191]\n\t"
        "s_and_b32 s95, %[it], 31\n\t"
        "s_cmp_le_u32 s95, %[nowv]\n\t"
        "s_cbranch_scc1 Lnb%=\n\t"
        "s_waitcnt vmcnt(17)\n\t"
        "s_barrier\n\t"
        "Lnb%=:\n\t"
        "s_setprio 0\n\t"
        "s_nop 3\n\t"
        "v_cvt_pk_bf16_f32 v228, v184, v185\n\t"
        "v_cvt_pk_bf16_f32 v229, v186, v187\n\t"
        "v_cvt_pk_bf16_f32 v230, v188, v189\n\t"
        "v_cvt_pk_bf16_f32 v231, v190, v191\n\t"
        "buffer_store_dwordx4 v[228:231], %[oa], %[rs], s94 offen sc1\n\t"
        "s_nop 1\n\t"
        :
        : [w0] "{v[16:47]}"(W[0]), [w1] "{v[48:79]}"(W[1]), [w2] "{v[80:111]}"(W[2]), [w3] "{v[112:143]}"(W[3]), [w4] "{v[144:175]}"(W[4]), [xa] "v"(xa), [oa] "v"(oa), [rs] "s"(rs), [bl] "s"(bl), [wp] "s"(wp), [it] "s"(it), [nowv] "n"(NOWV), [ca] "s"(ca), [fc] "s"(fc), [fw] "s"(fw), [fo] "s"(fo), [xv] "v"(xv), [xs] "s"(xs), [xd] "s"(xd), [ys] "s"(ys), [yd] "s"(yd)
        : "s88", "s89", "s91", "s92", "s93", "s94", "s95", "v176", "v177", "v178", "v179", "v180", "v181", "v182", "v183", "v184", "v185", "v186", "v187", "v188", "v189", "v190", "v191", "v192", "v193", "v194", "v195", "v196", "v197", "v198", "v199", "v200", "v201", "v202", "v203", "v204", "v205", "v206", "v207", "v208", "v209", "v210", "v211", "v212", "v213", "v214", "v215", "v216", "v217", "v218", "v219", "v220", "v221", "v222", "v223", "v224", "v225", "v226", "v227", "v228", "v229", "v230", "v231", "v232", "v233", "v234", "vcc", "scc", "memory");
}

#define TP0 198
#define TP1 16
#define TP2 184
#define TP3 30
#define TP4 0
#define TP5 130
#define TP6 112
#define TP7 208
#define TP8 4
#define TP9 24
#define TP10 4
#define TP11 2
#define TP12 2
#define TP13 150
#define TP14 70
#define TP15 4
#define WAHEAD 3
#define FILLG 4
#define WFIRST 2
#define EARLY_N 35
#define BUMP_N 3
#define BUMP_WIN 0xffu
#define BUMP_S0 8
#define BUMP_MAX 3
#define TV(idx) ((u32)(TP##idx) * 8u)
#define TLOAD() do { } while (0)

__device__ __forceinline__ void nap(u32 n) {
    u32 hi = n >> 6, lo = (n >> 4) & 3u;
    asm volatile("s_cmp_eq_u32 %0, 0\n\ts_cbranch_scc1 Ln%=\nLm%=:\n\ts_sleep 1\n\ts_sub_u32 %0, %0, 1\n\ts_cmp_lg_u32 %0, 0\n\ts_cbranch_scc1 Lm%=\nLn%=:\n\t"
                 "s_cmp_eq_u32 %1, 0\n\ts_cbranch_scc1 Lp%=\nLo%=:\n\ts_sub_u32 %1, %1, 1\n\ts_cmp_lg_u32 %1, 0\n\ts_cbranch_scc1 Lo%=\nLp%=:"
                 : "+s"(hi), "+s"(lo) :: "scc", "memory");
}
typedef __attribute__((address_space(3))) u32* lds_u32p;
typedef __attribute__((address_space(3))) volatile u32* lds_vu32p;
typedef __attribute__((address_space(3))) u16* lds_u16p;
typedef __attribute__((address_space(3))) u32x2* lds_u32x2p;
typedef __attribute__((address_space(3))) u32x4* lds_u32x4p;
__device__ __forceinline__ u32 rfl(u32 x) { return __builtin_amdgcn_readfirstlane(x); }
__device__ __forceinline__ bool has_sent(u32x4 v) { return (v[0] == SENT) | (v[1] == SENT) | (v[2] == SENT) | (v[3] == SENT); }
__device__ __forceinline__ u32 now32() { return (u32)__builtin_amdgcn_s_memtime(); }
__device__ __forceinline__ u32 step_tag(int s) { return (u32)(s + 1) * 0x10001u; }

template <int S0 = 0>
__device__ __forceinline__ u32 unless_neg(u32 v, int s) {
    u32 r;
    asm("s_cmp_lt_i32 %2, %3\n\ts_cselect_b32 %0, 0, %1" : "=s"(r) : "s"(v), "s"(s), "n"(S0) : "scc");
    return r;
}
template <int S0 = 0>
__device__ __forceinline__ u32x4 live(u32x4 srd, int s) { srd[2] = unless_neg<S0>(srd[2], s); return srd; }
__device__ __forceinline__ float shx(float v, int m, int lane) { return __int_as_float(__builtin_amdgcn_ds_bpermute((lane ^ m) << 2, __float_as_int(v))); }
__device__ __forceinline__ void mem0_image(const u16* __restrict__ mem0, char* smem, const int tid) {
    for (int c = tid; c < 640; c += 512) {
        u32x4 v = *(const u32x4*)(mem0 + (c & 7) * D_ + (c >> 3) * 8);
#pragma unroll
        for (int i = 0; i < 4; ++i) v[i] = v[i] == SENT ? ~0u : v[i];
        ((u32x4*)smem)[c + (c >= 576 ? 64 : 0)] = v;
    }
}
__device__ __forceinline__ u32 once_more(u32& left, u32 lbase, u32* errw) {
    if (__builtin_expect(left != 0, 1)) { --left; return 2u; }
    atomicOr(errw, 1u);
    const int lane = lane_id();
    for (int k = 0; k < 11; ++k) *(lds_u32x4p)(lbase + (u32)(k * 1024 + lane * 16)) = (u32x4){0u, 0u, 0u, 0u};
    asm volatile("s_waitcnt lgkmcnt(0)" ::: "memory");
    return 0u;
}
constexpr u32 TS_MISS = 0x10000u;
__device__ __forceinline__ u32 read_ts(rsrc_t rs, u32 off, int s, u32& m) {
    const int lane = lane_id();
    const u32 vo = lane < 8 ? (u32)lane * 128u : OOB, base = (u32)__builtin_amdgcn_s_memrealtime() * 24u - 0x100000u;
    u32x4 t;
#pragma unroll 1
    for (u32 spins = 1;; ++spins) {
        CFENCE();
        t = ldx4(rs, vo, off);
        m = (u32)__ballot(t[3] == (u32)s + 1u);
        if (((m | (m >> 4)) & 15u) == 15u) break;
        m = 0;
        if (spins >= 0x4000u) break;
        __builtin_amdgcn_s_sleep(4);
    }
    u32 v = t[3] == (u32)s + 1u ? t[1] - base : 0u;
    v = max(v, (u32)__builtin_amdgcn_update_dpp(0u, v, 0xb1, 0xf, 0xf, false));
    v = max(v, (u32)__builtin_amdgcn_update_dpp(0u, v, 0x4e, 0xf, 0xf, false));
    v = max(v, (u32)__builtin_amdgcn_update_dpp(0u, v, 0x141, 0xf, 0xf, false));
    return to_sgpr(v + base);
}
__device__ __forceinline__ u32 read_ts_late(rsrc_t rs, u32x4 srd, u32 off, int s, u32& m, u32 gap = 1024) {
    u32 t;
    ts_look(t, m, srd, off, (u32)s + 1u);
    if (m) return t;
    nap(gap);
    return read_ts(rs, off, s, m);
}
__device__ __forceinline__ void until(u32 t) { for (int q = 0; q < 256 && (int)(t - (u32)__builtin_amdgcn_s_memrealtime() * 24u) > 0; ++q) __builtin_amdgcn_s_sleep(1); }
__device__ __forceinline__ u32 umin3(u32 a, u32 b, u32 c) { return min(min(a, b), c); }
__device__ __forceinline__ u32 dist(u32 d, u32x4 v, u32 k) { return umin3(umin3(d, v[0] ^ k, v[1] ^ k), v[2] ^ k, v[3] ^ k); }
#define COUNT(i) do { } while (0)
#define COUNT0() do { } while (0)
#define COUNT1(i) do { } while (0)
__device__ __forceinline__ void touch(u32x4 srd, u32 wp, int s, u32 lds) {
    u32 sv;
    const u32 off = slot_off(wp, s) + SL_TS + 2048u + ((~((wp >> 3) ^ ((u32)s >> 5)) & 1u) << 18);
    asm volatile("s_mov_b32 %0, m0\n\ts_mov_b32 m0, %3\n\ts_nop 3\n\tbuffer_load_dword off, %1, %2 lds\n\ts_mov_b32 m0, %0" : "=&s"(sv) : "s"(srd), "s"(off), "s"(lds) : "memory");
}
__device__ __forceinline__ void track_rate(u32 c0, u32 r0, u32& pc, u32& pr, u32& kr) {
    const u32 dr = r0 - pr;
    if (dr >= 6000u) {
        if (dr < 40000u) kr = to_sgpr(min(max((u32)(4096.f * (float)(c0 - pc) * __builtin_amdgcn_rcpf((float)dr) + 0.5f), 2560u), 4352u));
        pc = c0; pr = r0;
    }
}

#define LANES() const int lane = lane_id(), n = lane & 15, g = lane >> 4; (void)n; (void)g
constexpr u32 L_Q = 12288, L_K = 18432, L_E = 24576, L_DR = 25088, L_MSK = 25600, L_KQ = 32768;
constexpr float LIM_X = 32.f, LIM_D = 1.0995116e12f;
__device__ __forceinline__ u32 kq_buf(int sg) { return L_KQ + (u32)(sg % 3) * 16384u; }

__device__ __forceinline__ u32 p_pos(int row, int g, int dg) { return (u32)((row >> 1) * 64 + (g & 1) * 32 + (row & 1) * 16 + dg * 8 + (g >> 1) * 4); }
__device__ __forceinline__ void s_slow(const int h, const int s, rsrc_t rs, u32 so, u32 lbase) {
    const int lane = lane_id(), row = lane & 7, j = lane >> 3;
    const float ce = 0.08838834764831845f * 1.4426950408889634f;
    float sc[5] = {0.f, 0.f, 0.f, 0.f, 0.f};
    const u32 kx = lbase + kq_buf(s) + (u32)((j >> 2) * 4096 + (j & 3) * 64);
#pragma unroll 1
    for (int e = 0; e < DH_; ++e) {
        const u32 qa = lbase + L_Q + (u32)((e >> 4) * 768 + (row >> 2) * 192 + (e & 15) * 8 + (row & 3) * 2);
        const float q = bf2f(*(lds_u16p)qa);
        const u32 ka = kx + (u32)((e >> 5) * 1024 + ((e >> 2) & 3) * 256 + ((e >> 4) & 1) * 8 + (e & 3) * 2);
#pragma unroll
        for (int i = 0; i < 4; ++i) sc[i] += q * bf2f(*(lds_u16p)(ka + i * 16));
        sc[4] += q * bf2f(*(lds_u16p)(lbase + L_K + (u32)((e >> 4) * 768 + (j >> 1) * 192 + (e & 15) * 8 + (j & 1) * 2)));
    }
    sc[4] *= ce;
    const u32x2 ev = *(lds_u32x2p)(lbase + L_E + (u32)((j >> 1) * 128 + row * 16 + (j & 1) * 8));
    float se = (bf2f((u16)ev[0]) + bf2f((u16)(ev[0] >> 16))) + (bf2f((u16)ev[1]) + bf2f((u16)(ev[1] >> 16)));
    float mx = fmaxf(fmaxf(fmaxf(sc[0], sc[1]), fmaxf(sc[2], sc[3])), sc[4]);
#pragma unroll
    for (int o = 8; o < 64; o <<= 1) { se += shx(se, o, lane); mx = fmaxf(mx, shx(mx, o, lane)); }
    const bool rew = ev[0] == ~0u || ev[1] == ~0u;
    u64 rw = __ballot(rew);
    rw |= rw >> 32; rw |= rw >> 16; rw |= rw >> 8;
    const bool again = (rw >> row) & 1u;
    float al = exp2f(-mx);
    if (again) {
        const u32x2 en = {pack2(exp2f(sc[0] - mx), exp2f(sc[1] - mx)), pack2(exp2f(sc[2] - mx), exp2f(sc[3] - mx))};
        se = (bf2f((u16)en[0]) + bf2f((u16)(en[0] >> 16))) + (bf2f((u16)en[1]) + bf2f((u16)(en[1] >> 16)));
        al = 1.f;
    }
    float sn = se;
#pragma unroll
    for (int o = 8; o < 64; o <<= 1) sn += shx(sn, o, lane);
    if (again) se = sn;
    const float em = exp2f(sc[4] - mx);
    float sm = em;
#pragma unroll
    for (int o = 8; o < 64; o <<= 1) sm += shx(sm, o, lane);
    const float rd = 1.f / (sm + al * se);
    if (rw & 255u) {
#pragma unroll 1
        for (u32 spins = 0; spins < SPIN_CAP && !__all(*(lds_vu32p)(lbase + L_DR + (u32)(lane & 1) * 4) == step_tag(s)); ++spins) __builtin_amdgcn_s_sleep(1);
        const u32x2 en = {pack2(exp2f(sc[0] - mx), exp2f(sc[1] - mx)), pack2(exp2f(sc[2] - mx), exp2f(sc[3] - mx))};
        const u32 eo = again ? (u32)((h >> 3) * 32768 + ((h >> 1) & 3) * 1024 + (j >> 1) * 256 + (h & 1) * 128 + row * 16 + (j & 1) * 8) : OOB;
#pragma unroll
        for (int c = 0; c < NCP; ++c) stx2(rs, eo, en, so + cpy(c));
    }
    DRAIN();
    const u32 pm = pack2(em * rd, shx(em * rd, 8, lane));
    const int g = j >> 1;
    u32 dg = pack2(row == 2 * g ? al * rd : 0.f, row == 2 * g + 1 ? al * rd : 0.f);
    if (again && (row >> 1) == (g ^ 1)) dg = MARK;
    const int gj = h >> 2;
    const u32 po = SL_P + (u32)((gj >> 2) * 32768 + (gj & 1) * 2048 + ((gj >> 1) & 1) * 1024 + (h & 3) * 256) + p_pos(row, g, (j ^ ~h) & 1);
    const bool done = ldx1(rs, po, so) != SENT;
#pragma unroll
    for (int c = 0; c < NCP; ++c) stx1(rs, done ? OOB : po, (j & 1) ? dg : pm, so + cpy(c));
}

__device__ __forceinline__ void s_role(const int wave, const int h, const int xg, const u64 tag, const u16* __restrict__ mem0, const u32x32 (&W)[5],
                                       char* __restrict__ ws, char* smem) {
    const int lane = lane_id(), tid = wave * 64 + lane, n = lane & 15, g = lane >> 4;
    u32* errw = (u32*)(ws + OFF_ERR);
    const rsrc_t rs = mk_rsrc(ws);
    const u32x4 srd = mk_srd(ws);
    const u32 lbase = (u32)(uintptr_t)(__attribute__((address_space(3))) char*)smem;
    TLOAD();
    int wslot = 0; const int wid = 128 + h;
    WSTAMP(2);
    mem0_image(mem0, smem, tid);
    if (tid < 448) *(u32x4*)(smem + L_K + tid * 16) = (u32x4){0u, 0u, 0u, 0u};
    const u32 wp = wait_init(rs, tag, 1);
    if (wave == 2) XSTAMP(wid, 224);
    const u32 sidx = (n & 2) ? 0xEEEE : 0x4444;
    const u32 va = lbase + (u32)(g * 256 + (n & 2) * 64 + (n >> 2) * 32 + (n & 1) * 16);
    const u32 ow = lbase + (u32)((wave & 3) * 768 + n * 8) + (wave >= 4 ? L_Q + (u32)((g >> 1) * 192 + (g & 1) * 4) : L_K + (u32)g * 192);
    u32 rnd = 0;
    const float ce = 0.08838834764831845f * 1.4426950408889634f;
    STAMPS();
    COUNT0();
#define BAR0() asm volatile("s_waitcnt vmcnt(0) lgkmcnt(0)\n\ts_barrier" ::: "memory")
#define Q_LAST() do { u32 redo; do s_l(rnd, redo, lbase); while (__builtin_expect(redo, 0)); } while (0)

    if ((wave >> 1) == 2) {
        LANES();
        const int kt = wave & 1;
        const u32 kxa = lbase + (u32)(kt * 4096 + lane * 16), qtr = lbase + L_Q + (u32)((lane & 1) * 192 + (lane >> 2) * 8);
        const u32 ep = (u32)((n & 7) * 16 + (g & 1) * 8 + (n >> 3) * 4);
        const u32 eo = (u32)((h >> 3) * 32768 + ((h >> 1) & 3) * 1024 + (2 * kt + (g >> 1)) * 256 + (h & 1) * 128) + ep;
        const u32 ew = lbase + L_E + (u32)((2 * kt + (g >> 1)) * 128) + ep;
        const u32 ts = slot_v(wp, lane) + put_off(wp, 2u, lane), tk = kq_buf(lane);
#pragma unroll 1
        for (int s = -1; s + 1 < NSEG; ++s) {
            BAR0();
            s_e(W, rnd, va, sidx, ow, lbase, kxa + tab(tk, s & 63), qtr, (u32)kt * 768u, eo, ew, LIM_X, live(srd, s), tab(ts, s & 63) STA);
            DRAIN();
            *(lds_u32p)(lbase + L_DR + (u32)kt * 4) = step_tag(s);
            FLUSH();
        }
        BAR0();
        Q_LAST();
    } else if (wave == 6) {
        LANES();
        const int gj = h >> 2;
        const u32 po = SL_P + (u32)((gj >> 2) * 32768 + (gj & 1) * 2048 + ((gj >> 1) & 1) * 1024 + (h & 3) * 256) + p_pos(n & 7, g, ((n >> 3) ^ ~h) & 1);
        const u32 er = lbase + L_E + (u32)(g * 128 + (n & 7) * 16);
        const u32 qtr = lbase + L_Q + (u32)((lane & 1) * 192 + (lane >> 2) * 8), ktr = lbase + L_K + (u32)((lane & 3) * 192 + (lane >> 2) * 8);
        const u32x4 ones = {0x3f803f80u, 0x3f803f80u, 0x3f803f80u, 0x3f803f80u};
        const u32 ts = slot_v(wp, lane) + put_off(wp, 2u, lane);
#pragma unroll 1
        for (int s = -1; s + 1 < NSEG; ++s) {
            const u32 so = tab(ts, s & 63);
            u32 bad;
            BAR0();
            s_f(W, rnd, va, sidx, ow, lbase, bad, ones, qtr, ktr, er, po, n < 8 ? 1.f : 0.f, n == 8 + 2 * g ? 1.f : 0.f, n == 9 + 2 * g ? 1.f : 0.f, live(srd, s), so, ce, LIM_D STA);
            if (__builtin_expect(bad && s >= 0, 0)) s_slow(h, s, rs, so, lbase);
            FLUSH();
        }
        BAR0();
        Q_LAST();
    } else if (wave == 7) {
        LANES();
        const u32 vm = (u32)lane * 16, cp = (u32)xg ^ (wp >> 1);
        u32 c0, r0, kr = 3950;
        calib(c0, r0);
        const u32 tc = slot_v(wp ^ 8u, lane) + copy_off(cp, lane);
        u32 pc = c0, pr = r0, tg = r0, sc = 0, left = SPIN_CAP;
        asm volatile("s_waitcnt lgkmcnt(0)" ::: "memory");
#pragma unroll 1
        for (int s = -1; s + 1 < NSEG; ++s) {
            const u32 so = sc;
            sc = tab(tc, s & 63);
            u32 poll = (0x12u >> (min(s + 1, 2) * 2)) & 3u;
            if (s == 0) BAR0();
            const u32 rn0 = rnd;
            u32 t1 = 0, tm = (u32)s + 1u;
            for (;;) {
                s_m(W, rnd, va, sidx, ow, lbase, left, c0, r0, t1, tm, tg, kr, vm, so, lbase + (poll & 2u) * 40960u, live(srd, s), poll, lane < 8 ? vm * 8u : OOB, sc + SL_TS STA);
                if (__builtin_expect(left != ~0u, 1)) break;
                left = 0;
                once_more(left, lbase, errw);
                poll = 0;
            }
            if (rnd - rn0 > 1u) COUNT(0);
            tg = t1;
            if (__builtin_expect(!tm && s >= 0, 0)) tg = read_ts(rs, sc + SL_TS, s, tm);
            track_rate(c0, r0, pc, pr, kr);
            FLUSH();
        }
        const u32 so = sc;
        for (u32 poll = 1;;) {
            if (poll) gather(tg, kr, vm, so, lbase, srd);
            if (poll == 1) LBAR();
            DRAIN();
            u32 redo;
            s_l(rnd, redo, lbase);
            if (__builtin_expect(!redo, 1)) break;
            poll = once_more(left, lbase, errw);
        }
    } else if (wave < 2) {
        LANES();
        const int nt = wave & 1;
        const u32 ktr = lbase + L_K + (u32)((lane & 3) * 192 + (lane >> 2) * 8);
        f32x4* msk = (f32x4*)(smem + L_MSK) + nt * 128 + lane;
#pragma unroll
        for (int q = 0; q < 2; ++q) {
            f32x4 m;
#pragma unroll
            for (int i = 0; i < 4; ++i) m[i] = q * 16 + 4 * g + i <= nt * 16 + n ? 0.f : -__builtin_huge_valf();
            msk[q * 64] = m;
        }
        const u32 vl = lbase + (u32)lane * 16;
        const u32 o0 = (u32)OFF_PX + (u32)((h * 2 + nt) * 1024 + lane * 16);
        const u32 o1 = (u32)OFF_PXM + (u32)(((h >> 2) * 2 + nt) * 1024 + ((h & 3) * 16 + n) * 16 + g * 4);
        u32x4 p0 = {0u, 0u, 0u, 0u};
        u32 p1 = 0;
#pragma unroll 1
        for (int s = -1; s < NSEG; ++s) {
            const u32 off = s > 0 ? 0u : OOB;
            BAR0();
            stx4(rs, (o0 + (u32)s * 65536 - 65536) | off, p0);
            stx1(rs, (o1 + (u32)s * 16384 - 16384) | off, p1);
            if (s + 1 < NSEG) s_k0(W, rnd, va, sidx, ow, lbase STA);
            else {
                s_k2(W, rnd, va, sidx, ow, lbase);
                DRAIN();
                stx1(rs, lane == 0 ? (u32)(OFF_FP + (h >> 2) * 128 + ((h & 3) * 2 + nt) * 4) : OOB, 1u);
            }
            px_step(p0, p1, vl + kq_buf(s + 3), vl + kq_buf(s + 3) + 8192 + (u32)nt * 4096, vl + L_MSK + (u32)nt * 2048, ktr, ce);
            STAMP(3);
            FLUSH();
        }
        stx4(rs, o0 + (NSEG - 1) * 65536, p0);
        stx1(rs, o1 + (NSEG - 1) * 16384, p1);
        DRAIN();
        stx1(rs, lane == 0 ? (u32)(OFF_FZ + ((h >> 2) * 2 + nt) * 128 + (h & 3) * 4) : OOB, 1u);
    } else if (wave == 2) {
        LANES();
        u32 cap = SPIN_CAP;
        u32 have = 0;
        auto seen = [&](u32x4 f) {
            u32 a = min(2 * rfl(f[0]), 2 * rfl(f[1]) + 1);
            if (h < 16) a = min(a, max(rfl(f[2]), 4u) - 1u);
            have = max(have, a);
        };
        const u32 tt = slot_v(wp ^ 8u, lane) + SL_TS + 2048u;
        *(lds_u32p)(lbase + 11520u + (u32)lane * 4) = 0u;
#pragma unroll 1
        for (int s = -1; s + 1 < NSEG; ++s) {
            const u32 pfo = far_off(wp ^ 1u, NEAR_KX + (u32)((s + 1) * H_ + h) * 8192);
            BAR0();
            seen(*(lds_u32x4p)(lbase + 11520u));
            s_k0(W, rnd, va, sidx, ow, lbase STA);
            u32 spins = 0;
#pragma unroll 1
            for (; have <= (u32)(s + 1) && spins < cap; ++spins) {
                CFENCE();
                seen(ldx4(rs, 0u, (u32)(OFF_FA + h * 128)));
                if (spins) __builtin_amdgcn_s_sleep(2);
            }
            if (spins >= cap) { atomicOr(errw, 8u); cap = 1; }
            CFENCE();
            if (s < 0) XSTAMP(wid, 226);
            s_fetch((u32)lane * 16, lane == s + 1 ? tt : OOB, srd, pfo, lbase + kq_buf(s + 1), lbase + 11264u);
            s_fetch((u32)lane * 16, have <= (u32)(s + 4) && lane < 4 ? (u32)(OFF_FA + h * 128 + lane * 4) : OOB, srd, pfo - 2u * (NEAR_KX - NEAR_QX),
                    lbase + kq_buf(s + 1) + 8192, lbase + 11520u);
            FLUSH();
        }
        BAR0();
        s_k2(W, rnd, va, sidx, ow, lbase);
    } else {
#pragma unroll 1
        for (int s = -1; s + 1 < NSEG; ++s) {
            BAR0();
            s_k0(W, rnd, va, sidx, ow, lbase STA);
            FLUSH();
        }
        BAR0();
        s_k2(W, rnd, va, sidx, ow, lbase);
    }
    COUNT1(0);
}

__device__ __forceinline__ void v_role(const int wave, const int h, const int xg, const u64 tag, const u16* __restrict__ mem0, const u32x32 (&W)[5],
                                       char* __restrict__ ws, char* smem) {
    const int lane = lane_id(), tid = wave * 64 + lane, n = lane & 15, g = lane >> 4;
    u32* errw = (u32*)(ws + OFF_ERR);
    const rsrc_t rs = mk_rsrc(ws);
    const u32x4 srd = mk_srd(ws);
    const u32 lbase = (u32)(uintptr_t)(__attribute__((address_space(3))) char*)smem;
    TLOAD();
    int wslot = 0; const int wid = 160 + h;
    WSTAMP(2);
    mem0_image(mem0, smem, tid);
    *(lds_u32p)(lbase + 14336) = 0u;
    const u32 wp = wait_init(rs, tag, 1);
    const u32 sidx = (n & (wave < 4 ? 2 : 8)) ? 0xEEEE : 0x4444;
    asm volatile("s_waitcnt vmcnt(0) lgkmcnt(0)" ::: "memory");
    STAMPS();
    COUNT0();
    u32 rnd = 0;
    if (wave < 4) {
        const u32 va = lbase + (u32)(g * 256 + (n & 2) * 64 + (n >> 2) * 32 + (n & 1) * 16), vw = lbase + 12288 + (u32)((wave * 16 + n) * 16 + g * 4);
        if (wave) {
#pragma unroll 1
            for (int s = 0; s < NSEG; ++s) {
                v_a0(W, rnd, va, sidx, vw, lbase STA);
                FLUSH();
                if (wave == 1 && s + 1 < NSEG) touch(srd, wp, s + 1, lbase + 15360u);
            }
        } else {
            const u32 tc = slot_v(wp ^ 8u, lane) + copy_off((u32)xg ^ (wp >> 1), lane);
            u32 c0, r0, kr = 3950;
            calib(c0, r0);
            u32 pc = c0, pr = r0, tg = r0, sc = 0, left = SPIN_CAP;
#pragma unroll 1
            for (int s = 0; s < NSEG; ++s) {
                const u32 so = sc, rn0 = rnd;
                sc = tab(tc, s);
                for (u32 poll = 2u - (((u32)s + 63u) >> 6);; poll = 0) {
                    v_a1(W, rnd, va, sidx, vw, lbase, left, c0, r0, tg + TV(15), kr, poll, (u32)lane * 16, so, lbase + (poll & 2u) * 16384u, live<1>(srd, s) STA);
                    if (__builtin_expect(left != ~0u, 1)) break;
                    left = 0;
                    once_more(left, lbase, errw);
                }
                const u32 look = rnd - rn0;
                if (look > 1) COUNT(3);
                u32 tm;
                if (s + 1 < NSEG) tg = look > 1 ? read_ts(rs, sc + SL_TS, s, tm) : read_ts_late(rs, srd, sc + SL_TS, s, tm, 0u);
                track_rate(c0, r0, pc, pr, kr);
                FLUSH();
            }
        }
    } else {
        const u32 vr = lbase + 12288 + (u32)((g * 8 + (n >> 2)) * 16 + (n & 1) * 8);
        u32 z[3];
#pragma unroll
        for (int p = 0; p < 3; ++p) z[p] = p == 2 && (g & 1) ? OOB : z_off(wp, (u32)((p * 2 + (g & 1)) * 8 + (wave & 3) * 2 + (g >> 1))) + (u32)(h * 256 + n * 16);
#pragma unroll 1
        for (int s = 0; s < NSEG; ++s) {
            const u32 sz = slot_off(wp, s);
            v_b(W, rnd, sidx, vr, z[0] + sz, z[1] + sz, z[2] + sz, srd, lbase STA);
            FLUSH();
        }
    }
    DRAIN();
    LBAR();
    stx1(rs, wave == 0 && lane_id() < 2 ? (u32)(OFF_FZ + ((h >> 2) * 2 + lane_id()) * 128 + 16 + (h & 3) * 4) : OOB, 1u);
    COUNT1(3);
}

constexpr u32 L_U = 8192, L_T = 16384;
__device__ __forceinline__ u32x4 sub4(const u32x32& v, const int i) { return (u32x4){v[i], v[i + 1], v[i + 2], v[i + 3]}; }
__device__ __forceinline__ void r_slow(const int s, rsrc_t rs, u32 so, u32 sz, u32 sm, u32 pv, u32 zv, u32 ra, u32 rb, u32 mo, u32 sidx, u32 lbase, u32& cap, u32* errw) {
    const int lane = lane_id(), n = lane & 15;
    u32 spins = 0;
    u32x4 p[8], z[8];
#pragma unroll 1
    for (; spins < cap; ++spins) {
        CFENCE();
        u32 d = 1;
#pragma unroll
        for (int j = 0; j < 8; ++j) p[j] = ldx4(rs, pv + (u32)((j & 1) * 256 + ((j >> 1) & 1) * 1024), so + (u32)(j >> 2) * 32768u);
#pragma unroll
        for (int j = 0; j < 8; ++j) z[j] = ldx4(rs, zv + (u32)j * 1024, sz);
#pragma unroll
        for (int j = 0; j < 8; ++j) d = dist(dist(d, p[j], SENT), z[j], SENT);
        if (!__any(d == 0)) break;
    }
    if (spins >= cap) { atomicOr(errw, 2u); cap = 2; }
#pragma unroll
    for (int j = 0; j < 8; ++j) {
#pragma unroll
        for (int i = 0; i < 4; ++i) p[j][i] = p[j][i] == MARK ? 0u : p[j][i];
    }
    *(lds_vu32p)(lbase + L_T + 12) = step_tag(s);
    LBAR();
    LBAR();
    f32x4 acc = {0.f, 0.f, 0.f, 0.f};
#pragma unroll
    for (int j = 0; j < 8; j += 2) {
        const u32 ua = (j == 6 ? rb : ra) + (u32)j * 1024;
        acc = smf((u32x4){p[j][2], p[j][3], p[j + 1][0], p[j + 1][1]}, z[j], z[j + 1], acc, (int)sidx);
        acc = smf((u32x4){p[j + 1][2], p[j + 1][3], p[(j + 2) & 7][0], p[(j + 2) & 7][1]}, *(lds_u32x4p)ua, *(lds_u32x4p)(ua + 1024), acc, (int)sidx);
    }
    const float x0 = acc[0] + acc[2], x1 = acc[1] + acc[3], o = __shfl_xor(n < 8 ? x1 : x0, 8);
    const u32 r = n < 8 ? pack2(x0, o) : pack2(o, x1);
#pragma unroll
    for (int c = 0; c < NCP; ++c) stx1(rs, mo, r, sm + cpy(c));
    LBAR();
}

__device__ __forceinline__ void r_role(const int wave, const int ct, const int xg, const u32 wp, char* __restrict__ ws, char* smem) {
    const int lane = lane_id(), tid = wave * 64 + lane, n = lane & 15, g = lane >> 4;
    u32* errw = (u32*)(ws + OFF_ERR);
    const rsrc_t rs = mk_rsrc(ws);
    const u32x4 srd = mk_srd(ws);
    const u32 lbase = (u32)(uintptr_t)(__attribute__((address_space(3))) char*)smem;
    if (tid < 6) *(u32*)(smem + L_T + 8 + tid * 4) = ~0u;
    asm volatile("s_waitcnt lgkmcnt(0)" ::: "memory");
    int wslot = 0; const int wid = 192 + ct;
    WSTAMP(1);
    TLOAD();
    const u32 cp = (u32)xg ^ (wp >> 1);
    u32 cap = SPIN_CAP;
    STAMPS();
    LOGS();
    COUNT0();
    if (wave == 0) {
        const int kp = ct >= 36 ? 10 : ct >> 2;
        const u32 mb = (u32)((kp >> 2) * 32768 + (kp & 3) * 1024 + (ct & 3) * 256);
        const u32 mo = mb + (u32)(((n & 7) >> 2) * 128 + (2 * g + (n >> 3)) * 16 + (n & 3) * 4);
        const u32 pv = (u32)((g >> 1) * 2048 + (g & 1) * 512 + n * 16), zv = (u32)(g * 256 + (n & 7) * 32 + (n >> 3) * 16), sidx = (n & 2) ? 0xEEEE : 0x4444;
        const u32 ra = lbase + L_U + (u32)(((g & 1) ? 2048 : 256) + (g >> 1) * 512 + (n & 7) * 32 + (n >> 3) * 16), rb = ra - (u32)(g & 1) * 8192;
        const u32 te = slot_v(wp, lane) + copy_off(cp, lane) + SL_P, tm = slot_v(wp ^ 8u, lane) + put_off(wp, 0u, lane), tz = slot_z(wp, lane) + z_off(wp, (u32)ct);
#pragma unroll 1
        for (int s = -1; s + 1 < NSEG; ++s) {
            const u32 so = tab(te, s & 63), sm = tab(tm, s & 63), sz = tab(tz, s & 63);
            u32 bad, pass;
            r_f(bad, pass, sidx, pv, zv, ra, rb, mo, lbase + L_T + 16 + (u32)(lane & 3) * 4, live(srd, s), so, sz, sm, lbase, step_tag(s), unless_neg(cap, s), min(TV(8), 256u) STA);
            if (__builtin_expect(bad, 0)) { if (s < 0) LBAR(); else r_slow(s, rs, so, sz, sm, pv, zv, ra, rb, mo, sidx, lbase, cap, errw); }
            if (pass) COUNT(2);
            touch(srd, wp, s + 1, lbase + 17408u);
            LOG();
        }
    } else if (wave == 4) {
        u32 c0, r0, kr = 3950;
        calib(c0, r0);
        const u32 tc = slot_v(wp ^ 8u, lane) + copy_off(cp, lane) + SL_TS;
        u32 pc = c0, pr = r0, tg = r0 - 0x10000u, t2 = 0;
#pragma unroll 1
        for (int s = -1; s + 1 < NSEG; ++s) {
            u32 t1 = tg, ok = 0;
            if (s) r_t0(c0, r0, t1, t2, ok, tg + TV(6), tg + (t2 & 0xffffu), kr, (u32)(s & 127) + 1u, lbase, live(srd, s), tab(tc, s & 63) STA);
            if (s < 0) { if (!ok) { LBAR(); LBAR(); } }
            else if (__builtin_expect(!ok, 0)) {
                until(tg + TV(6) + TV(7) - 256u);
                r_t1(c0, r0, t1, t2, lane < 8 ? (u32)lane * 128u : OOB, kr, (u32)s + 1u, lbase, srd, tab(tc, s) STA);
            }
            if (__builtin_expect(ok == 2u && s >= 0, 0)) t1 = read_ts(rs, tab(tc, s), s, ok);
            tg = t1;
            if (__builtin_expect(rfl(*(lds_vu32p)(lbase + L_T + 8)) == step_tag(s), 0)) {
                LBAR();
                if (rfl(*(lds_vu32p)(lbase + L_T + 12)) == step_tag(s)) { LBAR(); LBAR(); }
            }
            track_rate(c0, r0, pc, pr, kr);
            LOG();
        }
    } else {
        const int q = wave == 5 ? 3 : wave - 1;
        u32 have = 0;
        u32x32 bn = {};
        auto seen = [&](u32x2 f) {
            u32 a = min(2 * f[0], 2 * f[1] + 1);
            a = min(a, (u32)__builtin_amdgcn_update_dpp(0u, a, 0xb1, 0xf, 0xf, false));
            a = min(a, (u32)__builtin_amdgcn_update_dpp(0u, a, 0x4e, 0xf, 0xf, false));
            a = min(a, (u32)__builtin_amdgcn_update_dpp(0u, a, 0x124, 0xf, 0xf, false));
            have = max(have, rfl(a));
        };
        const u32 fo = (u32)(OFF_FB + (q * 8 + (lane & 7)) * 128);
        auto load_b = [&](int sg, u32x32& bx) {
            u32 spins = 0;
#pragma unroll 1
            for (; have <= (u32)sg && spins < cap; ++spins) {
                CFENCE();
                seen(ldx2(rs, fo));
                if (spins) __builtin_amdgcn_s_sleep(4);
            }
            if (spins >= cap) { atomicOr(errw, 16u); cap = 1; }
            CFENCE();
            const u32 bs = far_off(wp, FAR_BX + (u32)(sg * NRED + ct) * 32768 + (u32)q * 8192);
            pull((u32)lane * 128, bs, srd, 1u, 0u, lbase + 17664u);
            u32x4 t[8];
#pragma unroll
            for (int i = 0; i < 8; ++i) t[i] = ldx4(rs, (u32)(lane * 16 + i * 1024), bs);
#pragma unroll
            for (int i = 0; i < 8; ++i)
#pragma unroll
                for (int j = 0; j < 4; ++j) bx[(i >> 1) * 8 + j * 2 + (i & 1)] = t[i][j];
        };
        const u32 eo = (u32)(q * 32768 + lane * 16), te = slot_v(wp, lane) + copy_off(cp, lane), sidx = n >= 8 ? 0xEEEE : 0x4444;
        const u32 uw = lbase + L_U + (u32)(q * 2048 + (g >> 1) * 256 + n * 16 + (g & 1) * 8);
        const u32 tb = far_v(wp, FAR_BX + (u32)(min(lane + 1, NSEG - 1) * NRED + ct) * 32768 + (u32)q * 8192);
        u32 s = ~0u, why = 0, left = 0;
#pragma unroll 1
        for (;;) {
            if (why & 1u) {
#pragma unroll
                for (int i = 0; i < 4; ++i) {
                    const f32x4 u = smf(ldx4(rs, eo + (u32)i * 1024, tab(te, (int)s - 1)), sub4(bn, i * 8), sub4(bn, i * 8 + 4), (f32x4){0.f, 0.f, 0.f, 0.f}, (int)sidx);
                    *(lds_u32x2p)(uw + (u32)i * 512) = (u32x2){pack2(u[0], u[1]), pack2(u[2], u[3])};
                }
                LBAR();
                LBAR();
            }
            if (s + 1 == NSEG) break;
            if (s == 0) left = SPIN_CAP;
            if ((int)s >= 0) load_b((int)s, bn);
            have = to_sgpr(have); left = to_sgpr(left);
            r_u(bn, s, have, left, why, sidx, eo, uw, lbase + L_T + 16 + (u32)q * 4, (u32)lane * 16, fo, te, tb, live(srd, (int)s), (u32)q * 4u, lbase STA LGA);
            s = to_sgpr(s); have = to_sgpr(have); left = to_sgpr(left); why = to_sgpr(why);
        }
        if (left == 0) atomicOr(errw, 4u);
    }
    COUNT1(wave ? 1 : 2);
    LOGOUT(32 + ct);
}

__device__ __forceinline__ void relay_role(const u32 wp, const int xg, char* __restrict__ ws) {
    const int lane = lane_id();
    const rsrc_t rs = mk_rsrc(ws);
    const u32 tc = slot_v(wp ^ 8u, lane) + copy_off((u32)xg ^ (wp >> 1), lane) + SL_TS;
#pragma unroll 1
    for (int k = 0; k + 1 < NSEG; ++k) {
        u32 t;
#pragma unroll 1
        for (u32 spins = 0;; ++spins) {
            u32 ok;
            t = read_ts(rs, tab(tc, k), k, ok);
            if (ok) break;
            if (spins >= 16u || ldx1(rs, (u32)OFF_ERR) != 0u) return;
        }
        if (k >= 3) stx1(rs, lane < 8 ? (u32)(OFF_RDY + lane * 128) : OOB, (u32)k - 2u);
        for (int q = 0; q < 64 && (int)((u32)__builtin_amdgcn_s_memrealtime() * 24u - t) < 2400; ++q) __builtin_amdgcn_s_sleep(2);
    }
}

__device__ __forceinline__ void lead_role(const u32 wp, const int xg, char* __restrict__ ws, char* smem) {
    const int lane = lane_id();
    u32* errw = (u32*)(ws + OFF_ERR);
    const rsrc_t rs = mk_rsrc(ws);
    const u32x4 srd = mk_srd(ws);
    TLOAD();
    STAMPS();
    const u32 tsw = lane < 4 ? SL_TS + (u32)((lane & 1) * 4096 + (lane >> 1) * 16384 + xg * 128) : OOB;
    const u32 tm = slot_v(wp ^ 8u, lane), tc = tm + copy_off((u32)xg ^ (wp >> 1), lane);
    u32 c0, r0, kr = 3950, ki = 265, bump = 0, hist = 0, dp = 0;
    calib(c0, r0);
    u32 pc = c0, pr = r0, tg = r0, cap = SPIN_CAP;
#pragma unroll 1
    for (int s = 0; s + 1 < NSEG; ++s) {
        u32 sr = tsw + tab(tm, s);
        const u32 mg = bump + (s < EARLY_N ? TV(10) : 0u) + (s < 4 ? TV(3) : 0u);
        const u32 de = (TV(0) * ki >> 8) + mg, dz = TV(1) * ki >> 8, per = max(de, dp) + (TV(2) * ki >> 8) + mg;
        dp = de;
        u32 look = 0;
        DRAIN();
        {
            const u32 so = tab(tc, (s + 63) & 63);
#pragma unroll 1
            for (;; ++look) {
                u64 tl;
                u32 miss;
                l_g(miss, c0, r0, tl, tg + TV(15), look ? 0u : kr, (u32)((lane >> 5) * 32768 + (lane & 31) * 128), lane < 16 ? (u32)(65536 + (lane >> 3) * 2048 + (lane & 7) * 128) : OOB, so, live<1>(srd, s), (u32x4){tg + de, tg + per, de | (dz << 16), (u32)s + 1u}, look || !s ? OOB : sr STA);
                if (!s) break;
                if (look && (int)((u32)tl * 24u + TV(14) - tg) > 0) tg = (u32)tl * 24u + TV(14);
                if (__builtin_expect(!miss, 1)) break;
                if (!look) {
                    sr += 512u;
                    tg += TV(7);
                }
                if (look >= cap) { atomicOr(errw, 4u); cap = 1; break; }
            }
        }
        if (!s) {
            bool dead = false;
            wait_word(rs, (u32)(OFF_FA + lane * 128), true, 1u, dead, errw, 8u, lane);
            XSTAMP(192 + xg, 227);
            tg = (u32)__builtin_amdgcn_s_memrealtime() * 24u + TV(13) - 512u;
        }
        if (!s || look) stx4(rs, sr, (u32x4){tg + de, tg + per, de | (dz << 16), (u32)s + 1u});
        nap(384);
        u32 m;
        tg = look ? read_ts(rs, tab(tc, s) + SL_TS, s, m) : read_ts_late(rs, srd, tab(tc, s) + SL_TS, s, m);
        touch(srd, wp, s + 1, (u32)(uintptr_t)(__attribute__((address_space(3))) char*)smem);
        track_rate(c0, r0, pc, pr, kr);
        hist = ((hist << 1) | (s >= BUMP_S0 && m != 15u ? 1u : 0u)) & BUMP_WIN;
        if (__builtin_popcount(hist) >= BUMP_N && bump < BUMP_MAX * TV(11)) { bump += TV(11); hist = 0; }
        if (pc == c0) ki = to_sgpr((u32)(1048576.f * __builtin_amdgcn_rcpf((float)kr) + 0.5f));
        FLUSH();
    }
}

__device__ __forceinline__ void fill_role(const u32 wp, const int fi, char* __restrict__ ws) {
    const int lane = lane_id();
    const rsrc_t rs = mk_rsrc(ws);
    TLOAD();
    const u32 oz = (u32)(fi * 65536 + lane * 16);
    nap(TV(4) * 64u);
#pragma unroll 1
    for (int s = 5; s < NSEG; ++s) {
        const u32 un = 0x38u << ((~((wp >> 3) ^ ((u32)s >> 5)) & 1u) * 8u), a = oz + slot_off(wp, s);
        const u32 a0 = (un >> (2 * fi)) & 1u ? OOB : a, a1 = (un >> (2 * fi + 1)) & 1u ? OOB : a + 32768u;
#pragma unroll
        for (u32 i = 0; i < 32; ++i) {
            stx4(rs, (i < 16 ? a0 : a1) + ((i >> 3) & 1u) * 16384 + (i & 7) * 1024, (u32x4){SENT, SENT, SENT, SENT});
            if (i == 15) {
                asm volatile("s_waitcnt vmcnt(16)" ::: "memory");
                stx1(rs, lane == 0 ? (u32)(OFF_FA + fi * 128 + 8) : OOB, (u32)s - 1u);
            }
            if (i % FILLG == FILLG - 1) nap(TV(9));
        }
    }
    DRAIN();
    stx1(rs, lane == 0 ? (u32)(OFF_FA + fi * 128 + 8) : OOB, (u32)NSEG + 1u);
}

__device__ __forceinline__ int tile_of(const int xg, const int hl, const int i) {
    const u32 pieces = hl ? 0x85410u : 0x97632u;
    return CFG ? (int)((pieces >> ((i >> 1) * 4)) & 15u) * 4 + (i & 1) * 2 + (xg & 1) : (int)((pieces >> (i * 4)) & 15u) * 4 + xg;
}
__device__ __forceinline__ void warm_role(const u32 wp, const int xg, const int wi, char* __restrict__ ws, char* smem) {
    const int lane = lane_id();
    const rsrc_t rs = mk_rsrc(ws);
    const u32x4 srd = mk_srd(ws);
    const u32 lbase = (u32)(uintptr_t)(__attribute__((address_space(3))) char*)smem;
    TLOAD();
    u32 have = 0, ta = 0;
    int next = 1;
    const u32 tc = slot_v(wp ^ 8u, lane) + copy_off((u32)xg ^ (wp >> 1), lane) + SL_TS;
    const u32 wq = wi < 0 ? wp ^ 1u : wp, ls = wi < 0 ? H_ * 8192u : NRED * 32768u;
    const u32 l0 = wi < 0 ? (wi & 1 ? NEAR_KX : NEAR_QX) + (u32)(xg * 8 + (lane & 7)) * 8192 : FAR_BX + (u32)(min(lane, 9) * 4 + xg) * 32768 + (u32)wi * 8192;
    auto look = [&] {
        const u32x2 f = ldx2(rs, wi < 0 ? (u32)(OFF_FA + (xg * 8 + (lane & 7)) * 128) : (u32)(OFF_FB + (wi * 8 + (lane & 7)) * 128));
        u32 a = min(2 * f[0], 2 * f[1] + 1);
#pragma unroll
        for (int q = 1; q < 8; q <<= 1) a = min(a, (u32)__shfl_xor((int)a, q));
        have = rfl(a);
    };
#pragma unroll 1
    for (u32 spins = 0; next < WFIRST && spins < 0x4000u; ++spins) {
        CFENCE();
        if (have <= (u32)next) { __builtin_amdgcn_s_sleep(4); look(); }
        else { pull((u32)lane * 128, far_v(wq, l0 + (u32)next * ls), srd, wi < 0 ? 8u : 10u, 64u, lbase + 69632u); ++next; }
    }
#pragma unroll 1
    for (int s = 0; s + 2 < NSEG; ++s) {
        if (s) until(ta + 1000u);
        u32 tm;
        ta = read_ts_late(rs, srd, tab(tc, s), s, tm);
        if (have <= (u32)s + WAHEAD) look();
        if (next < s + 2) next = s + 2;
#pragma unroll 1
        for (; next <= s + WAHEAD && next < NSEG && have > (u32)next; ++next) {
            const u32 tb = far_v(wq, l0 + (u32)next * ls);
            until(ta + TV(5) - 1024u);
            pull((u32)lane * 128, tb, srd, wi < 0 ? 8u : 10u, min(TV(12), 256u), lbase + 69632u);
        }
    }
    DRAIN();
}

__device__ __forceinline__ void topk_role(const u64 tag, char* __restrict__ ws) {
    const int lane = lane_id();
    const rsrc_t rs = mk_rsrc(ws);
    wait_init(rs, tag, 2);
    u32 v[32];
#pragma unroll
    for (int i = 0; i < 8; ++i) {
        const u32x4 t = ldx4(rs, (u32)lane * 16, (u32)(OFF_R + i * 1024));
#pragma unroll
        for (int j = 0; j < 4; ++j) v[i * 4 + j] = ((t[j] ^ ((u32)((int)t[j] >> 31) | 0x80000000u)) & 0xffff0000u) | (u32)(2047 - i * 256 - j - lane * 4);
    }
    u32 top = ~0u, res = 0;
#pragma unroll 1
    for (int c = 0; c < TOPC; ++c) {
        u32 d = ~0u;
#pragma unroll
        for (int i = 0; i < 32; ++i) d = min(d, top - v[i]);
        d = min(d, (u32)__builtin_amdgcn_update_dpp(0u, d, 0xb1, 0xf, 0xf, false));
        d = min(d, (u32)__builtin_amdgcn_update_dpp(0u, d, 0x4e, 0xf, 0xf, false));
        d = min(d, (u32)__builtin_amdgcn_update_dpp(0u, d, 0x124, 0xf, 0xf, false));
        d = min(d, (u32)__builtin_amdgcn_update_dpp(0u, d, 0x128, 0xf, 0xf, false));
        const u32 dm = min(min((u32)__builtin_amdgcn_readlane(d, 0), (u32)__builtin_amdgcn_readlane(d, 16)), min((u32)__builtin_amdgcn_readlane(d, 32), (u32)__builtin_amdgcn_readlane(d, 48)));
        top -= dm;
        if (lane == c) res = 2048u - (top & 2047u);
        top -= 1u;
    }
    stx1(rs, lane < TOPC ? (u32)(OFF_IDX + lane * 4) : OOB, res);
    XSTAMP(192, 224);
}

#define MOEV 0
#define MOE_LD(p) __builtin_nontemporal_load(p)
#define MOE_NT " nt"
template <int NK>
__device__ __forceinline__ void moe_ld(const u16* __restrict__ wp, const int ldw, const int nvalid, u32 (&r)[NK][8]) {
#pragma unroll
    for (int k = 0; k < NK; ++k)
#pragma unroll
        for (int i = 0; i < 8; ++i) r[k][i] = k < nvalid ? MOE_LD((const u32*)(wp + (size_t)(k * 32 + i) * ldw)) : 0u;
}
template <int NK>
__device__ __forceinline__ void moe_mma(const u32 (&r)[NK][8], const u32x4 (&af)[NK], f32x4& ce, f32x4& co) {
#pragma unroll
    for (int k = 0; k < NK; ++k) {
        u32x4 be, bo;
#pragma unroll
        for (int j = 0; j < 4; ++j) {
            be[j] = __builtin_amdgcn_perm(r[k][2 * j + 1], r[k][2 * j], 0x05040100u);
            bo[j] = __builtin_amdgcn_perm(r[k][2 * j + 1], r[k][2 * j], 0x07060302u);
        }
        ce = mfma16(af[k], be, ce);
        co = mfma16(af[k], bo, co);
    }
}
constexpr int NGU = FF_ / 32, NDN = D_ / 32;
__device__ __forceinline__ void moe_part(const int w, const int tid, const u16* __restrict__ wg, const u16* __restrict__ wu,
                                         const u16* __restrict__ wd, char* __restrict__ ws, char* smem) {
    const int wave = __builtin_amdgcn_readfirstlane(tid >> 6), lane = tid & 63;
    const int n = lane & 15, g = lane >> 4;
    u32* errw = (u32*)(ws + OFF_ERR);
    const rsrc_t rs = mk_rsrc(ws);
    bool dead = false;
    f32x4 (*red)[2][64] = (f32x4(*)[2][64])smem;
    const f32x4 z = {0.f, 0.f, 0.f, 0.f};
    wait_word(rs, (u32)(OFF_IDX + (n & 7) * 4), true, 1u, dead, errw, 16u, lane);
    const u32 row = (ldx1(rs, (u32)(OFF_IDX + (n & 7) * 4)) - 1u) & (S_ - 1);
    u32 r1[5][8], r2[7][8];
    const int kq = wave >> 1, dg = w - (64 - NDN), k0 = wave * 7, nk = wave == 7 ? 5 : 7;
    if (w < NGU) moe_ld<5>(((wave & 1) ? wu : wg) + (size_t)(kq * 160 + g * 8) * FF_ + w * 32 + n * 2, FF_, 5, r1);
    if (dg >= 0) moe_ld<7>(wd + (size_t)(k0 * 32 + g * 8) * D_ + dg * 32 + n * 2, D_, nk, r2);
    if (w < NGU) {
        u32x4 af[5];
#pragma unroll
        for (int k = 0; k < 5; ++k) af[k] = ldx4(rs, (u32)OFF_XN + (row * D_ + (u32)((kq * 5 + k) * 32 + g * 8)) * 2);
        f32x4 ce = z, co = z;
        moe_mma<5>(r1, af, ce, co);
        red[wave][0][lane] = ce; red[wave][1][lane] = co;
        __syncthreads();
        if (wave == 0 && g < 2) {
            f32x4 t[4];
#pragma unroll
            for (int q = 0; q < 4; ++q) t[q] = (red[q >> 1][q & 1][lane] + red[2 + (q >> 1)][q & 1][lane]) + (red[4 + (q >> 1)][q & 1][lane] + red[6 + (q >> 1)][q & 1][lane]);
#pragma unroll
            for (int i = 0; i < 4; ++i) {
                float res[2];
#pragma unroll
                for (int q = 0; q < 2; ++q) {
                    float gg = bfr(t[q][i]), uu = bfr(t[2 + q][i]);
                    float sg = bfr(gg / (1.0f + __expf(-gg)));
                    res[q] = sg * uu;
                }
                stx1(rs, (u32)(OFF_HB + ((size_t)(4 * g + i) * FF_ + w * 32 + n * 2) * 2), pack2(res[0], res[1]));
            }
        }
        if (wave == 0) {
            DRAIN();
            if (lane == 0) __hip_atomic_fetch_add((u32*)(ws + OFF_CNT), 1u, __ATOMIC_RELAXED, __HIP_MEMORY_SCOPE_AGENT);
        }
        __syncthreads();
    }
    if (dg >= 0) {
        const float rv = __uint_as_float(ldx1(rs, (u32)OFF_R + row * 4));
        wait_word(rs, (u32)OFF_CNT, true, (u32)NGU, dead, errw, 16u, lane);
        u32x4 af[7];
#pragma unroll
        for (int k = 0; k < 7; ++k) af[k] = ldx4(rs, (u32)(OFF_HB + ((size_t)(n & 7) * FF_ + (k0 + (k < nk ? k : 0)) * 32 + g * 8) * 2));
        f32x4 ye = z, yo = z;
        moe_mma<7>(r2, af, ye, yo);
        red[wave][0][lane] = ye; red[wave][1][lane] = yo;
        __syncthreads();
        if (wave == 0) {
            f32x4 t[2];
#pragma unroll
            for (int q = 0; q < 2; ++q)
                t[q] = ((red[0][q][lane] + red[1][q][lane]) + (red[2][q][lane] + red[3][q][lane])) + ((red[4][q][lane] + red[5][q][lane]) + (red[6][q][lane] + red[7][q][lane]));
            const float rs1 = bfr(1.0f / (1.0f + __expf(-rv)));
#pragma unroll
            for (int i = 0; i < 4; ++i) {
                const float rsg = __shfl(rs1, (4 * g + i) & 7);
                stx1(rs, g < 2 ? (u32)(OFF_UPD + ((size_t)(4 * g + i) * D_ + dg * 32 + n * 2) * 2) : OOB, pack2(bfr(t[0][i]) * rsg, bfr(t[1][i]) * rsg));
            }
            DRAIN();
            if (lane == 0) __hip_atomic_fetch_add((u32*)(ws + OFF_CNT + 256), 1u, __ATOMIC_RELAXED, __HIP_MEMORY_SCOPE_AGENT);
        }
        __syncthreads();
    }
}

__device__ __forceinline__ void moe_ahead(const int w, const int t, const u16* __restrict__ wg, const u16* __restrict__ wu, const u16* __restrict__ wd, u32 lds) {
    const int lane = lane_id(), t2 = w < NGU ? t - 20 : t;
    if (t2 >= (w >= 64 - NDN ? 27 : 0)) return;
    const u16* p = t2 < 0 ? (t < 10 ? wg : wu - 640 * FF_) + (size_t)(t * 64 + lane) * FF_ + w * 32 : wd + (size_t)(t2 * 64 + lane) * D_ + (w - (64 - NDN)) * 32;
    u32 sv;
    asm volatile("s_mov_b32 %0, m0\n\ts_mov_b32 m0, %2\n\ts_nop 0\n\tglobal_load_lds_dword %1, off" MOE_NT "\n\ts_mov_b32 m0, %0" : "=&s"(sv) : "v"(p), "s"(lds) : "memory");
}

#define LOOKST 18
constexpr int NBIG = (NSEG - 1) * 10, NTASK = NBIG + 80;

struct OutCtx { rsrc_t rs; const u16* x; u16* out; u32* errw; u32 wp; int wid; char* ws; };
template <int OT, int NT>
__device__ __forceinline__ void out_task(const OutCtx& cx, const int sg, const int c0, const int n0, const int tid, const int (&sel_idx)[TOPC], bool& adone, u32& rdy,
                                         bool& dead, char* redp, u32* nextp, u32* tctr) {
    const int wave = __builtin_amdgcn_readfirstlane(tid >> 6), lane = tid & 63, n = lane & 15, g = lane >> 4;
    const rsrc_t rs = cx.rs;
    if (!adone && rdy <= (u32)sg) {
        wait_word(rs, (u32)(OFF_FB + (wave * 4 + (lane & 3)) * 128 + (sg & 1) * 4), true, (u32)(sg >> 1) + 1, dead, cx.errw, 32u, lane);
        u32x2 f = ldx2(rs, (u32)(OFF_FB + (wave * 4 + (lane & 3)) * 128));
        adone = __all(f[0] >= NSEG / 2 && f[1] >= NSEG / 2);
    }
    u32x4 af[OT][5], bq[NT][5];
    const u32 bo = amap((u32)(wave * 4096 + lane * 16));
#pragma unroll
    for (int c = 0; c < OT; ++c) {
        const u32 bb = far_off(cx.wp, FAR_BX + (u32)(sg * 40 + c0 + c) * 32768);
#pragma unroll
        for (int hh = 0; hh < 4; ++hh) af[c][hh] = ldx4(rs, bo + hh * 1024, bb);
    }
    const int ti = (tid >> 6) & (OT * NT - 1);
    const int token = sg * SEGL + (n0 + (ti & (NT - 1))) * 16 + n;
    const int col = (c0 + ti / NT) * 16 + 4 * g;
    int sel = -1;
#pragma unroll
    for (int c = 0; c < TOPC; ++c) if (sel_idx[c] == token) sel = c;
    const u32x2 xv = *(const u32x2*)(cx.x + (size_t)token * D_ + col);
    const u32x2 nv = ldx2(rs, (u32)(OFF_XN + ((size_t)token * D_ + col) * 2));
    if (__any(sel >= 0)) wait_word(rs, (u32)(OFF_CNT + 256), true, (u32)NDN, dead, cx.errw, 256u, lane);
    const u32x2 uv = ldx2(rs, sel >= 0 ? (u32)(OFF_UPD + ((size_t)sel * D_ + col) * 2) : OOB);
    __builtin_amdgcn_sched_barrier(0);
    const bool late = sg + 4 >= NSEG;
    if (late) wait_flags(rs, (u32)((OT == 1 ? OFF_FZ + (wave * 2 + n0) * 128 : OFF_FP + wave * 128) + (lane & 1) * 16), true, 1u, dead, cx.errw, 64u, lane);
    else if (rdy <= (u32)sg) {
#pragma unroll 1
        for (u32 spins = 0; !dead; ++spins) {
            CFENCE();
            rdy = __builtin_amdgcn_readfirstlane(ldx1(rs, (u32)(OFF_RDY + (cx.wid & 7) * 128)));
            if (rdy > (u32)sg) break;
            __builtin_amdgcn_s_sleep(8);
            if (spins > SPIN_CAP) { if (lane == 0) atomicOr(cx.errw, 64u); dead = true; }
            if ((spins & 63) == 63 && ldx1(rs, (u32)OFF_ERR) != 0u) dead = true;
        }
        CFENCE();
    }
#pragma unroll
    for (int c = 0; c < OT; ++c) af[c][4] = ldx4(rs, (u32)(wave * 64 + lane) * 16, slot_off(cx.wp, sg) + z_off(cx.wp, (u32)(c0 + c)));
#pragma unroll
    for (int nt = 0; nt < NT; ++nt) {
#pragma unroll
        for (int hh = 0; hh < 4; ++hh) bq[nt][hh] = ldx4(rs, (u32)lane * 16, (u32)OFF_PX + (u32)((sg * H_ + wave * 4 + hh) * 2 + n0 + nt) * 1024);
        bq[nt][4] = ldx4(rs, (u32)lane * 16, (u32)OFF_PXM + (u32)((sg * 8 + wave) * 2 + n0 + nt) * 1024);
    }
    CFENCE();
    u32 next = 0;
    if (tid == 0) next = !late ? __hip_atomic_fetch_add(tctr, 1u, __ATOMIC_RELAXED, __HIP_MEMORY_SCOPE_AGENT)
                                 : __hip_atomic_load(tctr, __ATOMIC_RELAXED, __HIP_MEMORY_SCOPE_AGENT);
    f32x4 (*red)[OT * NT][64] = (f32x4(*)[OT * NT][64])redp;
#pragma unroll
    for (int c = 0; c < OT; ++c)
#pragma unroll
        for (int nt = 0; nt < NT; ++nt) {
            f32x4 acc = {0.f, 0.f, 0.f, 0.f};
#pragma unroll
            for (int k = 0; k < 5; ++k) acc = mfma16(af[c][k], bq[nt][k], acc);
            red[wave][c * NT + nt][lane] = acc;
        }
    if (tid == 0) {
        if (late && next < (u32)NTASK) next = __hip_atomic_fetch_add(tctr, 1u, __ATOMIC_RELAXED, __HIP_MEMORY_SCOPE_AGENT);
        *nextp = next;
    }
    LBAR();
    if (tid < OT * NT * 64) {
        f32x4 sum = red[0][ti][lane];
#pragma unroll
        for (int wv = 1; wv < 8; ++wv) sum += red[wv][ti][lane];
        float res[4];
#pragma unroll
        for (int q = 0; q < 4; ++q) {
            float xx = bf2f((u16)((xv[q >> 1] >> ((q & 1) * 16)) & 0xffff));
            float nn = bf2f((u16)((nv[q >> 1] >> ((q & 1) * 16)) & 0xffff));
            float bo2 = nn;
            if (sel >= 0) bo2 = bfr(nn + bf2f((u16)((uv[q >> 1] >> ((q & 1) * 16)) & 0xffff)));
            float gg = bfr(bo2 / (1.0f + __expf(-bo2)));
            float sv = bfr(sum[q]);
            res[q] = bfr(xx + bfr(sv * gg));
        }
        *(u32x2*)(cx.out + (size_t)token * D_ + col) = (u32x2){pack2(res[0], res[1]), pack2(res[2], res[3])};
    }
}

constexpr u32 L_VS = 40960;
__device__ __forceinline__ u32 xbuf(int b) { return b == 0 ? 65536u : (b == 1 ? 106496u : 0u); }
__device__ __forceinline__ void worker_role(const int wave, const int wid, const u64 tag, const u32 wp, const u16* __restrict__ x, const u32x32 (&W)[5],
                                            const u16* __restrict__ wg, const u16* __restrict__ wu,
                                            const u16* __restrict__ wd, char* __restrict__ ws, u16* __restrict__ out, char* smem) {
    const int lane = lane_id(), tid = wave * 64 + lane;
    const int n = lane & 15, g = lane >> 4;
    const int par = (wid >> 2) & 1, kind = (wid >> 3) & 1, h = (wid & 3) * 8 + (wid >> 4), type = kind * 2 + par;
    u32* errw = (u32*)(ws + OFF_ERR);
    const rsrc_t rs = mk_rsrc(ws);
    bool dead = false;
    const bool isqk = type < 2;
    int wslot = 1;
    u32* sh = (u32*)(smem + L_MISC);

    {
        const u32 lbase = (u32)(uintptr_t)(__attribute__((address_space(3))) char*)smem;
        const u32x4 srd = mk_srd(ws);
        const u32 vl = lbase + (u32)lane * 16, xp = (u32)(((wave >> 1) & 1) * 20480 + (wave < 4 ? 4096 + (wave & 1) * 8192 : (wave & 1) * 2048)), xv = xp + (u32)lane * 16, ca = lbase + L_MISC + 64;
        const u32 fo = (u32)(OFF_FA + kind * 4096 + h * 128 + par * 4);
        auto xsrc = [&](int it) { return it < NSEG / 2 ? (u32)OFF_XNP + (u32)(it * 2 + par) * 40960u : (u32)OFF_XNP; };
        auto now = [&](u32 v) {
            DRAIN();
            if (lane_id() == 0 && __hip_atomic_fetch_add((u32*)(smem + L_MISC + 68), 1u, __ATOMIC_RELAXED, __HIP_MEMORY_SCOPE_WORKGROUP) == v * 4 - 1) { stx1(rs, fo, v); XSTAMP(wid, 224 + v); }
        };
        WSTAMP(4);
        STAMPS();
        u32 b0 = xbuf(0), b1 = xbuf(1), b2 = xbuf(2);
        auto rot = [&] { const u32 t = b0; b0 = b1; b1 = b2; b2 = t; };
        if (wave < 4) {
            const u32 oq = (u32)(wave * 64 + lane) * 16;
#pragma unroll 1
            for (int it = 0; it < NSEG / 2; ++it) {
                if (isqk) {
                    if ((it & ~4) == 2) wait_init(rs, tag, 1 + (it >> 2));
                    const u32 ob = far_off(wp ^ 1u, NEAR_QX + (u32)((it * 2 + par) * H_ + h) * 8192);
                    wx0(W, vl + b0, oq, srd, xv, xsrc(it + 2), lbase + b2 + xp, ob, ob + 4096 STA);
                } else wx1(W, vl + b0, lbase + L_VS + (u32)(it & 1) * 8192 + oq STA);
                rot();
                CFLUSH();
                if (it < 3 || (it & 7) == 7) WSTAMP(0x10 + it);
            }
            DRAIN();
            LBAR();
        } else if (isqk) {
            const u32 oq = (u32)(tid & 255) * 16;
            LBAR();
#pragma unroll 1
            for (int it = 0; it < NSEG / 2; ++it) {
                const int sg = it * 2 + par;
                if ((it & ~4) == 2) sh[18] = wait_init(rs, tag, 1 + (it >> 2));
                const u32 ob = far_off(wp ^ 1u, NEAR_KX + (u32)(sg * H_ + h) * 8192);
                wk(W, vl + b0, oq, srd, ob, ob + 4096, 0.08838834764831845f * 1.4426950408889634f, xv, xsrc(it + 2), lbase + b2 + xp,
                   ca, (u32)it * 4 + 3, (u32)it, fo STA);
                rot();
                CFLUSH();
                if (it < NOWV) now((u32)it + 1);
                if (MOEV && it >= 6 && (wave & 2) == 0) moe_ahead(h * 2 + par, (it - 6) * 2 + (wave & 1), wg, wu, wd, lbase + L_MISC + 256);
            }
        } else {
            const u32 ob = amap((u32)(h * 1024 + lane * 16)), yp = 4096u + (u32)(wave & 1) * 6144u;
            LBAR();
#pragma unroll
            for (int i = 0; i < 10; ++i) {
                const u32 o = i < 2 ? (u32)i * 1024 : yp + (u32)(i - 2) * 1024;
                dma16_sc1(ws + xsrc(2) + xv + o, __builtin_amdgcn_readfirstlane(lbase + b2 + xp + o));
            }
            DRAIN();
            LBAR();
#pragma unroll 1
            for (int it = 1; it <= NSEG / 2; ++it) {
                rot();
                if ((it & ~4) == 2) sh[18] = wait_init(rs, tag, 1 + (it >> 2));
                wb(W, vl + L_VS + (u32)(~it & 1) * 8192, ob, srd, FAR_BX + (u32)((it * 2 - 2 + par) * NRED + (wave & 3) * 2) * 32768, wp, (u32)it,
                   xv, xsrc(it + 2), lbase + b2 + xp, xsrc(it + 2) + yp, lbase + b2 + xp + yp, ca, (u32)it * 4 - 1, (u32)it - 1, fo STA);
                CFLUSH();
                if (it <= NOWV) { now((u32)it); LBAR(); }
            }
        }
        DRAIN();
        LBAR();
        stx1(rs, wave == 0 && lane_id() == 0 ? fo : OOB, NSEG / 2);
    }
    WSTAMP(2);

    {
    const int lane = lane_id(), tid = wave * 64 + lane;
    if (isqk) moe_part(h * 2 + par, tid, wg, wu, wd, ws, smem);
    WSTAMP(5);
    u32* nextp = (u32*)(smem + L_MISC + 96);
    u32* tctr = (u32*)(ws + OFF_CNT + 512);
    if (tid == 0) nextp[0] = __hip_atomic_fetch_add(tctr, 1u, __ATOMIC_RELAXED, __HIP_MEMORY_SCOPE_AGENT);
    wait_flags(rs, (u32)OFF_IDX + (lane & 1) * 16, true, 1u, dead, errw, 256u, lane);
    __syncthreads();
    int sel_idx[TOPC];
    {
        u32x4 i0 = ldx4(rs, (u32)OFF_IDX), i1 = ldx4(rs, (u32)OFF_IDX + 16);
#pragma unroll
        for (int c = 0; c < 4; ++c) { sel_idx[c] = (int)i0[c] - 1; sel_idx[4 + c] = (int)i1[c] - 1; }
    }
    u32 order;
    {
        bool hit = false;
#pragma unroll
        for (int c = 0; c < TOPC; ++c) hit |= (sel_idx[c] >> 5) == lane;
        const u64 m = __ballot(hit && lane < 40), below = ((u64)1 << lane) - 1;
        const int pos = lane >= 40 ? lane : ((m >> lane) & 1 ? 40 - __popcll(m) + __popcll(m & below) : lane - __popcll(m & below));
        order = (u32)__builtin_amdgcn_ds_permute(pos * 4, lane);
    }
    WSTAMP(3);
    const OutCtx cx = {rs, x, out, errw, to_sgpr(sh[18]), wid, ws};
    bool adone = false;
    u32 rdy = 0;
#pragma unroll 1
    for (int tc = 0;; ++tc) {
        const int task = __builtin_amdgcn_readfirstlane(nextp[tc & 1]);
        if (task >= NTASK) break;
        char* redp = smem + (tc & 1) * 65536;
        if (task < NBIG) {
            WSTAMP(0x100 + task / 10);
            out_task<4, 2>(cx, (int)tab(order, task / 10), (task % 10) * 4, 0, tid, sel_idx, adone, rdy, dead, redp, nextp + ((tc + 1) & 1), tctr);
        } else {
            WSTAMP(0x100 + NSEG - 1);
            out_task<1, 1>(cx, NSEG - 1, (task - NBIG) >> 1, (task - NBIG) & 1, tid, sel_idx, adone, rdy, dead, redp, nextp + ((tc + 1) & 1), tctr);
        }
        WSTAMP(0x300);
        if (task == NTASK - 1) {
            wait_word(rs, (u32)(OFF_CNT + 768), true, 8u, dead, errw, 64u, lane);
            if (tid < 216) stx2(rs, tid < 192 ? (u32)(OFF_INI + tid * 8) : (u32)(OFF_SUM + (tid - 192) * 128), (u32x2){~(u32)tag, ~cal_tag(tag, ~(u32)tag)});
        }
    }
    }
}

constexpr int FUSED_LDS = L_MISC + 1024;
#define ORD_A 0x143210
#define ORD_C 0x101234
#define KERNEL_SYM "fused_kernel"
#define CODE_KB 176
extern "C" __global__ __launch_bounds__(512, 2) void fused_kernel(const u16* __restrict__ x, const u16* __restrict__ mem0, const u16* __restrict__ w_r,
                                                        const u16* __restrict__ Wq, const u16* __restrict__ Wk,
                                                        const u16* __restrict__ Wv, const u16* __restrict__ Wo,
                                                        const u16* __restrict__ wg, const u16* __restrict__ wu,
                                                        const u16* __restrict__ wd, char* __restrict__ ws, u16* __restrict__ out) {
    extern __shared__ __attribute__((aligned(16))) char smem[];
    const int wave = __builtin_amdgcn_readfirstlane(threadIdx.x >> 6);
    u64 gen = 0, tag = 0, old = 0;
    u32 xcc, se;
    asm volatile("s_getreg_b32 %0, hwreg(HW_REG_XCC_ID, 0, 3)" : "=s"(xcc));
    asm volatile("s_getreg_b32 %0, hwreg(HW_REG_HW_ID, 13, 2)" : "=s"(se));
    u32 bid = blockIdx.x;
    asm volatile("" : "+s"(bid));
    const bool first = (int)bid >= 0;
    u32 bq = first ? (bid & ~31u) + se * 8u + xcc : bid & 255u;
    asm volatile("" : "+s"(bq));
    const int b = (int)bq, k = b >> 3, xg = b & 3;
    if (threadIdx.x >= 448 && threadIdx.x < 453) {
        const int lane = threadIdx.x & 63;
        const u32 pg = (bid >> 3 & 31u) + 32u * (u32)lane;
        const char* tp = lane < 4 ? ws + min(pg << 21, (u32)WS_TOTAL - 4u) : (const char*)out + (size_t)(bid >> 3 & 3u) * 655360;
        u32 sv;
        asm volatile("s_mov_b32 %0, m0\n\ts_mov_b32 m0, %2\n\ts_nop 0\n\tglobal_load_lds_dword %1, off\n\ts_mov_b32 m0, %0"
                     : "=&s"(sv) : "v"(tp), "s"((u32)(uintptr_t)(__attribute__((address_space(3))) char*)smem + L_MISC + 256u) : "memory");
    }
    if (wave == 6 && (bid >> 3 & 31u) < CODE_KB / 8) {
        u32 sv;
        asm volatile("s_getpc_b64 s[98:99]\nLc%=:\n\ts_sub_u32 s98, s98, Lc%=-" KERNEL_SYM "\n\ts_subb_u32 s99, s99, 0\n\t"
                     "s_mov_b32 %0, m0\n\ts_mov_b32 m0, %2\n\ts_nop 0\n\tglobal_load_lds_dword %1, s[98:99]\n\ts_mov_b32 m0, %0"
                     : "=&s"(sv) : "v"((bid >> 3 & 31u) * 8192u + (threadIdx.x & 63u) * 128u),
                       "s"((u32)(uintptr_t)(__attribute__((address_space(3))) char*)smem + L_MISC + 768u) : "s98", "s99", "scc", "memory");
    }
    u64* claim = (u64*)(ws + OFF_CLAIM);
    u32* sh = (u32*)(smem + L_MISC);
    auto try_claim = [&] {
        if (wave == 0 && first) {
            asm volatile("s_load_dwordx2 %0, %1, 0x0\n\ts_waitcnt lgkmcnt(0)" : "=s"(gen) : "s"(ws + OFF_GEN) : "memory");
            tag = (gen + 0x9E3779B97F4A7C15ull) * 0xBF58476D1CE4E5B9ull;
            tag ^= tag >> 31;
            tag *= 0x94D049BB133111EBull;
            tag ^= tag >> 29;
            if (lane_id() == 0) {
                old = __hip_atomic_exchange(claim + b * 8, tag, __ATOMIC_RELAXED, __HIP_MEMORY_SCOPE_AGENT);
                sh[1] = (u32)tag; sh[2] = (u32)(tag >> 32); sh[3] = (u32)gen; sh[4] = (u32)(gen >> 32);
            }
        }
    };
    auto settle = [&] {
        const int tid = wave * 64 + lane_id();
        if (tid == 0 && first) {
            u32 vb = (u32)b;
#pragma unroll 1
            for (int i = 0; i < 256 && old == tag; ++i) {
                vb = (vb + 1) & 255u;
                old = __hip_atomic_exchange(claim + vb * 8, tag, __ATOMIC_RELAXED, __HIP_MEMORY_SCOPE_AGENT);
            }
            sh[0] = vb;
        }
        LBAR();
        const u32 bc = __builtin_amdgcn_readfirstlane(sh[0]);
        asm volatile("s_cmp_eq_u32 %0, %1\n\ts_cbranch_scc1 Lgo%=\n\t"
                     "s_or_b32 s2, %0, 0x80000000\n\ts_mov_b64 s[0:1], %2\n\tv_mov_b32 v0, %3\n\ts_waitcnt vmcnt(0) lgkmcnt(0)\n\t"
                     "s_getpc_b64 s[4:5]\nLpc%=:\n\ts_sub_u32 s4, s4, Lpc%=-" KERNEL_SYM "\n\ts_subb_u32 s5, s5, 0\n\ts_setpc_b64 s[4:5]\nLgo%=:"
                     :: "s"(bc), "s"(b), "s"(__builtin_amdgcn_kernarg_segment_ptr()), "v"(tid) : "s2", "scc", "memory");
        tag = (u64)(u32)__builtin_amdgcn_readfirstlane(sh[1]) | ((u64)(u32)__builtin_amdgcn_readfirstlane(sh[2]) << 32);
    };
    if (b & 4) {
        const int wid = k * 4 + xg, h = (wid & 3) * 8 + (wid >> 4), par = (wid >> 2) & 1;
        const bool isqk = ((wid >> 3) & 1) == 0;
        const int lane = lane_id();
        u32x32 W[5];
        u32 wp = 0;
        u32x2 f0;
        u64 tg2 = 0;
        bool got = false;
        auto xtile = [&](int t) {
#pragma unroll
            for (int i = 0; i < 5; ++i)
                dma16_sc1(ws + OFF_XNP + (size_t)(par + 2 * t) * 40960 + (wave * 5 + i) * 1024 + lane * 16,
                          __builtin_amdgcn_readfirstlane((u32)(uintptr_t)(__attribute__((address_space(3))) char*)smem + xbuf(t) + (u32)(wave * 5 + i) * 1024));
        };
        load_weights<ORD_A>([&](int st) {
            if (st == 0) try_claim();
            if (st == LOOKST) {
                asm volatile("global_load_dwordx2 %0, %1, off sc1" : "=&v"(f0) : "v"(ws + OFF_SUM + xcc * 128u) : "memory");
                tg2 = (u64)(u32)__builtin_amdgcn_readfirstlane(sh[1]) | ((u64)(u32)__builtin_amdgcn_readfirstlane(sh[2]) << 32);
            }
            if (st == 3) {
                asm volatile("" : "+v"(f0));
                got = __all(f0[1] == cal_tag(tg2, f0[0]));
                if (got) { xtile(0); xtile(1); }
                wp = to_sgpr(f0[0]);
            }
        }, (isqk ? Wq : Wv) + h * DH_, isqk ? Wk + h * DH_ : Wo + (size_t)h * DH_ * D_, !isqk, smem, wave, lane,
           wave >> 2, 2 * (wave & 3), wave >> 2, 2 * (wave & 3) + 1, W);
        settle();
        const rsrc_t rs = mk_rsrc(ws);
        if (!got) { wp = wait_init(rs, tag, 0); xtile(0); xtile(1); }
        sh[16] = sh[17] = 0u;
        DRAIN();
        LBAR();
        PIN_W(W);
        worker_role(wave, wid, tag, wp, x, W, wg, wu, wd, ws, out, smem);
        __builtin_amdgcn_endpgm();
    }
    const int hl = ((k >> 1) ^ (xg >> 1)) & 1, j = (k >> 2) * 2 + (k & 1);
    if (CFG ? xg < 2 : j < 8) {
        const int h = CFG ? xg * 16 + (j >> 3) * 8 + hl * 4 + ((j >> 1) & 3) : xg * 8 + hl * 4 + (j >> 1);
        u32x32 W[5];
        auto hook = [&](int st) {
            if (st == 0) try_claim();
        };
        if ((j & 1) == 0) {
            load_weights<ORD_C>(hook, Wq + h * DH_, Wk + h * DH_, false, smem, wave, lane_id(), wave < 4, wave & 3, wave < 4, 4 + (wave & 3), W);
            settle();
            PIN_W(W);
            s_role(wave, h, xg, tag, mem0, W, ws, smem);
            __builtin_amdgcn_endpgm();
        }
        if (wave < 4) { load_weights<ORD_C>(hook, Wv + h * DH_, Wo + (size_t)h * DH_ * D_, true, smem, wave, lane_id(), 0, wave, 0, 4 + wave, W); PIN_W(W); }
        else { load_weights<ORD_C | (1 << 21)>(hook, Wv + h * DH_, Wo + (size_t)h * DH_ * D_, true, smem, wave, lane_id(), 1, 2 * (wave & 3), 1, 2 * (wave & 3) + 1, W); PIN_W(W); }
        settle();
        PIN_W(W);
        v_role(wave, h, xg, tag, mem0, W, ws, smem);
        __builtin_amdgcn_endpgm();
    }
    constexpr int NR = CFG ? 10 : 5, NS = CFG ? 6 : 3, NP = CFG ? 4 : 2;
    const int jr = CFG ? j : j - 8, hi = (CFG ? xg - 2 : xg) * 2 + hl;
    try_claim();
    init_role([&] { settle(); return tag; }, wave, jr < NR ? hi * NR + jr : jr == NR ? 40 + hi : 48 + hi * NP + jr - (NR + NS - NP), xcc, ((se ^ xcc) >> 1) & 1u, x, w_r, Wq, Wk, Wv, Wo, ws, smem);
    gen = (u64)sh[3] | ((u64)sh[4] << 32);
    tag = (u64)(u32)__builtin_amdgcn_readfirstlane(sh[1]) | ((u64)(u32)__builtin_amdgcn_readfirstlane(sh[2]) << 32);
    const int wi = hl * NS + jr - NR;
    const bool aux = wi == 2 * NS - 1;
    if (jr < NR) { if (wave < 6) r_role(wave, tile_of(xg, hl, jr), xg, wait_init(mk_rsrc(ws), tag, 1), ws, smem); }
    else if (!aux) {
        if (wave >= 4) { if (wi == 0 && xg == 0 && wave == 7) topk_role(tag, ws); return; }
        const u32 wp = wait_init(mk_rsrc(ws), tag, 1);
        LBAR();
        if (wave == 0 && wi == 0 && lane_id() == 0) __hip_atomic_fetch_add((u32*)(ws + OFF_CNT + 768), 1u, __ATOMIC_RELAXED, __HIP_MEMORY_SCOPE_AGENT);
        if (wave == 2 && wi) fill_role(wp, xg * 4 + wi - 1, ws);
        if (wave == 3 && xg == 3 && wi == 4) relay_role(wp, xg, ws);
        if (wave == 1 || (wave == 2 && !wi)) warm_role(wp, xg, wi - wave, ws, smem);
    }
    else if (wave == 1) {
        const u32 wp = wait_init(mk_rsrc(ws), tag, 1);
        if (lane_id() == 0) __hip_atomic_fetch_add((u32*)(ws + OFF_CNT + 768), 1u, __ATOMIC_RELAXED, __HIP_MEMORY_SCOPE_AGENT);
        lead_role(wp, xg, ws, smem);
    }
    else if (aux && xg == 2 && wave == 0) {
        const rsrc_t rs = mk_rsrc(ws);
        const int lane = lane_id();
#pragma unroll 1
        for (u32 spins = 0; spins < SPIN_CAP; ++spins) {
            CFENCE();
            bool ok = true;
#pragma unroll
            for (int q = 0; q < 4; ++q) {
                u32x2 f = ldx2(rs, (u32)(OFF_CLAIM + (q * 64 + lane) * 64));
                ok = ok && f[0] == (u32)tag && f[1] == (u32)(tag >> 32);
            }
            if (__all(ok)) break;
            __builtin_amdgcn_s_sleep(32);
        }
        if (lane == 0) __hip_atomic_store((u64*)(ws + OFF_GEN), gen + 1ull, __ATOMIC_RELAXED, __HIP_MEMORY_SCOPE_AGENT);
#pragma unroll
        for (int q = 0; q < 4; ++q) stx2(rs, (u32)(OFF_CLAIM + (q * 64 + lane) * 64), (u32x2){~(u32)tag, 0u});
    }
}

extern "C" void kernel_launch(void* const* d_in, const int* in_sizes, int n_in, void* d_out, int out_size,
                              void* d_ws, size_t ws_size, hipStream_t stream) {
    const u16* x = (const u16*)d_in[0];
    const u16* mem0 = (const u16*)d_in[1];
    const u16* Wq = (const u16*)d_in[2];
    const u16* Wk = (const u16*)d_in[3];
    const u16* Wv = (const u16*)d_in[4];
    const u16* Wo = (const u16*)d_in[5];
    const u16* w_r = (const u16*)d_in[8];
    const u16* w_gate = (const u16*)d_in[9];
    const u16* w_up = (const u16*)d_in[10];
    const u16* w_down = (const u16*)d_in[11];
    char* ws = (char*)d_ws;
    u16* out = (u16*)d_out;

    hipFuncSetAttribute((const void*)fused_kernel, hipFuncAttributeMaxDynamicSharedMemorySize, FUSED_LDS);
    fused_kernel<<<256, 512, FUSED_LDS, stream>>>(x, mem0, w_r, Wq, Wk, Wv, Wo, w_gate, w_up, w_down, ws, out);
}
```

```cpp
#define CFG 0
#include <hip/hip_runtime.h>
#include <hip/hip_bf16.h>
#include <stdint.h>

constexpr int S_ = 2048, D_ = 640, H_ = 32, DH_ = 128, NSEG = 64, SEGL = 32, FF_ = 1728, TOPC = 8;
constexpr int HD_ = H_ * DH_;
constexpr int NRED = 40;

typedef unsigned short u16;
typedef unsigned int u32;
typedef unsigned long long u64;
typedef __attribute__((ext_vector_type(8))) __bf16 bf16x8;
typedef __attribute__((ext_vector_type(16))) __bf16 bf16x16;
typedef __attribute__((ext_vector_type(4))) float f32x4;
typedef __attribute__((ext_vector_type(4))) unsigned int u32x4;
typedef __attribute__((ext_vector_type(8))) unsigned int u32x8;
typedef __attribute__((ext_vector_type(2))) unsigned int u32x2;

__device__ __forceinline__ float bf2f(u16 h) { return __uint_as_float(((u32)h) << 16); }
typedef __bf16 bf16x2_t __attribute__((ext_vector_type(2)));
typedef float f32x2_t __attribute__((ext_vector_type(2)));
__device__ __forceinline__ u16 f2bf(float f) {
    __bf16 h = (__bf16)f;
    return __builtin_bit_cast(u16, h);
}
__device__ __forceinline__ float bfr(float f) { return (float)(__bf16)f; }
__device__ __forceinline__ u32 pack2(float a, float b) {
    f32x2_t f = {a, b};
    bf16x2_t h = __builtin_convertvector(f, bf16x2_t);
    return __builtin_bit_cast(u32, h);
}
__device__ __forceinline__ bf16x8 as_bf(u32x4 v) { return __builtin_bit_cast(bf16x8, v); }
__device__ __forceinline__ f32x4 mfma16(u32x4 a, u32x4 b, f32x4 c) {
    return __builtin_amdgcn_mfma_f32_16x16x32_bf16(as_bf(a), as_bf(b), c, 0, 0, 0);
}
__device__ __forceinline__ f32x4 smf(u32x4 a, u32x4 b0, u32x4 b1, f32x4 c, int idx) {
    u32x8 b = {b0[0], b0[1], b0[2], b0[3], b1[0], b1[1], b1[2], b1[3]};
    return __builtin_amdgcn_smfmac_f32_16x16x64_bf16(as_bf(a), __builtin_bit_cast(bf16x16, b), c, idx, 0, 0);
}
__device__ __forceinline__ int dmap(int p) {
    int q = p & 15;
    return (p & ~15) | (q & 1) | ((q & 2) << 2) | ((q & 4) >> 1) | ((q & 8) >> 1);
}

constexpr size_t al256(size_t x) { return (x + 255) & ~(size_t)255; }
constexpr size_t OFF_XN = 0;
constexpr size_t OFF_XNP = OFF_XN + al256((size_t)S_ * D_ * 2);
constexpr size_t OFF_R = OFF_XNP + al256((size_t)S_ * D_ * 2);
constexpr size_t OFF_PX = OFF_R + al256((size_t)S_ * 4);
constexpr size_t OFF_PXM = OFF_PX + al256((size_t)NSEG * H_ * 2048);
constexpr size_t OFF_HB = OFF_PXM + al256((size_t)NSEG * 8 * 2048);
constexpr size_t OFF_UPD = OFF_HB + al256((size_t)8 * FF_ * 2);
constexpr size_t OFF_GEN = OFF_UPD + al256((size_t)8 * D_ * 2);
constexpr size_t OFF_CLAIM = OFF_GEN + 256;
constexpr size_t OFF_INI = OFF_CLAIM + 16384;
constexpr size_t OFF_SUM = OFF_INI + 2048;
constexpr size_t OFF_ERR = OFF_SUM + 3072;
constexpr size_t OFF_FA = OFF_ERR + 256;
constexpr size_t OFF_FB = OFF_FA + 4096;
constexpr size_t OFF_FP = OFF_FB + 4096;
constexpr size_t OFF_FZ = OFF_FP + 1024;
constexpr size_t OFF_RDY = OFF_FZ + 2048;
constexpr size_t OFF_IDX = OFF_RDY + 1024;
constexpr size_t OFF_CNT = OFF_IDX + 256;
constexpr size_t ZERO_BEGIN = OFF_ERR, ZERO_END = OFF_CNT + 1024;
constexpr size_t OFF_WIN = (size_t)12 << 20;
constexpr size_t WS_TOTAL = OFF_WIN + ((size_t)188 << 20);
static_assert(ZERO_END <= OFF_WIN && WS_TOTAL <= ((size_t)256 << 20), "ws");

constexpr u32 SLOT0 = 52u << 20;
constexpr u32 SL_P = 4 * 32768, SL_TS = 2 * 32768 + 1024, SL_PG = SL_TS + 2048 + 384;
constexpr u32 FAR_BX = 1u << 20;
constexpr u32 NEAR_QX = 59u << 20;
constexpr u32 NEAR_KX = NEAR_QX + NSEG * H_ * 8192;
static_assert(2 * ((size_t)NEAR_KX + (size_t)NSEG * H_ * 8192) + ((size_t)2 << 20) <= WS_TOTAL - OFF_WIN && SLOT0 + (64u << 20) <= 2 * NEAR_QX
              && 2 * ((size_t)FAR_BX + (size_t)NSEG * NRED * 32768) + ((size_t)2 << 20) <= WS_TOTAL - OFF_WIN, "far");
__device__ __forceinline__ u32 slot_off(u32 wp, int s) {
    u32 r, t;
    asm("s_lshr_b32 %0, %3, 1\n\ts_xor_b32 %0, %0, %2\n\ts_and_b32 %0, %0, 1\n\ts_lshl_b32 %0, %0, 13\n\ts_lshl_b32 %1, %3, 20\n\t"
        "s_add_u32 %0, %0, %1\n\ts_andn2_b32 %1, %2, 15\n\ts_add_u32 %0, %0, %1\n\ts_add_u32 %0, %0, %4" : "=&s"(r), "=&s"(t) : "s"(wp), "s"(s), "n"(SLOT0) : "scc");
    return r;
}
__device__ __forceinline__ u32 slot_z(u32 wp, u32 s) { return (wp & ~15u) + SLOT0 + (s << 20) + (((wp ^ (s >> 1)) & 1u) << 13); }
__device__ __forceinline__ u32 slot_v(u32 wp, u32 s) { return slot_z(wp, s) + ((((wp >> 3) ^ (s >> 5)) & 1u) << 18); }
__device__ __forceinline__ u32 far_v(u32 wp, u32 L) { return (wp & ~15u) + 2 * L + ((~(wp ^ (L >> 20)) & 1u) << 13); }
__device__ __forceinline__ u32 tab(u32 t, int s) { return __builtin_amdgcn_readlane(t, s); }
__device__ __forceinline__ u32 copy_off(u32 c, u32 s) { return ((c & 1u) << 14) | ((((c >> 1) ^ s) & 1u) << 12); }
__device__ __forceinline__ u32 z_off(u32 wp, u32 ct) {
    const u32 zb = ct >> 1, pg = zb < 16 ? zb + 16 : 6 + (zb & 1u) + (zb & 2u) * 4;
    return pg * 32768 + (((ct ^ (wp >> 1) ^ (pg >> 4)) & 1u) << 14);
}
constexpr int NCP = CFG ? 2 : 4;
__device__ __forceinline__ u32 put_off(u32 wp, u32 x0, u32 s) { return CFG ? copy_off(x0 ^ (wp >> 1), s) & 4096u : 0u; }
__device__ __forceinline__ u32 cpy(u32 c) { return CFG ? (c & 1u) * 16384u : (c & 1u) * 4096u + (c >> 1) * 16384u; }
__device__ __forceinline__ u32 far_off(u32 wp, u32 L) {
    u32 r, t;
    asm("s_lshr_b32 %0, %3, 20\n\ts_xnor_b32 %0, %0, %2\n\ts_and_b32 %0, %0, 1\n\ts_lshl_b32 %0, %0, 13\n\ts_lshl_b32 %1, %3, 1\n\t"
        "s_add_u32 %0, %0, %1\n\ts_andn2_b32 %1, %2, 15\n\ts_add_u32 %0, %0, %1" : "=&s"(r), "=&s"(t) : "s"(wp), "s"(L) : "scc");
    return r;
}
constexpr u32 SENT = 0xffbfffbfu;
constexpr u32 MARK = 0xffbeffbeu;
constexpr u32 OOB = 0x80000000u;
__device__ __forceinline__ u32 amap(u32 L) { return ((L >> 13) << 14) | (L & 8191u); }
__device__ __forceinline__ u32 win_off(const char* ws) {
    u64 a = (u64)(uintptr_t)ws;
    u64 w = (a + OFF_WIN + ((u64)2 << 20) - 1) & ~(((u64)2 << 20) - 1);
    return (u32)(w - a);
}

constexpr int AUX_SC1 = 16;
typedef __amdgpu_buffer_rsrc_t rsrc_t;
__device__ __forceinline__ rsrc_t mk_rsrc(const void* p) {
    return __builtin_amdgcn_make_buffer_rsrc((void*)p, 0, 0x7fffffff, 0x00020000);
}
__device__ __forceinline__ u32x4 ldx4(rsrc_t r, u32 off, u32 so = 0) { return __builtin_amdgcn_raw_buffer_load_b128(r, off, so, AUX_SC1); }
__device__ __forceinline__ u32x2 ldx2(rsrc_t r, u32 off, u32 so = 0) { return __builtin_amdgcn_raw_buffer_load_b64(r, off, so, AUX_SC1); }
__device__ __forceinline__ u32 ldx1(rsrc_t r, u32 off, u32 so = 0) { return __builtin_amdgcn_raw_buffer_load_b32(r, off, so, AUX_SC1); }
__device__ __forceinline__ void stx4(rsrc_t r, u32 off, u32x4 v, u32 so = 0) { __builtin_amdgcn_raw_buffer_store_b128(v, r, off + so, 0, AUX_SC1); }
__device__ __forceinline__ void stx2(rsrc_t r, u32 off, u32x2 v, u32 so = 0) { __builtin_amdgcn_raw_buffer_store_b64(v, r, off, so, AUX_SC1); }
__device__ __forceinline__ void stx1(rsrc_t r, u32 off, u32 v, u32 so = 0) { __builtin_amdgcn_raw_buffer_store_b32(v, r, off, so, AUX_SC1); }
#define DRAIN() asm volatile("s_waitcnt vmcnt(0)" ::: "memory")
#define CFENCE() asm volatile("" ::: "memory")
#define LBAR() asm volatile("s_waitcnt lgkmcnt(0)\n\ts_barrier" ::: "memory")

constexpr u32 L_MISC = 147456;
constexpr u32 SPIN_CAP = 1u << 18;
__device__ __forceinline__ int lane_id() {
    int l;
    asm volatile("v_mbcnt_lo_u32_b32 %0, -1, 0\n\tv_mbcnt_hi_u32_b32 %0, -1, %0" : "=v"(l));
    __builtin_assume(l >= 0 && l < 64);
    return l;
}

__device__ __forceinline__ float xmax16(float x) {
    u32 a = __float_as_uint(x);
    auto r = __builtin_amdgcn_permlane16_swap(a, a, false, false);
    return fmaxf(__uint_as_float(r[0]), __uint_as_float(r[1]));
}
__device__ __forceinline__ float xmax32(float x) {
    u32 a = __float_as_uint(x);
    auto r = __builtin_amdgcn_permlane32_swap(a, a, false, false);
    return fmaxf(__uint_as_float(r[0]), __uint_as_float(r[1]));
}
__device__ __forceinline__ float xadd16(float x) {
    u32 a = __float_as_uint(x);
    auto r = __builtin_amdgcn_permlane16_swap(a, a, false, false);
    return __uint_as_float(r[0]) + __uint_as_float(r[1]);
}
__device__ __forceinline__ float xadd32(float x) {
    u32 a = __float_as_uint(x);
    auto r = __builtin_amdgcn_permlane32_swap(a, a, false, false);
    return __uint_as_float(r[0]) + __uint_as_float(r[1]);
}
__device__ __forceinline__ u32 umax3(u32 a, u32 b, u32 c) { return max(max(a, b), c); }

__device__ __forceinline__ void wait_flags(rsrc_t rs, u32 off, bool act, u32 target, bool& dead, u32* errw, u32 code, int lane) {
    u32 spins = 0;
#pragma unroll 1
    while (!dead) {
        CFENCE();
        u32x4 f = ldx4(rs, off);
        bool ok = !act || ((f[0] >= target) & (f[1] >= target) & (f[2] >= target) & (f[3] >= target));
        if (__all(ok)) break;
        __builtin_amdgcn_s_sleep(2);
        if (++spins > SPIN_CAP) { if (lane == 0) atomicOr(errw, code); dead = true; }
        if ((spins & 63) == 0 && ldx1(rs, (u32)OFF_ERR) != 0u) dead = true;
    }
    CFENCE();
}
__device__ __forceinline__ void wait_word(rsrc_t rs, u32 off, bool act, u32 target, bool& dead, u32* errw, u32 code, int lane) {
    u32 spins = 0;
#pragma unroll 1
    while (!dead) {
        CFENCE();
        u32 f = ldx1(rs, off);
        if (__all(!act || f >= target)) break;
        __builtin_amdgcn_s_sleep(4);
        if (++spins > SPIN_CAP) { if (lane == 0) atomicOr(errw, code); dead = true; }
        if ((spins & 63) == 0 && ldx1(rs, (u32)OFF_ERR) != 0u) dead = true;
    }
    CFENCE();
}

__device__ __forceinline__ u32 cal_tag(u64 tag, u32 v) { return ((u32)tag ^ (u32)(tag >> 32)) + v * 0x9E3779B1u; }
__device__ __forceinline__ u32 to_sgpr(u32 x) {
    u32 r;
    asm volatile("s_nop 0\n\tv_readfirstlane_b32 %0, %1\n\ts_nop 4" : "=s"(r) : "v"(x));
    return r;
}
__device__ __forceinline__ u32 wp_of(const char* ws, u32 v) {
    v += __builtin_amdgcn_update_dpp(0u, v, 0xb1, 0xf, 0xf, false);
    v += __builtin_amdgcn_update_dpp(0u, v, 0x4e, 0xf, 0xf, false);
    v += __builtin_amdgcn_update_dpp(0u, v, 0x124, 0xf, 0xf, false);
    v += __builtin_amdgcn_update_dpp(0u, v, 0x128, 0xf, 0xf, false);
    const u32 r = __builtin_amdgcn_readlane(v, 48) + 0x0f0f0f0fu;
    return to_sgpr(win_off(ws) | ((r >> 7) & 1u) | ((r >> 14) & 2u) | ((r >> 21) & 4u) | ((r >> 28) & 8u));
}
__device__ __forceinline__ u32 sum_off(int level) {
    u32 xcc;
    asm volatile("s_getreg_b32 %0, hwreg(HW_REG_XCC_ID, 0, 3)" : "=s"(xcc));
    return (u32)(OFF_SUM + level * 1024) + xcc * 128u;
}
__device__ __forceinline__ u32 wait_init(rsrc_t rs, u64 tag, int level) {
    const u32 so = sum_off(level);
    u32x2 c = {0u, 0u};
#pragma unroll 1
    for (u32 spins = 0; spins < SPIN_CAP; ++spins) {
        CFENCE();
        c = ldx2(rs, 0u, so);
        if (__all(c[1] == cal_tag(tag, c[0]))) break;
        __builtin_amdgcn_s_sleep(6);
    }
    CFENCE();
    return to_sgpr(min(c[0], (u32)OFF_WIN + 0x20000fu));
}

#define STAMPS() do { } while (0)
#define STAMP(k) do { } while (0)
#define RSTAMP(k) do { } while (0)
#define STAMP2(k, r) do { } while (0)
#define FLUSH() do { } while (0)
#define LOGS() do { } while (0)
#define LOG() do { } while (0)
#define LOGOUT(cu) do { } while (0)
#define CFLUSH() do { } while (0)
#define XSTAMP(w, sl) do { } while (0)
#define WSTAMP(id) do { } while (0)
#define CSTAMP(id) do { } while (0)
#define NPROBE 2
constexpr u32 INI_ZERO_CHUNKS = (u32)((ZERO_END - ZERO_BEGIN) / 16);
static_assert(INI_ZERO_CHUNKS <= 8 * 256, "zero");

__device__ __forceinline__ float rsum16(float x) {
    x += __uint_as_float(__builtin_amdgcn_update_dpp(0u, __float_as_uint(x), 0xb1, 0xf, 0xf, false));
    x += __uint_as_float(__builtin_amdgcn_update_dpp(0u, __float_as_uint(x), 0x4e, 0xf, 0xf, false));
    x += __uint_as_float(__builtin_amdgcn_update_dpp(0u, __float_as_uint(x), 0x124, 0xf, 0xf, false));
    x += __uint_as_float(__builtin_amdgcn_update_dpp(0u, __float_as_uint(x), 0x128, 0xf, 0xf, false));
    return x;
}

__device__ __forceinline__ void ln_regs(u32x4 (&xv)[5]) {
    float sum = 0.f;
#pragma unroll
    for (int i = 0; i < 5; ++i)
#pragma unroll
        for (int j = 0; j < 4; ++j) sum += __uint_as_float(xv[i][j] << 16) + __uint_as_float(xv[i][j] & 0xffff0000u);
    const float mean = rsum16(sum) * (1.0f / D_);
    float vs = 0.f;
#pragma unroll
    for (int i = 0; i < 5; ++i)
#pragma unroll
        for (int j = 0; j < 4; ++j) {
            float d0 = __uint_as_float(xv[i][j] << 16) - mean, d1 = __uint_as_float(xv[i][j] & 0xffff0000u) - mean;
            vs += d0 * d0 + d1 * d1;
        }
    const float var = bfr(rsum16(vs) * (1.0f / D_));
    const float m_bf = bfr(mean);
    const float rden = 1.0f / bfr(sqrtf(bfr(var + 1e-05f)));
#pragma unroll
    for (int i = 0; i < 5; ++i)
#pragma unroll
        for (int j = 0; j < 4; ++j)
            xv[i][j] = pack2(bfr(__uint_as_float(xv[i][j] << 16) - m_bf) * rden, bfr(__uint_as_float(xv[i][j] & 0xffff0000u) - m_bf) * rden);
}
__device__ __forceinline__ u32x4 ln_perm(const u32x4 o, const int n) {
    const u32 s0 = (n & 1) ? o[0] : o[2], s1 = (n & 1) ? o[1] : o[3];
    const u32 t0 = __builtin_amdgcn_update_dpp(0u, s0, 0xb1, 0xf, 0xf, false), t1 = __builtin_amdgcn_update_dpp(0u, s1, 0xb1, 0xf, 0xf, false);
    return (n & 1) ? (u32x4){t0, o[2], t1, o[3]} : (u32x4){o[0], t0, o[1], t1};
}
__device__ __forceinline__ u32 img_off(const int row, const int n, const int i) {
    return (u32)((row >> 4) * 20480 + (4 * i + (n >> 2)) * 1024 + (n & 3) * 256 + (row & 15) * 16);
}
__device__ __forceinline__ void ln_load(const int row0, const int lane, const u16* __restrict__ x, const u16* __restrict__ w_r, u32x4 (&xv)[5], u32x4 (&wv)[5]) {
    const int n = lane & 15, row = row0 + (lane >> 4);
#pragma unroll
    for (int i = 0; i < 5; ++i) xv[i] = *(const u32x4*)(x + (size_t)row * D_ + (n + 16 * i) * 8);
#pragma unroll
    for (int i = 0; i < 5; ++i) wv[i] = *(const u32x4*)(w_r + (n + 16 * i) * 8);
}
__device__ __forceinline__ void ln_store(const int row0, const int lane, const u32x4 (&xv)[5], const u32x4 (&wv)[5], rsrc_t rs, const bool img = true) {
    const int n = lane & 15, row = row0 + (lane >> 4);
#pragma unroll
    for (int i = 0; i < 5; ++i) if (img) stx4(rs, (u32)OFF_XNP + img_off(row, n, i), ln_perm(xv[i], n));
    CFENCE();
    float dot = 0.f;
#pragma unroll
    for (int i = 0; i < 5; ++i) {
        stx4(rs, (u32)OFF_XN + (u32)(row * D_ + (n + 16 * i) * 8) * 2, xv[i]);
#pragma unroll
        for (int j = 0; j < 4; ++j)
            dot += __uint_as_float(xv[i][j] << 16) * __uint_as_float(wv[i][j] << 16) + __uint_as_float(xv[i][j] & 0xffff0000u) * __uint_as_float(wv[i][j] & 0xffff0000u);
    }
    dot = rsum16(dot);
    stx1(rs, n == 0 ? (u32)(OFF_R + row * 4) : OOB, __float_as_uint(bfr(dot)));
}
__device__ __forceinline__ u32x4 mk_srd(const char* ws) {
    const u64 a = (u64)(uintptr_t)ws;
    return (u32x4){(u32)a, (u32)(a >> 32), 0x7fffffffu, 0x00020000u};
}

template <typename F>
__device__ __forceinline__ u32 probe(F&& early, const int pi, const u32 xcc, const u32 hl, char* __restrict__ ws) {
    const u32x4 srd = mk_srd(ws);
    const u32 base = win_off(ws) + SLOT0 + (8u << 20) + 6u * 32768u + (u32)pi * 128u;
    const int lane = lane_id();
    const u32 zero = 0;
    asm volatile("buffer_atomic_or %0, %1, %2, %3 offen sc1\n\ts_waitcnt vmcnt(0)" :: "v"(zero), "v"(lane < 16 ? (u32)lane * 2048u : OOB), "s"(srd), "s"(base) : "memory");
    const u32 one = lane == 0 ? 0u : OOB;
    auto lat = [&](u32 q) {
        u64 t0, t1;
        asm volatile("s_memtime %0\n\tbuffer_atomic_or %2, %3, %4, %5 offen sc1\n\ts_waitcnt vmcnt(0)\n\ts_memtime %1\n\ts_waitcnt lgkmcnt(0)"
                     : "=&s"(t0), "=&s"(t1) : "v"(zero), "v"(one), "s"(srd), "s"(base + q * 2048u) : "memory");
        return (u32)t1 - (u32)t0;
    };
    const u32 q0 = (u32)pi & 11u, a0 = lat(q0), a1 = lat(q0 | 4u), a2 = lat(q0 ^ 8u), a3 = lat((q0 ^ 8u) | 4u), a4 = lat(q0 ^ 2u), a5 = lat((q0 ^ 2u) | 4u);
    const int d13 = (int)min(min(a0, a2), a4) - (int)min(min(a1, a3), a5), v13 = min(max(d13 >> 4, -7), 7);
    const u32 b13 = d13 > 0 ? 4u : 0u;
    const u32 flip = (xcc >> 2) | ((xcc & 3u) << 1) | (hl << 3);
    u32 votes = (u32)(7 + ((flip & 1u) ? -v13 : v13));
    early(votes);
    u32 best = ~0u;
#pragma unroll 1
    for (u32 i = 0; i < 8 * NPROBE; ++i) {
        const u32 c = (i + (u32)pi) & 7u, t = lat((c & 3u) | b13 | (c & 4u) * 2u);
        if ((u32)lane == c) best = min(best, t);
    }
#pragma unroll
    for (u32 i = 1; i < 4; ++i) {
        const u32 bit = 8u >> i;
        const int d = (int)best - __shfl_xor((int)best, (int)bit);
        int v = 0;
#pragma unroll
        for (u32 c = 0; c < 8; ++c) if (!(c & bit)) v += min(max(__builtin_amdgcn_readlane(d, c), -28), 28);
        v = (v + 8) >> 4;
        if ((flip >> i) & 1u) v = -v;
        votes |= (u32)(xcc >= 4 ? 7 : 7 + v) << (8 * i);
    }
    return votes;
}

typedef __attribute__((ext_vector_type(32))) unsigned int u32x32;
__device__ __forceinline__ u32x4 wfrag(const u32x32 (&W)[5], const int i) {
    return (u32x4){W[i >> 3][(i & 7) * 4], W[i >> 3][(i & 7) * 4 + 1], W[i >> 3][(i & 7) * 4 + 2], W[i >> 3][(i & 7) * 4 + 3]};
}
#define PIN_W(W) asm volatile("" : "+{v[16:47]}"(W[0]), "+{v[48:79]}"(W[1]), "+{v[80:111]}"(W[2]), "+{v[112:143]}"(W[3]), "+{v[144:175]}"(W[4]))
typedef __attribute__((ext_vector_type(4))) short s16x4;
typedef __attribute__((address_space(3))) s16x4* lds_s16x4p;
__device__ __forceinline__ u32x2 trd(u32 ldsaddr) {
    return __builtin_bit_cast(u32x2, __builtin_amdgcn_ds_read_tr16_b64_v4i16((lds_s16x4p)ldsaddr));
}
template <bool NT>
__device__ __forceinline__ void dma16(const void* g, u32 lds_base) {
    u32 sv;
    if (NT)
        asm volatile("s_mov_b32 %0, m0\n\ts_mov_b32 m0, %2\n\ts_nop 0\n\tglobal_load_lds_dwordx4 %1, off nt\n\ts_mov_b32 m0, %0"
                     : "=&s"(sv) : "v"(g), "s"(lds_base) : "memory");
    else
        asm volatile("s_mov_b32 %0, m0\n\ts_mov_b32 m0, %2\n\ts_nop 0\n\tglobal_load_lds_dwordx4 %1, off\n\ts_mov_b32 m0, %0"
                     : "=&s"(sv) : "v"(g), "s"(lds_base) : "memory");
}
__device__ __forceinline__ void dma16_sc1(const void* g, u32 lds_base) {
    u32 sv;
    asm volatile("s_mov_b32 %0, m0\n\ts_mov_b32 m0, %2\n\ts_nop 0\n\tglobal_load_lds_dwordx4 %1, off sc1\n\ts_mov_b32 m0, %0"
                 : "=&s"(sv) : "v"(g), "s"(lds_base) : "memory");
}
template <int ORD, typename F>
__device__ __forceinline__ void load_weights(F&& after, const u16* __restrict__ p0, const u16* __restrict__ p1, const bool wo1, char* smem,
                                             const int wave, const int lane, const int sa, const int ta, const int sb, const int tb,
                                             u32x32 (&W)[5]) {
    const u32 lbase = (u32)(uintptr_t)(__attribute__((address_space(3))) char*)smem;
    const int strm = wave >> 2;
    const bool wo = wo1 && strm == 1;
    const u16* src = strm ? p1 : p0;
    const u32 sstep = wo ? 128u : 128u * HD_;
    u32 so[8];
#pragma unroll
    for (int i = 0; i < 8; ++i) {
        const int j = (wave & 3) * 8 + i, r = 4 * j + (lane >> 4);
        const int f = (lane >> 4) | (((j >> 1) & 1) << 2);
        const int ch = (lane & 15) ^ (f << 1);
        const int e = (r & ~31) | (((r >> 2) & 1) << 4) | (((r >> 3) & 3) << 2) | (r & 3);
        so[i] = wo ? (u32)(e * D_ + ch * 8) : (u32)(dmap(r) * HD_ + ch * 8);
    }
    auto issue = [&](int st) {
        const u32 dst = __builtin_amdgcn_readfirstlane(lbase + (u32)((st & 1) * 65536 + strm * 32768 + (wave & 3) * 8192));
#pragma unroll
        for (int i = 0; i < 8; ++i) dma16<((ORD >> 20) & 1) != 0>(src + so[i] + (size_t)((ORD >> (4 * st)) & 15) * sstep, dst + i * 1024);
    };
    const int g = lane >> 4, q = (lane >> 2) & 3, p = lane & 3;
    const int f = q | ((g & 1) << 2);
    const u32 ra = lbase + (u32)(sa * 32768 + (8 * g + q) * 256 + (((ta ^ f) << 1) | (p >> 1)) * 16 + (p & 1) * 8);
    const u32 rb = lbase + (u32)(sb * 32768 + (8 * g + q) * 256 + (((tb ^ f) << 1) | (p >> 1)) * 16 + (p & 1) * 8);
    issue(0);
    issue(1);
#pragma unroll
    for (int st = 0; st < 5; ++st) {
        if (st < 4) asm volatile("s_waitcnt vmcnt(8)" ::: "memory"); else asm volatile("s_waitcnt vmcnt(0)" ::: "memory");
        LBAR();
#pragma unroll
        for (int k = 0; k < 4; ++k) {
            const u32 o = (u32)((st & 1) * 65536 + k * 8192);
            u32x2 a0 = trd(ra + o), a1 = trd(ra + o + 1024), b0 = trd(rb + o), b1 = trd(rb + o + 1024);
            const int ia = (((ORD >> (4 * st)) & 15) * 4 + k) * 4, ib = 80 + ia;
            if ((ORD >> 21) & 1) {
                u32x32& w = W[(ORD >> (4 * st)) & 15];
                w[k * 8] = a0[0]; w[k * 8 + 1] = b0[0]; w[k * 8 + 2] = a0[1]; w[k * 8 + 3] = b0[1];
                w[k * 8 + 4] = a1[0]; w[k * 8 + 5] = b1[0]; w[k * 8 + 6] = a1[1]; w[k * 8 + 7] = b1[1];
            } else {
                W[ia >> 5][ia & 31] = a0[0]; W[ia >> 5][(ia & 31) + 1] = a0[1]; W[ia >> 5][(ia & 31) + 2] = a1[0]; W[ia >> 5][(ia & 31) + 3] = a1[1];
                W[ib >> 5][ib & 31] = b0[0]; W[ib >> 5][(ib & 31) + 1] = b0[1]; W[ib >> 5][(ib & 31) + 2] = b1[0]; W[ib >> 5][(ib & 31) + 3] = b1[1];
            }
        }
        LBAR();
        after(st + 16);
        if (st + 2 < 5) issue(st + 2);
        after(st);
    }
}

__device__ __forceinline__ void early_lines(const int t0, const u64 wq, const u64 wk, const u64 wv, const u64 wo, const u32 lds) {
#pragma unroll
    for (int q = 0; q < 3; ++q) {
        const int t = t0 + q * 320, m = t / 180, r = (t - m * 180) * 64 + lane_id(), row = r / 18, c = r - row * 18;
        const u64 w = m == 0 ? wo : m == 1 ? wv : m == 2 ? wk : wq;
        const u32 blk = ((c < 16 ? (u32)(c >> 1) * 4u + 1u : 31u) - ((u32)w >> 8)) & 31u;
        u32 sv;
        if (t < 720)
            asm volatile("s_mov_b32 %0, m0\n\ts_mov_b32 m0, %3\n\ts_nop 0\n\tglobal_load_lds_dword %1, %2\n\ts_mov_b32 m0, %0"
                         : "=&s"(sv) : "v"((u32)row * 8192u + blk * 256u + (u32)(c & 1) * 128u), "s"(w), "s"(lds) : "memory");
    }
}
template <typename F>
__device__ __forceinline__ void init_role(F&& settle, const int wave, const int ii, const u32 xcc, const u32 hl, const u16* __restrict__ x, const u16* __restrict__ w_r,
                                          const u16* Wq, const u16* Wk, const u16* Wv, const u16* Wo, char* __restrict__ ws, char* smem) {
    const int lane = lane_id(), tid = wave * 64 + lane;
    const rsrc_t rs = mk_rsrc(ws);
    u32* cnt = (u32*)(smem + L_MISC + 128);
    if (tid < 3) cnt[tid] = 0u;
    u64 tag;
    auto arrive = [&](int level) {
        if (lane == 0 && __hip_atomic_fetch_add(cnt + level, 1u, __ATOMIC_RELAXED, __HIP_MEMORY_SCOPE_WORKGROUP) == 7u)
            stx2(rs, (u32)(OFF_INI + (level * 64 + ii) * 8), (u32x2){0u, cal_tag(tag, 0u)});
    };
    if (ii >= 48) {
        if (wave == 1 && ii < 63) {
            const u32 votes = probe([&](u32 v) {
                tag = settle();
                stx2(rs, lane == 0 ? (u32)(OFF_INI + ii * 8) : OOB, (u32x2){v, cal_tag(tag, v)});
                DRAIN();
                XSTAMP(192 + ii, 220);
            }, ii - 48, xcc, hl, ws);
            XSTAMP(192 + ii, 223);
            stx2(rs, lane == 0 ? (u32)(OFF_INI + (64 + ii) * 8) : OOB, (u32x2){votes, cal_tag(tag, votes)});
        } else tag = settle();
        if (wave == 2 && ii == 63) {
            u32 wp = 0;
#pragma unroll 1
            for (int level = 0; level < 3; ++level) {
                const bool act = level == 2 ? lane < 48 : lane < 63 && (lane >= 40 || level == 1);
                u32x2 c = {0u, 0u};
                u32 spins = 0;
#pragma unroll 1
                for (; spins < SPIN_CAP; ++spins) {
                    CFENCE();
                    c = ldx2(rs, (u32)(OFF_INI + level * 512 + lane * 8));
                    if (__all(!act || c[1] == cal_tag(tag, c[0]))) break;
                }
                if (spins == SPIN_CAP) break;
                if (level < 2) wp = wp_of(ws, lane < 63 ? c[0] : 0x07070707u);
                stx2(rs, lane < 8 ? (u32)(OFF_SUM + level * 1024 + lane * 128) : OOB, (u32x2){wp, cal_tag(tag, wp)});
                XSTAMP(192 + level, 229);
            }
        }
        if (ii < 63) LBAR();
        return;
    }
    u32x4 xv[5], wv[5];
    if (ii < 40) {
        early_lines(wave * 40 + ii, (u64)(uintptr_t)Wq, (u64)(uintptr_t)Wk, (u64)(uintptr_t)Wv, (u64)(uintptr_t)Wo, (u32)(uintptr_t)(__attribute__((address_space(3))) char*)smem + L_MISC + 512u);
        tag = settle();
        const u32 wp = wait_init(rs, tag, 0), st = (u32)(ii * 8 + wave), so = slot_off(wp, (int)(st >> 6)) + (st & 63u) * 16384u + (u32)lane * 16;
#pragma unroll
        for (int i = 0; i < 8; ++i) stx4(rs, so + i * 1024, (u32x4){SENT, SENT, SENT, SENT});
        DRAIN();
        XSTAMP(192 + ii, 225);
        arrive(1);
    } else {
        const int row = (ii - 40) * 32 + wave * 4;
        u32x4 xw[5];
        ln_load(row, lane, x, w_r, xv, wv);
#pragma unroll
        for (int i = 0; i < 5; ++i) xw[i] = *(const u32x4*)(x + (size_t)(row + 256 + (lane >> 4)) * D_ + ((lane & 15) + 16 * i) * 8);
        ln_regs(xv);
        tag = settle();
        const u32 i = (u32)((ii - 40) * 512 + tid);
        stx4(rs, i < INI_ZERO_CHUNKS ? (u32)ZERO_BEGIN + i * 16 : OOB, (u32x4){0u, 0u, 0u, 0u});
#pragma unroll
        for (int i = 0; i < 5; ++i) stx4(rs, (u32)OFF_XNP + img_off(row + (lane >> 4), lane & 15, i), ln_perm(xv[i], lane & 15));
        ln_regs(xw);
        DRAIN();
        XSTAMP(192 + ii, 220);
        arrive(0);
        ln_store(row + 256, lane, xw, wv, rs);
        asm volatile("s_waitcnt vmcnt(6)" ::: "memory");
        arrive(1);
        ln_store(row, lane, xv, wv, rs, false);
    }
    {
        const int row = 512 + (ii * 8 + wave) * 4;
        ln_load(row, lane, x, w_r, xv, wv);
        ln_regs(xv);
        ln_store(row, lane, xv, wv, rs);
    }
    DRAIN();
    XSTAMP(192 + ii, 222);
    arrive(2);
    LBAR();
}

#define NOWV 4
#define STP
#define STA
#define LGP
#define LGA
__device__ __forceinline__ void s_fetch(u32 vm, u32 xo, u32x4 rs, u32 pfo, u32 pfd, u32 xd) {
    asm volatile(
        "s_mov_b32 s92, m0\n\t"
        "s_mov_b32 m0, %[pfd]\n\t"
        "s_add_u32 s93, %[pfo], 0x1000\n\t"
        "s_nop 2\n\t"
        "buffer_load_dwordx4 %[vm], %[rs], %[pfo] offen offset:0 sc1 lds\n\t"
        "buffer_load_dwordx4 %[vm], %[rs], %[pfo] offen offset:1024 sc1 lds\n\t"
        "buffer_load_dwordx4 %[vm], %[rs], %[pfo] offen offset:2048 sc1 lds\n\t"
        "buffer_load_dwordx4 %[vm], %[rs], %[pfo] offen offset:3072 sc1 lds\n\t"
        "s_add_u32 m0, m0, 0x1000\n\t"
        "s_nop 0\n\t"
        "buffer_load_dwordx4 %[vm], %[rs], s93 offen offset:0 sc1 lds\n\t"
        "buffer_load_dwordx4 %[vm], %[rs], s93 offen offset:1024 sc1 lds\n\t"
        "buffer_load_dwordx4 %[vm], %[rs], s93 offen offset:2048 sc1 lds\n\t"
        "buffer_load_dwordx4 %[vm], %[rs], s93 offen offset:3072 sc1 lds\n\t"
        "s_mov_b32 m0, %[xd]\n\t"
        "s_nop 0\n\t"
        "buffer_load_dword %[xo], %[rs], 0 offen sc1 lds\n\t"
        "s_mov_b32 m0, s92\n\t"
        :
        : [vm] "v"(vm), [xo] "v"(xo), [rs] "s"(rs), [pfo] "s"(pfo), [pfd] "s"(pfd), [xd] "s"(xd)
        : "s92", "s93", "vcc", "scc", "memory");
}

__device__ __forceinline__ void s_k0(const u32x32 (&W)[5], u32& rnd, u32 va, u32 sidx, u32 ow, u32 lb STP) {
    asm volatile(
        "v_mov_b64 v[248:249], 0\n\t"
        "v_mov_b64 v[250:251], 0\n\t"
        "v_mov_b64 v[252:253], 0\n\t"
        "v_mov_b64 v[254:255], 0\n\t"
        "v_mov_b32 v200, 0xffbfffbf\n\t"
        "v_mov_b32 v206, %[lb]\n\t"
        "s_add_u32 %[rnd], %[rnd], 1\n\t"
        "s_setprio 1\n\t"
        "s_waitcnt vmcnt(0) lgkmcnt(0)\n\t"
        "s_barrier\n\t"
        "s_branch Lmk%=\n\t"
        "Ltop%=:\n\t"
        "v_mov_b64 v[248:249], 0\n\t"
        "v_mov_b64 v[250:251], 0\n\t"
        "v_mov_b64 v[252:253], 0\n\t"
        "v_mov_b64 v[254:255], 0\n\t"
        "v_mov_b32 v200, 0xffbfffbf\n\t"
        "v_mov_b32 v206, %[lb]\n\t"
        "s_add_u32 %[rnd], %[rnd], 1\n\t"
        "s_setprio 1\n\t"
        "Lmk%=:\n\t"
        "ds_write_b32 %[ow], v200\n\t"
        "ds_write_b32 %[ow], v200 offset:3072\n\t"
        "s_waitcnt lgkmcnt(0)\n\t"
        "s_barrier\n\t"
        "s_nop 15\n\t"
        "s_nop 15\n\t"
        "s_nop 3\n\t"
        "ds_read_b128 v[208:211], %[va] offset:0\n\t"
        "ds_read_b128 v[212:215], %[va] offset:1024\n\t"
        "ds_read_b128 v[216:219], %[va] offset:2048\n\t"
        "ds_read_b128 v[220:223], %[va] offset:3072\n\t"
        "ds_read_b128 v[224:227], %[va] offset:4096\n\t"
        "ds_read_b128 v[228:231], %[va] offset:5120\n\t"
        "ds_read_b128 v[232:235], %[va] offset:6144\n\t"
        "ds_read_b128 v[236:239], %[va] offset:7168\n\t"
        "ds_read_b128 v[240:243], %[va] offset:8192\n\t"
        "ds_read_b128 v[244:247], %[va] offset:10240\n\t"
        "s_waitcnt lgkmcnt(0)\n\t"
        "v_smfmac_f32_16x16x64_bf16 v[248:251], v[208:211], v[16:23], %[sidx]\n\t"
        "v_smfmac_f32_16x16x64_bf16 v[248:251], v[212:215], v[24:31], %[sidx]\n\t"
        "v_smfmac_f32_16x16x64_bf16 v[248:251], v[216:219], v[32:39], %[sidx]\n\t"
        "v_smfmac_f32_16x16x64_bf16 v[248:251], v[220:223], v[40:47], %[sidx]\n\t"
        "v_smfmac_f32_16x16x64_bf16 v[248:251], v[224:227], v[48:55], %[sidx]\n\t"
        "v_smfmac_f32_16x16x64_bf16 v[248:251], v[228:231], v[56:63], %[sidx]\n\t"
        "v_smfmac_f32_16x16x64_bf16 v[248:251], v[232:235], v[64:71], %[sidx]\n\t"
        "v_smfmac_f32_16x16x64_bf16 v[248:251], v[236:239], v[72:79], %[sidx]\n\t"
        "v_smfmac_f32_16x16x64_bf16 v[248:251], v[240:243], v[80:87], %[sidx]\n\t"
        "v_smfmac_f32_16x16x64_bf16 v[248:251], v[244:247], v[88:95], %[sidx]\n\t"
        "v_smfmac_f32_16x16x64_bf16 v[252:255], v[208:211], v[96:103], %[sidx]\n\t"
        "v_smfmac_f32_16x16x64_bf16 v[252:255], v[212:215], v[104:111], %[sidx]\n\t"
        "v_smfmac_f32_16x16x64_bf16 v[252:255], v[216:219], v[112:119], %[sidx]\n\t"
        "v_add_f32 v248, v248, v250\n\t"
        "v_smfmac_f32_16x16x64_bf16 v[252:255], v[220:223], v[120:127], %[sidx]\n\t"
        "v_add_f32 v249, v249, v251\n\t"
        "v_smfmac_f32_16x16x64_bf16 v[252:255], v[224:227], v[128:135], %[sidx]\n\t"
        "v_cvt_pk_bf16_f32 v200, v248, v249\n\t"
        "v_smfmac_f32_16x16x64_bf16 v[252:255], v[228:231], v[136:143], %[sidx]\n\t"
        "ds_write_b32 %[ow], v200\n\t"
        "v_smfmac_f32_16x16x64_bf16 v[252:255], v[232:235], v[144:151], %[sidx]\n\t"
        "v_cmp_u_f32 vcc, v248, v249\n\t"
        "v_smfmac_f32_16x16x64_bf16 v[252:255], v[236:239], v[152:159], %[sidx]\n\t"
        "v_smfmac_f32_16x16x64_bf16 v[252:255], v[240:243], v[160:167], %[sidx]\n\t"
        "v_smfmac_f32_16x16x64_bf16 v[252:255], v[244:247], v[168:175], %[sidx]\n\t"
        "s_nop 6\n\t"
        "s_cbranch_vccnz Lnan%=\n\t"
        "Lok%=:\n\t"
        "v_add_f32 v252, v252, v254\n\t"
        "v_add_f32 v253, v253, v255\n\t"
        "v_cvt_pk_bf16_f32 v201, v252, v253\n\t"
        "ds_write_b32 %[ow], v201 offset:3072\n\t"
        "s_barrier\n\t"
        "s_setprio 0\n\t"
        "ds_read_b32 v207, v206 offset:25104\n\t"
        "s_waitcnt lgkmcnt(0)\n\t"
        "v_cmp_eq_u32 vcc, %[rnd], v207\n\t"
        "s_barrier\n\t"
        "s_cbranch_vccnz Ltop%=\n\t"
        "s_branch Lend%=\n\t"
        "Lnan%=:\n\t"
        "v_max_u32 v207, v208, v209\n\t"
        "v_max3_u32 v207, v207, v210, v211\n\t"
        "v_max3_u32 v207, v207, v212, v213\n\t"
        "v_max3_u32 v207, v207, v214, v215\n\t"
        "v_max3_u32 v207, v207, v216, v217\n\t"
        "v_max3_u32 v207, v207, v218, v219\n\t"
        "v_max3_u32 v207, v207, v220, v221\n\t"
        "v_max3_u32 v207, v207, v222, v223\n\t"
        "v_max3_u32 v207, v207, v224, v225\n\t"
        "v_max3_u32 v207, v207, v226, v227\n\t"
        "v_max3_u32 v207, v207, v228, v229\n\t"
        "v_max3_u32 v207, v207, v230, v231\n\t"
        "v_max3_u32 v207, v207, v232, v233\n\t"
        "v_max3_u32 v207, v207, v234, v235\n\t"
        "v_max3_u32 v207, v207, v236, v237\n\t"
        "v_max3_u32 v207, v207, v238, v239\n\t"
        "v_max3_u32 v207, v207, v240, v241\n\t"
        "v_max3_u32 v207, v207, v242, v243\n\t"
        "v_max3_u32 v207, v207, v244, v245\n\t"
        "v_max3_u32 v207, v207, v246, v247\n\t"
        "v_cmp_eq_u32 vcc, 0xffbfffbf, v207\n\t"
        "v_mov_b32 v207, %[rnd]\n\t"
        "s_cbranch_vccz Lok%=\n\t"
        "ds_write_b32 v206, v207 offset:25104\n\t"
        "s_waitcnt lgkmcnt(0)\n\t"
        "s_barrier\n\t"
        "s_barrier\n\t"
        "s_branch Ltop%=\n\t"
        "Lend%=:\n\t"
        : [rnd] "+&s"(rnd)
        : [w0] "{v[16:47]}"(W[0]), [w1] "{v[48:79]}"(W[1]), [w2] "{v[80:111]}"(W[2]), [w3] "{v[112:143]}"(W[3]), [w4] "{v[144:175]}"(W[4]), [va] "v"(va), [sidx] "v"(sidx), [ow] "v"(ow), [lb] "s"(lb)
        : "v200", "v201", "v202", "v203", "v204", "v205", "v206", "v207", "v208", "v209", "v210", "v211", "v212", "v213", "v214", "v215", "v216", "v217", "v218", "v219", "v220", "v221", "v222", "v223", "v224", "v225", "v226", "v227", "v228", "v229", "v230", "v231", "v232", "v233", "v234", "v235", "v236", "v237", "v238", "v239", "v240", "v241", "v242", "v243", "v244", "v245", "v246", "v247", "v248", "v249", "v250", "v251", "v252", "v253", "v254", "v255", "vcc", "scc", "memory");
}

__device__ __forceinline__ void s_k1(const u32x32 (&W)[5], u32& rnd, u32 va, u32 sidx, u32 ow, u32 lb, u32x4& p0, u32& p1, u32 ka, u32 qa, u32 ma, u32 ktr, u32 o0, u32 o1, float ce, u32x4 rs STP) {
    asm volatile(
        "v_mov_b64 v[248:249], 0\n\t"
        "v_mov_b64 v[250:251], 0\n\t"
        "v_mov_b64 v[252:253], 0\n\t"
        "v_mov_b64 v[254:255], 0\n\t"
        "v_mov_b32 v200, 0xffbfffbf\n\t"
        "v_mov_b32 v206, %[lb]\n\t"
        "s_add_u32 %[rnd], %[rnd], 1\n\t"
        "s_setprio 1\n\t"
        "s_waitcnt vmcnt(0) lgkmcnt(0)\n\t"
        "s_barrier\n\t"
        "buffer_store_dwordx4 v[192:195], %[o0], %[rs], 0 offen sc1\n\t"
        "buffer_store_dword v196, %[o1], %[rs], 0 offen sc1\n\t"
        "s_branch Lmk%=\n\t"
        "Ltop%=:\n\t"
        "v_mov_b64 v[248:249], 0\n\t"
        "v_mov_b64 v[250:251], 0\n\t"
        "v_mov_b64 v[252:253], 0\n\t"
        "v_mov_b64 v[254:255], 0\n\t"
        "v_mov_b32 v200, 0xffbfffbf\n\t"
        "v_mov_b32 v206, %[lb]\n\t"
        "s_add_u32 %[rnd], %[rnd], 1\n\t"
        "s_setprio 1\n\t"
        "Lmk%=:\n\t"
        "ds_write_b32 %[ow], v200\n\t"
        "ds_write_b32 %[ow], v200 offset:3072\n\t"
        "s_waitcnt lgkmcnt(0)\n\t"
        "s_barrier\n\t"
        "s_nop 15\n\t"
        "s_nop 15\n\t"
        "s_nop 3\n\t"
        "ds_read_b128 v[208:211], %[va] offset:0\n\t"
        "ds_read_b128 v[212:215], %[va] offset:1024\n\t"
        "ds_read_b128 v[216:219], %[va] offset:2048\n\t"
        "ds_read_b128 v[220:223], %[va] offset:3072\n\t"
        "ds_read_b128 v[224:227], %[va] offset:4096\n\t"
        "ds_read_b128 v[228:231], %[va] offset:5120\n\t"
        "ds_read_b128 v[232:235], %[va] offset:6144\n\t"
        "ds_read_b128 v[236:239], %[va] offset:7168\n\t"
        "ds_read_b128 v[240:243], %[va] offset:8192\n\t"
        "ds_read_b128 v[244:247], %[va] offset:10240\n\t"
        "s_waitcnt lgkmcnt(0)\n\t"
        "v_smfmac_f32_16x16x64_bf16 v[248:251], v[208:211], v[16:23], %[sidx]\n\t"
        "v_smfmac_f32_16x16x64_bf16 v[248:251], v[212:215], v[24:31], %[sidx]\n\t"
        "v_smfmac_f32_16x16x64_bf16 v[248:251], v[216:219], v[32:39], %[sidx]\n\t"
        "v_smfmac_f32_16x16x64_bf16 v[248:251], v[220:223], v[40:47], %[sidx]\n\t"
        "v_smfmac_f32_16x16x64_bf16 v[248:251], v[224:227], v[48:55], %[sidx]\n\t"
        "v_smfmac_f32_16x16x64_bf16 v[248:251], v[228:231], v[56:63], %[sidx]\n\t"
        "v_smfmac_f32_16x16x64_bf16 v[248:251], v[232:235], v[64:71], %[sidx]\n\t"
        "v_smfmac_f32_16x16x64_bf16 v[248:251], v[236:239], v[72:79], %[sidx]\n\t"
        "v_smfmac_f32_16x16x64_bf16 v[248:251], v[240:243], v[80:87], %[sidx]\n\t"
        "v_smfmac_f32_16x16x64_bf16 v[248:251], v[244:247], v[88:95], %[sidx]\n\t"
        "v_smfmac_f32_16x16x64_bf16 v[252:255], v[208:211], v[96:103], %[sidx]\n\t"
        "v_smfmac_f32_16x16x64_bf16 v[252:255], v[212:215], v[104:111], %[sidx]\n\t"
        "v_smfmac_f32_16x16x64_bf16 v[252:255], v[216:219], v[112:119], %[sidx]\n\t"
        "v_add_f32 v248, v248, v250\n\t"
        "v_smfmac_f32_16x16x64_bf16 v[252:255], v[220:223], v[120:127], %[sidx]\n\t"
        "v_add_f32 v249, v249, v251\n\t"
        "v_smfmac_f32_16x16x64_bf16 v[252:255], v[224:227], v[128:135], %[sidx]\n\t"
        "v_cvt_pk_bf16_f32 v200, v248, v249\n\t"
        "v_smfmac_f32_16x16x64_bf16 v[252:255], v[228:231], v[136:143], %[sidx]\n\t"
        "ds_write_b32 %[ow], v200\n\t"
        "v_smfmac_f32_16x16x64_bf16 v[252:255], v[232:235], v[144:151], %[sidx]\n\t"
        "v_cmp_u_f32 vcc, v248, v249\n\t"
        "v_smfmac_f32_16x16x64_bf16 v[252:255], v[236:239], v[152:159], %[sidx]\n\t"
        "v_smfmac_f32_16x16x64_bf16 v[252:255], v[240:243], v[160:167], %[sidx]\n\t"
        "v_smfmac_f32_16x16x64_bf16 v[252:255], v[244:247], v[168:175], %[sidx]\n\t"
        "s_nop 6\n\t"
        "s_cbranch_vccnz Lnan%=\n\t"
        "Lok%=:\n\t"
        "v_add_f32 v252, v252, v254\n\t"
        "v_add_f32 v253, v253, v255\n\t"
        "v_cvt_pk_bf16_f32 v201, v252, v253\n\t"
        "ds_write_b32 %[ow], v201 offset:3072\n\t"
        "s_barrier\n\t"
        "s_sleep 1\n\t"
        "ds_read_b128 v[240:243], %[ma] offset:0\n\t"
        "ds_read_b128 v[244:247], %[ma] offset:1024\n\t"
        "ds_read_b128 v[176:179], %[qa] offset:0\n\t"
        "ds_read_b128 v[180:183], %[qa] offset:1024\n\t"
        "ds_read_b128 v[184:187], %[qa] offset:2048\n\t"
        "ds_read_b128 v[188:191], %[qa] offset:3072\n\t"
        "ds_read_b128 v[208:211], %[ka] offset:0\n\t"
        "ds_read_b128 v[212:215], %[ka] offset:1024\n\t"
        "ds_read_b128 v[216:219], %[ka] offset:2048\n\t"
        "ds_read_b128 v[220:223], %[ka] offset:3072\n\t"
        "ds_read_b128 v[224:227], %[ka] offset:4096\n\t"
        "ds_read_b128 v[228:231], %[ka] offset:5120\n\t"
        "ds_read_b128 v[232:235], %[ka] offset:6144\n\t"
        "ds_read_b128 v[236:239], %[ka] offset:7168\n\t"
        "s_waitcnt lgkmcnt(7)\n\t"
        "v_mfma_f32_16x16x32_bf16 v[240:243], v[208:211], v[176:179], v[240:243]\n\t"
        "s_waitcnt lgkmcnt(6)\n\t"
        "v_mfma_f32_16x16x32_bf16 v[240:243], v[212:215], v[180:183], v[240:243]\n\t"
        "s_waitcnt lgkmcnt(5)\n\t"
        "v_mfma_f32_16x16x32_bf16 v[240:243], v[216:219], v[184:187], v[240:243]\n\t"
        "s_waitcnt lgkmcnt(4)\n\t"
        "v_mfma_f32_16x16x32_bf16 v[240:243], v[220:223], v[188:191], v[240:243]\n\t"
        "s_waitcnt lgkmcnt(3)\n\t"
        "v_mfma_f32_16x16x32_bf16 v[244:247], v[224:227], v[176:179], v[244:247]\n\t"
        "s_waitcnt lgkmcnt(2)\n\t"
        "v_mfma_f32_16x16x32_bf16 v[244:247], v[228:231], v[180:183], v[244:247]\n\t"
        "s_waitcnt lgkmcnt(1)\n\t"
        "v_mfma_f32_16x16x32_bf16 v[244:247], v[232:235], v[184:187], v[244:247]\n\t"
        "s_waitcnt lgkmcnt(0)\n\t"
        "v_mfma_f32_16x16x32_bf16 v[244:247], v[236:239], v[188:191], v[244:247]\n\t"
        "s_setprio 0\n\t"
        "ds_read_b32 v207, v206 offset:25104\n\t"
        "s_waitcnt lgkmcnt(0)\n\t"
        "v_cmp_eq_u32 vcc, %[rnd], v207\n\t"
        "s_barrier\n\t"
        "s_cbranch_vccnz Ltop%=\n\t"
        "ds_read_b64_tr_b16 v[208:209], %[ktr] offset:0\n\t"
        "ds_read_b64_tr_b16 v[210:211], %[ktr] offset:768\n\t"
        "ds_read_b64_tr_b16 v[212:213], %[ktr] offset:1536\n\t"
        "ds_read_b64_tr_b16 v[214:215], %[ktr] offset:2304\n\t"
        "ds_read_b64_tr_b16 v[216:217], %[ktr] offset:3072\n\t"
        "ds_read_b64_tr_b16 v[218:219], %[ktr] offset:3840\n\t"
        "ds_read_b64_tr_b16 v[220:221], %[ktr] offset:4608\n\t"
        "ds_read_b64_tr_b16 v[222:223], %[ktr] offset:5376\n\t"
        "s_waitcnt lgkmcnt(6)\n\t"
        "v_mfma_f32_16x16x32_bf16 v[248:251], v[208:211], v[176:179], 0\n\t"
        "s_waitcnt lgkmcnt(4)\n\t"
        "v_mfma_f32_16x16x32_bf16 v[248:251], v[212:215], v[180:183], v[248:251]\n\t"
        "s_waitcnt lgkmcnt(2)\n\t"
        "v_mfma_f32_16x16x32_bf16 v[248:251], v[216:219], v[184:187], v[248:251]\n\t"
        "s_waitcnt lgkmcnt(0)\n\t"
        "v_mfma_f32_16x16x32_bf16 v[248:251], v[220:223], v[188:191], v[248:251]\n\t"
        "v_max3_f32 v253, v240, v241, v242\n\t"
        "v_max3_f32 v253, v253, v243, v244\n\t"
        "v_max3_f32 v253, v253, v245, v246\n\t"
        "v_max_f32 v253, v253, v247\n\t"
        "s_nop 3\n\t"
        "v_mul_f32 v248, %[ce], v248\n\t"
        "v_mul_f32 v249, %[ce], v249\n\t"
        "v_max3_f32 v253, v253, v248, v249\n\t"
        "v_mov_b32 v254, v253\n\t"
        "s_nop 1\n\t"
        "v_permlane16_swap_b32 v253, v254\n\t"
        "v_max_f32 v253, v253, v254\n\t"
        "v_mov_b32 v254, v253\n\t"
        "s_nop 1\n\t"
        "v_permlane32_swap_b32 v253, v254\n\t"
        "v_max_f32 v253, v253, v254\n\t"
        "v_sub_f32 v240, v240, v253\n\t"
        "v_sub_f32 v241, v241, v253\n\t"
        "v_sub_f32 v242, v242, v253\n\t"
        "v_sub_f32 v243, v243, v253\n\t"
        "v_sub_f32 v244, v244, v253\n\t"
        "v_sub_f32 v245, v245, v253\n\t"
        "v_sub_f32 v246, v246, v253\n\t"
        "v_sub_f32 v247, v247, v253\n\t"
        "v_sub_f32 v248, v248, v253\n\t"
        "v_sub_f32 v249, v249, v253\n\t"
        "v_exp_f32 v240, v240\n\t"
        "v_exp_f32 v241, v241\n\t"
        "v_exp_f32 v242, v242\n\t"
        "v_exp_f32 v243, v243\n\t"
        "v_exp_f32 v244, v244\n\t"
        "v_exp_f32 v245, v245\n\t"
        "v_exp_f32 v246, v246\n\t"
        "v_exp_f32 v247, v247\n\t"
        "v_exp_f32 v248, v248\n\t"
        "v_exp_f32 v249, v249\n\t"
        "v_pk_add_f32 v[252:253], v[240:241], v[242:243]\n\t"
        "v_pk_add_f32 v[254:255], v[244:245], v[246:247]\n\t"
        "v_pk_add_f32 v[252:253], v[252:253], v[248:249]\n\t"
        "v_pk_add_f32 v[252:253], v[252:253], v[254:255]\n\t"
        "v_add_f32 v252, v252, v253\n\t"
        "v_mov_b32 v253, v252\n\t"
        "s_nop 1\n\t"
        "v_permlane16_swap_b32 v252, v253\n\t"
        "v_add_f32 v252, v252, v253\n\t"
        "v_mov_b32 v253, v252\n\t"
        "s_nop 1\n\t"
        "v_permlane32_swap_b32 v252, v253\n\t"
        "v_add_f32 v252, v252, v253\n\t"
        "v_rcp_f32 v252, v252\n\t"
        "s_nop 0\n\t"
        "v_pk_mul_f32 v[240:241], v[240:241], v[252:253] op_sel_hi:[1,0]\n\t"
        "v_pk_mul_f32 v[242:243], v[242:243], v[252:253] op_sel_hi:[1,0]\n\t"
        "v_pk_mul_f32 v[244:245], v[244:245], v[252:253] op_sel_hi:[1,0]\n\t"
        "v_pk_mul_f32 v[246:247], v[246:247], v[252:253] op_sel_hi:[1,0]\n\t"
        "v_pk_mul_f32 v[248:249], v[248:249], v[252:253] op_sel_hi:[1,0]\n\t"
        "v_cvt_pk_bf16_f32 v192, v240, v241\n\t"
        "v_cvt_pk_bf16_f32 v193, v242, v243\n\t"
        "v_cvt_pk_bf16_f32 v194, v244, v245\n\t"
        "v_cvt_pk_bf16_f32 v195, v246, v247\n\t"
        "v_cvt_pk_bf16_f32 v196, v248, v249\n\t"
        "s_branch Lend%=\n\t"
        "Lnan%=:\n\t"
        "v_max_u32 v207, v208, v209\n\t"
        "v_max3_u32 v207, v207, v210, v211\n\t"
        "v_max3_u32 v207, v207, v212, v213\n\t"
        "v_max3_u32 v207, v207, v214, v215\n\t"
        "v_max3_u32 v207, v207, v216, v217\n\t"
        "v_max3_u32 v207, v207, v218, v219\n\t"
        "v_max3_u32 v207, v207, v220, v221\n\t"
        "v_max3_u32 v207, v207, v222, v223\n\t"
        "v_max3_u32 v207, v207, v224, v225\n\t"
        "v_max3_u32 v207, v207, v226, v227\n\t"
        "v_max3_u32 v207, v207, v228, v229\n\t"
        "v_max3_u32 v207, v207, v230, v231\n\t"
        "v_max3_u32 v207, v207, v232, v233\n\t"
        "v_max3_u32 v207, v207, v234, v235\n\t"
        "v_max3_u32 v207, v207, v236, v237\n\t"
        "v_max3_u32 v207, v207, v238, v239\n\t"
        "v_max3_u32 v207, v207, v240, v241\n\t"
        "v_max3_u32 v207, v207, v242, v243\n\t"
        "v_max3_u32 v207, v207, v244, v245\n\t"
        "v_max3_u32 v207, v207, v246, v247\n\t"
        "v_cmp_eq_u32 vcc, 0xffbfffbf, v207\n\t"
        "v_mov_b32 v207, %[rnd]\n\t"
        "s_cbranch_vccz Lok%=\n\t"
        "ds_write_b32 v206, v207 offset:25104\n\t"
        "s_waitcnt lgkmcnt(0)\n\t"
        "s_barrier\n\t"
        "s_barrier\n\t"
        "s_branch Ltop%=\n\t"
        "Lend%=:\n\t"
        : [rnd] "+&s"(rnd), [p0] "+{v[192:195]}"(p0), [p1] "+{v196}"(p1)
        : [w0] "{v[16:47]}"(W[0]), [w1] "{v[48:79]}"(W[1]), [w2] "{v[80:111]}"(W[2]), [w3] "{v[112:143]}"(W[3]), [w4] "{v[144:175]}"(W[4]), [va] "v"(va), [sidx] "v"(sidx), [ow] "v"(ow), [lb] "s"(lb), [ka] "v"(ka), [qa] "v"(qa), [ma] "v"(ma), [ktr] "v"(ktr), [o0] "v"(o0), [o1] "v"(o1), [ce] "s"(ce), [rs] "s"(rs)
        : "v176", "v177", "v178", "v179", "v180", "v181", "v182", "v183", "v184", "v185", "v186", "v187", "v188", "v189", "v190", "v191", "v200", "v201", "v202", "v203", "v204", "v205", "v206", "v207", "v208", "v209", "v210", "v211", "v212", "v213", "v214", "v215", "v216", "v217", "v218", "v219", "v220", "v221", "v222", "v223", "v224", "v225", "v226", "v227", "v228", "v229", "v230", "v231", "v232", "v233", "v234", "v235", "v236", "v237", "v238", "v239", "v240", "v241", "v242", "v243", "v244", "v245", "v246", "v247", "v248", "v249", "v250", "v251", "v252", "v253", "v254", "v255", "vcc", "scc", "memory");
}

__device__ __forceinline__ void s_k2(const u32x32 (&W)[5], u32& rnd, u32 va, u32 sidx, u32 ow, u32 lb) {
    asm volatile(
        "v_mov_b64 v[248:249], 0\n\t"
        "v_mov_b64 v[250:251], 0\n\t"
        "v_mov_b64 v[252:253], 0\n\t"
        "v_mov_b64 v[254:255], 0\n\t"
        "v_mov_b32 v200, 0xffbfffbf\n\t"
        "v_mov_b32 v206, %[lb]\n\t"
        "s_add_u32 %[rnd], %[rnd], 1\n\t"
        "s_setprio 1\n\t"
        "s_waitcnt vmcnt(0) lgkmcnt(0)\n\t"
        "s_barrier\n\t"
        "s_branch Lmk%=\n\t"
        "Ltop%=:\n\t"
        "v_mov_b64 v[248:249], 0\n\t"
        "v_mov_b64 v[250:251], 0\n\t"
        "v_mov_b64 v[252:253], 0\n\t"
        "v_mov_b64 v[254:255], 0\n\t"
        "v_mov_b32 v200, 0xffbfffbf\n\t"
        "v_mov_b32 v206, %[lb]\n\t"
        "s_add_u32 %[rnd], %[rnd], 1\n\t"
        "s_setprio 1\n\t"
        "Lmk%=:\n\t"
        "ds_write_b32 %[ow], v200\n\t"
        "ds_write_b32 %[ow], v200 offset:3072\n\t"
        "s_waitcnt lgkmcnt(0)\n\t"
        "s_barrier\n\t"
        "ds_read_b128 v[208:211], %[va] offset:0\n\t"
        "ds_read_b128 v[212:215], %[va] offset:1024\n\t"
        "ds_read_b128 v[216:219], %[va] offset:2048\n\t"
        "ds_read_b128 v[220:223], %[va] offset:3072\n\t"
        "ds_read_b128 v[224:227], %[va] offset:4096\n\t"
        "ds_read_b128 v[228:231], %[va] offset:5120\n\t"
        "ds_read_b128 v[232:235], %[va] offset:6144\n\t"
        "ds_read_b128 v[236:239], %[va] offset:7168\n\t"
        "ds_read_b128 v[240:243], %[va] offset:8192\n\t"
        "ds_read_b128 v[244:247], %[va] offset:10240\n\t"
        "s_waitcnt lgkmcnt(9)\n\t"
        "v_smfmac_f32_16x16x64_bf16 v[248:251], v[208:211], v[16:23], %[sidx]\n\t"
        "s_waitcnt lgkmcnt(8)\n\t"
        "v_smfmac_f32_16x16x64_bf16 v[248:251], v[212:215], v[24:31], %[sidx]\n\t"
        "s_waitcnt lgkmcnt(7)\n\t"
        "v_smfmac_f32_16x16x64_bf16 v[248:251], v[216:219], v[32:39], %[sidx]\n\t"
        "s_waitcnt lgkmcnt(6)\n\t"
        "v_smfmac_f32_16x16x64_bf16 v[248:251], v[220:223], v[40:47], %[sidx]\n\t"
        "s_waitcnt lgkmcnt(5)\n\t"
        "v_smfmac_f32_16x16x64_bf16 v[248:251], v[224:227], v[48:55], %[sidx]\n\t"
        "s_waitcnt lgkmcnt(4)\n\t"
        "v_smfmac_f32_16x16x64_bf16 v[248:251], v[228:231], v[56:63], %[sidx]\n\t"
        "s_waitcnt lgkmcnt(3)\n\t"
        "v_smfmac_f32_16x16x64_bf16 v[248:251], v[232:235], v[64:71], %[sidx]\n\t"
        "s_waitcnt lgkmcnt(2)\n\t"
        "v_smfmac_f32_16x16x64_bf16 v[248:251], v[236:239], v[72:79], %[sidx]\n\t"
        "s_waitcnt lgkmcnt(1)\n\t"
        "v_smfmac_f32_16x16x64_bf16 v[248:251], v[240:243], v[80:87], %[sidx]\n\t"
        "s_waitcnt lgkmcnt(0)\n\t"
        "v_smfmac_f32_16x16x64_bf16 v[248:251], v[244:247], v[88:95], %[sidx]\n\t"
        "v_smfmac_f32_16x16x64_bf16 v[252:255], v[208:211], v[96:103], %[sidx]\n\t"
        "v_smfmac_f32_16x16x64_bf16 v[252:255], v[212:215], v[104:111], %[sidx]\n\t"
        "v_smfmac_f32_16x16x64_bf16 v[252:255], v[216:219], v[112:119], %[sidx]\n\t"
        "v_add_f32 v248, v248, v250\n\t"
        "v_smfmac_f32_16x16x64_bf16 v[252:255], v[220:223], v[120:127], %[sidx]\n\t"
        "v_add_f32 v249, v249, v251\n\t"
        "v_smfmac_f32_16x16x64_bf16 v[252:255], v[224:227], v[128:135], %[sidx]\n\t"
        "v_cvt_pk_bf16_f32 v200, v248, v249\n\t"
        "v_smfmac_f32_16x16x64_bf16 v[252:255], v[228:231], v[136:143], %[sidx]\n\t"
        "ds_write_b32 %[ow], v200\n\t"
        "v_smfmac_f32_16x16x64_bf16 v[252:255], v[232:235], v[144:151], %[sidx]\n\t"
        "v_cmp_u_f32 vcc, v248, v249\n\t"
        "v_smfmac_f32_16x16x64_bf16 v[252:255], v[236:239], v[152:159], %[sidx]\n\t"
        "v_smfmac_f32_16x16x64_bf16 v[252:255], v[240:243], v[160:167], %[sidx]\n\t"
        "v_smfmac_f32_16x16x64_bf16 v[252:255], v[244:247], v[168:175], %[sidx]\n\t"
        "s_nop 6\n\t"
        "s_cbranch_vccnz Lnan%=\n\t"
        "Lok%=:\n\t"
        "v_add_f32 v252, v252, v254\n\t"
        "v_add_f32 v253, v253, v255\n\t"
        "v_cvt_pk_bf16_f32 v201, v252, v253\n\t"
        "ds_write_b32 %[ow], v201 offset:3072\n\t"
        "s_barrier\n\t"
        "s_setprio 0\n\t"
        "ds_read_b32 v207, v206 offset:25104\n\t"
        "s_waitcnt lgkmcnt(0)\n\t"
        "v_cmp_eq_u32 vcc, %[rnd], v207\n\t"
        "s_barrier\n\t"
        "s_cbranch_vccnz Ltop%=\n\t"
        "s_branch Lend%=\n\t"
        "Lnan%=:\n\t"
        "v_max_u32 v207, v208, v209\n\t"
        "v_max3_u32 v207, v207, v210, v211\n\t"
        "v_max3_u32 v207, v207, v212, v213\n\t"
        "v_max3_u32 v207, v207, v214, v215\n\t"
        "v_max3_u32 v207, v207, v216, v217\n\t"
        "v_max3_u32 v207, v207, v218, v219\n\t"
        "v_max3_u32 v207, v207, v220, v221\n\t"
        "v_max3_u32 v207, v207, v222, v223\n\t"
        "v_max3_u32 v207, v207, v224, v225\n\t"
        "v_max3_u32 v207, v207, v226, v227\n\t"
        "v_max3_u32 v207, v207, v228, v229\n\t"
        "v_max3_u32 v207, v207, v230, v231\n\t"
        "v_max3_u32 v207, v207, v232, v233\n\t"
        "v_max3_u32 v207, v207, v234, v235\n\t"
        "v_max3_u32 v207, v207, v236, v237\n\t"
        "v_max3_u32 v207, v207, v238, v239\n\t"
        "v_max3_u32 v207, v207, v240, v241\n\t"
        "v_max3_u32 v207, v207, v242, v243\n\t"
        "v_max3_u32 v207, v207, v244, v245\n\t"
        "v_max3_u32 v207, v207, v246, v247\n\t"
        "v_cmp_eq_u32 vcc, 0xffbfffbf, v207\n\t"
        "v_mov_b32 v207, %[rnd]\n\t"
        "s_cbranch_vccz Lok%=\n\t"
        "ds_write_b32 v206, v207 offset:25104\n\t"
        "s_waitcnt lgkmcnt(0)\n\t"
        "s_barrier\n\t"
        "s_barrier\n\t"
        "s_branch Ltop%=\n\t"
        "Lend%=:\n\t"
        : [rnd] "+&s"(rnd)
        : [w0] "{v[16:47]}"(W[0]), [w1] "{v[48:79]}"(W[1]), [w2] "{v[80:111]}"(W[2]), [w3] "{v[112:143]}"(W[3]), [w4] "{v[144:175]}"(W[4]), [va] "v"(va), [sidx] "v"(sidx), [ow] "v"(ow), [lb] "s"(lb)
        : "v200", "v201", "v202", "v203", "v204", "v205", "v206", "v207", "v208", "v209", "v210", "v211", "v212", "v213", "v214", "v215", "v216", "v217", "v218", "v219", "v220", "v221", "v222", "v223", "v224", "v225", "v226", "v227", "v228", "v229", "v230", "v231", "v232", "v233", "v234", "v235", "v236", "v237", "v238", "v239", "v240", "v241", "v242", "v243", "v244", "v245", "v246", "v247", "v248", "v249", "v250", "v251", "v252", "v253", "v254", "v255", "vcc", "scc", "memory");
}

__device__ __forceinline__ void s_k3(const u32x32 (&W)[5], u32& rnd, u32 va, u32 sidx, u32 ow, u32 lb, u32x4& p0, u32& p1, u32 ka, u32 qa, u32 ma, u32 ktr, u32 o0, u32 o1, float ce, u32x4 rs, u32 fo) {
    asm volatile(
        "v_mov_b64 v[248:249], 0\n\t"
        "v_mov_b64 v[250:251], 0\n\t"
        "v_mov_b64 v[252:253], 0\n\t"
        "v_mov_b64 v[254:255], 0\n\t"
        "v_mov_b32 v200, 0xffbfffbf\n\t"
        "v_mov_b32 v206, %[lb]\n\t"
        "s_add_u32 %[rnd], %[rnd], 1\n\t"
        "s_setprio 1\n\t"
        "s_waitcnt vmcnt(0) lgkmcnt(0)\n\t"
        "s_barrier\n\t"
        "buffer_store_dwordx4 v[192:195], %[o0], %[rs], 0 offen sc1\n\t"
        "buffer_store_dword v196, %[o1], %[rs], 0 offen sc1\n\t"
        "s_branch Lmk%=\n\t"
        "Ltop%=:\n\t"
        "v_mov_b64 v[248:249], 0\n\t"
        "v_mov_b64 v[250:251], 0\n\t"
        "v_mov_b64 v[252:253], 0\n\t"
        "v_mov_b64 v[254:255], 0\n\t"
        "v_mov_b32 v200, 0xffbfffbf\n\t"
        "v_mov_b32 v206, %[lb]\n\t"
        "s_add_u32 %[rnd], %[rnd], 1\n\t"
        "s_setprio 1\n\t"
        "Lmk%=:\n\t"
        "ds_write_b32 %[ow], v200\n\t"
        "ds_write_b32 %[ow], v200 offset:3072\n\t"
        "s_waitcnt lgkmcnt(0)\n\t"
        "s_barrier\n\t"
        "ds_read_b128 v[208:211], %[va] offset:0\n\t"
        "ds_read_b128 v[212:215], %[va] offset:1024\n\t"
        "ds_read_b128 v[216:219], %[va] offset:2048\n\t"
        "ds_read_b128 v[220:223], %[va] offset:3072\n\t"
        "ds_read_b128 v[224:227], %[va] offset:4096\n\t"
        "ds_read_b128 v[228:231], %[va] offset:5120\n\t"
        "ds_read_b128 v[232:235], %[va] offset:6144\n\t"
        "ds_read_b128 v[236:239], %[va] offset:7168\n\t"
        "ds_read_b128 v[240:243], %[va] offset:8192\n\t"
        "ds_read_b128 v[244:247], %[va] offset:10240\n\t"
        "s_waitcnt lgkmcnt(9)\n\t"
        "v_smfmac_f32_16x16x64_bf16 v[248:251], v[208:211], v[16:23], %[sidx]\n\t"
        "s_waitcnt lgkmcnt(8)\n\t"
        "v_smfmac_f32_16x16x64_bf16 v[248:251], v[212:215], v[24:31], %[sidx]\n\t"
        "s_waitcnt lgkmcnt(7)\n\t"
        "v_smfmac_f32_16x16x64_bf16 v[248:251], v[216:219], v[32:39], %[sidx]\n\t"
        "s_waitcnt lgkmcnt(6)\n\t"
        "v_smfmac_f32_16x16x64_bf16 v[248:251], v[220:223], v[40:47], %[sidx]\n\t"
        "s_waitcnt lgkmcnt(5)\n\t"
        "v_smfmac_f32_16x16x64_bf16 v[248:251], v[224:227], v[48:55], %[sidx]\n\t"
        "s_waitcnt lgkmcnt(4)\n\t"
        "v_smfmac_f32_16x16x64_bf16 v[248:251], v[228:231], v[56:63], %[sidx]\n\t"
        "s_waitcnt lgkmcnt(3)\n\t"
        "v_smfmac_f32_16x16x64_bf16 v[248:251], v[232:235], v[64:71], %[sidx]\n\t"
        "s_waitcnt lgkmcnt(2)\n\t"
        "v_smfmac_f32_16x16x64_bf16 v[248:251], v[236:239], v[72:79], %[sidx]\n\t"
        "s_waitcnt lgkmcnt(1)\n\t"
        "v_smfmac_f32_16x16x64_bf16 v[248:251], v[240:243], v[80:87], %[sidx]\n\t"
        "s_waitcnt lgkmcnt(0)\n\t"
        "v_smfmac_f32_16x16x64_bf16 v[248:251], v[244:247], v[88:95], %[sidx]\n\t"
        "v_smfmac_f32_16x16x64_bf16 v[252:255], v[208:211], v[96:103], %[sidx]\n\t"
        "v_smfmac_f32_16x16x64_bf16 v[252:255], v[212:215], v[104:111], %[sidx]\n\t"
        "v_smfmac_f32_16x16x64_bf16 v[252:255], v[216:219], v[112:119], %[sidx]\n\t"
        "v_add_f32 v248, v248, v250\n\t"
        "v_smfmac_f32_16x16x64_bf16 v[252:255], v[220:223], v[120:127], %[sidx]\n\t"
        "v_add_f32 v249, v249, v251\n\t"
        "v_smfmac_f32_16x16x64_bf16 v[252:255], v[224:227], v[128:135], %[sidx]\n\t"
        "v_cvt_pk_bf16_f32 v200, v248, v249\n\t"
        "v_smfmac_f32_16x16x64_bf16 v[252:255], v[228:231], v[136:143], %[sidx]\n\t"
        "ds_write_b32 %[ow], v200\n\t"
        "v_smfmac_f32_16x16x64_bf16 v[252:255], v[232:235], v[144:151], %[sidx]\n\t"
        "v_cmp_u_f32 vcc, v248, v249\n\t"
        "v_smfmac_f32_16x16x64_bf16 v[252:255], v[236:239], v[152:159], %[sidx]\n\t"
        "v_smfmac_f32_16x16x64_bf16 v[252:255], v[240:243], v[160:167], %[sidx]\n\t"
        "v_smfmac_f32_16x16x64_bf16 v[252:255], v[244:247], v[168:175], %[sidx]\n\t"
        "s_nop 6\n\t"
        "s_cbranch_vccnz Lnan%=\n\t"
        "Lok%=:\n\t"
        "v_add_f32 v252, v252, v254\n\t"
        "v_add_f32 v253, v253, v255\n\t"
        "v_cvt_pk_bf16_f32 v201, v252, v253\n\t"
        "ds_write_b32 %[ow], v201 offset:3072\n\t"
        "s_barrier\n\t"
        "ds_read_b128 v[240:243], %[ma] offset:0\n\t"
        "ds_read_b128 v[244:247], %[ma] offset:1024\n\t"
        "ds_read_b128 v[176:179], %[qa] offset:0\n\t"
        "ds_read_b128 v[180:183], %[qa] offset:1024\n\t"
        "ds_read_b128 v[184:187], %[qa] offset:2048\n\t"
        "ds_read_b128 v[188:191], %[qa] offset:3072\n\t"
        "ds_read_b128 v[208:211], %[ka] offset:0\n\t"
        "ds_read_b128 v[212:215], %[ka] offset:1024\n\t"
        "ds_read_b128 v[216:219], %[ka] offset:2048\n\t"
        "ds_read_b128 v[220:223], %[ka] offset:3072\n\t"
        "ds_read_b128 v[224:227], %[ka] offset:4096\n\t"
        "ds_read_b128 v[228:231], %[ka] offset:5120\n\t"
        "ds_read_b128 v[232:235], %[ka] offset:6144\n\t"
        "ds_read_b128 v[236:239], %[ka] offset:7168\n\t"
        "s_waitcnt lgkmcnt(7)\n\t"
        "v_mfma_f32_16x16x32_bf16 v[240:243], v[208:211], v[176:179], v[240:243]\n\t"
        "s_waitcnt lgkmcnt(6)\n\t"
        "v_mfma_f32_16x16x32_bf16 v[240:243], v[212:215], v[180:183], v[240:243]\n\t"
        "s_waitcnt lgkmcnt(5)\n\t"
        "v_mfma_f32_16x16x32_bf16 v[240:243], v[216:219], v[184:187], v[240:243]\n\t"
        "s_waitcnt lgkmcnt(4)\n\t"
        "v_mfma_f32_16x16x32_bf16 v[240:243], v[220:223], v[188:191], v[240:243]\n\t"
        "s_waitcnt lgkmcnt(3)\n\t"
        "v_mfma_f32_16x16x32_bf16 v[244:247], v[224:227], v[176:179], v[244:247]\n\t"
        "s_waitcnt lgkmcnt(2)\n\t"
        "v_mfma_f32_16x16x32_bf16 v[244:247], v[228:231], v[180:183], v[244:247]\n\t"
        "s_waitcnt lgkmcnt(1)\n\t"
        "v_mfma_f32_16x16x32_bf16 v[244:247], v[232:235], v[184:187], v[244:247]\n\t"
        "s_waitcnt lgkmcnt(0)\n\t"
        "v_mfma_f32_16x16x32_bf16 v[244:247], v[236:239], v[188:191], v[244:247]\n\t"
        "s_setprio 0\n\t"
        "ds_read_b32 v207, v206 offset:25104\n\t"
        "s_waitcnt lgkmcnt(0)\n\t"
        "v_cmp_eq_u32 vcc, %[rnd], v207\n\t"
        "s_barrier\n\t"
        "s_cbranch_vccnz Ltop%=\n\t"
        "ds_read_b64_tr_b16 v[208:209], %[ktr] offset:0\n\t"
        "ds_read_b64_tr_b16 v[210:211], %[ktr] offset:768\n\t"
        "ds_read_b64_tr_b16 v[212:213], %[ktr] offset:1536\n\t"
        "ds_read_b64_tr_b16 v[214:215], %[ktr] offset:2304\n\t"
        "ds_read_b64_tr_b16 v[216:217], %[ktr] offset:3072\n\t"
        "ds_read_b64_tr_b16 v[218:219], %[ktr] offset:3840\n\t"
        "ds_read_b64_tr_b16 v[220:221], %[ktr] offset:4608\n\t"
        "ds_read_b64_tr_b16 v[222:223], %[ktr] offset:5376\n\t"
        "v_mov_b32 v200, 1\n\t"
        "s_waitcnt vmcnt(0)\n\t"
        "buffer_store_dword v200, %[fo], %[rs], 0 offen sc1\n\t"
        "s_waitcnt lgkmcnt(6)\n\t"
        "v_mfma_f32_16x16x32_bf16 v[248:251], v[208:211], v[176:179], 0\n\t"
        "s_waitcnt lgkmcnt(4)\n\t"
        "v_mfma_f32_16x16x32_bf16 v[248:251], v[212:215], v[180:183], v[248:251]\n\t"
        "s_waitcnt lgkmcnt(2)\n\t"
        "v_mfma_f32_16x16x32_bf16 v[248:251], v[216:219], v[184:187], v[248:251]\n\t"
        "s_waitcnt lgkmcnt(0)\n\t"
        "v_mfma_f32_16x16x32_bf16 v[248:251], v[220:223], v[188:191], v[248:251]\n\t"
        "v_max3_f32 v253, v240, v241, v242\n\t"
        "v_max3_f32 v253, v253, v243, v244\n\t"
        "v_max3_f32 v253, v253, v245, v246\n\t"
        "v_max_f32 v253, v253, v247\n\t"
        "s_nop 3\n\t"
        "v_mul_f32 v248, %[ce], v248\n\t"
        "v_mul_f32 v249, %[ce], v249\n\t"
        "v_max3_f32 v253, v253, v248, v249\n\t"
        "v_mov_b32 v254, v253\n\t"
        "s_nop 1\n\t"
        "v_permlane16_swap_b32 v253, v254\n\t"
        "v_max_f32 v253, v253, v254\n\t"
        "v_mov_b32 v254, v253\n\t"
        "s_nop 1\n\t"
        "v_permlane32_swap_b32 v253, v254\n\t"
        "v_max_f32 v253, v253, v254\n\t"
        "v_sub_f32 v240, v240, v253\n\t"
        "v_sub_f32 v241, v241, v253\n\t"
        "v_sub_f32 v242, v242, v253\n\t"
        "v_sub_f32 v243, v243, v253\n\t"
        "v_sub_f32 v244, v244, v253\n\t"
        "v_sub_f32 v245, v245, v253\n\t"
        "v_sub_f32 v246, v246, v253\n\t"
        "v_sub_f32 v247, v247, v253\n\t"
        "v_sub_f32 v248, v248, v253\n\t"
        "v_sub_f32 v249, v249, v253\n\t"
        "v_exp_f32 v240, v240\n\t"
        "v_exp_f32 v241, v241\n\t"
        "v_exp_f32 v242, v242\n\t"
        "v_exp_f32 v243, v243\n\t"
        "v_exp_f32 v244, v244\n\t"
        "v_exp_f32 v245, v245\n\t"
        "v_exp_f32 v246, v246\n\t"
        "v_exp_f32 v247, v247\n\t"
        "v_exp_f32 v248, v248\n\t"
        "v_exp_f32 v249, v249\n\t"
        "v_pk_add_f32 v[252:253], v[240:241], v[242:243]\n\t"
        "v_pk_add_f32 v[254:255], v[244:245], v[246:247]\n\t"
        "v_pk_add_f32 v[252:253], v[252:253], v[248:249]\n\t"
        "v_pk_add_f32 v[252:253], v[252:253], v[254:255]\n\t"
        "v_add_f32 v252, v252, v253\n\t"
        "v_mov_b32 v253, v252\n\t"
        "s_nop 1\n\t"
        "v_permlane16_swap_b32 v252, v253\n\t"
        "v_add_f32 v252, v252, v253\n\t"
        "v_mov_b32 v253, v252\n\t"
        "s_nop 1\n\t"
        "v_permlane32_swap_b32 v252, v253\n\t"
        "v_add_f32 v252, v252, v253\n\t"
        "v_rcp_f32 v252, v252\n\t"
        "s_nop 0\n\t"
        "v_pk_mul_f32 v[240:241], v[240:241], v[252:253] op_sel_hi:[1,0]\n\t"
        "v_pk_mul_f32 v[242:243], v[242:243], v[252:253] op_sel_hi:[1,0]\n\t"
        "v_pk_mul_f32 v[244:245], v[244:245], v[252:253] op_sel_hi:[1,0]\n\t"
        "v_pk_mul_f32 v[246:247], v[246:247], v[252:253] op_sel_hi:[1,0]\n\t"
        "v_pk_mul_f32 v[248:249], v[248:249], v[252:253] op_sel_hi:[1,0]\n\t"
        "v_cvt_pk_bf16_f32 v192, v240, v241\n\t"
        "v_cvt_pk_bf16_f32 v193, v242, v243\n\t"
        "v_cvt_pk_bf16_f32 v194, v244, v245\n\t"
        "v_cvt_pk_bf16_f32 v195, v246, v247\n\t"
        "v_cvt_pk_bf16_f32 v196, v248, v249\n\t"
        "s_branch Lend%=\n\t"
        "Lnan%=:\n\t"
        "v_max_u32 v207, v208, v209\n\t"
        "v_max3_u32 v207, v207, v210, v211\n\t"
        "v_max3_u32 v207, v207, v212, v213\n\t"
        "v_max3_u32 v207, v207, v214, v215\n\t"
        "v_max3_u32 v207, v207, v216, v217\n\t"
        "v_max3_u32 v207, v207, v218, v219\n\t"
        "v_max3_u32 v207, v207, v220, v221\n\t"
        "v_max3_u32 v207, v207, v222, v223\n\t"
        "v_max3_u32 v207, v207, v224, v225\n\t"
        "v_max3_u32 v207, v207, v226, v227\n\t"
        "v_max3_u32 v207, v207, v228, v229\n\t"
        "v_max3_u32 v207, v207, v230, v231\n\t"
        "v_max3_u32 v207, v207, v232, v233\n\t"
        "v_max3_u32 v207, v207, v234, v235\n\t"
        "v_max3_u32 v207, v207, v236, v237\n\t"
        "v_max3_u32 v207, v207, v238, v239\n\t"
        "v_max3_u32 v207, v207, v240, v241\n\t"
        "v_max3_u32 v207, v207, v242, v243\n\t"
        "v_max3_u32 v207, v207, v244, v245\n\t"
        "v_max3_u32 v207, v207, v246, v247\n\t"
        "v_cmp_eq_u32 vcc, 0xffbfffbf, v207\n\t"
        "v_mov_b32 v207, %[rnd]\n\t"
        "s_cbranch_vccz Lok%=\n\t"
        "ds_write_b32 v206, v207 offset:25104\n\t"
        "s_waitcnt lgkmcnt(0)\n\t"
        "s_barrier\n\t"
        "s_barrier\n\t"
        "s_branch Ltop%=\n\t"
        "Lend%=:\n\t"
        : [rnd] "+&s"(rnd), [p0] "+{v[192:195]}"(p0), [p1] "+{v196}"(p1)
        : [w0] "{v[16:47]}"(W[0]), [w1] "{v[48:79]}"(W[1]), [w2] "{v[80:111]}"(W[2]), [w3] "{v[112:143]}"(W[3]), [w4] "{v[144:175]}"(W[4]), [va] "v"(va), [sidx] "v"(sidx), [ow] "v"(ow), [lb] "s"(lb), [ka] "v"(ka), [qa] "v"(qa), [ma] "v"(ma), [ktr] "v"(ktr), [o0] "v"(o0), [o1] "v"(o1), [ce] "s"(ce), [rs] "s"(rs), [fo] "v"(fo)
        : "v176", "v177", "v178", "v179", "v180", "v181", "v182", "v183", "v184", "v185", "v186", "v187", "v188", "v189", "v190", "v191", "v200", "v201", "v202", "v203", "v204", "v205", "v206", "v207", "v208", "v209", "v210", "v211", "v212", "v213", "v214", "v215", "v216", "v217", "v218", "v219", "v220", "v221", "v222", "v223", "v224", "v225", "v226", "v227", "v228", "v229", "v230", "v231", "v232", "v233", "v234", "v235", "v236", "v237", "v238", "v239", "v240", "v241", "v242", "v243", "v244", "v245", "v246", "v247", "v248", "v249", "v250", "v251", "v252", "v253", "v254", "v255", "vcc", "scc", "memory");
}

__device__ __forceinline__ void s_l(u32& rnd, u32& redo, u32 lb) {
    asm volatile(
        "v_mov_b32 v206, %[lb]\n\t"
        "s_add_u32 %[rnd], %[rnd], 1\n\t"
        "s_mov_b32 %[redo], 1\n\t"
        "s_barrier\n\t"
        "s_barrier\n\t"
        "s_setprio 0\n\t"
        "ds_read_b32 v207, v206 offset:25104\n\t"
        "s_waitcnt lgkmcnt(0)\n\t"
        "v_cmp_eq_u32 vcc, %[rnd], v207\n\t"
        "s_barrier\n\t"
        "s_cbranch_vccnz Lend%=\n\t"
        "s_mov_b32 %[redo], 0\n\t"
        "Lend%=:\n\t"
        : [rnd] "+&s"(rnd), [redo] "=&s"(redo)
        : [lb] "s"(lb)
        : "v206", "v207", "vcc", "scc", "memory");
}

__device__ __forceinline__ void s_e(const u32x32 (&W)[5], u32& rnd, u32 va, u32 sidx, u32 ow, u32 lb, u32 kxa, u32 qtr, u32 qo, u32 eo, u32 ew, float lim, u32x4 rs, u32 so STP) {
    asm volatile(
        "s_add_u32 s92, %[so], 0x1000\n\t"
        "s_add_u32 s93, %[so], 0x4000\n\t"
        "s_add_u32 s94, %[so], 0x5000\n\t"
        "v_mov_b64 v[248:249], 0\n\t"
        "v_mov_b64 v[250:251], 0\n\t"
        "v_mov_b64 v[252:253], 0\n\t"
        "v_mov_b64 v[254:255], 0\n\t"
        "v_mov_b32 v200, 0xffbfffbf\n\t"
        "v_mov_b32 v206, %[lb]\n\t"
        "s_add_u32 %[rnd], %[rnd], 1\n\t"
        "s_setprio 3\n\t"
        "s_mov_b32 s95, 1\n\t"
        "s_waitcnt vmcnt(0) lgkmcnt(0)\n\t"
        "s_barrier\n\t"
        "ds_read_b128 v[176:179], %[kxa] offset:0\n\t"
        "ds_read_b128 v[180:183], %[kxa] offset:1024\n\t"
        "ds_read_b128 v[184:187], %[kxa] offset:2048\n\t"
        "ds_read_b128 v[188:191], %[kxa] offset:3072\n\t"
        "s_branch Lmk%=\n\t"
        "Ltop%=:\n\t"
        "v_mov_b64 v[248:249], 0\n\t"
        "v_mov_b64 v[250:251], 0\n\t"
        "v_mov_b64 v[252:253], 0\n\t"
        "v_mov_b64 v[254:255], 0\n\t"
        "v_mov_b32 v200, 0xffbfffbf\n\t"
        "v_mov_b32 v206, %[lb]\n\t"
        "s_add_u32 %[rnd], %[rnd], 1\n\t"
        "s_setprio 3\n\t"
        "s_mov_b32 s95, 1\n\t"
        "Lmk%=:\n\t"
        "ds_write_b32 %[ow], v200\n\t"
        "ds_write_b32 %[ow], v200 offset:3072\n\t"
        "s_waitcnt lgkmcnt(0)\n\t"
        "s_barrier\n\t"
        "ds_read_b128 v[208:211], %[va] offset:0\n\t"
        "ds_read_b128 v[212:215], %[va] offset:1024\n\t"
        "ds_read_b128 v[216:219], %[va] offset:2048\n\t"
        "ds_read_b128 v[220:223], %[va] offset:3072\n\t"
        "ds_read_b128 v[224:227], %[va] offset:4096\n\t"
        "ds_read_b128 v[228:231], %[va] offset:5120\n\t"
        "s_waitcnt lgkmcnt(5)\n\t"
        "v_smfmac_f32_16x16x64_bf16 v[248:251], v[208:211], v[16:23], %[sidx]\n\t"
        "ds_read_b128 v[232:235], %[va] offset:6144\n\t"
        "v_smfmac_f32_16x16x64_bf16 v[252:255], v[208:211], v[96:103], %[sidx]\n\t"
        "s_waitcnt lgkmcnt(5)\n\t"
        "v_smfmac_f32_16x16x64_bf16 v[248:251], v[212:215], v[24:31], %[sidx]\n\t"
        "ds_read_b128 v[236:239], %[va] offset:7168\n\t"
        "v_smfmac_f32_16x16x64_bf16 v[252:255], v[212:215], v[104:111], %[sidx]\n\t"
        "s_waitcnt lgkmcnt(5)\n\t"
        "v_smfmac_f32_16x16x64_bf16 v[248:251], v[216:219], v[32:39], %[sidx]\n\t"
        "ds_read_b128 v[240:243], %[va] offset:8192\n\t"
        "v_smfmac_f32_16x16x64_bf16 v[252:255], v[216:219], v[112:119], %[sidx]\n\t"
        "s_waitcnt lgkmcnt(5)\n\t"
        "v_smfmac_f32_16x16x64_bf16 v[248:251], v[220:223], v[40:47], %[sidx]\n\t"
        "ds_read_b128 v[244:247], %[va] offset:10240\n\t"
        "v_smfmac_f32_16x16x64_bf16 v[252:255], v[220:223], v[120:127], %[sidx]\n\t"
        "s_waitcnt lgkmcnt(5)\n\t"
        "v_smfmac_f32_16x16x64_bf16 v[248:251], v[224:227], v[48:55], %[sidx]\n\t"
        "s_waitcnt lgkmcnt(4)\n\t"
        "v_smfmac_f32_16x16x64_bf16 v[248:251], v[228:231], v[56:63], %[sidx]\n\t"
        "s_waitcnt lgkmcnt(3)\n\t"
        "v_smfmac_f32_16x16x64_bf16 v[248:251], v[232:235], v[64:71], %[sidx]\n\t"
        "s_waitcnt lgkmcnt(2)\n\t"
        "v_smfmac_f32_16x16x64_bf16 v[248:251], v[236:239], v[72:79], %[sidx]\n\t"
        "s_waitcnt lgkmcnt(1)\n\t"
        "v_smfmac_f32_16x16x64_bf16 v[248:251], v[240:243], v[80:87], %[sidx]\n\t"
        "s_waitcnt lgkmcnt(0)\n\t"
        "v_smfmac_f32_16x16x64_bf16 v[248:251], v[244:247], v[88:95], %[sidx]\n\t"
        "v_smfmac_f32_16x16x64_bf16 v[252:255], v[224:227], v[128:135], %[sidx]\n\t"
        "v_smfmac_f32_16x16x64_bf16 v[252:255], v[228:231], v[136:143], %[sidx]\n\t"
        "v_add_f32 v248, v248, v250\n\t"
        "v_smfmac_f32_16x16x64_bf16 v[252:255], v[232:235], v[144:151], %[sidx]\n\t"
        "v_add_f32 v249, v249, v251\n\t"
        "v_smfmac_f32_16x16x64_bf16 v[252:255], v[236:239], v[152:159], %[sidx]\n\t"
        "v_cvt_pk_bf16_f32 v200, v248, v249\n\t"
        "v_smfmac_f32_16x16x64_bf16 v[252:255], v[240:243], v[160:167], %[sidx]\n\t"
        "ds_write_b32 %[ow], v200\n\t"
        "v_smfmac_f32_16x16x64_bf16 v[252:255], v[244:247], v[168:175], %[sidx]\n\t"
        "v_cmp_u_f32 vcc, v248, v249\n\t"
        "s_nop 2\n\t"
        "ds_read_b64_tr_b16 v[192:193], %[qtr] offset:0\n\t"
        "ds_read_b64_tr_b16 v[194:195], %[qtr] offset:768\n\t"
        "ds_read_b64_tr_b16 v[196:197], %[qtr] offset:1536\n\t"
        "ds_read_b64_tr_b16 v[198:199], %[qtr] offset:2304\n\t"
        "s_cbranch_vccnz Lnan%=\n\t"
        "Lok%=:\n\t"
        "v_add_f32 v252, v252, v254\n\t"
        "v_add_f32 v253, v253, v255\n\t"
        "v_cvt_pk_bf16_f32 v201, v252, v253\n\t"
        "ds_write_b32 %[ow], v201 offset:3072\n\t"
        "s_waitcnt lgkmcnt(3)\n\t"
        "v_mfma_f32_16x16x32_bf16 v[248:251], v[176:179], v[192:195], 0\n\t"
        "s_waitcnt lgkmcnt(1)\n\t"
        "v_mfma_f32_16x16x32_bf16 v[248:251], v[180:183], v[196:199], v[248:251]\n\t"
        "s_waitcnt lgkmcnt(0)\n\t"
        "s_nop 1\n\t"
        "Lre%=:\n\t"
        "ds_read_b64_tr_b16 v[208:209], %[qtr] offset:3072\n\t"
        "ds_read_b64_tr_b16 v[210:211], %[qtr] offset:3840\n\t"
        "ds_read_b64_tr_b16 v[212:213], %[qtr] offset:4608\n\t"
        "ds_read_b64_tr_b16 v[214:215], %[qtr] offset:5376\n\t"
        "s_waitcnt lgkmcnt(2)\n\t"
        "v_mfma_f32_16x16x32_bf16 v[248:251], v[184:187], v[208:211], v[248:251]\n\t"
        "s_waitcnt lgkmcnt(0)\n\t"
        "v_mfma_f32_16x16x32_bf16 v[248:251], v[188:191], v[212:215], v[248:251]\n\t"
        "s_nop 7\n\t"
        "v_mov_b32_dpp v248, v250 quad_perm:[0,1,2,3] row_mask:0xf bank_mask:0xc\n\t"
        "v_mov_b32_dpp v249, v251 quad_perm:[0,1,2,3] row_mask:0xf bank_mask:0xc\n\t"
        "v_exp_f32 v202, v248\n\t"
        "v_exp_f32 v203, v249\n\t"
        "v_max_f32_e64 v204, |v248|, |v249|\n\t"
        "v_cvt_pk_bf16_f32 v202, v202, v203\n\t"
        "v_cmpx_le_f32 vcc, v204, %[lim]\n\t"
        "buffer_store_dword v202, %[eo], %[rs], %[so] offen sc1\n\t"
        "buffer_store_dword v202, %[eo], %[rs], s92 offen sc1\n\t"
        "buffer_store_dword v202, %[eo], %[rs], s93 offen sc1\n\t"
        "buffer_store_dword v202, %[eo], %[rs], s94 offen sc1\n\t"
        "s_mov_b64 exec, -1\n\t"
        "v_cndmask_b32 v202, -1, v202, vcc\n\t"
        "s_cmp_lg_u64 vcc, -1\n\t"
        "ds_write_b32 %[ew], v202\n\t"
        "s_cbranch_scc1 Lag%=\n\t"
        "Lgo%=:\n\t"
        "s_waitcnt lgkmcnt(0)\n\t"
        "s_barrier\n\t"
        "s_setprio 0\n\t"
        "ds_read_b32 v207, v206 offset:25104\n\t"
        "s_waitcnt lgkmcnt(0)\n\t"
        "v_cmp_eq_u32 vcc, %[rnd], v207\n\t"
        "s_barrier\n\t"
        "s_cbranch_vccnz Ltop%=\n\t"
        "s_branch Lend%=\n\t"
        "Lag%=:\n\t"
        "s_sub_u32 s95, s95, 1\n\t"
        "s_cbranch_scc1 Lgo%=\n\t"
        "Lqw%=:\n\t"
        "v_subrev_u32 v205, %[qo], %[ow]\n\t"
        "ds_read_b32 v192, v205 offset:0\n\t"
        "ds_read_b32 v193, v205 offset:768\n\t"
        "ds_read_b32 v194, v205 offset:1536\n\t"
        "ds_read_b32 v195, v205 offset:2304\n\t"
        "ds_read_b32 v196, v205 offset:3072\n\t"
        "ds_read_b32 v197, v205 offset:3840\n\t"
        "ds_read_b32 v198, v205 offset:4608\n\t"
        "ds_read_b32 v199, v205 offset:5376\n\t"
        "ds_read_b32 v207, v206 offset:25104\n\t"
        "s_waitcnt lgkmcnt(0)\n\t"
        "v_cmp_eq_u32 vcc, %[rnd], v207\n\t"
        "s_cbranch_vccnz Lgo%=\n\t"
        "v_max_u32 v207, v192, v193\n\t"
        "v_max3_u32 v207, v207, v194, v195\n\t"
        "v_max3_u32 v207, v207, v196, v197\n\t"
        "v_max3_u32 v207, v207, v198, v199\n\t"
        "v_cmp_eq_u32 vcc, 0xffbfffbf, v207\n\t"
        "s_cbranch_vccnz Lqw%=\n\t"
        "ds_read_b64_tr_b16 v[192:193], %[qtr] offset:0\n\t"
        "ds_read_b64_tr_b16 v[194:195], %[qtr] offset:768\n\t"
        "ds_read_b64_tr_b16 v[196:197], %[qtr] offset:1536\n\t"
        "ds_read_b64_tr_b16 v[198:199], %[qtr] offset:2304\n\t"
        "s_waitcnt lgkmcnt(2)\n\t"
        "v_mfma_f32_16x16x32_bf16 v[248:251], v[176:179], v[192:195], 0\n\t"
        "s_waitcnt lgkmcnt(0)\n\t"
        "v_mfma_f32_16x16x32_bf16 v[248:251], v[180:183], v[196:199], v[248:251]\n\t"
        "s_branch Lre%=\n\t"
        "Lnan%=:\n\t"
        "v_max_u32 v207, v208, v209\n\t"
        "v_max3_u32 v207, v207, v210, v211\n\t"
        "v_max3_u32 v207, v207, v212, v213\n\t"
        "v_max3_u32 v207, v207, v214, v215\n\t"
        "v_max3_u32 v207, v207, v216, v217\n\t"
        "v_max3_u32 v207, v207, v218, v219\n\t"
        "v_max3_u32 v207, v207, v220, v221\n\t"
        "v_max3_u32 v207, v207, v222, v223\n\t"
        "v_max3_u32 v207, v207, v224, v225\n\t"
        "v_max3_u32 v207, v207, v226, v227\n\t"
        "v_max3_u32 v207, v207, v228, v229\n\t"
        "v_max3_u32 v207, v207, v230, v231\n\t"
        "v_max3_u32 v207, v207, v232, v233\n\t"
        "v_max3_u32 v207, v207, v234, v235\n\t"
        "v_max3_u32 v207, v207, v236, v237\n\t"
        "v_max3_u32 v207, v207, v238, v239\n\t"
        "v_max3_u32 v207, v207, v240, v241\n\t"
        "v_max3_u32 v207, v207, v242, v243\n\t"
        "v_max3_u32 v207, v207, v244, v245\n\t"
        "v_max3_u32 v207, v207, v246, v247\n\t"
        "v_cmp_eq_u32 vcc, 0xffbfffbf, v207\n\t"
        "v_mov_b32 v207, %[rnd]\n\t"
        "s_cbranch_vccz Lok%=\n\t"
        "ds_write_b32 v206, v207 offset:25104\n\t"
        "s_waitcnt lgkmcnt(0)\n\t"
        "s_barrier\n\t"
        "s_barrier\n\t"
        "s_branch Ltop%=\n\t"
        "Lend%=:\n\t"
        : [rnd] "+&s"(rnd)
        : [w0] "{v[16:47]}"(W[0]), [w1] "{v[48:79]}"(W[1]), [w2] "{v[80:111]}"(W[2]), [w3] "{v[112:143]}"(W[3]), [w4] "{v[144:175]}"(W[4]), [va] "v"(va), [sidx] "v"(sidx), [ow] "v"(ow), [lb] "s"(lb), [kxa] "v"(kxa), [qtr] "v"(qtr), [eo] "v"(eo), [ew] "v"(ew), [lim] "v"(lim), [rs] "s"(rs), [so] "s"(so), [qo] "s"(qo)
        : "s92", "s93", "s94", "s95", "v176", "v177", "v178", "v179", "v180", "v181", "v182", "v183", "v184", "v185", "v186", "v187", "v188", "v189", "v190", "v191", "v192", "v193", "v194", "v195", "v196", "v197", "v198", "v199", "v200", "v201", "v202", "v203", "v204", "v205", "v206", "v207", "v208", "v209", "v210", "v211", "v212", "v213", "v214", "v215", "v216", "v217", "v218", "v219", "v220", "v221", "v222", "v223", "v224", "v225", "v226", "v227", "v228", "v229", "v230", "v231", "v232", "v233", "v234", "v235", "v236", "v237", "v238", "v239", "v240", "v241", "v242", "v243", "v244", "v245", "v246", "v247", "v248", "v249", "v250", "v251", "v252", "v253", "v254", "v255", "vcc", "scc", "memory");
}

__device__ __forceinline__ void s_f(const u32x32 (&W)[5], u32& rnd, u32 va, u32 sidx, u32 ow, u32 lb, u32& bad, u32x4 ones, u32 qtr, u32 ktr, u32 er, u32 po, float c1, float ind0, float ind1, u32x4 rs, u32 so, float ce, float big STP) {
    asm volatile(
        "s_add_u32 s92, %[so], 0x1000\n\t"
        "s_add_u32 s93, %[so], 0x4000\n\t"
        "s_add_u32 s94, %[so], 0x5000\n\t"
        "v_mov_b64 v[248:249], 0\n\t"
        "v_mov_b64 v[250:251], 0\n\t"
        "v_mov_b64 v[252:253], 0\n\t"
        "v_mov_b64 v[254:255], 0\n\t"
        "v_mov_b32 v200, 0xffbfffbf\n\t"
        "v_mov_b32 v206, %[lb]\n\t"
        "s_add_u32 %[rnd], %[rnd], 1\n\t"
        "s_setprio 3\n\t"
        "s_mov_b32 %[bad], 2\n\t"
        "v_mov_b32 v197, 0\n\t"
        "s_waitcnt vmcnt(0) lgkmcnt(0)\n\t"
        "s_barrier\n\t"
        "s_branch Lmk%=\n\t"
        "Ltop%=:\n\t"
        "v_mov_b64 v[248:249], 0\n\t"
        "v_mov_b64 v[250:251], 0\n\t"
        "v_mov_b64 v[252:253], 0\n\t"
        "v_mov_b64 v[254:255], 0\n\t"
        "v_mov_b32 v200, 0xffbfffbf\n\t"
        "v_mov_b32 v206, %[lb]\n\t"
        "s_add_u32 %[rnd], %[rnd], 1\n\t"
        "s_setprio 3\n\t"
        "s_mov_b32 %[bad], 2\n\t"
        "v_mov_b32 v197, 0\n\t"
        "Lmk%=:\n\t"
        "ds_write_b32 %[ow], v200\n\t"
        "ds_write_b32 %[ow], v200 offset:3072\n\t"
        "s_waitcnt lgkmcnt(0)\n\t"
        "s_barrier\n\t"
        "ds_read_b128 v[208:211], %[va] offset:0\n\t"
        "ds_read_b128 v[212:215], %[va] offset:1024\n\t"
        "ds_read_b128 v[216:219], %[va] offset:2048\n\t"
        "ds_read_b128 v[220:223], %[va] offset:3072\n\t"
        "ds_read_b128 v[224:227], %[va] offset:4096\n\t"
        "ds_read_b128 v[228:231], %[va] offset:5120\n\t"
        "s_waitcnt lgkmcnt(5)\n\t"
        "v_smfmac_f32_16x16x64_bf16 v[248:251], v[208:211], v[16:23], %[sidx]\n\t"
        "ds_read_b128 v[232:235], %[va] offset:6144\n\t"
        "v_smfmac_f32_16x16x64_bf16 v[252:255], v[208:211], v[96:103], %[sidx]\n\t"
        "s_waitcnt lgkmcnt(5)\n\t"
        "v_smfmac_f32_16x16x64_bf16 v[248:251], v[212:215], v[24:31], %[sidx]\n\t"
        "ds_read_b128 v[236:239], %[va] offset:7168\n\t"
        "v_smfmac_f32_16x16x64_bf16 v[252:255], v[212:215], v[104:111], %[sidx]\n\t"
        "s_waitcnt lgkmcnt(5)\n\t"
        "v_smfmac_f32_16x16x64_bf16 v[248:251], v[216:219], v[32:39], %[sidx]\n\t"
        "ds_read_b128 v[240:243], %[va] offset:8192\n\t"
        "v_smfmac_f32_16x16x64_bf16 v[252:255], v[216:219], v[112:119], %[sidx]\n\t"
        "s_waitcnt lgkmcnt(5)\n\t"
        "v_smfmac_f32_16x16x64_bf16 v[248:251], v[220:223], v[40:47], %[sidx]\n\t"
        "ds_read_b128 v[244:247], %[va] offset:10240\n\t"
        "v_smfmac_f32_16x16x64_bf16 v[252:255], v[220:223], v[120:127], %[sidx]\n\t"
        "s_waitcnt lgkmcnt(5)\n\t"
        "v_smfmac_f32_16x16x64_bf16 v[248:251], v[224:227], v[48:55], %[sidx]\n\t"
        "s_waitcnt lgkmcnt(4)\n\t"
        "v_smfmac_f32_16x16x64_bf16 v[248:251], v[228:231], v[56:63], %[sidx]\n\t"
        "s_waitcnt lgkmcnt(3)\n\t"
        "v_smfmac_f32_16x16x64_bf16 v[248:251], v[232:235], v[64:71], %[sidx]\n\t"
        "s_waitcnt lgkmcnt(2)\n\t"
        "v_smfmac_f32_16x16x64_bf16 v[248:251], v[236:239], v[72:79], %[sidx]\n\t"
        "s_waitcnt lgkmcnt(1)\n\t"
        "v_smfmac_f32_16x16x64_bf16 v[248:251], v[240:243], v[80:87], %[sidx]\n\t"
        "s_waitcnt lgkmcnt(0)\n\t"
        "v_smfmac_f32_16x16x64_bf16 v[248:251], v[244:247], v[88:95], %[sidx]\n\t"
        "v_smfmac_f32_16x16x64_bf16 v[252:255], v[224:227], v[128:135], %[sidx]\n\t"
        "v_smfmac_f32_16x16x64_bf16 v[252:255], v[228:231], v[136:143], %[sidx]\n\t"
        "v_add_f32 v248, v248, v250\n\t"
        "v_smfmac_f32_16x16x64_bf16 v[252:255], v[232:235], v[144:151], %[sidx]\n\t"
        "v_add_f32 v249, v249, v251\n\t"
        "v_smfmac_f32_16x16x64_bf16 v[252:255], v[236:239], v[152:159], %[sidx]\n\t"
        "v_cvt_pk_bf16_f32 v200, v248, v249\n\t"
        "v_smfmac_f32_16x16x64_bf16 v[252:255], v[240:243], v[160:167], %[sidx]\n\t"
        "ds_write_b32 %[ow], v200\n\t"
        "v_smfmac_f32_16x16x64_bf16 v[252:255], v[244:247], v[168:175], %[sidx]\n\t"
        "v_cmp_u_f32 vcc, v248, v249\n\t"
        "s_nop 6\n\t"
        "s_cbranch_vccnz Lnan%=\n\t"
        "Lok%=:\n\t"
        "v_add_f32 v252, v252, v254\n\t"
        "v_add_f32 v253, v253, v255\n\t"
        "v_cvt_pk_bf16_f32 v201, v252, v253\n\t"
        "ds_write_b32 %[ow], v201 offset:3072\n\t"
        "s_waitcnt lgkmcnt(0)\n\t"
        "s_nop 15\n\t"
        "ds_read_b64_tr_b16 v[180:181], %[qtr] offset:0\n\t"
        "ds_read_b64_tr_b16 v[182:183], %[qtr] offset:768\n\t"
        "ds_read_b64_tr_b16 v[184:185], %[qtr] offset:1536\n\t"
        "ds_read_b64_tr_b16 v[186:187], %[qtr] offset:2304\n\t"
        "ds_read_b64_tr_b16 v[188:189], %[qtr] offset:3072\n\t"
        "ds_read_b64_tr_b16 v[190:191], %[qtr] offset:3840\n\t"
        "ds_read_b64_tr_b16 v[192:193], %[qtr] offset:4608\n\t"
        "ds_read_b64_tr_b16 v[194:195], %[qtr] offset:5376\n\t"
        "s_nop 15\n\t"
        "s_nop 7\n\t"
        "ds_read_b64_tr_b16 v[224:225], %[ktr] offset:0\n\t"
        "ds_read_b64_tr_b16 v[226:227], %[ktr] offset:768\n\t"
        "ds_read_b64_tr_b16 v[228:229], %[ktr] offset:1536\n\t"
        "ds_read_b64_tr_b16 v[230:231], %[ktr] offset:2304\n\t"
        "s_nop 7\n\t"
        "ds_read_b64_tr_b16 v[232:233], %[ktr] offset:4608\n\t"
        "ds_read_b64_tr_b16 v[234:235], %[ktr] offset:5376\n\t"
        "s_waitcnt lgkmcnt(4)\n\t"
        "v_mfma_f32_16x16x32_bf16 v[248:251], v[224:227], v[180:183], 0\n\t"
        "s_waitcnt lgkmcnt(2)\n\t"
        "v_mfma_f32_16x16x32_bf16 v[248:251], v[228:231], v[184:187], v[248:251]\n\t"
        "s_waitcnt lgkmcnt(0)\n\t"
        "v_mfma_f32_16x16x32_bf16 v[248:251], v[232:235], v[192:195], v[248:251]\n\t"
        "s_barrier\n\t"
        "Lre%=:\n\t"
        "ds_read_b64_tr_b16 v[208:209], %[ktr] offset:3072\n\t"
        "ds_read_b64_tr_b16 v[210:211], %[ktr] offset:3840\n\t"
        "ds_read_b128 v[240:243], %[er]\n\t"
        "ds_read_b32 v207, v206 offset:25104\n\t"
        "s_waitcnt lgkmcnt(2)\n\t"
        "v_mfma_f32_16x16x32_bf16 v[248:251], v[208:211], v[188:191], v[248:251]\n\t"
        "s_waitcnt lgkmcnt(1)\n\t"
        "v_mfma_f32_16x16x32_bf16 v[244:247], v[176:179], v[240:243], 0\n\t"
        "s_nop 3\n\t"
        "v_mul_f32 v248, %[ce], v248\n\t"
        "v_mul_f32 v249, %[ce], v249\n\t"
        "v_exp_f32 v250, v248\n\t"
        "v_exp_f32 v251, v249\n\t"
        "s_nop 0\n\t"
        "v_cvt_pk_bf16_f32 v196, v250, v251\n\t"
        "v_fma_f32 v250, v250, %[c1], %[ind0]\n\t"
        "v_fma_f32 v251, v251, %[c1], %[ind1]\n\t"
        "v_mfma_f32_16x16x16_bf16 v[244:247], v[176:177], v[196:197], v[244:247]\n\t"
        "s_nop 7\n\t"
        "v_rcp_f32 v246, v244\n\t"
        "v_cmpx_gt_f32 vcc, %[big], v244\n\t"
        "v_pk_mul_f32 v[250:251], v[250:251], v[246:247] op_sel_hi:[1,0]\n\t"
        "v_cvt_pk_bf16_f32 v250, v250, v251\n\t"
        "buffer_store_dword v250, %[po], %[rs], %[so] offen sc1\n\t"
        "buffer_store_dword v250, %[po], %[rs], s92 offen sc1\n\t"
        "buffer_store_dword v250, %[po], %[rs], s93 offen sc1\n\t"
        "buffer_store_dword v250, %[po], %[rs], s94 offen sc1\n\t"
        "s_mov_b64 exec, -1\n\t"
        "s_cmp_eq_u32 %[bad], 1\n\t"
        "s_cbranch_scc1 Ltw%=\n\t"
        "s_cmp_lg_u64 vcc, -1\n\t"
        "s_cselect_b32 %[bad], 1, 0\n\t"
        "s_waitcnt lgkmcnt(0)\n\t"
        "v_cmp_eq_u32 vcc, %[rnd], v207\n\t"
        "s_barrier\n\t"
        "s_cbranch_vccnz Ltop%=\n\t"
        "s_cbranch_scc0 Lend%=\n\t"
        "ds_read_b64_tr_b16 v[180:181], %[qtr] offset:0\n\t"
        "ds_read_b64_tr_b16 v[182:183], %[qtr] offset:768\n\t"
        "ds_read_b64_tr_b16 v[184:185], %[qtr] offset:1536\n\t"
        "ds_read_b64_tr_b16 v[186:187], %[qtr] offset:2304\n\t"
        "ds_read_b64_tr_b16 v[188:189], %[qtr] offset:3072\n\t"
        "ds_read_b64_tr_b16 v[190:191], %[qtr] offset:3840\n\t"
        "ds_read_b64_tr_b16 v[192:193], %[qtr] offset:4608\n\t"
        "ds_read_b64_tr_b16 v[194:195], %[qtr] offset:5376\n\t"
        "ds_read_b64_tr_b16 v[224:225], %[ktr] offset:0\n\t"
        "ds_read_b64_tr_b16 v[226:227], %[ktr] offset:768\n\t"
        "ds_read_b64_tr_b16 v[228:229], %[ktr] offset:1536\n\t"
        "ds_read_b64_tr_b16 v[230:231], %[ktr] offset:2304\n\t"
        "ds_read_b64_tr_b16 v[232:233], %[ktr] offset:4608\n\t"
        "ds_read_b64_tr_b16 v[234:235], %[ktr] offset:5376\n\t"
        "s_waitcnt lgkmcnt(0)\n\t"
        "v_mfma_f32_16x16x32_bf16 v[248:251], v[224:227], v[180:183], 0\n\t"
        "v_mfma_f32_16x16x32_bf16 v[248:251], v[228:231], v[184:187], v[248:251]\n\t"
        "v_mfma_f32_16x16x32_bf16 v[248:251], v[232:235], v[192:195], v[248:251]\n\t"
        "s_branch Lre%=\n\t"
        "Lnan%=:\n\t"
        "v_max_u32 v207, v208, v209\n\t"
        "v_max3_u32 v207, v207, v210, v211\n\t"
        "v_max3_u32 v207, v207, v212, v213\n\t"
        "v_max3_u32 v207, v207, v214, v215\n\t"
        "v_max3_u32 v207, v207, v216, v217\n\t"
        "v_max3_u32 v207, v207, v218, v219\n\t"
        "v_max3_u32 v207, v207, v220, v221\n\t"
        "v_max3_u32 v207, v207, v222, v223\n\t"
        "v_max3_u32 v207, v207, v224, v225\n\t"
        "v_max3_u32 v207, v207, v226, v227\n\t"
        "v_max3_u32 v207, v207, v228, v229\n\t"
        "v_max3_u32 v207, v207, v230, v231\n\t"
        "v_max3_u32 v207, v207, v232, v233\n\t"
        "v_max3_u32 v207, v207, v234, v235\n\t"
        "v_max3_u32 v207, v207, v236, v237\n\t"
        "v_max3_u32 v207, v207, v238, v239\n\t"
        "v_max3_u32 v207, v207, v240, v241\n\t"
        "v_max3_u32 v207, v207, v242, v243\n\t"
        "v_max3_u32 v207, v207, v244, v245\n\t"
        "v_max3_u32 v207, v207, v246, v247\n\t"
        "v_cmp_eq_u32 vcc, 0xffbfffbf, v207\n\t"
        "v_mov_b32 v207, %[rnd]\n\t"
        "s_cbranch_vccz Lok%=\n\t"
        "ds_write_b32 v206, v207 offset:25104\n\t"
        "s_waitcnt lgkmcnt(0)\n\t"
        "s_barrier\n\t"
        "s_barrier\n\t"
        "s_branch Ltop%=\n\t"
        "Ltw%=:\n\t"
        "s_cmp_lg_u64 vcc, -1\n\t"
        "s_cselect_b32 %[bad], 1, 0\n\t"
        "s_waitcnt lgkmcnt(0)\n\t"
        "Lend%=:\n\t"
        "s_setprio 0\n\t"
        : [rnd] "+&s"(rnd), [bad] "=&s"(bad)
        : [w0] "{v[16:47]}"(W[0]), [w1] "{v[48:79]}"(W[1]), [w2] "{v[80:111]}"(W[2]), [w3] "{v[112:143]}"(W[3]), [w4] "{v[144:175]}"(W[4]), [va] "v"(va), [sidx] "v"(sidx), [ow] "v"(ow), [lb] "s"(lb), [ones] "{v[176:179]}"(ones), [qtr] "v"(qtr), [ktr] "v"(ktr), [er] "v"(er), [po] "v"(po), [c1] "v"(c1), [ind0] "v"(ind0), [ind1] "v"(ind1), [rs] "s"(rs), [so] "s"(so), [ce] "s"(ce), [big] "s"(big)
        : "s92", "s93", "s94", "v180", "v181", "v182", "v183", "v184", "v185", "v186", "v187", "v188", "v189", "v190", "v191", "v192", "v193", "v194", "v195", "v196", "v197", "v198", "v199", "v200", "v201", "v202", "v203", "v204", "v205", "v206", "v207", "v208", "v209", "v210", "v211", "v212", "v213", "v214", "v215", "v216", "v217", "v218", "v219", "v220", "v221", "v222", "v223", "v224", "v225", "v226", "v227", "v228", "v229", "v230", "v231", "v232", "v233", "v234", "v235", "v236", "v237", "v238", "v239", "v240", "v241", "v242", "v243", "v244", "v245", "v246", "v247", "v248", "v249", "v250", "v251", "v252", "v253", "v254", "v255", "vcc", "scc", "memory");
}

__device__ __forceinline__ void s_m(const u32x32 (&W)[5], u32& rnd, u32 va, u32 sidx, u32 ow, u32 lb, u32& left, u32& s, u32& so, u32& tg, u32& kr, u32& c0, u32& r0, u32& ok, u32& cnt, u32 vm, u32 tc, u32 tv, u32 md, u32x4 rs, u32 poll, u32 last STP LGP) {
    asm volatile(
        "s_mov_b32 s94, m0\n\t"
        "Lst%=:\n\t"
        "v_readlane_b32 s60, %[tc], %[s]\n\t"
        "s_add_u32 s92, %[so], 0x8000\n\t"
        "s_add_u32 s93, %[so], 0x10000\n\t"
        "s_mov_b32 m0, %[md]\n\t"
        "s_add_u32 s62, %[s], 1\n\t"
        "s_mov_b32 s58, %[c0]\n\t"
        "s_mov_b32 s59, %[r0]\n\t"
        "s_mov_b32 s63, %[rnd]\n\t"
        "s_setprio 3\n\t"
        "s_add_u32 s61, s60, 0x10400\n\t"
        "s_mov_b32 s96, %[left]\n\t"
        "s_cmp_eq_u32 %[poll], 0\n\t"
        "s_cbranch_scc1 Ltop%=\n\t"
        "v_add_u32 v204, %[md], %[vm]\n\t"
        "v_mov_b32 v208, 0xffbfffbf\n\t"
        "v_mov_b32 v209, v208\n\t"
        "v_mov_b64 v[210:211], v[208:209]\n\t"
        "ds_write_b128 v204, v[208:211] offset:5120\n\t"
        "ds_write_b128 v204, v[208:211] offset:6144\n\t"
        "ds_write_b128 v204, v[208:211] offset:7168\n\t"
        "ds_write_b128 v204, v[208:211] offset:8192\n\t"
        "ds_write_b128 v204, v[208:211] offset:10240\n\t"
        "Lda_%=:\n\t"
        "s_memrealtime s[90:91]\n\t"
        "s_waitcnt lgkmcnt(0)\n\t"
        "s_mul_i32 s90, s90, 24\n\t"
        "s_sub_u32 s88, %[tg], s90\n\t"
        "s_sub_u32 s89, s88, 620\n\t"
        "s_cmp_gt_u32 s89, 0x4000\n\t"
        "s_cbranch_scc1 Lea_%=\n\t"
        "s_sleep 1\n\t"
        "s_branch Lda_%=\n\t"
        "Lea_%=:\n\t"
        "s_memrealtime s[64:65]\n\t"
        "s_nop 0\n\t"
        "s_memrealtime s[66:67]\n\t"
        "s_nop 0\n\t"
        "s_memrealtime s[68:69]\n\t"
        "s_nop 0\n\t"
        "s_memrealtime s[70:71]\n\t"
        "s_nop 0\n\t"
        "s_memrealtime s[72:73]\n\t"
        "s_nop 0\n\t"
        "s_memrealtime s[74:75]\n\t"
        "s_memtime s[90:91]\n\t"
        "s_memrealtime s[76:77]\n\t"
        "s_nop 0\n\t"
        "s_memrealtime s[78:79]\n\t"
        "s_nop 0\n\t"
        "s_memrealtime s[80:81]\n\t"
        "s_nop 0\n\t"
        "s_memrealtime s[82:83]\n\t"
        "s_nop 0\n\t"
        "s_memrealtime s[84:85]\n\t"
        "s_nop 0\n\t"
        "s_memrealtime s[86:87]\n\t"
        "s_nop 0\n\t"
        "s_nop 7\n\t"
        "s_waitcnt lgkmcnt(0)\n\t"
        "s_add_u32 s64, s64, s66\n\t"
        "s_add_u32 s64, s64, s68\n\t"
        "s_add_u32 s64, s64, s70\n\t"
        "s_add_u32 s64, s64, s72\n\t"
        "s_add_u32 s64, s64, s74\n\t"
        "s_add_u32 s64, s64, s76\n\t"
        "s_add_u32 s64, s64, s78\n\t"
        "s_add_u32 s64, s64, s80\n\t"
        "s_add_u32 s64, s64, s82\n\t"
        "s_add_u32 s64, s64, s84\n\t"
        "s_add_u32 s64, s64, s86\n\t"
        "s_lshl_b32 s64, s64, 1\n\t"
        "s_mov_b32 %[c0], s90\n\t"
        "s_mov_b32 %[r0], s64\n\t"
        "s_sub_u32 s88, %[tg], s64\n\t"
        "s_mul_i32 s88, s88, %[kr]\n\t"
        "s_ashr_i32 s88, s88, 12\n\t"
        "s_sub_u32 s88, s88, 244\n\t"
        "s_nop 1\n\t"
        "s_max_i32 s88, s88, 0\n\t"
        "s_min_i32 s88, s88, 384\n\t"
        "s_andn2_b32 s88, s88, 3\n\t"
        "s_getpc_b64 s[90:91]\n\t"
        "Lga_%=:\n\t"
        "s_sub_u32 s90, s90, s88\n\t"
        "s_subb_u32 s91, s91, 0\n\t"
        "s_add_u32 s90, s90, Lza_%=-Lga_%=\n\t"
        "s_addc_u32 s91, s91, 0\n\t"
        "s_setpc_b64 s[90:91]\n\t"
        "s_nop 0\n\ts_nop 0\n\ts_nop 0\n\ts_nop 0\n\t"
        "s_nop 0\n\ts_nop 0\n\ts_nop 0\n\ts_nop 0\n\t"
        "s_nop 0\n\ts_nop 0\n\ts_nop 0\n\ts_nop 0\n\t"
        "s_nop 0\n\ts_nop 0\n\ts_nop 0\n\ts_nop 0\n\t"
        "s_nop 0\n\ts_nop 0\n\ts_nop 0\n\ts_nop 0\n\t"
        "s_nop 0\n\ts_nop 0\n\ts_nop 0\n\ts_nop 0\n\t"
        "s_nop 0\n\ts_nop 0\n\ts_nop 0\n\ts_nop 0\n\t"
        "s_nop 0\n\ts_nop 0\n\ts_nop 0\n\ts_nop 0\n\t"
        "s_nop 0\n\ts_nop 0\n\ts_nop 0\n\ts_nop 0\n\t"
        "s_nop 0\n\ts_nop 0\n\ts_nop 0\n\ts_nop 0\n\t"
        "s_nop 0\n\ts_nop 0\n\ts_nop 0\n\ts_nop 0\n\t"
        "s_nop 0\n\ts_nop 0\n\ts_nop 0\n\ts_nop 0\n\t"
        "s_nop 0\n\ts_nop 0\n\ts_nop 0\n\ts_nop 0\n\t"
        "s_nop 0\n\ts_nop 0\n\ts_nop 0\n\ts_nop 0\n\t"
        "s_nop 0\n\ts_nop 0\n\ts_nop 0\n\ts_nop 0\n\t"
        "s_nop 0\n\ts_nop 0\n\ts_nop 0\n\ts_nop 0\n\t"
        "s_nop 0\n\ts_nop 0\n\ts_nop 0\n\ts_nop 0\n\t"
        "s_nop 0\n\ts_nop 0\n\ts_nop 0\n\ts_nop 0\n\t"
        "s_nop 0\n\ts_nop 0\n\ts_nop 0\n\ts_nop 0\n\t"
        "s_nop 0\n\ts_nop 0\n\ts_nop 0\n\ts_nop 0\n\t"
        "s_nop 0\n\ts_nop 0\n\ts_nop 0\n\ts_nop 0\n\t"
        "s_nop 0\n\ts_nop 0\n\ts_nop 0\n\ts_nop 0\n\t"
        "s_nop 0\n\ts_nop 0\n\ts_nop 0\n\ts_nop 0\n\t"
        "s_nop 0\n\ts_nop 0\n\ts_nop 0\n\ts_nop 0\n\t"
        "Lza_%=:\n\t"
        "buffer_load_dwordx4 %[vm], %[rs], %[so] offen offset:0 sc1 lds\n\t"
        "buffer_load_dwordx4 %[vm], %[rs], %[so] offen offset:1024 sc1 lds\n\t"
        "buffer_load_dwordx4 %[vm], %[rs], %[so] offen offset:2048 sc1 lds\n\t"
        "buffer_load_dwordx4 %[vm], %[rs], %[so] offen offset:3072 sc1 lds\n\t"
        "s_add_u32 m0, m0, 0x1000\n\t"
        "s_nop 0\n\t"
        "buffer_load_dwordx4 %[vm], %[rs], s92 offen offset:0 sc1 lds\n\t"
        "buffer_load_dwordx4 %[vm], %[rs], s92 offen offset:1024 sc1 lds\n\t"
        "buffer_load_dwordx4 %[vm], %[rs], s92 offen offset:2048 sc1 lds\n\t"
        "buffer_load_dwordx4 %[vm], %[rs], s92 offen offset:3072 sc1 lds\n\t"
        "s_add_u32 m0, m0, 0x1000\n\t"
        "s_nop 0\n\t"
        "buffer_load_dwordx4 %[vm], %[rs], s93 offen offset:0 sc1 lds\n\t"
        "buffer_load_dwordx4 %[vm], %[rs], s93 offen offset:2048 sc1 lds\n\t"
        "s_barrier\n\t"
        "s_sub_u32 s59, %[r0], s59\n\t"
        "s_sub_u32 s58, %[c0], s58\n\t"
        "s_mul_i32 s88, s59, %[kr]\n\t"
        "s_lshr_b32 s88, s88, 12\n\t"
        "s_sub_u32 s58, s58, s88\n\t"
        "s_mul_i32 s58, s58, 3\n\t"
        "s_ashr_i32 s58, s58, 2\n\t"
        "s_sub_u32 s59, s59, 2048\n\t"
        "s_cmp_lt_u32 s59, 2560\n\t"
        "s_cselect_b32 s58, s58, 0\n\t"
        "s_add_u32 %[kr], %[kr], s58\n\t"
        "s_max_i32 %[kr], %[kr], 2560\n\t"
        "s_min_i32 %[kr], %[kr], 4352\n\t"
        "Ltop%=:\n\t"
        "v_mov_b64 v[248:249], 0\n\t"
        "v_mov_b64 v[250:251], 0\n\t"
        "v_mov_b64 v[252:253], 0\n\t"
        "v_mov_b64 v[254:255], 0\n\t"
        "v_mov_b32 v200, 0xffbfffbf\n\t"
        "v_mov_b32 v206, %[lb]\n\t"
        "s_add_u32 %[rnd], %[rnd], 1\n\t"
        "s_setprio 3\n\t"
        "Lmk%=:\n\t"
        "ds_write_b32 %[ow], v200\n\t"
        "ds_write_b32 %[ow], v200 offset:3072\n\t"
        "s_mov_b32 s95, 0\n\t"
        "s_waitcnt vmcnt(5)\n\t"
        "s_waitcnt lgkmcnt(0)\n\t"
        "s_barrier\n\t"
        "ds_read_b128 v[208:211], %[va] offset:0\n\t"
        "ds_read_b128 v[212:215], %[va] offset:1024\n\t"
        "ds_read_b128 v[216:219], %[va] offset:2048\n\t"
        "ds_read_b128 v[220:223], %[va] offset:3072\n\t"
        "ds_read_b128 v[224:227], %[va] offset:4096\n\t"
        "ds_read_b128 v[228:231], %[va] offset:5120\n\t"
        "s_waitcnt lgkmcnt(5)\n\t"
        "v_smfmac_f32_16x16x64_bf16 v[248:251], v[208:211], v[16:23], %[sidx]\n\t"
        "ds_read_b128 v[232:235], %[va] offset:6144\n\t"
        "v_smfmac_f32_16x16x64_bf16 v[252:255], v[208:211], v[96:103], %[sidx]\n\t"
        "s_waitcnt lgkmcnt(5)\n\t"
        "v_smfmac_f32_16x16x64_bf16 v[248:251], v[212:215], v[24:31], %[sidx]\n\t"
        "ds_read_b128 v[236:239], %[va] offset:7168\n\t"
        "v_smfmac_f32_16x16x64_bf16 v[252:255], v[212:215], v[104:111], %[sidx]\n\t"
        "s_waitcnt lgkmcnt(5)\n\t"
        "v_smfmac_f32_16x16x64_bf16 v[248:251], v[216:219], v[32:39], %[sidx]\n\t"
        "ds_read_b128 v[240:243], %[va] offset:8192\n\t"
        "v_smfmac_f32_16x16x64_bf16 v[252:255], v[216:219], v[112:119], %[sidx]\n\t"
        "s_waitcnt lgkmcnt(5)\n\t"
        "v_smfmac_f32_16x16x64_bf16 v[248:251], v[220:223], v[40:47], %[sidx]\n\t"
        "ds_read_b128 v[244:247], %[va] offset:10240\n\t"
        "v_smfmac_f32_16x16x64_bf16 v[252:255], v[220:223], v[120:127], %[sidx]\n\t"
        "s_waitcnt lgkmcnt(5)\n\t"
        "v_smfmac_f32_16x16x64_bf16 v[248:251], v[224:227], v[48:55], %[sidx]\n\t"
        "s_waitcnt lgkmcnt(4)\n\t"
        "v_smfmac_f32_16x16x64_bf16 v[248:251], v[228:231], v[56:63], %[sidx]\n\t"
        "s_waitcnt lgkmcnt(3)\n\t"
        "v_smfmac_f32_16x16x64_bf16 v[248:251], v[232:235], v[64:71], %[sidx]\n\t"
        "s_waitcnt lgkmcnt(2)\n\t"
        "v_smfmac_f32_16x16x64_bf16 v[248:251], v[236:239], v[72:79], %[sidx]\n\t"
        "s_waitcnt lgkmcnt(1)\n\t"
        "v_smfmac_f32_16x16x64_bf16 v[248:251], v[240:243], v[80:87], %[sidx]\n\t"
        "s_waitcnt lgkmcnt(0)\n\t"
        "v_smfmac_f32_16x16x64_bf16 v[248:251], v[244:247], v[88:95], %[sidx]\n\t"
        "v_smfmac_f32_16x16x64_bf16 v[252:255], v[224:227], v[128:135], %[sidx]\n\t"
        "v_smfmac_f32_16x16x64_bf16 v[252:255], v[228:231], v[136:143], %[sidx]\n\t"
        "v_add_f32 v248, v248, v250\n\t"
        "v_smfmac_f32_16x16x64_bf16 v[252:255], v[232:235], v[144:151], %[sidx]\n\t"
        "v_add_f32 v249, v249, v251\n\t"
        "v_smfmac_f32_16x16x64_bf16 v[252:255], v[236:239], v[152:159], %[sidx]\n\t"
        "v_cvt_pk_bf16_f32 v200, v248, v249\n\t"
        "v_smfmac_f32_16x16x64_bf16 v[252:255], v[240:243], v[160:167], %[sidx]\n\t"
        "ds_write_b32 %[ow], v200\n\t"
        "v_smfmac_f32_16x16x64_bf16 v[252:255], v[244:247], v[168:175], %[sidx]\n\t"
        "v_cmp_u_f32 vcc, v248, v249\n\t"
        "s_nop 6\n\t"
        "s_cbranch_vccnz Lnan%=\n\t"
        "Lok%=:\n\t"
        "v_add_f32 v252, v252, v254\n\t"
        "v_add_f32 v253, v253, v255\n\t"
        "v_cvt_pk_bf16_f32 v201, v252, v253\n\t"
        "ds_write_b32 %[ow], v201 offset:3072\n\t"
        "s_waitcnt lgkmcnt(0)\n\t"
        "s_cmp_lg_u32 s96, %[left]\n\t"
        "s_cbranch_scc1 Lv%=\n\t"
        "s_buffer_load_dwordx4 s[64:67], %[rs], s61 offset:0x0\n\t"
        "s_buffer_load_dwordx4 s[68:71], %[rs], s61 offset:0x80\n\t"
        "s_buffer_load_dwordx4 s[72:75], %[rs], s61 offset:0x100\n\t"
        "s_buffer_load_dwordx4 s[76:79], %[rs], s61 offset:0x180\n\t"
        "s_barrier\n\t"
        "s_setprio 0\n\t"
        "ds_read_b32 v207, v206 offset:25104\n\t"
        "s_waitcnt lgkmcnt(0)\n\t"
        "v_cmp_eq_u32 vcc, %[rnd], v207\n\t"
        "s_barrier\n\t"
        "s_cbranch_vccnz Lck%=\n\t"
        "s_xor_b32 s67, s67, s62\n\t"
        "s_xor_b32 s71, s71, s62\n\t"
        "s_xor_b32 s75, s75, s62\n\t"
        "s_xor_b32 s79, s79, s62\n\t"
        "s_sub_u32 s65, s65, %[r0]\n\t"
        "s_cmp_eq_u32 s67, 0\n\t"
        "s_cselect_b32 s65, s65, 0\n\t"
        "s_sub_u32 s69, s69, %[r0]\n\t"
        "s_cmp_eq_u32 s71, 0\n\t"
        "s_cselect_b32 s69, s69, 0\n\t"
        "s_sub_u32 s73, s73, %[r0]\n\t"
        "s_cmp_eq_u32 s75, 0\n\t"
        "s_cselect_b32 s73, s73, 0\n\t"
        "s_sub_u32 s77, s77, %[r0]\n\t"
        "s_cmp_eq_u32 s79, 0\n\t"
        "s_cselect_b32 s77, s77, 0\n\t"
        "s_max_u32 s65, s65, s69\n\t"
        "s_max_u32 s73, s73, s77\n\t"
        "s_max_u32 s65, s65, s73\n\t"
        "s_mov_b32 s64, s65\n\t"
        "s_sub_u32 s67, s67, 1\n\t"
        "s_cmp_lt_u32 s67, 0x20000\n\t"
        "s_cselect_b32 s64, 0, s64\n\t"
        "s_sub_u32 s71, s71, 1\n\t"
        "s_cmp_lt_u32 s71, 0x20000\n\t"
        "s_cselect_b32 s64, 0, s64\n\t"
        "s_sub_u32 s75, s75, 1\n\t"
        "s_cmp_lt_u32 s75, 0x20000\n\t"
        "s_cselect_b32 s64, 0, s64\n\t"
        "s_sub_u32 s79, s79, 1\n\t"
        "s_cmp_lt_u32 s79, 0x20000\n\t"
        "s_cselect_b32 s64, 0, s64\n\t"
        "s_add_u32 s65, s65, %[r0]\n\t"
        "s_cmp_lg_u32 s64, 0\n\t"
        "Lnx%=:\n\t"
        "s_cselect_b32 %[ok], 1, 0\n\t"
        "s_mov_b32 %[tg], s65\n\t"
        "s_mov_b32 %[so], s60\n\t"
        "s_sub_u32 s63, %[rnd], s63\n\t"
        "s_cmp_gt_u32 s63, 1\n\t"
        "s_addc_u32 %[cnt], %[cnt], 0\n\t"
        "s_mov_b32 %[s], s62\n\t"
        "s_waitcnt vmcnt(0)\n\t"
        "s_and_b32 s63, %[ok], %[poll]\n\t"
        "s_cmp_lt_i32 s62, %[last]\n\t"
        "s_cselect_b32 s63, s63, 0\n\t"
        "s_cmp_eq_u32 s63, 1\n\t"
        "s_cbranch_scc1 Lst%=\n\t"
        "s_branch Lend%=\n\t"
        "Lv%=:\n\t"
        "s_barrier\n\t"
        "buffer_load_dwordx4 v[196:199], %[tv], %[rs], s61 offen sc1\n\t"
        "s_setprio 0\n\t"
        "ds_read_b32 v207, v206 offset:25104\n\t"
        "s_waitcnt lgkmcnt(0)\n\t"
        "v_cmp_eq_u32 vcc, %[rnd], v207\n\t"
        "s_barrier\n\t"
        "s_cbranch_vccnz Lck%=\n\t"
        "s_sub_u32 s97, %[r0], 0x100000\n\t"
        "s_waitcnt vmcnt(0)\n\t"
        "v_cmp_eq_u32 vcc, s62, v199\n\t"
        "v_subrev_u32 v196, s97, v196\n\t"
        "v_subrev_u32 v197, s97, v197\n\t"
        "v_cndmask_b32 v196, 0, v196, vcc\n\t"
        "v_cndmask_b32 v197, 0, v197, vcc\n\t"
        "v_cndmask_b32 v198, 0, v198, vcc\n\t"
        "v_max_u32_dpp v196, v196, v196 quad_perm:[1,0,3,2] row_mask:0xf bank_mask:0xf\n\t"
        "v_max_u32_dpp v197, v197, v197 quad_perm:[1,0,3,2] row_mask:0xf bank_mask:0xf\n\t"
        "v_max_u32_dpp v198, v198, v198 quad_perm:[1,0,3,2] row_mask:0xf bank_mask:0xf\n\t"
        "v_max_u32_dpp v196, v196, v196 quad_perm:[2,3,0,1] row_mask:0xf bank_mask:0xf\n\t"
        "v_max_u32_dpp v197, v197, v197 quad_perm:[2,3,0,1] row_mask:0xf bank_mask:0xf\n\t"
        "v_max_u32_dpp v198, v198, v198 quad_perm:[2,3,0,1] row_mask:0xf bank_mask:0xf\n\t"
        "v_max_u32_dpp v196, v196, v196 row_half_mirror row_mask:0xf bank_mask:0xf\n\t"
        "v_max_u32_dpp v197, v197, v197 row_half_mirror row_mask:0xf bank_mask:0xf\n\t"
        "v_max_u32_dpp v198, v198, v198 row_half_mirror row_mask:0xf bank_mask:0xf\n\t"
        "s_lshr_b32 s96, vcc_lo, 4\n\t"
        "v_readfirstlane_b32 s65, v197\n\t"
        "s_add_u32 s65, s65, s97\n\t"
        "s_or_b32 s96, s96, vcc_lo\n\t"
        "s_and_b32 s96, s96, 15\n\t"
        "s_cmp_eq_u32 s96, 15\n\t"
        "s_branch Lnx%=\n\t"
        "Lnan%=:\n\t"
        "s_cmp_lg_u32 %[poll], 1\n\t"
        "s_cbranch_scc1 Lok%=\n\t"
        "s_mov_b32 m0, %[lb]\n\t"
        "v_add_u32 v204, %[lb], %[vm]\n\t"
        "buffer_load_dwordx4 %[vm], %[rs], %[so] offen offset:0 sc1 lds\n\t"
        "buffer_load_dwordx4 %[vm], %[rs], %[so] offen offset:1024 sc1 lds\n\t"
        "buffer_load_dwordx4 %[vm], %[rs], %[so] offen offset:2048 sc1 lds\n\t"
        "buffer_load_dwordx4 %[vm], %[rs], %[so] offen offset:3072 sc1 lds\n\t"
        "s_add_u32 m0, m0, 0x1000\n\t"
        "s_nop 0\n\t"
        "buffer_load_dwordx4 %[vm], %[rs], s92 offen offset:0 sc1 lds\n\t"
        "buffer_load_dwordx4 %[vm], %[rs], s92 offen offset:1024 sc1 lds\n\t"
        "buffer_load_dwordx4 %[vm], %[rs], s92 offen offset:2048 sc1 lds\n\t"
        "buffer_load_dwordx4 %[vm], %[rs], s92 offen offset:3072 sc1 lds\n\t"
        "s_add_u32 m0, m0, 0x1000\n\t"
        "s_nop 0\n\t"
        "buffer_load_dwordx4 %[vm], %[rs], s93 offen offset:0 sc1 lds\n\t"
        "buffer_load_dwordx4 %[vm], %[rs], s93 offen offset:2048 sc1 lds\n\t"
        "v_max_u32 v207, v208, v209\n\t"
        "v_max3_u32 v207, v207, v210, v211\n\t"
        "v_max3_u32 v207, v207, v212, v213\n\t"
        "v_max3_u32 v207, v207, v214, v215\n\t"
        "v_max3_u32 v207, v207, v216, v217\n\t"
        "v_max3_u32 v207, v207, v218, v219\n\t"
        "v_max3_u32 v207, v207, v220, v221\n\t"
        "v_max3_u32 v207, v207, v222, v223\n\t"
        "v_max3_u32 v207, v207, v224, v225\n\t"
        "v_max3_u32 v207, v207, v226, v227\n\t"
        "v_max3_u32 v207, v207, v228, v229\n\t"
        "v_max3_u32 v207, v207, v230, v231\n\t"
        "v_max3_u32 v207, v207, v232, v233\n\t"
        "v_max3_u32 v207, v207, v234, v235\n\t"
        "v_max3_u32 v207, v207, v236, v237\n\t"
        "v_max3_u32 v207, v207, v238, v239\n\t"
        "v_max3_u32 v207, v207, v240, v241\n\t"
        "v_max3_u32 v207, v207, v242, v243\n\t"
        "v_max3_u32 v207, v207, v244, v245\n\t"
        "v_max3_u32 v207, v207, v246, v247\n\t"
        "v_cmp_eq_u32 vcc, 0xffbfffbf, v207\n\t"
        "v_mov_b32 v207, %[rnd]\n\t"
        "s_cbranch_vccz Lok%=\n\t"
        "ds_write_b32 v206, v207 offset:25104\n\t"
        "s_mov_b32 s95, 1\n\t"
        "s_waitcnt lgkmcnt(0)\n\t"
        "s_branch Lcv%=\n\t"
        "Lck%=:\n\t"
        "v_add_u32 v204, %[lb], %[vm]\n\t"
        "Lcl%=:\n\t"
        "s_mov_b32 m0, %[lb]\n\t"
        "s_nop 0\n\t"
        "buffer_load_dwordx4 %[vm], %[rs], %[so] offen offset:0 sc1 lds\n\t"
        "buffer_load_dwordx4 %[vm], %[rs], %[so] offen offset:1024 sc1 lds\n\t"
        "buffer_load_dwordx4 %[vm], %[rs], %[so] offen offset:2048 sc1 lds\n\t"
        "buffer_load_dwordx4 %[vm], %[rs], %[so] offen offset:3072 sc1 lds\n\t"
        "s_add_u32 m0, m0, 0x1000\n\t"
        "s_nop 0\n\t"
        "buffer_load_dwordx4 %[vm], %[rs], s92 offen offset:0 sc1 lds\n\t"
        "buffer_load_dwordx4 %[vm], %[rs], s92 offen offset:1024 sc1 lds\n\t"
        "buffer_load_dwordx4 %[vm], %[rs], s92 offen offset:2048 sc1 lds\n\t"
        "buffer_load_dwordx4 %[vm], %[rs], s92 offen offset:3072 sc1 lds\n\t"
        "s_add_u32 m0, m0, 0x1000\n\t"
        "s_nop 0\n\t"
        "buffer_load_dwordx4 %[vm], %[rs], s93 offen offset:0 sc1 lds\n\t"
        "buffer_load_dwordx4 %[vm], %[rs], s93 offen offset:2048 sc1 lds\n\t"
        "Lcv%=:\n\t"
        "s_sub_u32 %[left], %[left], 1\n\t"
        "s_waitcnt vmcnt(9)\n\t"
        "ds_read_b128 v[208:211], v204 offset:0\n\t"
        "s_waitcnt vmcnt(8)\n\t"
        "ds_read_b128 v[212:215], v204 offset:1024\n\t"
        "s_waitcnt vmcnt(7)\n\t"
        "ds_read_b128 v[216:219], v204 offset:2048\n\t"
        "s_waitcnt vmcnt(6)\n\t"
        "ds_read_b128 v[220:223], v204 offset:3072\n\t"
        "s_waitcnt vmcnt(5)\n\t"
        "ds_read_b128 v[224:227], v204 offset:4096\n\t"
        "s_waitcnt vmcnt(4)\n\t"
        "ds_read_b128 v[228:231], v204 offset:5120\n\t"
        "s_waitcnt lgkmcnt(5)\n\t"
        "v_max3_u32 v207, v208, v208, v209\n\t"
        "v_max3_u32 v207, v207, v210, v211\n\t"
        "s_waitcnt vmcnt(3)\n\t"
        "ds_read_b128 v[232:235], v204 offset:6144\n\t"
        "s_waitcnt lgkmcnt(5)\n\t"
        "v_max3_u32 v207, v207, v212, v213\n\t"
        "v_max3_u32 v207, v207, v214, v215\n\t"
        "s_waitcnt vmcnt(2)\n\t"
        "ds_read_b128 v[236:239], v204 offset:7168\n\t"
        "s_waitcnt lgkmcnt(5)\n\t"
        "v_max3_u32 v207, v207, v216, v217\n\t"
        "v_max3_u32 v207, v207, v218, v219\n\t"
        "s_waitcnt vmcnt(1)\n\t"
        "ds_read_b128 v[240:243], v204 offset:8192\n\t"
        "s_waitcnt lgkmcnt(5)\n\t"
        "v_max3_u32 v207, v207, v220, v221\n\t"
        "v_max3_u32 v207, v207, v222, v223\n\t"
        "s_waitcnt vmcnt(0)\n\t"
        "ds_read_b128 v[244:247], v204 offset:10240\n\t"
        "s_waitcnt lgkmcnt(5)\n\t"
        "v_max3_u32 v207, v207, v224, v225\n\t"
        "v_max3_u32 v207, v207, v226, v227\n\t"
        "s_waitcnt lgkmcnt(4)\n\t"
        "v_max3_u32 v207, v207, v228, v229\n\t"
        "v_max3_u32 v207, v207, v230, v231\n\t"
        "s_waitcnt lgkmcnt(3)\n\t"
        "v_max3_u32 v207, v207, v232, v233\n\t"
        "v_max3_u32 v207, v207, v234, v235\n\t"
        "s_waitcnt lgkmcnt(2)\n\t"
        "v_max3_u32 v207, v207, v236, v237\n\t"
        "v_max3_u32 v207, v207, v238, v239\n\t"
        "s_waitcnt lgkmcnt(1)\n\t"
        "v_max3_u32 v207, v207, v240, v241\n\t"
        "v_max3_u32 v207, v207, v242, v243\n\t"
        "s_waitcnt lgkmcnt(0)\n\t"
        "v_max3_u32 v207, v207, v244, v245\n\t"
        "v_max3_u32 v207, v207, v246, v247\n\t"
        "v_cmp_eq_u32 vcc, 0xffbfffbf, v207\n\t"
        "s_cbranch_vccz Lcd%=\n\t"
        "s_cbranch_scc0 Lcl%=\n\t"
        "Lcd%=:\n\t"
        "s_max_i32 %[left], %[left], 0\n\t"
        "s_cmp_eq_u32 s95, 0\n\t"
        "s_cbranch_scc1 Lce%=\n\t"
        "s_barrier\n\t"
        "s_barrier\n\t"
        "Lce%=:\n\t"
        "s_cbranch_vccz Ltop%=\n\t"
        "s_mov_b32 %[ok], 2\n\t"
        "Lend%=:\n\t"
        "s_mov_b32 m0, s94\n\t"
        "s_waitcnt vmcnt(0)\n\t"
        : [rnd] "+&s"(rnd), [s] "+&s"(s), [so] "+&s"(so), [tg] "+&s"(tg), [kr] "+&s"(kr), [c0] "+&s"(c0), [r0] "+&s"(r0), [ok] "=&s"(ok), [cnt] "+&s"(cnt), [left] "+&s"(left)
        : [w0] "{v[16:47]}"(W[0]), [w1] "{v[48:79]}"(W[1]), [w2] "{v[80:111]}"(W[2]), [w3] "{v[112:143]}"(W[3]), [w4] "{v[144:175]}"(W[4]), [va] "v"(va), [sidx] "v"(sidx), [ow] "v"(ow), [vm] "v"(vm), [tc] "v"(tc), [tv] "v"(tv), [rs] "s"(rs), [lb] "s"(lb), [md] "s"(md), [poll] "s"(poll), [last] "s"(last)
        : "s56", "s57", "s58", "s59", "s60", "s61", "s62", "s63", "s64", "s65", "s66", "s67", "s68", "s69", "s70", "s71", "s72", "s73", "s74", "s75", "s76", "s77", "s78", "s79", "s80", "s81", "s82", "s83", "s84", "s85", "s86", "s87", "s88", "s89", "s90", "s91", "s92", "s93", "s94", "s95", "s96", "s97", "s98", "s99", "v196", "v197", "v198", "v199", "v200", "v201", "v202", "v203", "v204", "v205", "v206", "v207", "v208", "v209", "v210", "v211", "v212", "v213", "v214", "v215", "v216", "v217", "v218", "v219", "v220", "v221", "v222", "v223", "v224", "v225", "v226", "v227", "v228", "v229", "v230", "v231", "v232", "v233", "v234", "v235", "v236", "v237", "v238", "v239", "v240", "v241", "v242", "v243", "v244", "v245", "v246", "v247", "v248", "v249", "v250", "v251", "v252", "v253", "v254", "v255", "vcc", "scc", "memory");
}

__device__ __forceinline__ void gather(u32 tg, u32 kr, u32 vm, u32 so, u32 lb, u32x4 rs) {
    asm volatile(
        "s_mov_b32 s94, m0\n\t"
        "s_add_u32 s92, %[so], 0x8000\n\t"
        "s_add_u32 s93, %[so], 0x10000\n\t"
        "s_mov_b32 m0, %[lb]\n\t"
        "Lda_%=:\n\t"
        "s_memrealtime s[90:91]\n\t"
        "s_waitcnt lgkmcnt(0)\n\t"
        "s_mul_i32 s90, s90, 24\n\t"
        "s_sub_u32 s88, %[tg], s90\n\t"
        "s_sub_u32 s89, s88, 620\n\t"
        "s_cmp_gt_u32 s89, 0x4000\n\t"
        "s_cbranch_scc1 Lea_%=\n\t"
        "s_sleep 1\n\t"
        "s_branch Lda_%=\n\t"
        "Lea_%=:\n\t"
        "s_cmp_lt_i32 s88, 120\n\t"
        "s_cbranch_scc1 Lza_%=\n\t"
        "s_memrealtime s[64:65]\n\t"
        "s_nop 0\n\t"
        "s_memrealtime s[66:67]\n\t"
        "s_nop 0\n\t"
        "s_memrealtime s[68:69]\n\t"
        "s_nop 0\n\t"
        "s_memrealtime s[70:71]\n\t"
        "s_nop 0\n\t"
        "s_memrealtime s[72:73]\n\t"
        "s_nop 0\n\t"
        "s_memrealtime s[74:75]\n\t"
        "s_memtime s[90:91]\n\t"
        "s_memrealtime s[76:77]\n\t"
        "s_nop 0\n\t"
        "s_memrealtime s[78:79]\n\t"
        "s_nop 0\n\t"
        "s_memrealtime s[80:81]\n\t"
        "s_nop 0\n\t"
        "s_memrealtime s[82:83]\n\t"
        "s_nop 0\n\t"
        "s_memrealtime s[84:85]\n\t"
        "s_nop 0\n\t"
        "s_memrealtime s[86:87]\n\t"
        "s_nop 0\n\t"
        "s_nop 7\n\t"
        "s_waitcnt lgkmcnt(0)\n\t"
        "s_add_u32 s64, s64, s66\n\t"
        "s_add_u32 s64, s64, s68\n\t"
        "s_add_u32 s64, s64, s70\n\t"
        "s_add_u32 s64, s64, s72\n\t"
        "s_add_u32 s64, s64, s74\n\t"
        "s_add_u32 s64, s64, s76\n\t"
        "s_add_u32 s64, s64, s78\n\t"
        "s_add_u32 s64, s64, s80\n\t"
        "s_add_u32 s64, s64, s82\n\t"
        "s_add_u32 s64, s64, s84\n\t"
        "s_add_u32 s64, s64, s86\n\t"
        "s_lshl_b32 s64, s64, 1\n\t"
        "s_nop 1\n\t"
        "s_sub_u32 s88, %[tg], s64\n\t"
        "s_mul_i32 s88, s88, %[kr]\n\t"
        "s_ashr_i32 s88, s88, 12\n\t"
        "s_sub_u32 s88, s88, 244\n\t"
        "s_nop 1\n\t"
        "s_max_i32 s88, s88, 0\n\t"
        "s_min_i32 s88, s88, 384\n\t"
        "s_andn2_b32 s88, s88, 3\n\t"
        "s_getpc_b64 s[90:91]\n\t"
        "Lga_%=:\n\t"
        "s_sub_u32 s90, s90, s88\n\t"
        "s_subb_u32 s91, s91, 0\n\t"
        "s_add_u32 s90, s90, Lza_%=-Lga_%=\n\t"
        "s_addc_u32 s91, s91, 0\n\t"
        "s_setpc_b64 s[90:91]\n\t"
        "s_nop 0\n\ts_nop 0\n\ts_nop 0\n\ts_nop 0\n\t"
        "s_nop 0\n\ts_nop 0\n\ts_nop 0\n\ts_nop 0\n\t"
        "s_nop 0\n\ts_nop 0\n\ts_nop 0\n\ts_nop 0\n\t"
        "s_nop 0\n\ts_nop 0\n\ts_nop 0\n\ts_nop 0\n\t"
        "s_nop 0\n\ts_nop 0\n\ts_nop 0\n\ts_nop 0\n\t"
        "s_nop 0\n\ts_nop 0\n\ts_nop 0\n\ts_nop 0\n\t"
        "s_nop 0\n\ts_nop 0\n\ts_nop 0\n\ts_nop 0\n\t"
        "s_nop 0\n\ts_nop 0\n\ts_nop 0\n\ts_nop 0\n\t"
        "s_nop 0\n\ts_nop 0\n\ts_nop 0\n\ts_nop 0\n\t"
        "s_nop 0\n\ts_nop 0\n\ts_nop 0\n\ts_nop 0\n\t"
        "s_nop 0\n\ts_nop 0\n\ts_nop 0\n\ts_nop 0\n\t"
        "s_nop 0\n\ts_nop 0\n\ts_nop 0\n\ts_nop 0\n\t"
        "s_nop 0\n\ts_nop 0\n\ts_nop 0\n\ts_nop 0\n\t"
        "s_nop 0\n\ts_nop 0\n\ts_nop 0\n\ts_nop 0\n\t"
        "s_nop 0\n\ts_nop 0\n\ts_nop 0\n\ts_nop 0\n\t"
        "s_nop 0\n\ts_nop 0\n\ts_nop 0\n\ts_nop 0\n\t"
        "s_nop 0\n\ts_nop 0\n\ts_nop 0\n\ts_nop 0\n\t"
        "s_nop 0\n\ts_nop 0\n\ts_nop 0\n\ts_nop 0\n\t"
        "s_nop 0\n\ts_nop 0\n\ts_nop 0\n\ts_nop 0\n\t"
        "s_nop 0\n\ts_nop 0\n\ts_nop 0\n\ts_nop 0\n\t"
        "s_nop 0\n\ts_nop 0\n\ts_nop 0\n\ts_nop 0\n\t"
        "s_nop 0\n\ts_nop 0\n\ts_nop 0\n\ts_nop 0\n\t"
        "s_nop 0\n\ts_nop 0\n\ts_nop 0\n\ts_nop 0\n\t"
        "s_nop 0\n\ts_nop 0\n\ts_nop 0\n\ts_nop 0\n\t"
        "Lza_%=:\n\t"
        "buffer_load_dwordx4 %[vm], %[rs], %[so] offen offset:0 sc1 lds\n\t"
        "buffer_load_dwordx4 %[vm], %[rs], %[so] offen offset:1024 sc1 lds\n\t"
        "buffer_load_dwordx4 %[vm], %[rs], %[so] offen offset:2048 sc1 lds\n\t"
        "buffer_load_dwordx4 %[vm], %[rs], %[so] offen offset:3072 sc1 lds\n\t"
        "s_add_u32 m0, m0, 0x1000\n\t"
        "s_nop 0\n\t"
        "buffer_load_dwordx4 %[vm], %[rs], s92 offen offset:0 sc1 lds\n\t"
        "buffer_load_dwordx4 %[vm], %[rs], s92 offen offset:1024 sc1 lds\n\t"
        "buffer_load_dwordx4 %[vm], %[rs], s92 offen offset:2048 sc1 lds\n\t"
        "buffer_load_dwordx4 %[vm], %[rs], s92 offen offset:3072 sc1 lds\n\t"
        "s_add_u32 m0, m0, 0x1000\n\t"
        "s_nop 0\n\t"
        "buffer_load_dwordx4 %[vm], %[rs], s93 offen offset:0 sc1 lds\n\t"
        "buffer_load_dwordx4 %[vm], %[rs], s93 offen offset:2048 sc1 lds\n\t"
        "s_mov_b32 m0, s94\n\t"
        :
        : [rs] "s"(rs), [tg] "s"(tg), [kr] "s"(kr), [vm] "v"(vm), [so] "s"(so), [lb] "s"(lb)
        : "s64", "s65", "s66", "s67", "s68", "s69", "s70", "s71", "s72", "s73", "s74", "s75", "s76", "s77", "s78", "s79", "s80", "s81", "s82", "s83", "s84", "s85", "s86", "s87", "s88", "s89", "s90", "s91", "s92", "s93", "s94", "vcc", "scc", "memory");
}

__device__ __forceinline__ void ts_look(u32& t1, u32& t2, u32& ok, u32x4 rs, u32 tsr, u32 want) {
    asm volatile(
        "s_nop 4\n\t"
        "s_buffer_load_dwordx4 s[64:67], %[rs], %[tsr] offset:0x0\n\t"
        "s_buffer_load_dwordx4 s[68:71], %[rs], %[tsr] offset:0x80\n\t"
        "s_buffer_load_dwordx4 s[72:75], %[rs], %[tsr] offset:0x100\n\t"
        "s_buffer_load_dwordx4 s[76:79], %[rs], %[tsr] offset:0x180\n\t"
        "s_waitcnt lgkmcnt(0)\n\t"
        "s_max_u32 %[t2], s66, s70\n\t"
        "s_max_u32 s64, s74, s78\n\t"
        "s_max_u32 %[t2], %[t2], s64\n\t"
        "s_xor_b32 s67, s67, %[want]\n\t"
        "s_xor_b32 s71, s71, %[want]\n\t"
        "s_xor_b32 s75, s75, %[want]\n\t"
        "s_xor_b32 s79, s79, %[want]\n\t"
        "s_sub_u32 s69, s69, s65\n\t"
        "s_sub_u32 s73, s73, s65\n\t"
        "s_sub_u32 s77, s77, s65\n\t"
        "s_max_i32 s69, s69, s73\n\t"
        "s_max_i32 s77, s77, 0\n\t"
        "s_max_i32 s69, s69, s77\n\t"
        "s_add_u32 s65, s65, s69\n\t"
        "s_or_b32 s66, s67, s71\n\t"
        "s_or_b32 s70, s75, s79\n\t"
        "s_or_b32 s70, s70, s66\n\t"
        "s_cmp_eq_u32 s70, 0\n\t"
        "s_cselect_b32 %[ok], 15, 0\n\t"
        "s_mov_b32 %[t1], s65\n\t"
        : [t1] "=&s"(t1), [t2] "=&s"(t2), [ok] "=&s"(ok)
        : [rs] "s"(rs), [tsr] "s"(tsr), [want] "s"(want)
        : "s64", "s65", "s66", "s67", "s68", "s69", "s70", "s71", "s72", "s73", "s74", "s75", "s76", "s77", "s78", "s79", "vcc", "scc", "memory");
}

__device__ __forceinline__ void pull(u32 vo, u32 tb, u32x4 rs, u32 n, u32 gap, u32 ld) {
    asm volatile(
        "s_mov_b32 s94, m0\n\t"
        "s_mov_b32 m0, %[ld]\n\t"
        "s_mov_b32 s92, 0\n\t"
        "Lp%=:\n\t"
        "v_readlane_b32 s93, %[tb], s92\n\t"
        "s_getpc_b64 s[90:91]\n\t"
        "Lgg_%=:\n\t"
        "s_sub_u32 s90, s90, %[gap]\n\t"
        "s_subb_u32 s91, s91, 0\n\t"
        "s_add_u32 s90, s90, Lzg_%=-Lgg_%=\n\t"
        "s_addc_u32 s91, s91, 0\n\t"
        "s_setpc_b64 s[90:91]\n\t"
        "s_nop 0\n\ts_nop 0\n\ts_nop 0\n\ts_nop 0\n\t"
        "s_nop 0\n\ts_nop 0\n\ts_nop 0\n\ts_nop 0\n\t"
        "s_nop 0\n\ts_nop 0\n\ts_nop 0\n\ts_nop 0\n\t"
        "s_nop 0\n\ts_nop 0\n\ts_nop 0\n\ts_nop 0\n\t"
        "s_nop 0\n\ts_nop 0\n\ts_nop 0\n\ts_nop 0\n\t"
        "s_nop 0\n\ts_nop 0\n\ts_nop 0\n\ts_nop 0\n\t"
        "s_nop 0\n\ts_nop 0\n\ts_nop 0\n\ts_nop 0\n\t"
        "s_nop 0\n\ts_nop 0\n\ts_nop 0\n\ts_nop 0\n\t"
        "s_nop 0\n\ts_nop 0\n\ts_nop 0\n\ts_nop 0\n\t"
        "s_nop 0\n\ts_nop 0\n\ts_nop 0\n\ts_nop 0\n\t"
        "s_nop 0\n\ts_nop 0\n\ts_nop 0\n\ts_nop 0\n\t"
        "s_nop 0\n\ts_nop 0\n\ts_nop 0\n\ts_nop 0\n\t"
        "s_nop 0\n\ts_nop 0\n\ts_nop 0\n\ts_nop 0\n\t"
        "s_nop 0\n\ts_nop 0\n\ts_nop 0\n\ts_nop 0\n\t"
        "s_nop 0\n\ts_nop 0\n\ts_nop 0\n\ts_nop 0\n\t"
        "s_nop 0\n\ts_nop 0\n\ts_nop 0\n\ts_nop 0\n\t"
        "Lzg_%=:\n\t"
        "buffer_load_dword %[vo], %[rs], s93 offen sc1 lds\n\t"
        "s_add_u32 s92, s92, 1\n\t"
        "s_cmp_lt_u32 s92, %[n]\n\t"
        "s_cbranch_scc1 Lp%=\n\t"
        "s_mov_b32 m0, s94\n\t"
        :
        : [vo] "v"(vo), [tb] "v"(tb), [rs] "s"(rs), [n] "s"(n), [gap] "s"(gap), [ld] "s"(ld)
        : "s88", "s89", "s90", "s91", "s92", "s93", "s94", "vcc", "scc", "memory");
}

__device__ __forceinline__ void calib(u32& c0, u32& r0) {
    asm volatile(
        "s_memrealtime s[64:65]\n\t"
        "s_nop 0\n\t"
        "s_memrealtime s[66:67]\n\t"
        "s_nop 0\n\t"
        "s_memrealtime s[68:69]\n\t"
        "s_nop 0\n\t"
        "s_memrealtime s[70:71]\n\t"
        "s_nop 0\n\t"
        "s_memrealtime s[72:73]\n\t"
        "s_nop 0\n\t"
        "s_memrealtime s[74:75]\n\t"
        "s_memtime s[90:91]\n\t"
        "s_memrealtime s[76:77]\n\t"
        "s_nop 0\n\t"
        "s_memrealtime s[78:79]\n\t"
        "s_nop 0\n\t"
        "s_memrealtime s[80:81]\n\t"
        "s_nop 0\n\t"
        "s_memrealtime s[82:83]\n\t"
        "s_nop 0\n\t"
        "s_memrealtime s[84:85]\n\t"
        "s_nop 0\n\t"
        "s_memrealtime s[86:87]\n\t"
        "s_nop 0\n\t"
        "s_nop 7\n\t"
        "s_waitcnt lgkmcnt(0)\n\t"
        "s_add_u32 s64, s64, s66\n\t"
        "s_add_u32 s64, s64, s68\n\t"
        "s_add_u32 s64, s64, s70\n\t"
        "s_add_u32 s64, s64, s72\n\t"
        "s_add_u32 s64, s64, s74\n\t"
        "s_add_u32 s64, s64, s76\n\t"
        "s_add_u32 s64, s64, s78\n\t"
        "s_add_u32 s64, s64, s80\n\t"
        "s_add_u32 s64, s64, s82\n\t"
        "s_add_u32 s64, s64, s84\n\t"
        "s_add_u32 s64, s64, s86\n\t"
        "s_lshl_b32 s64, s64, 1\n\t"
        "s_mov_b32 %[c0], s90\n\t"
        "s_mov_b32 %[r0], s64\n\t"
        : [c0] "=&s"(c0), [r0] "=&s"(r0)
        :
        : "s64", "s65", "s66", "s67", "s68", "s69", "s70", "s71", "s72", "s73", "s74", "s75", "s76", "s77", "s78", "s79", "s80", "s81", "s82", "s83", "s84", "s85", "s86", "s87", "s88", "s89", "s90", "s91", "vcc", "scc", "memory");
}

__device__ __forceinline__ void v_a0(const u32x32 (&W)[5], u32& rnd, u32 va, u32 sidx, u32 vw, u32 lb STP) {
    asm volatile(
        "Ltop%=:\n\t"
        "v_mov_b64 v[248:249], 0\n\t"
        "v_mov_b64 v[250:251], 0\n\t"
        "v_mov_b64 v[252:253], 0\n\t"
        "v_mov_b64 v[254:255], 0\n\t"
        "v_mov_b32 v200, 0xffbfffbf\n\t"
        "v_mov_b32 v206, %[lb]\n\t"
        "ds_write_b32 %[vw], v200\n\t"
        "ds_write_b32 %[vw], v200 offset:1024\n\t"
        "s_add_u32 %[rnd], %[rnd], 1\n\t"
        "s_setprio 3\n\t"
        "s_waitcnt lgkmcnt(0)\n\t"
        "s_barrier\n\t"
        "ds_read_b128 v[208:211], %[va] offset:0\n\t"
        "ds_read_b128 v[212:215], %[va] offset:1024\n\t"
        "ds_read_b128 v[216:219], %[va] offset:2048\n\t"
        "ds_read_b128 v[220:223], %[va] offset:3072\n\t"
        "ds_read_b128 v[224:227], %[va] offset:4096\n\t"
        "ds_read_b128 v[228:231], %[va] offset:5120\n\t"
        "s_waitcnt lgkmcnt(5)\n\t"
        "v_smfmac_f32_16x16x64_bf16 v[248:251], v[208:211], v[16:23], %[sidx]\n\t"
        "ds_read_b128 v[232:235], %[va] offset:6144\n\t"
        "v_smfmac_f32_16x16x64_bf16 v[252:255], v[208:211], v[96:103], %[sidx]\n\t"
        "s_waitcnt lgkmcnt(5)\n\t"
        "v_smfmac_f32_16x16x64_bf16 v[248:251], v[212:215], v[24:31], %[sidx]\n\t"
        "ds_read_b128 v[236:239], %[va] offset:7168\n\t"
        "v_smfmac_f32_16x16x64_bf16 v[252:255], v[212:215], v[104:111], %[sidx]\n\t"
        "s_waitcnt lgkmcnt(5)\n\t"
        "v_smfmac_f32_16x16x64_bf16 v[248:251], v[216:219], v[32:39], %[sidx]\n\t"
        "ds_read_b128 v[240:243], %[va] offset:8192\n\t"
        "s_waitcnt lgkmcnt(5)\n\t"
        "v_smfmac_f32_16x16x64_bf16 v[248:251], v[220:223], v[40:47], %[sidx]\n\t"
        "ds_read_b128 v[244:247], %[va] offset:10240\n\t"
        "s_waitcnt lgkmcnt(5)\n\t"
        "v_smfmac_f32_16x16x64_bf16 v[248:251], v[224:227], v[48:55], %[sidx]\n\t"
        "s_waitcnt lgkmcnt(4)\n\t"
        "v_smfmac_f32_16x16x64_bf16 v[248:251], v[228:231], v[56:63], %[sidx]\n\t"
        "s_waitcnt lgkmcnt(3)\n\t"
        "v_smfmac_f32_16x16x64_bf16 v[248:251], v[232:235], v[64:71], %[sidx]\n\t"
        "s_waitcnt lgkmcnt(2)\n\t"
        "v_smfmac_f32_16x16x64_bf16 v[248:251], v[236:239], v[72:79], %[sidx]\n\t"
        "s_waitcnt lgkmcnt(1)\n\t"
        "v_smfmac_f32_16x16x64_bf16 v[248:251], v[240:243], v[80:87], %[sidx]\n\t"
        "s_waitcnt lgkmcnt(0)\n\t"
        "v_smfmac_f32_16x16x64_bf16 v[248:251], v[244:247], v[88:95], %[sidx]\n\t"
        "v_smfmac_f32_16x16x64_bf16 v[252:255], v[216:219], v[112:119], %[sidx]\n\t"
        "v_smfmac_f32_16x16x64_bf16 v[252:255], v[220:223], v[120:127], %[sidx]\n\t"
        "v_add_f32 v248, v248, v250\n\t"
        "v_smfmac_f32_16x16x64_bf16 v[252:255], v[224:227], v[128:135], %[sidx]\n\t"
        "v_add_f32 v249, v249, v251\n\t"
        "v_smfmac_f32_16x16x64_bf16 v[252:255], v[228:231], v[136:143], %[sidx]\n\t"
        "v_cvt_pk_bf16_f32 v200, v248, v249\n\t"
        "v_smfmac_f32_16x16x64_bf16 v[252:255], v[232:235], v[144:151], %[sidx]\n\t"
        "ds_write_b32 %[vw], v200\n\t"
        "v_smfmac_f32_16x16x64_bf16 v[252:255], v[236:239], v[152:159], %[sidx]\n\t"
        "v_cmp_u_f32 vcc, v248, v249\n\t"
        "v_smfmac_f32_16x16x64_bf16 v[252:255], v[240:243], v[160:167], %[sidx]\n\t"
        "v_smfmac_f32_16x16x64_bf16 v[252:255], v[244:247], v[168:175], %[sidx]\n\t"
        "s_nop 7\n\t"
        "v_add_f32 v252, v252, v254\n\t"
        "v_add_f32 v253, v253, v255\n\t"
        "v_cvt_pk_bf16_f32 v201, v252, v253\n\t"
        "ds_write_b32 %[vw], v201 offset:1024\n\t"
        "s_setprio 0\n\t"
        "s_cbranch_vccnz Lnan%=\n\t"
        "Lok%=:\n\t"
        "s_waitcnt lgkmcnt(0)\n\t"
        "s_barrier\n\t"
        "ds_read_b32 v207, v206 offset:14336\n\t"
        "s_waitcnt lgkmcnt(0)\n\t"
        "v_cmp_eq_u32 vcc, %[rnd], v207\n\t"
        "s_barrier\n\t"
        "s_cbranch_vccnz Ltop%=\n\t"
        "s_branch Lend%=\n\t"
        "Lnan%=:\n\t"
        "v_max_u32 v207, v208, v209\n\t"
        "v_max3_u32 v207, v207, v210, v211\n\t"
        "v_max3_u32 v207, v207, v212, v213\n\t"
        "v_max3_u32 v207, v207, v214, v215\n\t"
        "v_max3_u32 v207, v207, v216, v217\n\t"
        "v_max3_u32 v207, v207, v218, v219\n\t"
        "v_max3_u32 v207, v207, v220, v221\n\t"
        "v_max3_u32 v207, v207, v222, v223\n\t"
        "v_max3_u32 v207, v207, v224, v225\n\t"
        "v_max3_u32 v207, v207, v226, v227\n\t"
        "v_max3_u32 v207, v207, v228, v229\n\t"
        "v_max3_u32 v207, v207, v230, v231\n\t"
        "v_max3_u32 v207, v207, v232, v233\n\t"
        "v_max3_u32 v207, v207, v234, v235\n\t"
        "v_max3_u32 v207, v207, v236, v237\n\t"
        "v_max3_u32 v207, v207, v238, v239\n\t"
        "v_max3_u32 v207, v207, v240, v241\n\t"
        "v_max3_u32 v207, v207, v242, v243\n\t"
        "v_max3_u32 v207, v207, v244, v245\n\t"
        "v_max3_u32 v207, v207, v246, v247\n\t"
        "v_cmp_eq_u32 vcc, 0xffbfffbf, v207\n\t"
        "v_mov_b32 v207, %[rnd]\n\t"
        "s_cbranch_vccz Lok%=\n\t"
        "ds_write_b32 v206, v207 offset:14336\n\t"
        "s_branch Lok%=\n\t"
        "Lend%=:\n\t"
        : [rnd] "+&s"(rnd)
        : [w0] "{v[16:47]}"(W[0]), [w1] "{v[48:79]}"(W[1]), [w2] "{v[80:111]}"(W[2]), [w3] "{v[112:143]}"(W[3]), [w4] "{v[144:175]}"(W[4]), [va] "v"(va), [sidx] "v"(sidx), [vw] "v"(vw), [lb] "s"(lb)
        : "v200", "v201", "v202", "v203", "v204", "v205", "v206", "v207", "v208", "v209", "v210", "v211", "v212", "v213", "v214", "v215", "v216", "v217", "v218", "v219", "v220", "v221", "v222", "v223", "v224", "v225", "v226", "v227", "v228", "v229", "v230", "v231", "v232", "v233", "v234", "v235", "v236", "v237", "v238", "v239", "v240", "v241", "v242", "v243", "v244", "v245", "v246", "v247", "v248", "v249", "v250", "v251", "v252", "v253", "v254", "v255", "vcc", "scc", "memory");
}

__device__ __forceinline__ void v_a1(const u32x32 (&W)[5], u32& rnd, u32 va, u32 sidx, u32 vw, u32 lb, u32& left, u32& s, u32& so, u32& tg, u32& kr, u32& c0, u32& r0, u32& ok, u32& cnt, u32 vm, u32 tc, u32 tv, u32 md, u32x4 rs, u32 poll, u32 last, u32 lag STP LGP) {
    asm volatile(
        "s_mov_b32 s94, m0\n\t"
        "Lst%=:\n\t"
        "v_readlane_b32 s60, %[tc], %[s]\n\t"
        "s_add_u32 s92, %[so], 0x8000\n\t"
        "s_add_u32 s93, %[so], 0x10000\n\t"
        "s_mov_b32 m0, %[md]\n\t"
        "s_add_u32 s62, %[s], 1\n\t"
        "s_mov_b32 s58, %[c0]\n\t"
        "s_mov_b32 s59, %[r0]\n\t"
        "s_mov_b32 s63, %[rnd]\n\t"
        "s_setprio 3\n\t"
        "s_add_u32 s61, s60, 0x10400\n\t"
        "s_mov_b32 s96, %[left]\n\t"
        "s_cmp_eq_u32 %[poll], 0\n\t"
        "s_cbranch_scc1 Ltop%=\n\t"
        "v_add_u32 v204, %[md], %[vm]\n\t"
        "v_mov_b32 v208, 0xffbfffbf\n\t"
        "v_mov_b32 v209, v208\n\t"
        "v_mov_b64 v[210:211], v[208:209]\n\t"
        "ds_write_b128 v204, v[208:211] offset:6144\n\t"
        "ds_write_b128 v204, v[208:211] offset:7168\n\t"
        "ds_write_b128 v204, v[208:211] offset:8192\n\t"
        "ds_write_b128 v204, v[208:211] offset:10240\n\t"
        "s_add_u32 %[tg], %[tg], %[lag]\n\t"
        "Lda_%=:\n\t"
        "s_memrealtime s[90:91]\n\t"
        "s_waitcnt lgkmcnt(0)\n\t"
        "s_mul_i32 s90, s90, 24\n\t"
        "s_sub_u32 s88, %[tg], s90\n\t"
        "s_sub_u32 s89, s88, 620\n\t"
        "s_cmp_gt_u32 s89, 0x4000\n\t"
        "s_cbranch_scc1 Lea_%=\n\t"
        "s_sleep 1\n\t"
        "s_branch Lda_%=\n\t"
        "Lea_%=:\n\t"
        "s_memrealtime s[64:65]\n\t"
        "s_nop 0\n\t"
        "s_memrealtime s[66:67]\n\t"
        "s_nop 0\n\t"
        "s_memrealtime s[68:69]\n\t"
        "s_nop 0\n\t"
        "s_memrealtime s[70:71]\n\t"
        "s_nop 0\n\t"
        "s_memrealtime s[72:73]\n\t"
        "s_nop 0\n\t"
        "s_memrealtime s[74:75]\n\t"
        "s_memtime s[90:91]\n\t"
        "s_memrealtime s[76:77]\n\t"
        "s_nop 0\n\t"
        "s_memrealtime s[78:79]\n\t"
        "s_nop 0\n\t"
        "s_memrealtime s[80:81]\n\t"
        "s_nop 0\n\t"
        "s_memrealtime s[82:83]\n\t"
        "s_nop 0\n\t"
        "s_memrealtime s[84:85]\n\t"
        "s_nop 0\n\t"
        "s_memrealtime s[86:87]\n\t"
        "s_nop 0\n\t"
        "s_nop 7\n\t"
        "s_waitcnt lgkmcnt(0)\n\t"
        "s_add_u32 s64, s64, s66\n\t"
        "s_add_u32 s64, s64, s68\n\t"
        "s_add_u32 s64, s64, s70\n\t"
        "s_add_u32 s64, s64, s72\n\t"
        "s_add_u32 s64, s64, s74\n\t"
        "s_add_u32 s64, s64, s76\n\t"
        "s_add_u32 s64, s64, s78\n\t"
        "s_add_u32 s64, s64, s80\n\t"
        "s_add_u32 s64, s64, s82\n\t"
        "s_add_u32 s64, s64, s84\n\t"
        "s_add_u32 s64, s64, s86\n\t"
        "s_lshl_b32 s64, s64, 1\n\t"
        "s_mov_b32 %[c0], s90\n\t"
        "s_mov_b32 %[r0], s64\n\t"
        "s_sub_u32 s88, %[tg], s64\n\t"
        "s_mul_i32 s88, s88, %[kr]\n\t"
        "s_ashr_i32 s88, s88, 12\n\t"
        "s_sub_u32 s88, s88, 244\n\t"
        "s_nop 1\n\t"
        "s_max_i32 s88, s88, 0\n\t"
        "s_min_i32 s88, s88, 384\n\t"
        "s_andn2_b32 s88, s88, 3\n\t"
        "s_getpc_b64 s[90:91]\n\t"
        "Lga_%=:\n\t"
        "s_sub_u32 s90, s90, s88\n\t"
        "s_subb_u32 s91, s91, 0\n\t"
        "s_add_u32 s90, s90, Lza_%=-Lga_%=\n\t"
        "s_addc_u32 s91, s91, 0\n\t"
        "s_setpc_b64 s[90:91]\n\t"
        "s_nop 0\n\ts_nop 0\n\ts_nop 0\n\ts_nop 0\n\t"
        "s_nop 0\n\ts_nop 0\n\ts_nop 0\n\ts_nop 0\n\t"
        "s_nop 0\n\ts_nop 0\n\ts_nop 0\n\ts_nop 0\n\t"
        "s_nop 0\n\ts_nop 0\n\ts_nop 0\n\ts_nop 0\n\t"
        "s_nop 0\n\ts_nop 0\n\ts_nop 0\n\ts_nop 0\n\t"
        "s_nop 0\n\ts_nop 0\n\ts_nop 0\n\ts_nop 0\n\t"
        "s_nop 0\n\ts_nop 0\n\ts_nop 0\n\ts_nop 0\n\t"
        "s_nop 0\n\ts_nop 0\n\ts_nop 0\n\ts_nop 0\n\t"
        "s_nop 0\n\ts_nop 0\n\ts_nop 0\n\ts_nop 0\n\t"
        "s_nop 0\n\ts_nop 0\n\ts_nop 0\n\ts_nop 0\n\t"
        "s_nop 0\n\ts_nop 0\n\ts_nop 0\n\ts_nop 0\n\t"
        "s_nop 0\n\ts_nop 0\n\ts_nop 0\n\ts_nop 0\n\t"
        "s_nop 0\n\ts_nop 0\n\ts_nop 0\n\ts_nop 0\n\t"
        "s_nop 0\n\ts_nop 0\n\ts_nop 0\n\ts_nop 0\n\t"
        "s_nop 0\n\ts_nop 0\n\ts_nop 0\n\ts_nop 0\n\t"
        "s_nop 0\n\ts_nop 0\n\ts_nop 0\n\ts_nop 0\n\t"
        "s_nop 0\n\ts_nop 0\n\ts_nop 0\n\ts_nop 0\n\t"
        "s_nop 0\n\ts_nop 0\n\ts_nop 0\n\ts_nop 0\n\t"
        "s_nop 0\n\ts_nop 0\n\ts_nop 0\n\ts_nop 0\n\t"
        "s_nop 0\n\ts_nop 0\n\ts_nop 0\n\ts_nop 0\n\t"
        "s_nop 0\n\ts_nop 0\n\ts_nop 0\n\ts_nop 0\n\t"
        "s_nop 0\n\ts_nop 0\n\ts_nop 0\n\ts_nop 0\n\t"
        "s_nop 0\n\ts_nop 0\n\ts_nop 0\n\ts_nop 0\n\t"
        "s_nop 0\n\ts_nop 0\n\ts_nop 0\n\ts_nop 0\n\t"
        "Lza_%=:\n\t"
        "buffer_load_dwordx4 %[vm], %[rs], %[so] offen offset:0 sc1 lds\n\t"
        "buffer_load_dwordx4 %[vm], %[rs], %[so] offen offset:1024 sc1 lds\n\t"
        "buffer_load_dwordx4 %[vm], %[rs], %[so] offen offset:2048 sc1 lds\n\t"
        "buffer_load_dwordx4 %[vm], %[rs], %[so] offen offset:3072 sc1 lds\n\t"
        "s_add_u32 m0, m0, 0x1000\n\t"
        "s_nop 0\n\t"
        "buffer_load_dwordx4 %[vm], %[rs], s92 offen offset:0 sc1 lds\n\t"
        "buffer_load_dwordx4 %[vm], %[rs], s92 offen offset:1024 sc1 lds\n\t"
        "buffer_load_dwordx4 %[vm], %[rs], s92 offen offset:2048 sc1 lds\n\t"
        "buffer_load_dwordx4 %[vm], %[rs], s92 offen offset:3072 sc1 lds\n\t"
        "s_add_u32 m0, m0, 0x1000\n\t"
        "s_nop 0\n\t"
        "buffer_load_dwordx4 %[vm], %[rs], s93 offen offset:0 sc1 lds\n\t"
        "buffer_load_dwordx4 %[vm], %[rs], s93 offen offset:2048 sc1 lds\n\t"
        "s_sub_u32 s59, %[r0], s59\n\t"
        "s_sub_u32 s58, %[c0], s58\n\t"
        "s_mul_i32 s88, s59, %[kr]\n\t"
        "s_lshr_b32 s88, s88, 12\n\t"
        "s_sub_u32 s58, s58, s88\n\t"
        "s_mul_i32 s58, s58, 3\n\t"
        "s_ashr_i32 s58, s58, 2\n\t"
        "s_sub_u32 s59, s59, 2048\n\t"
        "s_cmp_lt_u32 s59, 2560\n\t"
        "s_cselect_b32 s58, s58, 0\n\t"
        "s_add_u32 %[kr], %[kr], s58\n\t"
        "s_max_i32 %[kr], %[kr], 2560\n\t"
        "s_min_i32 %[kr], %[kr], 4352\n\t"
        "Ltop%=:\n\t"
        "v_mov_b64 v[248:249], 0\n\t"
        "v_mov_b64 v[250:251], 0\n\t"
        "v_mov_b64 v[252:253], 0\n\t"
        "v_mov_b64 v[254:255], 0\n\t"
        "v_mov_b32 v200, 0xffbfffbf\n\t"
        "v_mov_b32 v206, %[lb]\n\t"
        "ds_write_b32 %[vw], v200\n\t"
        "ds_write_b32 %[vw], v200 offset:1024\n\t"
        "s_add_u32 %[rnd], %[rnd], 1\n\t"
        "s_setprio 3\n\t"
        "s_mov_b32 s95, 0\n\t"
        "s_waitcnt vmcnt(4) lgkmcnt(0)\n\t"
        "s_barrier\n\t"
        "ds_read_b128 v[208:211], %[va] offset:0\n\t"
        "ds_read_b128 v[212:215], %[va] offset:1024\n\t"
        "ds_read_b128 v[216:219], %[va] offset:2048\n\t"
        "ds_read_b128 v[220:223], %[va] offset:3072\n\t"
        "ds_read_b128 v[224:227], %[va] offset:4096\n\t"
        "ds_read_b128 v[228:231], %[va] offset:5120\n\t"
        "s_waitcnt lgkmcnt(5)\n\t"
        "v_smfmac_f32_16x16x64_bf16 v[248:251], v[208:211], v[16:23], %[sidx]\n\t"
        "ds_read_b128 v[232:235], %[va] offset:6144\n\t"
        "v_smfmac_f32_16x16x64_bf16 v[252:255], v[208:211], v[96:103], %[sidx]\n\t"
        "s_waitcnt lgkmcnt(5)\n\t"
        "v_smfmac_f32_16x16x64_bf16 v[248:251], v[212:215], v[24:31], %[sidx]\n\t"
        "ds_read_b128 v[236:239], %[va] offset:7168\n\t"
        "v_smfmac_f32_16x16x64_bf16 v[252:255], v[212:215], v[104:111], %[sidx]\n\t"
        "s_waitcnt lgkmcnt(5)\n\t"
        "v_smfmac_f32_16x16x64_bf16 v[248:251], v[216:219], v[32:39], %[sidx]\n\t"
        "ds_read_b128 v[240:243], %[va] offset:8192\n\t"
        "s_waitcnt lgkmcnt(5)\n\t"
        "v_smfmac_f32_16x16x64_bf16 v[248:251], v[220:223], v[40:47], %[sidx]\n\t"
        "ds_read_b128 v[244:247], %[va] offset:10240\n\t"
        "s_waitcnt lgkmcnt(5)\n\t"
        "v_smfmac_f32_16x16x64_bf16 v[248:251], v[224:227], v[48:55], %[sidx]\n\t"
        "s_waitcnt lgkmcnt(4)\n\t"
        "v_smfmac_f32_16x16x64_bf16 v[248:251], v[228:231], v[56:63], %[sidx]\n\t"
        "s_waitcnt lgkmcnt(3)\n\t"
        "v_smfmac_f32_16x16x64_bf16 v[248:251], v[232:235], v[64:71], %[sidx]\n\t"
        "s_waitcnt lgkmcnt(2)\n\t"
        "v_smfmac_f32_16x16x64_bf16 v[248:251], v[236:239], v[72:79], %[sidx]\n\t"
        "s_waitcnt lgkmcnt(1)\n\t"
        "v_smfmac_f32_16x16x64_bf16 v[248:251], v[240:243], v[80:87], %[sidx]\n\t"
        "s_waitcnt lgkmcnt(0)\n\t"
        "v_smfmac_f32_16x16x64_bf16 v[248:251], v[244:247], v[88:95], %[sidx]\n\t"
        "v_smfmac_f32_16x16x64_bf16 v[252:255], v[216:219], v[112:119], %[sidx]\n\t"
        "v_smfmac_f32_16x16x64_bf16 v[252:255], v[220:223], v[120:127], %[sidx]\n\t"
        "v_add_f32 v248, v248, v250\n\t"
        "v_smfmac_f32_16x16x64_bf16 v[252:255], v[224:227], v[128:135], %[sidx]\n\t"
        "v_add_f32 v249, v249, v251\n\t"
        "v_smfmac_f32_16x16x64_bf16 v[252:255], v[228:231], v[136:143], %[sidx]\n\t"
        "v_cvt_pk_bf16_f32 v200, v248, v249\n\t"
        "v_smfmac_f32_16x16x64_bf16 v[252:255], v[232:235], v[144:151], %[sidx]\n\t"
        "ds_write_b32 %[vw], v200\n\t"
        "v_smfmac_f32_16x16x64_bf16 v[252:255], v[236:239], v[152:159], %[sidx]\n\t"
        "v_cmp_u_f32 vcc, v248, v249\n\t"
        "v_smfmac_f32_16x16x64_bf16 v[252:255], v[240:243], v[160:167], %[sidx]\n\t"
        "v_smfmac_f32_16x16x64_bf16 v[252:255], v[244:247], v[168:175], %[sidx]\n\t"
        "s_nop 7\n\t"
        "v_add_f32 v252, v252, v254\n\t"
        "v_add_f32 v253, v253, v255\n\t"
        "v_cvt_pk_bf16_f32 v201, v252, v253\n\t"
        "ds_write_b32 %[vw], v201 offset:1024\n\t"
        "s_setprio 0\n\t"
        "s_cbranch_vccnz Lnan%=\n\t"
        "Lok%=:\n\t"
        "s_cmp_lg_u32 s96, %[left]\n\t"
        "s_cbranch_scc1 Lv%=\n\t"
        "s_buffer_load_dwordx4 s[64:67], %[rs], s61 offset:0x0\n\t"
        "s_buffer_load_dwordx4 s[68:71], %[rs], s61 offset:0x80\n\t"
        "s_buffer_load_dwordx4 s[72:75], %[rs], s61 offset:0x100\n\t"
        "s_buffer_load_dwordx4 s[76:79], %[rs], s61 offset:0x180\n\t"
        "s_waitcnt lgkmcnt(0)\n\t"
        "s_barrier\n\t"
        "ds_read_b32 v207, v206 offset:14336\n\t"
        "s_waitcnt lgkmcnt(0)\n\t"
        "v_cmp_eq_u32 vcc, %[rnd], v207\n\t"
        "s_barrier\n\t"
        "s_cbranch_vccnz Lck%=\n\t"
        "s_xor_b32 s67, s67, s62\n\t"
        "s_xor_b32 s71, s71, s62\n\t"
        "s_xor_b32 s75, s75, s62\n\t"
        "s_xor_b32 s79, s79, s62\n\t"
        "s_sub_u32 s65, s65, %[r0]\n\t"
        "s_cmp_eq_u32 s67, 0\n\t"
        "s_cselect_b32 s65, s65, 0\n\t"
        "s_sub_u32 s69, s69, %[r0]\n\t"
        "s_cmp_eq_u32 s71, 0\n\t"
        "s_cselect_b32 s69, s69, 0\n\t"
        "s_sub_u32 s73, s73, %[r0]\n\t"
        "s_cmp_eq_u32 s75, 0\n\t"
        "s_cselect_b32 s73, s73, 0\n\t"
        "s_sub_u32 s77, s77, %[r0]\n\t"
        "s_cmp_eq_u32 s79, 0\n\t"
        "s_cselect_b32 s77, s77, 0\n\t"
        "s_max_u32 s65, s65, s69\n\t"
        "s_max_u32 s73, s73, s77\n\t"
        "s_max_u32 s65, s65, s73\n\t"
        "s_mov_b32 s64, s65\n\t"
        "s_sub_u32 s67, s67, 1\n\t"
        "s_cmp_lt_u32 s67, 0x20000\n\t"
        "s_cselect_b32 s64, 0, s64\n\t"
        "s_sub_u32 s71, s71, 1\n\t"
        "s_cmp_lt_u32 s71, 0x20000\n\t"
        "s_cselect_b32 s64, 0, s64\n\t"
        "s_sub_u32 s75, s75, 1\n\t"
        "s_cmp_lt_u32 s75, 0x20000\n\t"
        "s_cselect_b32 s64, 0, s64\n\t"
        "s_sub_u32 s79, s79, 1\n\t"
        "s_cmp_lt_u32 s79, 0x20000\n\t"
        "s_cselect_b32 s64, 0, s64\n\t"
        "s_add_u32 s65, s65, %[r0]\n\t"
        "s_cmp_lg_u32 s64, 0\n\t"
        "Lnx%=:\n\t"
        "s_cselect_b32 %[ok], 1, 0\n\t"
        "s_mov_b32 %[tg], s65\n\t"
        "s_mov_b32 %[so], s60\n\t"
        "s_sub_u32 s63, %[rnd], s63\n\t"
        "s_cmp_gt_u32 s63, 1\n\t"
        "s_addc_u32 %[cnt], %[cnt], 0\n\t"
        "s_mov_b32 %[s], s62\n\t"
        "s_waitcnt vmcnt(0)\n\t"
        "s_and_b32 s63, %[ok], %[poll]\n\t"
        "s_cmp_lt_i32 s62, %[last]\n\t"
        "s_cselect_b32 s63, s63, 0\n\t"
        "s_cmp_eq_u32 s63, 1\n\t"
        "s_cbranch_scc1 Lst%=\n\t"
        "s_branch Lend%=\n\t"
        "Lv%=:\n\t"
        "s_sleep 4\n\t"
        "buffer_load_dwordx4 v[196:199], %[tv], %[rs], s61 offen sc1\n\t"
        "s_waitcnt lgkmcnt(0)\n\t"
        "s_barrier\n\t"
        "ds_read_b32 v207, v206 offset:14336\n\t"
        "s_waitcnt lgkmcnt(0)\n\t"
        "v_cmp_eq_u32 vcc, %[rnd], v207\n\t"
        "s_barrier\n\t"
        "s_cbranch_vccnz Lck%=\n\t"
        "s_sub_u32 s97, %[r0], 0x100000\n\t"
        "s_waitcnt vmcnt(0)\n\t"
        "v_cmp_eq_u32 vcc, s62, v199\n\t"
        "v_subrev_u32 v196, s97, v196\n\t"
        "v_subrev_u32 v197, s97, v197\n\t"
        "v_cndmask_b32 v196, 0, v196, vcc\n\t"
        "v_cndmask_b32 v197, 0, v197, vcc\n\t"
        "v_cndmask_b32 v198, 0, v198, vcc\n\t"
        "v_max_u32_dpp v196, v196, v196 quad_perm:[1,0,3,2] row_mask:0xf bank_mask:0xf\n\t"
        "v_max_u32_dpp v197, v197, v197 quad_perm:[1,0,3,2] row_mask:0xf bank_mask:0xf\n\t"
        "v_max_u32_dpp v198, v198, v198 quad_perm:[1,0,3,2] row_mask:0xf bank_mask:0xf\n\t"
        "v_max_u32_dpp v196, v196, v196 quad_perm:[2,3,0,1] row_mask:0xf bank_mask:0xf\n\t"
        "v_max_u32_dpp v197, v197, v197 quad_perm:[2,3,0,1] row_mask:0xf bank_mask:0xf\n\t"
        "v_max_u32_dpp v198, v198, v198 quad_perm:[2,3,0,1] row_mask:0xf bank_mask:0xf\n\t"
        "v_max_u32_dpp v196, v196, v196 row_half_mirror row_mask:0xf bank_mask:0xf\n\t"
        "v_max_u32_dpp v197, v197, v197 row_half_mirror row_mask:0xf bank_mask:0xf\n\t"
        "v_max_u32_dpp v198, v198, v198 row_half_mirror row_mask:0xf bank_mask:0xf\n\t"
        "s_lshr_b32 s96, vcc_lo, 4\n\t"
        "v_readfirstlane_b32 s65, v197\n\t"
        "s_add_u32 s65, s65, s97\n\t"
        "s_or_b32 s96, s96, vcc_lo\n\t"
        "s_and_b32 s96, s96, 15\n\t"
        "s_cmp_eq_u32 s96, 15\n\t"
        "s_branch Lnx%=\n\t"
        "s_branch Lend%=\n\t"
        "Lnan%=:\n\t"
        "s_cmp_lg_u32 %[poll], 1\n\t"
        "s_cbranch_scc1 Lok%=\n\t"
        "s_mov_b32 m0, %[lb]\n\t"
        "v_add_u32 v204, %[lb], %[vm]\n\t"
        "buffer_load_dwordx4 %[vm], %[rs], %[so] offen offset:0 sc1 lds\n\t"
        "buffer_load_dwordx4 %[vm], %[rs], %[so] offen offset:1024 sc1 lds\n\t"
        "buffer_load_dwordx4 %[vm], %[rs], %[so] offen offset:2048 sc1 lds\n\t"
        "buffer_load_dwordx4 %[vm], %[rs], %[so] offen offset:3072 sc1 lds\n\t"
        "s_add_u32 m0, m0, 0x1000\n\t"
        "s_nop 0\n\t"
        "buffer_load_dwordx4 %[vm], %[rs], s92 offen offset:0 sc1 lds\n\t"
        "buffer_load_dwordx4 %[vm], %[rs], s92 offen offset:1024 sc1 lds\n\t"
        "buffer_load_dwordx4 %[vm], %[rs], s92 offen offset:2048 sc1 lds\n\t"
        "buffer_load_dwordx4 %[vm], %[rs], s92 offen offset:3072 sc1 lds\n\t"
        "s_add_u32 m0, m0, 0x1000\n\t"
        "s_nop 0\n\t"
        "buffer_load_dwordx4 %[vm], %[rs], s93 offen offset:0 sc1 lds\n\t"
        "buffer_load_dwordx4 %[vm], %[rs], s93 offen offset:2048 sc1 lds\n\t"
        "v_max_u32 v207, v208, v209\n\t"
        "v_max3_u32 v207, v207, v210, v211\n\t"
        "v_max3_u32 v207, v207, v212, v213\n\t"
        "v_max3_u32 v207, v207, v214, v215\n\t"
        "v_max3_u32 v207, v207, v216, v217\n\t"
        "v_max3_u32 v207, v207, v218, v219\n\t"
        "v_max3_u32 v207, v207, v220, v221\n\t"
        "v_max3_u32 v207, v207, v222, v223\n\t"
        "v_max3_u32 v207, v207, v224, v225\n\t"
        "v_max3_u32 v207, v207, v226, v227\n\t"
        "v_max3_u32 v207, v207, v228, v229\n\t"
        "v_max3_u32 v207, v207, v230, v231\n\t"
        "v_max3_u32 v207, v207, v232, v233\n\t"
        "v_max3_u32 v207, v207, v234, v235\n\t"
        "v_max3_u32 v207, v207, v236, v237\n\t"
        "v_max3_u32 v207, v207, v238, v239\n\t"
        "v_max3_u32 v207, v207, v240, v241\n\t"
        "v_max3_u32 v207, v207, v242, v243\n\t"
        "v_max3_u32 v207, v207, v244, v245\n\t"
        "v_max3_u32 v207, v207, v246, v247\n\t"
        "v_cmp_eq_u32 vcc, 0xffbfffbf, v207\n\t"
        "v_mov_b32 v207, %[rnd]\n\t"
        "s_cbranch_vccz Lok%=\n\t"
        "ds_write_b32 v206, v207 offset:14336\n\t"
        "s_mov_b32 s95, 1\n\t"
        "s_waitcnt lgkmcnt(0)\n\t"
        "s_branch Lcv%=\n\t"
        "Lck%=:\n\t"
        "v_add_u32 v204, %[lb], %[vm]\n\t"
        "Lcl%=:\n\t"
        "s_mov_b32 m0, %[lb]\n\t"
        "s_nop 0\n\t"
        "buffer_load_dwordx4 %[vm], %[rs], %[so] offen offset:0 sc1 lds\n\t"
        "buffer_load_dwordx4 %[vm], %[rs], %[so] offen offset:1024 sc1 lds\n\t"
        "buffer_load_dwordx4 %[vm], %[rs], %[so] offen offset:2048 sc1 lds\n\t"
        "buffer_load_dwordx4 %[vm], %[rs], %[so] offen offset:3072 sc1 lds\n\t"
        "s_add_u32 m0, m0, 0x1000\n\t"
        "s_nop 0\n\t"
        "buffer_load_dwordx4 %[vm], %[rs], s92 offen offset:0 sc1 lds\n\t"
        "buffer_load_dwordx4 %[vm], %[rs], s92 offen offset:1024 sc1 lds\n\t"
        "buffer_load_dwordx4 %[vm], %[rs], s92 offen offset:2048 sc1 lds\n\t"
        "buffer_load_dwordx4 %[vm], %[rs], s92 offen offset:3072 sc1 lds\n\t"
        "s_add_u32 m0, m0, 0x1000\n\t"
        "s_nop 0\n\t"
        "buffer_load_dwordx4 %[vm], %[rs], s93 offen offset:0 sc1 lds\n\t"
        "buffer_load_dwordx4 %[vm], %[rs], s93 offen offset:2048 sc1 lds\n\t"
        "Lcv%=:\n\t"
        "s_sub_u32 %[left], %[left], 1\n\t"
        "s_waitcnt vmcnt(9)\n\t"
        "ds_read_b128 v[208:211], v204 offset:0\n\t"
        "s_waitcnt vmcnt(8)\n\t"
        "ds_read_b128 v[212:215], v204 offset:1024\n\t"
        "s_waitcnt vmcnt(7)\n\t"
        "ds_read_b128 v[216:219], v204 offset:2048\n\t"
        "s_waitcnt vmcnt(6)\n\t"
        "ds_read_b128 v[220:223], v204 offset:3072\n\t"
        "s_waitcnt vmcnt(5)\n\t"
        "ds_read_b128 v[224:227], v204 offset:4096\n\t"
        "s_waitcnt vmcnt(4)\n\t"
        "ds_read_b128 v[228:231], v204 offset:5120\n\t"
        "s_waitcnt lgkmcnt(5)\n\t"
        "v_max3_u32 v207, v208, v208, v209\n\t"
        "v_max3_u32 v207, v207, v210, v211\n\t"
        "s_waitcnt vmcnt(3)\n\t"
        "ds_read_b128 v[232:235], v204 offset:6144\n\t"
        "s_waitcnt lgkmcnt(5)\n\t"
        "v_max3_u32 v207, v207, v212, v213\n\t"
        "v_max3_u32 v207, v207, v214, v215\n\t"
        "s_waitcnt vmcnt(2)\n\t"
        "ds_read_b128 v[236:239], v204 offset:7168\n\t"
        "s_waitcnt lgkmcnt(5)\n\t"
        "v_max3_u32 v207, v207, v216, v217\n\t"
        "v_max3_u32 v207, v207, v218, v219\n\t"
        "s_waitcnt vmcnt(1)\n\t"
        "ds_read_b128 v[240:243], v204 offset:8192\n\t"
        "s_waitcnt lgkmcnt(5)\n\t"
        "v_max3_u32 v207, v207, v220, v221\n\t"
        "v_max3_u32 v207, v207, v222, v223\n\t"
        "s_waitcnt vmcnt(0)\n\t"
        "ds_read_b128 v[244:247], v204 offset:10240\n\t"
        "s_waitcnt lgkmcnt(5)\n\t"
        "v_max3_u32 v207, v207, v224, v225\n\t"
        "v_max3_u32 v207, v207, v226, v227\n\t"
        "s_waitcnt lgkmcnt(4)\n\t"
        "v_max3_u32 v207, v207, v228, v229\n\t"
        "v_max3_u32 v207, v207, v230, v231\n\t"
        "s_waitcnt lgkmcnt(3)\n\t"
        "v_max3_u32 v207, v207, v232, v233\n\t"
        "v_max3_u32 v207, v207, v234, v235\n\t"
        "s_waitcnt lgkmcnt(2)\n\t"
        "v_max3_u32 v207, v207, v236, v237\n\t"
        "v_max3_u32 v207, v207, v238, v239\n\t"
        "s_waitcnt lgkmcnt(1)\n\t"
        "v_max3_u32 v207, v207, v240, v241\n\t"
        "v_max3_u32 v207, v207, v242, v243\n\t"
        "s_waitcnt lgkmcnt(0)\n\t"
        "v_max3_u32 v207, v207, v244, v245\n\t"
        "v_max3_u32 v207, v207, v246, v247\n\t"
        "v_cmp_eq_u32 vcc, 0xffbfffbf, v207\n\t"
        "s_cbranch_vccz Lcd%=\n\t"
        "s_cbranch_scc0 Lcl%=\n\t"
        "Lcd%=:\n\t"
        "s_max_i32 %[left], %[left], 0\n\t"
        "s_cmp_eq_u32 s95, 0\n\t"
        "s_cbranch_scc1 Lce%=\n\t"
        "s_barrier\n\t"
        "s_barrier\n\t"
        "Lce%=:\n\t"
        "s_cbranch_vccz Ltop%=\n\t"
        "s_mov_b32 %[ok], 2\n\t"
        "Lend%=:\n\t"
        "s_mov_b32 m0, s94\n\t"
        "s_waitcnt vmcnt(0)\n\t"
        : [rnd] "+&s"(rnd), [s] "+&s"(s), [so] "+&s"(so), [tg] "+&s"(tg), [kr] "+&s"(kr), [c0] "+&s"(c0), [r0] "+&s"(r0), [ok] "=&s"(ok), [cnt] "+&s"(cnt), [left] "+&s"(left)
        : [w0] "{v[16:47]}"(W[0]), [w1] "{v[48:79]}"(W[1]), [w2] "{v[80:111]}"(W[2]), [w3] "{v[112:143]}"(W[3]), [w4] "{v[144:175]}"(W[4]), [va] "v"(va), [sidx] "v"(sidx), [vw] "v"(vw), [vm] "v"(vm), [tc] "v"(tc), [tv] "v"(tv), [rs] "s"(rs), [lb] "s"(lb), [md] "s"(md), [poll] "s"(poll), [last] "s"(last), [lag] "s"(lag)
        : "s56", "s57", "s58", "s59", "s60", "s61", "s62", "s63", "s64", "s65", "s66", "s67", "s68", "s69", "s70", "s71", "s72", "s73", "s74", "s75", "s76", "s77", "s78", "s79", "s80", "s81", "s82", "s83", "s84", "s85", "s86", "s87", "s88", "s89", "s90", "s91", "s92", "s93", "s94", "s95", "s96", "s97", "s98", "s99", "v196", "v197", "v198", "v199", "v200", "v201", "v202", "v203", "v204", "v205", "v206", "v207", "v208", "v209", "v210", "v211", "v212", "v213", "v214", "v215", "v216", "v217", "v218", "v219", "v220", "v221", "v222", "v223", "v224", "v225", "v226", "v227", "v228", "v229", "v230", "v231", "v232", "v233", "v234", "v235", "v236", "v237", "v238", "v239", "v240", "v241", "v242", "v243", "v244", "v245", "v246", "v247", "v248", "v249", "v250", "v251", "v252", "v253", "v254", "v255", "vcc", "scc", "memory");
}

__device__ __forceinline__ void v_b(const u32x32 (&W)[5], u32& rnd, u32 sidx, u32 vr, u32 z0, u32 z1, u32 z2, u32x4 rs, u32 lb STP) {
    asm volatile(
        "Ltop%=:\n\t"
        "v_mov_b64 v[224:225], 0\n\t"
        "v_mov_b64 v[226:227], 0\n\t"
        "v_mov_b64 v[228:229], 0\n\t"
        "v_mov_b64 v[230:231], 0\n\t"
        "v_mov_b64 v[232:233], 0\n\t"
        "v_mov_b64 v[234:235], 0\n\t"
        "v_mov_b64 v[236:237], 0\n\t"
        "v_mov_b64 v[238:239], 0\n\t"
        "v_mov_b64 v[240:241], 0\n\t"
        "v_mov_b64 v[242:243], 0\n\t"
        "v_mov_b32 v206, %[lb]\n\t"
        "s_add_u32 %[rnd], %[rnd], 1\n\t"
        "s_mov_b32 s92, 0\n\t"
        "s_setprio 2\n\t"
        "s_barrier\n\t"
        "s_nop 15\n\t"
        "s_nop 15\n\t"
        "s_nop 15\n\t"
        "s_nop 15\n\t"
        "s_nop 15\n\t"
        "s_nop 7\n\t"
        "Lre%=:\n\t"
        "ds_read_b64_tr_b16 v[208:209], %[vr] offset:0\n\t"
        "ds_read_b64_tr_b16 v[210:211], %[vr] offset:64\n\t"
        "ds_read_b64_tr_b16 v[212:213], %[vr] offset:512\n\t"
        "ds_read_b64_tr_b16 v[214:215], %[vr] offset:576\n\t"
        "s_waitcnt lgkmcnt(0)\n\t"
        "v_smfmac_f32_16x16x64_bf16 v[224:227], v[208:211], v[16:23], %[sidx]\n\t"
        "v_smfmac_f32_16x16x64_bf16 v[228:231], v[208:211], v[48:55], %[sidx]\n\t"
        "v_smfmac_f32_16x16x64_bf16 v[232:235], v[208:211], v[80:87], %[sidx]\n\t"
        "v_smfmac_f32_16x16x64_bf16 v[236:239], v[208:211], v[112:119], %[sidx]\n\t"
        "v_smfmac_f32_16x16x64_bf16 v[240:243], v[208:211], v[144:151], %[sidx]\n\t"
        "ds_read_b64_tr_b16 v[216:217], %[vr] offset:1024\n\t"
        "v_smfmac_f32_16x16x64_bf16 v[224:227], v[212:215], v[24:31], %[sidx]\n\t"
        "ds_read_b64_tr_b16 v[218:219], %[vr] offset:1088\n\t"
        "v_smfmac_f32_16x16x64_bf16 v[228:231], v[212:215], v[56:63], %[sidx]\n\t"
        "ds_read_b64_tr_b16 v[220:221], %[vr] offset:1536\n\t"
        "v_smfmac_f32_16x16x64_bf16 v[232:235], v[212:215], v[88:95], %[sidx]\n\t"
        "ds_read_b64_tr_b16 v[222:223], %[vr] offset:1600\n\t"
        "v_smfmac_f32_16x16x64_bf16 v[236:239], v[212:215], v[120:127], %[sidx]\n\t"
        "v_smfmac_f32_16x16x64_bf16 v[240:243], v[212:215], v[152:159], %[sidx]\n\t"
        "s_waitcnt lgkmcnt(0)\n\t"
        "v_smfmac_f32_16x16x64_bf16 v[224:227], v[216:219], v[32:39], %[sidx]\n\t"
        "v_smfmac_f32_16x16x64_bf16 v[224:227], v[220:223], v[40:47], %[sidx]\n\t"
        "v_smfmac_f32_16x16x64_bf16 v[228:231], v[216:219], v[64:71], %[sidx]\n\t"
        "v_smfmac_f32_16x16x64_bf16 v[228:231], v[220:223], v[72:79], %[sidx]\n\t"
        "v_fma_f32 v205, v224, v225, v226\n\t"
        "v_smfmac_f32_16x16x64_bf16 v[232:235], v[216:219], v[96:103], %[sidx]\n\t"
        "v_cmp_u_f32 vcc, v205, v227\n\t"
        "v_smfmac_f32_16x16x64_bf16 v[232:235], v[220:223], v[104:111], %[sidx]\n\t"
        "v_cvt_pk_bf16_f32 v244, v224, v225\n\t"
        "v_smfmac_f32_16x16x64_bf16 v[236:239], v[216:219], v[128:135], %[sidx]\n\t"
        "v_cvt_pk_bf16_f32 v245, v226, v227\n\t"
        "v_smfmac_f32_16x16x64_bf16 v[236:239], v[220:223], v[136:143], %[sidx]\n\t"
        "v_cvt_pk_bf16_f32 v246, v228, v229\n\t"
        "v_smfmac_f32_16x16x64_bf16 v[240:243], v[216:219], v[160:167], %[sidx]\n\t"
        "v_cvt_pk_bf16_f32 v247, v230, v231\n\t"
        "v_smfmac_f32_16x16x64_bf16 v[240:243], v[220:223], v[168:175], %[sidx]\n\t"
        "s_cbranch_vccnz Lnan%=\n\t"
        "Lst%=:\n\t"
        "v_permlane16_swap_b32 v244, v246\n\t"
        "v_permlane16_swap_b32 v245, v247\n\t"
        "v_cvt_pk_bf16_f32 v248, v232, v233\n\t"
        "v_cvt_pk_bf16_f32 v249, v234, v235\n\t"
        "v_cvt_pk_bf16_f32 v250, v236, v237\n\t"
        "v_cvt_pk_bf16_f32 v251, v238, v239\n\t"
        "s_nop 0\n\t"
        "v_permlane16_swap_b32 v248, v250\n\t"
        "v_permlane16_swap_b32 v249, v251\n\t"
        "v_cvt_pk_bf16_f32 v252, v240, v241\n\t"
        "v_cvt_pk_bf16_f32 v254, v240, v241\n\t"
        "v_cvt_pk_bf16_f32 v253, v242, v243\n\t"
        "v_cvt_pk_bf16_f32 v255, v242, v243\n\t"
        "s_nop 0\n\t"
        "v_permlane16_swap_b32 v252, v254\n\t"
        "v_permlane16_swap_b32 v253, v255\n\t"
        "buffer_store_dwordx4 v[244:247], %[z0], %[rs], 0 offen sc1\n\t"
        "buffer_store_dwordx4 v[248:251], %[z1], %[rs], 0 offen sc1\n\t"
        "buffer_store_dwordx4 v[252:255], %[z2], %[rs], 0 offen sc1\n\t"
        "s_setprio 0\n\t"
        "s_cmp_eq_u32 s92, 2\n\t"
        "s_cbranch_scc1 Ly%=\n\t"
        "Lx%=:\n\t"
        "s_waitcnt lgkmcnt(0)\n\t"
        "s_barrier\n\t"
        "ds_read_b32 v207, v206 offset:14336\n\t"
        "s_waitcnt lgkmcnt(0)\n\t"
        "v_cmp_eq_u32 vcc, %[rnd], v207\n\t"
        "s_cmp_eq_u32 s92, 1\n\t"
        "s_cbranch_vccnz Lz%=\n\t"
        "s_cbranch_scc0 Ly%=\n\t"
        "s_mov_b32 s92, 2\n\t"
        "v_mov_b64 v[224:225], 0\n\t"
        "v_mov_b64 v[226:227], 0\n\t"
        "v_mov_b64 v[228:229], 0\n\t"
        "v_mov_b64 v[230:231], 0\n\t"
        "v_mov_b64 v[232:233], 0\n\t"
        "v_mov_b64 v[234:235], 0\n\t"
        "v_mov_b64 v[236:237], 0\n\t"
        "v_mov_b64 v[238:239], 0\n\t"
        "v_mov_b64 v[240:241], 0\n\t"
        "v_mov_b64 v[242:243], 0\n\t"
        "s_branch Lre%=\n\t"
        "Lnan%=:\n\t"
        "s_cmp_eq_u32 s92, 2\n\t"
        "s_cbranch_scc1 Lst%=\n\t"
        "s_mov_b32 s92, 1\n\t"
        "s_branch Lx%=\n\t"
        "Lz%=:\n\t"
        "s_barrier\n\t"
        "s_branch Ltop%=\n\t"
        "Ly%=:\n\t"
        "s_barrier\n\t"
        : [rnd] "+&s"(rnd)
        : [w0] "{v[16:47]}"(W[0]), [w1] "{v[48:79]}"(W[1]), [w2] "{v[80:111]}"(W[2]), [w3] "{v[112:143]}"(W[3]), [w4] "{v[144:175]}"(W[4]), [sidx] "v"(sidx), [vr] "v"(vr), [z0] "v"(z0), [z1] "v"(z1), [z2] "v"(z2), [rs] "s"(rs), [lb] "s"(lb)
        : "s92", "v205", "v206", "v207", "v208", "v209", "v210", "v211", "v212", "v213", "v214", "v215", "v216", "v217", "v218", "v219", "v220", "v221", "v222", "v223", "v224", "v225", "v226", "v227", "v228", "v229", "v230", "v231", "v232", "v233", "v234", "v235", "v236", "v237", "v238", "v239", "v240", "v241", "v242", "v243", "v244", "v245", "v246", "v247", "v248", "v249", "v250", "v251", "v252", "v253", "v254", "v255", "vcc", "scc", "memory");
}

__device__ __forceinline__ void l_g(u32& miss, u32& c0, u32& r0, u64& tl, u32 tg, u32 kr, u32 m0o, u32 so, u32x4 rs, u32x4 rec, u32 sr, u32 lim, u32 sh STP) {
    asm volatile(
        "s_mov_b32 %[miss], 1\n\t"
        "v_or_b32 v62, 0x10000, v59\n\t"
        "s_nop 1\n\t"
        "s_cmp_eq_u32 %[kr], 0\n\t"
        "s_cbranch_scc1 Lza_%=\n\t"
        "Lda_%=:\n\t"
        "s_memrealtime s[90:91]\n\t"
        "s_waitcnt lgkmcnt(0)\n\t"
        "s_mul_i32 s90, s90, 24\n\t"
        "s_sub_u32 s88, %[tg], s90\n\t"
        "s_sub_u32 s89, s88, 620\n\t"
        "s_cmp_gt_u32 s89, 0x4000\n\t"
        "s_cbranch_scc1 Lea_%=\n\t"
        "s_sleep 1\n\t"
        "s_branch Lda_%=\n\t"
        "Lea_%=:\n\t"
        "s_memrealtime s[64:65]\n\t"
        "s_nop 0\n\t"
        "s_memrealtime s[66:67]\n\t"
        "s_nop 0\n\t"
        "s_memrealtime s[68:69]\n\t"
        "s_nop 0\n\t"
        "s_memrealtime s[70:71]\n\t"
        "s_nop 0\n\t"
        "s_memrealtime s[72:73]\n\t"
        "s_nop 0\n\t"
        "s_memrealtime s[74:75]\n\t"
        "s_memtime s[90:91]\n\t"
        "s_memrealtime s[76:77]\n\t"
        "s_nop 0\n\t"
        "s_memrealtime s[78:79]\n\t"
        "s_nop 0\n\t"
        "s_memrealtime s[80:81]\n\t"
        "s_nop 0\n\t"
        "s_memrealtime s[82:83]\n\t"
        "s_nop 0\n\t"
        "s_memrealtime s[84:85]\n\t"
        "s_nop 0\n\t"
        "s_memrealtime s[86:87]\n\t"
        "s_nop 0\n\t"
        "s_nop 7\n\t"
        "s_waitcnt lgkmcnt(0)\n\t"
        "s_add_u32 s64, s64, s66\n\t"
        "s_add_u32 s64, s64, s68\n\t"
        "s_add_u32 s64, s64, s70\n\t"
        "s_add_u32 s64, s64, s72\n\t"
        "s_add_u32 s64, s64, s74\n\t"
        "s_add_u32 s64, s64, s76\n\t"
        "s_add_u32 s64, s64, s78\n\t"
        "s_add_u32 s64, s64, s80\n\t"
        "s_add_u32 s64, s64, s82\n\t"
        "s_add_u32 s64, s64, s84\n\t"
        "s_add_u32 s64, s64, s86\n\t"
        "s_lshl_b32 s64, s64, 1\n\t"
        "s_mov_b32 %[c0], s90\n\t"
        "s_mov_b32 %[r0], s64\n\t"
        "s_sub_u32 s88, %[tg], s64\n\t"
        "s_mul_i32 s88, s88, %[kr]\n\t"
        "s_ashr_i32 s88, s88, 12\n\t"
        "s_sub_u32 s88, s88, 244\n\t"
        "s_nop 1\n\t"
        "s_max_i32 s88, s88, 0\n\t"
        "s_min_i32 s88, s88, 384\n\t"
        "s_andn2_b32 s88, s88, 3\n\t"
        "s_getpc_b64 s[90:91]\n\t"
        "Lga_%=:\n\t"
        "s_sub_u32 s90, s90, s88\n\t"
        "s_subb_u32 s91, s91, 0\n\t"
        "s_add_u32 s90, s90, Lza_%=-Lga_%=\n\t"
        "s_addc_u32 s91, s91, 0\n\t"
        "s_setpc_b64 s[90:91]\n\t"
        "s_nop 0\n\ts_nop 0\n\ts_nop 0\n\ts_nop 0\n\t"
        "s_nop 0\n\ts_nop 0\n\ts_nop 0\n\ts_nop 0\n\t"
        "s_nop 0\n\ts_nop 0\n\ts_nop 0\n\ts_nop 0\n\t"
        "s_nop 0\n\ts_nop 0\n\ts_nop 0\n\ts_nop 0\n\t"
        "s_nop 0\n\ts_nop 0\n\ts_nop 0\n\ts_nop 0\n\t"
        "s_nop 0\n\ts_nop 0\n\ts_nop 0\n\ts_nop 0\n\t"
        "s_nop 0\n\ts_nop 0\n\ts_nop 0\n\ts_nop 0\n\t"
        "s_nop 0\n\ts_nop 0\n\ts_nop 0\n\ts_nop 0\n\t"
        "s_nop 0\n\ts_nop 0\n\ts_nop 0\n\ts_nop 0\n\t"
        "s_nop 0\n\ts_nop 0\n\ts_nop 0\n\ts_nop 0\n\t"
        "s_nop 0\n\ts_nop 0\n\ts_nop 0\n\ts_nop 0\n\t"
        "s_nop 0\n\ts_nop 0\n\ts_nop 0\n\ts_nop 0\n\t"
        "s_nop 0\n\ts_nop 0\n\ts_nop 0\n\ts_nop 0\n\t"
        "s_nop 0\n\ts_nop 0\n\ts_nop 0\n\ts_nop 0\n\t"
        "s_nop 0\n\ts_nop 0\n\ts_nop 0\n\ts_nop 0\n\t"
        "s_nop 0\n\ts_nop 0\n\ts_nop 0\n\ts_nop 0\n\t"
        "s_nop 0\n\ts_nop 0\n\ts_nop 0\n\ts_nop 0\n\t"
        "s_nop 0\n\ts_nop 0\n\ts_nop 0\n\ts_nop 0\n\t"
        "s_nop 0\n\ts_nop 0\n\ts_nop 0\n\ts_nop 0\n\t"
        "s_nop 0\n\ts_nop 0\n\ts_nop 0\n\ts_nop 0\n\t"
        "s_nop 0\n\ts_nop 0\n\ts_nop 0\n\ts_nop 0\n\t"
        "s_nop 0\n\ts_nop 0\n\ts_nop 0\n\ts_nop 0\n\t"
        "s_nop 0\n\ts_nop 0\n\ts_nop 0\n\ts_nop 0\n\t"
        "s_nop 0\n\ts_nop 0\n\ts_nop 0\n\ts_nop 0\n\t"
        "Lza_%=:\n\t"
        "buffer_load_dword v60, %[m0o], %[rs], %[so] offen sc1\n\t"
        "s_memtime s[92:93]\n\t"
        "s_memrealtime %[tl]\n\t"
        "v_add_u32 v64, %[sh], v56\n\t"
        "v_add_u32 v65, %[sh], v57\n\t"
        "v_mov_b64 v[66:67], v[58:59]\n\t"
        "v_or_b32 v61, 0x20000, v59\n\t"
        "s_waitcnt vmcnt(0)\n\t"
        "s_memtime s[94:95]\n\t"
        "v_cmp_eq_u32 vcc, 0xffbfffbf, v60\n\t"
        "s_cbranch_vccnz Lm%=\n\t"
        "s_waitcnt lgkmcnt(0)\n\t"
        "s_sub_u32 s92, s94, s92\n\t"
        "s_cmp_gt_u32 s92, %[lim]\n\t"
        "s_cbranch_scc1 Ls%=\n\t"
        "buffer_store_dwordx4 v[56:59], %[sr], %[rs], 0 offen sc1\n\t"
        "s_mov_b32 %[miss], 0\n\t"
        "s_branch Lend%=\n\t"
        "Ls%=:\n\t"
        "buffer_store_dwordx4 v[64:67], %[sr], %[rs], 0 offen offset:512 sc1\n\t"
        "buffer_store_dword v61, %[sr], %[rs], 0 offen offset:12 sc1\n\t"
        "s_mov_b32 %[miss], 0\n\t"
        "s_branch Lend%=\n\t"
        "Lm%=:\n\t"
        "buffer_store_dword v62, %[sr], %[rs], 0 offen offset:12 sc1\n\t"
        "Lend%=:\n\t"
        "s_waitcnt lgkmcnt(0)\n\t"
        : [c0] "+&s"(c0), [r0] "+&s"(r0), [tl] "=&s"(tl), [miss] "=&s"(miss)
        : [m0o] "v"(m0o), [sr] "v"(sr), [rs] "s"(rs), [so] "s"(so), [tg] "s"(tg), [kr] "s"(kr), [rec] "{v[56:59]}"(rec), [lim] "s"(lim), [sh] "s"(sh)
        : "s64", "s65", "s66", "s67", "s68", "s69", "s70", "s71", "s72", "s73", "s74", "s75", "s76", "s77", "s78", "s79", "s80", "s81", "s82", "s83", "s84", "s85", "s86", "s87", "s88", "s89", "s90", "s91", "s92", "s93", "s94", "s95", "v60", "v61", "v62", "v64", "v65", "v66", "v67", "vcc", "scc", "memory");
}

__device__ __forceinline__ void r_te(u32 lb, u32 fp) {
    asm volatile(
        "v_mov_b32 v20, %[lb]\n\t"
        "s_cmp_eq_u32 %[fp], 1\n\t"
        "s_cbranch_scc1 Lt%=\n\t"
        "s_barrier\n\t"
        "ds_read_b32 v21, v20 offset:16392\n\t"
        "s_waitcnt lgkmcnt(0)\n\t"
        "v_cmp_eq_u32 vcc, %[fp], v21\n\t"
        "s_cbranch_vccz Lt%=\n\t"
        "s_barrier\n\t"
        "ds_read_b32 v21, v20 offset:16396\n\t"
        "s_waitcnt lgkmcnt(0)\n\t"
        "v_cmp_eq_u32 vcc, %[fp], v21\n\t"
        "s_cbranch_vccz Lt%=\n\t"
        "s_barrier\n\t"
        "s_barrier\n\t"
        "Lt%=:\n\t"
        :
        : [lb] "s"(lb), [fp] "s"(fp)
        : "v20", "v21", "vcc", "scc", "memory");
}

__device__ __forceinline__ void r_t0(u32& c0, u32& r0, u32& t1, u32& t2, u32& ok, u32 tl, u32 t0, u32 kr, u32 fv, u32 fp, u32 lb, u32x4 rs, u32 so STP) {
    asm volatile(
        "s_setprio 3\n\t"
        "v_mov_b32 v20, %[lb]\n\t"
        "s_mov_b32 %[ok], 0\n\t"
        "s_or_b32 s92, %[fv], 0x10000\n\t"
        "s_mov_b32 s51, %[fv]\n\t"
        "s_mov_b32 s55, %[fv]\n\t"
        "s_mov_b32 s59, %[fv]\n\t"
        "s_mov_b32 s63, %[fv]\n\t"
        "s_cmp_eq_u32 %[fp], 1\n\t"
        "s_cbranch_scc1 Lt%=\n\t"
        "s_barrier\n\t"
        "ds_read_b32 v21, v20 offset:16392\n\t"
        "s_waitcnt lgkmcnt(0)\n\t"
        "v_cmp_eq_u32 vcc, %[fp], v21\n\t"
        "s_cbranch_vccz Lt%=\n\t"
        "s_barrier\n\t"
        "ds_read_b32 v21, v20 offset:16396\n\t"
        "s_waitcnt lgkmcnt(0)\n\t"
        "v_cmp_eq_u32 vcc, %[fp], v21\n\t"
        "s_cbranch_vccz Lt%=\n\t"
        "s_barrier\n\t"
        "s_barrier\n\t"
        "Lt%=:\n\t"
        "s_bfe_u32 s95, %[t2], 0x8000c\n\t"
        "s_mul_i32 s95, s95, %[kr]\n\t"
        "s_lshr_b32 s95, s95, 12\n\t"
        "s_min_u32 s95, s95, 512\n\t"
        "s_andn2_b32 s95, s95, 3\n\t"
        "v_mov_b32 v21, s95\n\t"
        "ds_write_b32 v20, v21 offset:16384\n\t"
        "Ldl_%=:\n\t"
        "s_memrealtime s[90:91]\n\t"
        "s_waitcnt lgkmcnt(0)\n\t"
        "s_mul_i32 s90, s90, 24\n\t"
        "s_sub_u32 s88, %[tl], s90\n\t"
        "s_sub_u32 s89, s88, 620\n\t"
        "s_cmp_gt_u32 s89, 0x4000\n\t"
        "s_cbranch_scc1 Lel_%=\n\t"
        "s_sleep 1\n\t"
        "s_branch Ldl_%=\n\t"
        "Lel_%=:\n\t"
        "s_memrealtime s[64:65]\n\t"
        "s_nop 0\n\t"
        "s_memrealtime s[66:67]\n\t"
        "s_nop 0\n\t"
        "s_memrealtime s[68:69]\n\t"
        "s_nop 0\n\t"
        "s_memrealtime s[70:71]\n\t"
        "s_nop 0\n\t"
        "s_memrealtime s[72:73]\n\t"
        "s_nop 0\n\t"
        "s_memrealtime s[74:75]\n\t"
        "s_memtime s[90:91]\n\t"
        "s_memrealtime s[76:77]\n\t"
        "s_nop 0\n\t"
        "s_memrealtime s[78:79]\n\t"
        "s_nop 0\n\t"
        "s_memrealtime s[80:81]\n\t"
        "s_nop 0\n\t"
        "s_memrealtime s[82:83]\n\t"
        "s_nop 0\n\t"
        "s_memrealtime s[84:85]\n\t"
        "s_nop 0\n\t"
        "s_memrealtime s[86:87]\n\t"
        "s_nop 0\n\t"
        "s_nop 7\n\t"
        "s_waitcnt lgkmcnt(0)\n\t"
        "s_add_u32 s64, s64, s66\n\t"
        "s_add_u32 s64, s64, s68\n\t"
        "s_add_u32 s64, s64, s70\n\t"
        "s_add_u32 s64, s64, s72\n\t"
        "s_add_u32 s64, s64, s74\n\t"
        "s_add_u32 s64, s64, s76\n\t"
        "s_add_u32 s64, s64, s78\n\t"
        "s_add_u32 s64, s64, s80\n\t"
        "s_add_u32 s64, s64, s82\n\t"
        "s_add_u32 s64, s64, s84\n\t"
        "s_add_u32 s64, s64, s86\n\t"
        "s_lshl_b32 s64, s64, 1\n\t"
        "s_mov_b32 %[c0], s90\n\t"
        "s_mov_b32 %[r0], s64\n\t"
        "s_sub_u32 s88, %[tl], s64\n\t"
        "s_mul_i32 s88, s88, %[kr]\n\t"
        "s_ashr_i32 s88, s88, 12\n\t"
        "s_sub_u32 s88, s88, 244\n\t"
        "s_min_i32 s89, s88, 0\n\t"
        "s_nop 0\n\t"
        "s_max_i32 s88, s88, 0\n\t"
        "s_min_i32 s88, s88, 384\n\t"
        "s_andn2_b32 s88, s88, 3\n\t"
        "s_getpc_b64 s[90:91]\n\t"
        "Lgl_%=:\n\t"
        "s_sub_u32 s90, s90, s88\n\t"
        "s_subb_u32 s91, s91, 0\n\t"
        "s_add_u32 s90, s90, Lzl_%=-Lgl_%=\n\t"
        "s_addc_u32 s91, s91, 0\n\t"
        "s_setpc_b64 s[90:91]\n\t"
        "s_nop 0\n\ts_nop 0\n\ts_nop 0\n\ts_nop 0\n\t"
        "s_nop 0\n\ts_nop 0\n\ts_nop 0\n\ts_nop 0\n\t"
        "s_nop 0\n\ts_nop 0\n\ts_nop 0\n\ts_nop 0\n\t"
        "s_nop 0\n\ts_nop 0\n\ts_nop 0\n\ts_nop 0\n\t"
        "s_nop 0\n\ts_nop 0\n\ts_nop 0\n\ts_nop 0\n\t"
        "s_nop 0\n\ts_nop 0\n\ts_nop 0\n\ts_nop 0\n\t"
        "s_nop 0\n\ts_nop 0\n\ts_nop 0\n\ts_nop 0\n\t"
        "s_nop 0\n\ts_nop 0\n\ts_nop 0\n\ts_nop 0\n\t"
        "s_nop 0\n\ts_nop 0\n\ts_nop 0\n\ts_nop 0\n\t"
        "s_nop 0\n\ts_nop 0\n\ts_nop 0\n\ts_nop 0\n\t"
        "s_nop 0\n\ts_nop 0\n\ts_nop 0\n\ts_nop 0\n\t"
        "s_nop 0\n\ts_nop 0\n\ts_nop 0\n\ts_nop 0\n\t"
        "s_nop 0\n\ts_nop 0\n\ts_nop 0\n\ts_nop 0\n\t"
        "s_nop 0\n\ts_nop 0\n\ts_nop 0\n\ts_nop 0\n\t"
        "s_nop 0\n\ts_nop 0\n\ts_nop 0\n\ts_nop 0\n\t"
        "s_nop 0\n\ts_nop 0\n\ts_nop 0\n\ts_nop 0\n\t"
        "s_nop 0\n\ts_nop 0\n\ts_nop 0\n\ts_nop 0\n\t"
        "s_nop 0\n\ts_nop 0\n\ts_nop 0\n\ts_nop 0\n\t"
        "s_nop 0\n\ts_nop 0\n\ts_nop 0\n\ts_nop 0\n\t"
        "s_nop 0\n\ts_nop 0\n\ts_nop 0\n\ts_nop 0\n\t"
        "s_nop 0\n\ts_nop 0\n\ts_nop 0\n\ts_nop 0\n\t"
        "s_nop 0\n\ts_nop 0\n\ts_nop 0\n\ts_nop 0\n\t"
        "s_nop 0\n\ts_nop 0\n\ts_nop 0\n\ts_nop 0\n\t"
        "s_nop 0\n\ts_nop 0\n\ts_nop 0\n\ts_nop 0\n\t"
        "Lzl_%=:\n\t"
        "s_buffer_load_dwordx4 s[48:51], %[rs], %[so] offset:0x0\n\t"
        "s_buffer_load_dwordx4 s[52:55], %[rs], %[so] offset:0x80\n\t"
        "s_buffer_load_dwordx4 s[56:59], %[rs], %[so] offset:0x100\n\t"
        "s_buffer_load_dwordx4 s[60:63], %[rs], %[so] offset:0x180\n\t"
        "s_sub_u32 s88, %[t0], %[tl]\n\t"
        "s_mul_i32 s88, s88, %[kr]\n\t"
        "s_ashr_i32 s88, s88, 12\n\t"
        "s_add_u32 s88, s88, s89\n\t"
        "s_sub_u32 s88, s88, 156\n\t"
        "s_max_i32 s88, s88, 0\n\t"
        "s_min_i32 s88, s88, 1280\n\t"
        "s_andn2_b32 s88, s88, 3\n\t"
        "s_cmp_lt_i32 s89, -64\n\t"
        "s_cbranch_scc1 Lw%=\n\t"
        "s_getpc_b64 s[90:91]\n\t"
        "Lga_%=:\n\t"
        "s_sub_u32 s90, s90, s88\n\t"
        "s_subb_u32 s91, s91, 0\n\t"
        "s_add_u32 s90, s90, Lza_%=-Lga_%=\n\t"
        "s_addc_u32 s91, s91, 0\n\t"
        "s_setpc_b64 s[90:91]\n\t"
        "s_nop 0\n\ts_nop 0\n\ts_nop 0\n\ts_nop 0\n\t"
        "s_nop 0\n\ts_nop 0\n\ts_nop 0\n\ts_nop 0\n\t"
        "s_nop 0\n\ts_nop 0\n\ts_nop 0\n\ts_nop 0\n\t"
        "s_nop 0\n\ts_nop 0\n\ts_nop 0\n\ts_nop 0\n\t"
        "s_nop 0\n\ts_nop 0\n\ts_nop 0\n\ts_nop 0\n\t"
        "s_nop 0\n\ts_nop 0\n\ts_nop 0\n\ts_nop 0\n\t"
        "s_nop 0\n\ts_nop 0\n\ts_nop 0\n\ts_nop 0\n\t"
        "s_nop 0\n\ts_nop 0\n\ts_nop 0\n\ts_nop 0\n\t"
        "s_nop 0\n\ts_nop 0\n\ts_nop 0\n\ts_nop 0\n\t"
        "s_nop 0\n\ts_nop 0\n\ts_nop 0\n\ts_nop 0\n\t"
        "s_nop 0\n\ts_nop 0\n\ts_nop 0\n\ts_nop 0\n\t"
        "s_nop 0\n\ts_nop 0\n\ts_nop 0\n\ts_nop 0\n\t"
        "s_nop 0\n\ts_nop 0\n\ts_nop 0\n\ts_nop 0\n\t"
        "s_nop 0\n\ts_nop 0\n\ts_nop 0\n\ts_nop 0\n\t"
        "s_nop 0\n\ts_nop 0\n\ts_nop 0\n\ts_nop 0\n\t"
        "s_nop 0\n\ts_nop 0\n\ts_nop 0\n\ts_nop 0\n\t"
        "s_nop 0\n\ts_nop 0\n\ts_nop 0\n\ts_nop 0\n\t"
        "s_nop 0\n\ts_nop 0\n\ts_nop 0\n\ts_nop 0\n\t"
        "s_nop 0\n\ts_nop 0\n\ts_nop 0\n\ts_nop 0\n\t"
        "s_nop 0\n\ts_nop 0\n\ts_nop 0\n\ts_nop 0\n\t"
        "s_nop 0\n\ts_nop 0\n\ts_nop 0\n\ts_nop 0\n\t"
        "s_nop 0\n\ts_nop 0\n\ts_nop 0\n\ts_nop 0\n\t"
        "s_nop 0\n\ts_nop 0\n\ts_nop 0\n\ts_nop 0\n\t"
        "s_nop 0\n\ts_nop 0\n\ts_nop 0\n\ts_nop 0\n\t"
        "s_nop 0\n\ts_nop 0\n\ts_nop 0\n\ts_nop 0\n\t"
        "s_nop 0\n\ts_nop 0\n\ts_nop 0\n\ts_nop 0\n\t"
        "s_nop 0\n\ts_nop 0\n\ts_nop 0\n\ts_nop 0\n\t"
        "s_nop 0\n\ts_nop 0\n\ts_nop 0\n\ts_nop 0\n\t"
        "s_nop 0\n\ts_nop 0\n\ts_nop 0\n\ts_nop 0\n\t"
        "s_nop 0\n\ts_nop 0\n\ts_nop 0\n\ts_nop 0\n\t"
        "s_nop 0\n\ts_nop 0\n\ts_nop 0\n\ts_nop 0\n\t"
        "s_nop 0\n\ts_nop 0\n\ts_nop 0\n\ts_nop 0\n\t"
        "s_nop 0\n\ts_nop 0\n\ts_nop 0\n\ts_nop 0\n\t"
        "s_nop 0\n\ts_nop 0\n\ts_nop 0\n\ts_nop 0\n\t"
        "s_nop 0\n\ts_nop 0\n\ts_nop 0\n\ts_nop 0\n\t"
        "s_nop 0\n\ts_nop 0\n\ts_nop 0\n\ts_nop 0\n\t"
        "s_nop 0\n\ts_nop 0\n\ts_nop 0\n\ts_nop 0\n\t"
        "s_nop 0\n\ts_nop 0\n\ts_nop 0\n\ts_nop 0\n\t"
        "s_nop 0\n\ts_nop 0\n\ts_nop 0\n\ts_nop 0\n\t"
        "s_nop 0\n\ts_nop 0\n\ts_nop 0\n\ts_nop 0\n\t"
        "s_nop 0\n\ts_nop 0\n\ts_nop 0\n\ts_nop 0\n\t"
        "s_nop 0\n\ts_nop 0\n\ts_nop 0\n\ts_nop 0\n\t"
        "s_nop 0\n\ts_nop 0\n\ts_nop 0\n\ts_nop 0\n\t"
        "s_nop 0\n\ts_nop 0\n\ts_nop 0\n\ts_nop 0\n\t"
        "s_nop 0\n\ts_nop 0\n\ts_nop 0\n\ts_nop 0\n\t"
        "s_nop 0\n\ts_nop 0\n\ts_nop 0\n\ts_nop 0\n\t"
        "s_nop 0\n\ts_nop 0\n\ts_nop 0\n\ts_nop 0\n\t"
        "s_nop 0\n\ts_nop 0\n\ts_nop 0\n\ts_nop 0\n\t"
        "s_nop 0\n\ts_nop 0\n\ts_nop 0\n\ts_nop 0\n\t"
        "s_nop 0\n\ts_nop 0\n\ts_nop 0\n\ts_nop 0\n\t"
        "s_nop 0\n\ts_nop 0\n\ts_nop 0\n\ts_nop 0\n\t"
        "s_nop 0\n\ts_nop 0\n\ts_nop 0\n\ts_nop 0\n\t"
        "s_nop 0\n\ts_nop 0\n\ts_nop 0\n\ts_nop 0\n\t"
        "s_nop 0\n\ts_nop 0\n\ts_nop 0\n\ts_nop 0\n\t"
        "s_nop 0\n\ts_nop 0\n\ts_nop 0\n\ts_nop 0\n\t"
        "s_nop 0\n\ts_nop 0\n\ts_nop 0\n\ts_nop 0\n\t"
        "s_nop 0\n\ts_nop 0\n\ts_nop 0\n\ts_nop 0\n\t"
        "s_nop 0\n\ts_nop 0\n\ts_nop 0\n\ts_nop 0\n\t"
        "s_nop 0\n\ts_nop 0\n\ts_nop 0\n\ts_nop 0\n\t"
        "s_nop 0\n\ts_nop 0\n\ts_nop 0\n\ts_nop 0\n\t"
        "s_nop 0\n\ts_nop 0\n\ts_nop 0\n\ts_nop 0\n\t"
        "s_nop 0\n\ts_nop 0\n\ts_nop 0\n\ts_nop 0\n\t"
        "s_nop 0\n\ts_nop 0\n\ts_nop 0\n\ts_nop 0\n\t"
        "s_nop 0\n\ts_nop 0\n\ts_nop 0\n\ts_nop 0\n\t"
        "s_nop 0\n\ts_nop 0\n\ts_nop 0\n\ts_nop 0\n\t"
        "s_nop 0\n\ts_nop 0\n\ts_nop 0\n\ts_nop 0\n\t"
        "s_nop 0\n\ts_nop 0\n\ts_nop 0\n\ts_nop 0\n\t"
        "s_nop 0\n\ts_nop 0\n\ts_nop 0\n\ts_nop 0\n\t"
        "s_nop 0\n\ts_nop 0\n\ts_nop 0\n\ts_nop 0\n\t"
        "s_nop 0\n\ts_nop 0\n\ts_nop 0\n\ts_nop 0\n\t"
        "s_nop 0\n\ts_nop 0\n\ts_nop 0\n\ts_nop 0\n\t"
        "s_nop 0\n\ts_nop 0\n\ts_nop 0\n\ts_nop 0\n\t"
        "s_nop 0\n\ts_nop 0\n\ts_nop 0\n\ts_nop 0\n\t"
        "s_nop 0\n\ts_nop 0\n\ts_nop 0\n\ts_nop 0\n\t"
        "s_nop 0\n\ts_nop 0\n\ts_nop 0\n\ts_nop 0\n\t"
        "s_nop 0\n\ts_nop 0\n\ts_nop 0\n\ts_nop 0\n\t"
        "s_nop 0\n\ts_nop 0\n\ts_nop 0\n\ts_nop 0\n\t"
        "s_nop 0\n\ts_nop 0\n\ts_nop 0\n\ts_nop 0\n\t"
        "s_nop 0\n\ts_nop 0\n\ts_nop 0\n\ts_nop 0\n\t"
        "s_nop 0\n\ts_nop 0\n\ts_nop 0\n\ts_nop 0\n\t"
        "Lza_%=:\n\t"
        "s_cmp_lg_u32 s51, %[fv]\n\t"
        "s_cbranch_scc1 Ln%=\n\t"
        "s_cmp_lg_u32 s55, %[fv]\n\t"
        "s_cbranch_scc1 Ln%=\n\t"
        "s_cmp_lg_u32 s59, %[fv]\n\t"
        "s_cbranch_scc1 Ln%=\n\t"
        "s_cmp_lg_u32 s63, %[fv]\n\t"
        "s_cbranch_scc1 Ln%=\n\t"
        "s_barrier\n\t"
        "s_setprio 0\n\t"
        "s_waitcnt lgkmcnt(0)\n\t"
        "s_xor_b32 s51, s51, %[fv]\n\t"
        "s_xor_b32 s55, s55, %[fv]\n\t"
        "s_xor_b32 s59, s59, %[fv]\n\t"
        "s_xor_b32 s63, s63, %[fv]\n\t"
        "s_sub_u32 s49, s49, %[tl]\n\t"
        "s_cmp_eq_u32 s51, 0\n\t"
        "s_cselect_b32 s49, s49, 0\n\t"
        "s_cselect_b32 s50, s50, 0\n\t"
        "s_sub_u32 s53, s53, %[tl]\n\t"
        "s_cmp_eq_u32 s55, 0\n\t"
        "s_cselect_b32 s53, s53, 0\n\t"
        "s_cselect_b32 s54, s54, 0\n\t"
        "s_sub_u32 s57, s57, %[tl]\n\t"
        "s_cmp_eq_u32 s59, 0\n\t"
        "s_cselect_b32 s57, s57, 0\n\t"
        "s_cselect_b32 s58, s58, 0\n\t"
        "s_sub_u32 s61, s61, %[tl]\n\t"
        "s_cmp_eq_u32 s63, 0\n\t"
        "s_cselect_b32 s61, s61, 0\n\t"
        "s_cselect_b32 s62, s62, 0\n\t"
        "s_max_u32 s49, s49, s53\n\t"
        "s_max_u32 s57, s57, s61\n\t"
        "s_max_u32 s49, s49, s57\n\t"
        "s_max_u32 s50, s50, s54\n\t"
        "s_max_u32 s58, s58, s62\n\t"
        "s_max_u32 s50, s50, s58\n\t"
        "s_mov_b32 s48, s49\n\t"
        "s_sub_u32 s51, s51, 1\n\t"
        "s_cmp_lt_u32 s51, 0x20000\n\t"
        "s_cselect_b32 s48, 0, s48\n\t"
        "s_sub_u32 s55, s55, 1\n\t"
        "s_cmp_lt_u32 s55, 0x20000\n\t"
        "s_cselect_b32 s48, 0, s48\n\t"
        "s_sub_u32 s59, s59, 1\n\t"
        "s_cmp_lt_u32 s59, 0x20000\n\t"
        "s_cselect_b32 s48, 0, s48\n\t"
        "s_sub_u32 s63, s63, 1\n\t"
        "s_cmp_lt_u32 s63, 0x20000\n\t"
        "s_cselect_b32 s48, 0, s48\n\t"
        "s_add_u32 s49, s49, %[tl]\n\t"
        "s_cmp_lg_u32 s48, 0\n\t"
        "s_cselect_b32 %[ok], 1, 2\n\t"
        "s_cselect_b32 %[t1], s49, %[t1]\n\t"
        "s_cselect_b32 %[t2], s50, %[t2]\n\t"
        "s_branch Lm%=\n\t"
        "Lw%=:\n\t"
        "s_waitcnt lgkmcnt(0)\n\t"
        "s_sub_u32 s95, %[t0], %[r0]\n\t"
        "s_mul_i32 s95, s95, %[kr]\n\t"
        "s_ashr_i32 s95, s95, 12\n\t"
        "s_add_u32 s95, s95, %[c0]\n\t"
        "s_sub_u32 s95, s95, 64\n\t"
        "Ldc_%=:\n\t"
        "s_memtime s[90:91]\n\t"
        "s_waitcnt lgkmcnt(0)\n\t"
        "s_sub_u32 s88, s95, s90\n\t"
        "s_sub_u32 s89, s88, 336\n\t"
        "s_cmp_gt_u32 s89, 0x4000\n\t"
        "s_cbranch_scc1 Lec_%=\n\t"
        "s_sleep 1\n\t"
        "s_branch Ldc_%=\n\t"
        "Lec_%=:\n\t"
        "s_sub_u32 s88, s88, 112\n\t"
        "s_max_i32 s88, s88, 0\n\t"
        "s_min_i32 s88, s88, 224\n\t"
        "s_andn2_b32 s88, s88, 3\n\t"
        "s_getpc_b64 s[90:91]\n\t"
        "Lgc_%=:\n\t"
        "s_sub_u32 s90, s90, s88\n\t"
        "s_subb_u32 s91, s91, 0\n\t"
        "s_add_u32 s90, s90, Lzc_%=-Lgc_%=\n\t"
        "s_addc_u32 s91, s91, 0\n\t"
        "s_setpc_b64 s[90:91]\n\t"
        "s_nop 0\n\ts_nop 0\n\ts_nop 0\n\ts_nop 0\n\t"
        "s_nop 0\n\ts_nop 0\n\ts_nop 0\n\ts_nop 0\n\t"
        "s_nop 0\n\ts_nop 0\n\ts_nop 0\n\ts_nop 0\n\t"
        "s_nop 0\n\ts_nop 0\n\ts_nop 0\n\ts_nop 0\n\t"
        "s_nop 0\n\ts_nop 0\n\ts_nop 0\n\ts_nop 0\n\t"
        "s_nop 0\n\ts_nop 0\n\ts_nop 0\n\ts_nop 0\n\t"
        "s_nop 0\n\ts_nop 0\n\ts_nop 0\n\ts_nop 0\n\t"
        "s_nop 0\n\ts_nop 0\n\ts_nop 0\n\ts_nop 0\n\t"
        "s_nop 0\n\ts_nop 0\n\ts_nop 0\n\ts_nop 0\n\t"
        "s_nop 0\n\ts_nop 0\n\ts_nop 0\n\ts_nop 0\n\t"
        "s_nop 0\n\ts_nop 0\n\ts_nop 0\n\ts_nop 0\n\t"
        "s_nop 0\n\ts_nop 0\n\ts_nop 0\n\ts_nop 0\n\t"
        "s_nop 0\n\ts_nop 0\n\ts_nop 0\n\ts_nop 0\n\t"
        "s_nop 0\n\ts_nop 0\n\ts_nop 0\n\ts_nop 0\n\t"
        "Lzc_%=:\n\t"
        "s_branch Lza_%=\n\t"
        "Ln%=:\n\t"
        "s_mov_b32 %[ok], 3\n\t"
        "s_waitcnt lgkmcnt(0)\n\t"
        "s_cmp_eq_u32 s51, s92\n\t"
        "s_cselect_b32 %[ok], 0, %[ok]\n\t"
        "s_cmp_eq_u32 s55, s92\n\t"
        "s_cselect_b32 %[ok], 0, %[ok]\n\t"
        "s_cmp_eq_u32 s59, s92\n\t"
        "s_cselect_b32 %[ok], 0, %[ok]\n\t"
        "s_cmp_eq_u32 s63, s92\n\t"
        "s_cselect_b32 %[ok], 0, %[ok]\n\t"
        "Lm%=:\n\t"
        "s_waitcnt lgkmcnt(0)\n\t"
        : [c0] "+&s"(c0), [r0] "+&s"(r0), [t1] "+&s"(t1), [t2] "+&s"(t2), [ok] "=&s"(ok)
        : [rs] "s"(rs), [so] "s"(so), [kr] "s"(kr), [fv] "s"(fv), [lb] "s"(lb), [tl] "s"(tl), [t0] "s"(t0), [fp] "s"(fp)
        : "s48", "s49", "s50", "s51", "s52", "s53", "s54", "s55", "s56", "s57", "s58", "s59", "s60", "s61", "s62", "s63", "s64", "s65", "s66", "s67", "s68", "s69", "s70", "s71", "s72", "s73", "s74", "s75", "s76", "s77", "s78", "s79", "s80", "s81", "s82", "s83", "s84", "s85", "s86", "s87", "s88", "s89", "s90", "s91", "s92", "s93", "s94", "s95", "v20", "v21", "vcc", "scc", "memory");
}

__device__ __forceinline__ void r_t1(u32& c0, u32& r0, u32& t1, u32& t2, u32 tv, u32 kr, u32 fv, u32 lb, u32x4 rs, u32 so STP) {
    asm volatile(
        "s_setprio 3\n\t"
        "s_mov_b32 s92, 0x4000\n\t"
        "v_mov_b32 v20, %[lb]\n\t"
        "s_nop 2\n\t"
        "Lr%=:\n\t"
        "buffer_load_dwordx4 v[16:19], %[tv], %[rs], %[so] offen sc1\n\t"
        "s_memrealtime s[64:65]\n\t"
        "s_nop 0\n\t"
        "s_memrealtime s[66:67]\n\t"
        "s_nop 0\n\t"
        "s_memrealtime s[68:69]\n\t"
        "s_nop 0\n\t"
        "s_memrealtime s[70:71]\n\t"
        "s_nop 0\n\t"
        "s_memrealtime s[72:73]\n\t"
        "s_nop 0\n\t"
        "s_memrealtime s[74:75]\n\t"
        "s_memtime s[90:91]\n\t"
        "s_memrealtime s[76:77]\n\t"
        "s_nop 0\n\t"
        "s_memrealtime s[78:79]\n\t"
        "s_nop 0\n\t"
        "s_memrealtime s[80:81]\n\t"
        "s_nop 0\n\t"
        "s_memrealtime s[82:83]\n\t"
        "s_nop 0\n\t"
        "s_memrealtime s[84:85]\n\t"
        "s_nop 0\n\t"
        "s_memrealtime s[86:87]\n\t"
        "s_nop 0\n\t"
        "s_nop 7\n\t"
        "s_waitcnt lgkmcnt(0)\n\t"
        "s_add_u32 s64, s64, s66\n\t"
        "s_add_u32 s64, s64, s68\n\t"
        "s_add_u32 s64, s64, s70\n\t"
        "s_add_u32 s64, s64, s72\n\t"
        "s_add_u32 s64, s64, s74\n\t"
        "s_add_u32 s64, s64, s76\n\t"
        "s_add_u32 s64, s64, s78\n\t"
        "s_add_u32 s64, s64, s80\n\t"
        "s_add_u32 s64, s64, s82\n\t"
        "s_add_u32 s64, s64, s84\n\t"
        "s_add_u32 s64, s64, s86\n\t"
        "s_lshl_b32 s64, s64, 1\n\t"
        "s_mov_b32 %[c0], s90\n\t"
        "s_mov_b32 %[r0], s64\n\t"
        "s_sub_u32 s96, %[r0], 0x100000\n\t"
        "s_waitcnt vmcnt(0)\n\t"
        "v_cmp_eq_u32 vcc, %[fv], v19\n\t"
        "v_subrev_u32 v16, s96, v16\n\t"
        "v_subrev_u32 v17, s96, v17\n\t"
        "v_cndmask_b32 v16, 0, v16, vcc\n\t"
        "v_cndmask_b32 v17, 0, v17, vcc\n\t"
        "v_cndmask_b32 v18, 0, v18, vcc\n\t"
        "v_max_u32_dpp v16, v16, v16 quad_perm:[1,0,3,2] row_mask:0xf bank_mask:0xf\n\t"
        "v_max_u32_dpp v17, v17, v17 quad_perm:[1,0,3,2] row_mask:0xf bank_mask:0xf\n\t"
        "v_max_u32_dpp v18, v18, v18 quad_perm:[1,0,3,2] row_mask:0xf bank_mask:0xf\n\t"
        "v_max_u32_dpp v16, v16, v16 quad_perm:[2,3,0,1] row_mask:0xf bank_mask:0xf\n\t"
        "v_max_u32_dpp v17, v17, v17 quad_perm:[2,3,0,1] row_mask:0xf bank_mask:0xf\n\t"
        "v_max_u32_dpp v18, v18, v18 quad_perm:[2,3,0,1] row_mask:0xf bank_mask:0xf\n\t"
        "v_max_u32_dpp v16, v16, v16 row_half_mirror row_mask:0xf bank_mask:0xf\n\t"
        "v_max_u32_dpp v17, v17, v17 row_half_mirror row_mask:0xf bank_mask:0xf\n\t"
        "v_max_u32_dpp v18, v18, v18 row_half_mirror row_mask:0xf bank_mask:0xf\n\t"
        "s_lshr_b32 s94, vcc_lo, 4\n\t"
        "v_readfirstlane_b32 s93, v16\n\t"
        "v_readfirstlane_b32 %[t1], v17\n\t"
        "v_readfirstlane_b32 %[t2], v18\n\t"
        "s_add_u32 s93, s93, s96\n\t"
        "s_add_u32 %[t1], %[t1], s96\n\t"
        "s_or_b32 s94, s94, vcc_lo\n\t"
        "s_and_b32 s94, s94, 15\n\t"
        "s_cmp_eq_u32 s94, 15\n\t"
        "s_cbranch_scc0 Lx%=\n\t"
        "Lgo%=:\n\t"
        "s_bfe_u32 s95, %[t2], 0x8000c\n\t"
        "s_mul_i32 s95, s95, %[kr]\n\t"
        "s_lshr_b32 s95, s95, 12\n\t"
        "s_min_u32 s95, s95, 512\n\t"
        "s_andn2_b32 s95, s95, 3\n\t"
        "v_mov_b32 v21, s95\n\t"
        "ds_write_b32 v20, v21 offset:16384\n\t"
        "s_sub_u32 s95, s93, %[r0]\n\t"
        "s_mul_i32 s95, s95, %[kr]\n\t"
        "s_ashr_i32 s95, s95, 12\n\t"
        "s_add_u32 s95, s95, %[c0]\n\t"
        "Lda_%=:\n\t"
        "s_memtime s[90:91]\n\t"
        "s_waitcnt lgkmcnt(0)\n\t"
        "s_sub_u32 s88, s95, s90\n\t"
        "s_sub_u32 s89, s88, 336\n\t"
        "s_cmp_gt_u32 s89, 0x4000\n\t"
        "s_cbranch_scc1 Lea_%=\n\t"
        "s_sleep 1\n\t"
        "s_branch Lda_%=\n\t"
        "Lea_%=:\n\t"
        "s_sub_u32 s88, s88, 112\n\t"
        "s_max_i32 s88, s88, 0\n\t"
        "s_min_i32 s88, s88, 224\n\t"
        "s_andn2_b32 s88, s88, 3\n\t"
        "s_getpc_b64 s[90:91]\n\t"
        "Lga_%=:\n\t"
        "s_sub_u32 s90, s90, s88\n\t"
        "s_subb_u32 s91, s91, 0\n\t"
        "s_add_u32 s90, s90, Lza_%=-Lga_%=\n\t"
        "s_addc_u32 s91, s91, 0\n\t"
        "s_setpc_b64 s[90:91]\n\t"
        "s_nop 0\n\ts_nop 0\n\ts_nop 0\n\ts_nop 0\n\t"
        "s_nop 0\n\ts_nop 0\n\ts_nop 0\n\ts_nop 0\n\t"
        "s_nop 0\n\ts_nop 0\n\ts_nop 0\n\ts_nop 0\n\t"
        "s_nop 0\n\ts_nop 0\n\ts_nop 0\n\ts_nop 0\n\t"
        "s_nop 0\n\ts_nop 0\n\ts_nop 0\n\ts_nop 0\n\t"
        "s_nop 0\n\ts_nop 0\n\ts_nop 0\n\ts_nop 0\n\t"
        "s_nop 0\n\ts_nop 0\n\ts_nop 0\n\ts_nop 0\n\t"
        "s_nop 0\n\ts_nop 0\n\ts_nop 0\n\ts_nop 0\n\t"
        "s_nop 0\n\ts_nop 0\n\ts_nop 0\n\ts_nop 0\n\t"
        "s_nop 0\n\ts_nop 0\n\ts_nop 0\n\ts_nop 0\n\t"
        "s_nop 0\n\ts_nop 0\n\ts_nop 0\n\ts_nop 0\n\t"
        "s_nop 0\n\ts_nop 0\n\ts_nop 0\n\ts_nop 0\n\t"
        "s_nop 0\n\ts_nop 0\n\ts_nop 0\n\ts_nop 0\n\t"
        "s_nop 0\n\ts_nop 0\n\ts_nop 0\n\ts_nop 0\n\t"
        "Lza_%=:\n\t"
        "s_barrier\n\t"
        "s_setprio 0\n\t"
        "s_branch Lend%=\n\t"
        "Lx%=:\n\t"
        "s_sub_u32 s92, s92, 1\n\t"
        "s_cbranch_scc1 Lgo%=\n\t"
        "s_branch Lr%=\n\t"
        "Lend%=:\n\t"
        : [c0] "+&s"(c0), [r0] "+&s"(r0), [t1] "+&s"(t1), [t2] "+&s"(t2)
        : [tv] "v"(tv), [rs] "s"(rs), [so] "s"(so), [kr] "s"(kr), [fv] "s"(fv), [lb] "s"(lb)
        : "s64", "s65", "s66", "s67", "s68", "s69", "s70", "s71", "s72", "s73", "s74", "s75", "s76", "s77", "s78", "s79", "s80", "s81", "s82", "s83", "s84", "s85", "s86", "s87", "s88", "s89", "s90", "s91", "s92", "s93", "s94", "s95", "s96", "v16", "v17", "v18", "v19", "v20", "v21", "vcc", "scc", "memory");
}

__device__ __forceinline__ void r_u(u32x32& bn, u32& s, u32& have, u32& left, u32& why, u32 sidx, u32 eo, u32 uw, u32 fa, u32 vm, u32 tp, u32 te, u32 tb, u32x4 rs, u32 lag, u32 lb STP LGP) {
    asm volatile(
        "v_mov_b32 v125, %[lb]\n\t"
        "v_mov_b32 v80, 0xffbfffbf\n\t"
        "v_mov_b32 v81, v80\n\t"
        "ds_write_b64 %[uw], v[80:81] offset:0\n\t"
        "ds_write_b64 %[uw], v[80:81] offset:512\n\t"
        "ds_write_b64 %[uw], v[80:81] offset:1024\n\t"
        "ds_write_b64 %[uw], v[80:81] offset:1536\n\t"
        "Lp%=:\n\t"
        "v_readlane_b32 s82, %[te], %[s]\n\t"
        "v_readlane_b32 s83, %[tb], %[s]\n\t"
        "s_add_u32 s92, %[s], 1\n\t"
        "s_mul_i32 s93, s92, 0x10001\n\t"
        "s_mov_b32 s95, %[left]\n\t"
        "s_mov_b32 s94, 0\n\t"
        "v_mov_b32 v124, s93\n\t"
        "v_readlane_b32 s85, %[tp], %[s]\n\t"
        "s_add_u32 s84, s83, 0x1000\n\t"
        "s_setprio 2\n\t"
        "s_waitcnt lgkmcnt(0)\n\t"
        "s_getpc_b64 s[90:91]\n\t"
        "Lj%=:\n\t"
        "s_sub_u32 s90, s90, %[lag]\n\t"
        "s_subb_u32 s91, s91, 0\n\t"
        "s_add_u32 s90, s90, Lg%=-Lj%=\n\t"
        "s_addc_u32 s91, s91, 0\n\t"
        "s_barrier\n\t"
        "s_setpc_b64 s[90:91]\n\t"
        "s_nop 10\n\t"
        "s_nop 10\n\t"
        "s_nop 10\n\t"
        "Lg%=:\n\t"
        "buffer_load_dwordx4 v[48:51], %[eo], %[rs], s82 offen offset:0 sc1\n\t"
        "buffer_load_dwordx4 v[52:55], %[eo], %[rs], s82 offen offset:1024 sc1\n\t"
        "buffer_load_dwordx4 v[56:59], %[eo], %[rs], s82 offen offset:2048 sc1\n\t"
        "buffer_load_dwordx4 v[60:63], %[eo], %[rs], s82 offen offset:3072 sc1\n\t"
        "v_mov_b64 v[64:65], 0\n\t"
        "v_mov_b64 v[66:67], 0\n\t"
        "v_mov_b64 v[68:69], 0\n\t"
        "v_mov_b64 v[70:71], 0\n\t"
        "v_mov_b64 v[72:73], 0\n\t"
        "v_mov_b64 v[74:75], 0\n\t"
        "v_mov_b64 v[76:77], 0\n\t"
        "v_mov_b64 v[78:79], 0\n\t"
        "s_waitcnt vmcnt(3)\n\t"
        "v_smfmac_f32_16x16x64_bf16 v[64:67], v[48:51], v[16:23], %[sidx]\n\t"
        "s_waitcnt vmcnt(2)\n\t"
        "v_smfmac_f32_16x16x64_bf16 v[68:71], v[52:55], v[24:31], %[sidx]\n\t"
        "s_waitcnt vmcnt(1)\n\t"
        "v_smfmac_f32_16x16x64_bf16 v[72:75], v[56:59], v[32:39], %[sidx]\n\t"
        "v_cvt_pk_bf16_f32 v80, v64, v65\n\t"
        "v_cvt_pk_bf16_f32 v81, v66, v67\n\t"
        "ds_write_b64 %[uw], v[80:81] offset:0\n\t"
        "s_waitcnt vmcnt(0)\n\t"
        "v_smfmac_f32_16x16x64_bf16 v[76:79], v[60:63], v[40:47], %[sidx]\n\t"
        "v_cvt_pk_bf16_f32 v82, v68, v69\n\t"
        "v_cvt_pk_bf16_f32 v83, v70, v71\n\t"
        "ds_write_b64 %[uw], v[82:83] offset:512\n\t"
        "v_cvt_pk_bf16_f32 v84, v72, v73\n\t"
        "v_cvt_pk_bf16_f32 v85, v74, v75\n\t"
        "ds_write_b64 %[uw], v[84:85] offset:1024\n\t"
        "s_nop 1\n\t"
        "v_cvt_pk_bf16_f32 v86, v76, v77\n\t"
        "v_cvt_pk_bf16_f32 v87, v78, v79\n\t"
        "ds_write_b64 %[uw], v[86:87] offset:1536\n\t"
        "s_setprio 0\n\t"
        "s_cmp_lg_u32 s94, 0\n\t"
        "s_cbranch_scc1 Lc%=\n\t"
        "s_waitcnt lgkmcnt(0)\n\t"
        "s_barrier\n\t"
        "ds_read_b32 v88, v125 offset:16392\n\t"
        "s_waitcnt lgkmcnt(0)\n\t"
        "v_cmp_eq_u32 vcc, s93, v88\n\t"
        "s_cbranch_vccnz Lc%=\n\t"
        "Lok%=:\n\t"
        "s_cmp_gt_u32 %[have], s92\n\t"
        "s_mov_b32 %[why], 0\n\t"
        "buffer_load_dword v122, off, %[rs], s85 sc1\n\t"
        "s_cbranch_scc0 Lh%=\n\t"
        "Li%=:\n\t"
        "buffer_load_dwordx4 v[90:93], %[vm], %[rs], s83 offen offset:0 sc1\n\t"
        "buffer_load_dwordx4 v[94:97], %[vm], %[rs], s83 offen offset:1024 sc1\n\t"
        "buffer_load_dwordx4 v[98:101], %[vm], %[rs], s83 offen offset:2048 sc1\n\t"
        "buffer_load_dwordx4 v[102:105], %[vm], %[rs], s83 offen offset:3072 sc1\n\t"
        "buffer_load_dwordx4 v[106:109], %[vm], %[rs], s84 offen offset:0 sc1\n\t"
        "buffer_load_dwordx4 v[110:113], %[vm], %[rs], s84 offen offset:1024 sc1\n\t"
        "buffer_load_dwordx4 v[114:117], %[vm], %[rs], s84 offen offset:2048 sc1\n\t"
        "buffer_load_dwordx4 v[118:121], %[vm], %[rs], s84 offen offset:3072 sc1\n\t"
        "Ln%=:\n\t"
        "ds_read_b32 v88, v125 offset:16396\n\t"
        "v_mov_b32 v80, 0xffbfffbf\n\t"
        "v_mov_b32 v81, v80\n\t"
        "s_sub_u32 s95, s95, %[left]\n\t"
        "s_mov_b32 %[s], s92\n\t"
        "s_waitcnt lgkmcnt(0)\n\t"
        "v_cmp_eq_u32 vcc, s93, v88\n\t"
        "s_cmp_eq_u32 %[s], 63\n\t"
        "s_cbranch_vccnz Lr%=\n\t"
        "s_cbranch_scc1 Lz%=\n\t"
        "s_cmp_lg_u32 %[why], 0\n\t"
        "s_cbranch_scc1 Lend%=\n\t"
        "ds_write_b64 %[uw], v[80:81] offset:0\n\t"
        "ds_write_b64 %[uw], v[80:81] offset:512\n\t"
        "ds_write_b64 %[uw], v[80:81] offset:1024\n\t"
        "ds_write_b64 %[uw], v[80:81] offset:1536\n\t"
        "s_waitcnt vmcnt(6)\n\t"
        "v_mov_b32 v16, v90\n\t"
        "v_mov_b32 v17, v94\n\t"
        "v_mov_b32 v18, v91\n\t"
        "v_mov_b32 v19, v95\n\t"
        "v_mov_b32 v20, v92\n\t"
        "v_mov_b32 v21, v96\n\t"
        "v_mov_b32 v22, v93\n\t"
        "v_mov_b32 v23, v97\n\t"
        "v_readfirstlane_b32 s96, v122\n\t"
        "s_waitcnt vmcnt(4)\n\t"
        "v_mov_b32 v24, v98\n\t"
        "v_mov_b32 v25, v102\n\t"
        "v_mov_b32 v26, v99\n\t"
        "v_mov_b32 v27, v103\n\t"
        "v_mov_b32 v28, v100\n\t"
        "v_mov_b32 v29, v104\n\t"
        "v_mov_b32 v30, v101\n\t"
        "v_mov_b32 v31, v105\n\t"
        "s_waitcnt vmcnt(2)\n\t"
        "v_mov_b32 v32, v106\n\t"
        "v_mov_b32 v33, v110\n\t"
        "v_mov_b32 v34, v107\n\t"
        "v_mov_b32 v35, v111\n\t"
        "v_mov_b32 v36, v108\n\t"
        "v_mov_b32 v37, v112\n\t"
        "v_mov_b32 v38, v109\n\t"
        "v_mov_b32 v39, v113\n\t"
        "s_max_i32 %[have], %[have], s96\n\t"
        "s_waitcnt vmcnt(0)\n\t"
        "v_mov_b32 v40, v114\n\t"
        "v_mov_b32 v41, v118\n\t"
        "v_mov_b32 v42, v115\n\t"
        "v_mov_b32 v43, v119\n\t"
        "v_mov_b32 v44, v116\n\t"
        "v_mov_b32 v45, v120\n\t"
        "v_mov_b32 v46, v117\n\t"
        "v_mov_b32 v47, v121\n\t"
        "s_branch Lp%=\n\t"
        "Lc%=:\n\t"
        "v_max_u32 v88, v48, v49\n\t"
        "v_max3_u32 v88, v88, v50, v51\n\t"
        "v_max3_u32 v88, v88, v52, v53\n\t"
        "v_max3_u32 v88, v88, v54, v55\n\t"
        "v_max3_u32 v88, v88, v56, v57\n\t"
        "v_max3_u32 v88, v88, v58, v59\n\t"
        "v_max3_u32 v88, v88, v60, v61\n\t"
        "v_max3_u32 v88, v88, v62, v63\n\t"
        "v_cmp_eq_u32 vcc, 0xffbfffbf, v88\n\t"
        "s_mov_b32 s94, 1\n\t"
        "s_cbranch_vccz Ld%=\n\t"
        "s_sub_u32 %[left], %[left], 1\n\t"
        "s_cbranch_scc0 Lg%=\n\t"
        "s_mov_b32 %[left], 0\n\t"
        "Ld%=:\n\t"
        "ds_write_b32 %[fa], v124\n\t"
        "s_waitcnt lgkmcnt(0)\n\t"
        "s_barrier\n\t"
        "s_branch Lok%=\n\t"
        "Lh%=:\n\t"
        "s_waitcnt vmcnt(0)\n\t"
        "v_readfirstlane_b32 s96, v122\n\t"
        "s_cmp_eq_u32 s92, 0\n\t"
        "s_cselect_b32 s96, 0, s96\n\t"
        "s_max_i32 %[have], %[have], s96\n\t"
        "s_cmp_gt_u32 %[have], s92\n\t"
        "s_cbranch_scc1 Li%=\n\t"
        "s_mov_b32 %[why], 2\n\t"
        "s_branch Ln%=\n\t"
        "Lr%=:\n\t"
        "s_mov_b32 %[why], 3\n\t"
        "s_branch Lend%=\n\t"
        "Lz%=:\n\t"
        "s_mov_b32 %[why], 0\n\t"
        "Lend%=:\n\t"
        "s_waitcnt vmcnt(0)\n\t"
        : [bn] "+{v[16:47]}"(bn), [s] "+&s"(s), [have] "+&s"(have), [left] "+&s"(left), [why] "=&s"(why)
        : [sidx] "v"(sidx), [eo] "v"(eo), [uw] "v"(uw), [fa] "v"(fa), [vm] "v"(vm), [tp] "v"(tp), [te] "v"(te), [tb] "v"(tb), [rs] "s"(rs), [lag] "s"(lag), [lb] "s"(lb)
        : "s82", "s83", "s84", "s85", "s86", "s87", "s88", "s89", "s90", "s91", "s92", "s93", "s94", "s95", "s96", "s97", "s98", "s99", "v48", "v49", "v50", "v51", "v52", "v53", "v54", "v55", "v56", "v57", "v58", "v59", "v60", "v61", "v62", "v63", "v64", "v65", "v66", "v67", "v68", "v69", "v70", "v71", "v72", "v73", "v74", "v75", "v76", "v77", "v78", "v79", "v80", "v81", "v82", "v83", "v84", "v85", "v86", "v87", "v88", "v89", "v90", "v91", "v92", "v93", "v94", "v95", "v96", "v97", "v98", "v99", "v100", "v101", "v102", "v103", "v104", "v105", "v106", "v107", "v108", "v109", "v110", "v111", "v112", "v113", "v114", "v115", "v116", "v117", "v118", "v119", "v120", "v121", "v122", "v123", "v124", "v125", "vcc", "scc", "memory");
}

__device__ __forceinline__ void r_f(u32& bad, u32& pass, u32 sidx, u32 pv, u32 zv, u32 ra, u32 rb, u32 mo, u32 uf, u32x4 rs, u32 so, u32 sz, u32 sm, u32 lb, u32 fv, u32 cap, u32 zp, u32 px, u32 zx STP) {
    asm volatile(
        "v_mov_b32 v120, %[lb]\n\t"
        "s_add_u32 s92, %[sm], 0x1000\n\t"
        "s_add_u32 s93, %[sm], 0x4000\n\t"
        "s_add_u32 s94, %[sm], 0x5000\n\t"
        "s_xor_b32 s80, %[px], 0x0\n\t"
        "s_add_u32 s80, s80, %[so]\n\t"
        "s_xor_b32 s84, %[zx], 0x0\n\t"
        "s_add_u32 s84, s84, %[sz]\n\t"
        "s_xor_b32 s81, %[px], 0x400\n\t"
        "s_add_u32 s81, s81, %[so]\n\t"
        "s_xor_b32 s85, %[zx], 0x800\n\t"
        "s_add_u32 s85, s85, %[sz]\n\t"
        "s_xor_b32 s82, %[px], 0x8000\n\t"
        "s_add_u32 s82, s82, %[so]\n\t"
        "s_xor_b32 s86, %[zx], 0x1000\n\t"
        "s_add_u32 s86, s86, %[sz]\n\t"
        "s_xor_b32 s83, %[px], 0x8400\n\t"
        "s_add_u32 s83, s83, %[so]\n\t"
        "s_xor_b32 s87, %[zx], 0x1800\n\t"
        "s_add_u32 s87, s87, %[sz]\n\t"
        "s_mov_b32 %[pass], 0\n\t"
        "s_mov_b32 s97, 0\n\t"
        "s_mov_b32 %[bad], 0\n\t"
        "s_setprio 3\n\t"
        "s_barrier\n\t"
        "ds_read_b32 v116, v120 offset:16384\n\t"
        "s_waitcnt lgkmcnt(0)\n\t"
        "v_readfirstlane_b32 s98, v116\n\t"
        "s_nop 3\n\t"
        "s_getpc_b64 s[90:91]\n\t"
        "Lgp_%=:\n\t"
        "s_sub_u32 s90, s90, s98\n\t"
        "s_subb_u32 s91, s91, 0\n\t"
        "s_add_u32 s90, s90, Lzp_%=-Lgp_%=\n\t"
        "s_addc_u32 s91, s91, 0\n\t"
        "s_setpc_b64 s[90:91]\n\t"
        "s_nop 0\n\ts_nop 0\n\ts_nop 0\n\ts_nop 0\n\t"
        "s_nop 0\n\ts_nop 0\n\ts_nop 0\n\ts_nop 0\n\t"
        "s_nop 0\n\ts_nop 0\n\ts_nop 0\n\ts_nop 0\n\t"
        "s_nop 0\n\ts_nop 0\n\ts_nop 0\n\ts_nop 0\n\t"
        "s_nop 0\n\ts_nop 0\n\ts_nop 0\n\ts_nop 0\n\t"
        "s_nop 0\n\ts_nop 0\n\ts_nop 0\n\ts_nop 0\n\t"
        "s_nop 0\n\ts_nop 0\n\ts_nop 0\n\ts_nop 0\n\t"
        "s_nop 0\n\ts_nop 0\n\ts_nop 0\n\ts_nop 0\n\t"
        "s_nop 0\n\ts_nop 0\n\ts_nop 0\n\ts_nop 0\n\t"
        "s_nop 0\n\ts_nop 0\n\ts_nop 0\n\ts_nop 0\n\t"
        "s_nop 0\n\ts_nop 0\n\ts_nop 0\n\ts_nop 0\n\t"
        "s_nop 0\n\ts_nop 0\n\ts_nop 0\n\ts_nop 0\n\t"
        "s_nop 0\n\ts_nop 0\n\ts_nop 0\n\ts_nop 0\n\t"
        "s_nop 0\n\ts_nop 0\n\ts_nop 0\n\ts_nop 0\n\t"
        "s_nop 0\n\ts_nop 0\n\ts_nop 0\n\ts_nop 0\n\t"
        "s_nop 0\n\ts_nop 0\n\ts_nop 0\n\ts_nop 0\n\t"
        "s_nop 0\n\ts_nop 0\n\ts_nop 0\n\ts_nop 0\n\t"
        "s_nop 0\n\ts_nop 0\n\ts_nop 0\n\ts_nop 0\n\t"
        "s_nop 0\n\ts_nop 0\n\ts_nop 0\n\ts_nop 0\n\t"
        "s_nop 0\n\ts_nop 0\n\ts_nop 0\n\ts_nop 0\n\t"
        "s_nop 0\n\ts_nop 0\n\ts_nop 0\n\ts_nop 0\n\t"
        "s_nop 0\n\ts_nop 0\n\ts_nop 0\n\ts_nop 0\n\t"
        "s_nop 0\n\ts_nop 0\n\ts_nop 0\n\ts_nop 0\n\t"
        "s_nop 0\n\ts_nop 0\n\ts_nop 0\n\ts_nop 0\n\t"
        "s_nop 0\n\ts_nop 0\n\ts_nop 0\n\ts_nop 0\n\t"
        "s_nop 0\n\ts_nop 0\n\ts_nop 0\n\ts_nop 0\n\t"
        "s_nop 0\n\ts_nop 0\n\ts_nop 0\n\ts_nop 0\n\t"
        "s_nop 0\n\ts_nop 0\n\ts_nop 0\n\ts_nop 0\n\t"
        "s_nop 0\n\ts_nop 0\n\ts_nop 0\n\ts_nop 0\n\t"
        "s_nop 0\n\ts_nop 0\n\ts_nop 0\n\ts_nop 0\n\t"
        "s_nop 0\n\ts_nop 0\n\ts_nop 0\n\ts_nop 0\n\t"
        "s_nop 0\n\ts_nop 0\n\ts_nop 0\n\ts_nop 0\n\t"
        "Lzp_%=:\n\t"
        "buffer_load_dwordx4 v[48:51], %[zv], %[rs], s84 offen offset:0 sc1\n\t"
        "buffer_load_dwordx4 v[52:55], %[zv], %[rs], s84 offen offset:1024 sc1\n\t"
        "buffer_load_dwordx4 v[56:59], %[zv], %[rs], s85 offen offset:0 sc1\n\t"
        "s_getpc_b64 s[90:91]\n\t"
        "Lgq_%=:\n\t"
        "s_sub_u32 s90, s90, %[zp]\n\t"
        "s_subb_u32 s91, s91, 0\n\t"
        "s_add_u32 s90, s90, Lzq_%=-Lgq_%=\n\t"
        "s_addc_u32 s91, s91, 0\n\t"
        "s_setpc_b64 s[90:91]\n\t"
        "s_nop 0\n\ts_nop 0\n\ts_nop 0\n\ts_nop 0\n\t"
        "s_nop 0\n\ts_nop 0\n\ts_nop 0\n\ts_nop 0\n\t"
        "s_nop 0\n\ts_nop 0\n\ts_nop 0\n\ts_nop 0\n\t"
        "s_nop 0\n\ts_nop 0\n\ts_nop 0\n\ts_nop 0\n\t"
        "s_nop 0\n\ts_nop 0\n\ts_nop 0\n\ts_nop 0\n\t"
        "s_nop 0\n\ts_nop 0\n\ts_nop 0\n\ts_nop 0\n\t"
        "s_nop 0\n\ts_nop 0\n\ts_nop 0\n\ts_nop 0\n\t"
        "s_nop 0\n\ts_nop 0\n\ts_nop 0\n\ts_nop 0\n\t"
        "s_nop 0\n\ts_nop 0\n\ts_nop 0\n\ts_nop 0\n\t"
        "s_nop 0\n\ts_nop 0\n\ts_nop 0\n\ts_nop 0\n\t"
        "s_nop 0\n\ts_nop 0\n\ts_nop 0\n\ts_nop 0\n\t"
        "s_nop 0\n\ts_nop 0\n\ts_nop 0\n\ts_nop 0\n\t"
        "s_nop 0\n\ts_nop 0\n\ts_nop 0\n\ts_nop 0\n\t"
        "s_nop 0\n\ts_nop 0\n\ts_nop 0\n\ts_nop 0\n\t"
        "s_nop 0\n\ts_nop 0\n\ts_nop 0\n\ts_nop 0\n\t"
        "s_nop 0\n\ts_nop 0\n\ts_nop 0\n\ts_nop 0\n\t"
        "Lzq_%=:\n\t"
        "buffer_load_dwordx4 v[14:17], %[pv], %[rs], s80 offen offset:0 sc1\n\t"
        "buffer_load_dwordx4 v[60:63], %[zv], %[rs], s85 offen offset:1024 sc1\n\t"
        "buffer_load_dwordx4 v[18:21], %[pv], %[rs], s80 offen offset:256 sc1\n\t"
        "buffer_load_dwordx4 v[64:67], %[zv], %[rs], s86 offen offset:0 sc1\n\t"
        "buffer_load_dwordx4 v[22:25], %[pv], %[rs], s81 offen offset:0 sc1\n\t"
        "buffer_load_dwordx4 v[68:71], %[zv], %[rs], s86 offen offset:1024 sc1\n\t"
        "buffer_load_dwordx4 v[26:29], %[pv], %[rs], s81 offen offset:256 sc1\n\t"
        "buffer_load_dwordx4 v[72:75], %[zv], %[rs], s87 offen offset:0 sc1\n\t"
        "buffer_load_dwordx4 v[30:33], %[pv], %[rs], s82 offen offset:0 sc1\n\t"
        "buffer_load_dwordx4 v[76:79], %[zv], %[rs], s87 offen offset:1024 sc1\n\t"
        "buffer_load_dwordx4 v[34:37], %[pv], %[rs], s82 offen offset:256 sc1\n\t"
        "buffer_load_dwordx4 v[38:41], %[pv], %[rs], s83 offen offset:0 sc1\n\t"
        "buffer_load_dwordx4 v[42:45], %[pv], %[rs], s83 offen offset:256 sc1\n\t"
        "v_mov_b64 v[112:113], 0\n\t"
        "v_mov_b64 v[114:115], 0\n\t"
        "s_cmp_lg_u32 s97, 0\n\t"
        "s_cbranch_scc1 Lwu%=\n\t"
        "Lrd%=:\n\t"
        "s_waitcnt vmcnt(13)\n\t"
        "ds_read_b128 v[80:83], %[ra] offset:0\n\t"
        "ds_read_b128 v[84:87], %[ra] offset:1024\n\t"
        "s_waitcnt vmcnt(12)\n\t"
        "v_mov_b64 v[46:47], v[14:15]\n\t"
        "s_waitcnt vmcnt(10)\n\t"
        "v_smfmac_f32_16x16x64_bf16 v[112:115], v[16:19], v[48:55], %[sidx]\n\t"
        "s_waitcnt vmcnt(9)\n\t"
        "ds_read_b128 v[88:91], %[ra] offset:2048\n\t"
        "ds_read_b128 v[92:95], %[ra] offset:3072\n\t"
        "s_waitcnt vmcnt(8) lgkmcnt(2)\n\t"
        "v_smfmac_f32_16x16x64_bf16 v[112:115], v[20:23], v[80:87], %[sidx]\n\t"
        "s_waitcnt vmcnt(6)\n\t"
        "v_smfmac_f32_16x16x64_bf16 v[112:115], v[24:27], v[56:63], %[sidx]\n\t"
        "s_waitcnt vmcnt(5)\n\t"
        "ds_read_b128 v[96:99], %[ra] offset:4096\n\t"
        "ds_read_b128 v[100:103], %[ra] offset:5120\n\t"
        "ds_read_b128 v[104:107], %[rb] offset:6144\n\t"
        "ds_read_b128 v[108:111], %[rb] offset:7168\n\t"
        "s_waitcnt vmcnt(4) lgkmcnt(4)\n\t"
        "v_smfmac_f32_16x16x64_bf16 v[112:115], v[28:31], v[88:95], %[sidx]\n\t"
        "s_waitcnt vmcnt(2)\n\t"
        "v_smfmac_f32_16x16x64_bf16 v[112:115], v[32:35], v[64:71], %[sidx]\n\t"
        "s_waitcnt vmcnt(1) lgkmcnt(2)\n\t"
        "v_smfmac_f32_16x16x64_bf16 v[112:115], v[36:39], v[96:103], %[sidx]\n\t"
        "s_waitcnt vmcnt(0)\n\t"
        "v_smfmac_f32_16x16x64_bf16 v[112:115], v[40:43], v[72:79], %[sidx]\n\t"
        "s_waitcnt vmcnt(0) lgkmcnt(0)\n\t"
        "v_smfmac_f32_16x16x64_bf16 v[112:115], v[44:47], v[104:111], %[sidx]\n\t"
        "s_nop 7\n\t"
        "v_add_f32 v116, v112, v114\n\t"
        "v_add_f32 v117, v113, v115\n\t"
        "v_add_f32 v118, v113, v115\n\t"
        "v_mov_b32_dpp v118, v116 row_ror:8 row_mask:0xf bank_mask:0x3\n\t"
        "v_mov_b32_dpp v116, v117 row_ror:8 row_mask:0xf bank_mask:0xc\n\t"
        "v_cvt_pk_bf16_f32 v119, v116, v118\n\t"
        "v_cmpx_o_f32 vcc, v116, v118\n\t"
        "buffer_store_dword v119, %[mo], %[rs], %[sm] offen sc1\n\t"
        "buffer_store_dword v119, %[mo], %[rs], s92 offen sc1\n\t"
        "buffer_store_dword v119, %[mo], %[rs], s93 offen sc1\n\t"
        "buffer_store_dword v119, %[mo], %[rs], s94 offen sc1\n\t"
        "s_nop 2\n\t"
        "s_cmp_eq_u64 exec, -1\n\t"
        "s_mov_b64 exec, -1\n\t"
        "s_cbranch_scc0 Lag%=\n\t"
        "s_barrier\n\t"
        "s_branch Lend%=\n\t"
        "Lag%=:\n\t"
        "s_add_u32 %[pass], %[pass], 1\n\t"
        "s_cmp_eq_u32 s97, 0\n\t"
        "s_cbranch_scc1 Lfi%=\n\t"
        "v_max_u32 v116, v16, v17\n\t"
        "v_max3_u32 v116, v116, v18, v19\n\t"
        "v_max3_u32 v116, v116, v20, v21\n\t"
        "v_max3_u32 v116, v116, v22, v23\n\t"
        "v_max3_u32 v116, v116, v24, v25\n\t"
        "v_max3_u32 v116, v116, v26, v27\n\t"
        "v_max3_u32 v116, v116, v28, v29\n\t"
        "v_max3_u32 v116, v116, v30, v31\n\t"
        "v_max3_u32 v116, v116, v32, v33\n\t"
        "v_max3_u32 v116, v116, v34, v35\n\t"
        "v_max3_u32 v116, v116, v36, v37\n\t"
        "v_max3_u32 v116, v116, v38, v39\n\t"
        "v_max3_u32 v116, v116, v40, v41\n\t"
        "v_max3_u32 v116, v116, v42, v43\n\t"
        "v_max3_u32 v116, v116, v44, v45\n\t"
        "v_max3_u32 v116, v116, v46, v47\n\t"
        "v_max3_u32 v116, v116, v48, v49\n\t"
        "v_max3_u32 v116, v116, v50, v51\n\t"
        "v_max3_u32 v116, v116, v52, v53\n\t"
        "v_max3_u32 v116, v116, v54, v55\n\t"
        "v_max3_u32 v116, v116, v56, v57\n\t"
        "v_max3_u32 v116, v116, v58, v59\n\t"
        "v_max3_u32 v116, v116, v60, v61\n\t"
        "v_max3_u32 v116, v116, v62, v63\n\t"
        "v_max3_u32 v116, v116, v64, v65\n\t"
        "v_max3_u32 v116, v116, v66, v67\n\t"
        "v_max3_u32 v116, v116, v68, v69\n\t"
        "v_max3_u32 v116, v116, v70, v71\n\t"
        "v_max3_u32 v116, v116, v72, v73\n\t"
        "v_max3_u32 v116, v116, v74, v75\n\t"
        "v_max3_u32 v116, v116, v76, v77\n\t"
        "v_max3_u32 v116, v116, v78, v79\n\t"
        "v_max3_u32 v116, v116, v80, v81\n\t"
        "v_max3_u32 v116, v116, v82, v83\n\t"
        "v_max3_u32 v116, v116, v84, v85\n\t"
        "v_max3_u32 v116, v116, v86, v87\n\t"
        "v_max3_u32 v116, v116, v88, v89\n\t"
        "v_max3_u32 v116, v116, v90, v91\n\t"
        "v_max3_u32 v116, v116, v92, v93\n\t"
        "v_max3_u32 v116, v116, v94, v95\n\t"
        "v_max3_u32 v116, v116, v96, v97\n\t"
        "v_max3_u32 v116, v116, v98, v99\n\t"
        "v_max3_u32 v116, v116, v100, v101\n\t"
        "v_max3_u32 v116, v116, v102, v103\n\t"
        "v_max3_u32 v116, v116, v104, v105\n\t"
        "v_max3_u32 v116, v116, v106, v107\n\t"
        "v_max3_u32 v116, v116, v108, v109\n\t"
        "v_max3_u32 v116, v116, v110, v111\n\t"
        "v_cmp_eq_u32 vcc, 0xffbfffbf, v116\n\t"
        "s_cmp_lt_u32 %[pass], %[cap]\n\t"
        "s_cbranch_vccz Lbad%=\n\t"
        "s_cbranch_scc1 Lzp_%=\n\t"
        "Lbad%=:\n\t"
        "s_mov_b32 %[bad], 1\n\t"
        "s_branch Lend%=\n\t"
        "Lfi%=:\n\t"
        "s_mov_b32 s97, 1\n\t"
        "v_mov_b32 v116, %[fv]\n\t"
        "ds_write_b32 v120, v116 offset:16392\n\t"
        "s_waitcnt lgkmcnt(0)\n\t"
        "s_barrier\n\t"
        "s_sleep 2\n\t"
        "s_branch Lzp_%=\n\t"
        "Lwu%=:\n\t"
        "ds_read_b32 v121, %[uf]\n\t"
        "s_waitcnt lgkmcnt(0)\n\t"
        "v_cmp_ne_u32 vcc, %[fv], v121\n\t"
        "s_cbranch_vccnz Lwu%=\n\t"
        "s_branch Lrd%=\n\t"
        "Lend%=:\n\t"
        "s_setprio 0\n\t"
        : [bad] "=&s"(bad), [pass] "=&s"(pass)
        : [sidx] "v"(sidx), [pv] "v"(pv), [zv] "v"(zv), [ra] "v"(ra), [rb] "v"(rb), [mo] "v"(mo), [uf] "v"(uf), [rs] "s"(rs), [so] "s"(so), [sz] "s"(sz), [sm] "s"(sm), [lb] "s"(lb), [fv] "s"(fv), [cap] "s"(cap), [zp] "s"(zp), [px] "s"(px), [zx] "s"(zx)
        : "s80", "s81", "s82", "s83", "s84", "s85", "s86", "s87", "s88", "s89", "s90", "s91", "s92", "s93", "s94", "s95", "s96", "s97", "s98", "v14", "v15", "v16", "v17", "v18", "v19", "v20", "v21", "v22", "v23", "v24", "v25", "v26", "v27", "v28", "v29", "v30", "v31", "v32", "v33", "v34", "v35", "v36", "v37", "v38", "v39", "v40", "v41", "v42", "v43", "v44", "v45", "v46", "v47", "v48", "v49", "v50", "v51", "v52", "v53", "v54", "v55", "v56", "v57", "v58", "v59", "v60", "v61", "v62", "v63", "v64", "v65", "v66", "v67", "v68", "v69", "v70", "v71", "v72", "v73", "v74", "v75", "v76", "v77", "v78", "v79", "v80", "v81", "v82", "v83", "v84", "v85", "v86", "v87", "v88", "v89", "v90", "v91", "v92", "v93", "v94", "v95", "v96", "v97", "v98", "v99", "v100", "v101", "v102", "v103", "v104", "v105", "v106", "v107", "v108", "v109", "v110", "v111", "v112", "v113", "v114", "v115", "v116", "v117", "v118", "v119", "v120", "v121", "vcc", "scc", "memory");
}

__device__ __forceinline__ void wx0(const u32x32 (&W)[5], u32 xa, u32 oa, u32x4 rs, u32 xv, u32 xs, u32 xd, u32 o0, u32 o1 STP) {
    asm volatile(
        "ds_read_b128 v[192:195], %[xa] offset:0\n\t"
        "ds_read_b128 v[196:199], %[xa] offset:20480\n\t"
        "ds_read_b128 v[200:203], %[xa] offset:1024\n\t"
        "ds_read_b128 v[204:207], %[xa] offset:21504\n\t"
        "ds_read_b128 v[208:211], %[xa] offset:2048\n\t"
        "ds_read_b128 v[212:215], %[xa] offset:22528\n\t"
        "s_waitcnt vmcnt(8)\n\t"
        "s_barrier\n\t"
        "s_waitcnt lgkmcnt(4)\n\t"
        "v_mfma_f32_16x16x32_bf16 v[176:179], v[16:19], v[192:195], 0\n\t"
        "ds_read_b128 v[216:219], %[xa] offset:3072\n\t"
        "v_mfma_f32_16x16x32_bf16 v[184:187], v[16:19], v[196:199], 0\n\t"
        "ds_read_b128 v[220:223], %[xa] offset:23552\n\t"
        "v_mfma_f32_16x16x32_bf16 v[180:183], v[96:99], v[192:195], 0\n\t"
        "v_mfma_f32_16x16x32_bf16 v[188:191], v[96:99], v[196:199], 0\n\t"
        "s_waitcnt lgkmcnt(4)\n\t"
        "v_mfma_f32_16x16x32_bf16 v[176:179], v[20:23], v[200:203], v[176:179]\n\t"
        "ds_read_b128 v[192:195], %[xa] offset:4096\n\t"
        "v_mfma_f32_16x16x32_bf16 v[184:187], v[20:23], v[204:207], v[184:187]\n\t"
        "ds_read_b128 v[196:199], %[xa] offset:24576\n\t"
        "v_mfma_f32_16x16x32_bf16 v[180:183], v[100:103], v[200:203], v[180:183]\n\t"
        "v_mfma_f32_16x16x32_bf16 v[188:191], v[100:103], v[204:207], v[188:191]\n\t"
        "s_waitcnt lgkmcnt(4)\n\t"
        "v_mfma_f32_16x16x32_bf16 v[176:179], v[24:27], v[208:211], v[176:179]\n\t"
        "ds_read_b128 v[200:203], %[xa] offset:5120\n\t"
        "v_mfma_f32_16x16x32_bf16 v[184:187], v[24:27], v[212:215], v[184:187]\n\t"
        "ds_read_b128 v[204:207], %[xa] offset:25600\n\t"
        "v_mfma_f32_16x16x32_bf16 v[180:183], v[104:107], v[208:211], v[180:183]\n\t"
        "v_mfma_f32_16x16x32_bf16 v[188:191], v[104:107], v[212:215], v[188:191]\n\t"
        "s_waitcnt lgkmcnt(4)\n\t"
        "v_mfma_f32_16x16x32_bf16 v[176:179], v[28:31], v[216:219], v[176:179]\n\t"
        "ds_read_b128 v[208:211], %[xa] offset:6144\n\t"
        "v_mfma_f32_16x16x32_bf16 v[184:187], v[28:31], v[220:223], v[184:187]\n\t"
        "ds_read_b128 v[212:215], %[xa] offset:26624\n\t"
        "v_mfma_f32_16x16x32_bf16 v[180:183], v[108:111], v[216:219], v[180:183]\n\t"
        "v_mfma_f32_16x16x32_bf16 v[188:191], v[108:111], v[220:223], v[188:191]\n\t"
        "s_waitcnt lgkmcnt(4)\n\t"
        "v_mfma_f32_16x16x32_bf16 v[176:179], v[32:35], v[192:195], v[176:179]\n\t"
        "ds_read_b128 v[216:219], %[xa] offset:7168\n\t"
        "v_mfma_f32_16x16x32_bf16 v[184:187], v[32:35], v[196:199], v[184:187]\n\t"
        "ds_read_b128 v[220:223], %[xa] offset:27648\n\t"
        "v_mfma_f32_16x16x32_bf16 v[180:183], v[112:115], v[192:195], v[180:183]\n\t"
        "v_mfma_f32_16x16x32_bf16 v[188:191], v[112:115], v[196:199], v[188:191]\n\t"
        "s_waitcnt lgkmcnt(4)\n\t"
        "v_mfma_f32_16x16x32_bf16 v[176:179], v[36:39], v[200:203], v[176:179]\n\t"
        "ds_read_b128 v[192:195], %[xa] offset:8192\n\t"
        "v_mfma_f32_16x16x32_bf16 v[184:187], v[36:39], v[204:207], v[184:187]\n\t"
        "ds_read_b128 v[196:199], %[xa] offset:28672\n\t"
        "v_mfma_f32_16x16x32_bf16 v[180:183], v[116:119], v[200:203], v[180:183]\n\t"
        "v_mfma_f32_16x16x32_bf16 v[188:191], v[116:119], v[204:207], v[188:191]\n\t"
        "s_waitcnt lgkmcnt(4)\n\t"
        "v_mfma_f32_16x16x32_bf16 v[176:179], v[40:43], v[208:211], v[176:179]\n\t"
        "ds_read_b128 v[200:203], %[xa] offset:9216\n\t"
        "v_mfma_f32_16x16x32_bf16 v[184:187], v[40:43], v[212:215], v[184:187]\n\t"
        "ds_read_b128 v[204:207], %[xa] offset:29696\n\t"
        "v_mfma_f32_16x16x32_bf16 v[180:183], v[120:123], v[208:211], v[180:183]\n\t"
        "v_mfma_f32_16x16x32_bf16 v[188:191], v[120:123], v[212:215], v[188:191]\n\t"
        "s_waitcnt lgkmcnt(4)\n\t"
        "v_mfma_f32_16x16x32_bf16 v[176:179], v[44:47], v[216:219], v[176:179]\n\t"
        "ds_read_b128 v[208:211], %[xa] offset:10240\n\t"
        "v_mfma_f32_16x16x32_bf16 v[184:187], v[44:47], v[220:223], v[184:187]\n\t"
        "ds_read_b128 v[212:215], %[xa] offset:30720\n\t"
        "v_mfma_f32_16x16x32_bf16 v[180:183], v[124:127], v[216:219], v[180:183]\n\t"
        "v_mfma_f32_16x16x32_bf16 v[188:191], v[124:127], v[220:223], v[188:191]\n\t"
        "s_waitcnt lgkmcnt(4)\n\t"
        "v_mfma_f32_16x16x32_bf16 v[176:179], v[48:51], v[192:195], v[176:179]\n\t"
        "ds_read_b128 v[216:219], %[xa] offset:11264\n\t"
        "v_mfma_f32_16x16x32_bf16 v[184:187], v[48:51], v[196:199], v[184:187]\n\t"
        "ds_read_b128 v[220:223], %[xa] offset:31744\n\t"
        "v_mfma_f32_16x16x32_bf16 v[180:183], v[128:131], v[192:195], v[180:183]\n\t"
        "v_mfma_f32_16x16x32_bf16 v[188:191], v[128:131], v[196:199], v[188:191]\n\t"
        "s_waitcnt lgkmcnt(4)\n\t"
        "v_mfma_f32_16x16x32_bf16 v[176:179], v[52:55], v[200:203], v[176:179]\n\t"
        "ds_read_b128 v[192:195], %[xa] offset:12288\n\t"
        "v_mfma_f32_16x16x32_bf16 v[184:187], v[52:55], v[204:207], v[184:187]\n\t"
        "ds_read_b128 v[196:199], %[xa] offset:32768\n\t"
        "v_mfma_f32_16x16x32_bf16 v[180:183], v[132:135], v[200:203], v[180:183]\n\t"
        "v_mfma_f32_16x16x32_bf16 v[188:191], v[132:135], v[204:207], v[188:191]\n\t"
        "s_waitcnt lgkmcnt(4)\n\t"
        "v_mfma_f32_16x16x32_bf16 v[176:179], v[56:59], v[208:211], v[176:179]\n\t"
        "ds_read_b128 v[200:203], %[xa] offset:13312\n\t"
        "v_mfma_f32_16x16x32_bf16 v[184:187], v[56:59], v[212:215], v[184:187]\n\t"
        "ds_read_b128 v[204:207], %[xa] offset:33792\n\t"
        "v_mfma_f32_16x16x32_bf16 v[180:183], v[136:139], v[208:211], v[180:183]\n\t"
        "v_mfma_f32_16x16x32_bf16 v[188:191], v[136:139], v[212:215], v[188:191]\n\t"
        "s_waitcnt lgkmcnt(4)\n\t"
        "v_mfma_f32_16x16x32_bf16 v[176:179], v[60:63], v[216:219], v[176:179]\n\t"
        "ds_read_b128 v[208:211], %[xa] offset:14336\n\t"
        "v_mfma_f32_16x16x32_bf16 v[184:187], v[60:63], v[220:223], v[184:187]\n\t"
        "ds_read_b128 v[212:215], %[xa] offset:34816\n\t"
        "v_mfma_f32_16x16x32_bf16 v[180:183], v[140:143], v[216:219], v[180:183]\n\t"
        "v_mfma_f32_16x16x32_bf16 v[188:191], v[140:143], v[220:223], v[188:191]\n\t"
        "s_waitcnt lgkmcnt(4)\n\t"
        "v_mfma_f32_16x16x32_bf16 v[176:179], v[64:67], v[192:195], v[176:179]\n\t"
        "ds_read_b128 v[216:219], %[xa] offset:15360\n\t"
        "v_mfma_f32_16x16x32_bf16 v[184:187], v[64:67], v[196:199], v[184:187]\n\t"
        "ds_read_b128 v[220:223], %[xa] offset:35840\n\t"
        "v_mfma_f32_16x16x32_bf16 v[180:183], v[144:147], v[192:195], v[180:183]\n\t"
        "v_mfma_f32_16x16x32_bf16 v[188:191], v[144:147], v[196:199], v[188:191]\n\t"
        "s_waitcnt lgkmcnt(4)\n\t"
        "v_mfma_f32_16x16x32_bf16 v[176:179], v[68:71], v[200:203], v[176:179]\n\t"
        "ds_read_b128 v[192:195], %[xa] offset:16384\n\t"
        "v_mfma_f32_16x16x32_bf16 v[184:187], v[68:71], v[204:207], v[184:187]\n\t"
        "ds_read_b128 v[196:199], %[xa] offset:36864\n\t"
        "v_mfma_f32_16x16x32_bf16 v[180:183], v[148:151], v[200:203], v[180:183]\n\t"
        "v_mfma_f32_16x16x32_bf16 v[188:191], v[148:151], v[204:207], v[188:191]\n\t"
        "s_waitcnt lgkmcnt(4)\n\t"
        "v_mfma_f32_16x16x32_bf16 v[176:179], v[72:75], v[208:211], v[176:179]\n\t"
        "ds_read_b128 v[200:203], %[xa] offset:17408\n\t"
        "v_mfma_f32_16x16x32_bf16 v[184:187], v[72:75], v[212:215], v[184:187]\n\t"
        "ds_read_b128 v[204:207], %[xa] offset:37888\n\t"
        "v_mfma_f32_16x16x32_bf16 v[180:183], v[152:155], v[208:211], v[180:183]\n\t"
        "v_mfma_f32_16x16x32_bf16 v[188:191], v[152:155], v[212:215], v[188:191]\n\t"
        "s_waitcnt lgkmcnt(4)\n\t"
        "v_mfma_f32_16x16x32_bf16 v[176:179], v[76:79], v[216:219], v[176:179]\n\t"
        "ds_read_b128 v[208:211], %[xa] offset:18432\n\t"
        "v_mfma_f32_16x16x32_bf16 v[184:187], v[76:79], v[220:223], v[184:187]\n\t"
        "ds_read_b128 v[212:215], %[xa] offset:38912\n\t"
        "v_mfma_f32_16x16x32_bf16 v[180:183], v[156:159], v[216:219], v[180:183]\n\t"
        "v_mfma_f32_16x16x32_bf16 v[188:191], v[156:159], v[220:223], v[188:191]\n\t"
        "s_waitcnt lgkmcnt(4)\n\t"
        "v_mfma_f32_16x16x32_bf16 v[176:179], v[80:83], v[192:195], v[176:179]\n\t"
        "ds_read_b128 v[216:219], %[xa] offset:19456\n\t"
        "v_mfma_f32_16x16x32_bf16 v[184:187], v[80:83], v[196:199], v[184:187]\n\t"
        "ds_read_b128 v[220:223], %[xa] offset:39936\n\t"
        "v_mfma_f32_16x16x32_bf16 v[180:183], v[160:163], v[192:195], v[180:183]\n\t"
        "v_mfma_f32_16x16x32_bf16 v[188:191], v[160:163], v[196:199], v[188:191]\n\t"
        "s_waitcnt lgkmcnt(4)\n\t"
        "v_mfma_f32_16x16x32_bf16 v[176:179], v[84:87], v[200:203], v[176:179]\n\t"
        "v_mfma_f32_16x16x32_bf16 v[184:187], v[84:87], v[204:207], v[184:187]\n\t"
        "v_mfma_f32_16x16x32_bf16 v[180:183], v[164:167], v[200:203], v[180:183]\n\t"
        "v_mfma_f32_16x16x32_bf16 v[188:191], v[164:167], v[204:207], v[188:191]\n\t"
        "s_waitcnt lgkmcnt(2)\n\t"
        "v_mfma_f32_16x16x32_bf16 v[176:179], v[88:91], v[208:211], v[176:179]\n\t"
        "v_mfma_f32_16x16x32_bf16 v[184:187], v[88:91], v[212:215], v[184:187]\n\t"
        "v_mfma_f32_16x16x32_bf16 v[180:183], v[168:171], v[208:211], v[180:183]\n\t"
        "v_mfma_f32_16x16x32_bf16 v[188:191], v[168:171], v[212:215], v[188:191]\n\t"
        "s_waitcnt lgkmcnt(0)\n\t"
        "v_mfma_f32_16x16x32_bf16 v[176:179], v[92:95], v[216:219], v[176:179]\n\t"
        "v_mfma_f32_16x16x32_bf16 v[184:187], v[92:95], v[220:223], v[184:187]\n\t"
        "v_mfma_f32_16x16x32_bf16 v[180:183], v[172:175], v[216:219], v[180:183]\n\t"
        "v_mfma_f32_16x16x32_bf16 v[188:191], v[172:175], v[220:223], v[188:191]\n\t"
        "s_nop 4\n\t"
        "v_cvt_pk_bf16_f32 v224, v176, v177\n\t"
        "v_cvt_pk_bf16_f32 v225, v178, v179\n\t"
        "v_cvt_pk_bf16_f32 v226, v180, v181\n\t"
        "v_cvt_pk_bf16_f32 v227, v182, v183\n\t"
        "buffer_store_dwordx4 v[224:227], %[oa], %[rs], %[o0] offen sc1\n\t"
        "v_cvt_pk_bf16_f32 v228, v184, v185\n\t"
        "v_cvt_pk_bf16_f32 v229, v186, v187\n\t"
        "v_cvt_pk_bf16_f32 v230, v188, v189\n\t"
        "v_cvt_pk_bf16_f32 v231, v190, v191\n\t"
        "buffer_store_dwordx4 v[228:231], %[oa], %[rs], %[o1] offen sc1\n\t"
        "s_mov_b32 s88, m0\n\t"
        "s_mov_b32 m0, %[xd]\n\t"
        "s_add_u32 s89, %[xs], 0x1000\n\t"
        "buffer_load_dwordx4 %[xv], %[rs], %[xs] offen offset:0 sc1 lds\n\t"
        "buffer_load_dwordx4 %[xv], %[rs], %[xs] offen offset:1024 sc1 lds\n\t"
        "buffer_load_dwordx4 %[xv], %[rs], %[xs] offen offset:2048 sc1 lds\n\t"
        "buffer_load_dwordx4 %[xv], %[rs], %[xs] offen offset:3072 sc1 lds\n\t"
        "s_add_u32 m0, m0, 0x1000\n\t"
        "s_nop 0\n\t"
        "buffer_load_dwordx4 %[xv], %[rs], s89 offen offset:0 sc1 lds\n\t"
        "buffer_load_dwordx4 %[xv], %[rs], s89 offen offset:1024 sc1 lds\n\t"
        "buffer_load_dwordx4 %[xv], %[rs], s89 offen offset:2048 sc1 lds\n\t"
        "buffer_load_dwordx4 %[xv], %[rs], s89 offen offset:3072 sc1 lds\n\t"
        "s_mov_b32 m0, s88\n\t"
        :
        : [w0] "{v[16:47]}"(W[0]), [w1] "{v[48:79]}"(W[1]), [w2] "{v[80:111]}"(W[2]), [w3] "{v[112:143]}"(W[3]), [w4] "{v[144:175]}"(W[4]), [xa] "v"(xa), [oa] "v"(oa), [xv] "v"(xv), [xs] "s"(xs), [xd] "s"(xd), [rs] "s"(rs), [o0] "s"(o0), [o1] "s"(o1)
        : "s88", "s89", "v176", "v177", "v178", "v179", "v180", "v181", "v182", "v183", "v184", "v185", "v186", "v187", "v188", "v189", "v190", "v191", "v192", "v193", "v194", "v195", "v196", "v197", "v198", "v199", "v200", "v201", "v202", "v203", "v204", "v205", "v206", "v207", "v208", "v209", "v210", "v211", "v212", "v213", "v214", "v215", "v216", "v217", "v218", "v219", "v220", "v221", "v222", "v223", "v224", "v225", "v226", "v227", "v228", "v229", "v230", "v231", "vcc", "scc", "memory");
}

__device__ __forceinline__ void wx1(const u32x32 (&W)[5], u32 xa, u32 oa STP) {
    asm volatile(
        "ds_read_b128 v[192:195], %[xa] offset:0\n\t"
        "ds_read_b128 v[196:199], %[xa] offset:20480\n\t"
        "ds_read_b128 v[200:203], %[xa] offset:1024\n\t"
        "ds_read_b128 v[204:207], %[xa] offset:21504\n\t"
        "ds_read_b128 v[208:211], %[xa] offset:2048\n\t"
        "ds_read_b128 v[212:215], %[xa] offset:22528\n\t"
        "s_waitcnt lgkmcnt(6)\n\t"
        "s_barrier\n\t"
        "s_waitcnt lgkmcnt(4)\n\t"
        "v_mfma_f32_16x16x32_bf16 v[176:179], v[16:19], v[192:195], 0\n\t"
        "ds_read_b128 v[216:219], %[xa] offset:3072\n\t"
        "v_mfma_f32_16x16x32_bf16 v[184:187], v[16:19], v[196:199], 0\n\t"
        "ds_read_b128 v[220:223], %[xa] offset:23552\n\t"
        "v_mfma_f32_16x16x32_bf16 v[180:183], v[96:99], v[192:195], 0\n\t"
        "v_mfma_f32_16x16x32_bf16 v[188:191], v[96:99], v[196:199], 0\n\t"
        "s_waitcnt lgkmcnt(4)\n\t"
        "v_mfma_f32_16x16x32_bf16 v[176:179], v[20:23], v[200:203], v[176:179]\n\t"
        "ds_read_b128 v[192:195], %[xa] offset:4096\n\t"
        "v_mfma_f32_16x16x32_bf16 v[184:187], v[20:23], v[204:207], v[184:187]\n\t"
        "ds_read_b128 v[196:199], %[xa] offset:24576\n\t"
        "v_mfma_f32_16x16x32_bf16 v[180:183], v[100:103], v[200:203], v[180:183]\n\t"
        "v_mfma_f32_16x16x32_bf16 v[188:191], v[100:103], v[204:207], v[188:191]\n\t"
        "s_waitcnt lgkmcnt(4)\n\t"
        "v_mfma_f32_16x16x32_bf16 v[176:179], v[24:27], v[208:211], v[176:179]\n\t"
        "ds_read_b128 v[200:203], %[xa] offset:5120\n\t"
        "v_mfma_f32_16x16x32_bf16 v[184:187], v[24:27], v[212:215], v[184:187]\n\t"
        "ds_read_b128 v[204:207], %[xa] offset:25600\n\t"
        "v_mfma_f32_16x16x32_bf16 v[180:183], v[104:107], v[208:211], v[180:183]\n\t"
        "v_mfma_f32_16x16x32_bf16 v[188:191], v[104:107], v[212:215], v[188:191]\n\t"
        "s_waitcnt lgkmcnt(4)\n\t"
        "v_mfma_f32_16x16x32_bf16 v[176:179], v[28:31], v[216:219], v[176:179]\n\t"
        "ds_read_b128 v[208:211], %[xa] offset:6144\n\t"
        "v_mfma_f32_16x16x32_bf16 v[184:187], v[28:31], v[220:223], v[184:187]\n\t"
        "ds_read_b128 v[212:215], %[xa] offset:26624\n\t"
        "v_mfma_f32_16x16x32_bf16 v[180:183], v[108:111], v[216:219], v[180:183]\n\t"
        "v_mfma_f32_16x16x32_bf16 v[188:191], v[108:111], v[220:223], v[188:191]\n\t"
        "s_waitcnt lgkmcnt(4)\n\t"
        "v_mfma_f32_16x16x32_bf16 v[176:179], v[32:35], v[192:195], v[176:179]\n\t"
        "ds_read_b128 v[216:219], %[xa] offset:7168\n\t"
        "v_mfma_f32_16x16x32_bf16 v[184:187], v[32:35], v[196:199], v[184:187]\n\t"
        "ds_read_b128 v[220:223], %[xa] offset:27648\n\t"
        "v_mfma_f32_16x16x32_bf16 v[180:183], v[112:115], v[192:195], v[180:183]\n\t"
        "v_mfma_f32_16x16x32_bf16 v[188:191], v[112:115], v[196:199], v[188:191]\n\t"
        "s_waitcnt lgkmcnt(4)\n\t"
        "v_mfma_f32_16x16x32_bf16 v[176:179], v[36:39], v[200:203], v[176:179]\n\t"
        "ds_read_b128 v[192:195], %[xa] offset:8192\n\t"
        "v_mfma_f32_16x16x32_bf16 v[184:187], v[36:39], v[204:207], v[184:187]\n\t"
        "ds_read_b128 v[196:199], %[xa] offset:28672\n\t"
        "v_mfma_f32_16x16x32_bf16 v[180:183], v[116:119], v[200:203], v[180:183]\n\t"
        "v_mfma_f32_16x16x32_bf16 v[188:191], v[116:119], v[204:207], v[188:191]\n\t"
        "s_waitcnt lgkmcnt(4)\n\t"
        "v_mfma_f32_16x16x32_bf16 v[176:179], v[40:43], v[208:211], v[176:179]\n\t"
        "ds_read_b128 v[200:203], %[xa] offset:9216\n\t"
        "v_mfma_f32_16x16x32_bf16 v[184:187], v[40:43], v[212:215], v[184:187]\n\t"
        "ds_read_b128 v[204:207], %[xa] offset:29696\n\t"
        "v_mfma_f32_16x16x32_bf16 v[180:183], v[120:123], v[208:211], v[180:183]\n\t"
        "v_mfma_f32_16x16x32_bf16 v[188:191], v[120:123], v[212:215], v[188:191]\n\t"
        "s_waitcnt lgkmcnt(4)\n\t"
        "v_mfma_f32_16x16x32_bf16 v[176:179], v[44:47], v[216:219], v[176:179]\n\t"
        "ds_read_b128 v[208:211], %[xa] offset:10240\n\t"
        "v_mfma_f32_16x16x32_bf16 v[184:187], v[44:47], v[220:223], v[184:187]\n\t"
        "ds_read_b128 v[212:215], %[xa] offset:30720\n\t"
        "v_mfma_f32_16x16x32_bf16 v[180:183], v[124:127], v[216:219], v[180:183]\n\t"
        "v_mfma_f32_16x16x32_bf16 v[188:191], v[124:127], v[220:223], v[188:191]\n\t"
        "s_waitcnt lgkmcnt(4)\n\t"
        "v_mfma_f32_16x16x32_bf16 v[176:179], v[48:51], v[192:195], v[176:179]\n\t"
        "ds_read_b128 v[216:219], %[xa] offset:11264\n\t"
        "v_mfma_f32_16x16x32_bf16 v[184:187], v[48:51], v[196:199], v[184:187]\n\t"
        "ds_read_b128 v[220:223], %[xa] offset:31744\n\t"
        "v_mfma_f32_16x16x32_bf16 v[180:183], v[128:131], v[192:195], v[180:183]\n\t"
        "v_mfma_f32_16x16x32_bf16 v[188:191], v[128:131], v[196:199], v[188:191]\n\t"
        "s_waitcnt lgkmcnt(4)\n\t"
        "v_mfma_f32_16x16x32_bf16 v[176:179], v[52:55], v[200:203], v[176:179]\n\t"
        "ds_read_b128 v[192:195], %[xa] offset:12288\n\t"
        "v_mfma_f32_16x16x32_bf16 v[184:187], v[52:55], v[204:207], v[184:187]\n\t"
        "ds_read_b128 v[196:199], %[xa] offset:32768\n\t"
        "v_mfma_f32_16x16x32_bf16 v[180:183], v[132:135], v[200:203], v[180:183]\n\t"
        "v_mfma_f32_16x16x32_bf16 v[188:191], v[132:135], v[204:207], v[188:191]\n\t"
        "s_waitcnt lgkmcnt(4)\n\t"
        "v_mfma_f32_16x16x32_bf16 v[176:179], v[56:59], v[208:211], v[176:179]\n\t"
        "ds_read_b128 v[200:203], %[xa] offset:13312\n\t"
        "v_mfma_f32_16x16x32_bf16 v[184:187], v[56:59], v[212:215], v[184:187]\n\t"
        "ds_read_b128 v[204:207], %[xa] offset:33792\n\t"
        "v_mfma_f32_16x16x32_bf16 v[180:183], v[136:139], v[208:211], v[180:183]\n\t"
        "v_mfma_f32_16x16x32_bf16 v[188:191], v[136:139], v[212:215], v[188:191]\n\t"
        "s_waitcnt lgkmcnt(4)\n\t"
        "v_mfma_f32_16x16x32_bf16 v[176:179], v[60:63], v[216:219], v[176:179]\n\t"
        "ds_read_b128 v[208:211], %[xa] offset:14336\n\t"
        "v_mfma_f32_16x16x32_bf16 v[184:187], v[60:63], v[220:223], v[184:187]\n\t"
        "ds_read_b128 v[212:215], %[xa] offset:34816\n\t"
        "v_mfma_f32_16x16x32_bf16 v[180:183], v[140:143], v[216:219], v[180:183]\n\t"
        "v_mfma_f32_16x16x32_bf16 v[188:191], v[140:143], v[220:223], v[188:191]\n\t"
        "s_waitcnt lgkmcnt(4)\n\t"
        "v_mfma_f32_16x16x32_bf16 v[176:179], v[64:67], v[192:195], v[176:179]\n\t"
        "ds_read_b128 v[216:219], %[xa] offset:15360\n\t"
        "v_mfma_f32_16x16x32_bf16 v[184:187], v[64:67], v[196:199], v[184:187]\n\t"
        "ds_read_b128 v[220:223], %[xa] offset:35840\n\t"
        "v_mfma_f32_16x16x32_bf16 v[180:183], v[144:147], v[192:195], v[180:183]\n\t"
        "v_mfma_f32_16x16x32_bf16 v[188:191], v[144:147], v[196:199], v[188:191]\n\t"
        "s_waitcnt lgkmcnt(4)\n\t"
        "v_mfma_f32_16x16x32_bf16 v[176:179], v[68:71], v[200:203], v[176:179]\n\t"
        "ds_read_b128 v[192:195], %[xa] offset:16384\n\t"
        "v_mfma_f32_16x16x32_bf16 v[184:187], v[68:71], v[204:207], v[184:187]\n\t"
        "ds_read_b128 v[196:199], %[xa] offset:36864\n\t"
        "v_mfma_f32_16x16x32_bf16 v[180:183], v[148:151], v[200:203], v[180:183]\n\t"
        "v_mfma_f32_16x16x32_bf16 v[188:191], v[148:151], v[204:207], v[188:191]\n\t"
        "s_waitcnt lgkmcnt(4)\n\t"
        "v_mfma_f32_16x16x32_bf16 v[176:179], v[72:75], v[208:211], v[176:179]\n\t"
        "ds_read_b128 v[200:203], %[xa] offset:17408\n\t"
        "v_mfma_f32_16x16x32_bf16 v[184:187], v[72:75], v[212:215], v[184:187]\n\t"
        "ds_read_b128 v[204:207], %[xa] offset:37888\n\t"
        "v_mfma_f32_16x16x32_bf16 v[180:183], v[152:155], v[208:211], v[180:183]\n\t"
        "v_mfma_f32_16x16x32_bf16 v[188:191], v[152:155], v[212:215], v[188:191]\n\t"
        "s_waitcnt lgkmcnt(4)\n\t"
        "v_mfma_f32_16x16x32_bf16 v[176:179], v[76:79], v[216:219], v[176:179]\n\t"
        "ds_read_b128 v[208:211], %[xa] offset:18432\n\t"
        "v_mfma_f32_16x16x32_bf16 v[184:187], v[76:79], v[220:223], v[184:187]\n\t"
        "ds_read_b128 v[212:215], %[xa] offset:38912\n\t"
        "v_mfma_f32_16x16x32_bf16 v[180:183], v[156:159], v[216:219], v[180:183]\n\t"
        "v_mfma_f32_16x16x32_bf16 v[188:191], v[156:159], v[220:223], v[188:191]\n\t"
        "s_waitcnt lgkmcnt(4)\n\t"
        "v_mfma_f32_16x16x32_bf16 v[176:179], v[80:83], v[192:195], v[176:179]\n\t"
        "ds_read_b128 v[216:219], %[xa] offset:19456\n\t"
        "v_mfma_f32_16x16x32_bf16 v[184:187], v[80:83], v[196:199], v[184:187]\n\t"
        "ds_read_b128 v[220:223], %[xa] offset:39936\n\t"
        "v_mfma_f32_16x16x32_bf16 v[180:183], v[160:163], v[192:195], v[180:183]\n\t"
        "v_mfma_f32_16x16x32_bf16 v[188:191], v[160:163], v[196:199], v[188:191]\n\t"
        "s_waitcnt lgkmcnt(4)\n\t"
        "v_mfma_f32_16x16x32_bf16 v[176:179], v[84:87], v[200:203], v[176:179]\n\t"
        "v_mfma_f32_16x16x32_bf16 v[184:187], v[84:87], v[204:207], v[184:187]\n\t"
        "v_mfma_f32_16x16x32_bf16 v[180:183], v[164:167], v[200:203], v[180:183]\n\t"
        "v_mfma_f32_16x16x32_bf16 v[188:191], v[164:167], v[204:207], v[188:191]\n\t"
        "s_waitcnt lgkmcnt(2)\n\t"
        "v_mfma_f32_16x16x32_bf16 v[176:179], v[88:91], v[208:211], v[176:179]\n\t"
        "v_mfma_f32_16x16x32_bf16 v[184:187], v[88:91], v[212:215], v[184:187]\n\t"
        "v_mfma_f32_16x16x32_bf16 v[180:183], v[168:171], v[208:211], v[180:183]\n\t"
        "v_mfma_f32_16x16x32_bf16 v[188:191], v[168:171], v[212:215], v[188:191]\n\t"
        "s_waitcnt lgkmcnt(0)\n\t"
        "v_mfma_f32_16x16x32_bf16 v[176:179], v[92:95], v[216:219], v[176:179]\n\t"
        "v_mfma_f32_16x16x32_bf16 v[184:187], v[92:95], v[220:223], v[184:187]\n\t"
        "v_mfma_f32_16x16x32_bf16 v[180:183], v[172:175], v[216:219], v[180:183]\n\t"
        "v_mfma_f32_16x16x32_bf16 v[188:191], v[172:175], v[220:223], v[188:191]\n\t"
        "s_nop 4\n\t"
        "v_cvt_pk_bf16_f32 v224, v176, v177\n\t"
        "v_cvt_pk_bf16_f32 v225, v178, v179\n\t"
        "v_cvt_pk_bf16_f32 v226, v180, v181\n\t"
        "v_cvt_pk_bf16_f32 v227, v182, v183\n\t"
        "ds_write_b128 %[oa], v[224:227] offset:0\n\t"
        "v_cvt_pk_bf16_f32 v228, v184, v185\n\t"
        "v_cvt_pk_bf16_f32 v229, v186, v187\n\t"
        "v_cvt_pk_bf16_f32 v230, v188, v189\n\t"
        "v_cvt_pk_bf16_f32 v231, v190, v191\n\t"
        "ds_write_b128 %[oa], v[228:231] offset:4096\n\t"
        :
        : [w0] "{v[16:47]}"(W[0]), [w1] "{v[48:79]}"(W[1]), [w2] "{v[80:111]}"(W[2]), [w3] "{v[112:143]}"(W[3]), [w4] "{v[144:175]}"(W[4]), [xa] "v"(xa), [oa] "v"(oa)
        : "v176", "v177", "v178", "v179", "v180", "v181", "v182", "v183", "v184", "v185", "v186", "v187", "v188", "v189", "v190", "v191", "v192", "v193", "v194", "v195", "v196", "v197", "v198", "v199", "v200", "v201", "v202", "v203", "v204", "v205", "v206", "v207", "v208", "v209", "v210", "v211", "v212", "v213", "v214", "v215", "v216", "v217", "v218", "v219", "v220", "v221", "v222", "v223", "v224", "v225", "v226", "v227", "v228", "v229", "v230", "v231", "vcc", "scc", "memory");
}

__device__ __forceinline__ void wk(const u32x32 (&W)[5], u32 xa, u32 oa, u32x4 rs, u32 o0, u32 o1, float sc, u32 xv, u32 xs, u32 xd, u32 ca, u32 fc, u32 fw, u32 fo STP) {
    asm volatile(
        "ds_read_b128 v[192:195], %[xa] offset:0\n\t"
        "ds_read_b128 v[196:199], %[xa] offset:20480\n\t"
        "ds_read_b128 v[200:203], %[xa] offset:1024\n\t"
        "ds_read_b128 v[204:207], %[xa] offset:21504\n\t"
        "ds_read_b128 v[208:211], %[xa] offset:2048\n\t"
        "ds_read_b128 v[212:215], %[xa] offset:22528\n\t"
        "v_mov_b32 v232, %[ca]\n\t"
        "v_mov_b32 v233, 1\n\t"
        "v_mov_b32 v234, %[fw]\n\t"
        "s_mov_b32 s88, m0\n\t"
        "s_mov_b32 m0, %[xd]\n\t"
        "s_add_u32 s89, %[xs], 0x1000\n\t"
        "buffer_load_dwordx4 %[xv], %[rs], %[xs] offen offset:0 sc1 lds\n\t"
        "buffer_load_dwordx4 %[xv], %[rs], %[xs] offen offset:1024 sc1 lds\n\t"
        "s_mov_b32 m0, s88\n\t"
        "s_waitcnt lgkmcnt(4)\n\t"
        "v_mfma_f32_16x16x32_bf16 v[176:179], v[16:19], v[192:195], 0\n\t"
        "ds_read_b128 v[216:219], %[xa] offset:3072\n\t"
        "v_mfma_f32_16x16x32_bf16 v[184:187], v[16:19], v[196:199], 0\n\t"
        "ds_read_b128 v[220:223], %[xa] offset:23552\n\t"
        "v_mfma_f32_16x16x32_bf16 v[180:183], v[96:99], v[192:195], 0\n\t"
        "v_mfma_f32_16x16x32_bf16 v[188:191], v[96:99], v[196:199], 0\n\t"
        "s_waitcnt lgkmcnt(4)\n\t"
        "v_mfma_f32_16x16x32_bf16 v[176:179], v[20:23], v[200:203], v[176:179]\n\t"
        "ds_read_b128 v[192:195], %[xa] offset:4096\n\t"
        "v_mfma_f32_16x16x32_bf16 v[184:187], v[20:23], v[204:207], v[184:187]\n\t"
        "ds_read_b128 v[196:199], %[xa] offset:24576\n\t"
        "v_mfma_f32_16x16x32_bf16 v[180:183], v[100:103], v[200:203], v[180:183]\n\t"
        "v_mfma_f32_16x16x32_bf16 v[188:191], v[100:103], v[204:207], v[188:191]\n\t"
        "s_waitcnt lgkmcnt(4)\n\t"
        "v_mfma_f32_16x16x32_bf16 v[176:179], v[24:27], v[208:211], v[176:179]\n\t"
        "ds_read_b128 v[200:203], %[xa] offset:5120\n\t"
        "v_mfma_f32_16x16x32_bf16 v[184:187], v[24:27], v[212:215], v[184:187]\n\t"
        "ds_read_b128 v[204:207], %[xa] offset:25600\n\t"
        "v_mfma_f32_16x16x32_bf16 v[180:183], v[104:107], v[208:211], v[180:183]\n\t"
        "v_mfma_f32_16x16x32_bf16 v[188:191], v[104:107], v[212:215], v[188:191]\n\t"
        "s_waitcnt lgkmcnt(4)\n\t"
        "v_mfma_f32_16x16x32_bf16 v[176:179], v[28:31], v[216:219], v[176:179]\n\t"
        "ds_read_b128 v[208:211], %[xa] offset:6144\n\t"
        "v_mfma_f32_16x16x32_bf16 v[184:187], v[28:31], v[220:223], v[184:187]\n\t"
        "ds_read_b128 v[212:215], %[xa] offset:26624\n\t"
        "v_mfma_f32_16x16x32_bf16 v[180:183], v[108:111], v[216:219], v[180:183]\n\t"
        "v_mfma_f32_16x16x32_bf16 v[188:191], v[108:111], v[220:223], v[188:191]\n\t"
        "s_waitcnt lgkmcnt(4)\n\t"
        "v_mfma_f32_16x16x32_bf16 v[176:179], v[32:35], v[192:195], v[176:179]\n\t"
        "ds_read_b128 v[216:219], %[xa] offset:7168\n\t"
        "v_mfma_f32_16x16x32_bf16 v[184:187], v[32:35], v[196:199], v[184:187]\n\t"
        "ds_read_b128 v[220:223], %[xa] offset:27648\n\t"
        "v_mfma_f32_16x16x32_bf16 v[180:183], v[112:115], v[192:195], v[180:183]\n\t"
        "v_mfma_f32_16x16x32_bf16 v[188:191], v[112:115], v[196:199], v[188:191]\n\t"
        "s_waitcnt lgkmcnt(4)\n\t"
        "v_mfma_f32_16x16x32_bf16 v[176:179], v[36:39], v[200:203], v[176:179]\n\t"
        "ds_read_b128 v[192:195], %[xa] offset:8192\n\t"
        "v_mfma_f32_16x16x32_bf16 v[184:187], v[36:39], v[204:207], v[184:187]\n\t"
        "ds_read_b128 v[196:199], %[xa] offset:28672\n\t"
        "v_mfma_f32_16x16x32_bf16 v[180:183], v[116:119], v[200:203], v[180:183]\n\t"
        "v_mfma_f32_16x16x32_bf16 v[188:191], v[116:119], v[204:207], v[188:191]\n\t"
        "s_waitcnt lgkmcnt(4)\n\t"
        "v_mfma_f32_16x16x32_bf16 v[176:179], v[40:43], v[208:211], v[176:179]\n\t"
        "ds_read_b128 v[200:203], %[xa] offset:9216\n\t"
        "v_mfma_f32_16x16x32_bf16 v[184:187], v[40:43], v[212:215], v[184:187]\n\t"
        "ds_read_b128 v[204:207], %[xa] offset:29696\n\t"
        "v_mfma_f32_16x16x32_bf16 v[180:183], v[120:123], v[208:211], v[180:183]\n\t"
        "s_waitcnt vmcnt(0)\n\t"
        "s_mov_b64 exec, 1\n\t"
        "ds_add_rtn_u32 v233, v232, v233\n\t"
        "s_mov_b64 exec, -1\n\t"
        "v_mfma_f32_16x16x32_bf16 v[188:191], v[120:123], v[212:215], v[188:191]\n\t"
        "s_waitcnt lgkmcnt(5)\n\t"
        "v_mfma_f32_16x16x32_bf16 v[176:179], v[44:47], v[216:219], v[176:179]\n\t"
        "ds_read_b128 v[208:211], %[xa] offset:10240\n\t"
        "v_mfma_f32_16x16x32_bf16 v[184:187], v[44:47], v[220:223], v[184:187]\n\t"
        "ds_read_b128 v[212:215], %[xa] offset:30720\n\t"
        "v_mfma_f32_16x16x32_bf16 v[180:183], v[124:127], v[216:219], v[180:183]\n\t"
        "v_mfma_f32_16x16x32_bf16 v[188:191], v[124:127], v[220:223], v[188:191]\n\t"
        "s_waitcnt lgkmcnt(5)\n\t"
        "v_mfma_f32_16x16x32_bf16 v[176:179], v[48:51], v[192:195], v[176:179]\n\t"
        "ds_read_b128 v[216:219], %[xa] offset:11264\n\t"
        "v_mfma_f32_16x16x32_bf16 v[184:187], v[48:51], v[196:199], v[184:187]\n\t"
        "ds_read_b128 v[220:223], %[xa] offset:31744\n\t"
        "v_mfma_f32_16x16x32_bf16 v[180:183], v[128:131], v[192:195], v[180:183]\n\t"
        "s_waitcnt lgkmcnt(4)\n\t"
        "s_mov_b64 exec, 1\n\t"
        "v_cmpx_eq_u32 vcc, %[fc], v233\n\t"
        "buffer_store_dword v234, off, %[rs], %[fo] sc1\n\t"
        "s_mov_b64 exec, -1\n\t"
        "v_mfma_f32_16x16x32_bf16 v[188:191], v[128:131], v[196:199], v[188:191]\n\t"
        "s_waitcnt lgkmcnt(5)\n\t"
        "v_mfma_f32_16x16x32_bf16 v[176:179], v[52:55], v[200:203], v[176:179]\n\t"
        "ds_read_b128 v[192:195], %[xa] offset:12288\n\t"
        "v_mfma_f32_16x16x32_bf16 v[184:187], v[52:55], v[204:207], v[184:187]\n\t"
        "ds_read_b128 v[196:199], %[xa] offset:32768\n\t"
        "v_mfma_f32_16x16x32_bf16 v[180:183], v[132:135], v[200:203], v[180:183]\n\t"
        "v_mfma_f32_16x16x32_bf16 v[188:191], v[132:135], v[204:207], v[188:191]\n\t"
        "s_waitcnt lgkmcnt(4)\n\t"
        "v_mfma_f32_16x16x32_bf16 v[176:179], v[56:59], v[208:211], v[176:179]\n\t"
        "ds_read_b128 v[200:203], %[xa] offset:13312\n\t"
        "v_mfma_f32_16x16x32_bf16 v[184:187], v[56:59], v[212:215], v[184:187]\n\t"
        "ds_read_b128 v[204:207], %[xa] offset:33792\n\t"
        "v_mfma_f32_16x16x32_bf16 v[180:183], v[136:139], v[208:211], v[180:183]\n\t"
        "v_mfma_f32_16x16x32_bf16 v[188:191], v[136:139], v[212:215], v[188:191]\n\t"
        "s_waitcnt lgkmcnt(4)\n\t"
        "v_mfma_f32_16x16x32_bf16 v[176:179], v[60:63], v[216:219], v[176:179]\n\t"
        "ds_read_b128 v[208:211], %[xa] offset:14336\n\t"
        "v_mfma_f32_16x16x32_bf16 v[184:187], v[60:63], v[220:223], v[184:187]\n\t"
        "ds_read_b128 v[212:215], %[xa] offset:34816\n\t"
        "v_mfma_f32_16x16x32_bf16 v[180:183], v[140:143], v[216:219], v[180:183]\n\t"
        "v_mfma_f32_16x16x32_bf16 v[188:191], v[140:143], v[220:223], v[188:191]\n\t"
        "s_waitcnt lgkmcnt(4)\n\t"
        "v_mfma_f32_16x16x32_bf16 v[176:179], v[64:67], v[192:195], v[176:179]\n\t"
        "ds_read_b128 v[216:219], %[xa] offset:15360\n\t"
        "v_mfma_f32_16x16x32_bf16 v[184:187], v[64:67], v[196:199], v[184:187]\n\t"
        "ds_read_b128 v[220:223], %[xa] offset:35840\n\t"
        "v_mfma_f32_16x16x32_bf16 v[180:183], v[144:147], v[192:195], v[180:183]\n\t"
        "v_mfma_f32_16x16x32_bf16 v[188:191], v[144:147], v[196:199], v[188:191]\n\t"
        "s_waitcnt lgkmcnt(4)\n\t"
        "v_mfma_f32_16x16x32_bf16 v[176:179], v[68:71], v[200:203], v[176:179]\n\t"
        "ds_read_b128 v[192:195], %[xa] offset:16384\n\t"
        "v_mfma_f32_16x16x32_bf16 v[184:187], v[68:71], v[204:207], v[184:187]\n\t"
        "ds_read_b128 v[196:199], %[xa] offset:36864\n\t"
        "v_mfma_f32_16x16x32_bf16 v[180:183], v[148:151], v[200:203], v[180:183]\n\t"
        "v_mfma_f32_16x16x32_bf16 v[188:191], v[148:151], v[204:207], v[188:191]\n\t"
        "s_waitcnt lgkmcnt(4)\n\t"
        "v_mfma_f32_16x16x32_bf16 v[176:179], v[72:75], v[208:211], v[176:179]\n\t"
        "ds_read_b128 v[200:203], %[xa] offset:17408\n\t"
        "v_mfma_f32_16x16x32_bf16 v[184:187], v[72:75], v[212:215], v[184:187]\n\t"
        "ds_read_b128 v[204:207], %[xa] offset:37888\n\t"
        "v_mfma_f32_16x16x32_bf16 v[180:183], v[152:155], v[208:211], v[180:183]\n\t"
        "v_mfma_f32_16x16x32_bf16 v[188:191], v[152:155], v[212:215], v[188:191]\n\t"
        "s_waitcnt lgkmcnt(4)\n\t"
        "v_mfma_f32_16x16x32_bf16 v[176:179], v[76:79], v[216:219], v[176:179]\n\t"
        "ds_read_b128 v[208:211], %[xa] offset:18432\n\t"
        "v_mfma_f32_16x16x32_bf16 v[184:187], v[76:79], v[220:223], v[184:187]\n\t"
        "ds_read_b128 v[212:215], %[xa] offset:38912\n\t"
        "v_mfma_f32_16x16x32_bf16 v[180:183], v[156:159], v[216:219], v[180:183]\n\t"
        "v_mfma_f32_16x16x32_bf16 v[188:191], v[156:159], v[220:223], v[188:191]\n\t"
        "s_waitcnt lgkmcnt(4)\n\t"
        "v_mfma_f32_16x16x32_bf16 v[176:179], v[80:83], v[192:195], v[176:179]\n\t"
        "ds_read_b128 v[216:219], %[xa] offset:19456\n\t"
        "v_mfma_f32_16x16x32_bf16 v[184:187], v[80:83], v[196:199], v[184:187]\n\t"
        "ds_read_b128 v[220:223], %[xa] offset:39936\n\t"
        "v_mfma_f32_16x16x32_bf16 v[180:183], v[160:163], v[192:195], v[180:183]\n\t"
        "v_mfma_f32_16x16x32_bf16 v[188:191], v[160:163], v[196:199], v[188:191]\n\t"
        "s_waitcnt lgkmcnt(4)\n\t"
        "v_mfma_f32_16x16x32_bf16 v[176:179], v[84:87], v[200:203], v[176:179]\n\t"
        "v_mfma_f32_16x16x32_bf16 v[184:187], v[84:87], v[204:207], v[184:187]\n\t"
        "v_mfma_f32_16x16x32_bf16 v[180:183], v[164:167], v[200:203], v[180:183]\n\t"
        "v_mfma_f32_16x16x32_bf16 v[188:191], v[164:167], v[204:207], v[188:191]\n\t"
        "s_waitcnt lgkmcnt(2)\n\t"
        "v_mfma_f32_16x16x32_bf16 v[176:179], v[88:91], v[208:211], v[176:179]\n\t"
        "v_mfma_f32_16x16x32_bf16 v[184:187], v[88:91], v[212:215], v[184:187]\n\t"
        "v_mfma_f32_16x16x32_bf16 v[180:183], v[168:171], v[208:211], v[180:183]\n\t"
        "v_mfma_f32_16x16x32_bf16 v[188:191], v[168:171], v[212:215], v[188:191]\n\t"
        "s_waitcnt lgkmcnt(0)\n\t"
        "v_mfma_f32_16x16x32_bf16 v[176:179], v[92:95], v[216:219], v[176:179]\n\t"
        "v_mfma_f32_16x16x32_bf16 v[184:187], v[92:95], v[220:223], v[184:187]\n\t"
        "v_mfma_f32_16x16x32_bf16 v[180:183], v[172:175], v[216:219], v[180:183]\n\t"
        "v_mfma_f32_16x16x32_bf16 v[188:191], v[172:175], v[220:223], v[188:191]\n\t"
        "s_nop 0\n\t"
        "s_barrier\n\t"
        "s_nop 4\n\t"
        "v_mul_f32 v176, %[sc], v176\n\t"
        "v_mul_f32 v177, %[sc], v177\n\t"
        "v_mul_f32 v178, %[sc], v178\n\t"
        "v_mul_f32 v179, %[sc], v179\n\t"
        "v_mul_f32 v180, %[sc], v180\n\t"
        "v_mul_f32 v181, %[sc], v181\n\t"
        "v_mul_f32 v182, %[sc], v182\n\t"
        "v_mul_f32 v183, %[sc], v183\n\t"
        "v_cvt_pk_bf16_f32 v224, v176, v177\n\t"
        "v_cvt_pk_bf16_f32 v225, v178, v179\n\t"
        "v_cvt_pk_bf16_f32 v226, v180, v181\n\t"
        "v_cvt_pk_bf16_f32 v227, v182, v183\n\t"
        "buffer_store_dwordx4 v[224:227], %[oa], %[rs], %[o0] offen sc1\n\t"
        "v_mul_f32 v184, %[sc], v184\n\t"
        "v_mul_f32 v185, %[sc], v185\n\t"
        "v_mul_f32 v186, %[sc], v186\n\t"
        "v_mul_f32 v187, %[sc], v187\n\t"
        "v_mul_f32 v188, %[sc], v188\n\t"
        "v_mul_f32 v189, %[sc], v189\n\t"
        "v_mul_f32 v190, %[sc], v190\n\t"
        "v_mul_f32 v191, %[sc], v191\n\t"
        "v_cvt_pk_bf16_f32 v228, v184, v185\n\t"
        "v_cvt_pk_bf16_f32 v229, v186, v187\n\t"
        "v_cvt_pk_bf16_f32 v230, v188, v189\n\t"
        "v_cvt_pk_bf16_f32 v231, v190, v191\n\t"
        "buffer_store_dwordx4 v[228:231], %[oa], %[rs], %[o1] offen sc1\n\t"
        "s_nop 1\n\t"
        :
        : [w0] "{v[16:47]}"(W[0]), [w1] "{v[48:79]}"(W[1]), [w2] "{v[80:111]}"(W[2]), [w3] "{v[112:143]}"(W[3]), [w4] "{v[144:175]}"(W[4]), [xa] "v"(xa), [oa] "v"(oa), [rs] "s"(rs), [o0] "s"(o0), [o1] "s"(o1), [sc] "s"(sc), [ca] "s"(ca), [fc] "s"(fc), [fw] "s"(fw), [fo] "s"(fo), [xv] "v"(xv), [xs] "s"(xs), [xd] "s"(xd)
        : "s88", "s89", "v176", "v177", "v178", "v179", "v180", "v181", "v182", "v183", "v184", "v185", "v186", "v187", "v188", "v189", "v190", "v191", "v192", "v193", "v194", "v195", "v196", "v197", "v198", "v199", "v200", "v201", "v202", "v203", "v204", "v205", "v206", "v207", "v208", "v209", "v210", "v211", "v212", "v213", "v214", "v215", "v216", "v217", "v218", "v219", "v220", "v221", "v222", "v223", "v224", "v225", "v226", "v227", "v228", "v229", "v230", "v231", "v232", "v233", "v234", "vcc", "scc", "memory");
}

__device__ __forceinline__ void wb(const u32x32 (&W)[5], u32 xa, u32 oa, u32x4 rs, u32 bl, u32 wp, u32 it, u32 xv, u32 xs, u32 xd, u32 ys, u32 yd, u32 ca, u32 fc, u32 fw, u32 fo STP) {
    asm volatile(
        "ds_read_b128 v[192:195], %[xa] offset:0\n\t"
        "ds_read_b128 v[196:199], %[xa] offset:1024\n\t"
        "ds_read_b128 v[200:203], %[xa] offset:2048\n\t"
        "ds_read_b128 v[204:207], %[xa] offset:3072\n\t"
        "ds_read_b128 v[208:211], %[xa] offset:4096\n\t"
        "ds_read_b128 v[212:215], %[xa] offset:5120\n\t"
        "ds_read_b128 v[216:219], %[xa] offset:6144\n\t"
        "ds_read_b128 v[220:223], %[xa] offset:7168\n\t"
        "v_mov_b32 v232, %[ca]\n\t"
        "v_mov_b32 v233, 1\n\t"
        "v_mov_b32 v234, %[fw]\n\t"
        "s_andn2_b32 s91, %[wp], 15\n\t"
        "s_mov_b32 s92, %[bl]\n\t"
        "s_mov_b32 s88, m0\n\t"
        "s_mov_b32 m0, %[xd]\n\t"
        "s_add_u32 s89, %[xs], 0x1000\n\t"
        "buffer_load_dwordx4 %[xv], %[rs], %[xs] offen offset:0 sc1 lds\n\t"
        "buffer_load_dwordx4 %[xv], %[rs], %[xs] offen offset:1024 sc1 lds\n\t"
        "s_mov_b32 m0, s88\n\t"
        "s_setprio 1\n\t"
        "s_waitcnt lgkmcnt(0)\n\t"
        "v_mfma_f32_16x16x32_bf16 v[176:179], v[192:195], v[16:19], 0\n\t"
        "s_lshr_b32 s95, s92, 20\n\t"
        "s_xnor_b32 s95, s95, %[wp]\n\t"
        "v_mfma_f32_16x16x32_bf16 v[180:183], v[208:211], v[16:19], 0\n\t"
        "s_and_b32 s95, s95, 1\n\t"
        "s_lshl_b32 s95, s95, 13\n\t"
        "v_mfma_f32_16x16x32_bf16 v[176:179], v[196:199], v[20:23], v[176:179]\n\t"
        "v_mfma_f32_16x16x32_bf16 v[180:183], v[212:215], v[20:23], v[180:183]\n\t"
        "v_mfma_f32_16x16x32_bf16 v[176:179], v[200:203], v[24:27], v[176:179]\n\t"
        "v_mfma_f32_16x16x32_bf16 v[180:183], v[216:219], v[24:27], v[180:183]\n\t"
        "v_mfma_f32_16x16x32_bf16 v[176:179], v[204:207], v[28:31], v[176:179]\n\t"
        "v_mfma_f32_16x16x32_bf16 v[180:183], v[220:223], v[28:31], v[180:183]\n\t"
        "s_lshl1_add_u32 s93, s92, s91\n\t"
        "s_add_u32 s93, s93, s95\n\t"
        "v_mfma_f32_16x16x32_bf16 v[184:187], v[192:195], v[96:99], 0\n\t"
        "s_add_u32 s92, s92, 0x40000\n\t"
        "v_mfma_f32_16x16x32_bf16 v[188:191], v[208:211], v[96:99], 0\n\t"
        "s_add_u32 s94, s93, 0x10000\n\t"
        "v_mfma_f32_16x16x32_bf16 v[184:187], v[196:199], v[100:103], v[184:187]\n\t"
        "v_cvt_pk_bf16_f32 v224, v176, v177\n\t"
        "v_mfma_f32_16x16x32_bf16 v[188:191], v[212:215], v[100:103], v[188:191]\n\t"
        "v_cvt_pk_bf16_f32 v225, v178, v179\n\t"
        "v_mfma_f32_16x16x32_bf16 v[184:187], v[200:203], v[104:107], v[184:187]\n\t"
        "v_cvt_pk_bf16_f32 v226, v180, v181\n\t"
        "v_mfma_f32_16x16x32_bf16 v[188:191], v[216:219], v[104:107], v[188:191]\n\t"
        "v_cvt_pk_bf16_f32 v227, v182, v183\n\t"
        "v_mfma_f32_16x16x32_bf16 v[184:187], v[204:207], v[108:111], v[184:187]\n\t"
        "buffer_store_dwordx4 v[224:227], %[oa], %[rs], s93 offen sc1\n\t"
        "v_mfma_f32_16x16x32_bf16 v[188:191], v[220:223], v[108:111], v[188:191]\n\t"
        "v_mfma_f32_16x16x32_bf16 v[176:179], v[192:195], v[32:35], 0\n\t"
        "s_lshr_b32 s95, s92, 20\n\t"
        "s_xnor_b32 s95, s95, %[wp]\n\t"
        "v_mfma_f32_16x16x32_bf16 v[180:183], v[208:211], v[32:35], 0\n\t"
        "s_and_b32 s95, s95, 1\n\t"
        "s_lshl_b32 s95, s95, 13\n\t"
        "v_mfma_f32_16x16x32_bf16 v[176:179], v[196:199], v[36:39], v[176:179]\n\t"
        "v_cvt_pk_bf16_f32 v228, v184, v185\n\t"
        "v_mfma_f32_16x16x32_bf16 v[180:183], v[212:215], v[36:39], v[180:183]\n\t"
        "v_cvt_pk_bf16_f32 v229, v186, v187\n\t"
        "v_mfma_f32_16x16x32_bf16 v[176:179], v[200:203], v[40:43], v[176:179]\n\t"
        "v_cvt_pk_bf16_f32 v230, v188, v189\n\t"
        "v_mfma_f32_16x16x32_bf16 v[180:183], v[216:219], v[40:43], v[180:183]\n\t"
        "v_cvt_pk_bf16_f32 v231, v190, v191\n\t"
        "v_mfma_f32_16x16x32_bf16 v[176:179], v[204:207], v[44:47], v[176:179]\n\t"
        "buffer_store_dwordx4 v[228:231], %[oa], %[rs], s94 offen sc1\n\t"
        "v_mfma_f32_16x16x32_bf16 v[180:183], v[220:223], v[44:47], v[180:183]\n\t"
        "s_lshl1_add_u32 s93, s92, s91\n\t"
        "s_add_u32 s93, s93, s95\n\t"
        "v_mfma_f32_16x16x32_bf16 v[184:187], v[192:195], v[112:115], 0\n\t"
        "s_add_u32 s92, s92, 0x40000\n\t"
        "v_mfma_f32_16x16x32_bf16 v[188:191], v[208:211], v[112:115], 0\n\t"
        "s_add_u32 s94, s93, 0x10000\n\t"
        "v_mfma_f32_16x16x32_bf16 v[184:187], v[196:199], v[116:119], v[184:187]\n\t"
        "v_cvt_pk_bf16_f32 v224, v176, v177\n\t"
        "v_mfma_f32_16x16x32_bf16 v[188:191], v[212:215], v[116:119], v[188:191]\n\t"
        "v_cvt_pk_bf16_f32 v225, v178, v179\n\t"
        "v_mfma_f32_16x16x32_bf16 v[184:187], v[200:203], v[120:123], v[184:187]\n\t"
        "v_cvt_pk_bf16_f32 v226, v180, v181\n\t"
        "v_mfma_f32_16x16x32_bf16 v[188:191], v[216:219], v[120:123], v[188:191]\n\t"
        "v_cvt_pk_bf16_f32 v227, v182, v183\n\t"
        "v_mfma_f32_16x16x32_bf16 v[184:187], v[204:207], v[124:127], v[184:187]\n\t"
        "buffer_store_dwordx4 v[224:227], %[oa], %[rs], s93 offen sc1\n\t"
        "v_mfma_f32_16x16x32_bf16 v[188:191], v[220:223], v[124:127], v[188:191]\n\t"
        "s_waitcnt vmcnt(5)\n\t"
        "s_mov_b64 exec, 1\n\t"
        "ds_add_rtn_u32 v233, v232, v233\n\t"
        "s_mov_b64 exec, -1\n\t"
        "v_mfma_f32_16x16x32_bf16 v[176:179], v[192:195], v[48:51], 0\n\t"
        "s_lshr_b32 s95, s92, 20\n\t"
        "s_xnor_b32 s95, s95, %[wp]\n\t"
        "v_mfma_f32_16x16x32_bf16 v[180:183], v[208:211], v[48:51], 0\n\t"
        "s_and_b32 s95, s95, 1\n\t"
        "s_lshl_b32 s95, s95, 13\n\t"
        "v_mfma_f32_16x16x32_bf16 v[176:179], v[196:199], v[52:55], v[176:179]\n\t"
        "v_cvt_pk_bf16_f32 v228, v184, v185\n\t"
        "v_mfma_f32_16x16x32_bf16 v[180:183], v[212:215], v[52:55], v[180:183]\n\t"
        "v_cvt_pk_bf16_f32 v229, v186, v187\n\t"
        "v_mfma_f32_16x16x32_bf16 v[176:179], v[200:203], v[56:59], v[176:179]\n\t"
        "v_cvt_pk_bf16_f32 v230, v188, v189\n\t"
        "v_mfma_f32_16x16x32_bf16 v[180:183], v[216:219], v[56:59], v[180:183]\n\t"
        "v_cvt_pk_bf16_f32 v231, v190, v191\n\t"
        "v_mfma_f32_16x16x32_bf16 v[176:179], v[204:207], v[60:63], v[176:179]\n\t"
        "buffer_store_dwordx4 v[228:231], %[oa], %[rs], s94 offen sc1\n\t"
        "v_mfma_f32_16x16x32_bf16 v[180:183], v[220:223], v[60:63], v[180:183]\n\t"
        "s_lshl1_add_u32 s93, s92, s91\n\t"
        "s_add_u32 s93, s93, s95\n\t"
        "v_mfma_f32_16x16x32_bf16 v[184:187], v[192:195], v[128:131], 0\n\t"
        "s_add_u32 s92, s92, 0x40000\n\t"
        "v_mfma_f32_16x16x32_bf16 v[188:191], v[208:211], v[128:131], 0\n\t"
        "s_add_u32 s94, s93, 0x10000\n\t"
        "v_mfma_f32_16x16x32_bf16 v[184:187], v[196:199], v[132:135], v[184:187]\n\t"
        "v_cvt_pk_bf16_f32 v224, v176, v177\n\t"
        "v_mfma_f32_16x16x32_bf16 v[188:191], v[212:215], v[132:135], v[188:191]\n\t"
        "v_cvt_pk_bf16_f32 v225, v178, v179\n\t"
        "v_mfma_f32_16x16x32_bf16 v[184:187], v[200:203], v[136:139], v[184:187]\n\t"
        "v_cvt_pk_bf16_f32 v226, v180, v181\n\t"
        "v_mfma_f32_16x16x32_bf16 v[188:191], v[216:219], v[136:139], v[188:191]\n\t"
        "v_cvt_pk_bf16_f32 v227, v182, v183\n\t"
        "v_mfma_f32_16x16x32_bf16 v[184:187], v[204:207], v[140:143], v[184:187]\n\t"
        "buffer_store_dwordx4 v[224:227], %[oa], %[rs], s93 offen sc1\n\t"
        "v_mfma_f32_16x16x32_bf16 v[188:191], v[220:223], v[140:143], v[188:191]\n\t"
        "s_waitcnt lgkmcnt(0)\n\t"
        "s_mov_b64 exec, 1\n\t"
        "v_cmpx_eq_u32 vcc, %[fc], v233\n\t"
        "buffer_store_dword v234, off, %[rs], %[fo] sc1\n\t"
        "s_mov_b64 exec, -1\n\t"
        "s_cmp_le_u32 %[it], %[nowv]\n\t"
        "s_cbranch_scc1 Lp%=\n\t"
        "s_setprio 0\n\t"
        "Lp%=:\n\t"
        "s_mov_b32 s88, m0\n\t"
        "s_mov_b32 m0, %[yd]\n\t"
        "s_add_u32 s89, %[ys], 0x1000\n\t"
        "buffer_load_dwordx4 %[xv], %[rs], %[ys] offen offset:0 sc1 lds\n\t"
        "buffer_load_dwordx4 %[xv], %[rs], %[ys] offen offset:1024 sc1 lds\n\t"
        "buffer_load_dwordx4 %[xv], %[rs], %[ys] offen offset:2048 sc1 lds\n\t"
        "buffer_load_dwordx4 %[xv], %[rs], %[ys] offen offset:3072 sc1 lds\n\t"
        "s_add_u32 m0, m0, 0x1000\n\t"
        "s_nop 0\n\t"
        "buffer_load_dwordx4 %[xv], %[rs], s89 offen offset:0 sc1 lds\n\t"
        "buffer_load_dwordx4 %[xv], %[rs], s89 offen offset:1024 sc1 lds\n\t"
        "buffer_load_dwordx4 %[xv], %[rs], s89 offen offset:2048 sc1 lds\n\t"
        "buffer_load_dwordx4 %[xv], %[rs], s89 offen offset:3072 sc1 lds\n\t"
        "s_mov_b32 m0, s88\n\t"
        "v_mfma_f32_16x16x32_bf16 v[176:179], v[192:195], v[64:67], 0\n\t"
        "s_lshr_b32 s95, s92, 20\n\t"
        "s_xnor_b32 s95, s95, %[wp]\n\t"
        "v_mfma_f32_16x16x32_bf16 v[180:183], v[208:211], v[64:67], 0\n\t"
        "s_and_b32 s95, s95, 1\n\t"
        "s_lshl_b32 s95, s95, 13\n\t"
        "v_mfma_f32_16x16x32_bf16 v[176:179], v[196:199], v[68:71], v[176:179]\n\t"
        "v_cvt_pk_bf16_f32 v228, v184, v185\n\t"
        "v_mfma_f32_16x16x32_bf16 v[180:183], v[212:215], v[68:71], v[180:183]\n\t"
        "v_cvt_pk_bf16_f32 v229, v186, v187\n\t"
        "v_mfma_f32_16x16x32_bf16 v[176:179], v[200:203], v[72:75], v[176:179]\n\t"
        "v_cvt_pk_bf16_f32 v230, v188, v189\n\t"
        "v_mfma_f32_16x16x32_bf16 v[180:183], v[216:219], v[72:75], v[180:183]\n\t"
        "v_cvt_pk_bf16_f32 v231, v190, v191\n\t"
        "v_mfma_f32_16x16x32_bf16 v[176:179], v[204:207], v[76:79], v[176:179]\n\t"
        "buffer_store_dwordx4 v[228:231], %[oa], %[rs], s94 offen sc1\n\t"
        "v_mfma_f32_16x16x32_bf16 v[180:183], v[220:223], v[76:79], v[180:183]\n\t"
        "s_lshl1_add_u32 s93, s92, s91\n\t"
        "s_add_u32 s93, s93, s95\n\t"
        "v_mfma_f32_16x16x32_bf16 v[184:187], v[192:195], v[144:147], 0\n\t"
        "s_add_u32 s92, s92, 0x40000\n\t"
        "v_mfma_f32_16x16x32_bf16 v[188:191], v[208:211], v[144:147], 0\n\t"
        "s_add_u32 s94, s93, 0x10000\n\t"
        "v_mfma_f32_16x16x32_bf16 v[184:187], v[196:199], v[148:151], v[184:187]\n\t"
        "v_cvt_pk_bf16_f32 v224, v176, v177\n\t"
        "v_mfma_f32_16x16x32_bf16 v[188:191], v[212:215], v[148:151], v[188:191]\n\t"
        "v_cvt_pk_bf16_f32 v225, v178, v179\n\t"
        "v_mfma_f32_16x16x32_bf16 v[184:187], v[200:203], v[152:155], v[184:187]\n\t"
        "v_cvt_pk_bf16_f32 v226, v180, v181\n\t"
        "v_mfma_f32_16x16x32_bf16 v[188:191], v[216:219], v[152:155], v[188:191]\n\t"
        "v_cvt_pk_bf16_f32 v227, v182, v183\n\t"
        "v_mfma_f32_16x16x32_bf16 v[184:187], v[204:207], v[156:159], v[184:187]\n\t"
        "buffer_store_dwordx4 v[224:227], %[oa], %[rs], s93 offen sc1\n\t"
        "v_mfma_f32_16x16x32_bf16 v[188:191], v[220:223], v[156:159], v[188:191]\n\t"
        "v_mfma_f32_16x16x32_bf16 v[176:179], v[192:195], v[80:83], 0\n\t"
        "s_lshr_b32 s95, s92, 20\n\t"
        "s_xnor_b32 s95, s95, %[wp]\n\t"
        "v_mfma_f32_16x16x32_bf16 v[180:183], v[208:211], v[80:83], 0\n\t"
        "s_and_b32 s95, s95, 1\n\t"
        "s_lshl_b32 s95, s95, 13\n\t"
        "v_mfma_f32_16x16x32_bf16 v[176:179], v[196:199], v[84:87], v[176:179]\n\t"
        "v_cvt_pk_bf16_f32 v228, v184, v185\n\t"
        "v_mfma_f32_16x16x32_bf16 v[180:183], v[212:215], v[84:87], v[180:183]\n\t"
        "v_cvt_pk_bf16_f32 v229, v186, v187\n\t"
        "v_mfma_f32_16x16x32_bf16 v[176:179], v[200:203], v[88:91], v[176:179]\n\t"
        "v_cvt_pk_bf16_f32 v230, v188, v189\n\t"
        "v_mfma_f32_16x16x32_bf16 v[180:183], v[216:219], v[88:91], v[180:183]\n\t"
        "v_cvt_pk_bf16_f32 v231, v190, v191\n\t"
        "v_mfma_f32_16x16x32_bf16 v[176:179], v[204:207], v[92:95], v[176:179]\n\t"
        "buffer_store_dwordx4 v[228:231], %[oa], %[rs], s94 offen sc1\n\t"
        "v_mfma_f32_16x16x32_bf16 v[180:183], v[220:223], v[92:95], v[180:183]\n\t"
        "s_lshl1_add_u32 s93, s92, s91\n\t"
        "s_add_u32 s93, s93, s95\n\t"
        "v_mfma_f32_16x16x32_bf16 v[184:187], v[192:195], v[160:163], 0\n\t"
        "s_add_u32 s92, s92, 0x40000\n\t"
        "v_mfma_f32_16x16x32_bf16 v[188:191], v[208:211], v[160:163], 0\n\t"
        "s_add_u32 s94, s93, 0x10000\n\t"
        "v_mfma_f32_16x16x32_bf16 v[184:187], v[196:199], v[164:167], v[184:187]\n\t"
        "v_cvt_pk_bf16_f32 v224, v176, v177\n\t"
        "v_mfma_f32_16x16x32_bf16 v[188:191], v[212:215], v[164:167], v[188:191]\n\t"
        "v_cvt_pk_bf16_f32 v225, v178, v179\n\t"
        "v_mfma_f32_16x16x32_bf16 v[184:187], v[200:203], v[168:171], v[184:187]\n\t"
        "v_cvt_pk_bf16_f32 v226, v180, v181\n\t"
        "v_mfma_f32_16x16x32_bf16 v[188:191], v[216:219], v[168:171], v[188:191]\n\t"
        "v_cvt_pk_bf16_f32 v227, v182, v183\n\t"
        "v_mfma_f32_16x16x32_bf16 v[184:187], v[204:207], v[172:175], v[184:187]\n\t"
        "buffer_store_dwordx4 v[224:227], %[oa], %[rs], s93 offen sc1\n\t"
        "v_mfma_f32_16x16x32_bf16 v[188:191], v[220:223], v[172:175], v[188:191]\n\t"
        "s_and_b32 s95, %[it], 31\n\t"
        "s_cmp_le_u32 s95, %[nowv]\n\t"
        "s_cbranch_scc1 Lnb%=\n\t"
        "s_waitcnt vmcnt(17)\n\t"
        "s_barrier\n\t"
        "Lnb%=:\n\t"
        "s_setprio 0\n\t"
        "s_nop 3\n\t"
        "v_cvt_pk_bf16_f32 v228, v184, v185\n\t"
        "v_cvt_pk_bf16_f32 v229, v186, v187\n\t"
        "v_cvt_pk_bf16_f32 v230, v188, v189\n\t"
        "v_cvt_pk_bf16_f32 v231, v190, v191\n\t"
        "buffer_store_dwordx4 v[228:231], %[oa], %[rs], s94 offen sc1\n\t"
        "s_nop 1\n\t"
        :
        : [w0] "{v[16:47]}"(W[0]), [w1] "{v[48:79]}"(W[1]), [w2] "{v[80:111]}"(W[2]), [w3] "{v[112:143]}"(W[3]), [w4] "{v[144:175]}"(W[4]), [xa] "v"(xa), [oa] "v"(oa), [rs] "s"(rs), [bl] "s"(bl), [wp] "s"(wp), [it] "s"(it), [nowv] "n"(NOWV), [ca] "s"(ca), [fc] "s"(fc), [fw] "s"(fw), [fo] "s"(fo), [xv] "v"(xv), [xs] "s"(xs), [xd] "s"(xd), [ys] "s"(ys), [yd] "s"(yd)
        : "s88", "s89", "s91", "s92", "s93", "s94", "s95", "v176", "v177", "v178", "v179", "v180", "v181", "v182", "v183", "v184", "v185", "v186", "v187", "v188", "v189", "v190", "v191", "v192", "v193", "v194", "v195", "v196", "v197", "v198", "v199", "v200", "v201", "v202", "v203", "v204", "v205", "v206", "v207", "v208", "v209", "v210", "v211", "v212", "v213", "v214", "v215", "v216", "v217", "v218", "v219", "v220", "v221", "v222", "v223", "v224", "v225", "v226", "v227", "v228", "v229", "v230", "v231", "v232", "v233", "v234", "vcc", "scc", "memory");
}

#define TP0 186
#define TP1 18
#define TP2 184
#define TP3 30
#define TP4 150
#define TP5 130
#define TP6 104
#define TP7 240
#define TP8 8
#define TP9 14
#define TP10 3
#define TP11 2
#define TP12 1
#define TP13 150
#define TP14 110
#define TP15 4
#define WAHEAD 3
#define FILLG 4
#define WFIRST 2
#define ROT xg
#define SLOW_RT 620u
#define WLATE 600u
#define EARLY_N 35
#define BUMP_N 3
#define BUMP_WIN 0xffu
#define BUMP_S0 8
#define BUMP_MAX 3
#define TV(idx) ((u32)(TP##idx) * 8u)
#define TLOAD() do { } while (0)

__device__ __forceinline__ void nap(u32 n) {
    u32 hi = n >> 6, lo = (n >> 4) & 3u;
    asm volatile("s_cmp_eq_u32 %0, 0\n\ts_cbranch_scc1 Ln%=\nLm%=:\n\ts_sleep 1\n\ts_sub_u32 %0, %0, 1\n\ts_cmp_lg_u32 %0, 0\n\ts_cbranch_scc1 Lm%=\nLn%=:\n\t"
                 "s_cmp_eq_u32 %1, 0\n\ts_cbranch_scc1 Lp%=\nLo%=:\n\ts_sub_u32 %1, %1, 1\n\ts_cmp_lg_u32 %1, 0\n\ts_cbranch_scc1 Lo%=\nLp%=:"
                 : "+s"(hi), "+s"(lo) :: "scc", "memory");
}
typedef __attribute__((address_space(3))) u32* lds_u32p;
typedef __attribute__((address_space(3))) volatile u32* lds_vu32p;
typedef __attribute__((address_space(3))) u16* lds_u16p;
typedef __attribute__((address_space(3))) u32x2* lds_u32x2p;
typedef __attribute__((address_space(3))) u32x4* lds_u32x4p;
__device__ __forceinline__ u32 rfl(u32 x) { return __builtin_amdgcn_readfirstlane(x); }
__device__ __forceinline__ bool has_sent(u32x4 v) { return (v[0] == SENT) | (v[1] == SENT) | (v[2] == SENT) | (v[3] == SENT); }
__device__ __forceinline__ u32 now32() { return (u32)__builtin_amdgcn_s_memtime(); }
__device__ __forceinline__ u32 step_tag(int s) { return (u32)(s + 1) * 0x10001u; }

#define UNI(a, b, c, d, e, f, g, h, i, j) do { } while (0)
template <int S0 = 0>
__device__ __forceinline__ u32 unless_neg(u32 v, int s) {
    u32 r;
    asm("s_cmp_lt_i32 %2, %3\n\ts_cselect_b32 %0, 0, %1" : "=s"(r) : "s"(v), "s"(s), "n"(S0) : "scc");
    return r;
}
template <int S0 = 0>
__device__ __forceinline__ u32x4 live(u32x4 srd, int s) { srd[2] = unless_neg<S0>(srd[2], s); return srd; }
__device__ __forceinline__ float shx(float v, int m, int lane) { return __int_as_float(__builtin_amdgcn_ds_bpermute((lane ^ m) << 2, __float_as_int(v))); }
__device__ __forceinline__ void mem0_image(const u16* __restrict__ mem0, char* smem, const int tid) {
    for (int c = tid; c < 640; c += 512) {
        u32x4 v = *(const u32x4*)(mem0 + (c & 7) * D_ + (c >> 3) * 8);
#pragma unroll
        for (int i = 0; i < 4; ++i) v[i] = v[i] == SENT ? ~0u : v[i];
        ((u32x4*)smem)[c + (c >= 576 ? 64 : 0)] = v;
    }
}
__device__ __forceinline__ u32 once_more(u32& left, u32 lbase, u32* errw) {
    if (__builtin_expect(left != 0, 1)) { --left; return 2u; }
    atomicOr(errw, 1u);
    const int lane = lane_id();
    for (int k = 0; k < 11; ++k) *(lds_u32x4p)(lbase + (u32)(k * 1024 + lane * 16)) = (u32x4){0u, 0u, 0u, 0u};
    asm volatile("s_waitcnt lgkmcnt(0)" ::: "memory");
    return 0u;
}
constexpr u32 TS_MISS = 0x10000u;
__device__ __forceinline__ u32 read_ts(rsrc_t rs, u32 off, int s, u32& m, u32* t2 = nullptr) {
    const int lane = lane_id();
    const u32 vo = lane < 8 ? (u32)lane * 128u : OOB, base = (u32)__builtin_amdgcn_s_memrealtime() * 24u - 0x100000u;
    u32x4 t;
#pragma unroll 1
    for (u32 spins = 1;; ++spins) {
        CFENCE();
        t = ldx4(rs, vo, off);
        m = (u32)__ballot(t[3] == (u32)s + 1u);
        if (((m | (m >> 4)) & 15u) == 15u) break;
        m = 0;
        if (spins >= 0x4000u) break;
        __builtin_amdgcn_s_sleep(4);
    }
    u32 v = t[3] == (u32)s + 1u ? t[1] - base : 0u;
    v = max(v, (u32)__builtin_amdgcn_update_dpp(0u, v, 0xb1, 0xf, 0xf, false));
    v = max(v, (u32)__builtin_amdgcn_update_dpp(0u, v, 0x4e, 0xf, 0xf, false));
    v = max(v, (u32)__builtin_amdgcn_update_dpp(0u, v, 0x141, 0xf, 0xf, false));
    if (t2) {
        u32 w = t[3] == (u32)s + 1u ? t[2] : 0u;
        w = max(w, (u32)__builtin_amdgcn_update_dpp(0u, w, 0xb1, 0xf, 0xf, false));
        w = max(w, (u32)__builtin_amdgcn_update_dpp(0u, w, 0x4e, 0xf, 0xf, false));
        w = max(w, (u32)__builtin_amdgcn_update_dpp(0u, w, 0x141, 0xf, 0xf, false));
        *t2 = to_sgpr(w);
    }
    return to_sgpr(v + base);
}
__device__ __forceinline__ u32 read_ts_late(rsrc_t rs, u32x4 srd, u32 off, int s, u32& m, u32& t2, u32 gap = 1024) {
    u32 t;
    ts_look(t, t2, m, srd, off, (u32)s + 1u);
    if (m) return t;
    nap(gap);
    return read_ts(rs, off, s, m, &t2);
}
__device__ __forceinline__ void until(u32 t) { for (int q = 0; q < 256 && (int)(t - (u32)__builtin_amdgcn_s_memrealtime() * 24u) > 0; ++q) __builtin_amdgcn_s_sleep(1); }
__device__ __forceinline__ u32 umin3(u32 a, u32 b, u32 c) { return min(min(a, b), c); }
__device__ __forceinline__ u32 dist(u32 d, u32x4 v, u32 k) { return umin3(umin3(d, v[0] ^ k, v[1] ^ k), v[2] ^ k, v[3] ^ k); }
#define COUNT(i) do { } while (0)
#define COUNT0() do { } while (0)
#define COUNT1(i) do { } while (0)
__device__ __forceinline__ void touch(u32x4 srd, u32 wp, int s, u32 lds) {
    u32 sv;
    const u32 off = slot_off(wp, s) + SL_TS + 2048u + ((~((wp >> 3) ^ ((u32)s >> 5)) & 1u) << 18);
    asm volatile("s_mov_b32 %0, m0\n\ts_mov_b32 m0, %3\n\ts_nop 3\n\tbuffer_load_dword off, %1, %2 lds\n\ts_mov_b32 m0, %0" : "=&s"(sv) : "s"(srd), "s"(off), "s"(lds) : "memory");
}
__device__ __forceinline__ void track_rate(u32 c0, u32 r0, u32& pc, u32& pr, u32& kr) {
    const u32 dr = r0 - pr;
    if (dr >= 6000u) {
        if (dr < 40000u) kr = to_sgpr(min(max((u32)(4096.f * (float)(c0 - pc) * __builtin_amdgcn_rcpf((float)dr) + 0.5f), 2560u), 4352u));
        pc = c0; pr = r0;
    }
}

#define LANES() const int lane = lane_id(), n = lane & 15, g = lane >> 4; (void)n; (void)g
constexpr u32 L_Q = 12288, L_K = 18432, L_E = 24576, L_DR = 25088, L_MSK = 25600, L_KQ = 32768;
constexpr float LIM_X = 32.f, LIM_D = 1.0995116e12f;
__device__ __forceinline__ u32 kq_buf(int sg) { return L_KQ + (u32)(sg % 3) * 16384u; }

__device__ __forceinline__ u32 p_pos(int row, int g, int dg) { return (u32)((row >> 1) * 64 + (g & 1) * 32 + (row & 1) * 16 + dg * 8 + (g >> 1) * 4); }
__device__ __forceinline__ void s_slow(const int h, const int s, rsrc_t rs, u32 so, u32 lbase) {
    const int lane = lane_id(), row = lane & 7, j = lane >> 3;
    const float ce = 0.08838834764831845f * 1.4426950408889634f;
    float sc[5] = {0.f, 0.f, 0.f, 0.f, 0.f};
    const u32 kx = lbase + kq_buf(s) + (u32)((j >> 2) * 4096 + (j & 3) * 64);
#pragma unroll 1
    for (int e = 0; e < DH_; ++e) {
        const u32 qa = lbase + L_Q + (u32)((e >> 4) * 768 + (row >> 2) * 192 + (e & 15) * 8 + (row & 3) * 2);
        const float q = bf2f(*(lds_u16p)qa);
        const u32 ka = kx + (u32)((e >> 5) * 1024 + ((e >> 2) & 3) * 256 + ((e >> 4) & 1) * 8 + (e & 3) * 2);
#pragma unroll
        for (int i = 0; i < 4; ++i) sc[i] += q * bf2f(*(lds_u16p)(ka + i * 16));
        sc[4] += q * bf2f(*(lds_u16p)(lbase + L_K + (u32)((e >> 4) * 768 + (j >> 1) * 192 + (e & 15) * 8 + (j & 1) * 2)));
    }
    sc[4] *= ce;
    const u32x2 ev = *(lds_u32x2p)(lbase + L_E + (u32)((j >> 1) * 128 + row * 16 + (j & 1) * 8));
    float se = (bf2f((u16)ev[0]) + bf2f((u16)(ev[0] >> 16))) + (bf2f((u16)ev[1]) + bf2f((u16)(ev[1] >> 16)));
    float mx = fmaxf(fmaxf(fmaxf(sc[0], sc[1]), fmaxf(sc[2], sc[3])), sc[4]);
#pragma unroll
    for (int o = 8; o < 64; o <<= 1) { se += shx(se, o, lane); mx = fmaxf(mx, shx(mx, o, lane)); }
    const bool rew = ev[0] == ~0u || ev[1] == ~0u;
    u64 rw = __ballot(rew);
    rw |= rw >> 32; rw |= rw >> 16; rw |= rw >> 8;
    const bool again = (rw >> row) & 1u;
    float al = exp2f(-mx);
    if (again) {
        const u32x2 en = {pack2(exp2f(sc[0] - mx), exp2f(sc[1] - mx)), pack2(exp2f(sc[2] - mx), exp2f(sc[3] - mx))};
        se = (bf2f((u16)en[0]) + bf2f((u16)(en[0] >> 16))) + (bf2f((u16)en[1]) + bf2f((u16)(en[1] >> 16)));
        al = 1.f;
    }
    float sn = se;
#pragma unroll
    for (int o = 8; o < 64; o <<= 1) sn += shx(sn, o, lane);
    if (again) se = sn;
    const float em = exp2f(sc[4] - mx);
    float sm = em;
#pragma unroll
    for (int o = 8; o < 64; o <<= 1) sm += shx(sm, o, lane);
    const float rd = 1.f / (sm + al * se);
    if (rw & 255u) {
#pragma unroll 1
        for (u32 spins = 0; spins < SPIN_CAP && !__all(*(lds_vu32p)(lbase + L_DR + (u32)(lane & 1) * 4) == step_tag(s)); ++spins) __builtin_amdgcn_s_sleep(1);
        const u32x2 en = {pack2(exp2f(sc[0] - mx), exp2f(sc[1] - mx)), pack2(exp2f(sc[2] - mx), exp2f(sc[3] - mx))};
        const u32 eo = again ? (u32)((h >> 3) * 32768 + ((h >> 1) & 3) * 1024 + (j >> 1) * 256 + (h & 1) * 128 + row * 16 + (j & 1) * 8) : OOB;
#pragma unroll
        for (int c = 0; c < NCP; ++c) stx2(rs, eo, en, so + cpy(c));
    }
    DRAIN();
    const u32 pm = pack2(em * rd, shx(em * rd, 8, lane));
    const int g = j >> 1;
    u32 dg = pack2(row == 2 * g ? al * rd : 0.f, row == 2 * g + 1 ? al * rd : 0.f);
    if (again && (row >> 1) == (g ^ 1)) dg = MARK;
    const int gj = h >> 2;
    const u32 po = SL_P + (u32)((gj >> 2) * 32768 + (gj & 1) * 2048 + ((gj >> 1) & 1) * 1024 + (h & 3) * 256) + p_pos(row, g, (j ^ ~h) & 1);
    const bool done = ldx1(rs, po, so) != SENT;
#pragma unroll
    for (int c = 0; c < NCP; ++c) stx1(rs, done ? OOB : po, (j & 1) ? dg : pm, so + cpy(c));
}

__device__ __forceinline__ void s_role(const int wave, const int h, const int xg, const u64 tag, const u16* __restrict__ mem0, const u32x32 (&W)[5],
                                       char* __restrict__ ws, char* smem) {
    const int lane = lane_id(), tid = wave * 64 + lane, n = lane & 15, g = lane >> 4;
    u32* errw = (u32*)(ws + OFF_ERR);
    const rsrc_t rs = mk_rsrc(ws);
    const u32x4 srd = mk_srd(ws);
    const u32 lbase = (u32)(uintptr_t)(__attribute__((address_space(3))) char*)smem;
    TLOAD();
    int wslot = 0; const int wid = 128 + h;
    WSTAMP(2);
    mem0_image(mem0, smem, tid);
    if (tid < 448) *(u32x4*)(smem + L_K + tid * 16) = (u32x4){0u, 0u, 0u, 0u};
    const u32 wp = wait_init(rs, tag, 1);
    if (wave == 2) XSTAMP(wid, 224);
    const u32 sidx = (n & 2) ? 0xEEEE : 0x4444;
    const u32 va = lbase + (u32)(g * 256 + (n & 2) * 64 + (n >> 2) * 32 + (n & 1) * 16);
    const u32 ow = lbase + (u32)((wave & 3) * 768 + n * 8) + (wave >= 4 ? L_Q + (u32)((g >> 1) * 192 + (g & 1) * 4) : L_K + (u32)g * 192);
    u32 rnd = 0;
    const float ce = 0.08838834764831845f * 1.4426950408889634f;
    STAMPS();
    COUNT0();
#define BAR0() asm volatile("s_waitcnt vmcnt(0) lgkmcnt(0)\n\ts_barrier" ::: "memory")
#define Q_LAST() do { u32 redo; do s_l(rnd, redo, lbase); while (__builtin_expect(redo, 0)); } while (0)

    if ((wave >> 1) == 2) {
        LANES();
        const int kt = wave & 1;
        const u32 kxa = lbase + (u32)(kt * 4096 + lane * 16), qtr = lbase + L_Q + (u32)((lane & 1) * 192 + (lane >> 2) * 8);
        const u32 ep = (u32)((n & 7) * 16 + (g & 1) * 8 + (n >> 3) * 4);
        const u32 eo = (u32)((h >> 3) * 32768 + ((h >> 1) & 3) * 1024 + (2 * kt + (g >> 1)) * 256 + (h & 1) * 128) + ep;
        const u32 ew = lbase + L_E + (u32)((2 * kt + (g >> 1)) * 128) + ep;
        const u32 ts = slot_v(wp, lane) + put_off(wp, 2u, lane), tk = kq_buf(lane);
#pragma unroll 1
        for (int s = -1; s + 1 < NSEG; ++s) {
            s_e(W, rnd, va, sidx, ow, lbase, kxa + tab(tk, s & 63), qtr, (u32)kt * 768u, eo, ew, LIM_X, live(srd, s), tab(ts, s & 63) STA);
            DRAIN();
            *(lds_u32p)(lbase + L_DR + (u32)kt * 4) = step_tag(s);
            FLUSH();
        }
        BAR0();
        Q_LAST();
    } else if (wave == 6) {
        LANES();
        const int gj = h >> 2;
        const u32 po = SL_P + (u32)((gj >> 2) * 32768 + (gj & 1) * 2048 + ((gj >> 1) & 1) * 1024 + (h & 3) * 256) + p_pos(n & 7, g, ((n >> 3) ^ ~h) & 1);
        const u32 er = lbase + L_E + (u32)(g * 128 + (n & 7) * 16);
        const u32 qtr = lbase + L_Q + (u32)((lane & 1) * 192 + (lane >> 2) * 8), ktr = lbase + L_K + (u32)((lane & 3) * 192 + (lane >> 2) * 8);
        const u32x4 ones = {0x3f803f80u, 0x3f803f80u, 0x3f803f80u, 0x3f803f80u};
        const u32 ts = slot_v(wp, lane) + put_off(wp, 2u, lane);
#pragma unroll 1
        for (int s = -1; s + 1 < NSEG; ++s) {
            const u32 so = tab(ts, s & 63);
            u32 bad;
            s_f(W, rnd, va, sidx, ow, lbase, bad, ones, qtr, ktr, er, po, n < 8 ? 1.f : 0.f, n == 8 + 2 * g ? 1.f : 0.f, n == 9 + 2 * g ? 1.f : 0.f, live(srd, s), so, ce, LIM_D STA);
            if (__builtin_expect(bad && s >= 0, 0)) s_slow(h, s, rs, so, lbase);
            FLUSH();
        }
        BAR0();
        Q_LAST();
    } else if (wave == 7) {
        LANES();
        const u32 vm = (u32)lane * 16, cp = (u32)xg ^ (wp >> 1);
        u32 c0, r0, kr = 3950;
        calib(c0, r0);
        const u32 tc = slot_v(wp ^ 8u, lane) + copy_off(cp, lane);
        u32 tg = r0, so = 0, left = SPIN_CAP, s = ~0u, poll = 2, cnt = 0;
        LOGS();
        asm volatile("s_waitcnt lgkmcnt(0)" ::: "memory");
#pragma unroll 1
        while (s + 1 != NSEG) {
            u32 ok;
            s_m(W, rnd, va, sidx, ow, lbase, left, s, so, tg, kr, c0, r0, ok, cnt, vm, tc, lane < 8 ? vm * 8u : OOB, lbase + (poll & 2u) * 40960u, live(srd, (int)s), poll,
                NSEG - 1 STA LGA);
            UNI(rnd, left, s, so, tg, kr, c0, r0, ok, cnt);
            if (__builtin_expect(ok == 2u, 0)) { once_more(left, lbase, errw); poll = 0; continue; }
            if (s == 0) { BAR0(); poll = 0; continue; }
            poll = 1;
            if (!ok) tg = read_ts(rs, so + SL_TS, (int)s - 1, ok);
        }
        for (u32 poll = 1;;) {
            if (poll) gather(tg, kr, vm, so, lbase, srd);
            if (poll == 1) LBAR();
            DRAIN();
            u32 redo;
            s_l(rnd, redo, lbase);
            if (__builtin_expect(!redo, 1)) break;
            poll = once_more(left, lbase, errw);
        }
    } else if (wave < 2) {
        LANES();
        const int nt = wave & 1;
        const u32 ktr = lbase + L_K + (u32)((lane & 3) * 192 + (lane >> 2) * 8);
        f32x4* msk = (f32x4*)(smem + L_MSK) + nt * 128 + lane;
#pragma unroll
        for (int q = 0; q < 2; ++q) {
            f32x4 m;
#pragma unroll
            for (int i = 0; i < 4; ++i) m[i] = q * 16 + 4 * g + i <= nt * 16 + n ? 0.f : -__builtin_huge_valf();
            msk[q * 64] = m;
        }
        const u32 vl = lbase + (u32)lane * 16;
        const u32 o0 = (u32)OFF_PX + (u32)((h * 2 + nt) * 1024 + lane * 16);
        const u32 o1 = (u32)OFF_PXM + (u32)(((h >> 2) * 2 + nt) * 1024 + ((h & 3) * 16 + n) * 16 + g * 4);
        u32x4 p0 = {0u, 0u, 0u, 0u};
        u32 p1 = 0;
#pragma unroll 1
        for (int s = -1; s + 1 < NSEG; ++s) {
            const u32 kb = vl + kq_buf(s + 3);
            s_k1(W, rnd, va, sidx, ow, lbase, p0, p1, kb, kb + 8192 + (u32)nt * 4096, vl + L_MSK + (u32)nt * 2048, ktr, o0 + (u32)max(s - 1, 0) * 65536,
                 o1 + (u32)max(s - 1, 0) * 16384, ce, live<1>(srd, s) STA);
            rnd = to_sgpr(rnd);
            FLUSH();
        }
        s_k3(W, rnd, va, sidx, ow, lbase, p0, p1, vl + kq_buf(NSEG - 1), vl + kq_buf(NSEG - 1) + 8192 + (u32)nt * 4096, vl + L_MSK + (u32)nt * 2048, ktr,
             o0 + (NSEG - 2) * 65536, o1 + (NSEG - 2) * 16384, ce, srd, lane == 0 ? (u32)(OFF_FP + (h >> 2) * 128 + ((h & 3) * 2 + nt) * 4) : OOB);
        stx4(rs, o0 + (NSEG - 1) * 65536, p0);
        stx1(rs, o1 + (NSEG - 1) * 16384, p1);
        DRAIN();
        stx1(rs, lane == 0 ? (u32)(OFF_FZ + ((h >> 2) * 2 + nt) * 128 + (h & 3) * 4) : OOB, 1u);
    } else if (wave == 2) {
        LANES();
        u32 cap = SPIN_CAP;
        u32 have = 0;
        auto seen = [&](u32x4 f) {
            u32 a = min(2 * rfl(f[0]), 2 * rfl(f[1]) + 1);
            if (h < 16) a = min(a, max(rfl(f[2]), 4u) - 1u);
            have = max(have, a);
        };
        const u32 tt = slot_v(wp ^ 8u, lane) + SL_TS + 2048u;
        const u32 tp = slot_v(wp ^ 8u, lane) + copy_off((u32)xg ^ (wp >> 1), lane) + SL_PG - 128u;
        *(lds_u32p)(lbase + 11520u + (u32)lane * 4) = 0u;
#pragma unroll 1
        for (int s = -1; s + 1 < NSEG; ++s) {
            const u32 pfo = far_off(wp ^ 1u, NEAR_KX + (u32)((s + 1) * H_ + h) * 8192);
            s_k0(W, rnd, va, sidx, ow, lbase STA);
            have = (u32)max((int)have, (int)rfl(*(lds_vu32p)(lbase + 11520u + (u32)((s + 63) & 63) * 4)));
            u32 spins = 0;
#pragma unroll 1
            for (; have <= (u32)(s + 1) && spins < cap; ++spins) {
                CFENCE();
                seen(ldx4(rs, 0u, (u32)(OFF_FA + h * 128)));
                if (spins) __builtin_amdgcn_s_sleep(2);
            }
            if (spins >= cap) { atomicOr(errw, 8u); cap = 1; }
            CFENCE();
            if (s < 0) XSTAMP(wid, 226);
            s_fetch((u32)lane * 16, lane == s + 1 ? tt : OOB, srd, pfo, lbase + kq_buf(s + 1), lbase + 11264u);
            s_fetch((u32)lane * 16, lane == s ? tp : OOB, srd, pfo - 2u * (NEAR_KX - NEAR_QX),
                    lbase + kq_buf(s + 1) + 8192, lbase + 11520u);
            FLUSH();
        }
        s_k2(W, rnd, va, sidx, ow, lbase);
    } else {
#pragma unroll 1
        for (int s = -1; s + 1 < NSEG; ++s) {
            s_k0(W, rnd, va, sidx, ow, lbase STA);
            FLUSH();
        }
        s_k2(W, rnd, va, sidx, ow, lbase);
    }
    COUNT1(0);
}

__device__ __forceinline__ void v_role(const int wave, const int h, const int xg, const u64 tag, const u16* __restrict__ mem0, const u32x32 (&W)[5],
                                       char* __restrict__ ws, char* smem) {
    const int lane = lane_id(), tid = wave * 64 + lane, n = lane & 15, g = lane >> 4;
    u32* errw = (u32*)(ws + OFF_ERR);
    const rsrc_t rs = mk_rsrc(ws);
    const u32x4 srd = mk_srd(ws);
    const u32 lbase = (u32)(uintptr_t)(__attribute__((address_space(3))) char*)smem;
    TLOAD();
    int wslot = 0; const int wid = 160 + h;
    WSTAMP(2);
    mem0_image(mem0, smem, tid);
    *(lds_u32p)(lbase + 14336) = 0u;
    const u32 wp = wait_init(rs, tag, 1);
    const u32 sidx = (n & (wave < 4 ? 2 : 8)) ? 0xEEEE : 0x4444;
    asm volatile("s_waitcnt vmcnt(0) lgkmcnt(0)" ::: "memory");
    STAMPS();
    COUNT0();
    u32 rnd = 0;
    if (wave < 4) {
        const u32 va = lbase + (u32)(g * 256 + (n & 2) * 64 + (n >> 2) * 32 + (n & 1) * 16), vw = lbase + 12288 + (u32)((wave * 16 + n) * 16 + g * 4);
        if (wave) {
#pragma unroll 1
            for (int s = 0; s < NSEG; ++s) {
                v_a0(W, rnd, va, sidx, vw, lbase STA);
                FLUSH();
                if (wave == 1 && s + 1 < NSEG) touch(srd, wp, s + 1, lbase + 15360u);
            }
        } else {
            const u32 tc = slot_v(wp ^ 8u, lane) + copy_off((u32)xg ^ (wp >> 1), lane);
            u32 c0, r0, kr = 3950;
            calib(c0, r0);
            u32 tg = r0, so = 0, left = SPIN_CAP, s = 0, poll = 2, cnt = 0;
            LOGS();
#pragma unroll 1
            while (s != NSEG) {
                u32 ok;
                v_a1(W, rnd, va, sidx, vw, lbase, left, s, so, tg, kr, c0, r0, ok, cnt, (u32)lane * 16, tc, lane < 8 ? (u32)lane * 128u : OOB, lbase + (poll & 2u) * 16384u,
                     live<1>(srd, (int)s), poll, NSEG, TV(15) STA LGA);
                UNI(rnd, left, s, so, tg, kr, c0, r0, ok, cnt);
                if (__builtin_expect(ok == 2u, 0)) { once_more(left, lbase, errw); poll = 0; continue; }
                poll = 1;
                if (!ok && s != NSEG) tg = read_ts(rs, so + SL_TS, (int)s - 1, ok);
            }
        }
    } else {
        const u32 vr = lbase + 12288 + (u32)((g * 8 + (n >> 2)) * 16 + (n & 1) * 8);
        u32 z[3];
#pragma unroll
        for (int p = 0; p < 3; ++p) z[p] = p == 2 && (g & 1) ? OOB : z_off(wp, (u32)((p * 2 + (g & 1)) * 8 + (wave & 3) * 2 + (g >> 1))) + (u32)(h * 256 + n * 16);
#pragma unroll 1
        for (int s = 0; s < NSEG; ++s) {
            const u32 sz = slot_off(wp, s);
            v_b(W, rnd, sidx, vr, z[0] + sz, z[1] + sz, z[2] + sz, srd, lbase STA);
            FLUSH();
        }
    }
    DRAIN();
    LBAR();
    stx1(rs, wave == 0 && lane_id() < 2 ? (u32)(OFF_FZ + ((h >> 2) * 2 + lane_id()) * 128 + 16 + (h & 3) * 4) : OOB, 1u);
    COUNT1(3);
}

constexpr u32 L_U = 8192, L_T = 16384;
__device__ __forceinline__ u32x4 sub4(const u32x32& v, const int i) { return (u32x4){v[i], v[i + 1], v[i + 2], v[i + 3]}; }
__device__ __forceinline__ void r_slow(const int s, rsrc_t rs, u32 so, u32 sz, u32 sm, u32 pv, u32 zv, u32 ra, u32 rb, u32 mo, u32 sidx, u32 lbase, u32 gx, u32& cap, u32* errw) {
    const int lane = lane_id(), n = lane & 15;
    u32 spins = 0;
    u32x4 p[8], z[8];
#pragma unroll 1
    for (; spins < cap; ++spins) {
        CFENCE();
        u32 d = 1;
#pragma unroll
        for (int j = 0; j < 8; ++j) p[j] = ldx4(rs, pv + (u32)(j & 1) * 256, so + ((gx ^ (u32)(j >> 1)) & 1u) * 1024u + ((gx ^ (u32)(j >> 1)) >> 1) * 32768u);
#pragma unroll
        for (int j = 0; j < 8; ++j) z[j] = ldx4(rs, zv + (u32)(j & 1) * 1024, sz + (gx ^ (u32)(j >> 1)) * 2048u);
#pragma unroll
        for (int j = 0; j < 8; ++j) d = dist(dist(d, p[j], SENT), z[j], SENT);
        if (!__any(d == 0)) break;
    }
    if (spins >= cap) { atomicOr(errw, 2u); cap = 2; }
#pragma unroll
    for (int j = 0; j < 8; ++j) {
#pragma unroll
        for (int i = 0; i < 4; ++i) p[j][i] = p[j][i] == MARK ? 0u : p[j][i];
    }
    *(lds_vu32p)(lbase + L_T + 12) = step_tag(s);
    LBAR();
    LBAR();
    f32x4 acc = {0.f, 0.f, 0.f, 0.f};
#pragma unroll
    for (int j = 0; j < 8; j += 2) {
        const u32 ua = (j == 6 ? rb : ra) + (u32)j * 1024;
        acc = smf((u32x4){p[j][2], p[j][3], p[j + 1][0], p[j + 1][1]}, z[j], z[j + 1], acc, (int)sidx);
        acc = smf((u32x4){p[j + 1][2], p[j + 1][3], p[(j + 2) & 7][0], p[(j + 2) & 7][1]}, *(lds_u32x4p)ua, *(lds_u32x4p)(ua + 1024), acc, (int)sidx);
    }
    const float x0 = acc[0] + acc[2], x1 = acc[1] + acc[3], o = __shfl_xor(n < 8 ? x1 : x0, 8);
    const u32 r = n < 8 ? pack2(x0, o) : pack2(o, x1);
#pragma unroll
    for (int c = 0; c < NCP; ++c) stx1(rs, mo, r, sm + cpy(c));
    LBAR();
}

__device__ __forceinline__ void r_role(const int wave, const int ct, const int xg, const u32 wp, char* __restrict__ ws, char* smem) {
    const int lane = lane_id(), tid = wave * 64 + lane, n = lane & 15, g = lane >> 4;
    u32* errw = (u32*)(ws + OFF_ERR);
    const rsrc_t rs = mk_rsrc(ws);
    const u32x4 srd = mk_srd(ws);
    const u32 lbase = (u32)(uintptr_t)(__attribute__((address_space(3))) char*)smem;
    if (tid < 6) *(u32*)(smem + L_T + 8 + tid * 4) = ~0u;
    asm volatile("s_waitcnt lgkmcnt(0)" ::: "memory");
    int wslot = 0; const int wid = 192 + ct;
    WSTAMP(1);
    TLOAD();
    const u32 cp = (u32)xg ^ (wp >> 1);
    u32 cap = SPIN_CAP;
    STAMPS();
    LOGS();
    COUNT0();
    if (wave == 0) {
        const int kp = ct >= 36 ? 10 : ct >> 2;
        const u32 mb = (u32)((kp >> 2) * 32768 + (kp & 3) * 1024 + (ct & 3) * 256);
        const u32 mo = mb + (u32)(((n & 7) >> 2) * 128 + (2 * g + (n >> 3)) * 16 + (n & 3) * 4);
        const u32 pv = (u32)((g >> 1) * 2048 + (g & 1) * 512 + n * 16), zv = (u32)(g * 256 + (n & 7) * 32 + (n >> 3) * 16), sidx = (n & 2) ? 0xEEEE : 0x4444;
        const u32 ra = lbase + L_U + (u32)(((g & 1) ? 2048 : 256) + (g >> 1) * 512 + (n & 7) * 32 + (n >> 3) * 16), rb = ra - (u32)(g & 1) * 8192;
        const u32 te = slot_v(wp, lane) + copy_off(cp, lane) + SL_P, tm = slot_v(wp ^ 8u, lane) + put_off(wp, 0u, lane), tz = slot_z(wp, lane) + z_off(wp, (u32)ct);
#pragma unroll 1
        for (int s = -1; s + 1 < NSEG; ++s) {
            const u32 so = tab(te, s & 63), sm = tab(tm, s & 63), sz = tab(tz, s & 63);
            u32 bad, pass;
            r_f(bad, pass, sidx, pv, zv, ra, rb, mo, lbase + L_T + 16 + (u32)(lane & 3) * 4, live(srd, s), so, sz, sm, lbase, step_tag(s), unless_neg(cap, s), min(TV(8), 256u), (u32)((ROT >> 1) * 32768 + (ROT & 1) * 1024), (u32)ROT * 2048u STA);
            if (__builtin_expect(bad, 0)) { if (s < 0) LBAR(); else r_slow(s, rs, so, sz, sm, pv, zv, ra, rb, mo, sidx, lbase, (u32)ROT, cap, errw); }
            if (pass) COUNT(2);
            touch(srd, wp, s + 1, lbase + 17408u);
            LOG();
        }
    } else if (wave == 4) {
        u32 c0, r0, kr = 3950;
        calib(c0, r0);
        const u32 tc = slot_v(wp ^ 8u, lane) + copy_off(cp, lane) + SL_TS;
        u32 pc = c0, pr = r0, tg = r0 - 0x10000u, t2 = 0, fp = 1;
#pragma unroll 1
        for (int s = -1; s + 1 < NSEG; ++s) {
            u32 t1 = tg, ok = 0;
            if (s) r_t0(c0, r0, t1, t2, ok, tg + TV(6), tg + (t2 & 0xfffu), kr, (u32)(s & 127) + 1u, fp, lbase, live(srd, s), tab(tc, s & 63) STA);
            else r_te(lbase, fp);
            fp = step_tag(s);
            if (s < 0) { if (ok == 0u || ok == 3u) LBAR(); }
            else if (__builtin_expect(ok == 0u || ok == 3u, 0)) {
                if (!ok) until(tg + TV(6) + TV(7) - 256u);
                r_t1(c0, r0, t1, t2, lane < 8 ? (u32)lane * 128u : OOB, kr, (u32)s + 1u, lbase, srd, tab(tc, s) STA);
            }
            if (__builtin_expect(ok == 2u && s >= 0, 0)) { r_te(lbase, fp); fp = 1; t1 = read_ts(rs, tab(tc, s), s, ok); }
            tg = t1;
            track_rate(c0, r0, pc, pr, kr);
            LOG();
        }
        r_te(lbase, fp);
    } else {
        const int o = wave == 5 ? 3 : wave - 1, q = o ^ ROT;
        u32 have = 0;
        u32x32 bn = {};
        auto seen = [&](u32x2 f) {
            u32 a = min(2 * f[0], 2 * f[1] + 1);
            a = min(a, (u32)__builtin_amdgcn_update_dpp(0u, a, 0xb1, 0xf, 0xf, false));
            a = min(a, (u32)__builtin_amdgcn_update_dpp(0u, a, 0x4e, 0xf, 0xf, false));
            a = min(a, (u32)__builtin_amdgcn_update_dpp(0u, a, 0x124, 0xf, 0xf, false));
            have = max(have, rfl(a));
        };
        const u32 fo = (u32)(OFF_FB + (q * 8 + (lane & 7)) * 128);
        auto load_b = [&](int sg, u32x32& bx) {
            u32 spins = 0;
#pragma unroll 1
            for (; have <= (u32)sg && spins < cap; ++spins) {
                CFENCE();
                seen(ldx2(rs, fo));
                if (spins) __builtin_amdgcn_s_sleep(4);
            }
            if (spins >= cap) { atomicOr(errw, 16u); cap = 1; }
            CFENCE();
            const u32 bs = far_off(wp, FAR_BX + (u32)(sg * NRED + ct) * 32768 + (u32)q * 8192);
            pull((u32)lane * 128, bs, srd, 1u, 0u, lbase + 17664u);
            u32x4 t[8];
#pragma unroll
            for (int i = 0; i < 8; ++i) t[i] = ldx4(rs, (u32)(lane * 16 + i * 1024), bs);
#pragma unroll
            for (int i = 0; i < 8; ++i)
#pragma unroll
                for (int j = 0; j < 4; ++j) bx[(i >> 1) * 8 + j * 2 + (i & 1)] = t[i][j];
        };
        const u32 eo = (u32)(q * 32768 + lane * 16), te = slot_v(wp, lane) + copy_off(cp, lane), sidx = n >= 8 ? 0xEEEE : 0x4444;
        const u32 uw = lbase + L_U + (u32)(o * 2048 + (g >> 1) * 256 + n * 16 + (g & 1) * 8);
        const u32 tb = far_v(wp, FAR_BX + (u32)(min(lane + 1, NSEG - 1) * NRED + ct) * 32768 + (u32)q * 8192);
        const u32 tp = slot_v(wp ^ 8u, lane) + copy_off(cp, lane) + SL_PG + (u32)q * 128;
        u32 s = ~0u, why = 0, left = 0;
#pragma unroll 1
        for (;;) {
            if (why & 1u) {
#pragma unroll
                for (int i = 0; i < 4; ++i) {
                    const f32x4 u = smf(ldx4(rs, eo + (u32)i * 1024, tab(te, (int)s - 1)), sub4(bn, i * 8), sub4(bn, i * 8 + 4), (f32x4){0.f, 0.f, 0.f, 0.f}, (int)sidx);
                    *(lds_u32x2p)(uw + (u32)i * 512) = (u32x2){pack2(u[0], u[1]), pack2(u[2], u[3])};
                }
                LBAR();
                LBAR();
            }
            if (s + 1 == NSEG) break;
            if (s == 0) left = SPIN_CAP;
            if ((int)s >= 0) load_b((int)s, bn);
            have = to_sgpr(have); left = to_sgpr(left);
            r_u(bn, s, have, left, why, sidx, eo, uw, lbase + L_T + 16 + (u32)q * 4, (u32)lane * 16, tp, te, tb, live(srd, (int)s), (u32)o * 4u, lbase STA LGA);
            s = to_sgpr(s); have = to_sgpr(have); left = to_sgpr(left); why = to_sgpr(why);
        }
        if (left == 0) atomicOr(errw, 4u);
    }
    COUNT1(wave ? 1 : 2);
    LOGOUT(32 + ct);
}

__device__ __forceinline__ void relay_role(const u32 wp, const int xg, char* __restrict__ ws) {
    const int lane = lane_id();
    const rsrc_t rs = mk_rsrc(ws);
    const u32 tc = slot_v(wp ^ 8u, lane) + copy_off((u32)xg ^ (wp >> 1), lane) + SL_TS;
#pragma unroll 1
    for (int k = 0; k + 1 < NSEG; ++k) {
        u32 t;
#pragma unroll 1
        for (u32 spins = 0;; ++spins) {
            u32 ok;
            t = read_ts(rs, tab(tc, k), k, ok);
            if (ok) break;
            if (spins >= 16u || ldx1(rs, (u32)OFF_ERR) != 0u) return;
        }
        if (k >= 3) stx1(rs, lane < 8 ? (u32)(OFF_RDY + lane * 128) : OOB, (u32)k - 2u);
        for (int q = 0; q < 64 && (int)((u32)__builtin_amdgcn_s_memrealtime() * 24u - t) < 2400; ++q) __builtin_amdgcn_s_sleep(2);
    }
}

__device__ __forceinline__ void lead_role(const u32 wp, const int xg, char* __restrict__ ws, char* smem) {
    const int lane = lane_id();
    u32* errw = (u32*)(ws + OFF_ERR);
    const rsrc_t rs = mk_rsrc(ws);
    const u32x4 srd = mk_srd(ws);
    TLOAD();
    STAMPS();
    const u32 tsw = lane < 4 ? SL_TS + (u32)((lane & 1) * 4096 + (lane >> 1) * 16384 + xg * 128) : OOB;
    const u32 tm = slot_v(wp ^ 8u, lane), tc = tm + copy_off((u32)xg ^ (wp >> 1), lane);
    const u32 m0o = lane < 40 ? (u32)((lane >> 4) * 32768 + (lane & 15) * 256 + (lane >= 36 ? 1024 : 0) + 128) : OOB;
    u32 c0, r0, kr = 3950, ki = 4240, bump = 0, hist = 0, dp = 0;
    calib(c0, r0);
    u32 pc = c0, pr = r0, tg = r0, cap = SPIN_CAP, m = 15, de, dd, per, kc = ki;
    auto plan = [&](int s) {
        const u32 mg = bump + (s < EARLY_N ? TV(10) : 0u) + (s < 4 ? TV(3) : 0u);
        de = (TV(0) * kc >> 12) + mg;
        dd = de | ((TV(1) * kc >> 12) << 12) | ((ki - 2048u) << 20);
        per = max(de, dp) + (TV(2) * kc >> 12) + mg;
        dp = de;
    };
    plan(0);
    LOGS();
#pragma unroll 1
    for (int s = 0; s + 1 < NSEG; ++s) {
        u32 sr = tsw + tab(tm, s), look = 0;
        const u32 so = tab(tc, (s + 63) & 63);
#pragma unroll 1
        for (;; ++look) {
            u64 tl;
            u32 miss;
            l_g(miss, c0, r0, tl, tg, look ? 0u : kr, m0o, so, live<1>(srd, s), (u32x4){tg + de, tg + per, dd, (u32)s + 1u}, look || !s ? OOB : sr, SLOW_RT, TV(4) STA);
            if (!s) break;
            if (look && (int)((u32)tl * 24u + TV(14) - tg) > 0) tg = (u32)tl * 24u + TV(14);
            if (__builtin_expect(!miss, 1)) break;
            if (!look) {
                sr += 512u;
                tg += TV(7);
            }
            if (look >= cap) { atomicOr(errw, 4u); cap = 1; break; }
        }
        if (!s) {
            bool dead = false;
            wait_word(rs, (u32)(OFF_FA + lane * 128), true, 1u, dead, errw, 8u, lane);
            XSTAMP(192 + xg, 227);
            tg = (u32)__builtin_amdgcn_s_memrealtime() * 24u + TV(13) - 512u;
        }
        if (!s || look) stx4(rs, sr, (u32x4){tg + de, tg + per, dd, (u32)s + 1u});
        touch(srd, wp, s + 1, (u32)(uintptr_t)(__attribute__((address_space(3))) char*)smem);
        track_rate(c0, r0, pc, pr, kr);
        if (pc == c0) { const u32 kn = to_sgpr((u32)(16777216.f * __builtin_amdgcn_rcpf((float)kr) + 0.5f)); ki = min(max(kn, (ki * 3u + kn) >> 2), 6000u); }
        hist = ((hist << 1) | (s > BUMP_S0 && m != 15u ? 1u : 0u)) & BUMP_WIN;
        if (__builtin_popcount(hist) >= BUMP_N && bump < BUMP_MAX * TV(11)) { bump += TV(11); hist = 0; }
        plan(s + 1);
        asm volatile("s_sleep 3" : "+s"(de), "+s"(dd), "+s"(per));
        u32 t2;
        tg = look ? read_ts(rs, tab(tc, s) + SL_TS, s, m, &t2) : read_ts_late(rs, srd, tab(tc, s) + SL_TS, s, m, t2, 256u);
        kc = max(t2 >> 20, 1800u) + 2048u;
        LOG();
    }
    if (xg == 0) LOGOUT(104);
}

__device__ __forceinline__ void fill_role(const u32 wp, const int fi, char* __restrict__ ws) {
    const int lane = lane_id();
    const rsrc_t rs = mk_rsrc(ws);
    TLOAD();
    const u32 oz = (u32)(fi * 65536 + lane * 16);
#pragma unroll 1
    for (int s = 5; s < NSEG; ++s) {
        const u32 un = 0x38u << ((~((wp >> 3) ^ ((u32)s >> 5)) & 1u) * 8u), a = oz + slot_off(wp, s);
        const u32 a0 = (un >> (2 * fi)) & 1u ? OOB : a, a1 = (un >> (2 * fi + 1)) & 1u ? OOB : a + 32768u;
#pragma unroll
        for (u32 i = 0; i < 32; ++i) {
            stx4(rs, (i < 16 ? a0 : a1) + ((i >> 3) & 1u) * 16384 + (i & 7) * 1024, (u32x4){SENT, SENT, SENT, SENT});
            if (i == 15) {
                asm volatile("s_waitcnt vmcnt(16)" ::: "memory");
                stx1(rs, lane == 0 ? (u32)(OFF_FA + fi * 128 + 8) : OOB, (u32)s - 1u);
            }
            if (i % FILLG == FILLG - 1) nap(TV(9));
        }
    }
    DRAIN();
    stx1(rs, lane == 0 ? (u32)(OFF_FA + fi * 128 + 8) : OOB, (u32)NSEG + 1u);
}

__device__ __forceinline__ int tile_of(const int xg, const int hl, const int i) {
    const u32 pieces = hl ? 0x85410u : 0x97632u;
    return CFG ? (int)((pieces >> ((i >> 1) * 4)) & 15u) * 4 + (i & 1) * 2 + (xg & 1) : (int)((pieces >> (i * 4)) & 15u) * 4 + xg;
}
__device__ __forceinline__ void warm_role(const u32 wp, const int xg, const int wi, char* __restrict__ ws, char* smem) {
    const int lane = lane_id();
    const rsrc_t rs = mk_rsrc(ws);
    const u32x4 srd = mk_srd(ws);
    const u32 lbase = (u32)(uintptr_t)(__attribute__((address_space(3))) char*)smem;
    TLOAD();
    u32 have = 0, ta = 0;
    int next = 1;
    const u32 tc = slot_v(wp ^ 8u, lane) + copy_off((u32)xg ^ (wp >> 1), lane) + SL_TS;
    const u32 wq = wi < 0 ? wp ^ 1u : wp, ls = wi < 0 ? H_ * 8192u : NRED * 32768u;
    const u32 l0 = wi < 0 ? (wi & 1 ? NEAR_KX : NEAR_QX) + (u32)(xg * 8 + (lane & 7)) * 8192 : FAR_BX + (u32)(min(lane, 9) * 4 + xg) * 32768 + (u32)wi * 8192;
    auto look = [&] {
        const u32x4 f = ldx4(rs, wi < 0 ? (u32)(OFF_FA + (xg * 8 + (lane & 7)) * 128) : (u32)(OFF_FB + (wi * 8 + (lane & 7)) * 128));
        u32 a = min(2 * f[0], 2 * f[1] + 1);
        if (wi < 0 && xg < 2) a = min(a, max(f[2], 4u) - 1u);
#pragma unroll
        for (int q = 1; q < 8; q <<= 1) a = min(a, (u32)__shfl_xor((int)a, q));
        have = rfl(a);
    };
#pragma unroll 1
    for (u32 spins = 0; next < WFIRST && spins < 0x4000u; ++spins) {
        CFENCE();
        if (have <= (u32)next) { __builtin_amdgcn_s_sleep(4); look(); }
        else { pull((u32)lane * 128, far_v(wq, l0 + (u32)next * ls), srd, wi < 0 ? 8u : 10u, 64u, lbase + 69632u); ++next; }
    }
#pragma unroll 1
    for (int s = 0; s + 2 < NSEG; ++s) {
        if (s) until(ta + 1000u);
        u32 tm, t2;
        ta = read_ts_late(rs, srd, tab(tc, s), s, tm, t2);
        if (have <= (u32)s + 8u) look();
        for (int q = 0; q < 64 && have <= (u32)min(s + WAHEAD, NSEG - 1) && (int)(ta + TV(5) - 1024u + WLATE - (u32)__builtin_amdgcn_s_memrealtime() * 24u) > 0; ++q) { CFENCE(); look(); }
        stx1(rs, lane == 0 ? (u32)(SL_PG - SL_TS + wi * 128) : OOB, have, tab(tc, s + 1));
        if (next < s + 2) next = s + 2;
#pragma unroll 1
        for (; next <= s + WAHEAD && next < NSEG && have > (u32)next; ++next) {
            const u32 tb = far_v(wq, l0 + (u32)next * ls);
            until(ta + TV(5) - 1024u);
            pull((u32)lane * 128, tb, srd, wi < 0 ? 8u : 10u, min(TV(12), 256u), lbase + 69632u);
        }
    }
    DRAIN();
}

__device__ __forceinline__ void topk_role(const u64 tag, char* __restrict__ ws) {
    const int lane = lane_id();
    const rsrc_t rs = mk_rsrc(ws);
    wait_init(rs, tag, 2);
    u32 v[32];
#pragma unroll
    for (int i = 0; i < 8; ++i) {
        const u32x4 t = ldx4(rs, (u32)lane * 16, (u32)(OFF_R + i * 1024));
#pragma unroll
        for (int j = 0; j < 4; ++j) v[i * 4 + j] = ((t[j] ^ ((u32)((int)t[j] >> 31) | 0x80000000u)) & 0xffff0000u) | (u32)(2047 - i * 256 - j - lane * 4);
    }
    u32 top = ~0u, res = 0;
#pragma unroll 1
    for (int c = 0; c < TOPC; ++c) {
        u32 d = ~0u;
#pragma unroll
        for (int i = 0; i < 32; ++i) d = min(d, top - v[i]);
        d = min(d, (u32)__builtin_amdgcn_update_dpp(0u, d, 0xb1, 0xf, 0xf, false));
        d = min(d, (u32)__builtin_amdgcn_update_dpp(0u, d, 0x4e, 0xf, 0xf, false));
        d = min(d, (u32)__builtin_amdgcn_update_dpp(0u, d, 0x124, 0xf, 0xf, false));
        d = min(d, (u32)__builtin_amdgcn_update_dpp(0u, d, 0x128, 0xf, 0xf, false));
        const u32 dm = min(min((u32)__builtin_amdgcn_readlane(d, 0), (u32)__builtin_amdgcn_readlane(d, 16)), min((u32)__builtin_amdgcn_readlane(d, 32), (u32)__builtin_amdgcn_readlane(d, 48)));
        top -= dm;
        if (lane == c) res = 2048u - (top & 2047u);
        top -= 1u;
    }
    stx1(rs, lane < TOPC ? (u32)(OFF_IDX + lane * 4) : OOB, res);
    XSTAMP(192, 224);
}

#define MOEV 0
#define MOE_LD(p) __builtin_nontemporal_load(p)
#define MOE_NT " nt"
template <int NK>
__device__ __forceinline__ void moe_ld(const u16* __restrict__ wp, const int ldw, const int nvalid, u32 (&r)[NK][8]) {
#pragma unroll
    for (int k = 0; k < NK; ++k)
#pragma unroll
        for (int i = 0; i < 8; ++i) r[k][i] = k < nvalid ? MOE_LD((const u32*)(wp + (size_t)(k * 32 + i) * ldw)) : 0u;
}
template <int NK>
__device__ __forceinline__ void moe_mma(const u32 (&r)[NK][8], const u32x4 (&af)[NK], f32x4& ce, f32x4& co) {
#pragma unroll
    for (int k = 0; k < NK; ++k) {
        u32x4 be, bo;
#pragma unroll
        for (int j = 0; j < 4; ++j) {
            be[j] = __builtin_amdgcn_perm(r[k][2 * j + 1], r[k][2 * j], 0x05040100u);
            bo[j] = __builtin_amdgcn_perm(r[k][2 * j + 1], r[k][2 * j], 0x07060302u);
        }
        ce = mfma16(af[k], be, ce);
        co = mfma16(af[k], bo, co);
    }
}
constexpr int NGU = FF_ / 32, NDN = D_ / 32;
__device__ __forceinline__ void moe_part(const int w, const int tid, const u16* __restrict__ wg, const u16* __restrict__ wu,
                                         const u16* __restrict__ wd, char* __restrict__ ws, char* smem) {
    const int wave = __builtin_amdgcn_readfirstlane(tid >> 6), lane = tid & 63;
    const int n = lane & 15, g = lane >> 4;
    u32* errw = (u32*)(ws + OFF_ERR);
    const rsrc_t rs = mk_rsrc(ws);
    bool dead = false;
    f32x4 (*red)[2][64] = (f32x4(*)[2][64])smem;
    const f32x4 z = {0.f, 0.f, 0.f, 0.f};
    wait_word(rs, (u32)(OFF_IDX + (n & 7) * 4), true, 1u, dead, errw, 16u, lane);
    const u32 row = (ldx1(rs, (u32)(OFF_IDX + (n & 7) * 4)) - 1u) & (S_ - 1);
    u32 r1[5][8], r2[7][8];
    const int kq = wave >> 1, dg = w - (64 - NDN), k0 = wave * 7, nk = wave == 7 ? 5 : 7;
    if (w < NGU) moe_ld<5>(((wave & 1) ? wu : wg) + (size_t)(kq * 160 + g * 8) * FF_ + w * 32 + n * 2, FF_, 5, r1);
    if (dg >= 0) moe_ld<7>(wd + (size_t)(k0 * 32 + g * 8) * D_ + dg * 32 + n * 2, D_, nk, r2);
    if (w < NGU) {
        u32x4 af[5];
#pragma unroll
        for (int k = 0; k < 5; ++k) af[k] = ldx4(rs, (u32)OFF_XN + (row * D_ + (u32)((kq * 5 + k) * 32 + g * 8)) * 2);
        f32x4 ce = z, co = z;
        moe_mma<5>(r1, af, ce, co);
        red[wave][0][lane] = ce; red[wave][1][lane] = co;
        __syncthreads();
        if (wave == 0 && g < 2) {
            f32x4 t[4];
#pragma unroll
            for (int q = 0; q < 4; ++q) t[q] = (red[q >> 1][q & 1][lane] + red[2 + (q >> 1)][q & 1][lane]) + (red[4 + (q >> 1)][q & 1][lane] + red[6 + (q >> 1)][q & 1][lane]);
#pragma unroll
            for (int i = 0; i < 4; ++i) {
                float res[2];
#pragma unroll
                for (int q = 0; q < 2; ++q) {
                    float gg = bfr(t[q][i]), uu = bfr(t[2 + q][i]);
                    float sg = bfr(gg / (1.0f + __expf(-gg)));
                    res[q] = sg * uu;
                }
                stx1(rs, (u32)(OFF_HB + ((size_t)(4 * g + i) * FF_ + w * 32 + n * 2) * 2), pack2(res[0], res[1]));
            }
        }
        if (wave == 0) {
            DRAIN();
            if (lane == 0) __hip_atomic_fetch_add((u32*)(ws + OFF_CNT), 1u, __ATOMIC_RELAXED, __HIP_MEMORY_SCOPE_AGENT);
        }
        __syncthreads();
    }
    if (dg >= 0) {
        const float rv = __uint_as_float(ldx1(rs, (u32)OFF_R + row * 4));
        wait_word(rs, (u32)OFF_CNT, true, (u32)NGU, dead, errw, 16u, lane);
        u32x4 af[7];
#pragma unroll
        for (int k = 0; k < 7; ++k) af[k] = ldx4(rs, (u32)(OFF_HB + ((size_t)(n & 7) * FF_ + (k0 + (k < nk ? k : 0)) * 32 + g * 8) * 2));
        f32x4 ye = z, yo = z;
        moe_mma<7>(r2, af, ye, yo);
        red[wave][0][lane] = ye; red[wave][1][lane] = yo;
        __syncthreads();
        if (wave == 0) {
            f32x4 t[2];
#pragma unroll
            for (int q = 0; q < 2; ++q)
                t[q] = ((red[0][q][lane] + red[1][q][lane]) + (red[2][q][lane] + red[3][q][lane])) + ((red[4][q][lane] + red[5][q][lane]) + (red[6][q][lane] + red[7][q][lane]));
            const float rs1 = bfr(1.0f / (1.0f + __expf(-rv)));
#pragma unroll
            for (int i = 0; i < 4; ++i) {
                const float rsg = __shfl(rs1, (4 * g + i) & 7);
                stx1(rs, g < 2 ? (u32)(OFF_UPD + ((size_t)(4 * g + i) * D_ + dg * 32 + n * 2) * 2) : OOB, pack2(bfr(t[0][i]) * rsg, bfr(t[1][i]) * rsg));
            }
            DRAIN();
            if (lane == 0) __hip_atomic_fetch_add((u32*)(ws + OFF_CNT + 256), 1u, __ATOMIC_RELAXED, __HIP_MEMORY_SCOPE_AGENT);
        }
        __syncthreads();
    }
}

__device__ __forceinline__ void moe_ahead(const int w, const int t, const u16* __restrict__ wg, const u16* __restrict__ wu, const u16* __restrict__ wd, u32 lds) {
    const int lane = lane_id(), t2 = w < NGU ? t - 20 : t;
    if (t2 >= (w >= 64 - NDN ? 27 : 0)) return;
    const u16* p = t2 < 0 ? (t < 10 ? wg : wu - 640 * FF_) + (size_t)(t * 64 + lane) * FF_ + w * 32 : wd + (size_t)(t2 * 64 + lane) * D_ + (w - (64 - NDN)) * 32;
    u32 sv;
    asm volatile("s_mov_b32 %0, m0\n\ts_mov_b32 m0, %2\n\ts_nop 0\n\tglobal_load_lds_dword %1, off" MOE_NT "\n\ts_mov_b32 m0, %0" : "=&s"(sv) : "v"(p), "s"(lds) : "memory");
}

#define LOOKST 18
constexpr int NBIG = (NSEG - 1) * 10, NTASK = NBIG + 80;

struct OutCtx { rsrc_t rs; const u16* x; u16* out; u32* errw; u32 wp; int wid; char* ws; };
template <int OT, int NT>
__device__ __forceinline__ void out_task(const OutCtx& cx, const int sg, const int c0, const int n0, const int tid, const int (&sel_idx)[TOPC], bool& adone, u32& rdy,
                                         bool& dead, char* redp, u32* nextp, u32* tctr) {
    const int wave = __builtin_amdgcn_readfirstlane(tid >> 6), lane = tid & 63, n = lane & 15, g = lane >> 4;
    const rsrc_t rs = cx.rs;
    if (!adone && rdy <= (u32)sg) {
        wait_word(rs, (u32)(OFF_FB + (wave * 4 + (lane & 3)) * 128 + (sg & 1) * 4), true, (u32)(sg >> 1) + 1, dead, cx.errw, 32u, lane);
        u32x2 f = ldx2(rs, (u32)(OFF_FB + (wave * 4 + (lane & 3)) * 128));
        adone = __all(f[0] >= NSEG / 2 && f[1] >= NSEG / 2);
    }
    u32x4 af[OT][5], bq[NT][5];
    const u32 bo = amap((u32)(wave * 4096 + lane * 16));
#pragma unroll
    for (int c = 0; c < OT; ++c) {
        const u32 bb = far_off(cx.wp, FAR_BX + (u32)(sg * 40 + c0 + c) * 32768);
#pragma unroll
        for (int hh = 0; hh < 4; ++hh) af[c][hh] = ldx4(rs, bo + hh * 1024, bb);
    }
    const int ti = (tid >> 6) & (OT * NT - 1);
    const int token = sg * SEGL + (n0 + (ti & (NT - 1))) * 16 + n;
    const int col = (c0 + ti / NT) * 16 + 4 * g;
    int sel = -1;
#pragma unroll
    for (int c = 0; c < TOPC; ++c) if (sel_idx[c] == token) sel = c;
    const u32x2 xv = *(const u32x2*)(cx.x + (size_t)token * D_ + col);
    const u32x2 nv = ldx2(rs, (u32)(OFF_XN + ((size_t)token * D_ + col) * 2));
    if (__any(sel >= 0)) wait_word(rs, (u32)(OFF_CNT + 256), true, (u32)NDN, dead, cx.errw, 256u, lane);
    const u32x2 uv = ldx2(rs, sel >= 0 ? (u32)(OFF_UPD + ((size_t)sel * D_ + col) * 2) : OOB);
    __builtin_amdgcn_sched_barrier(0);
    const bool late = sg + 4 >= NSEG;
    if (late) wait_flags(rs, (u32)((OT == 1 ? OFF_FZ + (wave * 2 + n0) * 128 : OFF_FP + wave * 128) + (lane & 1) * 16), true, 1u, dead, cx.errw, 64u, lane);
    else if (rdy <= (u32)sg) {
#pragma unroll 1
        for (u32 spins = 0; !dead; ++spins) {
            CFENCE();
            rdy = __builtin_amdgcn_readfirstlane(ldx1(rs, (u32)(OFF_RDY + (cx.wid & 7) * 128)));
            if (rdy > (u32)sg) break;
            __builtin_amdgcn_s_sleep(8);
            if (spins > SPIN_CAP) { if (lane == 0) atomicOr(cx.errw, 64u); dead = true; }
            if ((spins & 63) == 63 && ldx1(rs, (u32)OFF_ERR) != 0u) dead = true;
        }
        CFENCE();
    }
#pragma unroll
    for (int c = 0; c < OT; ++c) af[c][4] = ldx4(rs, (u32)(wave * 64 + lane) * 16, slot_off(cx.wp, sg) + z_off(cx.wp, (u32)(c0 + c)));
#pragma unroll
    for (int nt = 0; nt < NT; ++nt) {
#pragma unroll
        for (int hh = 0; hh < 4; ++hh) bq[nt][hh] = ldx4(rs, (u32)lane * 16, (u32)OFF_PX + (u32)((sg * H_ + wave * 4 + hh) * 2 + n0 + nt) * 1024);
        bq[nt][4] = ldx4(rs, (u32)lane * 16, (u32)OFF_PXM + (u32)((sg * 8 + wave) * 2 + n0 + nt) * 1024);
    }
    CFENCE();
    u32 next = 0;
    if (tid == 0) next = !late ? __hip_atomic_fetch_add(tctr, 1u, __ATOMIC_RELAXED, __HIP_MEMORY_SCOPE_AGENT)
                                 : __hip_atomic_load(tctr, __ATOMIC_RELAXED, __HIP_MEMORY_SCOPE_AGENT);
    f32x4 (*red)[OT * NT][64] = (f32x4(*)[OT * NT][64])redp;
#pragma unroll
    for (int c = 0; c < OT; ++c)
#pragma unroll
        for (int nt = 0; nt < NT; ++nt) {
            f32x4 acc = {0.f, 0.f, 0.f, 0.f};
#pragma unroll
            for (int k = 0; k < 5; ++k) acc = mfma16(af[c][k], bq[nt][k], acc);
            red[wave][c * NT + nt][lane] = acc;
        }
    if (tid == 0) {
        if (late && next < (u32)NTASK) next = __hip_atomic_fetch_add(tctr, 1u, __ATOMIC_RELAXED, __HIP_MEMORY_SCOPE_AGENT);
        *nextp = next;
    }
    LBAR();
    if (tid < OT * NT * 64) {
        f32x4 sum = red[0][ti][lane];
#pragma unroll
        for (int wv = 1; wv < 8; ++wv) sum += red[wv][ti][lane];
        float res[4];
#pragma unroll
        for (int q = 0; q < 4; ++q) {
            float xx = bf2f((u16)((xv[q >> 1] >> ((q & 1) * 16)) & 0xffff));
            float nn = bf2f((u16)((nv[q >> 1] >> ((q & 1) * 16)) & 0xffff));
            float bo2 = nn;
            if (sel >= 0) bo2 = bfr(nn + bf2f((u16)((uv[q >> 1] >> ((q & 1) * 16)) & 0xffff)));
            float gg = bfr(bo2 / (1.0f + __expf(-bo2)));
            float sv = bfr(sum[q]);
            res[q] = bfr(xx + bfr(sv * gg));
        }
        *(u32x2*)(cx.out + (size_t)token * D_ + col) = (u32x2){pack2(res[0], res[1]), pack2(res[2], res[3])};
    }
}

constexpr u32 L_VS = 40960;
__device__ __forceinline__ u32 xbuf(int b) { return b == 0 ? 65536u : (b == 1 ? 106496u : 0u); }
__device__ __forceinline__ void worker_role(const int wave, const int wid, const u64 tag, const u32 wp, const u16* __restrict__ x, const u32x32 (&W)[5],
                                            const u16* __restrict__ wg, const u16* __restrict__ wu,
                                            const u16* __restrict__ wd, char* __restrict__ ws, u16* __restrict__ out, char* smem) {
    const int lane = lane_id(), tid = wave * 64 + lane;
    const int n = lane & 15, g = lane >> 4;
    const int par = (wid >> 2) & 1, kind = (wid >> 3) & 1, h = (wid & 3) * 8 + (wid >> 4), type = kind * 2 + par;
    u32* errw = (u32*)(ws + OFF_ERR);
    const rsrc_t rs = mk_rsrc(ws);
    bool dead = false;
    const bool isqk = type < 2;
    int wslot = 1;
    u32* sh = (u32*)(smem + L_MISC);

    {
        const u32 lbase = (u32)(uintptr_t)(__attribute__((address_space(3))) char*)smem;
        const u32x4 srd = mk_srd(ws);
        const u32 vl = lbase + (u32)lane * 16, xp = (u32)(((wave >> 1) & 1) * 20480 + (wave < 4 ? 4096 + (wave & 1) * 8192 : (wave & 1) * 2048)), xv = xp + (u32)lane * 16, ca = lbase + L_MISC + 64;
        const u32 fo = (u32)(OFF_FA + kind * 4096 + h * 128 + par * 4);
        auto xsrc = [&](int it) { return it < NSEG / 2 ? (u32)OFF_XNP + (u32)(it * 2 + par) * 40960u : (u32)OFF_XNP; };
        auto now = [&](u32 v) {
            DRAIN();
            if (lane_id() == 0 && __hip_atomic_fetch_add((u32*)(smem + L_MISC + 68), 1u, __ATOMIC_RELAXED, __HIP_MEMORY_SCOPE_WORKGROUP) == v * 4 - 1) { stx1(rs, fo, v); XSTAMP(wid, 224 + v); }
        };
        WSTAMP(4);
        STAMPS();
        u32 b0 = xbuf(0), b1 = xbuf(1), b2 = xbuf(2);
        auto rot = [&] { const u32 t = b0; b0 = b1; b1 = b2; b2 = t; };
        if (wave < 4) {
            const u32 oq = (u32)(wave * 64 + lane) * 16;
#pragma unroll 1
            for (int it = 0; it < NSEG / 2; ++it) {
                if (isqk) {
                    if ((it & ~4) == 2) wait_init(rs, tag, 1 + (it >> 2));
                    const u32 ob = far_off(wp ^ 1u, NEAR_QX + (u32)((it * 2 + par) * H_ + h) * 8192);
                    wx0(W, vl + b0, oq, srd, xv, xsrc(it + 2), lbase + b2 + xp, ob, ob + 4096 STA);
                } else wx1(W, vl + b0, lbase + L_VS + (u32)(it & 1) * 8192 + oq STA);
                rot();
                CFLUSH();
                if (it < 3 || (it & 7) == 7) WSTAMP(0x10 + it);
            }
            DRAIN();
            LBAR();
        } else if (isqk) {
            const u32 oq = (u32)(tid & 255) * 16;
            LBAR();
#pragma unroll 1
            for (int it = 0; it < NSEG / 2; ++it) {
                const int sg = it * 2 + par;
                if ((it & ~4) == 2) sh[18] = wait_init(rs, tag, 1 + (it >> 2));
                const u32 ob = far_off(wp ^ 1u, NEAR_KX + (u32)(sg * H_ + h) * 8192);
                wk(W, vl + b0, oq, srd, ob, ob + 4096, 0.08838834764831845f * 1.4426950408889634f, xv, xsrc(it + 2), lbase + b2 + xp,
                   ca, (u32)it * 4 + 3, (u32)it, fo STA);
                rot();
                CFLUSH();
                if (it < NOWV) now((u32)it + 1);
                if (MOEV && it >= 6 && (wave & 2) == 0) moe_ahead(h * 2 + par, (it - 6) * 2 + (wave & 1), wg, wu, wd, lbase + L_MISC + 256);
            }
        } else {
            const u32 ob = amap((u32)(h * 1024 + lane * 16)), yp = 4096u + (u32)(wave & 1) * 6144u;
            LBAR();
#pragma unroll
            for (int i = 0; i < 10; ++i) {
                const u32 o = i < 2 ? (u32)i * 1024 : yp + (u32)(i - 2) * 1024;
                dma16_sc1(ws + xsrc(2) + xv + o, __builtin_amdgcn_readfirstlane(lbase + b2 + xp + o));
            }
            DRAIN();
            LBAR();
#pragma unroll 1
            for (int it = 1; it <= NSEG / 2; ++it) {
                rot();
                if ((it & ~4) == 2) sh[18] = wait_init(rs, tag, 1 + (it >> 2));
                wb(W, vl + L_VS + (u32)(~it & 1) * 8192, ob, srd, FAR_BX + (u32)((it * 2 - 2 + par) * NRED + (wave & 3) * 2) * 32768, wp, (u32)it,
                   xv, xsrc(it + 2), lbase + b2 + xp, xsrc(it + 2) + yp, lbase + b2 + xp + yp, ca, (u32)it * 4 - 1, (u32)it - 1, fo STA);
                CFLUSH();
                if (it <= NOWV) { now((u32)it); LBAR(); }
            }
        }
        DRAIN();
        LBAR();
        stx1(rs, wave == 0 && lane_id() == 0 ? fo : OOB, NSEG / 2);
    }
    WSTAMP(2);

    {
    const int lane = lane_id(), tid = wave * 64 + lane;
    if (isqk) moe_part(h * 2 + par, tid, wg, wu, wd, ws, smem);
    WSTAMP(5);
    u32* nextp = (u32*)(smem + L_MISC + 96);
    u32* tctr = (u32*)(ws + OFF_CNT + 512);
    if (tid == 0) nextp[0] = __hip_atomic_fetch_add(tctr, 1u, __ATOMIC_RELAXED, __HIP_MEMORY_SCOPE_AGENT);
    wait_flags(rs, (u32)OFF_IDX + (lane & 1) * 16, true, 1u, dead, errw, 256u, lane);
    __syncthreads();
    int sel_idx[TOPC];
    {
        u32x4 i0 = ldx4(rs, (u32)OFF_IDX), i1 = ldx4(rs, (u32)OFF_IDX + 16);
#pragma unroll
        for (int c = 0; c < 4; ++c) { sel_idx[c] = (int)i0[c] - 1; sel_idx[4 + c] = (int)i1[c] - 1; }
    }
    u32 order;
    {
        bool hit = false;
#pragma unroll
        for (int c = 0; c < TOPC; ++c) hit |= (sel_idx[c] >> 5) == lane;
        const u64 m = __ballot(hit && lane < 40), below = ((u64)1 << lane) - 1;
        const int pos = lane >= 40 ? lane : ((m >> lane) & 1 ? 40 - __popcll(m) + __popcll(m & below) : lane - __popcll(m & below));
        order = (u32)__builtin_amdgcn_ds_permute(pos * 4, lane);
    }
    WSTAMP(3);
    const OutCtx cx = {rs, x, out, errw, to_sgpr(sh[18]), wid, ws};
    bool adone = false;
    u32 rdy = 0;
#pragma unroll 1
    for (int tc = 0;; ++tc) {
        const int task = __builtin_amdgcn_readfirstlane(nextp[tc & 1]);
        if (task >= NTASK) break;
        char* redp = smem + (tc & 1) * 65536;
        if (task < NBIG) {
            WSTAMP(0x100 + task / 10);
            out_task<4, 2>(cx, (int)tab(order, task / 10), (task % 10) * 4, 0, tid, sel_idx, adone, rdy, dead, redp, nextp + ((tc + 1) & 1), tctr);
        } else {
            WSTAMP(0x100 + NSEG - 1);
            out_task<1, 1>(cx, NSEG - 1, (task - NBIG) >> 1, (task - NBIG) & 1, tid, sel_idx, adone, rdy, dead, redp, nextp + ((tc + 1) & 1), tctr);
        }
        WSTAMP(0x300);
        if (task == NTASK - 1) {
            wait_word(rs, (u32)(OFF_CNT + 768), true, 8u, dead, errw, 64u, lane);
            if (tid < 216) stx2(rs, tid < 192 ? (u32)(OFF_INI + tid * 8) : (u32)(OFF_SUM + (tid - 192) * 128), (u32x2){~(u32)tag, ~cal_tag(tag, ~(u32)tag)});
        }
    }
    }
}

constexpr int FUSED_LDS = L_MISC + 1024;
#define ORD_A 0x143210
#define ORD_C 0x101234
#define KERNEL_SYM "fused_kernel"
#define CODE_KB 176
extern "C" __global__ __launch_bounds__(512, 2) void fused_kernel(const u16* __restrict__ x, const u16* __restrict__ mem0, const u16* __restrict__ w_r,
                                                        const u16* __restrict__ Wq, const u16* __restrict__ Wk,
                                                        const u16* __restrict__ Wv, const u16* __restrict__ Wo,
                                                        const u16* __restrict__ wg, const u16* __restrict__ wu,
                                                        const u16* __restrict__ wd, char* __restrict__ ws, u16* __restrict__ out) {
    extern __shared__ __attribute__((aligned(16))) char smem[];
    const int wave = __builtin_amdgcn_readfirstlane(threadIdx.x >> 6);
    u64 gen = 0, tag = 0, old = 0;
    u32 xcc, se;
    asm volatile("s_getreg_b32 %0, hwreg(HW_REG_XCC_ID, 0, 3)" : "=s"(xcc));
    asm volatile("s_getreg_b32 %0, hwreg(HW_REG_HW_ID, 13, 2)" : "=s"(se));
    u32 bid = blockIdx.x;
    asm volatile("" : "+s"(bid));
    const bool first = (int)bid >= 0;
    u32 bq = first ? (bid & ~31u) + se * 8u + xcc : bid & 255u;
    asm volatile("" : "+s"(bq));
    const int b = (int)bq, k = b >> 3, xg = b & 3;
    if (threadIdx.x >= 448 && threadIdx.x < 453) {
        const int lane = threadIdx.x & 63;
        const u32 pg = (bid >> 3 & 31u) + 32u * (u32)lane;
        const char* tp = lane < 4 ? ws + min(pg << 21, (u32)WS_TOTAL - 4u) : (const char*)out + (size_t)(bid >> 3 & 3u) * 655360;
        u32 sv;
        asm volatile("s_mov_b32 %0, m0\n\ts_mov_b32 m0, %2\n\ts_nop 0\n\tglobal_load_lds_dword %1, off\n\ts_mov_b32 m0, %0"
                     : "=&s"(sv) : "v"(tp), "s"((u32)(uintptr_t)(__attribute__((address_space(3))) char*)smem + L_MISC + 256u) : "memory");
    }
    if (wave == 6 && (bid >> 3 & 31u) < CODE_KB / 8) {
        u32 sv;
        asm volatile("s_getpc_b64 s[98:99]\nLc%=:\n\ts_sub_u32 s98, s98, Lc%=-" KERNEL_SYM "\n\ts_subb_u32 s99, s99, 0\n\t"
                     "s_mov_b32 %0, m0\n\ts_mov_b32 m0, %2\n\ts_nop 0\n\tglobal_load_lds_dword %1, s[98:99]\n\ts_mov_b32 m0, %0"
                     : "=&s"(sv) : "v"((bid >> 3 & 31u) * 8192u + (threadIdx.x & 63u) * 128u),
                       "s"((u32)(uintptr_t)(__attribute__((address_space(3))) char*)smem + L_MISC + 768u) : "s98", "s99", "scc", "memory");
    }
    u64* claim = (u64*)(ws + OFF_CLAIM);
    u32* sh = (u32*)(smem + L_MISC);
    auto try_claim = [&] {
        if (wave == 0 && first) {
            asm volatile("s_load_dwordx2 %0, %1, 0x0\n\ts_waitcnt lgkmcnt(0)" : "=s"(gen) : "s"(ws + OFF_GEN) : "memory");
            tag = (gen + 0x9E3779B97F4A7C15ull) * 0xBF58476D1CE4E5B9ull;
            tag ^= tag >> 31;
            tag *= 0x94D049BB133111EBull;
            tag ^= tag >> 29;
            if (lane_id() == 0) {
                old = __hip_atomic_exchange(claim + b * 8, tag, __ATOMIC_RELAXED, __HIP_MEMORY_SCOPE_AGENT);
                sh[1] = (u32)tag; sh[2] = (u32)(tag >> 32); sh[3] = (u32)gen; sh[4] = (u32)(gen >> 32);
            }
        }
    };
    auto settle = [&] {
        const int tid = wave * 64 + lane_id();
        if (tid == 0 && first) {
            u32 vb = (u32)b;
#pragma unroll 1
            for (int i = 0; i < 256 && old == tag; ++i) {
                vb = (vb + 1) & 255u;
                old = __hip_atomic_exchange(claim + vb * 8, tag, __ATOMIC_RELAXED, __HIP_MEMORY_SCOPE_AGENT);
            }
            sh[0] = vb;
        }
        LBAR();
        const u32 bc = __builtin_amdgcn_readfirstlane(sh[0]);
        asm volatile("s_cmp_eq_u32 %0, %1\n\ts_cbranch_scc1 Lgo%=\n\t"
                     "s_or_b32 s2, %0, 0x80000000\n\ts_mov_b64 s[0:1], %2\n\tv_mov_b32 v0, %3\n\ts_waitcnt vmcnt(0) lgkmcnt(0)\n\t"
                     "s_getpc_b64 s[4:5]\nLpc%=:\n\ts_sub_u32 s4, s4, Lpc%=-" KERNEL_SYM "\n\ts_subb_u32 s5, s5, 0\n\ts_setpc_b64 s[4:5]\nLgo%=:"
                     :: "s"(bc), "s"(b), "s"(__builtin_amdgcn_kernarg_segment_ptr()), "v"(tid) : "s2", "scc", "memory");
        tag = (u64)(u32)__builtin_amdgcn_readfirstlane(sh[1]) | ((u64)(u32)__builtin_amdgcn_readfirstlane(sh[2]) << 32);
    };
    if (b & 4) {
        const int wid = k * 4 + xg, h = (wid & 3) * 8 + (wid >> 4), par = (wid >> 2) & 1;
        const bool isqk = ((wid >> 3) & 1) == 0;
        const int lane = lane_id();
        u32x32 W[5];
        u32 wp = 0;
        u32x2 f0;
        u64 tg2 = 0;
        bool got = false;
        auto xtile = [&](int t) {
#pragma unroll
            for (int i = 0; i < 5; ++i)
                dma16_sc1(ws + OFF_XNP + (size_t)(par + 2 * t) * 40960 + (wave * 5 + i) * 1024 + lane * 16,
                          __builtin_amdgcn_readfirstlane((u32)(uintptr_t)(__attribute__((address_space(3))) char*)smem + xbuf(t) + (u32)(wave * 5 + i) * 1024));
        };
        load_weights<ORD_A>([&](int st) {
            if (st == 0) try_claim();
            if (st == LOOKST) {
                asm volatile("global_load_dwordx2 %0, %1, off sc1" : "=&v"(f0) : "v"(ws + OFF_SUM + xcc * 128u) : "memory");
                tg2 = (u64)(u32)__builtin_amdgcn_readfirstlane(sh[1]) | ((u64)(u32)__builtin_amdgcn_readfirstlane(sh[2]) << 32);
            }
            if (st == 3) {
                asm volatile("" : "+v"(f0));
                got = __all(f0[1] == cal_tag(tg2, f0[0]));
                if (got) { xtile(0); xtile(1); }
                wp = to_sgpr(f0[0]);
            }
        }, (isqk ? Wq : Wv) + h * DH_, isqk ? Wk + h * DH_ : Wo + (size_t)h * DH_ * D_, !isqk, smem, wave, lane,
           wave >> 2, 2 * (wave & 3), wave >> 2, 2 * (wave & 3) + 1, W);
        settle();
        const rsrc_t rs = mk_rsrc(ws);
        if (!got) { wp = wait_init(rs, tag, 0); xtile(0); xtile(1); }
        sh[16] = sh[17] = 0u;
        DRAIN();
        LBAR();
        PIN_W(W);
        worker_role(wave, wid, tag, wp, x, W, wg, wu, wd, ws, out, smem);
        __builtin_amdgcn_endpgm();
    }
    const int hl = ((k >> 1) ^ (xg >> 1)) & 1, j = (k >> 2) * 2 + (k & 1);
    if (CFG ? xg < 2 : j < 8) {
        const int h = CFG ? xg * 16 + (j >> 3) * 8 + hl * 4 + ((j >> 1) & 3) : xg * 8 + hl * 4 + (j >> 1);
        u32x32 W[5];
        auto hook = [&](int st) {
            if (st == 0) try_claim();
        };
        if ((j & 1) == 0) {
            load_weights<ORD_C>(hook, Wq + h * DH_, Wk + h * DH_, false, smem, wave, lane_id(), wave < 4, wave & 3, wave < 4, 4 + (wave & 3), W);
            settle();
            PIN_W(W);
            s_role(wave, h, xg, tag, mem0, W, ws, smem);
            __builtin_amdgcn_endpgm();
        }
        if (wave < 4) { load_weights<ORD_C>(hook, Wv + h * DH_, Wo + (size_t)h * DH_ * D_, true, smem, wave, lane_id(), 0, wave, 0, 4 + wave, W); PIN_W(W); }
        else { load_weights<ORD_C | (1 << 21)>(hook, Wv + h * DH_, Wo + (size_t)h * DH_ * D_, true, smem, wave, lane_id(), 1, 2 * (wave & 3), 1, 2 * (wave & 3) + 1, W); PIN_W(W); }
        settle();
        PIN_W(W);
        v_role(wave, h, xg, tag, mem0, W, ws, smem);
        __builtin_amdgcn_endpgm();
    }
    constexpr int NR = CFG ? 10 : 5, NS = CFG ? 6 : 3, NP = CFG ? 4 : 2;
    const int jr = CFG ? j : j - 8, hi = (CFG ? xg - 2 : xg) * 2 + hl;
    try_claim();
    init_role([&] { settle(); return tag; }, wave, jr < NR ? hi * NR + jr : jr == NR ? 40 + hi : 48 + hi * NP + jr - (NR + NS - NP), xcc, ((se ^ xcc) >> 1) & 1u, x, w_r, Wq, Wk, Wv, Wo, ws, smem);
    gen = (u64)sh[3] | ((u64)sh[4] << 32);
    tag = (u64)(u32)__builtin_amdgcn_readfirstlane(sh[1]) | ((u64)(u32)__builtin_amdgcn_readfirstlane(sh[2]) << 32);
    const int wi = hl * NS + jr - NR;
    const bool aux = wi == 2 * NS - 1;
    if (jr < NR) { if (wave < 6) r_role(wave, tile_of(xg, hl, jr), xg, wait_init(mk_rsrc(ws), tag, 1), ws, smem); }
    else if (!aux) {
        if (wave >= 4) { if (wi == 0 && xg == 0 && wave == 7) topk_role(tag, ws); return; }
        const u32 wp = wait_init(mk_rsrc(ws), tag, 1);
        LBAR();
        if (wave == 0 && wi == 0 && lane_id() == 0) __hip_atomic_fetch_add((u32*)(ws + OFF_CNT + 768), 1u, __ATOMIC_RELAXED, __HIP_MEMORY_SCOPE_AGENT);
        if (wave == 2 && wi) fill_role(wp, xg * 4 + wi - 1, ws);
        if (wave == 3 && xg == 3 && wi == 4) relay_role(wp, xg, ws);
        if (wave == 1 || (wave == 2 && !wi)) warm_role(wp, xg, wi - wave, ws, smem);
    }
    else if (wave == 1) {
        const u32 wp = wait_init(mk_rsrc(ws), tag, 1);
        if (lane_id() == 0) __hip_atomic_fetch_add((u32*)(ws + OFF_CNT + 768), 1u, __ATOMIC_RELAXED, __HIP_MEMORY_SCOPE_AGENT);
        lead_role(wp, xg, ws, smem);
    }
    else if (aux && xg == 2 && wave == 0) {
        const rsrc_t rs = mk_rsrc(ws);
        const int lane = lane_id();
#pragma unroll 1
        for (u32 spins = 0; spins < SPIN_CAP; ++spins) {
            CFENCE();
            bool ok = true;
#pragma unroll
            for (int q = 0; q < 4; ++q) {
                u32x2 f = ldx2(rs, (u32)(OFF_CLAIM + (q * 64 + lane) * 64));
                ok = ok && f[0] == (u32)tag && f[1] == (u32)(tag >> 32);
            }
            if (__all(ok)) break;
            __builtin_amdgcn_s_sleep(32);
        }
        if (lane == 0) __hip_atomic_store((u64*)(ws + OFF_GEN), gen + 1ull, __ATOMIC_RELAXED, __HIP_MEMORY_SCOPE_AGENT);
#pragma unroll
        for (int q = 0; q < 4; ++q) stx2(rs, (u32)(OFF_CLAIM + (q * 64 + lane) * 64), (u32x2){~(u32)tag, 0u});
    }
}

extern "C" void kernel_launch(void* const* d_in, const int* in_sizes, int n_in, void* d_out, int out_size,
                              void* d_ws, size_t ws_size, hipStream_t stream) {
    const u16* x = (const u16*)d_in[0];
    const u16* mem0 = (const u16*)d_in[1];
    const u16* Wq = (const u16*)d_in[2];
    const u16* Wk = (const u16*)d_in[3];
    const u16* Wv = (const u16*)d_in[4];
    const u16* Wo = (const u16*)d_in[5];
    const u16* w_r = (const u16*)d_in[8];
    const u16* w_gate = (const u16*)d_in[9];
    const u16* w_up = (const u16*)d_in[10];
    const u16* w_down = (const u16*)d_in[11];
    char* ws = (char*)d_ws;
    u16* out = (u16*)d_out;

    hipFuncSetAttribute((const void*)fused_kernel, hipFuncAttributeMaxDynamicSharedMemorySize, FUSED_LDS);
    fused_kernel<<<256, 512, FUSED_LDS, stream>>>(x, mem0, w_r, Wq, Wk, Wv, Wo, w_gate, w_up, w_down, ws, out);
}
```

```cpp
#define CFG 0
#include <hip/hip_runtime.h>
#include <hip/hip_bf16.h>
#include <stdint.h>

constexpr int S_ = 2048, D_ = 640, H_ = 32, DH_ = 128, NSEG = 64, SEGL = 32, FF_ = 1728, TOPC = 8;
constexpr int HD_ = H_ * DH_;
constexpr int NRED = 40;

typedef unsigned short u16;
typedef unsigned int u32;
typedef unsigned long long u64;
typedef __attribute__((ext_vector_type(8))) __bf16 bf16x8;
typedef __attribute__((ext_vector_type(16))) __bf16 bf16x16;
typedef __attribute__((ext_vector_type(4))) float f32x4;
typedef __attribute__((ext_vector_type(4))) unsigned int u32x4;
typedef __attribute__((ext_vector_type(8))) unsigned int u32x8;
typedef __attribute__((ext_vector_type(2))) unsigned int u32x2;

__device__ __forceinline__ float bf2f(u16 h) { return __uint_as_float(((u32)h) << 16); }
typedef __bf16 bf16x2_t __attribute__((ext_vector_type(2)));
typedef float f32x2_t __attribute__((ext_vector_type(2)));
__device__ __forceinline__ u16 f2bf(float f) {
    __bf16 h = (__bf16)f;
    return __builtin_bit_cast(u16, h);
}
__device__ __forceinline__ float bfr(float f) { return (float)(__bf16)f; }
__device__ __forceinline__ u32 pack2(float a, float b) {
    f32x2_t f = {a, b};
    bf16x2_t h = __builtin_convertvector(f, bf16x2_t);
    return __builtin_bit_cast(u32, h);
}
__device__ __forceinline__ bf16x8 as_bf(u32x4 v) { return __builtin_bit_cast(bf16x8, v); }
__device__ __forceinline__ f32x4 mfma16(u32x4 a, u32x4 b, f32x4 c) {
    return __builtin_amdgcn_mfma_f32_16x16x32_bf16(as_bf(a), as_bf(b), c, 0, 0, 0);
}
__device__ __forceinline__ f32x4 smf(u32x4 a, u32x4 b0, u32x4 b1, f32x4 c, int idx) {
    u32x8 b = {b0[0], b0[1], b0[2], b0[3], b1[0], b1[1], b1[2], b1[3]};
    return __builtin_amdgcn_smfmac_f32_16x16x64_bf16(as_bf(a), __builtin_bit_cast(bf16x16, b), c, idx, 0, 0);
}
__device__ __forceinline__ int dmap(int p) {
    int q = p & 15;
    return (p & ~15) | (q & 1) | ((q & 2) << 2) | ((q & 4) >> 1) | ((q & 8) >> 1);
}

constexpr size_t al256(size_t x) { return (x + 255) & ~(size_t)255; }
constexpr size_t OFF_XN = 0;
constexpr size_t OFF_XNP = OFF_XN + al256((size_t)S_ * D_ * 2);
constexpr size_t OFF_R = OFF_XNP + al256((size_t)S_ * D_ * 2);
constexpr size_t OFF_PX = OFF_R + al256((size_t)S_ * 4);
constexpr size_t OFF_PXM = OFF_PX + al256((size_t)NSEG * H_ * 2048);
constexpr size_t OFF_HB = OFF_PXM + al256((size_t)NSEG * 8 * 2048);
constexpr size_t OFF_UPD = OFF_HB + al256((size_t)8 * FF_ * 2);
constexpr size_t OFF_GEN = OFF_UPD + al256((size_t)8 * D_ * 2);
constexpr size_t OFF_CLAIM = OFF_GEN + 256;
constexpr size_t OFF_INI = OFF_CLAIM + 16384;
constexpr size_t OFF_SUM = OFF_INI + 2048;
constexpr size_t OFF_ERR = OFF_SUM + 3072;
constexpr size_t OFF_FA = OFF_ERR + 256;
constexpr size_t OFF_FB = OFF_FA + 4096;
constexpr size_t OFF_FP = OFF_FB + 4096;
constexpr size_t OFF_FZ = OFF_FP + 1024;
constexpr size_t OFF_RDY = OFF_FZ + 2048;
constexpr size_t OFF_IDX = OFF_RDY + 1024;
constexpr size_t OFF_CNT = OFF_IDX + 256;
constexpr size_t ZERO_BEGIN = OFF_ERR, ZERO_END = OFF_CNT + 1024;
constexpr size_t OFF_WIN = (size_t)12 << 20;
constexpr size_t WS_TOTAL = OFF_WIN + ((size_t)188 << 20);
static_assert(ZERO_END <= OFF_WIN && WS_TOTAL <= ((size_t)256 << 20), "ws");

constexpr u32 SLOT0 = 52u << 20;
constexpr u32 SL_P = 4 * 32768, SL_TS = 2 * 32768 + 1024;
constexpr u32 FAR_BX = 1u << 20;
constexpr u32 NEAR_QX = 59u << 20;
constexpr u32 NEAR_KX = NEAR_QX + NSEG * H_ * 8192;
static_assert(2 * ((size_t)NEAR_KX + (size_t)NSEG * H_ * 8192) + ((size_t)2 << 20) <= WS_TOTAL - OFF_WIN && SLOT0 + (64u << 20) <= 2 * NEAR_QX
              && 2 * ((size_t)FAR_BX + (size_t)NSEG * NRED * 32768) + ((size_t)2 << 20) <= WS_TOTAL - OFF_WIN, "far");
__device__ __forceinline__ u32 slot_off(u32 wp, int s) {
    u32 r, t;
    asm("s_lshr_b32 %0, %3, 1\n\ts_xor_b32 %0, %0, %2\n\ts_and_b32 %0, %0, 1\n\ts_lshl_b32 %0, %0, 13\n\ts_lshl_b32 %1, %3, 20\n\t"
        "s_add_u32 %0, %0, %1\n\ts_andn2_b32 %1, %2, 15\n\ts_add_u32 %0, %0, %1\n\ts_add_u32 %0, %0, %4" : "=&s"(r), "=&s"(t) : "s"(wp), "s"(s), "n"(SLOT0) : "scc");
    return r;
}
__device__ __forceinline__ u32 slot_z(u32 wp, u32 s) { return (wp & ~15u) + SLOT0 + (s << 20) + (((wp ^ (s >> 1)) & 1u) << 13); }
__device__ __forceinline__ u32 slot_v(u32 wp, u32 s) { return slot_z(wp, s) + ((((wp >> 3) ^ (s >> 5)) & 1u) << 18); }
__device__ __forceinline__ u32 far_v(u32 wp, u32 L) { return (wp & ~15u) + 2 * L + ((~(wp ^ (L >> 20)) & 1u) << 13); }
__device__ __forceinline__ u32 tab(u32 t, int s) { return __builtin_amdgcn_readlane(t, s); }
__device__ __forceinline__ u32 copy_off(u32 c, u32 s) { return ((c & 1u) << 14) | ((((c >> 1) ^ s) & 1u) << 12); }
__device__ __forceinline__ u32 z_off(u32 wp, u32 ct) {
    const u32 zb = ct >> 1, pg = zb < 16 ? zb + 16 : 6 + (zb & 1u) + (zb & 2u) * 4;
    return pg * 32768 + (((ct ^ (wp >> 1) ^ (pg >> 4)) & 1u) << 14);
}
constexpr int NCP = CFG ? 2 : 4;
__device__ __forceinline__ u32 put_off(u32 wp, u32 x0, u32 s) { return CFG ? copy_off(x0 ^ (wp >> 1), s) & 4096u : 0u; }
__device__ __forceinline__ u32 cpy(u32 c) { return CFG ? (c & 1u) * 16384u : (c & 1u) * 4096u + (c >> 1) * 16384u; }
__device__ __forceinline__ u32 far_off(u32 wp, u32 L) {
    u32 r, t;
    asm("s_lshr_b32 %0, %3, 20\n\ts_xnor_b32 %0, %0, %2\n\ts_and_b32 %0, %0, 1\n\ts_lshl_b32 %0, %0, 13\n\ts_lshl_b32 %1, %3, 1\n\t"
        "s_add_u32 %0, %0, %1\n\ts_andn2_b32 %1, %2, 15\n\ts_add_u32 %0, %0, %1" : "=&s"(r), "=&s"(t) : "s"(wp), "s"(L) : "scc");
    return r;
}
constexpr u32 SENT = 0xffbfffbfu;
constexpr u32 MARK = 0xffbeffbeu;
constexpr u32 OOB = 0x80000000u;
__device__ __forceinline__ u32 amap(u32 L) { return ((L >> 13) << 14) | (L & 8191u); }
__device__ __forceinline__ u32 win_off(const char* ws) {
    u64 a = (u64)(uintptr_t)ws;
    u64 w = (a + OFF_WIN + ((u64)2 << 20) - 1) & ~(((u64)2 << 20) - 1);
    return (u32)(w - a);
}

constexpr int AUX_SC1 = 16;
typedef __amdgpu_buffer_rsrc_t rsrc_t;
__device__ __forceinline__ rsrc_t mk_rsrc(const void* p) {
    return __builtin_amdgcn_make_buffer_rsrc((void*)p, 0, 0x7fffffff, 0x00020000);
}
__device__ __forceinline__ u32x4 ldx4(rsrc_t r, u32 off, u32 so = 0) { return __builtin_amdgcn_raw_buffer_load_b128(r, off, so, AUX_SC1); }
__device__ __forceinline__ u32x2 ldx2(rsrc_t r, u32 off, u32 so = 0) { return __builtin_amdgcn_raw_buffer_load_b64(r, off, so, AUX_SC1); }
__device__ __forceinline__ u32 ldx1(rsrc_t r, u32 off, u32 so = 0) { return __builtin_amdgcn_raw_buffer_load_b32(r, off, so, AUX_SC1); }
__device__ __forceinline__ void stx4(rsrc_t r, u32 off, u32x4 v, u32 so = 0) { __builtin_amdgcn_raw_buffer_store_b128(v, r, off + so, 0, AUX_SC1); }
__device__ __forceinline__ void stx2(rsrc_t r, u32 off, u32x2 v, u32 so = 0) { __builtin_amdgcn_raw_buffer_store_b64(v, r, off, so, AUX_SC1); }
__device__ __forceinline__ void stx1(rsrc_t r, u32 off, u32 v, u32 so = 0) { __builtin_amdgcn_raw_buffer_store_b32(v, r, off, so, AUX_SC1); }
#define DRAIN() asm volatile("s_waitcnt vmcnt(0)" ::: "memory")
#define CFENCE() asm volatile("" ::: "memory")
#define LBAR() asm volatile("s_waitcnt lgkmcnt(0)\n\ts_barrier" ::: "memory")

constexpr u32 L_MISC = 147456;
constexpr u32 SPIN_CAP = 1u << 18;
__device__ __forceinline__ int lane_id() {
    int l;
    asm volatile("v_mbcnt_lo_u32_b32 %0, -1, 0\n\tv_mbcnt_hi_u32_b32 %0, -1, %0" : "=v"(l));
    __builtin_assume(l >= 0 && l < 64);
    return l;
}

__device__ __forceinline__ float xmax16(float x) {
    u32 a = __float_as_uint(x);
    auto r = __builtin_amdgcn_permlane16_swap(a, a, false, false);
    return fmaxf(__uint_as_float(r[0]), __uint_as_float(r[1]));
}
__device__ __forceinline__ float xmax32(float x) {
    u32 a = __float_as_uint(x);
    auto r = __builtin_amdgcn_permlane32_swap(a, a, false, false);
    return fmaxf(__uint_as_float(r[0]), __uint_as_float(r[1]));
}
__device__ __forceinline__ float xadd16(float x) {
    u32 a = __float_as_uint(x);
    auto r = __builtin_amdgcn_permlane16_swap(a, a, false, false);
    return __uint_as_float(r[0]) + __uint_as_float(r[1]);
}
__device__ __forceinline__ float xadd32(float x) {
    u32 a = __float_as_uint(x);
    auto r = __builtin_amdgcn_permlane32_swap(a, a, false, false);
    return __uint_as_float(r[0]) + __uint_as_float(r[1]);
}
__device__ __forceinline__ u32 umax3(u32 a, u32 b, u32 c) { return max(max(a, b), c); }

__device__ __forceinline__ void wait_flags(rsrc_t rs, u32 off, bool act, u32 target, bool& dead, u32* errw, u32 code, int lane) {
    u32 spins = 0;
#pragma unroll 1
    while (!dead) {
        CFENCE();
        u32x4 f = ldx4(rs, off);
        bool ok = !act || ((f[0] >= target) & (f[1] >= target) & (f[2] >= target) & (f[3] >= target));
        if (__all(ok)) break;
        __builtin_amdgcn_s_sleep(2);
        if (++spins > SPIN_CAP) { if (lane == 0) atomicOr(errw, code); dead = true; }
        if ((spins & 63) == 0 && ldx1(rs, (u32)OFF_ERR) != 0u) dead = true;
    }
    CFENCE();
}
__device__ __forceinline__ void wait_word(rsrc_t rs, u32 off, bool act, u32 target, bool& dead, u32* errw, u32 code, int lane) {
    u32 spins = 0;
#pragma unroll 1
    while (!dead) {
        CFENCE();
        u32 f = ldx1(rs, off);
        if (__all(!act || f >= target)) break;
        __builtin_amdgcn_s_sleep(4);
        if (++spins > SPIN_CAP) { if (lane == 0) atomicOr(errw, code); dead = true; }
        if ((spins & 63) == 0 && ldx1(rs, (u32)OFF_ERR) != 0u) dead = true;
    }
    CFENCE();
}

__device__ __forceinline__ u32 cal_tag(u64 tag, u32 v) { return ((u32)tag ^ (u32)(tag >> 32)) + v * 0x9E3779B1u; }
__device__ __forceinline__ u32 to_sgpr(u32 x) {
    u32 r;
    asm volatile("s_nop 0\n\tv_readfirstlane_b32 %0, %1\n\ts_nop 4" : "=s"(r) : "v"(x));
    return r;
}
__device__ __forceinline__ u32 wp_of(const char* ws, u32 v) {
    v += __builtin_amdgcn_update_dpp(0u, v, 0xb1, 0xf, 0xf, false);
    v += __builtin_amdgcn_update_dpp(0u, v, 0x4e, 0xf, 0xf, false);
    v += __builtin_amdgcn_update_dpp(0u, v, 0x124, 0xf, 0xf, false);
    v += __builtin_amdgcn_update_dpp(0u, v, 0x128, 0xf, 0xf, false);
    const u32 r = __builtin_amdgcn_readlane(v, 48) + 0x0f0f0f0fu;
    return to_sgpr(win_off(ws) | ((r >> 7) & 1u) | ((r >> 14) & 2u) | ((r >> 21) & 4u) | ((r >> 28) & 8u));
}
__device__ __forceinline__ u32 sum_off(int level) {
    u32 xcc;
    asm volatile("s_getreg_b32 %0, hwreg(HW_REG_XCC_ID, 0, 3)" : "=s"(xcc));
    return (u32)(OFF_SUM + level * 1024) + xcc * 128u;
}
__device__ __forceinline__ u32 wait_init(rsrc_t rs, u64 tag, int level) {
    const u32 so = sum_off(level);
    u32x2 c = {0u, 0u};
#pragma unroll 1
    for (u32 spins = 0; spins < SPIN_CAP; ++spins) {
        CFENCE();
        c = ldx2(rs, 0u, so);
        if (__all(c[1] == cal_tag(tag, c[0]))) break;
        __builtin_amdgcn_s_sleep(6);
    }
    CFENCE();
    return to_sgpr(min(c[0], (u32)OFF_WIN + 0x20000fu));
}

#define STAMPS() do { } while (0)
#define STAMP(k) do { } while (0)
#define RSTAMP(k) do { } while (0)
#define STAMP2(k, r) do { } while (0)
#define FLUSH() do { } while (0)
#define LOGS() do { } while (0)
#define LOG() do { } while (0)
#define LOGOUT(cu) do { } while (0)
#define CFLUSH() do { } while (0)
#define XSTAMP(w, sl) do { } while (0)
#define WSTAMP(id) do { } while (0)
#define CSTAMP(id) do { } while (0)
#define NPROBE 2
constexpr u32 INI_ZERO_CHUNKS = (u32)((ZERO_END - ZERO_BEGIN) / 16);
static_assert(INI_ZERO_CHUNKS <= 8 * 256, "zero");

__device__ __forceinline__ float rsum16(float x) {
    x += __uint_as_float(__builtin_amdgcn_update_dpp(0u, __float_as_uint(x), 0xb1, 0xf, 0xf, false));
    x += __uint_as_float(__builtin_amdgcn_update_dpp(0u, __float_as_uint(x), 0x4e, 0xf, 0xf, false));
    x += __uint_as_float(__builtin_amdgcn_update_dpp(0u, __float_as_uint(x), 0x124, 0xf, 0xf, false));
    x += __uint_as_float(__builtin_amdgcn_update_dpp(0u, __float_as_uint(x), 0x128, 0xf, 0xf, false));
    return x;
}

__device__ __forceinline__ void ln_regs(u32x4 (&xv)[5]) {
    float sum = 0.f;
#pragma unroll
    for (int i = 0; i < 5; ++i)
#pragma unroll
        for (int j = 0; j < 4; ++j) sum += __uint_as_float(xv[i][j] << 16) + __uint_as_float(xv[i][j] & 0xffff0000u);
    const float mean = rsum16(sum) * (1.0f / D_);
    float vs = 0.f;
#pragma unroll
    for (int i = 0; i < 5; ++i)
#pragma unroll
        for (int j = 0; j < 4; ++j) {
            float d0 = __uint_as_float(xv[i][j] << 16) - mean, d1 = __uint_as_float(xv[i][j] & 0xffff0000u) - mean;
            vs += d0 * d0 + d1 * d1;
        }
    const float var = bfr(rsum16(vs) * (1.0f / D_));
    const float m_bf = bfr(mean);
    const float rden = 1.0f / bfr(sqrtf(bfr(var + 1e-05f)));
#pragma unroll
    for (int i = 0; i < 5; ++i)
#pragma unroll
        for (int j = 0; j < 4; ++j)
            xv[i][j] = pack2(bfr(__uint_as_float(xv[i][j] << 16) - m_bf) * rden, bfr(__uint_as_float(xv[i][j] & 0xffff0000u) - m_bf) * rden);
}
__device__ __forceinline__ u32x4 ln_perm(const u32x4 o, const int n) {
    const u32 s0 = (n & 1) ? o[0] : o[2], s1 = (n & 1) ? o[1] : o[3];
    const u32 t0 = __builtin_amdgcn_update_dpp(0u, s0, 0xb1, 0xf, 0xf, false), t1 = __builtin_amdgcn_update_dpp(0u, s1, 0xb1, 0xf, 0xf, false);
    return (n & 1) ? (u32x4){t0, o[2], t1, o[3]} : (u32x4){o[0], t0, o[1], t1};
}
__device__ __forceinline__ u32 img_off(const int row, const int n, const int i) {
    return (u32)((row >> 4) * 20480 + (4 * i + (n >> 2)) * 1024 + (n & 3) * 256 + (row & 15) * 16);
}
__device__ __forceinline__ void ln_load(const int row0, const int lane, const u16* __restrict__ x, const u16* __restrict__ w_r, u32x4 (&xv)[5], u32x4 (&wv)[5]) {
    const int n = lane & 15, row = row0 + (lane >> 4);
#pragma unroll
    for (int i = 0; i < 5; ++i) xv[i] = *(const u32x4*)(x + (size_t)row * D_ + (n + 16 * i) * 8);
#pragma unroll
    for (int i = 0; i < 5; ++i) wv[i] = *(const u32x4*)(w_r + (n + 16 * i) * 8);
}
__device__ __forceinline__ void ln_store(const int row0, const int lane, const u32x4 (&xv)[5], const u32x4 (&wv)[5], rsrc_t rs, const bool img = true) {
    const int n = lane & 15, row = row0 + (lane >> 4);
#pragma unroll
    for (int i = 0; i < 5; ++i) if (img) stx4(rs, (u32)OFF_XNP + img_off(row, n, i), ln_perm(xv[i], n));
    CFENCE();
    float dot = 0.f;
#pragma unroll
    for (int i = 0; i < 5; ++i) {
        stx4(rs, (u32)OFF_XN + (u32)(row * D_ + (n + 16 * i) * 8) * 2, xv[i]);
#pragma unroll
        for (int j = 0; j < 4; ++j)
            dot += __uint_as_float(xv[i][j] << 16) * __uint_as_float(wv[i][j] << 16) + __uint_as_float(xv[i][j] & 0xffff0000u) * __uint_as_float(wv[i][j] & 0xffff0000u);
    }
    dot = rsum16(dot);
    stx1(rs, n == 0 ? (u32)(OFF_R + row * 4) : OOB, __float_as_uint(bfr(dot)));
}
__device__ __forceinline__ u32x4 mk_srd(const char* ws) {
    const u64 a = (u64)(uintptr_t)ws;
    return (u32x4){(u32)a, (u32)(a >> 32), 0x7fffffffu, 0x00020000u};
}

template <typename F>
__device__ __forceinline__ u32 probe(F&& early, const int pi, const u32 xcc, const u32 hl, char* __restrict__ ws) {
    const u32x4 srd = mk_srd(ws);
    const u32 base = win_off(ws) + SLOT0 + (8u << 20) + 6u * 32768u + (u32)pi * 128u;
    const int lane = lane_id();
    const u32 zero = 0;
    asm volatile("buffer_atomic_or %0, %1, %2, %3 offen sc1\n\ts_waitcnt vmcnt(0)" :: "v"(zero), "v"(lane < 16 ? (u32)lane * 2048u : OOB), "s"(srd), "s"(base) : "memory");
    const u32 one = lane == 0 ? 0u : OOB;
    auto lat = [&](u32 q) {
        u64 t0, t1;
        asm volatile("s_memtime %0\n\tbuffer_atomic_or %2, %3, %4, %5 offen sc1\n\ts_waitcnt vmcnt(0)\n\ts_memtime %1\n\ts_waitcnt lgkmcnt(0)"
                     : "=&s"(t0), "=&s"(t1) : "v"(zero), "v"(one), "s"(srd), "s"(base + q * 2048u) : "memory");
        return (u32)t1 - (u32)t0;
    };
    const u32 q0 = (u32)pi & 11u, a0 = lat(q0), a1 = lat(q0 | 4u), a2 = lat(q0 ^ 8u), a3 = lat((q0 ^ 8u) | 4u), a4 = lat(q0 ^ 2u), a5 = lat((q0 ^ 2u) | 4u);
    const int d13 = (int)min(min(a0, a2), a4) - (int)min(min(a1, a3), a5), v13 = min(max(d13 >> 4, -7), 7);
    const u32 b13 = d13 > 0 ? 4u : 0u;
    const u32 flip = (xcc >> 2) | ((xcc & 3u) << 1) | (hl << 3);
    u32 votes = (u32)(7 + ((flip & 1u) ? -v13 : v13));
    early(votes);
    u32 best = ~0u;
#pragma unroll 1
    for (u32 i = 0; i < 8 * NPROBE; ++i) {
        const u32 c = (i + (u32)pi) & 7u, t = lat((c & 3u) | b13 | (c & 4u) * 2u);
        if ((u32)lane == c) best = min(best, t);
    }
#pragma unroll
    for (u32 i = 1; i < 4; ++i) {
        const u32 bit = 8u >> i;
        const int d = (int)best - __shfl_xor((int)best, (int)bit);
        int v = 0;
#pragma unroll
        for (u32 c = 0; c < 8; ++c) if (!(c & bit)) v += min(max(__builtin_amdgcn_readlane(d, c), -28), 28);
        v = (v + 8) >> 4;
        if ((flip >> i) & 1u) v = -v;
        votes |= (u32)(xcc >= 4 ? 7 : 7 + v) << (8 * i);
    }
    return votes;
}

typedef __attribute__((ext_vector_type(32))) unsigned int u32x32;
__device__ __forceinline__ u32x4 wfrag(const u32x32 (&W)[5], const int i) {
    return (u32x4){W[i >> 3][(i & 7) * 4], W[i >> 3][(i & 7) * 4 + 1], W[i >> 3][(i & 7) * 4 + 2], W[i >> 3][(i & 7) * 4 + 3]};
}
#define PIN_W(W) asm volatile("" : "+{v[16:47]}"(W[0]), "+{v[48:79]}"(W[1]), "+{v[80:111]}"(W[2]), "+{v[112:143]}"(W[3]), "+{v[144:175]}"(W[4]))
typedef __attribute__((ext_vector_type(4))) short s16x4;
typedef __attribute__((address_space(3))) s16x4* lds_s16x4p;
__device__ __forceinline__ u32x2 trd(u32 ldsaddr) {
    return __builtin_bit_cast(u32x2, __builtin_amdgcn_ds_read_tr16_b64_v4i16((lds_s16x4p)ldsaddr));
}
template <bool NT>
__device__ __forceinline__ void dma16(const void* g, u32 lds_base) {
    u32 sv;
    if (NT)
        asm volatile("s_mov_b32 %0, m0\n\ts_mov_b32 m0, %2\n\ts_nop 0\n\tglobal_load_lds_dwordx4 %1, off nt\n\ts_mov_b32 m0, %0"
                     : "=&s"(sv) : "v"(g), "s"(lds_base) : "memory");
    else
        asm volatile("s_mov_b32 %0, m0\n\ts_mov_b32 m0, %2\n\ts_nop 0\n\tglobal_load_lds_dwordx4 %1, off\n\ts_mov_b32 m0, %0"
                     : "=&s"(sv) : "v"(g), "s"(lds_base) : "memory");
}
__device__ __forceinline__ void dma16_sc1(const void* g, u32 lds_base) {
    u32 sv;
    asm volatile("s_mov_b32 %0, m0\n\ts_mov_b32 m0, %2\n\ts_nop 0\n\tglobal_load_lds_dwordx4 %1, off sc1\n\ts_mov_b32 m0, %0"
                 : "=&s"(sv) : "v"(g), "s"(lds_base) : "memory");
}
template <int ORD, typename F>
__device__ __forceinline__ void load_weights(F&& after, const u16* __restrict__ p0, const u16* __restrict__ p1, const bool wo1, char* smem,
                                             const int wave, const int lane, const int sa, const int ta, const int sb, const int tb,
                                             u32x32 (&W)[5]) {
    const u32 lbase = (u32)(uintptr_t)(__attribute__((address_space(3))) char*)smem;
    const int strm = wave >> 2;
    const bool wo = wo1 && strm == 1;
    const u16* src = strm ? p1 : p0;
    const u32 sstep = wo ? 128u : 128u * HD_;
    u32 so[8];
#pragma unroll
    for (int i = 0; i < 8; ++i) {
        const int j = (wave & 3) * 8 + i, r = 4 * j + (lane >> 4);
        const int f = (lane >> 4) | (((j >> 1) & 1) << 2);
        const int ch = (lane & 15) ^ (f << 1);
        const int e = (r & ~31) | (((r >> 2) & 1) << 4) | (((r >> 3) & 3) << 2) | (r & 3);
        so[i] = wo ? (u32)(e * D_ + ch * 8) : (u32)(dmap(r) * HD_ + ch * 8);
    }
    auto issue = [&](int st) {
        const u32 dst = __builtin_amdgcn_readfirstlane(lbase + (u32)((st & 1) * 65536 + strm * 32768 + (wave & 3) * 8192));
#pragma unroll
        for (int i = 0; i < 8; ++i) dma16<((ORD >> 20) & 1) != 0>(src + so[i] + (size_t)((ORD >> (4 * st)) & 15) * sstep, dst + i * 1024);
    };
    const int g = lane >> 4, q = (lane >> 2) & 3, p = lane & 3;
    const int f = q | ((g & 1) << 2);
    const u32 ra = lbase + (u32)(sa * 32768 + (8 * g + q) * 256 + (((ta ^ f) << 1) | (p >> 1)) * 16 + (p & 1) * 8);
    const u32 rb = lbase + (u32)(sb * 32768 + (8 * g + q) * 256 + (((tb ^ f) << 1) | (p >> 1)) * 16 + (p & 1) * 8);
    issue(0);
    issue(1);
#pragma unroll
    for (int st = 0; st < 5; ++st) {
        if (st < 4) asm volatile("s_waitcnt vmcnt(8)" ::: "memory"); else asm volatile("s_waitcnt vmcnt(0)" ::: "memory");
        LBAR();
#pragma unroll
        for (int k = 0; k < 4; ++k) {
            const u32 o = (u32)((st & 1) * 65536 + k * 8192);
            u32x2 a0 = trd(ra + o), a1 = trd(ra + o + 1024), b0 = trd(rb + o), b1 = trd(rb + o + 1024);
            const int ia = (((ORD >> (4 * st)) & 15) * 4 + k) * 4, ib = 80 + ia;
            if ((ORD >> 21) & 1) {
                u32x32& w = W[(ORD >> (4 * st)) & 15];
                w[k * 8] = a0[0]; w[k * 8 + 1] = b0[0]; w[k * 8 + 2] = a0[1]; w[k * 8 + 3] = b0[1];
                w[k * 8 + 4] = a1[0]; w[k * 8 + 5] = b1[0]; w[k * 8 + 6] = a1[1]; w[k * 8 + 7] = b1[1];
            } else {
                W[ia >> 5][ia & 31] = a0[0]; W[ia >> 5][(ia & 31) + 1] = a0[1]; W[ia >> 5][(ia & 31) + 2] = a1[0]; W[ia >> 5][(ia & 31) + 3] = a1[1];
                W[ib >> 5][ib & 31] = b0[0]; W[ib >> 5][(ib & 31) + 1] = b0[1]; W[ib >> 5][(ib & 31) + 2] = b1[0]; W[ib >> 5][(ib & 31) + 3] = b1[1];
            }
        }
        LBAR();
        after(st + 16);
        if (st + 2 < 5) issue(st + 2);
        after(st);
    }
}

__device__ __forceinline__ void early_lines(const int t0, const u64 wq, const u64 wk, const u64 wv, const u64 wo, const u32 lds) {
#pragma unroll
    for (int q = 0; q < 3; ++q) {
        const int t = t0 + q * 320, m = t / 180, r = (t - m * 180) * 64 + lane_id(), row = r / 18, c = r - row * 18;
        const u64 w = m == 0 ? wo : m == 1 ? wv : m == 2 ? wk : wq;
        const u32 blk = ((c < 16 ? (u32)(c >> 1) * 4u + 1u : 31u) - ((u32)w >> 8)) & 31u;
        u32 sv;
        if (t < 720)
            asm volatile("s_mov_b32 %0, m0\n\ts_mov_b32 m0, %3\n\ts_nop 0\n\tglobal_load_lds_dword %1, %2\n\ts_mov_b32 m0, %0"
                         : "=&s"(sv) : "v"((u32)row * 8192u + blk * 256u + (u32)(c & 1) * 128u), "s"(w), "s"(lds) : "memory");
    }
}
template <typename F>
__device__ __forceinline__ void init_role(F&& settle, const int wave, const int ii, const u32 xcc, const u32 hl, const u16* __restrict__ x, const u16* __restrict__ w_r,
                                          const u16* Wq, const u16* Wk, const u16* Wv, const u16* Wo, char* __restrict__ ws, char* smem) {
    const int lane = lane_id(), tid = wave * 64 + lane;
    const rsrc_t rs = mk_rsrc(ws);
    u32* cnt = (u32*)(smem + L_MISC + 128);
    if (tid < 3) cnt[tid] = 0u;
    u64 tag;
    auto arrive = [&](int level) {
        if (lane == 0 && __hip_atomic_fetch_add(cnt + level, 1u, __ATOMIC_RELAXED, __HIP_MEMORY_SCOPE_WORKGROUP) == 7u)
            stx2(rs, (u32)(OFF_INI + (level * 64 + ii) * 8), (u32x2){0u, cal_tag(tag, 0u)});
    };
    if (ii >= 48) {
        if (wave == 1 && ii < 63) {
            const u32 votes = probe([&](u32 v) {
                tag = settle();
                stx2(rs, lane == 0 ? (u32)(OFF_INI + ii * 8) : OOB, (u32x2){v, cal_tag(tag, v)});
                DRAIN();
                XSTAMP(192 + ii, 220);
            }, ii - 48, xcc, hl, ws);
            XSTAMP(192 + ii, 223);
            stx2(rs, lane == 0 ? (u32)(OFF_INI + (64 + ii) * 8) : OOB, (u32x2){votes, cal_tag(tag, votes)});
        } else tag = settle();
        if (wave == 2 && ii == 63) {
            u32 wp = 0;
#pragma unroll 1
            for (int level = 0; level < 3; ++level) {
                const bool act = level == 2 ? lane < 48 : lane < 63 && (lane >= 40 || level == 1);
                u32x2 c = {0u, 0u};
                u32 spins = 0;
#pragma unroll 1
                for (; spins < SPIN_CAP; ++spins) {
                    CFENCE();
                    c = ldx2(rs, (u32)(OFF_INI + level * 512 + lane * 8));
                    if (__all(!act || c[1] == cal_tag(tag, c[0]))) break;
                }
                if (spins == SPIN_CAP) break;
                if (level < 2) wp = wp_of(ws, lane < 63 ? c[0] : 0x07070707u);
                stx2(rs, lane < 8 ? (u32)(OFF_SUM + level * 1024 + lane * 128) : OOB, (u32x2){wp, cal_tag(tag, wp)});
                XSTAMP(192 + level, 229);
            }
        }
        if (ii < 63) LBAR();
        return;
    }
    u32x4 xv[5], wv[5];
    if (ii < 40) {
        early_lines(wave * 40 + ii, (u64)(uintptr_t)Wq, (u64)(uintptr_t)Wk, (u64)(uintptr_t)Wv, (u64)(uintptr_t)Wo, (u32)(uintptr_t)(__attribute__((address_space(3))) char*)smem + L_MISC + 512u);
        tag = settle();
        const u32 wp = wait_init(rs, tag, 0), st = (u32)(ii * 8 + wave), so = slot_off(wp, (int)(st >> 6)) + (st & 63u) * 16384u + (u32)lane * 16;
#pragma unroll
        for (int i = 0; i < 8; ++i) stx4(rs, so + i * 1024, (u32x4){SENT, SENT, SENT, SENT});
        DRAIN();
        XSTAMP(192 + ii, 225);
        arrive(1);
    } else {
        const int row = (ii - 40) * 32 + wave * 4;
        u32x4 xw[5];
        ln_load(row, lane, x, w_r, xv, wv);
#pragma unroll
        for (int i = 0; i < 5; ++i) xw[i] = *(const u32x4*)(x + (size_t)(row + 256 + (lane >> 4)) * D_ + ((lane & 15) + 16 * i) * 8);
        ln_regs(xv);
        tag = settle();
        const u32 i = (u32)((ii - 40) * 512 + tid);
        stx4(rs, i < INI_ZERO_CHUNKS ? (u32)ZERO_BEGIN + i * 16 : OOB, (u32x4){0u, 0u, 0u, 0u});
#pragma unroll
        for (int i = 0; i < 5; ++i) stx4(rs, (u32)OFF_XNP + img_off(row + (lane >> 4), lane & 15, i), ln_perm(xv[i], lane & 15));
        ln_regs(xw);
        DRAIN();
        XSTAMP(192 + ii, 220);
        arrive(0);
        ln_store(row + 256, lane, xw, wv, rs);
        asm volatile("s_waitcnt vmcnt(6)" ::: "memory");
        arrive(1);
        ln_store(row, lane, xv, wv, rs, false);
    }
    {
        const int row = 512 + (ii * 8 + wave) * 4;
        ln_load(row, lane, x, w_r, xv, wv);
        ln_regs(xv);
        ln_store(row, lane, xv, wv, rs);
    }
    DRAIN();
    XSTAMP(192 + ii, 222);
    arrive(2);
    LBAR();
}

#define NOWV 2
#define STP
#define STA
#define LGP
#define LGA
__device__ __forceinline__ void s_fetch(u32 vm, u32 xo, u32x4 rs, u32 pfo, u32 pfd, u32 xd) {
    asm volatile(
        "s_mov_b32 s92, m0\n\t"
        "s_mov_b32 m0, %[pfd]\n\t"
        "s_add_u32 s93, %[pfo], 0x1000\n\t"
        "s_nop 2\n\t"
        "buffer_load_dwordx4 %[vm], %[rs], %[pfo] offen offset:0 sc1 lds\n\t"
        "buffer_load_dwordx4 %[vm], %[rs], %[pfo] offen offset:1024 sc1 lds\n\t"
        "buffer_load_dwordx4 %[vm], %[rs], %[pfo] offen offset:2048 sc1 lds\n\t"
        "buffer_load_dwordx4 %[vm], %[rs], %[pfo] offen offset:3072 sc1 lds\n\t"
        "s_add_u32 m0, m0, 0x1000\n\t"
        "s_nop 0\n\t"
        "buffer_load_dwordx4 %[vm], %[rs], s93 offen offset:0 sc1 lds\n\t"
        "buffer_load_dwordx4 %[vm], %[rs], s93 offen offset:1024 sc1 lds\n\t"
        "buffer_load_dwordx4 %[vm], %[rs], s93 offen offset:2048 sc1 lds\n\t"
        "buffer_load_dwordx4 %[vm], %[rs], s93 offen offset:3072 sc1 lds\n\t"
        "s_mov_b32 m0, %[xd]\n\t"
        "s_nop 0\n\t"
        "buffer_load_dword %[xo], %[rs], 0 offen sc1 lds\n\t"
        "s_mov_b32 m0, s92\n\t"
        :
        : [vm] "v"(vm), [xo] "v"(xo), [rs] "s"(rs), [pfo] "s"(pfo), [pfd] "s"(pfd), [xd] "s"(xd)
        : "s92", "s93", "vcc", "scc", "memory");
}

__device__ __forceinline__ void s_k0(const u32x32 (&W)[5], u32& rnd, u32 va, u32 sidx, u32 ow, u32 lb STP) {
    asm volatile(
        "Ltop%=:\n\t"
        "v_mov_b64 v[248:249], 0\n\t"
        "v_mov_b64 v[250:251], 0\n\t"
        "v_mov_b64 v[252:253], 0\n\t"
        "v_mov_b64 v[254:255], 0\n\t"
        "v_mov_b32 v200, 0xffbfffbf\n\t"
        "v_mov_b32 v206, %[lb]\n\t"
        "ds_write_b32 %[ow], v200\n\t"
        "ds_write_b32 %[ow], v200 offset:3072\n\t"
        "s_add_u32 %[rnd], %[rnd], 1\n\t"
        "s_setprio 1\n\t"
        "s_waitcnt lgkmcnt(0)\n\t"
        "s_barrier\n\t"
        "s_nop 15\n\t"
        "s_nop 11\n\t"
        "ds_read_b128 v[208:211], %[va] offset:0\n\t"
        "ds_read_b128 v[212:215], %[va] offset:1024\n\t"
        "ds_read_b128 v[216:219], %[va] offset:2048\n\t"
        "ds_read_b128 v[220:223], %[va] offset:3072\n\t"
        "ds_read_b128 v[224:227], %[va] offset:4096\n\t"
        "ds_read_b128 v[228:231], %[va] offset:5120\n\t"
        "ds_read_b128 v[232:235], %[va] offset:6144\n\t"
        "ds_read_b128 v[236:239], %[va] offset:7168\n\t"
        "ds_read_b128 v[240:243], %[va] offset:8192\n\t"
        "ds_read_b128 v[244:247], %[va] offset:10240\n\t"
        "s_waitcnt lgkmcnt(0)\n\t"
        "v_smfmac_f32_16x16x64_bf16 v[248:251], v[208:211], v[16:23], %[sidx]\n\t"
        "v_smfmac_f32_16x16x64_bf16 v[248:251], v[212:215], v[24:31], %[sidx]\n\t"
        "v_smfmac_f32_16x16x64_bf16 v[248:251], v[216:219], v[32:39], %[sidx]\n\t"
        "v_smfmac_f32_16x16x64_bf16 v[248:251], v[220:223], v[40:47], %[sidx]\n\t"
        "v_smfmac_f32_16x16x64_bf16 v[248:251], v[224:227], v[48:55], %[sidx]\n\t"
        "v_smfmac_f32_16x16x64_bf16 v[248:251], v[228:231], v[56:63], %[sidx]\n\t"
        "v_smfmac_f32_16x16x64_bf16 v[248:251], v[232:235], v[64:71], %[sidx]\n\t"
        "v_smfmac_f32_16x16x64_bf16 v[248:251], v[236:239], v[72:79], %[sidx]\n\t"
        "v_smfmac_f32_16x16x64_bf16 v[248:251], v[240:243], v[80:87], %[sidx]\n\t"
        "v_smfmac_f32_16x16x64_bf16 v[248:251], v[244:247], v[88:95], %[sidx]\n\t"
        "v_smfmac_f32_16x16x64_bf16 v[252:255], v[208:211], v[96:103], %[sidx]\n\t"
        "v_smfmac_f32_16x16x64_bf16 v[252:255], v[212:215], v[104:111], %[sidx]\n\t"
        "v_smfmac_f32_16x16x64_bf16 v[252:255], v[216:219], v[112:119], %[sidx]\n\t"
        "v_add_f32 v248, v248, v250\n\t"
        "v_smfmac_f32_16x16x64_bf16 v[252:255], v[220:223], v[120:127], %[sidx]\n\t"
        "v_add_f32 v249, v249, v251\n\t"
        "v_smfmac_f32_16x16x64_bf16 v[252:255], v[224:227], v[128:135], %[sidx]\n\t"
        "v_cvt_pk_bf16_f32 v200, v248, v249\n\t"
        "v_smfmac_f32_16x16x64_bf16 v[252:255], v[228:231], v[136:143], %[sidx]\n\t"
        "ds_write_b32 %[ow], v200\n\t"
        "v_smfmac_f32_16x16x64_bf16 v[252:255], v[232:235], v[144:151], %[sidx]\n\t"
        "v_cmp_u_f32 vcc, v248, v249\n\t"
        "v_smfmac_f32_16x16x64_bf16 v[252:255], v[236:239], v[152:159], %[sidx]\n\t"
        "v_smfmac_f32_16x16x64_bf16 v[252:255], v[240:243], v[160:167], %[sidx]\n\t"
        "v_smfmac_f32_16x16x64_bf16 v[252:255], v[244:247], v[168:175], %[sidx]\n\t"
        "s_nop 6\n\t"
        "s_cbranch_vccnz Lnan%=\n\t"
        "Lok%=:\n\t"
        "v_add_f32 v252, v252, v254\n\t"
        "v_add_f32 v253, v253, v255\n\t"
        "v_cvt_pk_bf16_f32 v201, v252, v253\n\t"
        "ds_write_b32 %[ow], v201 offset:3072\n\t"
        "s_barrier\n\t"
        "s_setprio 0\n\t"
        "ds_read_b32 v207, v206 offset:25104\n\t"
        "s_waitcnt lgkmcnt(0)\n\t"
        "v_cmp_eq_u32 vcc, %[rnd], v207\n\t"
        "s_barrier\n\t"
        "s_cbranch_vccnz Ltop%=\n\t"
        "s_branch Lend%=\n\t"
        "Lnan%=:\n\t"
        "v_max_u32 v207, v208, v209\n\t"
        "v_max3_u32 v207, v207, v210, v211\n\t"
        "v_max3_u32 v207, v207, v212, v213\n\t"
        "v_max3_u32 v207, v207, v214, v215\n\t"
        "v_max3_u32 v207, v207, v216, v217\n\t"
        "v_max3_u32 v207, v207, v218, v219\n\t"
        "v_max3_u32 v207, v207, v220, v221\n\t"
        "v_max3_u32 v207, v207, v222, v223\n\t"
        "v_max3_u32 v207, v207, v224, v225\n\t"
        "v_max3_u32 v207, v207, v226, v227\n\t"
        "v_max3_u32 v207, v207, v228, v229\n\t"
        "v_max3_u32 v207, v207, v230, v231\n\t"
        "v_max3_u32 v207, v207, v232, v233\n\t"
        "v_max3_u32 v207, v207, v234, v235\n\t"
        "v_max3_u32 v207, v207, v236, v237\n\t"
        "v_max3_u32 v207, v207, v238, v239\n\t"
        "v_max3_u32 v207, v207, v240, v241\n\t"
        "v_max3_u32 v207, v207, v242, v243\n\t"
        "v_max3_u32 v207, v207, v244, v245\n\t"
        "v_max3_u32 v207, v207, v246, v247\n\t"
        "v_cmp_eq_u32 vcc, 0xffbfffbf, v207\n\t"
        "v_mov_b32 v207, %[rnd]\n\t"
        "s_cbranch_vccz Lok%=\n\t"
        "ds_write_b32 v206, v207 offset:25104\n\t"
        "s_waitcnt lgkmcnt(0)\n\t"
        "s_barrier\n\t"
        "s_barrier\n\t"
        "s_branch Ltop%=\n\t"
        "Lend%=:\n\t"
        : [rnd] "+&s"(rnd)
        : [w0] "{v[16:47]}"(W[0]), [w1] "{v[48:79]}"(W[1]), [w2] "{v[80:111]}"(W[2]), [w3] "{v[112:143]}"(W[3]), [w4] "{v[144:175]}"(W[4]), [va] "v"(va), [sidx] "v"(sidx), [ow] "v"(ow), [lb] "s"(lb)
        : "v200", "v201", "v202", "v203", "v204", "v205", "v206", "v207", "v208", "v209", "v210", "v211", "v212", "v213", "v214", "v215", "v216", "v217", "v218", "v219", "v220", "v221", "v222", "v223", "v224", "v225", "v226", "v227", "v228", "v229", "v230", "v231", "v232", "v233", "v234", "v235", "v236", "v237", "v238", "v239", "v240", "v241", "v242", "v243", "v244", "v245", "v246", "v247", "v248", "v249", "v250", "v251", "v252", "v253", "v254", "v255", "vcc", "scc", "memory");
}

__device__ __forceinline__ void s_k2(const u32x32 (&W)[5], u32& rnd, u32 va, u32 sidx, u32 ow, u32 lb) {
    asm volatile(
        "Ltop%=:\n\t"
        "v_mov_b64 v[248:249], 0\n\t"
        "v_mov_b64 v[250:251], 0\n\t"
        "v_mov_b64 v[252:253], 0\n\t"
        "v_mov_b64 v[254:255], 0\n\t"
        "v_mov_b32 v200, 0xffbfffbf\n\t"
        "v_mov_b32 v206, %[lb]\n\t"
        "ds_write_b32 %[ow], v200\n\t"
        "ds_write_b32 %[ow], v200 offset:3072\n\t"
        "s_add_u32 %[rnd], %[rnd], 1\n\t"
        "s_setprio 1\n\t"
        "s_waitcnt lgkmcnt(0)\n\t"
        "s_barrier\n\t"
        "ds_read_b128 v[208:211], %[va] offset:0\n\t"
        "ds_read_b128 v[212:215], %[va] offset:1024\n\t"
        "ds_read_b128 v[216:219], %[va] offset:2048\n\t"
        "ds_read_b128 v[220:223], %[va] offset:3072\n\t"
        "ds_read_b128 v[224:227], %[va] offset:4096\n\t"
        "ds_read_b128 v[228:231], %[va] offset:5120\n\t"
        "ds_read_b128 v[232:235], %[va] offset:6144\n\t"
        "ds_read_b128 v[236:239], %[va] offset:7168\n\t"
        "ds_read_b128 v[240:243], %[va] offset:8192\n\t"
        "ds_read_b128 v[244:247], %[va] offset:10240\n\t"
        "s_waitcnt lgkmcnt(9)\n\t"
        "v_smfmac_f32_16x16x64_bf16 v[248:251], v[208:211], v[16:23], %[sidx]\n\t"
        "s_waitcnt lgkmcnt(8)\n\t"
        "v_smfmac_f32_16x16x64_bf16 v[248:251], v[212:215], v[24:31], %[sidx]\n\t"
        "s_waitcnt lgkmcnt(7)\n\t"
        "v_smfmac_f32_16x16x64_bf16 v[248:251], v[216:219], v[32:39], %[sidx]\n\t"
        "s_waitcnt lgkmcnt(6)\n\t"
        "v_smfmac_f32_16x16x64_bf16 v[248:251], v[220:223], v[40:47], %[sidx]\n\t"
        "s_waitcnt lgkmcnt(5)\n\t"
        "v_smfmac_f32_16x16x64_bf16 v[248:251], v[224:227], v[48:55], %[sidx]\n\t"
        "s_waitcnt lgkmcnt(4)\n\t"
        "v_smfmac_f32_16x16x64_bf16 v[248:251], v[228:231], v[56:63], %[sidx]\n\t"
        "s_waitcnt lgkmcnt(3)\n\t"
        "v_smfmac_f32_16x16x64_bf16 v[248:251], v[232:235], v[64:71], %[sidx]\n\t"
        "s_waitcnt lgkmcnt(2)\n\t"
        "v_smfmac_f32_16x16x64_bf16 v[248:251], v[236:239], v[72:79], %[sidx]\n\t"
        "s_waitcnt lgkmcnt(1)\n\t"
        "v_smfmac_f32_16x16x64_bf16 v[248:251], v[240:243], v[80:87], %[sidx]\n\t"
        "s_waitcnt lgkmcnt(0)\n\t"
        "v_smfmac_f32_16x16x64_bf16 v[248:251], v[244:247], v[88:95], %[sidx]\n\t"
        "v_smfmac_f32_16x16x64_bf16 v[252:255], v[208:211], v[96:103], %[sidx]\n\t"
        "v_smfmac_f32_16x16x64_bf16 v[252:255], v[212:215], v[104:111], %[sidx]\n\t"
        "v_smfmac_f32_16x16x64_bf16 v[252:255], v[216:219], v[112:119], %[sidx]\n\t"
        "v_add_f32 v248, v248, v250\n\t"
        "v_smfmac_f32_16x16x64_bf16 v[252:255], v[220:223], v[120:127], %[sidx]\n\t"
        "v_add_f32 v249, v249, v251\n\t"
        "v_smfmac_f32_16x16x64_bf16 v[252:255], v[224:227], v[128:135], %[sidx]\n\t"
        "v_cvt_pk_bf16_f32 v200, v248, v249\n\t"
        "v_smfmac_f32_16x16x64_bf16 v[252:255], v[228:231], v[136:143], %[sidx]\n\t"
        "ds_write_b32 %[ow], v200\n\t"
        "v_smfmac_f32_16x16x64_bf16 v[252:255], v[232:235], v[144:151], %[sidx]\n\t"
        "v_cmp_u_f32 vcc, v248, v249\n\t"
        "v_smfmac_f32_16x16x64_bf16 v[252:255], v[236:239], v[152:159], %[sidx]\n\t"
        "v_smfmac_f32_16x16x64_bf16 v[252:255], v[240:243], v[160:167], %[sidx]\n\t"
        "v_smfmac_f32_16x16x64_bf16 v[252:255], v[244:247], v[168:175], %[sidx]\n\t"
        "s_nop 6\n\t"
        "s_cbranch_vccnz Lnan%=\n\t"
        "Lok%=:\n\t"
        "v_add_f32 v252, v252, v254\n\t"
        "v_add_f32 v253, v253, v255\n\t"
        "v_cvt_pk_bf16_f32 v201, v252, v253\n\t"
        "ds_write_b32 %[ow], v201 offset:3072\n\t"
        "s_barrier\n\t"
        "s_setprio 0\n\t"
        "ds_read_b32 v207, v206 offset:25104\n\t"
        "s_waitcnt lgkmcnt(0)\n\t"
        "v_cmp_eq_u32 vcc, %[rnd], v207\n\t"
        "s_barrier\n\t"
        "s_cbranch_vccnz Ltop%=\n\t"
        "s_branch Lend%=\n\t"
        "Lnan%=:\n\t"
        "v_max_u32 v207, v208, v209\n\t"
        "v_max3_u32 v207, v207, v210, v211\n\t"
        "v_max3_u32 v207, v207, v212, v213\n\t"
        "v_max3_u32 v207, v207, v214, v215\n\t"
        "v_max3_u32 v207, v207, v216, v217\n\t"
        "v_max3_u32 v207, v207, v218, v219\n\t"
        "v_max3_u32 v207, v207, v220, v221\n\t"
        "v_max3_u32 v207, v207, v222, v223\n\t"
        "v_max3_u32 v207, v207, v224, v225\n\t"
        "v_max3_u32 v207, v207, v226, v227\n\t"
        "v_max3_u32 v207, v207, v228, v229\n\t"
        "v_max3_u32 v207, v207, v230, v231\n\t"
        "v_max3_u32 v207, v207, v232, v233\n\t"
        "v_max3_u32 v207, v207, v234, v235\n\t"
        "v_max3_u32 v207, v207, v236, v237\n\t"
        "v_max3_u32 v207, v207, v238, v239\n\t"
        "v_max3_u32 v207, v207, v240, v241\n\t"
        "v_max3_u32 v207, v207, v242, v243\n\t"
        "v_max3_u32 v207, v207, v244, v245\n\t"
        "v_max3_u32 v207, v207, v246, v247\n\t"
        "v_cmp_eq_u32 vcc, 0xffbfffbf, v207\n\t"
        "v_mov_b32 v207, %[rnd]\n\t"
        "s_cbranch_vccz Lok%=\n\t"
        "ds_write_b32 v206, v207 offset:25104\n\t"
        "s_waitcnt lgkmcnt(0)\n\t"
        "s_barrier\n\t"
        "s_barrier\n\t"
        "s_branch Ltop%=\n\t"
        "Lend%=:\n\t"
        : [rnd] "+&s"(rnd)
        : [w0] "{v[16:47]}"(W[0]), [w1] "{v[48:79]}"(W[1]), [w2] "{v[80:111]}"(W[2]), [w3] "{v[112:143]}"(W[3]), [w4] "{v[144:175]}"(W[4]), [va] "v"(va), [sidx] "v"(sidx), [ow] "v"(ow), [lb] "s"(lb)
        : "v200", "v201", "v202", "v203", "v204", "v205", "v206", "v207", "v208", "v209", "v210", "v211", "v212", "v213", "v214", "v215", "v216", "v217", "v218", "v219", "v220", "v221", "v222", "v223", "v224", "v225", "v226", "v227", "v228", "v229", "v230", "v231", "v232", "v233", "v234", "v235", "v236", "v237", "v238", "v239", "v240", "v241", "v242", "v243", "v244", "v245", "v246", "v247", "v248", "v249", "v250", "v251", "v252", "v253", "v254", "v255", "vcc", "scc", "memory");
}

__device__ __forceinline__ void s_l(u32& rnd, u32& redo, u32 lb) {
    asm volatile(
        "v_mov_b32 v206, %[lb]\n\t"
        "s_add_u32 %[rnd], %[rnd], 1\n\t"
        "s_mov_b32 %[redo], 1\n\t"
        "s_barrier\n\t"
        "s_barrier\n\t"
        "s_setprio 0\n\t"
        "ds_read_b32 v207, v206 offset:25104\n\t"
        "s_waitcnt lgkmcnt(0)\n\t"
        "v_cmp_eq_u32 vcc, %[rnd], v207\n\t"
        "s_barrier\n\t"
        "s_cbranch_vccnz Lend%=\n\t"
        "s_mov_b32 %[redo], 0\n\t"
        "Lend%=:\n\t"
        : [rnd] "+&s"(rnd), [redo] "=&s"(redo)
        : [lb] "s"(lb)
        : "v206", "v207", "vcc", "scc", "memory");
}

__device__ __forceinline__ void s_e(const u32x32 (&W)[5], u32& rnd, u32 va, u32 sidx, u32 ow, u32 lb, u32 kxa, u32 qtr, u32 qo, u32 eo, u32 ew, float lim, u32x4 rs, u32 so STP) {
    asm volatile(
        "ds_read_b128 v[176:179], %[kxa] offset:0\n\t"
        "ds_read_b128 v[180:183], %[kxa] offset:1024\n\t"
        "ds_read_b128 v[184:187], %[kxa] offset:2048\n\t"
        "ds_read_b128 v[188:191], %[kxa] offset:3072\n\t"
        "s_add_u32 s92, %[so], 0x1000\n\t"
        "s_add_u32 s93, %[so], 0x4000\n\t"
        "s_add_u32 s94, %[so], 0x5000\n\t"
        "Ltop%=:\n\t"
        "v_mov_b64 v[248:249], 0\n\t"
        "v_mov_b64 v[250:251], 0\n\t"
        "v_mov_b64 v[252:253], 0\n\t"
        "v_mov_b64 v[254:255], 0\n\t"
        "v_mov_b32 v200, 0xffbfffbf\n\t"
        "v_mov_b32 v206, %[lb]\n\t"
        "ds_write_b32 %[ow], v200\n\t"
        "ds_write_b32 %[ow], v200 offset:3072\n\t"
        "s_add_u32 %[rnd], %[rnd], 1\n\t"
        "s_setprio 3\n\t"
        "s_mov_b32 s95, 1\n\t"
        "s_waitcnt lgkmcnt(0)\n\t"
        "s_barrier\n\t"
        "ds_read_b128 v[208:211], %[va] offset:0\n\t"
        "ds_read_b128 v[212:215], %[va] offset:1024\n\t"
        "ds_read_b128 v[216:219], %[va] offset:2048\n\t"
        "ds_read_b128 v[220:223], %[va] offset:3072\n\t"
        "ds_read_b128 v[224:227], %[va] offset:4096\n\t"
        "ds_read_b128 v[228:231], %[va] offset:5120\n\t"
        "s_waitcnt lgkmcnt(5)\n\t"
        "v_smfmac_f32_16x16x64_bf16 v[248:251], v[208:211], v[16:23], %[sidx]\n\t"
        "ds_read_b128 v[232:235], %[va] offset:6144\n\t"
        "v_smfmac_f32_16x16x64_bf16 v[252:255], v[208:211], v[96:103], %[sidx]\n\t"
        "s_waitcnt lgkmcnt(5)\n\t"
        "v_smfmac_f32_16x16x64_bf16 v[248:251], v[212:215], v[24:31], %[sidx]\n\t"
        "ds_read_b128 v[236:239], %[va] offset:7168\n\t"
        "v_smfmac_f32_16x16x64_bf16 v[252:255], v[212:215], v[104:111], %[sidx]\n\t"
        "s_waitcnt lgkmcnt(5)\n\t"
        "v_smfmac_f32_16x16x64_bf16 v[248:251], v[216:219], v[32:39], %[sidx]\n\t"
        "ds_read_b128 v[240:243], %[va] offset:8192\n\t"
        "v_smfmac_f32_16x16x64_bf16 v[252:255], v[216:219], v[112:119], %[sidx]\n\t"
        "s_waitcnt lgkmcnt(5)\n\t"
        "v_smfmac_f32_16x16x64_bf16 v[248:251], v[220:223], v[40:47], %[sidx]\n\t"
        "ds_read_b128 v[244:247], %[va] offset:10240\n\t"
        "v_smfmac_f32_16x16x64_bf16 v[252:255], v[220:223], v[120:127], %[sidx]\n\t"
        "s_waitcnt lgkmcnt(5)\n\t"
        "v_smfmac_f32_16x16x64_bf16 v[248:251], v[224:227], v[48:55], %[sidx]\n\t"
        "s_waitcnt lgkmcnt(4)\n\t"
        "v_smfmac_f32_16x16x64_bf16 v[248:251], v[228:231], v[56:63], %[sidx]\n\t"
        "s_waitcnt lgkmcnt(3)\n\t"
        "v_smfmac_f32_16x16x64_bf16 v[248:251], v[232:235], v[64:71], %[sidx]\n\t"
        "s_waitcnt lgkmcnt(2)\n\t"
        "v_smfmac_f32_16x16x64_bf16 v[248:251], v[236:239], v[72:79], %[sidx]\n\t"
        "s_waitcnt lgkmcnt(1)\n\t"
        "v_smfmac_f32_16x16x64_bf16 v[248:251], v[240:243], v[80:87], %[sidx]\n\t"
        "s_waitcnt lgkmcnt(0)\n\t"
        "v_smfmac_f32_16x16x64_bf16 v[248:251], v[244:247], v[88:95], %[sidx]\n\t"
        "v_smfmac_f32_16x16x64_bf16 v[252:255], v[224:227], v[128:135], %[sidx]\n\t"
        "v_smfmac_f32_16x16x64_bf16 v[252:255], v[228:231], v[136:143], %[sidx]\n\t"
        "v_add_f32 v248, v248, v250\n\t"
        "v_smfmac_f32_16x16x64_bf16 v[252:255], v[232:235], v[144:151], %[sidx]\n\t"
        "v_add_f32 v249, v249, v251\n\t"
        "v_smfmac_f32_16x16x64_bf16 v[252:255], v[236:239], v[152:159], %[sidx]\n\t"
        "v_cvt_pk_bf16_f32 v200, v248, v249\n\t"
        "v_smfmac_f32_16x16x64_bf16 v[252:255], v[240:243], v[160:167], %[sidx]\n\t"
        "ds_write_b32 %[ow], v200\n\t"
        "v_smfmac_f32_16x16x64_bf16 v[252:255], v[244:247], v[168:175], %[sidx]\n\t"
        "v_cmp_u_f32 vcc, v248, v249\n\t"
        "s_nop 2\n\t"
        "ds_read_b64_tr_b16 v[192:193], %[qtr] offset:0\n\t"
        "ds_read_b64_tr_b16 v[194:195], %[qtr] offset:768\n\t"
        "ds_read_b64_tr_b16 v[196:197], %[qtr] offset:1536\n\t"
        "ds_read_b64_tr_b16 v[198:199], %[qtr] offset:2304\n\t"
        "s_cbranch_vccnz Lnan%=\n\t"
        "Lok%=:\n\t"
        "v_add_f32 v252, v252, v254\n\t"
        "v_add_f32 v253, v253, v255\n\t"
        "v_cvt_pk_bf16_f32 v201, v252, v253\n\t"
        "ds_write_b32 %[ow], v201 offset:3072\n\t"
        "s_waitcnt lgkmcnt(3)\n\t"
        "v_mfma_f32_16x16x32_bf16 v[248:251], v[176:179], v[192:195], 0\n\t"
        "s_waitcnt lgkmcnt(1)\n\t"
        "v_mfma_f32_16x16x32_bf16 v[248:251], v[180:183], v[196:199], v[248:251]\n\t"
        "s_waitcnt lgkmcnt(0)\n\t"
        "s_nop 1\n\t"
        "Lre%=:\n\t"
        "ds_read_b64_tr_b16 v[208:209], %[qtr] offset:3072\n\t"
        "ds_read_b64_tr_b16 v[210:211], %[qtr] offset:3840\n\t"
        "ds_read_b64_tr_b16 v[212:213], %[qtr] offset:4608\n\t"
        "ds_read_b64_tr_b16 v[214:215], %[qtr] offset:5376\n\t"
        "s_waitcnt lgkmcnt(2)\n\t"
        "v_mfma_f32_16x16x32_bf16 v[248:251], v[184:187], v[208:211], v[248:251]\n\t"
        "s_waitcnt lgkmcnt(0)\n\t"
        "v_mfma_f32_16x16x32_bf16 v[248:251], v[188:191], v[212:215], v[248:251]\n\t"
        "s_nop 7\n\t"
        "v_mov_b32_dpp v248, v250 quad_perm:[0,1,2,3] row_mask:0xf bank_mask:0xc\n\t"
        "v_mov_b32_dpp v249, v251 quad_perm:[0,1,2,3] row_mask:0xf bank_mask:0xc\n\t"
        "v_exp_f32 v202, v248\n\t"
        "v_exp_f32 v203, v249\n\t"
        "v_max_f32_e64 v204, |v248|, |v249|\n\t"
        "v_cvt_pk_bf16_f32 v202, v202, v203\n\t"
        "v_cmpx_le_f32 vcc, v204, %[lim]\n\t"
        "buffer_store_dword v202, %[eo], %[rs], %[so] offen sc1\n\t"
        "buffer_store_dword v202, %[eo], %[rs], s92 offen sc1\n\t"
        "buffer_store_dword v202, %[eo], %[rs], s93 offen sc1\n\t"
        "buffer_store_dword v202, %[eo], %[rs], s94 offen sc1\n\t"
        "s_mov_b64 exec, -1\n\t"
        "v_cndmask_b32 v202, -1, v202, vcc\n\t"
        "s_cmp_lg_u64 vcc, -1\n\t"
        "ds_write_b32 %[ew], v202\n\t"
        "s_cbranch_scc1 Lag%=\n\t"
        "Lgo%=:\n\t"
        "s_waitcnt lgkmcnt(0)\n\t"
        "s_barrier\n\t"
        "s_setprio 0\n\t"
        "ds_read_b32 v207, v206 offset:25104\n\t"
        "s_waitcnt lgkmcnt(0)\n\t"
        "v_cmp_eq_u32 vcc, %[rnd], v207\n\t"
        "s_barrier\n\t"
        "s_cbranch_vccnz Ltop%=\n\t"
        "s_branch Lend%=\n\t"
        "Lag%=:\n\t"
        "s_sub_u32 s95, s95, 1\n\t"
        "s_cbranch_scc1 Lgo%=\n\t"
        "Lqw%=:\n\t"
        "v_subrev_u32 v205, %[qo], %[ow]\n\t"
        "ds_read_b32 v192, v205 offset:0\n\t"
        "ds_read_b32 v193, v205 offset:768\n\t"
        "ds_read_b32 v194, v205 offset:1536\n\t"
        "ds_read_b32 v195, v205 offset:2304\n\t"
        "ds_read_b32 v196, v205 offset:3072\n\t"
        "ds_read_b32 v197, v205 offset:3840\n\t"
        "ds_read_b32 v198, v205 offset:4608\n\t"
        "ds_read_b32 v199, v205 offset:5376\n\t"
        "ds_read_b32 v207, v206 offset:25104\n\t"
        "s_waitcnt lgkmcnt(0)\n\t"
        "v_cmp_eq_u32 vcc, %[rnd], v207\n\t"
        "s_cbranch_vccnz Lgo%=\n\t"
        "v_max_u32 v207, v192, v193\n\t"
        "v_max3_u32 v207, v207, v194, v195\n\t"
        "v_max3_u32 v207, v207, v196, v197\n\t"
        "v_max3_u32 v207, v207, v198, v199\n\t"
        "v_cmp_eq_u32 vcc, 0xffbfffbf, v207\n\t"
        "s_cbranch_vccnz Lqw%=\n\t"
        "ds_read_b64_tr_b16 v[192:193], %[qtr] offset:0\n\t"
        "ds_read_b64_tr_b16 v[194:195], %[qtr] offset:768\n\t"
        "ds_read_b64_tr_b16 v[196:197], %[qtr] offset:1536\n\t"
        "ds_read_b64_tr_b16 v[198:199], %[qtr] offset:2304\n\t"
        "s_waitcnt lgkmcnt(2)\n\t"
        "v_mfma_f32_16x16x32_bf16 v[248:251], v[176:179], v[192:195], 0\n\t"
        "s_waitcnt lgkmcnt(0)\n\t"
        "v_mfma_f32_16x16x32_bf16 v[248:251], v[180:183], v[196:199], v[248:251]\n\t"
        "s_branch Lre%=\n\t"
        "Lnan%=:\n\t"
        "v_max_u32 v207, v208, v209\n\t"
        "v_max3_u32 v207, v207, v210, v211\n\t"
        "v_max3_u32 v207, v207, v212, v213\n\t"
        "v_max3_u32 v207, v207, v214, v215\n\t"
        "v_max3_u32 v207, v207, v216, v217\n\t"
        "v_max3_u32 v207, v207, v218, v219\n\t"
        "v_max3_u32 v207, v207, v220, v221\n\t"
        "v_max3_u32 v207, v207, v222, v223\n\t"
        "v_max3_u32 v207, v207, v224, v225\n\t"
        "v_max3_u32 v207, v207, v226, v227\n\t"
        "v_max3_u32 v207, v207, v228, v229\n\t"
        "v_max3_u32 v207, v207, v230, v231\n\t"
        "v_max3_u32 v207, v207, v232, v233\n\t"
        "v_max3_u32 v207, v207, v234, v235\n\t"
        "v_max3_u32 v207, v207, v236, v237\n\t"
        "v_max3_u32 v207, v207, v238, v239\n\t"
        "v_max3_u32 v207, v207, v240, v241\n\t"
        "v_max3_u32 v207, v207, v242, v243\n\t"
        "v_max3_u32 v207, v207, v244, v245\n\t"
        "v_max3_u32 v207, v207, v246, v247\n\t"
        "v_cmp_eq_u32 vcc, 0xffbfffbf, v207\n\t"
        "v_mov_b32 v207, %[rnd]\n\t"
        "s_cbranch_vccz Lok%=\n\t"
        "ds_write_b32 v206, v207 offset:25104\n\t"
        "s_waitcnt lgkmcnt(0)\n\t"
        "s_barrier\n\t"
        "s_barrier\n\t"
        "s_branch Ltop%=\n\t"
        "Lend%=:\n\t"
        : [rnd] "+&s"(rnd)
        : [w0] "{v[16:47]}"(W[0]), [w1] "{v[48:79]}"(W[1]), [w2] "{v[80:111]}"(W[2]), [w3] "{v[112:143]}"(W[3]), [w4] "{v[144:175]}"(W[4]), [va] "v"(va), [sidx] "v"(sidx), [ow] "v"(ow), [lb] "s"(lb), [kxa] "v"(kxa), [qtr] "v"(qtr), [eo] "v"(eo), [ew] "v"(ew), [lim] "v"(lim), [rs] "s"(rs), [so] "s"(so), [qo] "s"(qo)
        : "s92", "s93", "s94", "s95", "v176", "v177", "v178", "v179", "v180", "v181", "v182", "v183", "v184", "v185", "v186", "v187", "v188", "v189", "v190", "v191", "v192", "v193", "v194", "v195", "v196", "v197", "v198", "v199", "v200", "v201", "v202", "v203", "v204", "v205", "v206", "v207", "v208", "v209", "v210", "v211", "v212", "v213", "v214", "v215", "v216", "v217", "v218", "v219", "v220", "v221", "v222", "v223", "v224", "v225", "v226", "v227", "v228", "v229", "v230", "v231", "v232", "v233", "v234", "v235", "v236", "v237", "v238", "v239", "v240", "v241", "v242", "v243", "v244", "v245", "v246", "v247", "v248", "v249", "v250", "v251", "v252", "v253", "v254", "v255", "vcc", "scc", "memory");
}

__device__ __forceinline__ void s_f(const u32x32 (&W)[5], u32& rnd, u32 va, u32 sidx, u32 ow, u32 lb, u32& bad, u32x4 ones, u32 qtr, u32 ktr, u32 er, u32 po, float c1, float ind0, float ind1, u32x4 rs, u32 so, float ce, float big STP) {
    asm volatile(
        "s_add_u32 s92, %[so], 0x1000\n\t"
        "s_add_u32 s93, %[so], 0x4000\n\t"
        "s_add_u32 s94, %[so], 0x5000\n\t"
        "Ltop%=:\n\t"
        "v_mov_b64 v[248:249], 0\n\t"
        "v_mov_b64 v[250:251], 0\n\t"
        "v_mov_b64 v[252:253], 0\n\t"
        "v_mov_b64 v[254:255], 0\n\t"
        "v_mov_b32 v200, 0xffbfffbf\n\t"
        "v_mov_b32 v206, %[lb]\n\t"
        "ds_write_b32 %[ow], v200\n\t"
        "ds_write_b32 %[ow], v200 offset:3072\n\t"
        "s_add_u32 %[rnd], %[rnd], 1\n\t"
        "s_setprio 3\n\t"
        "s_mov_b32 %[bad], 2\n\t"
        "v_mov_b32 v197, 0\n\t"
        "s_waitcnt lgkmcnt(0)\n\t"
        "s_barrier\n\t"
        "ds_read_b128 v[208:211], %[va] offset:0\n\t"
        "ds_read_b128 v[212:215], %[va] offset:1024\n\t"
        "ds_read_b128 v[216:219], %[va] offset:2048\n\t"
        "ds_read_b128 v[220:223], %[va] offset:3072\n\t"
        "ds_read_b128 v[224:227], %[va] offset:4096\n\t"
        "ds_read_b128 v[228:231], %[va] offset:5120\n\t"
        "s_waitcnt lgkmcnt(5)\n\t"
        "v_smfmac_f32_16x16x64_bf16 v[248:251], v[208:211], v[16:23], %[sidx]\n\t"
        "ds_read_b128 v[232:235], %[va] offset:6144\n\t"
        "v_smfmac_f32_16x16x64_bf16 v[252:255], v[208:211], v[96:103], %[sidx]\n\t"
        "s_waitcnt lgkmcnt(5)\n\t"
        "v_smfmac_f32_16x16x64_bf16 v[248:251], v[212:215], v[24:31], %[sidx]\n\t"
        "ds_read_b128 v[236:239], %[va] offset:7168\n\t"
        "v_smfmac_f32_16x16x64_bf16 v[252:255], v[212:215], v[104:111], %[sidx]\n\t"
        "s_waitcnt lgkmcnt(5)\n\t"
        "v_smfmac_f32_16x16x64_bf16 v[248:251], v[216:219], v[32:39], %[sidx]\n\t"
        "ds_read_b128 v[240:243], %[va] offset:8192\n\t"
        "v_smfmac_f32_16x16x64_bf16 v[252:255], v[216:219], v[112:119], %[sidx]\n\t"
        "s_waitcnt lgkmcnt(5)\n\t"
        "v_smfmac_f32_16x16x64_bf16 v[248:251], v[220:223], v[40:47], %[sidx]\n\t"
        "ds_read_b128 v[244:247], %[va] offset:10240\n\t"
        "v_smfmac_f32_16x16x64_bf16 v[252:255], v[220:223], v[120:127], %[sidx]\n\t"
        "s_waitcnt lgkmcnt(5)\n\t"
        "v_smfmac_f32_16x16x64_bf16 v[248:251], v[224:227], v[48:55], %[sidx]\n\t"
        "s_waitcnt lgkmcnt(4)\n\t"
        "v_smfmac_f32_16x16x64_bf16 v[248:251], v[228:231], v[56:63], %[sidx]\n\t"
        "s_waitcnt lgkmcnt(3)\n\t"
        "v_smfmac_f32_16x16x64_bf16 v[248:251], v[232:235], v[64:71], %[sidx]\n\t"
        "s_waitcnt lgkmcnt(2)\n\t"
        "v_smfmac_f32_16x16x64_bf16 v[248:251], v[236:239], v[72:79], %[sidx]\n\t"
        "s_waitcnt lgkmcnt(1)\n\t"
        "v_smfmac_f32_16x16x64_bf16 v[248:251], v[240:243], v[80:87], %[sidx]\n\t"
        "s_waitcnt lgkmcnt(0)\n\t"
        "v_smfmac_f32_16x16x64_bf16 v[248:251], v[244:247], v[88:95], %[sidx]\n\t"
        "v_smfmac_f32_16x16x64_bf16 v[252:255], v[224:227], v[128:135], %[sidx]\n\t"
        "v_smfmac_f32_16x16x64_bf16 v[252:255], v[228:231], v[136:143], %[sidx]\n\t"
        "v_add_f32 v248, v248, v250\n\t"
        "v_smfmac_f32_16x16x64_bf16 v[252:255], v[232:235], v[144:151], %[sidx]\n\t"
        "v_add_f32 v249, v249, v251\n\t"
        "v_smfmac_f32_16x16x64_bf16 v[252:255], v[236:239], v[152:159], %[sidx]\n\t"
        "v_cvt_pk_bf16_f32 v200, v248, v249\n\t"
        "v_smfmac_f32_16x16x64_bf16 v[252:255], v[240:243], v[160:167], %[sidx]\n\t"
        "ds_write_b32 %[ow], v200\n\t"
        "v_smfmac_f32_16x16x64_bf16 v[252:255], v[244:247], v[168:175], %[sidx]\n\t"
        "v_cmp_u_f32 vcc, v248, v249\n\t"
        "s_nop 6\n\t"
        "s_cbranch_vccnz Lnan%=\n\t"
        "Lok%=:\n\t"
        "v_add_f32 v252, v252, v254\n\t"
        "v_add_f32 v253, v253, v255\n\t"
        "v_cvt_pk_bf16_f32 v201, v252, v253\n\t"
        "ds_write_b32 %[ow], v201 offset:3072\n\t"
        "s_waitcnt lgkmcnt(0)\n\t"
        "s_nop 15\n\t"
        "ds_read_b64_tr_b16 v[180:181], %[qtr] offset:0\n\t"
        "ds_read_b64_tr_b16 v[182:183], %[qtr] offset:768\n\t"
        "ds_read_b64_tr_b16 v[184:185], %[qtr] offset:1536\n\t"
        "ds_read_b64_tr_b16 v[186:187], %[qtr] offset:2304\n\t"
        "ds_read_b64_tr_b16 v[188:189], %[qtr] offset:3072\n\t"
        "ds_read_b64_tr_b16 v[190:191], %[qtr] offset:3840\n\t"
        "ds_read_b64_tr_b16 v[192:193], %[qtr] offset:4608\n\t"
        "ds_read_b64_tr_b16 v[194:195], %[qtr] offset:5376\n\t"
        "s_nop 15\n\t"
        "s_nop 7\n\t"
        "ds_read_b64_tr_b16 v[224:225], %[ktr] offset:0\n\t"
        "ds_read_b64_tr_b16 v[226:227], %[ktr] offset:768\n\t"
        "ds_read_b64_tr_b16 v[228:229], %[ktr] offset:1536\n\t"
        "ds_read_b64_tr_b16 v[230:231], %[ktr] offset:2304\n\t"
        "s_nop 7\n\t"
        "ds_read_b64_tr_b16 v[232:233], %[ktr] offset:4608\n\t"
        "ds_read_b64_tr_b16 v[234:235], %[ktr] offset:5376\n\t"
        "s_waitcnt lgkmcnt(4)\n\t"
        "v_mfma_f32_16x16x32_bf16 v[248:251], v[224:227], v[180:183], 0\n\t"
        "s_waitcnt lgkmcnt(2)\n\t"
        "v_mfma_f32_16x16x32_bf16 v[248:251], v[228:231], v[184:187], v[248:251]\n\t"
        "s_waitcnt lgkmcnt(0)\n\t"
        "v_mfma_f32_16x16x32_bf16 v[248:251], v[232:235], v[192:195], v[248:251]\n\t"
        "s_barrier\n\t"
        "Lre%=:\n\t"
        "ds_read_b64_tr_b16 v[208:209], %[ktr] offset:3072\n\t"
        "ds_read_b64_tr_b16 v[210:211], %[ktr] offset:3840\n\t"
        "ds_read_b128 v[240:243], %[er]\n\t"
        "ds_read_b32 v207, v206 offset:25104\n\t"
        "s_waitcnt lgkmcnt(2)\n\t"
        "v_mfma_f32_16x16x32_bf16 v[248:251], v[208:211], v[188:191], v[248:251]\n\t"
        "s_waitcnt lgkmcnt(1)\n\t"
        "v_mfma_f32_16x16x32_bf16 v[244:247], v[176:179], v[240:243], 0\n\t"
        "s_nop 3\n\t"
        "v_mul_f32 v248, %[ce], v248\n\t"
        "v_mul_f32 v249, %[ce], v249\n\t"
        "v_exp_f32 v250, v248\n\t"
        "v_exp_f32 v251, v249\n\t"
        "s_nop 0\n\t"
        "v_cvt_pk_bf16_f32 v196, v250, v251\n\t"
        "v_fma_f32 v250, v250, %[c1], %[ind0]\n\t"
        "v_fma_f32 v251, v251, %[c1], %[ind1]\n\t"
        "v_mfma_f32_16x16x16_bf16 v[244:247], v[176:177], v[196:197], v[244:247]\n\t"
        "s_nop 7\n\t"
        "v_rcp_f32 v246, v244\n\t"
        "v_cmpx_gt_f32 vcc, %[big], v244\n\t"
        "v_pk_mul_f32 v[250:251], v[250:251], v[246:247] op_sel_hi:[1,0]\n\t"
        "v_cvt_pk_bf16_f32 v250, v250, v251\n\t"
        "buffer_store_dword v250, %[po], %[rs], %[so] offen sc1\n\t"
        "buffer_store_dword v250, %[po], %[rs], s92 offen sc1\n\t"
        "buffer_store_dword v250, %[po], %[rs], s93 offen sc1\n\t"
        "buffer_store_dword v250, %[po], %[rs], s94 offen sc1\n\t"
        "s_mov_b64 exec, -1\n\t"
        "s_cmp_eq_u32 %[bad], 1\n\t"
        "s_cbranch_scc1 Ltw%=\n\t"
        "s_cmp_lg_u64 vcc, -1\n\t"
        "s_cselect_b32 %[bad], 1, 0\n\t"
        "s_waitcnt lgkmcnt(0)\n\t"
        "v_cmp_eq_u32 vcc, %[rnd], v207\n\t"
        "s_barrier\n\t"
        "s_cbranch_vccnz Ltop%=\n\t"
        "s_cbranch_scc0 Lend%=\n\t"
        "ds_read_b64_tr_b16 v[180:181], %[qtr] offset:0\n\t"
        "ds_read_b64_tr_b16 v[182:183], %[qtr] offset:768\n\t"
        "ds_read_b64_tr_b16 v[184:185], %[qtr] offset:1536\n\t"
        "ds_read_b64_tr_b16 v[186:187], %[qtr] offset:2304\n\t"
        "ds_read_b64_tr_b16 v[188:189], %[qtr] offset:3072\n\t"
        "ds_read_b64_tr_b16 v[190:191], %[qtr] offset:3840\n\t"
        "ds_read_b64_tr_b16 v[192:193], %[qtr] offset:4608\n\t"
        "ds_read_b64_tr_b16 v[194:195], %[qtr] offset:5376\n\t"
        "ds_read_b64_tr_b16 v[224:225], %[ktr] offset:0\n\t"
        "ds_read_b64_tr_b16 v[226:227], %[ktr] offset:768\n\t"
        "ds_read_b64_tr_b16 v[228:229], %[ktr] offset:1536\n\t"
        "ds_read_b64_tr_b16 v[230:231], %[ktr] offset:2304\n\t"
        "ds_read_b64_tr_b16 v[232:233], %[ktr] offset:4608\n\t"
        "ds_read_b64_tr_b16 v[234:235], %[ktr] offset:5376\n\t"
        "s_waitcnt lgkmcnt(0)\n\t"
        "v_mfma_f32_16x16x32_bf16 v[248:251], v[224:227], v[180:183], 0\n\t"
        "v_mfma_f32_16x16x32_bf16 v[248:251], v[228:231], v[184:187], v[248:251]\n\t"
        "v_mfma_f32_16x16x32_bf16 v[248:251], v[232:235], v[192:195], v[248:251]\n\t"
        "s_branch Lre%=\n\t"
        "Lnan%=:\n\t"
        "v_max_u32 v207, v208, v209\n\t"
        "v_max3_u32 v207, v207, v210, v211\n\t"
        "v_max3_u32 v207, v207, v212, v213\n\t"
        "v_max3_u32 v207, v207, v214, v215\n\t"
        "v_max3_u32 v207, v207, v216, v217\n\t"
        "v_max3_u32 v207, v207, v218, v219\n\t"
        "v_max3_u32 v207, v207, v220, v221\n\t"
        "v_max3_u32 v207, v207, v222, v223\n\t"
        "v_max3_u32 v207, v207, v224, v225\n\t"
        "v_max3_u32 v207, v207, v226, v227\n\t"
        "v_max3_u32 v207, v207, v228, v229\n\t"
        "v_max3_u32 v207, v207, v230, v231\n\t"
        "v_max3_u32 v207, v207, v232, v233\n\t"
        "v_max3_u32 v207, v207, v234, v235\n\t"
        "v_max3_u32 v207, v207, v236, v237\n\t"
        "v_max3_u32 v207, v207, v238, v239\n\t"
        "v_max3_u32 v207, v207, v240, v241\n\t"
        "v_max3_u32 v207, v207, v242, v243\n\t"
        "v_max3_u32 v207, v207, v244, v245\n\t"
        "v_max3_u32 v207, v207, v246, v247\n\t"
        "v_cmp_eq_u32 vcc, 0xffbfffbf, v207\n\t"
        "v_mov_b32 v207, %[rnd]\n\t"
        "s_cbranch_vccz Lok%=\n\t"
        "ds_write_b32 v206, v207 offset:25104\n\t"
        "s_waitcnt lgkmcnt(0)\n\t"
        "s_barrier\n\t"
        "s_barrier\n\t"
        "s_branch Ltop%=\n\t"
        "Ltw%=:\n\t"
        "s_cmp_lg_u64 vcc, -1\n\t"
        "s_cselect_b32 %[bad], 1, 0\n\t"
        "s_waitcnt lgkmcnt(0)\n\t"
        "Lend%=:\n\t"
        "s_setprio 0\n\t"
        : [rnd] "+&s"(rnd), [bad] "=&s"(bad)
        : [w0] "{v[16:47]}"(W[0]), [w1] "{v[48:79]}"(W[1]), [w2] "{v[80:111]}"(W[2]), [w3] "{v[112:143]}"(W[3]), [w4] "{v[144:175]}"(W[4]), [va] "v"(va), [sidx] "v"(sidx), [ow] "v"(ow), [lb] "s"(lb), [ones] "{v[176:179]}"(ones), [qtr] "v"(qtr), [ktr] "v"(ktr), [er] "v"(er), [po] "v"(po), [c1] "v"(c1), [ind0] "v"(ind0), [ind1] "v"(ind1), [rs] "s"(rs), [so] "s"(so), [ce] "s"(ce), [big] "s"(big)
        : "s92", "s93", "s94", "v180", "v181", "v182", "v183", "v184", "v185", "v186", "v187", "v188", "v189", "v190", "v191", "v192", "v193", "v194", "v195", "v196", "v197", "v198", "v199", "v200", "v201", "v202", "v203", "v204", "v205", "v206", "v207", "v208", "v209", "v210", "v211", "v212", "v213", "v214", "v215", "v216", "v217", "v218", "v219", "v220", "v221", "v222", "v223", "v224", "v225", "v226", "v227", "v228", "v229", "v230", "v231", "v232", "v233", "v234", "v235", "v236", "v237", "v238", "v239", "v240", "v241", "v242", "v243", "v244", "v245", "v246", "v247", "v248", "v249", "v250", "v251", "v252", "v253", "v254", "v255", "vcc", "scc", "memory");
}

__device__ __forceinline__ void s_m(const u32x32 (&W)[5], u32& rnd, u32 va, u32 sidx, u32 ow, u32 lb, u32& left, u32& c0, u32& r0, u32& t1, u32& t3, u32 tg, u32 kr, u32 vm, u32 so, u32 md, u32x4 rs, u32 poll, u32 tv, u32 tsr STP) {
    asm volatile(
        "s_mov_b32 s94, m0\n\t"
        "s_add_u32 s92, %[so], 0x8000\n\t"
        "s_add_u32 s93, %[so], 0x10000\n\t"
        "s_mov_b32 m0, %[md]\n\t"
        "s_setprio 3\n\t"
        "s_mov_b32 s96, %[left]\n\t"
        "s_cmp_eq_u32 %[poll], 0\n\t"
        "s_cbranch_scc1 Ltop%=\n\t"
        "Lda_%=:\n\t"
        "s_memrealtime s[90:91]\n\t"
        "s_waitcnt lgkmcnt(0)\n\t"
        "s_mul_i32 s90, s90, 24\n\t"
        "s_sub_u32 s88, %[tg], s90\n\t"
        "s_sub_u32 s89, s88, 620\n\t"
        "s_cmp_gt_u32 s89, 0x4000\n\t"
        "s_cbranch_scc1 Lea_%=\n\t"
        "s_sleep 1\n\t"
        "s_branch Lda_%=\n\t"
        "Lea_%=:\n\t"
        "s_memrealtime s[64:65]\n\t"
        "s_nop 0\n\t"
        "s_memrealtime s[66:67]\n\t"
        "s_nop 0\n\t"
        "s_memrealtime s[68:69]\n\t"
        "s_nop 0\n\t"
        "s_memrealtime s[70:71]\n\t"
        "s_nop 0\n\t"
        "s_memrealtime s[72:73]\n\t"
        "s_nop 0\n\t"
        "s_memrealtime s[74:75]\n\t"
        "s_memtime s[90:91]\n\t"
        "s_memrealtime s[76:77]\n\t"
        "s_nop 0\n\t"
        "s_memrealtime s[78:79]\n\t"
        "s_nop 0\n\t"
        "s_memrealtime s[80:81]\n\t"
        "s_nop 0\n\t"
        "s_memrealtime s[82:83]\n\t"
        "s_nop 0\n\t"
        "s_memrealtime s[84:85]\n\t"
        "s_nop 0\n\t"
        "s_memrealtime s[86:87]\n\t"
        "s_nop 0\n\t"
        "s_nop 7\n\t"
        "s_waitcnt lgkmcnt(0)\n\t"
        "s_add_u32 s64, s64, s66\n\t"
        "s_add_u32 s64, s64, s68\n\t"
        "s_add_u32 s64, s64, s70\n\t"
        "s_add_u32 s64, s64, s72\n\t"
        "s_add_u32 s64, s64, s74\n\t"
        "s_add_u32 s64, s64, s76\n\t"
        "s_add_u32 s64, s64, s78\n\t"
        "s_add_u32 s64, s64, s80\n\t"
        "s_add_u32 s64, s64, s82\n\t"
        "s_add_u32 s64, s64, s84\n\t"
        "s_add_u32 s64, s64, s86\n\t"
        "s_lshl_b32 s64, s64, 1\n\t"
        "s_mov_b32 %[c0], s90\n\t"
        "s_mov_b32 %[r0], s64\n\t"
        "s_sub_u32 s88, %[tg], s64\n\t"
        "s_mul_i32 s88, s88, %[kr]\n\t"
        "s_ashr_i32 s88, s88, 12\n\t"
        "s_sub_u32 s88, s88, 244\n\t"
        "s_nop 1\n\t"
        "s_max_i32 s88, s88, 0\n\t"
        "s_min_i32 s88, s88, 384\n\t"
        "s_andn2_b32 s88, s88, 3\n\t"
        "s_getpc_b64 s[90:91]\n\t"
        "Lga_%=:\n\t"
        "s_sub_u32 s90, s90, s88\n\t"
        "s_subb_u32 s91, s91, 0\n\t"
        "s_add_u32 s90, s90, Lza_%=-Lga_%=\n\t"
        "s_addc_u32 s91, s91, 0\n\t"
        "s_setpc_b64 s[90:91]\n\t"
        "s_nop 0\n\ts_nop 0\n\ts_nop 0\n\ts_nop 0\n\t"
        "s_nop 0\n\ts_nop 0\n\ts_nop 0\n\ts_nop 0\n\t"
        "s_nop 0\n\ts_nop 0\n\ts_nop 0\n\ts_nop 0\n\t"
        "s_nop 0\n\ts_nop 0\n\ts_nop 0\n\ts_nop 0\n\t"
        "s_nop 0\n\ts_nop 0\n\ts_nop 0\n\ts_nop 0\n\t"
        "s_nop 0\n\ts_nop 0\n\ts_nop 0\n\ts_nop 0\n\t"
        "s_nop 0\n\ts_nop 0\n\ts_nop 0\n\ts_nop 0\n\t"
        "s_nop 0\n\ts_nop 0\n\ts_nop 0\n\ts_nop 0\n\t"
        "s_nop 0\n\ts_nop 0\n\ts_nop 0\n\ts_nop 0\n\t"
        "s_nop 0\n\ts_nop 0\n\ts_nop 0\n\ts_nop 0\n\t"
        "s_nop 0\n\ts_nop 0\n\ts_nop 0\n\ts_nop 0\n\t"
        "s_nop 0\n\ts_nop 0\n\ts_nop 0\n\ts_nop 0\n\t"
        "s_nop 0\n\ts_nop 0\n\ts_nop 0\n\ts_nop 0\n\t"
        "s_nop 0\n\ts_nop 0\n\ts_nop 0\n\ts_nop 0\n\t"
        "s_nop 0\n\ts_nop 0\n\ts_nop 0\n\ts_nop 0\n\t"
        "s_nop 0\n\ts_nop 0\n\ts_nop 0\n\ts_nop 0\n\t"
        "s_nop 0\n\ts_nop 0\n\ts_nop 0\n\ts_nop 0\n\t"
        "s_nop 0\n\ts_nop 0\n\ts_nop 0\n\ts_nop 0\n\t"
        "s_nop 0\n\ts_nop 0\n\ts_nop 0\n\ts_nop 0\n\t"
        "s_nop 0\n\ts_nop 0\n\ts_nop 0\n\ts_nop 0\n\t"
        "s_nop 0\n\ts_nop 0\n\ts_nop 0\n\ts_nop 0\n\t"
        "s_nop 0\n\ts_nop 0\n\ts_nop 0\n\ts_nop 0\n\t"
        "s_nop 0\n\ts_nop 0\n\ts_nop 0\n\ts_nop 0\n\t"
        "s_nop 0\n\ts_nop 0\n\ts_nop 0\n\ts_nop 0\n\t"
        "Lza_%=:\n\t"
        "buffer_load_dwordx4 %[vm], %[rs], %[so] offen offset:0 sc1 lds\n\t"
        "buffer_load_dwordx4 %[vm], %[rs], %[so] offen offset:1024 sc1 lds\n\t"
        "buffer_load_dwordx4 %[vm], %[rs], %[so] offen offset:2048 sc1 lds\n\t"
        "buffer_load_dwordx4 %[vm], %[rs], %[so] offen offset:3072 sc1 lds\n\t"
        "s_add_u32 m0, m0, 0x1000\n\t"
        "s_nop 0\n\t"
        "buffer_load_dwordx4 %[vm], %[rs], s92 offen offset:0 sc1 lds\n\t"
        "buffer_load_dwordx4 %[vm], %[rs], s92 offen offset:1024 sc1 lds\n\t"
        "buffer_load_dwordx4 %[vm], %[rs], s92 offen offset:2048 sc1 lds\n\t"
        "buffer_load_dwordx4 %[vm], %[rs], s92 offen offset:3072 sc1 lds\n\t"
        "s_add_u32 m0, m0, 0x1000\n\t"
        "s_nop 0\n\t"
        "buffer_load_dwordx4 %[vm], %[rs], s93 offen offset:0 sc1 lds\n\t"
        "buffer_load_dwordx4 %[vm], %[rs], s93 offen offset:2048 sc1 lds\n\t"
        "s_barrier\n\t"
        "Ltop%=:\n\t"
        "v_mov_b64 v[248:249], 0\n\t"
        "v_mov_b64 v[250:251], 0\n\t"
        "v_mov_b64 v[252:253], 0\n\t"
        "v_mov_b64 v[254:255], 0\n\t"
        "v_mov_b32 v200, 0xffbfffbf\n\t"
        "v_mov_b32 v206, %[lb]\n\t"
        "ds_write_b32 %[ow], v200\n\t"
        "ds_write_b32 %[ow], v200 offset:3072\n\t"
        "s_add_u32 %[rnd], %[rnd], 1\n\t"
        "s_setprio 3\n\t"
        "s_mov_b32 s95, 0\n\t"
        "s_waitcnt vmcnt(0)\n\t"
        "s_waitcnt lgkmcnt(0)\n\t"
        "s_barrier\n\t"
        "ds_read_b128 v[208:211], %[va] offset:0\n\t"
        "ds_read_b128 v[212:215], %[va] offset:1024\n\t"
        "ds_read_b128 v[216:219], %[va] offset:2048\n\t"
        "ds_read_b128 v[220:223], %[va] offset:3072\n\t"
        "ds_read_b128 v[224:227], %[va] offset:4096\n\t"
        "ds_read_b128 v[228:231], %[va] offset:5120\n\t"
        "s_waitcnt lgkmcnt(5)\n\t"
        "v_smfmac_f32_16x16x64_bf16 v[248:251], v[208:211], v[16:23], %[sidx]\n\t"
        "ds_read_b128 v[232:235], %[va] offset:6144\n\t"
        "v_smfmac_f32_16x16x64_bf16 v[252:255], v[208:211], v[96:103], %[sidx]\n\t"
        "s_waitcnt lgkmcnt(5)\n\t"
        "v_smfmac_f32_16x16x64_bf16 v[248:251], v[212:215], v[24:31], %[sidx]\n\t"
        "ds_read_b128 v[236:239], %[va] offset:7168\n\t"
        "v_smfmac_f32_16x16x64_bf16 v[252:255], v[212:215], v[104:111], %[sidx]\n\t"
        "s_waitcnt lgkmcnt(5)\n\t"
        "v_smfmac_f32_16x16x64_bf16 v[248:251], v[216:219], v[32:39], %[sidx]\n\t"
        "ds_read_b128 v[240:243], %[va] offset:8192\n\t"
        "v_smfmac_f32_16x16x64_bf16 v[252:255], v[216:219], v[112:119], %[sidx]\n\t"
        "s_waitcnt lgkmcnt(5)\n\t"
        "v_smfmac_f32_16x16x64_bf16 v[248:251], v[220:223], v[40:47], %[sidx]\n\t"
        "ds_read_b128 v[244:247], %[va] offset:10240\n\t"
        "v_smfmac_f32_16x16x64_bf16 v[252:255], v[220:223], v[120:127], %[sidx]\n\t"
        "s_waitcnt lgkmcnt(5)\n\t"
        "v_smfmac_f32_16x16x64_bf16 v[248:251], v[224:227], v[48:55], %[sidx]\n\t"
        "s_waitcnt lgkmcnt(4)\n\t"
        "v_smfmac_f32_16x16x64_bf16 v[248:251], v[228:231], v[56:63], %[sidx]\n\t"
        "s_waitcnt lgkmcnt(3)\n\t"
        "v_smfmac_f32_16x16x64_bf16 v[248:251], v[232:235], v[64:71], %[sidx]\n\t"
        "s_waitcnt lgkmcnt(2)\n\t"
        "v_smfmac_f32_16x16x64_bf16 v[248:251], v[236:239], v[72:79], %[sidx]\n\t"
        "s_waitcnt lgkmcnt(1)\n\t"
        "v_smfmac_f32_16x16x64_bf16 v[248:251], v[240:243], v[80:87], %[sidx]\n\t"
        "s_waitcnt lgkmcnt(0)\n\t"
        "v_smfmac_f32_16x16x64_bf16 v[248:251], v[244:247], v[88:95], %[sidx]\n\t"
        "v_smfmac_f32_16x16x64_bf16 v[252:255], v[224:227], v[128:135], %[sidx]\n\t"
        "v_smfmac_f32_16x16x64_bf16 v[252:255], v[228:231], v[136:143], %[sidx]\n\t"
        "v_add_f32 v248, v248, v250\n\t"
        "v_smfmac_f32_16x16x64_bf16 v[252:255], v[232:235], v[144:151], %[sidx]\n\t"
        "v_add_f32 v249, v249, v251\n\t"
        "v_smfmac_f32_16x16x64_bf16 v[252:255], v[236:239], v[152:159], %[sidx]\n\t"
        "v_cvt_pk_bf16_f32 v200, v248, v249\n\t"
        "v_smfmac_f32_16x16x64_bf16 v[252:255], v[240:243], v[160:167], %[sidx]\n\t"
        "ds_write_b32 %[ow], v200\n\t"
        "v_smfmac_f32_16x16x64_bf16 v[252:255], v[244:247], v[168:175], %[sidx]\n\t"
        "v_cmp_u_f32 vcc, v248, v249\n\t"
        "s_nop 6\n\t"
        "s_cbranch_vccnz Lnan%=\n\t"
        "Lok%=:\n\t"
        "v_add_f32 v252, v252, v254\n\t"
        "v_add_f32 v253, v253, v255\n\t"
        "v_cvt_pk_bf16_f32 v201, v252, v253\n\t"
        "ds_write_b32 %[ow], v201 offset:3072\n\t"
        "s_waitcnt lgkmcnt(0)\n\t"
        "s_cmp_lg_u32 s96, %[left]\n\t"
        "s_cbranch_scc1 Lv%=\n\t"
        "s_buffer_load_dwordx4 s[64:67], %[rs], %[tsr] offset:0x0\n\t"
        "s_buffer_load_dwordx4 s[68:71], %[rs], %[tsr] offset:0x80\n\t"
        "s_buffer_load_dwordx4 s[72:75], %[rs], %[tsr] offset:0x100\n\t"
        "s_buffer_load_dwordx4 s[76:79], %[rs], %[tsr] offset:0x180\n\t"
        "s_barrier\n\t"
        "s_setprio 0\n\t"
        "ds_read_b32 v207, v206 offset:25104\n\t"
        "s_waitcnt lgkmcnt(0)\n\t"
        "v_cmp_eq_u32 vcc, %[rnd], v207\n\t"
        "s_barrier\n\t"
        "s_cbranch_vccnz Lck%=\n\t"
        "s_xor_b32 s67, s67, %[t3]\n\t"
        "s_xor_b32 s71, s71, %[t3]\n\t"
        "s_xor_b32 s75, s75, %[t3]\n\t"
        "s_xor_b32 s79, s79, %[t3]\n\t"
        "s_sub_u32 s69, s69, s65\n\t"
        "s_sub_u32 s73, s73, s65\n\t"
        "s_sub_u32 s77, s77, s65\n\t"
        "s_max_i32 s69, s69, s73\n\t"
        "s_max_i32 s77, s77, 0\n\t"
        "s_max_i32 s69, s69, s77\n\t"
        "s_add_u32 s65, s65, s69\n\t"
        "s_or_b32 s66, s67, s71\n\t"
        "s_or_b32 s70, s75, s79\n\t"
        "s_or_b32 s70, s70, s66\n\t"
        "s_cmp_eq_u32 s70, 0\n\t"
        "s_cselect_b32 %[t3], 1, 0\n\t"
        "s_mov_b32 %[t1], s65\n\t"
        "s_branch Lend%=\n\t"
        "Lv%=:\n\t"
        "s_barrier\n\t"
        "buffer_load_dwordx4 v[196:199], %[tv], %[rs], %[tsr] offen sc1\n\t"
        "s_setprio 0\n\t"
        "ds_read_b32 v207, v206 offset:25104\n\t"
        "s_waitcnt lgkmcnt(0)\n\t"
        "v_cmp_eq_u32 vcc, %[rnd], v207\n\t"
        "s_barrier\n\t"
        "s_cbranch_vccnz Lck%=\n\t"
        "s_sub_u32 s97, %[r0], 0x100000\n\t"
        "s_waitcnt vmcnt(0)\n\t"
        "v_cmp_eq_u32 vcc, %[t3], v199\n\t"
        "v_subrev_u32 v196, s97, v196\n\t"
        "v_subrev_u32 v197, s97, v197\n\t"
        "v_cndmask_b32 v196, 0, v196, vcc\n\t"
        "v_cndmask_b32 v197, 0, v197, vcc\n\t"
        "v_cndmask_b32 v198, 0, v198, vcc\n\t"
        "v_max_u32_dpp v196, v196, v196 quad_perm:[1,0,3,2] row_mask:0xf bank_mask:0xf\n\t"
        "v_max_u32_dpp v197, v197, v197 quad_perm:[1,0,3,2] row_mask:0xf bank_mask:0xf\n\t"
        "v_max_u32_dpp v198, v198, v198 quad_perm:[1,0,3,2] row_mask:0xf bank_mask:0xf\n\t"
        "v_max_u32_dpp v196, v196, v196 quad_perm:[2,3,0,1] row_mask:0xf bank_mask:0xf\n\t"
        "v_max_u32_dpp v197, v197, v197 quad_perm:[2,3,0,1] row_mask:0xf bank_mask:0xf\n\t"
        "v_max_u32_dpp v198, v198, v198 quad_perm:[2,3,0,1] row_mask:0xf bank_mask:0xf\n\t"
        "v_max_u32_dpp v196, v196, v196 row_half_mirror row_mask:0xf bank_mask:0xf\n\t"
        "v_max_u32_dpp v197, v197, v197 row_half_mirror row_mask:0xf bank_mask:0xf\n\t"
        "v_max_u32_dpp v198, v198, v198 row_half_mirror row_mask:0xf bank_mask:0xf\n\t"
        "s_lshr_b32 s96, vcc_lo, 4\n\t"
        "v_readfirstlane_b32 %[t1], v197\n\t"
        "s_add_u32 %[t1], %[t1], s97\n\t"
        "s_or_b32 s96, s96, vcc_lo\n\t"
        "s_and_b32 s96, s96, 15\n\t"
        "s_cmp_eq_u32 s96, 15\n\t"
        "s_cselect_b32 %[t3], 1, 0\n\t"
        "s_branch Lend%=\n\t"
        "Lnan%=:\n\t"
        "s_cmp_lg_u32 %[poll], 1\n\t"
        "s_cbranch_scc1 Lok%=\n\t"
        "s_mov_b32 m0, %[lb]\n\t"
        "v_add_u32 v204, %[lb], %[vm]\n\t"
        "buffer_load_dwordx4 %[vm], %[rs], %[so] offen offset:0 sc1 lds\n\t"
        "buffer_load_dwordx4 %[vm], %[rs], %[so] offen offset:1024 sc1 lds\n\t"
        "buffer_load_dwordx4 %[vm], %[rs], %[so] offen offset:2048 sc1 lds\n\t"
        "buffer_load_dwordx4 %[vm], %[rs], %[so] offen offset:3072 sc1 lds\n\t"
        "s_add_u32 m0, m0, 0x1000\n\t"
        "s_nop 0\n\t"
        "buffer_load_dwordx4 %[vm], %[rs], s92 offen offset:0 sc1 lds\n\t"
        "buffer_load_dwordx4 %[vm], %[rs], s92 offen offset:1024 sc1 lds\n\t"
        "buffer_load_dwordx4 %[vm], %[rs], s92 offen offset:2048 sc1 lds\n\t"
        "buffer_load_dwordx4 %[vm], %[rs], s92 offen offset:3072 sc1 lds\n\t"
        "s_add_u32 m0, m0, 0x1000\n\t"
        "s_nop 0\n\t"
        "buffer_load_dwordx4 %[vm], %[rs], s93 offen offset:0 sc1 lds\n\t"
        "buffer_load_dwordx4 %[vm], %[rs], s93 offen offset:2048 sc1 lds\n\t"
        "v_max_u32 v207, v208, v209\n\t"
        "v_max3_u32 v207, v207, v210, v211\n\t"
        "v_max3_u32 v207, v207, v212, v213\n\t"
        "v_max3_u32 v207, v207, v214, v215\n\t"
        "v_max3_u32 v207, v207, v216, v217\n\t"
        "v_max3_u32 v207, v207, v218, v219\n\t"
        "v_max3_u32 v207, v207, v220, v221\n\t"
        "v_max3_u32 v207, v207, v222, v223\n\t"
        "v_max3_u32 v207, v207, v224, v225\n\t"
        "v_max3_u32 v207, v207, v226, v227\n\t"
        "v_max3_u32 v207, v207, v228, v229\n\t"
        "v_max3_u32 v207, v207, v230, v231\n\t"
        "v_max3_u32 v207, v207, v232, v233\n\t"
        "v_max3_u32 v207, v207, v234, v235\n\t"
        "v_max3_u32 v207, v207, v236, v237\n\t"
        "v_max3_u32 v207, v207, v238, v239\n\t"
        "v_max3_u32 v207, v207, v240, v241\n\t"
        "v_max3_u32 v207, v207, v242, v243\n\t"
        "v_max3_u32 v207, v207, v244, v245\n\t"
        "v_max3_u32 v207, v207, v246, v247\n\t"
        "v_cmp_eq_u32 vcc, 0xffbfffbf, v207\n\t"
        "v_mov_b32 v207, %[rnd]\n\t"
        "s_cbranch_vccz Lok%=\n\t"
        "ds_write_b32 v206, v207 offset:25104\n\t"
        "s_mov_b32 s95, 1\n\t"
        "s_waitcnt lgkmcnt(0)\n\t"
        "s_branch Lcv%=\n\t"
        "Lck%=:\n\t"
        "v_add_u32 v204, %[lb], %[vm]\n\t"
        "Lcl%=:\n\t"
        "s_mov_b32 m0, %[lb]\n\t"
        "s_nop 0\n\t"
        "buffer_load_dwordx4 %[vm], %[rs], %[so] offen offset:0 sc1 lds\n\t"
        "buffer_load_dwordx4 %[vm], %[rs], %[so] offen offset:1024 sc1 lds\n\t"
        "buffer_load_dwordx4 %[vm], %[rs], %[so] offen offset:2048 sc1 lds\n\t"
        "buffer_load_dwordx4 %[vm], %[rs], %[so] offen offset:3072 sc1 lds\n\t"
        "s_add_u32 m0, m0, 0x1000\n\t"
        "s_nop 0\n\t"
        "buffer_load_dwordx4 %[vm], %[rs], s92 offen offset:0 sc1 lds\n\t"
        "buffer_load_dwordx4 %[vm], %[rs], s92 offen offset:1024 sc1 lds\n\t"
        "buffer_load_dwordx4 %[vm], %[rs], s92 offen offset:2048 sc1 lds\n\t"
        "buffer_load_dwordx4 %[vm], %[rs], s92 offen offset:3072 sc1 lds\n\t"
        "s_add_u32 m0, m0, 0x1000\n\t"
        "s_nop 0\n\t"
        "buffer_load_dwordx4 %[vm], %[rs], s93 offen offset:0 sc1 lds\n\t"
        "buffer_load_dwordx4 %[vm], %[rs], s93 offen offset:2048 sc1 lds\n\t"
        "Lcv%=:\n\t"
        "s_sub_u32 %[left], %[left], 1\n\t"
        "s_waitcnt vmcnt(9)\n\t"
        "ds_read_b128 v[208:211], v204 offset:0\n\t"
        "s_waitcnt vmcnt(8)\n\t"
        "ds_read_b128 v[212:215], v204 offset:1024\n\t"
        "s_waitcnt vmcnt(7)\n\t"
        "ds_read_b128 v[216:219], v204 offset:2048\n\t"
        "s_waitcnt vmcnt(6)\n\t"
        "ds_read_b128 v[220:223], v204 offset:3072\n\t"
        "s_waitcnt vmcnt(5)\n\t"
        "ds_read_b128 v[224:227], v204 offset:4096\n\t"
        "s_waitcnt vmcnt(4)\n\t"
        "ds_read_b128 v[228:231], v204 offset:5120\n\t"
        "s_waitcnt lgkmcnt(5)\n\t"
        "v_max3_u32 v207, v208, v208, v209\n\t"
        "v_max3_u32 v207, v207, v210, v211\n\t"
        "s_waitcnt vmcnt(3)\n\t"
        "ds_read_b128 v[232:235], v204 offset:6144\n\t"
        "s_waitcnt lgkmcnt(5)\n\t"
        "v_max3_u32 v207, v207, v212, v213\n\t"
        "v_max3_u32 v207, v207, v214, v215\n\t"
        "s_waitcnt vmcnt(2)\n\t"
        "ds_read_b128 v[236:239], v204 offset:7168\n\t"
        "s_waitcnt lgkmcnt(5)\n\t"
        "v_max3_u32 v207, v207, v216, v217\n\t"
        "v_max3_u32 v207, v207, v218, v219\n\t"
        "s_waitcnt vmcnt(1)\n\t"
        "ds_read_b128 v[240:243], v204 offset:8192\n\t"
        "s_waitcnt lgkmcnt(5)\n\t"
        "v_max3_u32 v207, v207, v220, v221\n\t"
        "v_max3_u32 v207, v207, v222, v223\n\t"
        "s_waitcnt vmcnt(0)\n\t"
        "ds_read_b128 v[244:247], v204 offset:10240\n\t"
        "s_waitcnt lgkmcnt(5)\n\t"
        "v_max3_u32 v207, v207, v224, v225\n\t"
        "v_max3_u32 v207, v207, v226, v227\n\t"
        "s_waitcnt lgkmcnt(4)\n\t"
        "v_max3_u32 v207, v207, v228, v229\n\t"
        "v_max3_u32 v207, v207, v230, v231\n\t"
        "s_waitcnt lgkmcnt(3)\n\t"
        "v_max3_u32 v207, v207, v232, v233\n\t"
        "v_max3_u32 v207, v207, v234, v235\n\t"
        "s_waitcnt lgkmcnt(2)\n\t"
        "v_max3_u32 v207, v207, v236, v237\n\t"
        "v_max3_u32 v207, v207, v238, v239\n\t"
        "s_waitcnt lgkmcnt(1)\n\t"
        "v_max3_u32 v207, v207, v240, v241\n\t"
        "v_max3_u32 v207, v207, v242, v243\n\t"
        "s_waitcnt lgkmcnt(0)\n\t"
        "v_max3_u32 v207, v207, v244, v245\n\t"
        "v_max3_u32 v207, v207, v246, v247\n\t"
        "v_cmp_eq_u32 vcc, 0xffbfffbf, v207\n\t"
        "s_cbranch_vccz Lcd%=\n\t"
        "s_cbranch_scc0 Lcl%=\n\t"
        "Lcd%=:\n\t"
        "s_cmp_eq_u32 s95, 0\n\t"
        "s_cbranch_scc1 Lce%=\n\t"
        "s_barrier\n\t"
        "s_barrier\n\t"
        "Lce%=:\n\t"
        "s_cbranch_vccz Ltop%=\n\t"
        "Lend%=:\n\t"
        "s_mov_b32 m0, s94\n\t"
        "s_waitcnt vmcnt(0)\n\t"
        : [rnd] "+&s"(rnd), [c0] "+&s"(c0), [r0] "+&s"(r0), [t1] "+&s"(t1), [t3] "+&s"(t3), [left] "+&s"(left)
        : [w0] "{v[16:47]}"(W[0]), [w1] "{v[48:79]}"(W[1]), [w2] "{v[80:111]}"(W[2]), [w3] "{v[112:143]}"(W[3]), [w4] "{v[144:175]}"(W[4]), [va] "v"(va), [sidx] "v"(sidx), [ow] "v"(ow), [rs] "s"(rs), [poll] "s"(poll), [tg] "s"(tg), [kr] "s"(kr), [tsr] "s"(tsr), [vm] "v"(vm), [so] "s"(so), [lb] "s"(lb), [md] "s"(md), [tv] "v"(tv)
        : "s64", "s65", "s66", "s67", "s68", "s69", "s70", "s71", "s72", "s73", "s74", "s75", "s76", "s77", "s78", "s79", "s80", "s81", "s82", "s83", "s84", "s85", "s86", "s87", "s88", "s89", "s90", "s91", "s92", "s93", "s94", "s95", "s96", "s97", "v196", "v197", "v198", "v199", "v200", "v201", "v202", "v203", "v204", "v205", "v206", "v207", "v208", "v209", "v210", "v211", "v212", "v213", "v214", "v215", "v216", "v217", "v218", "v219", "v220", "v221", "v222", "v223", "v224", "v225", "v226", "v227", "v228", "v229", "v230", "v231", "v232", "v233", "v234", "v235", "v236", "v237", "v238", "v239", "v240", "v241", "v242", "v243", "v244", "v245", "v246", "v247", "v248", "v249", "v250", "v251", "v252", "v253", "v254", "v255", "vcc", "scc", "memory");
}

__device__ __forceinline__ void gather(u32 tg, u32 kr, u32 vm, u32 so, u32 lb, u32x4 rs) {
    asm volatile(
        "s_mov_b32 s94, m0\n\t"
        "s_add_u32 s92, %[so], 0x8000\n\t"
        "s_add_u32 s93, %[so], 0x10000\n\t"
        "s_mov_b32 m0, %[lb]\n\t"
        "Lda_%=:\n\t"
        "s_memrealtime s[90:91]\n\t"
        "s_waitcnt lgkmcnt(0)\n\t"
        "s_mul_i32 s90, s90, 24\n\t"
        "s_sub_u32 s88, %[tg], s90\n\t"
        "s_sub_u32 s89, s88, 620\n\t"
        "s_cmp_gt_u32 s89, 0x4000\n\t"
        "s_cbranch_scc1 Lea_%=\n\t"
        "s_sleep 1\n\t"
        "s_branch Lda_%=\n\t"
        "Lea_%=:\n\t"
        "s_cmp_lt_i32 s88, 120\n\t"
        "s_cbranch_scc1 Lza_%=\n\t"
        "s_memrealtime s[64:65]\n\t"
        "s_nop 0\n\t"
        "s_memrealtime s[66:67]\n\t"
        "s_nop 0\n\t"
        "s_memrealtime s[68:69]\n\t"
        "s_nop 0\n\t"
        "s_memrealtime s[70:71]\n\t"
        "s_nop 0\n\t"
        "s_memrealtime s[72:73]\n\t"
        "s_nop 0\n\t"
        "s_memrealtime s[74:75]\n\t"
        "s_memtime s[90:91]\n\t"
        "s_memrealtime s[76:77]\n\t"
        "s_nop 0\n\t"
        "s_memrealtime s[78:79]\n\t"
        "s_nop 0\n\t"
        "s_memrealtime s[80:81]\n\t"
        "s_nop 0\n\t"
        "s_memrealtime s[82:83]\n\t"
        "s_nop 0\n\t"
        "s_memrealtime s[84:85]\n\t"
        "s_nop 0\n\t"
        "s_memrealtime s[86:87]\n\t"
        "s_nop 0\n\t"
        "s_nop 7\n\t"
        "s_waitcnt lgkmcnt(0)\n\t"
        "s_add_u32 s64, s64, s66\n\t"
        "s_add_u32 s64, s64, s68\n\t"
        "s_add_u32 s64, s64, s70\n\t"
        "s_add_u32 s64, s64, s72\n\t"
        "s_add_u32 s64, s64, s74\n\t"
        "s_add_u32 s64, s64, s76\n\t"
        "s_add_u32 s64, s64, s78\n\t"
        "s_add_u32 s64, s64, s80\n\t"
        "s_add_u32 s64, s64, s82\n\t"
        "s_add_u32 s64, s64, s84\n\t"
        "s_add_u32 s64, s64, s86\n\t"
        "s_lshl_b32 s64, s64, 1\n\t"
        "s_nop 1\n\t"
        "s_sub_u32 s88, %[tg], s64\n\t"
        "s_mul_i32 s88, s88, %[kr]\n\t"
        "s_ashr_i32 s88, s88, 12\n\t"
        "s_sub_u32 s88, s88, 244\n\t"
        "s_nop 1\n\t"
        "s_max_i32 s88, s88, 0\n\t"
        "s_min_i32 s88, s88, 384\n\t"
        "s_andn2_b32 s88, s88, 3\n\t"
        "s_getpc_b64 s[90:91]\n\t"
        "Lga_%=:\n\t"
        "s_sub_u32 s90, s90, s88\n\t"
        "s_subb_u32 s91, s91, 0\n\t"
        "s_add_u32 s90, s90, Lza_%=-Lga_%=\n\t"
        "s_addc_u32 s91, s91, 0\n\t"
        "s_setpc_b64 s[90:91]\n\t"
        "s_nop 0\n\ts_nop 0\n\ts_nop 0\n\ts_nop 0\n\t"
        "s_nop 0\n\ts_nop 0\n\ts_nop 0\n\ts_nop 0\n\t"
        "s_nop 0\n\ts_nop 0\n\ts_nop 0\n\ts_nop 0\n\t"
        "s_nop 0\n\ts_nop 0\n\ts_nop 0\n\ts_nop 0\n\t"
        "s_nop 0\n\ts_nop 0\n\ts_nop 0\n\ts_nop 0\n\t"
        "s_nop 0\n\ts_nop 0\n\ts_nop 0\n\ts_nop 0\n\t"
        "s_nop 0\n\ts_nop 0\n\ts_nop 0\n\ts_nop 0\n\t"
        "s_nop 0\n\ts_nop 0\n\ts_nop 0\n\ts_nop 0\n\t"
        "s_nop 0\n\ts_nop 0\n\ts_nop 0\n\ts_nop 0\n\t"
        "s_nop 0\n\ts_nop 0\n\ts_nop 0\n\ts_nop 0\n\t"
        "s_nop 0\n\ts_nop 0\n\ts_nop 0\n\ts_nop 0\n\t"
        "s_nop 0\n\ts_nop 0\n\ts_nop 0\n\ts_nop 0\n\t"
        "s_nop 0\n\ts_nop 0\n\ts_nop 0\n\ts_nop 0\n\t"
        "s_nop 0\n\ts_nop 0\n\ts_nop 0\n\ts_nop 0\n\t"
        "s_nop 0\n\ts_nop 0\n\ts_nop 0\n\ts_nop 0\n\t"
        "s_nop 0\n\ts_nop 0\n\ts_nop 0\n\ts_nop 0\n\t"
        "s_nop 0\n\ts_nop 0\n\ts_nop 0\n\ts_nop 0\n\t"
        "s_nop 0\n\ts_nop 0\n\ts_nop 0\n\ts_nop 0\n\t"
        "s_nop 0\n\ts_nop 0\n\ts_nop 0\n\ts_nop 0\n\t"
        "s_nop 0\n\ts_nop 0\n\ts_nop 0\n\ts_nop 0\n\t"
        "s_nop 0\n\ts_nop 0\n\ts_nop 0\n\ts_nop 0\n\t"
        "s_nop 0\n\ts_nop 0\n\ts_nop 0\n\ts_nop 0\n\t"
        "s_nop 0\n\ts_nop 0\n\ts_nop 0\n\ts_nop 0\n\t"
        "s_nop 0\n\ts_nop 0\n\ts_nop 0\n\ts_nop 0\n\t"
        "Lza_%=:\n\t"
        "buffer_load_dwordx4 %[vm], %[rs], %[so] offen offset:0 sc1 lds\n\t"
        "buffer_load_dwordx4 %[vm], %[rs], %[so] offen offset:1024 sc1 lds\n\t"
        "buffer_load_dwordx4 %[vm], %[rs], %[so] offen offset:2048 sc1 lds\n\t"
        "buffer_load_dwordx4 %[vm], %[rs], %[so] offen offset:3072 sc1 lds\n\t"
        "s_add_u32 m0, m0, 0x1000\n\t"
        "s_nop 0\n\t"
        "buffer_load_dwordx4 %[vm], %[rs], s92 offen offset:0 sc1 lds\n\t"
        "buffer_load_dwordx4 %[vm], %[rs], s92 offen offset:1024 sc1 lds\n\t"
        "buffer_load_dwordx4 %[vm], %[rs], s92 offen offset:2048 sc1 lds\n\t"
        "buffer_load_dwordx4 %[vm], %[rs], s92 offen offset:3072 sc1 lds\n\t"
        "s_add_u32 m0, m0, 0x1000\n\t"
        "s_nop 0\n\t"
        "buffer_load_dwordx4 %[vm], %[rs], s93 offen offset:0 sc1 lds\n\t"
        "buffer_load_dwordx4 %[vm], %[rs], s93 offen offset:2048 sc1 lds\n\t"
        "s_mov_b32 m0, s94\n\t"
        :
        : [rs] "s"(rs), [tg] "s"(tg), [kr] "s"(kr), [vm] "v"(vm), [so] "s"(so), [lb] "s"(lb)
        : "s64", "s65", "s66", "s67", "s68", "s69", "s70", "s71", "s72", "s73", "s74", "s75", "s76", "s77", "s78", "s79", "s80", "s81", "s82", "s83", "s84", "s85", "s86", "s87", "s88", "s89", "s90", "s91", "s92", "s93", "s94", "vcc", "scc", "memory");
}

__device__ __forceinline__ void ts_look(u32& t1, u32& ok, u32x4 rs, u32 tsr, u32 want) {
    asm volatile(
        "s_nop 4\n\t"
        "s_buffer_load_dwordx4 s[64:67], %[rs], %[tsr] offset:0x0\n\t"
        "s_buffer_load_dwordx4 s[68:71], %[rs], %[tsr] offset:0x80\n\t"
        "s_buffer_load_dwordx4 s[72:75], %[rs], %[tsr] offset:0x100\n\t"
        "s_buffer_load_dwordx4 s[76:79], %[rs], %[tsr] offset:0x180\n\t"
        "s_waitcnt lgkmcnt(0)\n\t"
        "s_xor_b32 s67, s67, %[want]\n\t"
        "s_xor_b32 s71, s71, %[want]\n\t"
        "s_xor_b32 s75, s75, %[want]\n\t"
        "s_xor_b32 s79, s79, %[want]\n\t"
        "s_sub_u32 s69, s69, s65\n\t"
        "s_sub_u32 s73, s73, s65\n\t"
        "s_sub_u32 s77, s77, s65\n\t"
        "s_max_i32 s69, s69, s73\n\t"
        "s_max_i32 s77, s77, 0\n\t"
        "s_max_i32 s69, s69, s77\n\t"
        "s_add_u32 s65, s65, s69\n\t"
        "s_or_b32 s66, s67, s71\n\t"
        "s_or_b32 s70, s75, s79\n\t"
        "s_or_b32 s70, s70, s66\n\t"
        "s_cmp_eq_u32 s70, 0\n\t"
        "s_cselect_b32 %[ok], 15, 0\n\t"
        "s_mov_b32 %[t1], s65\n\t"
        : [t1] "=&s"(t1), [ok] "=&s"(ok)
        : [rs] "s"(rs), [tsr] "s"(tsr), [want] "s"(want)
        : "s64", "s65", "s66", "s67", "s68", "s69", "s70", "s71", "s72", "s73", "s74", "s75", "s76", "s77", "s78", "s79", "vcc", "scc", "memory");
}

__device__ __forceinline__ void pull(u32 vo, u32 tb, u32x4 rs, u32 n, u32 gap, u32 ld) {
    asm volatile(
        "s_mov_b32 s94, m0\n\t"
        "s_mov_b32 m0, %[ld]\n\t"
        "s_mov_b32 s92, 0\n\t"
        "Lp%=:\n\t"
        "v_readlane_b32 s93, %[tb], s92\n\t"
        "s_getpc_b64 s[90:91]\n\t"
        "Lgg_%=:\n\t"
        "s_sub_u32 s90, s90, %[gap]\n\t"
        "s_subb_u32 s91, s91, 0\n\t"
        "s_add_u32 s90, s90, Lzg_%=-Lgg_%=\n\t"
        "s_addc_u32 s91, s91, 0\n\t"
        "s_setpc_b64 s[90:91]\n\t"
        "s_nop 0\n\ts_nop 0\n\ts_nop 0\n\ts_nop 0\n\t"
        "s_nop 0\n\ts_nop 0\n\ts_nop 0\n\ts_nop 0\n\t"
        "s_nop 0\n\ts_nop 0\n\ts_nop 0\n\ts_nop 0\n\t"
        "s_nop 0\n\ts_nop 0\n\ts_nop 0\n\ts_nop 0\n\t"
        "s_nop 0\n\ts_nop 0\n\ts_nop 0\n\ts_nop 0\n\t"
        "s_nop 0\n\ts_nop 0\n\ts_nop 0\n\ts_nop 0\n\t"
        "s_nop 0\n\ts_nop 0\n\ts_nop 0\n\ts_nop 0\n\t"
        "s_nop 0\n\ts_nop 0\n\ts_nop 0\n\ts_nop 0\n\t"
        "s_nop 0\n\ts_nop 0\n\ts_nop 0\n\ts_nop 0\n\t"
        "s_nop 0\n\ts_nop 0\n\ts_nop 0\n\ts_nop 0\n\t"
        "s_nop 0\n\ts_nop 0\n\ts_nop 0\n\ts_nop 0\n\t"
        "s_nop 0\n\ts_nop 0\n\ts_nop 0\n\ts_nop 0\n\t"
        "s_nop 0\n\ts_nop 0\n\ts_nop 0\n\ts_nop 0\n\t"
        "s_nop 0\n\ts_nop 0\n\ts_nop 0\n\ts_nop 0\n\t"
        "s_nop 0\n\ts_nop 0\n\ts_nop 0\n\ts_nop 0\n\t"
        "s_nop 0\n\ts_nop 0\n\ts_nop 0\n\ts_nop 0\n\t"
        "Lzg_%=:\n\t"
        "buffer_load_dword %[vo], %[rs], s93 offen sc1 lds\n\t"
        "s_add_u32 s92, s92, 1\n\t"
        "s_cmp_lt_u32 s92, %[n]\n\t"
        "s_cbranch_scc1 Lp%=\n\t"
        "s_mov_b32 m0, s94\n\t"
        :
        : [vo] "v"(vo), [tb] "v"(tb), [rs] "s"(rs), [n] "s"(n), [gap] "s"(gap), [ld] "s"(ld)
        : "s88", "s89", "s90", "s91", "s92", "s93", "s94", "vcc", "scc", "memory");
}

__device__ __forceinline__ void calib(u32& c0, u32& r0) {
    asm volatile(
        "s_memrealtime s[64:65]\n\t"
        "s_nop 0\n\t"
        "s_memrealtime s[66:67]\n\t"
        "s_nop 0\n\t"
        "s_memrealtime s[68:69]\n\t"
        "s_nop 0\n\t"
        "s_memrealtime s[70:71]\n\t"
        "s_nop 0\n\t"
        "s_memrealtime s[72:73]\n\t"
        "s_nop 0\n\t"
        "s_memrealtime s[74:75]\n\t"
        "s_memtime s[90:91]\n\t"
        "s_memrealtime s[76:77]\n\t"
        "s_nop 0\n\t"
        "s_memrealtime s[78:79]\n\t"
        "s_nop 0\n\t"
        "s_memrealtime s[80:81]\n\t"
        "s_nop 0\n\t"
        "s_memrealtime s[82:83]\n\t"
        "s_nop 0\n\t"
        "s_memrealtime s[84:85]\n\t"
        "s_nop 0\n\t"
        "s_memrealtime s[86:87]\n\t"
        "s_nop 0\n\t"
        "s_nop 7\n\t"
        "s_waitcnt lgkmcnt(0)\n\t"
        "s_add_u32 s64, s64, s66\n\t"
        "s_add_u32 s64, s64, s68\n\t"
        "s_add_u32 s64, s64, s70\n\t"
        "s_add_u32 s64, s64, s72\n\t"
        "s_add_u32 s64, s64, s74\n\t"
        "s_add_u32 s64, s64, s76\n\t"
        "s_add_u32 s64, s64, s78\n\t"
        "s_add_u32 s64, s64, s80\n\t"
        "s_add_u32 s64, s64, s82\n\t"
        "s_add_u32 s64, s64, s84\n\t"
        "s_add_u32 s64, s64, s86\n\t"
        "s_lshl_b32 s64, s64, 1\n\t"
        "s_mov_b32 %[c0], s90\n\t"
        "s_mov_b32 %[r0], s64\n\t"
        : [c0] "=&s"(c0), [r0] "=&s"(r0)
        :
        : "s64", "s65", "s66", "s67", "s68", "s69", "s70", "s71", "s72", "s73", "s74", "s75", "s76", "s77", "s78", "s79", "s80", "s81", "s82", "s83", "s84", "s85", "s86", "s87", "s88", "s89", "s90", "s91", "vcc", "scc", "memory");
}

__device__ __forceinline__ void px_step(u32x4& p0, u32& p1, u32 ka, u32 qa, u32 ma, u32 ktr, float ce) {
    asm volatile(
        "ds_read_b128 v[240:243], %[ma] offset:0\n\t"
        "ds_read_b128 v[244:247], %[ma] offset:1024\n\t"
        "ds_read_b128 v[208:211], %[qa] offset:0\n\t"
        "ds_read_b128 v[224:227], %[ka] offset:0\n\t"
        "ds_read_b128 v[212:215], %[qa] offset:1024\n\t"
        "ds_read_b128 v[228:231], %[ka] offset:1024\n\t"
        "ds_read_b128 v[216:219], %[qa] offset:2048\n\t"
        "ds_read_b128 v[232:235], %[ka] offset:2048\n\t"
        "ds_read_b128 v[220:223], %[qa] offset:3072\n\t"
        "ds_read_b128 v[236:239], %[ka] offset:3072\n\t"
        "s_waitcnt lgkmcnt(6)\n\t"
        "v_mfma_f32_16x16x32_bf16 v[240:243], v[224:227], v[208:211], v[240:243]\n\t"
        "ds_read_b128 v[224:227], %[ka] offset:4096\n\t"
        "s_waitcnt lgkmcnt(5)\n\t"
        "v_mfma_f32_16x16x32_bf16 v[240:243], v[228:231], v[212:215], v[240:243]\n\t"
        "ds_read_b128 v[228:231], %[ka] offset:5120\n\t"
        "s_waitcnt lgkmcnt(4)\n\t"
        "v_mfma_f32_16x16x32_bf16 v[240:243], v[232:235], v[216:219], v[240:243]\n\t"
        "ds_read_b128 v[232:235], %[ka] offset:6144\n\t"
        "s_waitcnt lgkmcnt(3)\n\t"
        "v_mfma_f32_16x16x32_bf16 v[240:243], v[236:239], v[220:223], v[240:243]\n\t"
        "ds_read_b128 v[236:239], %[ka] offset:7168\n\t"
        "s_waitcnt lgkmcnt(3)\n\t"
        "v_mfma_f32_16x16x32_bf16 v[244:247], v[224:227], v[208:211], v[244:247]\n\t"
        "ds_read_b64_tr_b16 v[224:225], %[ktr] offset:0\n\t"
        "ds_read_b64_tr_b16 v[226:227], %[ktr] offset:768\n\t"
        "s_waitcnt lgkmcnt(4)\n\t"
        "v_mfma_f32_16x16x32_bf16 v[244:247], v[228:231], v[212:215], v[244:247]\n\t"
        "ds_read_b64_tr_b16 v[228:229], %[ktr] offset:1536\n\t"
        "ds_read_b64_tr_b16 v[230:231], %[ktr] offset:2304\n\t"
        "s_waitcnt lgkmcnt(5)\n\t"
        "v_mfma_f32_16x16x32_bf16 v[244:247], v[232:235], v[216:219], v[244:247]\n\t"
        "ds_read_b64_tr_b16 v[232:233], %[ktr] offset:3072\n\t"
        "ds_read_b64_tr_b16 v[234:235], %[ktr] offset:3840\n\t"
        "s_waitcnt lgkmcnt(6)\n\t"
        "v_mfma_f32_16x16x32_bf16 v[244:247], v[236:239], v[220:223], v[244:247]\n\t"
        "ds_read_b64_tr_b16 v[236:237], %[ktr] offset:4608\n\t"
        "ds_read_b64_tr_b16 v[238:239], %[ktr] offset:5376\n\t"
        "s_waitcnt lgkmcnt(6)\n\t"
        "v_mfma_f32_16x16x32_bf16 v[248:251], v[224:227], v[208:211], 0\n\t"
        "s_waitcnt lgkmcnt(4)\n\t"
        "v_mfma_f32_16x16x32_bf16 v[248:251], v[228:231], v[212:215], v[248:251]\n\t"
        "s_waitcnt lgkmcnt(2)\n\t"
        "v_mfma_f32_16x16x32_bf16 v[248:251], v[232:235], v[216:219], v[248:251]\n\t"
        "s_waitcnt lgkmcnt(0)\n\t"
        "v_mfma_f32_16x16x32_bf16 v[248:251], v[236:239], v[220:223], v[248:251]\n\t"
        "v_max3_f32 v253, v240, v241, v242\n\t"
        "v_max3_f32 v253, v253, v243, v244\n\t"
        "v_max3_f32 v253, v253, v245, v246\n\t"
        "v_max_f32 v253, v253, v247\n\t"
        "s_nop 3\n\t"
        "v_mul_f32 v248, %[ce], v248\n\t"
        "v_mul_f32 v249, %[ce], v249\n\t"
        "v_max3_f32 v253, v253, v248, v249\n\t"
        "v_mov_b32 v254, v253\n\t"
        "s_nop 1\n\t"
        "v_permlane16_swap_b32 v253, v254\n\t"
        "v_max_f32 v253, v253, v254\n\t"
        "v_mov_b32 v254, v253\n\t"
        "s_nop 1\n\t"
        "v_permlane32_swap_b32 v253, v254\n\t"
        "v_max_f32 v253, v253, v254\n\t"
        "v_sub_f32 v240, v240, v253\n\t"
        "v_sub_f32 v241, v241, v253\n\t"
        "v_sub_f32 v242, v242, v253\n\t"
        "v_sub_f32 v243, v243, v253\n\t"
        "v_sub_f32 v244, v244, v253\n\t"
        "v_sub_f32 v245, v245, v253\n\t"
        "v_sub_f32 v246, v246, v253\n\t"
        "v_sub_f32 v247, v247, v253\n\t"
        "v_sub_f32 v248, v248, v253\n\t"
        "v_sub_f32 v249, v249, v253\n\t"
        "v_exp_f32 v240, v240\n\t"
        "v_exp_f32 v241, v241\n\t"
        "v_exp_f32 v242, v242\n\t"
        "v_exp_f32 v243, v243\n\t"
        "v_exp_f32 v244, v244\n\t"
        "v_exp_f32 v245, v245\n\t"
        "v_exp_f32 v246, v246\n\t"
        "v_exp_f32 v247, v247\n\t"
        "v_exp_f32 v248, v248\n\t"
        "v_exp_f32 v249, v249\n\t"
        "v_pk_add_f32 v[252:253], v[240:241], v[242:243]\n\t"
        "v_pk_add_f32 v[254:255], v[244:245], v[246:247]\n\t"
        "v_pk_add_f32 v[252:253], v[252:253], v[248:249]\n\t"
        "v_pk_add_f32 v[252:253], v[252:253], v[254:255]\n\t"
        "v_add_f32 v252, v252, v253\n\t"
        "v_mov_b32 v253, v252\n\t"
        "s_nop 1\n\t"
        "v_permlane16_swap_b32 v252, v253\n\t"
        "v_add_f32 v252, v252, v253\n\t"
        "v_mov_b32 v253, v252\n\t"
        "s_nop 1\n\t"
        "v_permlane32_swap_b32 v252, v253\n\t"
        "v_add_f32 v252, v252, v253\n\t"
        "v_rcp_f32 v252, v252\n\t"
        "s_nop 0\n\t"
        "v_pk_mul_f32 v[240:241], v[240:241], v[252:253] op_sel_hi:[1,0]\n\t"
        "v_pk_mul_f32 v[242:243], v[242:243], v[252:253] op_sel_hi:[1,0]\n\t"
        "v_pk_mul_f32 v[244:245], v[244:245], v[252:253] op_sel_hi:[1,0]\n\t"
        "v_pk_mul_f32 v[246:247], v[246:247], v[252:253] op_sel_hi:[1,0]\n\t"
        "v_pk_mul_f32 v[248:249], v[248:249], v[252:253] op_sel_hi:[1,0]\n\t"
        "v_cvt_pk_bf16_f32 v192, v240, v241\n\t"
        "v_cvt_pk_bf16_f32 v193, v242, v243\n\t"
        "v_cvt_pk_bf16_f32 v194, v244, v245\n\t"
        "v_cvt_pk_bf16_f32 v195, v246, v247\n\t"
        "v_cvt_pk_bf16_f32 v196, v248, v249\n\t"
        : [p0] "=&{v[192:195]}"(p0), [p1] "=&{v196}"(p1)
        : [ka] "v"(ka), [qa] "v"(qa), [ma] "v"(ma), [ktr] "v"(ktr), [ce] "s"(ce)
        : "v200", "v201", "v202", "v203", "v204", "v205", "v206", "v207", "v208", "v209", "v210", "v211", "v212", "v213", "v214", "v215", "v216", "v217", "v218", "v219", "v220", "v221", "v222", "v223", "v224", "v225", "v226", "v227", "v228", "v229", "v230", "v231", "v232", "v233", "v234", "v235", "v236", "v237", "v238", "v239", "v240", "v241", "v242", "v243", "v244", "v245", "v246", "v247", "v248", "v249", "v250", "v251", "v252", "v253", "v254", "v255", "vcc", "scc", "memory");
}

__device__ __forceinline__ void v_a0(const u32x32 (&W)[5], u32& rnd, u32 va, u32 sidx, u32 vw, u32 lb STP) {
    asm volatile(
        "Ltop%=:\n\t"
        "v_mov_b64 v[248:249], 0\n\t"
        "v_mov_b64 v[250:251], 0\n\t"
        "v_mov_b64 v[252:253], 0\n\t"
        "v_mov_b64 v[254:255], 0\n\t"
        "v_mov_b32 v200, 0xffbfffbf\n\t"
        "v_mov_b32 v206, %[lb]\n\t"
        "ds_write_b32 %[vw], v200\n\t"
        "ds_write_b32 %[vw], v200 offset:1024\n\t"
        "s_add_u32 %[rnd], %[rnd], 1\n\t"
        "s_setprio 3\n\t"
        "s_waitcnt lgkmcnt(0)\n\t"
        "s_barrier\n\t"
        "ds_read_b128 v[208:211], %[va] offset:0\n\t"
        "ds_read_b128 v[212:215], %[va] offset:1024\n\t"
        "ds_read_b128 v[216:219], %[va] offset:2048\n\t"
        "ds_read_b128 v[220:223], %[va] offset:3072\n\t"
        "ds_read_b128 v[224:227], %[va] offset:4096\n\t"
        "ds_read_b128 v[228:231], %[va] offset:5120\n\t"
        "s_waitcnt lgkmcnt(5)\n\t"
        "v_smfmac_f32_16x16x64_bf16 v[248:251], v[208:211], v[16:23], %[sidx]\n\t"
        "ds_read_b128 v[232:235], %[va] offset:6144\n\t"
        "v_smfmac_f32_16x16x64_bf16 v[252:255], v[208:211], v[96:103], %[sidx]\n\t"
        "s_waitcnt lgkmcnt(5)\n\t"
        "v_smfmac_f32_16x16x64_bf16 v[248:251], v[212:215], v[24:31], %[sidx]\n\t"
        "ds_read_b128 v[236:239], %[va] offset:7168\n\t"
        "v_smfmac_f32_16x16x64_bf16 v[252:255], v[212:215], v[104:111], %[sidx]\n\t"
        "s_waitcnt lgkmcnt(5)\n\t"
        "v_smfmac_f32_16x16x64_bf16 v[248:251], v[216:219], v[32:39], %[sidx]\n\t"
        "ds_read_b128 v[240:243], %[va] offset:8192\n\t"
        "s_waitcnt lgkmcnt(5)\n\t"
        "v_smfmac_f32_16x16x64_bf16 v[248:251], v[220:223], v[40:47], %[sidx]\n\t"
        "ds_read_b128 v[244:247], %[va] offset:10240\n\t"
        "s_waitcnt lgkmcnt(5)\n\t"
        "v_smfmac_f32_16x16x64_bf16 v[248:251], v[224:227], v[48:55], %[sidx]\n\t"
        "s_waitcnt lgkmcnt(4)\n\t"
        "v_smfmac_f32_16x16x64_bf16 v[248:251], v[228:231], v[56:63], %[sidx]\n\t"
        "s_waitcnt lgkmcnt(3)\n\t"
        "v_smfmac_f32_16x16x64_bf16 v[248:251], v[232:235], v[64:71], %[sidx]\n\t"
        "s_waitcnt lgkmcnt(2)\n\t"
        "v_smfmac_f32_16x16x64_bf16 v[248:251], v[236:239], v[72:79], %[sidx]\n\t"
        "s_waitcnt lgkmcnt(1)\n\t"
        "v_smfmac_f32_16x16x64_bf16 v[248:251], v[240:243], v[80:87], %[sidx]\n\t"
        "s_waitcnt lgkmcnt(0)\n\t"
        "v_smfmac_f32_16x16x64_bf16 v[248:251], v[244:247], v[88:95], %[sidx]\n\t"
        "v_smfmac_f32_16x16x64_bf16 v[252:255], v[216:219], v[112:119], %[sidx]\n\t"
        "v_smfmac_f32_16x16x64_bf16 v[252:255], v[220:223], v[120:127], %[sidx]\n\t"
        "v_add_f32 v248, v248, v250\n\t"
        "v_smfmac_f32_16x16x64_bf16 v[252:255], v[224:227], v[128:135], %[sidx]\n\t"
        "v_add_f32 v249, v249, v251\n\t"
        "v_smfmac_f32_16x16x64_bf16 v[252:255], v[228:231], v[136:143], %[sidx]\n\t"
        "v_cvt_pk_bf16_f32 v200, v248, v249\n\t"
        "v_smfmac_f32_16x16x64_bf16 v[252:255], v[232:235], v[144:151], %[sidx]\n\t"
        "ds_write_b32 %[vw], v200\n\t"
        "v_smfmac_f32_16x16x64_bf16 v[252:255], v[236:239], v[152:159], %[sidx]\n\t"
        "v_cmp_u_f32 vcc, v248, v249\n\t"
        "v_smfmac_f32_16x16x64_bf16 v[252:255], v[240:243], v[160:167], %[sidx]\n\t"
        "v_smfmac_f32_16x16x64_bf16 v[252:255], v[244:247], v[168:175], %[sidx]\n\t"
        "s_nop 7\n\t"
        "v_add_f32 v252, v252, v254\n\t"
        "v_add_f32 v253, v253, v255\n\t"
        "v_cvt_pk_bf16_f32 v201, v252, v253\n\t"
        "ds_write_b32 %[vw], v201 offset:1024\n\t"
        "s_setprio 0\n\t"
        "s_cbranch_vccnz Lnan%=\n\t"
        "Lok%=:\n\t"
        "s_waitcnt lgkmcnt(0)\n\t"
        "s_barrier\n\t"
        "ds_read_b32 v207, v206 offset:14336\n\t"
        "s_waitcnt lgkmcnt(0)\n\t"
        "v_cmp_eq_u32 vcc, %[rnd], v207\n\t"
        "s_barrier\n\t"
        "s_cbranch_vccnz Ltop%=\n\t"
        "s_branch Lend%=\n\t"
        "Lnan%=:\n\t"
        "v_max_u32 v207, v208, v209\n\t"
        "v_max3_u32 v207, v207, v210, v211\n\t"
        "v_max3_u32 v207, v207, v212, v213\n\t"
        "v_max3_u32 v207, v207, v214, v215\n\t"
        "v_max3_u32 v207, v207, v216, v217\n\t"
        "v_max3_u32 v207, v207, v218, v219\n\t"
        "v_max3_u32 v207, v207, v220, v221\n\t"
        "v_max3_u32 v207, v207, v222, v223\n\t"
        "v_max3_u32 v207, v207, v224, v225\n\t"
        "v_max3_u32 v207, v207, v226, v227\n\t"
        "v_max3_u32 v207, v207, v228, v229\n\t"
        "v_max3_u32 v207, v207, v230, v231\n\t"
        "v_max3_u32 v207, v207, v232, v233\n\t"
        "v_max3_u32 v207, v207, v234, v235\n\t"
        "v_max3_u32 v207, v207, v236, v237\n\t"
        "v_max3_u32 v207, v207, v238, v239\n\t"
        "v_max3_u32 v207, v207, v240, v241\n\t"
        "v_max3_u32 v207, v207, v242, v243\n\t"
        "v_max3_u32 v207, v207, v244, v245\n\t"
        "v_max3_u32 v207, v207, v246, v247\n\t"
        "v_cmp_eq_u32 vcc, 0xffbfffbf, v207\n\t"
        "v_mov_b32 v207, %[rnd]\n\t"
        "s_cbranch_vccz Lok%=\n\t"
        "ds_write_b32 v206, v207 offset:14336\n\t"
        "s_branch Lok%=\n\t"
        "Lend%=:\n\t"
        : [rnd] "+&s"(rnd)
        : [w0] "{v[16:47]}"(W[0]), [w1] "{v[48:79]}"(W[1]), [w2] "{v[80:111]}"(W[2]), [w3] "{v[112:143]}"(W[3]), [w4] "{v[144:175]}"(W[4]), [va] "v"(va), [sidx] "v"(sidx), [vw] "v"(vw), [lb] "s"(lb)
        : "v200", "v201", "v202", "v203", "v204", "v205", "v206", "v207", "v208", "v209", "v210", "v211", "v212", "v213", "v214", "v215", "v216", "v217", "v218", "v219", "v220", "v221", "v222", "v223", "v224", "v225", "v226", "v227", "v228", "v229", "v230", "v231", "v232", "v233", "v234", "v235", "v236", "v237", "v238", "v239", "v240", "v241", "v242", "v243", "v244", "v245", "v246", "v247", "v248", "v249", "v250", "v251", "v252", "v253", "v254", "v255", "vcc", "scc", "memory");
}

__device__ __forceinline__ void v_a1(const u32x32 (&W)[5], u32& rnd, u32 va, u32 sidx, u32 vw, u32 lb, u32& left, u32& c0, u32& r0, u32 tg, u32 kr, u32 poll, u32 vm, u32 so, u32 md, u32x4 rs STP) {
    asm volatile(
        "s_mov_b32 s94, m0\n\t"
        "s_add_u32 s92, %[so], 0x8000\n\t"
        "s_add_u32 s93, %[so], 0x10000\n\t"
        "s_mov_b32 m0, %[md]\n\t"
        "s_setprio 3\n\t"
        "s_cmp_eq_u32 %[poll], 0\n\t"
        "s_cbranch_scc1 Ltop%=\n\t"
        "Lda_%=:\n\t"
        "s_memrealtime s[90:91]\n\t"
        "s_waitcnt lgkmcnt(0)\n\t"
        "s_mul_i32 s90, s90, 24\n\t"
        "s_sub_u32 s88, %[tg], s90\n\t"
        "s_sub_u32 s89, s88, 620\n\t"
        "s_cmp_gt_u32 s89, 0x4000\n\t"
        "s_cbranch_scc1 Lea_%=\n\t"
        "s_sleep 1\n\t"
        "s_branch Lda_%=\n\t"
        "Lea_%=:\n\t"
        "s_memrealtime s[64:65]\n\t"
        "s_nop 0\n\t"
        "s_memrealtime s[66:67]\n\t"
        "s_nop 0\n\t"
        "s_memrealtime s[68:69]\n\t"
        "s_nop 0\n\t"
        "s_memrealtime s[70:71]\n\t"
        "s_nop 0\n\t"
        "s_memrealtime s[72:73]\n\t"
        "s_nop 0\n\t"
        "s_memrealtime s[74:75]\n\t"
        "s_memtime s[90:91]\n\t"
        "s_memrealtime s[76:77]\n\t"
        "s_nop 0\n\t"
        "s_memrealtime s[78:79]\n\t"
        "s_nop 0\n\t"
        "s_memrealtime s[80:81]\n\t"
        "s_nop 0\n\t"
        "s_memrealtime s[82:83]\n\t"
        "s_nop 0\n\t"
        "s_memrealtime s[84:85]\n\t"
        "s_nop 0\n\t"
        "s_memrealtime s[86:87]\n\t"
        "s_nop 0\n\t"
        "s_nop 7\n\t"
        "s_waitcnt lgkmcnt(0)\n\t"
        "s_add_u32 s64, s64, s66\n\t"
        "s_add_u32 s64, s64, s68\n\t"
        "s_add_u32 s64, s64, s70\n\t"
        "s_add_u32 s64, s64, s72\n\t"
        "s_add_u32 s64, s64, s74\n\t"
        "s_add_u32 s64, s64, s76\n\t"
        "s_add_u32 s64, s64, s78\n\t"
        "s_add_u32 s64, s64, s80\n\t"
        "s_add_u32 s64, s64, s82\n\t"
        "s_add_u32 s64, s64, s84\n\t"
        "s_add_u32 s64, s64, s86\n\t"
        "s_lshl_b32 s64, s64, 1\n\t"
        "s_mov_b32 %[c0], s90\n\t"
        "s_mov_b32 %[r0], s64\n\t"
        "s_sub_u32 s88, %[tg], s64\n\t"
        "s_mul_i32 s88, s88, %[kr]\n\t"
        "s_ashr_i32 s88, s88, 12\n\t"
        "s_sub_u32 s88, s88, 244\n\t"
        "s_nop 1\n\t"
        "s_max_i32 s88, s88, 0\n\t"
        "s_min_i32 s88, s88, 384\n\t"
        "s_andn2_b32 s88, s88, 3\n\t"
        "s_getpc_b64 s[90:91]\n\t"
        "Lga_%=:\n\t"
        "s_sub_u32 s90, s90, s88\n\t"
        "s_subb_u32 s91, s91, 0\n\t"
        "s_add_u32 s90, s90, Lza_%=-Lga_%=\n\t"
        "s_addc_u32 s91, s91, 0\n\t"
        "s_setpc_b64 s[90:91]\n\t"
        "s_nop 0\n\ts_nop 0\n\ts_nop 0\n\ts_nop 0\n\t"
        "s_nop 0\n\ts_nop 0\n\ts_nop 0\n\ts_nop 0\n\t"
        "s_nop 0\n\ts_nop 0\n\ts_nop 0\n\ts_nop 0\n\t"
        "s_nop 0\n\ts_nop 0\n\ts_nop 0\n\ts_nop 0\n\t"
        "s_nop 0\n\ts_nop 0\n\ts_nop 0\n\ts_nop 0\n\t"
        "s_nop 0\n\ts_nop 0\n\ts_nop 0\n\ts_nop 0\n\t"
        "s_nop 0\n\ts_nop 0\n\ts_nop 0\n\ts_nop 0\n\t"
        "s_nop 0\n\ts_nop 0\n\ts_nop 0\n\ts_nop 0\n\t"
        "s_nop 0\n\ts_nop 0\n\ts_nop 0\n\ts_nop 0\n\t"
        "s_nop 0\n\ts_nop 0\n\ts_nop 0\n\ts_nop 0\n\t"
        "s_nop 0\n\ts_nop 0\n\ts_nop 0\n\ts_nop 0\n\t"
        "s_nop 0\n\ts_nop 0\n\ts_nop 0\n\ts_nop 0\n\t"
        "s_nop 0\n\ts_nop 0\n\ts_nop 0\n\ts_nop 0\n\t"
        "s_nop 0\n\ts_nop 0\n\ts_nop 0\n\ts_nop 0\n\t"
        "s_nop 0\n\ts_nop 0\n\ts_nop 0\n\ts_nop 0\n\t"
        "s_nop 0\n\ts_nop 0\n\ts_nop 0\n\ts_nop 0\n\t"
        "s_nop 0\n\ts_nop 0\n\ts_nop 0\n\ts_nop 0\n\t"
        "s_nop 0\n\ts_nop 0\n\ts_nop 0\n\ts_nop 0\n\t"
        "s_nop 0\n\ts_nop 0\n\ts_nop 0\n\ts_nop 0\n\t"
        "s_nop 0\n\ts_nop 0\n\ts_nop 0\n\ts_nop 0\n\t"
        "s_nop 0\n\ts_nop 0\n\ts_nop 0\n\ts_nop 0\n\t"
        "s_nop 0\n\ts_nop 0\n\ts_nop 0\n\ts_nop 0\n\t"
        "s_nop 0\n\ts_nop 0\n\ts_nop 0\n\ts_nop 0\n\t"
        "s_nop 0\n\ts_nop 0\n\ts_nop 0\n\ts_nop 0\n\t"
        "Lza_%=:\n\t"
        "buffer_load_dwordx4 %[vm], %[rs], %[so] offen offset:0 sc1 lds\n\t"
        "buffer_load_dwordx4 %[vm], %[rs], %[so] offen offset:1024 sc1 lds\n\t"
        "buffer_load_dwordx4 %[vm], %[rs], %[so] offen offset:2048 sc1 lds\n\t"
        "buffer_load_dwordx4 %[vm], %[rs], %[so] offen offset:3072 sc1 lds\n\t"
        "s_add_u32 m0, m0, 0x1000\n\t"
        "s_nop 0\n\t"
        "buffer_load_dwordx4 %[vm], %[rs], s92 offen offset:0 sc1 lds\n\t"
        "buffer_load_dwordx4 %[vm], %[rs], s92 offen offset:1024 sc1 lds\n\t"
        "buffer_load_dwordx4 %[vm], %[rs], s92 offen offset:2048 sc1 lds\n\t"
        "buffer_load_dwordx4 %[vm], %[rs], s92 offen offset:3072 sc1 lds\n\t"
        "s_add_u32 m0, m0, 0x1000\n\t"
        "s_nop 0\n\t"
        "buffer_load_dwordx4 %[vm], %[rs], s93 offen offset:0 sc1 lds\n\t"
        "buffer_load_dwordx4 %[vm], %[rs], s93 offen offset:2048 sc1 lds\n\t"
        "Ltop%=:\n\t"
        "v_mov_b64 v[248:249], 0\n\t"
        "v_mov_b64 v[250:251], 0\n\t"
        "v_mov_b64 v[252:253], 0\n\t"
        "v_mov_b64 v[254:255], 0\n\t"
        "v_mov_b32 v200, 0xffbfffbf\n\t"
        "v_mov_b32 v206, %[lb]\n\t"
        "ds_write_b32 %[vw], v200\n\t"
        "ds_write_b32 %[vw], v200 offset:1024\n\t"
        "s_add_u32 %[rnd], %[rnd], 1\n\t"
        "s_setprio 3\n\t"
        "s_mov_b32 s95, 0\n\t"
        "s_waitcnt vmcnt(0) lgkmcnt(0)\n\t"
        "s_barrier\n\t"
        "ds_read_b128 v[208:211], %[va] offset:0\n\t"
        "ds_read_b128 v[212:215], %[va] offset:1024\n\t"
        "ds_read_b128 v[216:219], %[va] offset:2048\n\t"
        "ds_read_b128 v[220:223], %[va] offset:3072\n\t"
        "ds_read_b128 v[224:227], %[va] offset:4096\n\t"
        "ds_read_b128 v[228:231], %[va] offset:5120\n\t"
        "s_waitcnt lgkmcnt(5)\n\t"
        "v_smfmac_f32_16x16x64_bf16 v[248:251], v[208:211], v[16:23], %[sidx]\n\t"
        "ds_read_b128 v[232:235], %[va] offset:6144\n\t"
        "v_smfmac_f32_16x16x64_bf16 v[252:255], v[208:211], v[96:103], %[sidx]\n\t"
        "s_waitcnt lgkmcnt(5)\n\t"
        "v_smfmac_f32_16x16x64_bf16 v[248:251], v[212:215], v[24:31], %[sidx]\n\t"
        "ds_read_b128 v[236:239], %[va] offset:7168\n\t"
        "v_smfmac_f32_16x16x64_bf16 v[252:255], v[212:215], v[104:111], %[sidx]\n\t"
        "s_waitcnt lgkmcnt(5)\n\t"
        "v_smfmac_f32_16x16x64_bf16 v[248:251], v[216:219], v[32:39], %[sidx]\n\t"
        "ds_read_b128 v[240:243], %[va] offset:8192\n\t"
        "s_waitcnt lgkmcnt(5)\n\t"
        "v_smfmac_f32_16x16x64_bf16 v[248:251], v[220:223], v[40:47], %[sidx]\n\t"
        "ds_read_b128 v[244:247], %[va] offset:10240\n\t"
        "s_waitcnt lgkmcnt(5)\n\t"
        "v_smfmac_f32_16x16x64_bf16 v[248:251], v[224:227], v[48:55], %[sidx]\n\t"
        "s_waitcnt lgkmcnt(4)\n\t"
        "v_smfmac_f32_16x16x64_bf16 v[248:251], v[228:231], v[56:63], %[sidx]\n\t"
        "s_waitcnt lgkmcnt(3)\n\t"
        "v_smfmac_f32_16x16x64_bf16 v[248:251], v[232:235], v[64:71], %[sidx]\n\t"
        "s_waitcnt lgkmcnt(2)\n\t"
        "v_smfmac_f32_16x16x64_bf16 v[248:251], v[236:239], v[72:79], %[sidx]\n\t"
        "s_waitcnt lgkmcnt(1)\n\t"
        "v_smfmac_f32_16x16x64_bf16 v[248:251], v[240:243], v[80:87], %[sidx]\n\t"
        "s_waitcnt lgkmcnt(0)\n\t"
        "v_smfmac_f32_16x16x64_bf16 v[248:251], v[244:247], v[88:95], %[sidx]\n\t"
        "v_smfmac_f32_16x16x64_bf16 v[252:255], v[216:219], v[112:119], %[sidx]\n\t"
        "v_smfmac_f32_16x16x64_bf16 v[252:255], v[220:223], v[120:127], %[sidx]\n\t"
        "v_add_f32 v248, v248, v250\n\t"
        "v_smfmac_f32_16x16x64_bf16 v[252:255], v[224:227], v[128:135], %[sidx]\n\t"
        "v_add_f32 v249, v249, v251\n\t"
        "v_smfmac_f32_16x16x64_bf16 v[252:255], v[228:231], v[136:143], %[sidx]\n\t"
        "v_cvt_pk_bf16_f32 v200, v248, v249\n\t"
        "v_smfmac_f32_16x16x64_bf16 v[252:255], v[232:235], v[144:151], %[sidx]\n\t"
        "ds_write_b32 %[vw], v200\n\t"
        "v_smfmac_f32_16x16x64_bf16 v[252:255], v[236:239], v[152:159], %[sidx]\n\t"
        "v_cmp_u_f32 vcc, v248, v249\n\t"
        "v_smfmac_f32_16x16x64_bf16 v[252:255], v[240:243], v[160:167], %[sidx]\n\t"
        "v_smfmac_f32_16x16x64_bf16 v[252:255], v[244:247], v[168:175], %[sidx]\n\t"
        "s_nop 7\n\t"
        "v_add_f32 v252, v252, v254\n\t"
        "v_add_f32 v253, v253, v255\n\t"
        "v_cvt_pk_bf16_f32 v201, v252, v253\n\t"
        "ds_write_b32 %[vw], v201 offset:1024\n\t"
        "s_setprio 0\n\t"
        "s_cbranch_vccnz Lnan%=\n\t"
        "Lok%=:\n\t"
        "s_waitcnt lgkmcnt(0)\n\t"
        "s_barrier\n\t"
        "ds_read_b32 v207, v206 offset:14336\n\t"
        "s_waitcnt lgkmcnt(0)\n\t"
        "v_cmp_eq_u32 vcc, %[rnd], v207\n\t"
        "s_barrier\n\t"
        "s_cbranch_vccnz Lck%=\n\t"
        "s_branch Lend%=\n\t"
        "Lnan%=:\n\t"
        "s_cmp_lg_u32 %[poll], 1\n\t"
        "s_cbranch_scc1 Lok%=\n\t"
        "s_mov_b32 m0, %[lb]\n\t"
        "v_add_u32 v204, %[lb], %[vm]\n\t"
        "buffer_load_dwordx4 %[vm], %[rs], %[so] offen offset:0 sc1 lds\n\t"
        "buffer_load_dwordx4 %[vm], %[rs], %[so] offen offset:1024 sc1 lds\n\t"
        "buffer_load_dwordx4 %[vm], %[rs], %[so] offen offset:2048 sc1 lds\n\t"
        "buffer_load_dwordx4 %[vm], %[rs], %[so] offen offset:3072 sc1 lds\n\t"
        "s_add_u32 m0, m0, 0x1000\n\t"
        "s_nop 0\n\t"
        "buffer_load_dwordx4 %[vm], %[rs], s92 offen offset:0 sc1 lds\n\t"
        "buffer_load_dwordx4 %[vm], %[rs], s92 offen offset:1024 sc1 lds\n\t"
        "buffer_load_dwordx4 %[vm], %[rs], s92 offen offset:2048 sc1 lds\n\t"
        "buffer_load_dwordx4 %[vm], %[rs], s92 offen offset:3072 sc1 lds\n\t"
        "s_add_u32 m0, m0, 0x1000\n\t"
        "s_nop 0\n\t"
        "buffer_load_dwordx4 %[vm], %[rs], s93 offen offset:0 sc1 lds\n\t"
        "buffer_load_dwordx4 %[vm], %[rs], s93 offen offset:2048 sc1 lds\n\t"
        "v_max_u32 v207, v208, v209\n\t"
        "v_max3_u32 v207, v207, v210, v211\n\t"
        "v_max3_u32 v207, v207, v212, v213\n\t"
        "v_max3_u32 v207, v207, v214, v215\n\t"
        "v_max3_u32 v207, v207, v216, v217\n\t"
        "v_max3_u32 v207, v207, v218, v219\n\t"
        "v_max3_u32 v207, v207, v220, v221\n\t"
        "v_max3_u32 v207, v207, v222, v223\n\t"
        "v_max3_u32 v207, v207, v224, v225\n\t"
        "v_max3_u32 v207, v207, v226, v227\n\t"
        "v_max3_u32 v207, v207, v228, v229\n\t"
        "v_max3_u32 v207, v207, v230, v231\n\t"
        "v_max3_u32 v207, v207, v232, v233\n\t"
        "v_max3_u32 v207, v207, v234, v235\n\t"
        "v_max3_u32 v207, v207, v236, v237\n\t"
        "v_max3_u32 v207, v207, v238, v239\n\t"
        "v_max3_u32 v207, v207, v240, v241\n\t"
        "v_max3_u32 v207, v207, v242, v243\n\t"
        "v_max3_u32 v207, v207, v244, v245\n\t"
        "v_max3_u32 v207, v207, v246, v247\n\t"
        "v_cmp_eq_u32 vcc, 0xffbfffbf, v207\n\t"
        "v_mov_b32 v207, %[rnd]\n\t"
        "s_cbranch_vccz Lok%=\n\t"
        "ds_write_b32 v206, v207 offset:14336\n\t"
        "s_mov_b32 s95, 1\n\t"
        "s_waitcnt lgkmcnt(0)\n\t"
        "s_branch Lcv%=\n\t"
        "Lck%=:\n\t"
        "v_add_u32 v204, %[lb], %[vm]\n\t"
        "Lcl%=:\n\t"
        "s_mov_b32 m0, %[lb]\n\t"
        "s_nop 0\n\t"
        "buffer_load_dwordx4 %[vm], %[rs], %[so] offen offset:0 sc1 lds\n\t"
        "buffer_load_dwordx4 %[vm], %[rs], %[so] offen offset:1024 sc1 lds\n\t"
        "buffer_load_dwordx4 %[vm], %[rs], %[so] offen offset:2048 sc1 lds\n\t"
        "buffer_load_dwordx4 %[vm], %[rs], %[so] offen offset:3072 sc1 lds\n\t"
        "s_add_u32 m0, m0, 0x1000\n\t"
        "s_nop 0\n\t"
        "buffer_load_dwordx4 %[vm], %[rs], s92 offen offset:0 sc1 lds\n\t"
        "buffer_load_dwordx4 %[vm], %[rs], s92 offen offset:1024 sc1 lds\n\t"
        "buffer_load_dwordx4 %[vm], %[rs], s92 offen offset:2048 sc1 lds\n\t"
        "buffer_load_dwordx4 %[vm], %[rs], s92 offen offset:3072 sc1 lds\n\t"
        "s_add_u32 m0, m0, 0x1000\n\t"
        "s_nop 0\n\t"
        "buffer_load_dwordx4 %[vm], %[rs], s93 offen offset:0 sc1 lds\n\t"
        "buffer_load_dwordx4 %[vm], %[rs], s93 offen offset:2048 sc1 lds\n\t"
        "Lcv%=:\n\t"
        "s_sub_u32 %[left], %[left], 1\n\t"
        "s_waitcnt vmcnt(9)\n\t"
        "ds_read_b128 v[208:211], v204 offset:0\n\t"
        "s_waitcnt vmcnt(8)\n\t"
        "ds_read_b128 v[212:215], v204 offset:1024\n\t"
        "s_waitcnt vmcnt(7)\n\t"
        "ds_read_b128 v[216:219], v204 offset:2048\n\t"
        "s_waitcnt vmcnt(6)\n\t"
        "ds_read_b128 v[220:223], v204 offset:3072\n\t"
        "s_waitcnt vmcnt(5)\n\t"
        "ds_read_b128 v[224:227], v204 offset:4096\n\t"
        "s_waitcnt vmcnt(4)\n\t"
        "ds_read_b128 v[228:231], v204 offset:5120\n\t"
        "s_waitcnt lgkmcnt(5)\n\t"
        "v_max3_u32 v207, v208, v208, v209\n\t"
        "v_max3_u32 v207, v207, v210, v211\n\t"
        "s_waitcnt vmcnt(3)\n\t"
        "ds_read_b128 v[232:235], v204 offset:6144\n\t"
        "s_waitcnt lgkmcnt(5)\n\t"
        "v_max3_u32 v207, v207, v212, v213\n\t"
        "v_max3_u32 v207, v207, v214, v215\n\t"
        "s_waitcnt vmcnt(2)\n\t"
        "ds_read_b128 v[236:239], v204 offset:7168\n\t"
        "s_waitcnt lgkmcnt(5)\n\t"
        "v_max3_u32 v207, v207, v216, v217\n\t"
        "v_max3_u32 v207, v207, v218, v219\n\t"
        "s_waitcnt vmcnt(1)\n\t"
        "ds_read_b128 v[240:243], v204 offset:8192\n\t"
        "s_waitcnt lgkmcnt(5)\n\t"
        "v_max3_u32 v207, v207, v220, v221\n\t"
        "v_max3_u32 v207, v207, v222, v223\n\t"
        "s_waitcnt vmcnt(0)\n\t"
        "ds_read_b128 v[244:247], v204 offset:10240\n\t"
        "s_waitcnt lgkmcnt(5)\n\t"
        "v_max3_u32 v207, v207, v224, v225\n\t"
        "v_max3_u32 v207, v207, v226, v227\n\t"
        "s_waitcnt lgkmcnt(4)\n\t"
        "v_max3_u32 v207, v207, v228, v229\n\t"
        "v_max3_u32 v207, v207, v230, v231\n\t"
        "s_waitcnt lgkmcnt(3)\n\t"
        "v_max3_u32 v207, v207, v232, v233\n\t"
        "v_max3_u32 v207, v207, v234, v235\n\t"
        "s_waitcnt lgkmcnt(2)\n\t"
        "v_max3_u32 v207, v207, v236, v237\n\t"
        "v_max3_u32 v207, v207, v238, v239\n\t"
        "s_waitcnt lgkmcnt(1)\n\t"
        "v_max3_u32 v207, v207, v240, v241\n\t"
        "v_max3_u32 v207, v207, v242, v243\n\t"
        "s_waitcnt lgkmcnt(0)\n\t"
        "v_max3_u32 v207, v207, v244, v245\n\t"
        "v_max3_u32 v207, v207, v246, v247\n\t"
        "v_cmp_eq_u32 vcc, 0xffbfffbf, v207\n\t"
        "s_cbranch_vccz Lcd%=\n\t"
        "s_cbranch_scc0 Lcl%=\n\t"
        "Lcd%=:\n\t"
        "s_cmp_eq_u32 s95, 0\n\t"
        "s_cbranch_scc1 Lce%=\n\t"
        "s_barrier\n\t"
        "s_barrier\n\t"
        "Lce%=:\n\t"
        "s_cbranch_vccz Ltop%=\n\t"
        "Lend%=:\n\t"
        "s_mov_b32 m0, s94\n\t"
        "s_waitcnt vmcnt(0)\n\t"
        : [rnd] "+&s"(rnd), [c0] "+&s"(c0), [r0] "+&s"(r0), [left] "+&s"(left)
        : [w0] "{v[16:47]}"(W[0]), [w1] "{v[48:79]}"(W[1]), [w2] "{v[80:111]}"(W[2]), [w3] "{v[112:143]}"(W[3]), [w4] "{v[144:175]}"(W[4]), [va] "v"(va), [sidx] "v"(sidx), [vw] "v"(vw), [rs] "s"(rs), [tg] "s"(tg), [kr] "s"(kr), [poll] "s"(poll), [vm] "v"(vm), [so] "s"(so), [lb] "s"(lb), [md] "s"(md)
        : "s64", "s65", "s66", "s67", "s68", "s69", "s70", "s71", "s72", "s73", "s74", "s75", "s76", "s77", "s78", "s79", "s80", "s81", "s82", "s83", "s84", "s85", "s86", "s87", "s88", "s89", "s90", "s91", "s92", "s93", "s94", "s95", "v200", "v201", "v202", "v203", "v204", "v205", "v206", "v207", "v208", "v209", "v210", "v211", "v212", "v213", "v214", "v215", "v216", "v217", "v218", "v219", "v220", "v221", "v222", "v223", "v224", "v225", "v226", "v227", "v228", "v229", "v230", "v231", "v232", "v233", "v234", "v235", "v236", "v237", "v238", "v239", "v240", "v241", "v242", "v243", "v244", "v245", "v246", "v247", "v248", "v249", "v250", "v251", "v252", "v253", "v254", "v255", "vcc", "scc", "memory");
}

__device__ __forceinline__ void v_b(const u32x32 (&W)[5], u32& rnd, u32 sidx, u32 vr, u32 z0, u32 z1, u32 z2, u32x4 rs, u32 lb STP) {
    asm volatile(
        "Ltop%=:\n\t"
        "v_mov_b64 v[224:225], 0\n\t"
        "v_mov_b64 v[226:227], 0\n\t"
        "v_mov_b64 v[228:229], 0\n\t"
        "v_mov_b64 v[230:231], 0\n\t"
        "v_mov_b64 v[232:233], 0\n\t"
        "v_mov_b64 v[234:235], 0\n\t"
        "v_mov_b64 v[236:237], 0\n\t"
        "v_mov_b64 v[238:239], 0\n\t"
        "v_mov_b64 v[240:241], 0\n\t"
        "v_mov_b64 v[242:243], 0\n\t"
        "v_mov_b32 v206, %[lb]\n\t"
        "s_add_u32 %[rnd], %[rnd], 1\n\t"
        "s_mov_b32 s92, 0\n\t"
        "s_setprio 2\n\t"
        "s_barrier\n\t"
        "s_nop 15\n\t"
        "s_nop 15\n\t"
        "s_nop 15\n\t"
        "s_nop 15\n\t"
        "s_nop 15\n\t"
        "s_nop 7\n\t"
        "Lre%=:\n\t"
        "ds_read_b64_tr_b16 v[208:209], %[vr] offset:0\n\t"
        "ds_read_b64_tr_b16 v[210:211], %[vr] offset:64\n\t"
        "ds_read_b64_tr_b16 v[212:213], %[vr] offset:512\n\t"
        "ds_read_b64_tr_b16 v[214:215], %[vr] offset:576\n\t"
        "s_waitcnt lgkmcnt(0)\n\t"
        "v_smfmac_f32_16x16x64_bf16 v[224:227], v[208:211], v[16:23], %[sidx]\n\t"
        "v_smfmac_f32_16x16x64_bf16 v[228:231], v[208:211], v[48:55], %[sidx]\n\t"
        "v_smfmac_f32_16x16x64_bf16 v[232:235], v[208:211], v[80:87], %[sidx]\n\t"
        "v_smfmac_f32_16x16x64_bf16 v[236:239], v[208:211], v[112:119], %[sidx]\n\t"
        "v_smfmac_f32_16x16x64_bf16 v[240:243], v[208:211], v[144:151], %[sidx]\n\t"
        "ds_read_b64_tr_b16 v[216:217], %[vr] offset:1024\n\t"
        "v_smfmac_f32_16x16x64_bf16 v[224:227], v[212:215], v[24:31], %[sidx]\n\t"
        "ds_read_b64_tr_b16 v[218:219], %[vr] offset:1088\n\t"
        "v_smfmac_f32_16x16x64_bf16 v[228:231], v[212:215], v[56:63], %[sidx]\n\t"
        "ds_read_b64_tr_b16 v[220:221], %[vr] offset:1536\n\t"
        "v_smfmac_f32_16x16x64_bf16 v[232:235], v[212:215], v[88:95], %[sidx]\n\t"
        "ds_read_b64_tr_b16 v[222:223], %[vr] offset:1600\n\t"
        "v_smfmac_f32_16x16x64_bf16 v[236:239], v[212:215], v[120:127], %[sidx]\n\t"
        "v_smfmac_f32_16x16x64_bf16 v[240:243], v[212:215], v[152:159], %[sidx]\n\t"
        "s_waitcnt lgkmcnt(0)\n\t"
        "v_smfmac_f32_16x16x64_bf16 v[224:227], v[216:219], v[32:39], %[sidx]\n\t"
        "v_smfmac_f32_16x16x64_bf16 v[224:227], v[220:223], v[40:47], %[sidx]\n\t"
        "v_smfmac_f32_16x16x64_bf16 v[228:231], v[216:219], v[64:71], %[sidx]\n\t"
        "v_smfmac_f32_16x16x64_bf16 v[228:231], v[220:223], v[72:79], %[sidx]\n\t"
        "v_fma_f32 v205, v224, v225, v226\n\t"
        "v_smfmac_f32_16x16x64_bf16 v[232:235], v[216:219], v[96:103], %[sidx]\n\t"
        "v_cmp_u_f32 vcc, v205, v227\n\t"
        "v_smfmac_f32_16x16x64_bf16 v[232:235], v[220:223], v[104:111], %[sidx]\n\t"
        "v_cvt_pk_bf16_f32 v244, v224, v225\n\t"
        "v_smfmac_f32_16x16x64_bf16 v[236:239], v[216:219], v[128:135], %[sidx]\n\t"
        "v_cvt_pk_bf16_f32 v245, v226, v227\n\t"
        "v_smfmac_f32_16x16x64_bf16 v[236:239], v[220:223], v[136:143], %[sidx]\n\t"
        "v_cvt_pk_bf16_f32 v246, v228, v229\n\t"
        "v_smfmac_f32_16x16x64_bf16 v[240:243], v[216:219], v[160:167], %[sidx]\n\t"
        "v_cvt_pk_bf16_f32 v247, v230, v231\n\t"
        "v_smfmac_f32_16x16x64_bf16 v[240:243], v[220:223], v[168:175], %[sidx]\n\t"
        "s_cbranch_vccnz Lnan%=\n\t"
        "Lst%=:\n\t"
        "v_permlane16_swap_b32 v244, v246\n\t"
        "v_permlane16_swap_b32 v245, v247\n\t"
        "buffer_store_dwordx4 v[244:247], %[z0], %[rs], 0 offen sc1\n\t"
        "v_cvt_pk_bf16_f32 v248, v232, v233\n\t"
        "v_cvt_pk_bf16_f32 v249, v234, v235\n\t"
        "v_cvt_pk_bf16_f32 v250, v236, v237\n\t"
        "v_cvt_pk_bf16_f32 v251, v238, v239\n\t"
        "s_nop 0\n\t"
        "v_permlane16_swap_b32 v248, v250\n\t"
        "v_permlane16_swap_b32 v249, v251\n\t"
        "buffer_store_dwordx4 v[248:251], %[z1], %[rs], 0 offen sc1\n\t"
        "v_cvt_pk_bf16_f32 v252, v240, v241\n\t"
        "v_cvt_pk_bf16_f32 v254, v240, v241\n\t"
        "v_cvt_pk_bf16_f32 v253, v242, v243\n\t"
        "v_cvt_pk_bf16_f32 v255, v242, v243\n\t"
        "s_nop 0\n\t"
        "v_permlane16_swap_b32 v252, v254\n\t"
        "v_permlane16_swap_b32 v253, v255\n\t"
        "buffer_store_dwordx4 v[252:255], %[z2], %[rs], 0 offen sc1\n\t"
        "s_setprio 0\n\t"
        "s_cmp_eq_u32 s92, 2\n\t"
        "s_cbranch_scc1 Ly%=\n\t"
        "Lx%=:\n\t"
        "s_waitcnt lgkmcnt(0)\n\t"
        "s_barrier\n\t"
        "ds_read_b32 v207, v206 offset:14336\n\t"
        "s_waitcnt lgkmcnt(0)\n\t"
        "v_cmp_eq_u32 vcc, %[rnd], v207\n\t"
        "s_cmp_eq_u32 s92, 1\n\t"
        "s_cbranch_vccnz Lz%=\n\t"
        "s_cbranch_scc0 Ly%=\n\t"
        "s_mov_b32 s92, 2\n\t"
        "v_mov_b64 v[224:225], 0\n\t"
        "v_mov_b64 v[226:227], 0\n\t"
        "v_mov_b64 v[228:229], 0\n\t"
        "v_mov_b64 v[230:231], 0\n\t"
        "v_mov_b64 v[232:233], 0\n\t"
        "v_mov_b64 v[234:235], 0\n\t"
        "v_mov_b64 v[236:237], 0\n\t"
        "v_mov_b64 v[238:239], 0\n\t"
        "v_mov_b64 v[240:241], 0\n\t"
        "v_mov_b64 v[242:243], 0\n\t"
        "s_branch Lre%=\n\t"
        "Lnan%=:\n\t"
        "s_cmp_eq_u32 s92, 2\n\t"
        "s_cbranch_scc1 Lst%=\n\t"
        "s_mov_b32 s92, 1\n\t"
        "s_branch Lx%=\n\t"
        "Lz%=:\n\t"
        "s_barrier\n\t"
        "s_branch Ltop%=\n\t"
        "Ly%=:\n\t"
        "s_barrier\n\t"
        : [rnd] "+&s"(rnd)
        : [w0] "{v[16:47]}"(W[0]), [w1] "{v[48:79]}"(W[1]), [w2] "{v[80:111]}"(W[2]), [w3] "{v[112:143]}"(W[3]), [w4] "{v[144:175]}"(W[4]), [sidx] "v"(sidx), [vr] "v"(vr), [z0] "v"(z0), [z1] "v"(z1), [z2] "v"(z2), [rs] "s"(rs), [lb] "s"(lb)
        : "s92", "v205", "v206", "v207", "v208", "v209", "v210", "v211", "v212", "v213", "v214", "v215", "v216", "v217", "v218", "v219", "v220", "v221", "v222", "v223", "v224", "v225", "v226", "v227", "v228", "v229", "v230", "v231", "v232", "v233", "v234", "v235", "v236", "v237", "v238", "v239", "v240", "v241", "v242", "v243", "v244", "v245", "v246", "v247", "v248", "v249", "v250", "v251", "v252", "v253", "v254", "v255", "vcc", "scc", "memory");
}

__device__ __forceinline__ void l_g(u32& miss, u32& c0, u32& r0, u64& tl, u32 tg, u32 kr, u32 m0o, u32 m1o, u32 so, u32x4 rs, u32x4 rec, u32 sr STP) {
    asm volatile(
        "s_mov_b32 %[miss], 1\n\t"
        "v_or_b32 v62, 0x10000, v59\n\t"
        "s_nop 1\n\t"
        "s_cmp_eq_u32 %[kr], 0\n\t"
        "s_cbranch_scc1 Lza_%=\n\t"
        "Lda_%=:\n\t"
        "s_memrealtime s[90:91]\n\t"
        "s_waitcnt lgkmcnt(0)\n\t"
        "s_mul_i32 s90, s90, 24\n\t"
        "s_sub_u32 s88, %[tg], s90\n\t"
        "s_sub_u32 s89, s88, 620\n\t"
        "s_cmp_gt_u32 s89, 0x4000\n\t"
        "s_cbranch_scc1 Lea_%=\n\t"
        "s_sleep 1\n\t"
        "s_branch Lda_%=\n\t"
        "Lea_%=:\n\t"
        "s_memrealtime s[64:65]\n\t"
        "s_nop 0\n\t"
        "s_memrealtime s[66:67]\n\t"
        "s_nop 0\n\t"
        "s_memrealtime s[68:69]\n\t"
        "s_nop 0\n\t"
        "s_memrealtime s[70:71]\n\t"
        "s_nop 0\n\t"
        "s_memrealtime s[72:73]\n\t"
        "s_nop 0\n\t"
        "s_memrealtime s[74:75]\n\t"
        "s_memtime s[90:91]\n\t"
        "s_memrealtime s[76:77]\n\t"
        "s_nop 0\n\t"
        "s_memrealtime s[78:79]\n\t"
        "s_nop 0\n\t"
        "s_memrealtime s[80:81]\n\t"
        "s_nop 0\n\t"
        "s_memrealtime s[82:83]\n\t"
        "s_nop 0\n\t"
        "s_memrealtime s[84:85]\n\t"
        "s_nop 0\n\t"
        "s_memrealtime s[86:87]\n\t"
        "s_nop 0\n\t"
        "s_nop 7\n\t"
        "s_waitcnt lgkmcnt(0)\n\t"
        "s_add_u32 s64, s64, s66\n\t"
        "s_add_u32 s64, s64, s68\n\t"
        "s_add_u32 s64, s64, s70\n\t"
        "s_add_u32 s64, s64, s72\n\t"
        "s_add_u32 s64, s64, s74\n\t"
        "s_add_u32 s64, s64, s76\n\t"
        "s_add_u32 s64, s64, s78\n\t"
        "s_add_u32 s64, s64, s80\n\t"
        "s_add_u32 s64, s64, s82\n\t"
        "s_add_u32 s64, s64, s84\n\t"
        "s_add_u32 s64, s64, s86\n\t"
        "s_lshl_b32 s64, s64, 1\n\t"
        "s_mov_b32 %[c0], s90\n\t"
        "s_mov_b32 %[r0], s64\n\t"
        "s_sub_u32 s88, %[tg], s64\n\t"
        "s_mul_i32 s88, s88, %[kr]\n\t"
        "s_ashr_i32 s88, s88, 12\n\t"
        "s_sub_u32 s88, s88, 244\n\t"
        "s_nop 1\n\t"
        "s_max_i32 s88, s88, 0\n\t"
        "s_min_i32 s88, s88, 384\n\t"
        "s_andn2_b32 s88, s88, 3\n\t"
        "s_getpc_b64 s[90:91]\n\t"
        "Lga_%=:\n\t"
        "s_sub_u32 s90, s90, s88\n\t"
        "s_subb_u32 s91, s91, 0\n\t"
        "s_add_u32 s90, s90, Lza_%=-Lga_%=\n\t"
        "s_addc_u32 s91, s91, 0\n\t"
        "s_setpc_b64 s[90:91]\n\t"
        "s_nop 0\n\ts_nop 0\n\ts_nop 0\n\ts_nop 0\n\t"
        "s_nop 0\n\ts_nop 0\n\ts_nop 0\n\ts_nop 0\n\t"
        "s_nop 0\n\ts_nop 0\n\ts_nop 0\n\ts_nop 0\n\t"
        "s_nop 0\n\ts_nop 0\n\ts_nop 0\n\ts_nop 0\n\t"
        "s_nop 0\n\ts_nop 0\n\ts_nop 0\n\ts_nop 0\n\t"
        "s_nop 0\n\ts_nop 0\n\ts_nop 0\n\ts_nop 0\n\t"
        "s_nop 0\n\ts_nop 0\n\ts_nop 0\n\ts_nop 0\n\t"
        "s_nop 0\n\ts_nop 0\n\ts_nop 0\n\ts_nop 0\n\t"
        "s_nop 0\n\ts_nop 0\n\ts_nop 0\n\ts_nop 0\n\t"
        "s_nop 0\n\ts_nop 0\n\ts_nop 0\n\ts_nop 0\n\t"
        "s_nop 0\n\ts_nop 0\n\ts_nop 0\n\ts_nop 0\n\t"
        "s_nop 0\n\ts_nop 0\n\ts_nop 0\n\ts_nop 0\n\t"
        "s_nop 0\n\ts_nop 0\n\ts_nop 0\n\ts_nop 0\n\t"
        "s_nop 0\n\ts_nop 0\n\ts_nop 0\n\ts_nop 0\n\t"
        "s_nop 0\n\ts_nop 0\n\ts_nop 0\n\ts_nop 0\n\t"
        "s_nop 0\n\ts_nop 0\n\ts_nop 0\n\ts_nop 0\n\t"
        "s_nop 0\n\ts_nop 0\n\ts_nop 0\n\ts_nop 0\n\t"
        "s_nop 0\n\ts_nop 0\n\ts_nop 0\n\ts_nop 0\n\t"
        "s_nop 0\n\ts_nop 0\n\ts_nop 0\n\ts_nop 0\n\t"
        "s_nop 0\n\ts_nop 0\n\ts_nop 0\n\ts_nop 0\n\t"
        "s_nop 0\n\ts_nop 0\n\ts_nop 0\n\ts_nop 0\n\t"
        "s_nop 0\n\ts_nop 0\n\ts_nop 0\n\ts_nop 0\n\t"
        "s_nop 0\n\ts_nop 0\n\ts_nop 0\n\ts_nop 0\n\t"
        "s_nop 0\n\ts_nop 0\n\ts_nop 0\n\ts_nop 0\n\t"
        "Lza_%=:\n\t"
        "buffer_load_dword v60, %[m0o], %[rs], %[so] offen sc1\n\t"
        "buffer_load_dword v61, %[m1o], %[rs], %[so] offen sc1\n\t"
        "s_memrealtime %[tl]\n\t"
        "s_waitcnt vmcnt(0)\n\t"
        "v_max_u32 v60, v60, v61\n\t"
        "v_cmp_eq_u32 vcc, 0xffbfffbf, v60\n\t"
        "s_cbranch_vccnz Lm%=\n\t"
        "buffer_store_dwordx4 v[56:59], %[sr], %[rs], 0 offen sc1\n\t"
        "s_mov_b32 %[miss], 0\n\t"
        "s_branch Lend%=\n\t"
        "Lm%=:\n\t"
        "buffer_store_dword v62, %[sr], %[rs], 0 offen offset:12 sc1\n\t"
        "Lend%=:\n\t"
        "s_waitcnt lgkmcnt(0)\n\t"
        : [c0] "+&s"(c0), [r0] "+&s"(r0), [tl] "=&s"(tl), [miss] "=&s"(miss)
        : [m0o] "v"(m0o), [m1o] "v"(m1o), [sr] "v"(sr), [rs] "s"(rs), [so] "s"(so), [tg] "s"(tg), [kr] "s"(kr), [rec] "{v[56:59]}"(rec)
        : "s64", "s65", "s66", "s67", "s68", "s69", "s70", "s71", "s72", "s73", "s74", "s75", "s76", "s77", "s78", "s79", "s80", "s81", "s82", "s83", "s84", "s85", "s86", "s87", "s88", "s89", "s90", "s91", "v60", "v61", "v62", "vcc", "scc", "memory");
}

__device__ __forceinline__ void r_t0(u32& c0, u32& r0, u32& t1, u32& t2, u32& ok, u32 tl, u32 t0, u32 kr, u32 fv, u32 lb, u32x4 rs, u32 so STP) {
    asm volatile(
        "s_setprio 3\n\t"
        "v_mov_b32 v20, %[lb]\n\t"
        "s_mov_b32 %[ok], 0\n\t"
        "s_or_b32 s92, %[fv], 0x10000\n\t"
        "s_lshr_b32 s95, %[t2], 16\n\t"
        "s_mul_i32 s95, s95, %[kr]\n\t"
        "s_lshr_b32 s95, s95, 12\n\t"
        "s_min_u32 s95, s95, 512\n\t"
        "s_andn2_b32 s95, s95, 3\n\t"
        "v_mov_b32 v21, s95\n\t"
        "ds_write_b32 v20, v21 offset:16384\n\t"
        "Ldl_%=:\n\t"
        "s_memrealtime s[90:91]\n\t"
        "s_waitcnt lgkmcnt(0)\n\t"
        "s_mul_i32 s90, s90, 24\n\t"
        "s_sub_u32 s88, %[tl], s90\n\t"
        "s_sub_u32 s89, s88, 620\n\t"
        "s_cmp_gt_u32 s89, 0x4000\n\t"
        "s_cbranch_scc1 Lel_%=\n\t"
        "s_sleep 1\n\t"
        "s_branch Ldl_%=\n\t"
        "Lel_%=:\n\t"
        "s_memrealtime s[64:65]\n\t"
        "s_nop 0\n\t"
        "s_memrealtime s[66:67]\n\t"
        "s_nop 0\n\t"
        "s_memrealtime s[68:69]\n\t"
        "s_nop 0\n\t"
        "s_memrealtime s[70:71]\n\t"
        "s_nop 0\n\t"
        "s_memrealtime s[72:73]\n\t"
        "s_nop 0\n\t"
        "s_memrealtime s[74:75]\n\t"
        "s_memtime s[90:91]\n\t"
        "s_memrealtime s[76:77]\n\t"
        "s_nop 0\n\t"
        "s_memrealtime s[78:79]\n\t"
        "s_nop 0\n\t"
        "s_memrealtime s[80:81]\n\t"
        "s_nop 0\n\t"
        "s_memrealtime s[82:83]\n\t"
        "s_nop 0\n\t"
        "s_memrealtime s[84:85]\n\t"
        "s_nop 0\n\t"
        "s_memrealtime s[86:87]\n\t"
        "s_nop 0\n\t"
        "s_nop 7\n\t"
        "s_waitcnt lgkmcnt(0)\n\t"
        "s_add_u32 s64, s64, s66\n\t"
        "s_add_u32 s64, s64, s68\n\t"
        "s_add_u32 s64, s64, s70\n\t"
        "s_add_u32 s64, s64, s72\n\t"
        "s_add_u32 s64, s64, s74\n\t"
        "s_add_u32 s64, s64, s76\n\t"
        "s_add_u32 s64, s64, s78\n\t"
        "s_add_u32 s64, s64, s80\n\t"
        "s_add_u32 s64, s64, s82\n\t"
        "s_add_u32 s64, s64, s84\n\t"
        "s_add_u32 s64, s64, s86\n\t"
        "s_lshl_b32 s64, s64, 1\n\t"
        "s_mov_b32 %[c0], s90\n\t"
        "s_mov_b32 %[r0], s64\n\t"
        "s_sub_u32 s88, %[tl], s64\n\t"
        "s_mul_i32 s88, s88, %[kr]\n\t"
        "s_ashr_i32 s88, s88, 12\n\t"
        "s_sub_u32 s88, s88, 244\n\t"
        "s_nop 1\n\t"
        "s_max_i32 s88, s88, 0\n\t"
        "s_min_i32 s88, s88, 384\n\t"
        "s_andn2_b32 s88, s88, 3\n\t"
        "s_getpc_b64 s[90:91]\n\t"
        "Lgl_%=:\n\t"
        "s_sub_u32 s90, s90, s88\n\t"
        "s_subb_u32 s91, s91, 0\n\t"
        "s_add_u32 s90, s90, Lzl_%=-Lgl_%=\n\t"
        "s_addc_u32 s91, s91, 0\n\t"
        "s_setpc_b64 s[90:91]\n\t"
        "s_nop 0\n\ts_nop 0\n\ts_nop 0\n\ts_nop 0\n\t"
        "s_nop 0\n\ts_nop 0\n\ts_nop 0\n\ts_nop 0\n\t"
        "s_nop 0\n\ts_nop 0\n\ts_nop 0\n\ts_nop 0\n\t"
        "s_nop 0\n\ts_nop 0\n\ts_nop 0\n\ts_nop 0\n\t"
        "s_nop 0\n\ts_nop 0\n\ts_nop 0\n\ts_nop 0\n\t"
        "s_nop 0\n\ts_nop 0\n\ts_nop 0\n\ts_nop 0\n\t"
        "s_nop 0\n\ts_nop 0\n\ts_nop 0\n\ts_nop 0\n\t"
        "s_nop 0\n\ts_nop 0\n\ts_nop 0\n\ts_nop 0\n\t"
        "s_nop 0\n\ts_nop 0\n\ts_nop 0\n\ts_nop 0\n\t"
        "s_nop 0\n\ts_nop 0\n\ts_nop 0\n\ts_nop 0\n\t"
        "s_nop 0\n\ts_nop 0\n\ts_nop 0\n\ts_nop 0\n\t"
        "s_nop 0\n\ts_nop 0\n\ts_nop 0\n\ts_nop 0\n\t"
        "s_nop 0\n\ts_nop 0\n\ts_nop 0\n\ts_nop 0\n\t"
        "s_nop 0\n\ts_nop 0\n\ts_nop 0\n\ts_nop 0\n\t"
        "s_nop 0\n\ts_nop 0\n\ts_nop 0\n\ts_nop 0\n\t"
        "s_nop 0\n\ts_nop 0\n\ts_nop 0\n\ts_nop 0\n\t"
        "s_nop 0\n\ts_nop 0\n\ts_nop 0\n\ts_nop 0\n\t"
        "s_nop 0\n\ts_nop 0\n\ts_nop 0\n\ts_nop 0\n\t"
        "s_nop 0\n\ts_nop 0\n\ts_nop 0\n\ts_nop 0\n\t"
        "s_nop 0\n\ts_nop 0\n\ts_nop 0\n\ts_nop 0\n\t"
        "s_nop 0\n\ts_nop 0\n\ts_nop 0\n\ts_nop 0\n\t"
        "s_nop 0\n\ts_nop 0\n\ts_nop 0\n\ts_nop 0\n\t"
        "s_nop 0\n\ts_nop 0\n\ts_nop 0\n\ts_nop 0\n\t"
        "s_nop 0\n\ts_nop 0\n\ts_nop 0\n\ts_nop 0\n\t"
        "Lzl_%=:\n\t"
        "s_buffer_load_dwordx4 s[48:51], %[rs], %[so] offset:0x0\n\t"
        "s_buffer_load_dwordx4 s[52:55], %[rs], %[so] offset:0x80\n\t"
        "s_buffer_load_dwordx4 s[56:59], %[rs], %[so] offset:0x100\n\t"
        "s_buffer_load_dwordx4 s[60:63], %[rs], %[so] offset:0x180\n\t"
        "s_sub_u32 s95, %[t0], %[r0]\n\t"
        "s_mul_i32 s95, s95, %[kr]\n\t"
        "s_ashr_i32 s95, s95, 12\n\t"
        "s_add_u32 s95, s95, %[c0]\n\t"
        "s_waitcnt lgkmcnt(0)\n\t"
        "s_cmp_eq_u32 s51, s92\n\t"
        "s_cbranch_scc1 Lend%=\n\t"
        "s_cmp_eq_u32 s55, s92\n\t"
        "s_cbranch_scc1 Lend%=\n\t"
        "s_cmp_eq_u32 s59, s92\n\t"
        "s_cbranch_scc1 Lend%=\n\t"
        "s_cmp_eq_u32 s63, s92\n\t"
        "s_cbranch_scc1 Lend%=\n\t"
        "Lda_%=:\n\t"
        "s_memtime s[90:91]\n\t"
        "s_waitcnt lgkmcnt(0)\n\t"
        "s_sub_u32 s88, s95, s90\n\t"
        "s_sub_u32 s89, s88, 336\n\t"
        "s_cmp_gt_u32 s89, 0x4000\n\t"
        "s_cbranch_scc1 Lea_%=\n\t"
        "s_sleep 1\n\t"
        "s_branch Lda_%=\n\t"
        "Lea_%=:\n\t"
        "s_sub_u32 s88, s88, 112\n\t"
        "s_max_i32 s88, s88, 0\n\t"
        "s_min_i32 s88, s88, 224\n\t"
        "s_andn2_b32 s88, s88, 3\n\t"
        "s_getpc_b64 s[90:91]\n\t"
        "Lga_%=:\n\t"
        "s_sub_u32 s90, s90, s88\n\t"
        "s_subb_u32 s91, s91, 0\n\t"
        "s_add_u32 s90, s90, Lza_%=-Lga_%=\n\t"
        "s_addc_u32 s91, s91, 0\n\t"
        "s_setpc_b64 s[90:91]\n\t"
        "s_nop 0\n\ts_nop 0\n\ts_nop 0\n\ts_nop 0\n\t"
        "s_nop 0\n\ts_nop 0\n\ts_nop 0\n\ts_nop 0\n\t"
        "s_nop 0\n\ts_nop 0\n\ts_nop 0\n\ts_nop 0\n\t"
        "s_nop 0\n\ts_nop 0\n\ts_nop 0\n\ts_nop 0\n\t"
        "s_nop 0\n\ts_nop 0\n\ts_nop 0\n\ts_nop 0\n\t"
        "s_nop 0\n\ts_nop 0\n\ts_nop 0\n\ts_nop 0\n\t"
        "s_nop 0\n\ts_nop 0\n\ts_nop 0\n\ts_nop 0\n\t"
        "s_nop 0\n\ts_nop 0\n\ts_nop 0\n\ts_nop 0\n\t"
        "s_nop 0\n\ts_nop 0\n\ts_nop 0\n\ts_nop 0\n\t"
        "s_nop 0\n\ts_nop 0\n\ts_nop 0\n\ts_nop 0\n\t"
        "s_nop 0\n\ts_nop 0\n\ts_nop 0\n\ts_nop 0\n\t"
        "s_nop 0\n\ts_nop 0\n\ts_nop 0\n\ts_nop 0\n\t"
        "s_nop 0\n\ts_nop 0\n\ts_nop 0\n\ts_nop 0\n\t"
        "s_nop 0\n\ts_nop 0\n\ts_nop 0\n\ts_nop 0\n\t"
        "Lza_%=:\n\t"
        "s_barrier\n\t"
        "s_setprio 0\n\t"
        "s_xor_b32 s51, s51, %[fv]\n\t"
        "s_xor_b32 s55, s55, %[fv]\n\t"
        "s_xor_b32 s59, s59, %[fv]\n\t"
        "s_xor_b32 s63, s63, %[fv]\n\t"
        "s_sub_u32 s53, s53, s49\n\t"
        "s_sub_u32 s57, s57, s49\n\t"
        "s_sub_u32 s61, s61, s49\n\t"
        "s_max_i32 s53, s53, s57\n\t"
        "s_max_i32 s61, s61, 0\n\t"
        "s_max_i32 s53, s53, s61\n\t"
        "s_add_u32 s49, s49, s53\n\t"
        "s_max_u32 s50, s50, s54\n\t"
        "s_max_u32 s50, s50, s58\n\t"
        "s_max_u32 s50, s50, s62\n\t"
        "s_or_b32 s58, s51, s55\n\t"
        "s_or_b32 s54, s59, s63\n\t"
        "s_or_b32 s54, s54, s58\n\t"
        "s_cmp_eq_u32 s54, 0\n\t"
        "s_cselect_b32 %[ok], 1, 2\n\t"
        "s_cselect_b32 %[t1], s49, %[t1]\n\t"
        "s_cselect_b32 %[t2], s50, %[t2]\n\t"
        "s_barrier\n\t"
        "Lend%=:\n\t"
        : [c0] "+&s"(c0), [r0] "+&s"(r0), [t1] "+&s"(t1), [t2] "+&s"(t2), [ok] "=&s"(ok)
        : [rs] "s"(rs), [so] "s"(so), [kr] "s"(kr), [fv] "s"(fv), [lb] "s"(lb), [tl] "s"(tl), [t0] "s"(t0)
        : "s48", "s49", "s50", "s51", "s52", "s53", "s54", "s55", "s56", "s57", "s58", "s59", "s60", "s61", "s62", "s63", "s64", "s65", "s66", "s67", "s68", "s69", "s70", "s71", "s72", "s73", "s74", "s75", "s76", "s77", "s78", "s79", "s80", "s81", "s82", "s83", "s84", "s85", "s86", "s87", "s88", "s89", "s90", "s91", "s92", "s93", "s94", "s95", "v20", "v21", "vcc", "scc", "memory");
}

__device__ __forceinline__ void r_t1(u32& c0, u32& r0, u32& t1, u32& t2, u32 tv, u32 kr, u32 fv, u32 lb, u32x4 rs, u32 so STP) {
    asm volatile(
        "s_setprio 3\n\t"
        "s_mov_b32 s92, 0x4000\n\t"
        "v_mov_b32 v20, %[lb]\n\t"
        "s_nop 2\n\t"
        "Lr%=:\n\t"
        "buffer_load_dwordx4 v[16:19], %[tv], %[rs], %[so] offen sc1\n\t"
        "s_memrealtime s[64:65]\n\t"
        "s_nop 0\n\t"
        "s_memrealtime s[66:67]\n\t"
        "s_nop 0\n\t"
        "s_memrealtime s[68:69]\n\t"
        "s_nop 0\n\t"
        "s_memrealtime s[70:71]\n\t"
        "s_nop 0\n\t"
        "s_memrealtime s[72:73]\n\t"
        "s_nop 0\n\t"
        "s_memrealtime s[74:75]\n\t"
        "s_memtime s[90:91]\n\t"
        "s_memrealtime s[76:77]\n\t"
        "s_nop 0\n\t"
        "s_memrealtime s[78:79]\n\t"
        "s_nop 0\n\t"
        "s_memrealtime s[80:81]\n\t"
        "s_nop 0\n\t"
        "s_memrealtime s[82:83]\n\t"
        "s_nop 0\n\t"
        "s_memrealtime s[84:85]\n\t"
        "s_nop 0\n\t"
        "s_memrealtime s[86:87]\n\t"
        "s_nop 0\n\t"
        "s_nop 7\n\t"
        "s_waitcnt lgkmcnt(0)\n\t"
        "s_add_u32 s64, s64, s66\n\t"
        "s_add_u32 s64, s64, s68\n\t"
        "s_add_u32 s64, s64, s70\n\t"
        "s_add_u32 s64, s64, s72\n\t"
        "s_add_u32 s64, s64, s74\n\t"
        "s_add_u32 s64, s64, s76\n\t"
        "s_add_u32 s64, s64, s78\n\t"
        "s_add_u32 s64, s64, s80\n\t"
        "s_add_u32 s64, s64, s82\n\t"
        "s_add_u32 s64, s64, s84\n\t"
        "s_add_u32 s64, s64, s86\n\t"
        "s_lshl_b32 s64, s64, 1\n\t"
        "s_mov_b32 %[c0], s90\n\t"
        "s_mov_b32 %[r0], s64\n\t"
        "s_sub_u32 s96, %[r0], 0x100000\n\t"
        "s_waitcnt vmcnt(0)\n\t"
        "v_cmp_eq_u32 vcc, %[fv], v19\n\t"
        "v_subrev_u32 v16, s96, v16\n\t"
        "v_subrev_u32 v17, s96, v17\n\t"
        "v_cndmask_b32 v16, 0, v16, vcc\n\t"
        "v_cndmask_b32 v17, 0, v17, vcc\n\t"
        "v_cndmask_b32 v18, 0, v18, vcc\n\t"
        "v_max_u32_dpp v16, v16, v16 quad_perm:[1,0,3,2] row_mask:0xf bank_mask:0xf\n\t"
        "v_max_u32_dpp v17, v17, v17 quad_perm:[1,0,3,2] row_mask:0xf bank_mask:0xf\n\t"
        "v_max_u32_dpp v18, v18, v18 quad_perm:[1,0,3,2] row_mask:0xf bank_mask:0xf\n\t"
        "v_max_u32_dpp v16, v16, v16 quad_perm:[2,3,0,1] row_mask:0xf bank_mask:0xf\n\t"
        "v_max_u32_dpp v17, v17, v17 quad_perm:[2,3,0,1] row_mask:0xf bank_mask:0xf\n\t"
        "v_max_u32_dpp v18, v18, v18 quad_perm:[2,3,0,1] row_mask:0xf bank_mask:0xf\n\t"
        "v_max_u32_dpp v16, v16, v16 row_half_mirror row_mask:0xf bank_mask:0xf\n\t"
        "v_max_u32_dpp v17, v17, v17 row_half_mirror row_mask:0xf bank_mask:0xf\n\t"
        "v_max_u32_dpp v18, v18, v18 row_half_mirror row_mask:0xf bank_mask:0xf\n\t"
        "s_lshr_b32 s94, vcc_lo, 4\n\t"
        "v_readfirstlane_b32 s93, v16\n\t"
        "v_readfirstlane_b32 %[t1], v17\n\t"
        "v_readfirstlane_b32 %[t2], v18\n\t"
        "s_add_u32 s93, s93, s96\n\t"
        "s_add_u32 %[t1], %[t1], s96\n\t"
        "s_or_b32 s94, s94, vcc_lo\n\t"
        "s_and_b32 s94, s94, 15\n\t"
        "s_cmp_eq_u32 s94, 15\n\t"
        "s_cbranch_scc0 Lx%=\n\t"
        "Lgo%=:\n\t"
        "s_lshr_b32 s95, %[t2], 16\n\t"
        "s_mul_i32 s95, s95, %[kr]\n\t"
        "s_lshr_b32 s95, s95, 12\n\t"
        "s_min_u32 s95, s95, 512\n\t"
        "s_andn2_b32 s95, s95, 3\n\t"
        "v_mov_b32 v21, s95\n\t"
        "ds_write_b32 v20, v21 offset:16384\n\t"
        "s_sub_u32 s95, s93, %[r0]\n\t"
        "s_mul_i32 s95, s95, %[kr]\n\t"
        "s_ashr_i32 s95, s95, 12\n\t"
        "s_add_u32 s95, s95, %[c0]\n\t"
        "Lda_%=:\n\t"
        "s_memtime s[90:91]\n\t"
        "s_waitcnt lgkmcnt(0)\n\t"
        "s_sub_u32 s88, s95, s90\n\t"
        "s_sub_u32 s89, s88, 336\n\t"
        "s_cmp_gt_u32 s89, 0x4000\n\t"
        "s_cbranch_scc1 Lea_%=\n\t"
        "s_sleep 1\n\t"
        "s_branch Lda_%=\n\t"
        "Lea_%=:\n\t"
        "s_sub_u32 s88, s88, 112\n\t"
        "s_max_i32 s88, s88, 0\n\t"
        "s_min_i32 s88, s88, 224\n\t"
        "s_andn2_b32 s88, s88, 3\n\t"
        "s_getpc_b64 s[90:91]\n\t"
        "Lga_%=:\n\t"
        "s_sub_u32 s90, s90, s88\n\t"
        "s_subb_u32 s91, s91, 0\n\t"
        "s_add_u32 s90, s90, Lza_%=-Lga_%=\n\t"
        "s_addc_u32 s91, s91, 0\n\t"
        "s_setpc_b64 s[90:91]\n\t"
        "s_nop 0\n\ts_nop 0\n\ts_nop 0\n\ts_nop 0\n\t"
        "s_nop 0\n\ts_nop 0\n\ts_nop 0\n\ts_nop 0\n\t"
        "s_nop 0\n\ts_nop 0\n\ts_nop 0\n\ts_nop 0\n\t"
        "s_nop 0\n\ts_nop 0\n\ts_nop 0\n\ts_nop 0\n\t"
        "s_nop 0\n\ts_nop 0\n\ts_nop 0\n\ts_nop 0\n\t"
        "s_nop 0\n\ts_nop 0\n\ts_nop 0\n\ts_nop 0\n\t"
        "s_nop 0\n\ts_nop 0\n\ts_nop 0\n\ts_nop 0\n\t"
        "s_nop 0\n\ts_nop 0\n\ts_nop 0\n\ts_nop 0\n\t"
        "s_nop 0\n\ts_nop 0\n\ts_nop 0\n\ts_nop 0\n\t"
        "s_nop 0\n\ts_nop 0\n\ts_nop 0\n\ts_nop 0\n\t"
        "s_nop 0\n\ts_nop 0\n\ts_nop 0\n\ts_nop 0\n\t"
        "s_nop 0\n\ts_nop 0\n\ts_nop 0\n\ts_nop 0\n\t"
        "s_nop 0\n\ts_nop 0\n\ts_nop 0\n\ts_nop 0\n\t"
        "s_nop 0\n\ts_nop 0\n\ts_nop 0\n\ts_nop 0\n\t"
        "Lza_%=:\n\t"
        "s_barrier\n\t"
        "s_setprio 0\n\t"
        "s_barrier\n\t"
        "s_branch Lend%=\n\t"
        "Lx%=:\n\t"
        "s_sub_u32 s92, s92, 1\n\t"
        "s_cbranch_scc1 Lgo%=\n\t"
        "s_branch Lr%=\n\t"
        "Lend%=:\n\t"
        : [c0] "+&s"(c0), [r0] "+&s"(r0), [t1] "+&s"(t1), [t2] "+&s"(t2)
        : [tv] "v"(tv), [rs] "s"(rs), [so] "s"(so), [kr] "s"(kr), [fv] "s"(fv), [lb] "s"(lb)
        : "s64", "s65", "s66", "s67", "s68", "s69", "s70", "s71", "s72", "s73", "s74", "s75", "s76", "s77", "s78", "s79", "s80", "s81", "s82", "s83", "s84", "s85", "s86", "s87", "s88", "s89", "s90", "s91", "s92", "s93", "s94", "s95", "s96", "v16", "v17", "v18", "v19", "v20", "v21", "vcc", "scc", "memory");
}

__device__ __forceinline__ void r_u(u32x32& bn, u32& s, u32& have, u32& left, u32& why, u32 sidx, u32 eo, u32 uw, u32 fa, u32 vm, u32 fo, u32 te, u32 tb, u32x4 rs, u32 lag, u32 lb STP LGP) {
    asm volatile(
        "v_mov_b32 v125, %[lb]\n\t"
        "v_mov_b32 v80, 0xffbfffbf\n\t"
        "v_mov_b32 v81, v80\n\t"
        "ds_write_b64 %[uw], v[80:81] offset:0\n\t"
        "ds_write_b64 %[uw], v[80:81] offset:512\n\t"
        "ds_write_b64 %[uw], v[80:81] offset:1024\n\t"
        "ds_write_b64 %[uw], v[80:81] offset:1536\n\t"
        "Lp%=:\n\t"
        "v_readlane_b32 s82, %[te], %[s]\n\t"
        "v_readlane_b32 s83, %[tb], %[s]\n\t"
        "s_add_u32 s92, %[s], 1\n\t"
        "s_mul_i32 s93, s92, 0x10001\n\t"
        "s_mov_b32 s95, %[left]\n\t"
        "s_mov_b32 s94, 0\n\t"
        "v_mov_b32 v124, s93\n\t"
        "s_add_u32 s97, %[s], 4\n\t"
        "s_cmp_le_u32 %[have], s97\n\t"
        "s_cselect_b32 s96, 0, 0x80000000\n\t"
        "v_or_b32 v126, s96, %[fo]\n\t"
        "s_add_u32 s84, s83, 0x1000\n\t"
        "s_setprio 2\n\t"
        "s_waitcnt lgkmcnt(0)\n\t"
        "s_getpc_b64 s[90:91]\n\t"
        "Lj%=:\n\t"
        "s_sub_u32 s90, s90, %[lag]\n\t"
        "s_subb_u32 s91, s91, 0\n\t"
        "s_add_u32 s90, s90, Lg%=-Lj%=\n\t"
        "s_addc_u32 s91, s91, 0\n\t"
        "s_barrier\n\t"
        "s_setpc_b64 s[90:91]\n\t"
        "s_nop 10\n\t"
        "s_nop 10\n\t"
        "s_nop 10\n\t"
        "Lg%=:\n\t"
        "buffer_load_dwordx4 v[48:51], %[eo], %[rs], s82 offen offset:0 sc1\n\t"
        "buffer_load_dwordx4 v[52:55], %[eo], %[rs], s82 offen offset:1024 sc1\n\t"
        "buffer_load_dwordx4 v[56:59], %[eo], %[rs], s82 offen offset:2048 sc1\n\t"
        "buffer_load_dwordx4 v[60:63], %[eo], %[rs], s82 offen offset:3072 sc1\n\t"
        "v_mov_b64 v[64:65], 0\n\t"
        "v_mov_b64 v[66:67], 0\n\t"
        "v_mov_b64 v[68:69], 0\n\t"
        "v_mov_b64 v[70:71], 0\n\t"
        "v_mov_b64 v[72:73], 0\n\t"
        "v_mov_b64 v[74:75], 0\n\t"
        "v_mov_b64 v[76:77], 0\n\t"
        "v_mov_b64 v[78:79], 0\n\t"
        "s_waitcnt vmcnt(3)\n\t"
        "v_smfmac_f32_16x16x64_bf16 v[64:67], v[48:51], v[16:23], %[sidx]\n\t"
        "s_waitcnt vmcnt(2)\n\t"
        "v_smfmac_f32_16x16x64_bf16 v[68:71], v[52:55], v[24:31], %[sidx]\n\t"
        "s_waitcnt vmcnt(1)\n\t"
        "v_smfmac_f32_16x16x64_bf16 v[72:75], v[56:59], v[32:39], %[sidx]\n\t"
        "v_cvt_pk_bf16_f32 v80, v64, v65\n\t"
        "v_cvt_pk_bf16_f32 v81, v66, v67\n\t"
        "ds_write_b64 %[uw], v[80:81] offset:0\n\t"
        "s_waitcnt vmcnt(0)\n\t"
        "v_smfmac_f32_16x16x64_bf16 v[76:79], v[60:63], v[40:47], %[sidx]\n\t"
        "v_cvt_pk_bf16_f32 v82, v68, v69\n\t"
        "v_cvt_pk_bf16_f32 v83, v70, v71\n\t"
        "ds_write_b64 %[uw], v[82:83] offset:512\n\t"
        "v_cvt_pk_bf16_f32 v84, v72, v73\n\t"
        "v_cvt_pk_bf16_f32 v85, v74, v75\n\t"
        "ds_write_b64 %[uw], v[84:85] offset:1024\n\t"
        "s_nop 1\n\t"
        "v_cvt_pk_bf16_f32 v86, v76, v77\n\t"
        "v_cvt_pk_bf16_f32 v87, v78, v79\n\t"
        "ds_write_b64 %[uw], v[86:87] offset:1536\n\t"
        "s_setprio 0\n\t"
        "s_cmp_lg_u32 s94, 0\n\t"
        "s_cbranch_scc1 Lc%=\n\t"
        "s_waitcnt lgkmcnt(0)\n\t"
        "s_barrier\n\t"
        "ds_read_b32 v88, v125 offset:16392\n\t"
        "s_waitcnt lgkmcnt(0)\n\t"
        "v_cmp_eq_u32 vcc, s93, v88\n\t"
        "s_cbranch_vccnz Lc%=\n\t"
        "Lok%=:\n\t"
        "s_cmp_gt_u32 %[have], s92\n\t"
        "s_cselect_b32 %[why], 0, 2\n\t"
        "buffer_load_dwordx2 v[122:123], v126, %[rs], 0 offen sc1\n\t"
        "s_cbranch_scc0 Ln%=\n\t"
        "buffer_load_dwordx4 v[90:93], %[vm], %[rs], s83 offen offset:0 sc1\n\t"
        "buffer_load_dwordx4 v[94:97], %[vm], %[rs], s83 offen offset:1024 sc1\n\t"
        "buffer_load_dwordx4 v[98:101], %[vm], %[rs], s83 offen offset:2048 sc1\n\t"
        "buffer_load_dwordx4 v[102:105], %[vm], %[rs], s83 offen offset:3072 sc1\n\t"
        "buffer_load_dwordx4 v[106:109], %[vm], %[rs], s84 offen offset:0 sc1\n\t"
        "buffer_load_dwordx4 v[110:113], %[vm], %[rs], s84 offen offset:1024 sc1\n\t"
        "buffer_load_dwordx4 v[114:117], %[vm], %[rs], s84 offen offset:2048 sc1\n\t"
        "buffer_load_dwordx4 v[118:121], %[vm], %[rs], s84 offen offset:3072 sc1\n\t"
        "Ln%=:\n\t"
        "ds_read_b32 v88, v125 offset:16396\n\t"
        "v_mov_b32 v80, 0xffbfffbf\n\t"
        "v_mov_b32 v81, v80\n\t"
        "s_sub_u32 s95, s95, %[left]\n\t"
        "s_mov_b32 %[s], s92\n\t"
        "s_waitcnt lgkmcnt(0)\n\t"
        "v_cmp_eq_u32 vcc, s93, v88\n\t"
        "s_cmp_eq_u32 %[s], 63\n\t"
        "s_cbranch_vccnz Lr%=\n\t"
        "s_cbranch_scc1 Lz%=\n\t"
        "s_cmp_lg_u32 %[why], 0\n\t"
        "s_cbranch_scc1 Lend%=\n\t"
        "ds_write_b64 %[uw], v[80:81] offset:0\n\t"
        "ds_write_b64 %[uw], v[80:81] offset:512\n\t"
        "ds_write_b64 %[uw], v[80:81] offset:1024\n\t"
        "ds_write_b64 %[uw], v[80:81] offset:1536\n\t"
        "s_waitcnt vmcnt(8)\n\t"
        "v_lshlrev_b32 v122, 1, v122\n\t"
        "v_lshl_or_b32 v123, v123, 1, 1\n\t"
        "v_min_u32 v122, v122, v123\n\t"
        "s_waitcnt vmcnt(6)\n\t"
        "v_mov_b32 v16, v90\n\t"
        "v_mov_b32 v17, v94\n\t"
        "v_min_u32_dpp v122, v122, v122 quad_perm:[1,0,3,2] row_mask:0xf bank_mask:0xf\n\t"
        "v_mov_b32 v18, v91\n\t"
        "v_mov_b32 v19, v95\n\t"
        "v_min_u32_dpp v122, v122, v122 quad_perm:[2,3,0,1] row_mask:0xf bank_mask:0xf\n\t"
        "v_mov_b32 v20, v92\n\t"
        "v_mov_b32 v21, v96\n\t"
        "v_min_u32_dpp v122, v122, v122 row_ror:4 row_mask:0xf bank_mask:0xf\n\t"
        "v_mov_b32 v22, v93\n\t"
        "v_mov_b32 v23, v97\n\t"
        "v_readfirstlane_b32 s96, v122\n\t"
        "s_waitcnt vmcnt(4)\n\t"
        "v_mov_b32 v24, v98\n\t"
        "v_mov_b32 v25, v102\n\t"
        "v_mov_b32 v26, v99\n\t"
        "v_mov_b32 v27, v103\n\t"
        "v_mov_b32 v28, v100\n\t"
        "v_mov_b32 v29, v104\n\t"
        "v_mov_b32 v30, v101\n\t"
        "v_mov_b32 v31, v105\n\t"
        "s_waitcnt vmcnt(2)\n\t"
        "v_mov_b32 v32, v106\n\t"
        "v_mov_b32 v33, v110\n\t"
        "v_mov_b32 v34, v107\n\t"
        "v_mov_b32 v35, v111\n\t"
        "v_mov_b32 v36, v108\n\t"
        "v_mov_b32 v37, v112\n\t"
        "v_mov_b32 v38, v109\n\t"
        "v_mov_b32 v39, v113\n\t"
        "s_max_u32 %[have], %[have], s96\n\t"
        "s_waitcnt vmcnt(0)\n\t"
        "v_mov_b32 v40, v114\n\t"
        "v_mov_b32 v41, v118\n\t"
        "v_mov_b32 v42, v115\n\t"
        "v_mov_b32 v43, v119\n\t"
        "v_mov_b32 v44, v116\n\t"
        "v_mov_b32 v45, v120\n\t"
        "v_mov_b32 v46, v117\n\t"
        "v_mov_b32 v47, v121\n\t"
        "s_branch Lp%=\n\t"
        "Lc%=:\n\t"
        "v_max_u32 v88, v48, v49\n\t"
        "v_max3_u32 v88, v88, v50, v51\n\t"
        "v_max3_u32 v88, v88, v52, v53\n\t"
        "v_max3_u32 v88, v88, v54, v55\n\t"
        "v_max3_u32 v88, v88, v56, v57\n\t"
        "v_max3_u32 v88, v88, v58, v59\n\t"
        "v_max3_u32 v88, v88, v60, v61\n\t"
        "v_max3_u32 v88, v88, v62, v63\n\t"
        "v_cmp_eq_u32 vcc, 0xffbfffbf, v88\n\t"
        "s_mov_b32 s94, 1\n\t"
        "s_cbranch_vccz Ld%=\n\t"
        "s_sub_u32 %[left], %[left], 1\n\t"
        "s_cbranch_scc0 Lg%=\n\t"
        "s_mov_b32 %[left], 0\n\t"
        "Ld%=:\n\t"
        "ds_write_b32 %[fa], v124\n\t"
        "s_waitcnt lgkmcnt(0)\n\t"
        "s_barrier\n\t"
        "s_branch Lok%=\n\t"
        "Lr%=:\n\t"
        "s_mov_b32 %[why], 3\n\t"
        "s_branch Lend%=\n\t"
        "Lz%=:\n\t"
        "s_mov_b32 %[why], 0\n\t"
        "Lend%=:\n\t"
        "s_waitcnt vmcnt(0)\n\t"
        : [bn] "+{v[16:47]}"(bn), [s] "+&s"(s), [have] "+&s"(have), [left] "+&s"(left), [why] "=&s"(why)
        : [sidx] "v"(sidx), [eo] "v"(eo), [uw] "v"(uw), [fa] "v"(fa), [vm] "v"(vm), [fo] "v"(fo), [te] "v"(te), [tb] "v"(tb), [rs] "s"(rs), [lag] "s"(lag), [lb] "s"(lb)
        : "s82", "s83", "s84", "s85", "s86", "s87", "s88", "s89", "s90", "s91", "s92", "s93", "s94", "s95", "s96", "s97", "s98", "s99", "v48", "v49", "v50", "v51", "v52", "v53", "v54", "v55", "v56", "v57", "v58", "v59", "v60", "v61", "v62", "v63", "v64", "v65", "v66", "v67", "v68", "v69", "v70", "v71", "v72", "v73", "v74", "v75", "v76", "v77", "v78", "v79", "v80", "v81", "v82", "v83", "v84", "v85", "v86", "v87", "v88", "v89", "v90", "v91", "v92", "v93", "v94", "v95", "v96", "v97", "v98", "v99", "v100", "v101", "v102", "v103", "v104", "v105", "v106", "v107", "v108", "v109", "v110", "v111", "v112", "v113", "v114", "v115", "v116", "v117", "v118", "v119", "v120", "v121", "v122", "v123", "v124", "v125", "v126", "vcc", "scc", "memory");
}

__device__ __forceinline__ void r_f(u32& bad, u32& pass, u32 sidx, u32 pv, u32 zv, u32 ra, u32 rb, u32 mo, u32 uf, u32x4 rs, u32 so, u32 sz, u32 sm, u32 lb, u32 fv, u32 cap, u32 zp STP) {
    asm volatile(
        "v_mov_b32 v120, %[lb]\n\t"
        "s_add_u32 s92, %[sm], 0x1000\n\t"
        "s_add_u32 s93, %[sm], 0x4000\n\t"
        "s_add_u32 s94, %[sm], 0x5000\n\t"
        "s_add_u32 s95, %[so], 0x8000\n\t"
        "s_add_u32 s96, %[sz], 0x1000\n\t"
        "s_mov_b32 %[pass], 0\n\t"
        "s_mov_b32 s97, 0\n\t"
        "s_mov_b32 %[bad], 0\n\t"
        "s_setprio 3\n\t"
        "s_barrier\n\t"
        "ds_read_b32 v116, v120 offset:16384\n\t"
        "s_waitcnt lgkmcnt(0)\n\t"
        "v_readfirstlane_b32 s98, v116\n\t"
        "s_nop 3\n\t"
        "s_getpc_b64 s[90:91]\n\t"
        "Lgp_%=:\n\t"
        "s_sub_u32 s90, s90, s98\n\t"
        "s_subb_u32 s91, s91, 0\n\t"
        "s_add_u32 s90, s90, Lzp_%=-Lgp_%=\n\t"
        "s_addc_u32 s91, s91, 0\n\t"
        "s_setpc_b64 s[90:91]\n\t"
        "s_nop 0\n\ts_nop 0\n\ts_nop 0\n\ts_nop 0\n\t"
        "s_nop 0\n\ts_nop 0\n\ts_nop 0\n\ts_nop 0\n\t"
        "s_nop 0\n\ts_nop 0\n\ts_nop 0\n\ts_nop 0\n\t"
        "s_nop 0\n\ts_nop 0\n\ts_nop 0\n\ts_nop 0\n\t"
        "s_nop 0\n\ts_nop 0\n\ts_nop 0\n\ts_nop 0\n\t"
        "s_nop 0\n\ts_nop 0\n\ts_nop 0\n\ts_nop 0\n\t"
        "s_nop 0\n\ts_nop 0\n\ts_nop 0\n\ts_nop 0\n\t"
        "s_nop 0\n\ts_nop 0\n\ts_nop 0\n\ts_nop 0\n\t"
        "s_nop 0\n\ts_nop 0\n\ts_nop 0\n\ts_nop 0\n\t"
        "s_nop 0\n\ts_nop 0\n\ts_nop 0\n\ts_nop 0\n\t"
        "s_nop 0\n\ts_nop 0\n\ts_nop 0\n\ts_nop 0\n\t"
        "s_nop 0\n\ts_nop 0\n\ts_nop 0\n\ts_nop 0\n\t"
        "s_nop 0\n\ts_nop 0\n\ts_nop 0\n\ts_nop 0\n\t"
        "s_nop 0\n\ts_nop 0\n\ts_nop 0\n\ts_nop 0\n\t"
        "s_nop 0\n\ts_nop 0\n\ts_nop 0\n\ts_nop 0\n\t"
        "s_nop 0\n\ts_nop 0\n\ts_nop 0\n\ts_nop 0\n\t"
        "s_nop 0\n\ts_nop 0\n\ts_nop 0\n\ts_nop 0\n\t"
        "s_nop 0\n\ts_nop 0\n\ts_nop 0\n\ts_nop 0\n\t"
        "s_nop 0\n\ts_nop 0\n\ts_nop 0\n\ts_nop 0\n\t"
        "s_nop 0\n\ts_nop 0\n\ts_nop 0\n\ts_nop 0\n\t"
        "s_nop 0\n\ts_nop 0\n\ts_nop 0\n\ts_nop 0\n\t"
        "s_nop 0\n\ts_nop 0\n\ts_nop 0\n\ts_nop 0\n\t"
        "s_nop 0\n\ts_nop 0\n\ts_nop 0\n\ts_nop 0\n\t"
        "s_nop 0\n\ts_nop 0\n\ts_nop 0\n\ts_nop 0\n\t"
        "s_nop 0\n\ts_nop 0\n\ts_nop 0\n\ts_nop 0\n\t"
        "s_nop 0\n\ts_nop 0\n\ts_nop 0\n\ts_nop 0\n\t"
        "s_nop 0\n\ts_nop 0\n\ts_nop 0\n\ts_nop 0\n\t"
        "s_nop 0\n\ts_nop 0\n\ts_nop 0\n\ts_nop 0\n\t"
        "s_nop 0\n\ts_nop 0\n\ts_nop 0\n\ts_nop 0\n\t"
        "s_nop 0\n\ts_nop 0\n\ts_nop 0\n\ts_nop 0\n\t"
        "s_nop 0\n\ts_nop 0\n\ts_nop 0\n\ts_nop 0\n\t"
        "s_nop 0\n\ts_nop 0\n\ts_nop 0\n\ts_nop 0\n\t"
        "Lzp_%=:\n\t"
        "buffer_load_dwordx4 v[48:51], %[zv], %[rs], %[sz] offen offset:0 sc1\n\t"
        "buffer_load_dwordx4 v[52:55], %[zv], %[rs], %[sz] offen offset:1024 sc1\n\t"
        "buffer_load_dwordx4 v[56:59], %[zv], %[rs], %[sz] offen offset:2048 sc1\n\t"
        "s_getpc_b64 s[90:91]\n\t"
        "Lgq_%=:\n\t"
        "s_sub_u32 s90, s90, %[zp]\n\t"
        "s_subb_u32 s91, s91, 0\n\t"
        "s_add_u32 s90, s90, Lzq_%=-Lgq_%=\n\t"
        "s_addc_u32 s91, s91, 0\n\t"
        "s_setpc_b64 s[90:91]\n\t"
        "s_nop 0\n\ts_nop 0\n\ts_nop 0\n\ts_nop 0\n\t"
        "s_nop 0\n\ts_nop 0\n\ts_nop 0\n\ts_nop 0\n\t"
        "s_nop 0\n\ts_nop 0\n\ts_nop 0\n\ts_nop 0\n\t"
        "s_nop 0\n\ts_nop 0\n\ts_nop 0\n\ts_nop 0\n\t"
        "s_nop 0\n\ts_nop 0\n\ts_nop 0\n\ts_nop 0\n\t"
        "s_nop 0\n\ts_nop 0\n\ts_nop 0\n\ts_nop 0\n\t"
        "s_nop 0\n\ts_nop 0\n\ts_nop 0\n\ts_nop 0\n\t"
        "s_nop 0\n\ts_nop 0\n\ts_nop 0\n\ts_nop 0\n\t"
        "s_nop 0\n\ts_nop 0\n\ts_nop 0\n\ts_nop 0\n\t"
        "s_nop 0\n\ts_nop 0\n\ts_nop 0\n\ts_nop 0\n\t"
        "s_nop 0\n\ts_nop 0\n\ts_nop 0\n\ts_nop 0\n\t"
        "s_nop 0\n\ts_nop 0\n\ts_nop 0\n\ts_nop 0\n\t"
        "s_nop 0\n\ts_nop 0\n\ts_nop 0\n\ts_nop 0\n\t"
        "s_nop 0\n\ts_nop 0\n\ts_nop 0\n\ts_nop 0\n\t"
        "s_nop 0\n\ts_nop 0\n\ts_nop 0\n\ts_nop 0\n\t"
        "s_nop 0\n\ts_nop 0\n\ts_nop 0\n\ts_nop 0\n\t"
        "Lzq_%=:\n\t"
        "buffer_load_dwordx4 v[14:17], %[pv], %[rs], %[so] offen offset:0 sc1\n\t"
        "buffer_load_dwordx4 v[60:63], %[zv], %[rs], %[sz] offen offset:3072 sc1\n\t"
        "buffer_load_dwordx4 v[18:21], %[pv], %[rs], %[so] offen offset:256 sc1\n\t"
        "buffer_load_dwordx4 v[64:67], %[zv], %[rs], s96 offen offset:0 sc1\n\t"
        "buffer_load_dwordx4 v[22:25], %[pv], %[rs], %[so] offen offset:1024 sc1\n\t"
        "buffer_load_dwordx4 v[68:71], %[zv], %[rs], s96 offen offset:1024 sc1\n\t"
        "buffer_load_dwordx4 v[26:29], %[pv], %[rs], %[so] offen offset:1280 sc1\n\t"
        "buffer_load_dwordx4 v[72:75], %[zv], %[rs], s96 offen offset:2048 sc1\n\t"
        "buffer_load_dwordx4 v[30:33], %[pv], %[rs], s95 offen offset:0 sc1\n\t"
        "buffer_load_dwordx4 v[76:79], %[zv], %[rs], s96 offen offset:3072 sc1\n\t"
        "buffer_load_dwordx4 v[34:37], %[pv], %[rs], s95 offen offset:256 sc1\n\t"
        "buffer_load_dwordx4 v[38:41], %[pv], %[rs], s95 offen offset:1024 sc1\n\t"
        "buffer_load_dwordx4 v[42:45], %[pv], %[rs], s95 offen offset:1280 sc1\n\t"
        "Lrd%=:\n\t"
        "v_mov_b64 v[112:113], 0\n\t"
        "v_mov_b64 v[114:115], 0\n\t"
        "ds_read_b32 v121, %[uf]\n\t"
        "s_waitcnt vmcnt(13)\n\t"
        "ds_read_b128 v[80:83], %[ra] offset:0\n\t"
        "ds_read_b128 v[84:87], %[ra] offset:1024\n\t"
        "s_waitcnt vmcnt(12)\n\t"
        "v_mov_b64 v[46:47], v[14:15]\n\t"
        "s_waitcnt vmcnt(10)\n\t"
        "v_smfmac_f32_16x16x64_bf16 v[112:115], v[16:19], v[48:55], %[sidx]\n\t"
        "s_waitcnt vmcnt(9)\n\t"
        "ds_read_b128 v[88:91], %[ra] offset:2048\n\t"
        "ds_read_b128 v[92:95], %[ra] offset:3072\n\t"
        "s_waitcnt vmcnt(8) lgkmcnt(2)\n\t"
        "v_smfmac_f32_16x16x64_bf16 v[112:115], v[20:23], v[80:87], %[sidx]\n\t"
        "s_waitcnt vmcnt(6)\n\t"
        "v_smfmac_f32_16x16x64_bf16 v[112:115], v[24:27], v[56:63], %[sidx]\n\t"
        "s_waitcnt vmcnt(5)\n\t"
        "ds_read_b128 v[96:99], %[ra] offset:4096\n\t"
        "ds_read_b128 v[100:103], %[ra] offset:5120\n\t"
        "ds_read_b128 v[104:107], %[rb] offset:6144\n\t"
        "ds_read_b128 v[108:111], %[rb] offset:7168\n\t"
        "s_waitcnt vmcnt(4) lgkmcnt(4)\n\t"
        "v_smfmac_f32_16x16x64_bf16 v[112:115], v[28:31], v[88:95], %[sidx]\n\t"
        "s_waitcnt vmcnt(2)\n\t"
        "v_smfmac_f32_16x16x64_bf16 v[112:115], v[32:35], v[64:71], %[sidx]\n\t"
        "s_waitcnt vmcnt(1) lgkmcnt(2)\n\t"
        "v_smfmac_f32_16x16x64_bf16 v[112:115], v[36:39], v[96:103], %[sidx]\n\t"
        "s_waitcnt vmcnt(0)\n\t"
        "v_smfmac_f32_16x16x64_bf16 v[112:115], v[40:43], v[72:79], %[sidx]\n\t"
        "s_waitcnt vmcnt(0) lgkmcnt(0)\n\t"
        "v_smfmac_f32_16x16x64_bf16 v[112:115], v[44:47], v[104:111], %[sidx]\n\t"
        "s_nop 7\n\t"
        "v_add_f32 v116, v112, v114\n\t"
        "v_add_f32 v117, v113, v115\n\t"
        "v_add_f32 v118, v113, v115\n\t"
        "v_mov_b32_dpp v118, v116 row_ror:8 row_mask:0xf bank_mask:0x3\n\t"
        "v_mov_b32_dpp v116, v117 row_ror:8 row_mask:0xf bank_mask:0xc\n\t"
        "v_cvt_pk_bf16_f32 v119, v116, v118\n\t"
        "v_cmpx_o_f32 vcc, v116, v118\n\t"
        "buffer_store_dword v119, %[mo], %[rs], %[sm] offen sc1\n\t"
        "buffer_store_dword v119, %[mo], %[rs], s92 offen sc1\n\t"
        "buffer_store_dword v119, %[mo], %[rs], s93 offen sc1\n\t"
        "buffer_store_dword v119, %[mo], %[rs], s94 offen sc1\n\t"
        "s_nop 2\n\t"
        "s_cmp_eq_u64 exec, -1\n\t"
        "s_mov_b64 exec, -1\n\t"
        "s_cbranch_scc0 Lag%=\n\t"
        "s_barrier\n\t"
        "s_branch Lend%=\n\t"
        "Lag%=:\n\t"
        "v_cmp_ne_u32 vcc, %[fv], v121\n\t"
        "s_add_u32 %[pass], %[pass], 1\n\t"
        "s_cmp_eq_u32 s97, 0\n\t"
        "s_cbranch_scc1 Lfi%=\n\t"
        "s_cbranch_vccnz Lzp_%=\n\t"
        "v_max_u32 v116, v16, v17\n\t"
        "v_max3_u32 v116, v116, v18, v19\n\t"
        "v_max3_u32 v116, v116, v20, v21\n\t"
        "v_max3_u32 v116, v116, v22, v23\n\t"
        "v_max3_u32 v116, v116, v24, v25\n\t"
        "v_max3_u32 v116, v116, v26, v27\n\t"
        "v_max3_u32 v116, v116, v28, v29\n\t"
        "v_max3_u32 v116, v116, v30, v31\n\t"
        "v_max3_u32 v116, v116, v32, v33\n\t"
        "v_max3_u32 v116, v116, v34, v35\n\t"
        "v_max3_u32 v116, v116, v36, v37\n\t"
        "v_max3_u32 v116, v116, v38, v39\n\t"
        "v_max3_u32 v116, v116, v40, v41\n\t"
        "v_max3_u32 v116, v116, v42, v43\n\t"
        "v_max3_u32 v116, v116, v44, v45\n\t"
        "v_max3_u32 v116, v116, v46, v47\n\t"
        "v_max3_u32 v116, v116, v48, v49\n\t"
        "v_max3_u32 v116, v116, v50, v51\n\t"
        "v_max3_u32 v116, v116, v52, v53\n\t"
        "v_max3_u32 v116, v116, v54, v55\n\t"
        "v_max3_u32 v116, v116, v56, v57\n\t"
        "v_max3_u32 v116, v116, v58, v59\n\t"
        "v_max3_u32 v116, v116, v60, v61\n\t"
        "v_max3_u32 v116, v116, v62, v63\n\t"
        "v_max3_u32 v116, v116, v64, v65\n\t"
        "v_max3_u32 v116, v116, v66, v67\n\t"
        "v_max3_u32 v116, v116, v68, v69\n\t"
        "v_max3_u32 v116, v116, v70, v71\n\t"
        "v_max3_u32 v116, v116, v72, v73\n\t"
        "v_max3_u32 v116, v116, v74, v75\n\t"
        "v_max3_u32 v116, v116, v76, v77\n\t"
        "v_max3_u32 v116, v116, v78, v79\n\t"
        "v_max3_u32 v116, v116, v80, v81\n\t"
        "v_max3_u32 v116, v116, v82, v83\n\t"
        "v_max3_u32 v116, v116, v84, v85\n\t"
        "v_max3_u32 v116, v116, v86, v87\n\t"
        "v_max3_u32 v116, v116, v88, v89\n\t"
        "v_max3_u32 v116, v116, v90, v91\n\t"
        "v_max3_u32 v116, v116, v92, v93\n\t"
        "v_max3_u32 v116, v116, v94, v95\n\t"
        "v_max3_u32 v116, v116, v96, v97\n\t"
        "v_max3_u32 v116, v116, v98, v99\n\t"
        "v_max3_u32 v116, v116, v100, v101\n\t"
        "v_max3_u32 v116, v116, v102, v103\n\t"
        "v_max3_u32 v116, v116, v104, v105\n\t"
        "v_max3_u32 v116, v116, v106, v107\n\t"
        "v_max3_u32 v116, v116, v108, v109\n\t"
        "v_max3_u32 v116, v116, v110, v111\n\t"
        "v_cmp_eq_u32 vcc, 0xffbfffbf, v116\n\t"
        "s_cmp_lt_u32 %[pass], %[cap]\n\t"
        "s_cbranch_vccz Lbad%=\n\t"
        "s_cbranch_scc1 Lzp_%=\n\t"
        "Lbad%=:\n\t"
        "s_mov_b32 %[bad], 1\n\t"
        "s_branch Lend%=\n\t"
        "Lfi%=:\n\t"
        "s_mov_b32 s97, 1\n\t"
        "v_mov_b32 v116, %[fv]\n\t"
        "ds_write_b32 v120, v116 offset:16392\n\t"
        "s_waitcnt lgkmcnt(0)\n\t"
        "s_barrier\n\t"
        "s_branch Lrd%=\n\t"
        "Lend%=:\n\t"
        "s_setprio 0\n\t"
        : [bad] "=&s"(bad), [pass] "=&s"(pass)
        : [sidx] "v"(sidx), [pv] "v"(pv), [zv] "v"(zv), [ra] "v"(ra), [rb] "v"(rb), [mo] "v"(mo), [uf] "v"(uf), [rs] "s"(rs), [so] "s"(so), [sz] "s"(sz), [sm] "s"(sm), [lb] "s"(lb), [fv] "s"(fv), [cap] "s"(cap), [zp] "s"(zp)
        : "s88", "s89", "s90", "s91", "s92", "s93", "s94", "s95", "s96", "s97", "s98", "v14", "v15", "v16", "v17", "v18", "v19", "v20", "v21", "v22", "v23", "v24", "v25", "v26", "v27", "v28", "v29", "v30", "v31", "v32", "v33", "v34", "v35", "v36", "v37", "v38", "v39", "v40", "v41", "v42", "v43", "v44", "v45", "v46", "v47", "v48", "v49", "v50", "v51", "v52", "v53", "v54", "v55", "v56", "v57", "v58", "v59", "v60", "v61", "v62", "v63", "v64", "v65", "v66", "v67", "v68", "v69", "v70", "v71", "v72", "v73", "v74", "v75", "v76", "v77", "v78", "v79", "v80", "v81", "v82", "v83", "v84", "v85", "v86", "v87", "v88", "v89", "v90", "v91", "v92", "v93", "v94", "v95", "v96", "v97", "v98", "v99", "v100", "v101", "v102", "v103", "v104", "v105", "v106", "v107", "v108", "v109", "v110", "v111", "v112", "v113", "v114", "v115", "v116", "v117", "v118", "v119", "v120", "v121", "vcc", "scc", "memory");
}

__device__ __forceinline__ void wx0(const u32x32 (&W)[5], u32 xa, u32 oa, u32x4 rs, u32 xv, u32 xs, u32 xd, u32 o0, u32 o1 STP) {
    asm volatile(
        "ds_read_b128 v[192:195], %[xa] offset:0\n\t"
        "ds_read_b128 v[196:199], %[xa] offset:20480\n\t"
        "ds_read_b128 v[200:203], %[xa] offset:1024\n\t"
        "ds_read_b128 v[204:207], %[xa] offset:21504\n\t"
        "ds_read_b128 v[208:211], %[xa] offset:2048\n\t"
        "ds_read_b128 v[212:215], %[xa] offset:22528\n\t"
        "s_waitcnt vmcnt(8)\n\t"
        "s_barrier\n\t"
        "s_waitcnt lgkmcnt(4)\n\t"
        "v_mfma_f32_16x16x32_bf16 v[176:179], v[16:19], v[192:195], 0\n\t"
        "ds_read_b128 v[216:219], %[xa] offset:3072\n\t"
        "v_mfma_f32_16x16x32_bf16 v[184:187], v[16:19], v[196:199], 0\n\t"
        "ds_read_b128 v[220:223], %[xa] offset:23552\n\t"
        "v_mfma_f32_16x16x32_bf16 v[180:183], v[96:99], v[192:195], 0\n\t"
        "v_mfma_f32_16x16x32_bf16 v[188:191], v[96:99], v[196:199], 0\n\t"
        "s_waitcnt lgkmcnt(4)\n\t"
        "v_mfma_f32_16x16x32_bf16 v[176:179], v[20:23], v[200:203], v[176:179]\n\t"
        "ds_read_b128 v[192:195], %[xa] offset:4096\n\t"
        "v_mfma_f32_16x16x32_bf16 v[184:187], v[20:23], v[204:207], v[184:187]\n\t"
        "ds_read_b128 v[196:199], %[xa] offset:24576\n\t"
        "v_mfma_f32_16x16x32_bf16 v[180:183], v[100:103], v[200:203], v[180:183]\n\t"
        "v_mfma_f32_16x16x32_bf16 v[188:191], v[100:103], v[204:207], v[188:191]\n\t"
        "s_waitcnt lgkmcnt(4)\n\t"
        "v_mfma_f32_16x16x32_bf16 v[176:179], v[24:27], v[208:211], v[176:179]\n\t"
        "ds_read_b128 v[200:203], %[xa] offset:5120\n\t"
        "v_mfma_f32_16x16x32_bf16 v[184:187], v[24:27], v[212:215], v[184:187]\n\t"
        "ds_read_b128 v[204:207], %[xa] offset:25600\n\t"
        "v_mfma_f32_16x16x32_bf16 v[180:183], v[104:107], v[208:211], v[180:183]\n\t"
        "v_mfma_f32_16x16x32_bf16 v[188:191], v[104:107], v[212:215], v[188:191]\n\t"
        "s_waitcnt lgkmcnt(4)\n\t"
        "v_mfma_f32_16x16x32_bf16 v[176:179], v[28:31], v[216:219], v[176:179]\n\t"
        "ds_read_b128 v[208:211], %[xa] offset:6144\n\t"
        "v_mfma_f32_16x16x32_bf16 v[184:187], v[28:31], v[220:223], v[184:187]\n\t"
        "ds_read_b128 v[212:215], %[xa] offset:26624\n\t"
        "v_mfma_f32_16x16x32_bf16 v[180:183], v[108:111], v[216:219], v[180:183]\n\t"
        "v_mfma_f32_16x16x32_bf16 v[188:191], v[108:111], v[220:223], v[188:191]\n\t"
        "s_waitcnt lgkmcnt(4)\n\t"
        "v_mfma_f32_16x16x32_bf16 v[176:179], v[32:35], v[192:195], v[176:179]\n\t"
        "ds_read_b128 v[216:219], %[xa] offset:7168\n\t"
        "v_mfma_f32_16x16x32_bf16 v[184:187], v[32:35], v[196:199], v[184:187]\n\t"
        "ds_read_b128 v[220:223], %[xa] offset:27648\n\t"
        "v_mfma_f32_16x16x32_bf16 v[180:183], v[112:115], v[192:195], v[180:183]\n\t"
        "v_mfma_f32_16x16x32_bf16 v[188:191], v[112:115], v[196:199], v[188:191]\n\t"
        "s_waitcnt lgkmcnt(4)\n\t"
        "v_mfma_f32_16x16x32_bf16 v[176:179], v[36:39], v[200:203], v[176:179]\n\t"
        "ds_read_b128 v[192:195], %[xa] offset:8192\n\t"
        "v_mfma_f32_16x16x32_bf16 v[184:187], v[36:39], v[204:207], v[184:187]\n\t"
        "ds_read_b128 v[196:199], %[xa] offset:28672\n\t"
        "v_mfma_f32_16x16x32_bf16 v[180:183], v[116:119], v[200:203], v[180:183]\n\t"
        "v_mfma_f32_16x16x32_bf16 v[188:191], v[116:119], v[204:207], v[188:191]\n\t"
        "s_waitcnt lgkmcnt(4)\n\t"
        "v_mfma_f32_16x16x32_bf16 v[176:179], v[40:43], v[208:211], v[176:179]\n\t"
        "ds_read_b128 v[200:203], %[xa] offset:9216\n\t"
        "v_mfma_f32_16x16x32_bf16 v[184:187], v[40:43], v[212:215], v[184:187]\n\t"
        "ds_read_b128 v[204:207], %[xa] offset:29696\n\t"
        "v_mfma_f32_16x16x32_bf16 v[180:183], v[120:123], v[208:211], v[180:183]\n\t"
        "v_mfma_f32_16x16x32_bf16 v[188:191], v[120:123], v[212:215], v[188:191]\n\t"
        "s_waitcnt lgkmcnt(4)\n\t"
        "v_mfma_f32_16x16x32_bf16 v[176:179], v[44:47], v[216:219], v[176:179]\n\t"
        "ds_read_b128 v[208:211], %[xa] offset:10240\n\t"
        "v_mfma_f32_16x16x32_bf16 v[184:187], v[44:47], v[220:223], v[184:187]\n\t"
        "ds_read_b128 v[212:215], %[xa] offset:30720\n\t"
        "v_mfma_f32_16x16x32_bf16 v[180:183], v[124:127], v[216:219], v[180:183]\n\t"
        "v_mfma_f32_16x16x32_bf16 v[188:191], v[124:127], v[220:223], v[188:191]\n\t"
        "s_waitcnt lgkmcnt(4)\n\t"
        "v_mfma_f32_16x16x32_bf16 v[176:179], v[48:51], v[192:195], v[176:179]\n\t"
        "ds_read_b128 v[216:219], %[xa] offset:11264\n\t"
        "v_mfma_f32_16x16x32_bf16 v[184:187], v[48:51], v[196:199], v[184:187]\n\t"
        "ds_read_b128 v[220:223], %[xa] offset:31744\n\t"
        "v_mfma_f32_16x16x32_bf16 v[180:183], v[128:131], v[192:195], v[180:183]\n\t"
        "v_mfma_f32_16x16x32_bf16 v[188:191], v[128:131], v[196:199], v[188:191]\n\t"
        "s_waitcnt lgkmcnt(4)\n\t"
        "v_mfma_f32_16x16x32_bf16 v[176:179], v[52:55], v[200:203], v[176:179]\n\t"
        "ds_read_b128 v[192:195], %[xa] offset:12288\n\t"
        "v_mfma_f32_16x16x32_bf16 v[184:187], v[52:55], v[204:207], v[184:187]\n\t"
        "ds_read_b128 v[196:199], %[xa] offset:32768\n\t"
        "v_mfma_f32_16x16x32_bf16 v[180:183], v[132:135], v[200:203], v[180:183]\n\t"
        "v_mfma_f32_16x16x32_bf16 v[188:191], v[132:135], v[204:207], v[188:191]\n\t"
        "s_waitcnt lgkmcnt(4)\n\t"
        "v_mfma_f32_16x16x32_bf16 v[176:179], v[56:59], v[208:211], v[176:179]\n\t"
        "ds_read_b128 v[200:203], %[xa] offset:13312\n\t"
        "v_mfma_f32_16x16x32_bf16 v[184:187], v[56:59], v[212:215], v[184:187]\n\t"
        "ds_read_b128 v[204:207], %[xa] offset:33792\n\t"
        "v_mfma_f32_16x16x32_bf16 v[180:183], v[136:139], v[208:211], v[180:183]\n\t"
        "v_mfma_f32_16x16x32_bf16 v[188:191], v[136:139], v[212:215], v[188:191]\n\t"
        "s_waitcnt lgkmcnt(4)\n\t"
        "v_mfma_f32_16x16x32_bf16 v[176:179], v[60:63], v[216:219], v[176:179]\n\t"
        "ds_read_b128 v[208:211], %[xa] offset:14336\n\t"
        "v_mfma_f32_16x16x32_bf16 v[184:187], v[60:63], v[220:223], v[184:187]\n\t"
        "ds_read_b128 v[212:215], %[xa] offset:34816\n\t"
        "v_mfma_f32_16x16x32_bf16 v[180:183], v[140:143], v[216:219], v[180:183]\n\t"
        "v_mfma_f32_16x16x32_bf16 v[188:191], v[140:143], v[220:223], v[188:191]\n\t"
        "s_waitcnt lgkmcnt(4)\n\t"
        "v_mfma_f32_16x16x32_bf16 v[176:179], v[64:67], v[192:195], v[176:179]\n\t"
        "ds_read_b128 v[216:219], %[xa] offset:15360\n\t"
        "v_mfma_f32_16x16x32_bf16 v[184:187], v[64:67], v[196:199], v[184:187]\n\t"
        "ds_read_b128 v[220:223], %[xa] offset:35840\n\t"
        "v_mfma_f32_16x16x32_bf16 v[180:183], v[144:147], v[192:195], v[180:183]\n\t"
        "v_mfma_f32_16x16x32_bf16 v[188:191], v[144:147], v[196:199], v[188:191]\n\t"
        "s_waitcnt lgkmcnt(4)\n\t"
        "v_mfma_f32_16x16x32_bf16 v[176:179], v[68:71], v[200:203], v[176:179]\n\t"
        "ds_read_b128 v[192:195], %[xa] offset:16384\n\t"
        "v_mfma_f32_16x16x32_bf16 v[184:187], v[68:71], v[204:207], v[184:187]\n\t"
        "ds_read_b128 v[196:199], %[xa] offset:36864\n\t"
        "v_mfma_f32_16x16x32_bf16 v[180:183], v[148:151], v[200:203], v[180:183]\n\t"
        "v_mfma_f32_16x16x32_bf16 v[188:191], v[148:151], v[204:207], v[188:191]\n\t"
        "s_waitcnt lgkmcnt(4)\n\t"
        "v_mfma_f32_16x16x32_bf16 v[176:179], v[72:75], v[208:211], v[176:179]\n\t"
        "ds_read_b128 v[200:203], %[xa] offset:17408\n\t"
        "v_mfma_f32_16x16x32_bf16 v[184:187], v[72:75], v[212:215], v[184:187]\n\t"
        "ds_read_b128 v[204:207], %[xa] offset:37888\n\t"
        "v_mfma_f32_16x16x32_bf16 v[180:183], v[152:155], v[208:211], v[180:183]\n\t"
        "v_mfma_f32_16x16x32_bf16 v[188:191], v[152:155], v[212:215], v[188:191]\n\t"
        "s_waitcnt lgkmcnt(4)\n\t"
        "v_mfma_f32_16x16x32_bf16 v[176:179], v[76:79], v[216:219], v[176:179]\n\t"
        "ds_read_b128 v[208:211], %[xa] offset:18432\n\t"
        "v_mfma_f32_16x16x32_bf16 v[184:187], v[76:79], v[220:223], v[184:187]\n\t"
        "ds_read_b128 v[212:215], %[xa] offset:38912\n\t"
        "v_mfma_f32_16x16x32_bf16 v[180:183], v[156:159], v[216:219], v[180:183]\n\t"
        "v_mfma_f32_16x16x32_bf16 v[188:191], v[156:159], v[220:223], v[188:191]\n\t"
        "s_waitcnt lgkmcnt(4)\n\t"
        "v_mfma_f32_16x16x32_bf16 v[176:179], v[80:83], v[192:195], v[176:179]\n\t"
        "ds_read_b128 v[216:219], %[xa] offset:19456\n\t"
        "v_mfma_f32_16x16x32_bf16 v[184:187], v[80:83], v[196:199], v[184:187]\n\t"
        "ds_read_b128 v[220:223], %[xa] offset:39936\n\t"
        "v_mfma_f32_16x16x32_bf16 v[180:183], v[160:163], v[192:195], v[180:183]\n\t"
        "v_mfma_f32_16x16x32_bf16 v[188:191], v[160:163], v[196:199], v[188:191]\n\t"
        "s_waitcnt lgkmcnt(4)\n\t"
        "v_mfma_f32_16x16x32_bf16 v[176:179], v[84:87], v[200:203], v[176:179]\n\t"
        "v_mfma_f32_16x16x32_bf16 v[184:187], v[84:87], v[204:207], v[184:187]\n\t"
        "v_mfma_f32_16x16x32_bf16 v[180:183], v[164:167], v[200:203], v[180:183]\n\t"
        "v_mfma_f32_16x16x32_bf16 v[188:191], v[164:167], v[204:207], v[188:191]\n\t"
        "s_waitcnt lgkmcnt(2)\n\t"
        "v_mfma_f32_16x16x32_bf16 v[176:179], v[88:91], v[208:211], v[176:179]\n\t"
        "v_mfma_f32_16x16x32_bf16 v[184:187], v[88:91], v[212:215], v[184:187]\n\t"
        "v_mfma_f32_16x16x32_bf16 v[180:183], v[168:171], v[208:211], v[180:183]\n\t"
        "v_mfma_f32_16x16x32_bf16 v[188:191], v[168:171], v[212:215], v[188:191]\n\t"
        "s_waitcnt lgkmcnt(0)\n\t"
        "v_mfma_f32_16x16x32_bf16 v[176:179], v[92:95], v[216:219], v[176:179]\n\t"
        "v_mfma_f32_16x16x32_bf16 v[184:187], v[92:95], v[220:223], v[184:187]\n\t"
        "v_mfma_f32_16x16x32_bf16 v[180:183], v[172:175], v[216:219], v[180:183]\n\t"
        "v_mfma_f32_16x16x32_bf16 v[188:191], v[172:175], v[220:223], v[188:191]\n\t"
        "s_nop 4\n\t"
        "v_cvt_pk_bf16_f32 v224, v176, v177\n\t"
        "v_cvt_pk_bf16_f32 v225, v178, v179\n\t"
        "v_cvt_pk_bf16_f32 v226, v180, v181\n\t"
        "v_cvt_pk_bf16_f32 v227, v182, v183\n\t"
        "buffer_store_dwordx4 v[224:227], %[oa], %[rs], %[o0] offen sc1\n\t"
        "v_cvt_pk_bf16_f32 v228, v184, v185\n\t"
        "v_cvt_pk_bf16_f32 v229, v186, v187\n\t"
        "v_cvt_pk_bf16_f32 v230, v188, v189\n\t"
        "v_cvt_pk_bf16_f32 v231, v190, v191\n\t"
        "buffer_store_dwordx4 v[228:231], %[oa], %[rs], %[o1] offen sc1\n\t"
        "s_mov_b32 s88, m0\n\t"
        "s_mov_b32 m0, %[xd]\n\t"
        "s_add_u32 s89, %[xs], 0x1000\n\t"
        "buffer_load_dwordx4 %[xv], %[rs], %[xs] offen offset:0 sc1 lds\n\t"
        "buffer_load_dwordx4 %[xv], %[rs], %[xs] offen offset:1024 sc1 lds\n\t"
        "buffer_load_dwordx4 %[xv], %[rs], %[xs] offen offset:2048 sc1 lds\n\t"
        "buffer_load_dwordx4 %[xv], %[rs], %[xs] offen offset:3072 sc1 lds\n\t"
        "s_add_u32 m0, m0, 0x1000\n\t"
        "s_nop 0\n\t"
        "buffer_load_dwordx4 %[xv], %[rs], s89 offen offset:0 sc1 lds\n\t"
        "buffer_load_dwordx4 %[xv], %[rs], s89 offen offset:1024 sc1 lds\n\t"
        "buffer_load_dwordx4 %[xv], %[rs], s89 offen offset:2048 sc1 lds\n\t"
        "buffer_load_dwordx4 %[xv], %[rs], s89 offen offset:3072 sc1 lds\n\t"
        "s_mov_b32 m0, s88\n\t"
        :
        : [w0] "{v[16:47]}"(W[0]), [w1] "{v[48:79]}"(W[1]), [w2] "{v[80:111]}"(W[2]), [w3] "{v[112:143]}"(W[3]), [w4] "{v[144:175]}"(W[4]), [xa] "v"(xa), [oa] "v"(oa), [xv] "v"(xv), [xs] "s"(xs), [xd] "s"(xd), [rs] "s"(rs), [o0] "s"(o0), [o1] "s"(o1)
        : "s88", "s89", "v176", "v177", "v178", "v179", "v180", "v181", "v182", "v183", "v184", "v185", "v186", "v187", "v188", "v189", "v190", "v191", "v192", "v193", "v194", "v195", "v196", "v197", "v198", "v199", "v200", "v201", "v202", "v203", "v204", "v205", "v206", "v207", "v208", "v209", "v210", "v211", "v212", "v213", "v214", "v215", "v216", "v217", "v218", "v219", "v220", "v221", "v222", "v223", "v224", "v225", "v226", "v227", "v228", "v229", "v230", "v231", "vcc", "scc", "memory");
}

__device__ __forceinline__ void wx1(const u32x32 (&W)[5], u32 xa, u32 oa STP) {
    asm volatile(
        "ds_read_b128 v[192:195], %[xa] offset:0\n\t"
        "ds_read_b128 v[196:199], %[xa] offset:20480\n\t"
        "ds_read_b128 v[200:203], %[xa] offset:1024\n\t"
        "ds_read_b128 v[204:207], %[xa] offset:21504\n\t"
        "ds_read_b128 v[208:211], %[xa] offset:2048\n\t"
        "ds_read_b128 v[212:215], %[xa] offset:22528\n\t"
        "s_waitcnt lgkmcnt(6)\n\t"
        "s_barrier\n\t"
        "s_waitcnt lgkmcnt(4)\n\t"
        "v_mfma_f32_16x16x32_bf16 v[176:179], v[16:19], v[192:195], 0\n\t"
        "ds_read_b128 v[216:219], %[xa] offset:3072\n\t"
        "v_mfma_f32_16x16x32_bf16 v[184:187], v[16:19], v[196:199], 0\n\t"
        "ds_read_b128 v[220:223], %[xa] offset:23552\n\t"
        "v_mfma_f32_16x16x32_bf16 v[180:183], v[96:99], v[192:195], 0\n\t"
        "v_mfma_f32_16x16x32_bf16 v[188:191], v[96:99], v[196:199], 0\n\t"
        "s_waitcnt lgkmcnt(4)\n\t"
        "v_mfma_f32_16x16x32_bf16 v[176:179], v[20:23], v[200:203], v[176:179]\n\t"
        "ds_read_b128 v[192:195], %[xa] offset:4096\n\t"
        "v_mfma_f32_16x16x32_bf16 v[184:187], v[20:23], v[204:207], v[184:187]\n\t"
        "ds_read_b128 v[196:199], %[xa] offset:24576\n\t"
        "v_mfma_f32_16x16x32_bf16 v[180:183], v[100:103], v[200:203], v[180:183]\n\t"
        "v_mfma_f32_16x16x32_bf16 v[188:191], v[100:103], v[204:207], v[188:191]\n\t"
        "s_waitcnt lgkmcnt(4)\n\t"
        "v_mfma_f32_16x16x32_bf16 v[176:179], v[24:27], v[208:211], v[176:179]\n\t"
        "ds_read_b128 v[200:203], %[xa] offset:5120\n\t"
        "v_mfma_f32_16x16x32_bf16 v[184:187], v[24:27], v[212:215], v[184:187]\n\t"
        "ds_read_b128 v[204:207], %[xa] offset:25600\n\t"
        "v_mfma_f32_16x16x32_bf16 v[180:183], v[104:107], v[208:211], v[180:183]\n\t"
        "v_mfma_f32_16x16x32_bf16 v[188:191], v[104:107], v[212:215], v[188:191]\n\t"
        "s_waitcnt lgkmcnt(4)\n\t"
        "v_mfma_f32_16x16x32_bf16 v[176:179], v[28:31], v[216:219], v[176:179]\n\t"
        "ds_read_b128 v[208:211], %[xa] offset:6144\n\t"
        "v_mfma_f32_16x16x32_bf16 v[184:187], v[28:31], v[220:223], v[184:187]\n\t"
        "ds_read_b128 v[212:215], %[xa] offset:26624\n\t"
        "v_mfma_f32_16x16x32_bf16 v[180:183], v[108:111], v[216:219], v[180:183]\n\t"
        "v_mfma_f32_16x16x32_bf16 v[188:191], v[108:111], v[220:223], v[188:191]\n\t"
        "s_waitcnt lgkmcnt(4)\n\t"
        "v_mfma_f32_16x16x32_bf16 v[176:179], v[32:35], v[192:195], v[176:179]\n\t"
        "ds_read_b128 v[216:219], %[xa] offset:7168\n\t"
        "v_mfma_f32_16x16x32_bf16 v[184:187], v[32:35], v[196:199], v[184:187]\n\t"
        "ds_read_b128 v[220:223], %[xa] offset:27648\n\t"
        "v_mfma_f32_16x16x32_bf16 v[180:183], v[112:115], v[192:195], v[180:183]\n\t"
        "v_mfma_f32_16x16x32_bf16 v[188:191], v[112:115], v[196:199], v[188:191]\n\t"
        "s_waitcnt lgkmcnt(4)\n\t"
        "v_mfma_f32_16x16x32_bf16 v[176:179], v[36:39], v[200:203], v[176:179]\n\t"
        "ds_read_b128 v[192:195], %[xa] offset:8192\n\t"
        "v_mfma_f32_16x16x32_bf16 v[184:187], v[36:39], v[204:207], v[184:187]\n\t"
        "ds_read_b128 v[196:199], %[xa] offset:28672\n\t"
        "v_mfma_f32_16x16x32_bf16 v[180:183], v[116:119], v[200:203], v[180:183]\n\t"
        "v_mfma_f32_16x16x32_bf16 v[188:191], v[116:119], v[204:207], v[188:191]\n\t"
        "s_waitcnt lgkmcnt(4)\n\t"
        "v_mfma_f32_16x16x32_bf16 v[176:179], v[40:43], v[208:211], v[176:179]\n\t"
        "ds_read_b128 v[200:203], %[xa] offset:9216\n\t"
        "v_mfma_f32_16x16x32_bf16 v[184:187], v[40:43], v[212:215], v[184:187]\n\t"
        "ds_read_b128 v[204:207], %[xa] offset:29696\n\t"
        "v_mfma_f32_16x16x32_bf16 v[180:183], v[120:123], v[208:211], v[180:183]\n\t"
        "v_mfma_f32_16x16x32_bf16 v[188:191], v[120:123], v[212:215], v[188:191]\n\t"
        "s_waitcnt lgkmcnt(4)\n\t"
        "v_mfma_f32_16x16x32_bf16 v[176:179], v[44:47], v[216:219], v[176:179]\n\t"
        "ds_read_b128 v[208:211], %[xa] offset:10240\n\t"
        "v_mfma_f32_16x16x32_bf16 v[184:187], v[44:47], v[220:223], v[184:187]\n\t"
        "ds_read_b128 v[212:215], %[xa] offset:30720\n\t"
        "v_mfma_f32_16x16x32_bf16 v[180:183], v[124:127], v[216:219], v[180:183]\n\t"
        "v_mfma_f32_16x16x32_bf16 v[188:191], v[124:127], v[220:223], v[188:191]\n\t"
        "s_waitcnt lgkmcnt(4)\n\t"
        "v_mfma_f32_16x16x32_bf16 v[176:179], v[48:51], v[192:195], v[176:179]\n\t"
        "ds_read_b128 v[216:219], %[xa] offset:11264\n\t"
        "v_mfma_f32_16x16x32_bf16 v[184:187], v[48:51], v[196:199], v[184:187]\n\t"
        "ds_read_b128 v[220:223], %[xa] offset:31744\n\t"
        "v_mfma_f32_16x16x32_bf16 v[180:183], v[128:131], v[192:195], v[180:183]\n\t"
        "v_mfma_f32_16x16x32_bf16 v[188:191], v[128:131], v[196:199], v[188:191]\n\t"
        "s_waitcnt lgkmcnt(4)\n\t"
        "v_mfma_f32_16x16x32_bf16 v[176:179], v[52:55], v[200:203], v[176:179]\n\t"
        "ds_read_b128 v[192:195], %[xa] offset:12288\n\t"
        "v_mfma_f32_16x16x32_bf16 v[184:187], v[52:55], v[204:207], v[184:187]\n\t"
        "ds_read_b128 v[196:199], %[xa] offset:32768\n\t"
        "v_mfma_f32_16x16x32_bf16 v[180:183], v[132:135], v[200:203], v[180:183]\n\t"
        "v_mfma_f32_16x16x32_bf16 v[188:191], v[132:135], v[204:207], v[188:191]\n\t"
        "s_waitcnt lgkmcnt(4)\n\t"
        "v_mfma_f32_16x16x32_bf16 v[176:179], v[56:59], v[208:211], v[176:179]\n\t"
        "ds_read_b128 v[200:203], %[xa] offset:13312\n\t"
        "v_mfma_f32_16x16x32_bf16 v[184:187], v[56:59], v[212:215], v[184:187]\n\t"
        "ds_read_b128 v[204:207], %[xa] offset:33792\n\t"
        "v_mfma_f32_16x16x32_bf16 v[180:183], v[136:139], v[208:211], v[180:183]\n\t"
        "v_mfma_f32_16x16x32_bf16 v[188:191], v[136:139], v[212:215], v[188:191]\n\t"
        "s_waitcnt lgkmcnt(4)\n\t"
        "v_mfma_f32_16x16x32_bf16 v[176:179], v[60:63], v[216:219], v[176:179]\n\t"
        "ds_read_b128 v[208:211], %[xa] offset:14336\n\t"
        "v_mfma_f32_16x16x32_bf16 v[184:187], v[60:63], v[220:223], v[184:187]\n\t"
        "ds_read_b128 v[212:215], %[xa] offset:34816\n\t"
        "v_mfma_f32_16x16x32_bf16 v[180:183], v[140:143], v[216:219], v[180:183]\n\t"
        "v_mfma_f32_16x16x32_bf16 v[188:191], v[140:143], v[220:223], v[188:191]\n\t"
        "s_waitcnt lgkmcnt(4)\n\t"
        "v_mfma_f32_16x16x32_bf16 v[176:179], v[64:67], v[192:195], v[176:179]\n\t"
        "ds_read_b128 v[216:219], %[xa] offset:15360\n\t"
        "v_mfma_f32_16x16x32_bf16 v[184:187], v[64:67], v[196:199], v[184:187]\n\t"
        "ds_read_b128 v[220:223], %[xa] offset:35840\n\t"
        "v_mfma_f32_16x16x32_bf16 v[180:183], v[144:147], v[192:195], v[180:183]\n\t"
        "v_mfma_f32_16x16x32_bf16 v[188:191], v[144:147], v[196:199], v[188:191]\n\t"
        "s_waitcnt lgkmcnt(4)\n\t"
        "v_mfma_f32_16x16x32_bf16 v[176:179], v[68:71], v[200:203], v[176:179]\n\t"
        "ds_read_b128 v[192:195], %[xa] offset:16384\n\t"
        "v_mfma_f32_16x16x32_bf16 v[184:187], v[68:71], v[204:207], v[184:187]\n\t"
        "ds_read_b128 v[196:199], %[xa] offset:36864\n\t"
        "v_mfma_f32_16x16x32_bf16 v[180:183], v[148:151], v[200:203], v[180:183]\n\t"
        "v_mfma_f32_16x16x32_bf16 v[188:191], v[148:151], v[204:207], v[188:191]\n\t"
        "s_waitcnt lgkmcnt(4)\n\t"
        "v_mfma_f32_16x16x32_bf16 v[176:179], v[72:75], v[208:211], v[176:179]\n\t"
        "ds_read_b128 v[200:203], %[xa] offset:17408\n\t"
        "v_mfma_f32_16x16x32_bf16 v[184:187], v[72:75], v[212:215], v[184:187]\n\t"
        "ds_read_b128 v[204:207], %[xa] offset:37888\n\t"
        "v_mfma_f32_16x16x32_bf16 v[180:183], v[152:155], v[208:211], v[180:183]\n\t"
        "v_mfma_f32_16x16x32_bf16 v[188:191], v[152:155], v[212:215], v[188:191]\n\t"
        "s_waitcnt lgkmcnt(4)\n\t"
        "v_mfma_f32_16x16x32_bf16 v[176:179], v[76:79], v[216:219], v[176:179]\n\t"
        "ds_read_b128 v[208:211], %[xa] offset:18432\n\t"
        "v_mfma_f32_16x16x32_bf16 v[184:187], v[76:79], v[220:223], v[184:187]\n\t"
        "ds_read_b128 v[212:215], %[xa] offset:38912\n\t"
        "v_mfma_f32_16x16x32_bf16 v[180:183], v[156:159], v[216:219], v[180:183]\n\t"
        "v_mfma_f32_16x16x32_bf16 v[188:191], v[156:159], v[220:223], v[188:191]\n\t"
        "s_waitcnt lgkmcnt(4)\n\t"
        "v_mfma_f32_16x16x32_bf16 v[176:179], v[80:83], v[192:195], v[176:179]\n\t"
        "ds_read_b128 v[216:219], %[xa] offset:19456\n\t"
        "v_mfma_f32_16x16x32_bf16 v[184:187], v[80:83], v[196:199], v[184:187]\n\t"
        "ds_read_b128 v[220:223], %[xa] offset:39936\n\t"
        "v_mfma_f32_16x16x32_bf16 v[180:183], v[160:163], v[192:195], v[180:183]\n\t"
        "v_mfma_f32_16x16x32_bf16 v[188:191], v[160:163], v[196:199], v[188:191]\n\t"
        "s_waitcnt lgkmcnt(4)\n\t"
        "v_mfma_f32_16x16x32_bf16 v[176:179], v[84:87], v[200:203], v[176:179]\n\t"
        "v_mfma_f32_16x16x32_bf16 v[184:187], v[84:87], v[204:207], v[184:187]\n\t"
        "v_mfma_f32_16x16x32_bf16 v[180:183], v[164:167], v[200:203], v[180:183]\n\t"
        "v_mfma_f32_16x16x32_bf16 v[188:191], v[164:167], v[204:207], v[188:191]\n\t"
        "s_waitcnt lgkmcnt(2)\n\t"
        "v_mfma_f32_16x16x32_bf16 v[176:179], v[88:91], v[208:211], v[176:179]\n\t"
        "v_mfma_f32_16x16x32_bf16 v[184:187], v[88:91], v[212:215], v[184:187]\n\t"
        "v_mfma_f32_16x16x32_bf16 v[180:183], v[168:171], v[208:211], v[180:183]\n\t"
        "v_mfma_f32_16x16x32_bf16 v[188:191], v[168:171], v[212:215], v[188:191]\n\t"
        "s_waitcnt lgkmcnt(0)\n\t"
        "v_mfma_f32_16x16x32_bf16 v[176:179], v[92:95], v[216:219], v[176:179]\n\t"
        "v_mfma_f32_16x16x32_bf16 v[184:187], v[92:95], v[220:223], v[184:187]\n\t"
        "v_mfma_f32_16x16x32_bf16 v[180:183], v[172:175], v[216:219], v[180:183]\n\t"
        "v_mfma_f32_16x16x32_bf16 v[188:191], v[172:175], v[220:223], v[188:191]\n\t"
        "s_nop 4\n\t"
        "v_cvt_pk_bf16_f32 v224, v176, v177\n\t"
        "v_cvt_pk_bf16_f32 v225, v178, v179\n\t"
        "v_cvt_pk_bf16_f32 v226, v180, v181\n\t"
        "v_cvt_pk_bf16_f32 v227, v182, v183\n\t"
        "ds_write_b128 %[oa], v[224:227] offset:0\n\t"
        "v_cvt_pk_bf16_f32 v228, v184, v185\n\t"
        "v_cvt_pk_bf16_f32 v229, v186, v187\n\t"
        "v_cvt_pk_bf16_f32 v230, v188, v189\n\t"
        "v_cvt_pk_bf16_f32 v231, v190, v191\n\t"
        "ds_write_b128 %[oa], v[228:231] offset:4096\n\t"
        :
        : [w0] "{v[16:47]}"(W[0]), [w1] "{v[48:79]}"(W[1]), [w2] "{v[80:111]}"(W[2]), [w3] "{v[112:143]}"(W[3]), [w4] "{v[144:175]}"(W[4]), [xa] "v"(xa), [oa] "v"(oa)
        : "v176", "v177", "v178", "v179", "v180", "v181", "v182", "v183", "v184", "v185", "v186", "v187", "v188", "v189", "v190", "v191", "v192", "v193", "v194", "v195", "v196", "v197", "v198", "v199", "v200", "v201", "v202", "v203", "v204", "v205", "v206", "v207", "v208", "v209", "v210", "v211", "v212", "v213", "v214", "v215", "v216", "v217", "v218", "v219", "v220", "v221", "v222", "v223", "v224", "v225", "v226", "v227", "v228", "v229", "v230", "v231", "vcc", "scc", "memory");
}

__device__ __forceinline__ void wk(const u32x32 (&W)[5], u32 xa, u32 oa, u32x4 rs, u32 o0, u32 o1, float sc, u32 xv, u32 xs, u32 xd, u32 ca, u32 fc, u32 fw, u32 fo STP) {
    asm volatile(
        "ds_read_b128 v[192:195], %[xa] offset:0\n\t"
        "ds_read_b128 v[196:199], %[xa] offset:20480\n\t"
        "ds_read_b128 v[200:203], %[xa] offset:1024\n\t"
        "ds_read_b128 v[204:207], %[xa] offset:21504\n\t"
        "ds_read_b128 v[208:211], %[xa] offset:2048\n\t"
        "ds_read_b128 v[212:215], %[xa] offset:22528\n\t"
        "v_mov_b32 v232, %[ca]\n\t"
        "v_mov_b32 v233, 1\n\t"
        "v_mov_b32 v234, %[fw]\n\t"
        "s_mov_b32 s88, m0\n\t"
        "s_mov_b32 m0, %[xd]\n\t"
        "s_add_u32 s89, %[xs], 0x1000\n\t"
        "buffer_load_dwordx4 %[xv], %[rs], %[xs] offen offset:0 sc1 lds\n\t"
        "buffer_load_dwordx4 %[xv], %[rs], %[xs] offen offset:1024 sc1 lds\n\t"
        "s_mov_b32 m0, s88\n\t"
        "s_waitcnt lgkmcnt(4)\n\t"
        "v_mfma_f32_16x16x32_bf16 v[176:179], v[16:19], v[192:195], 0\n\t"
        "ds_read_b128 v[216:219], %[xa] offset:3072\n\t"
        "v_mfma_f32_16x16x32_bf16 v[184:187], v[16:19], v[196:199], 0\n\t"
        "ds_read_b128 v[220:223], %[xa] offset:23552\n\t"
        "v_mfma_f32_16x16x32_bf16 v[180:183], v[96:99], v[192:195], 0\n\t"
        "v_mfma_f32_16x16x32_bf16 v[188:191], v[96:99], v[196:199], 0\n\t"
        "s_waitcnt lgkmcnt(4)\n\t"
        "v_mfma_f32_16x16x32_bf16 v[176:179], v[20:23], v[200:203], v[176:179]\n\t"
        "ds_read_b128 v[192:195], %[xa] offset:4096\n\t"
        "v_mfma_f32_16x16x32_bf16 v[184:187], v[20:23], v[204:207], v[184:187]\n\t"
        "ds_read_b128 v[196:199], %[xa] offset:24576\n\t"
        "v_mfma_f32_16x16x32_bf16 v[180:183], v[100:103], v[200:203], v[180:183]\n\t"
        "v_mfma_f32_16x16x32_bf16 v[188:191], v[100:103], v[204:207], v[188:191]\n\t"
        "s_waitcnt lgkmcnt(4)\n\t"
        "v_mfma_f32_16x16x32_bf16 v[176:179], v[24:27], v[208:211], v[176:179]\n\t"
        "ds_read_b128 v[200:203], %[xa] offset:5120\n\t"
        "v_mfma_f32_16x16x32_bf16 v[184:187], v[24:27], v[212:215], v[184:187]\n\t"
        "ds_read_b128 v[204:207], %[xa] offset:25600\n\t"
        "v_mfma_f32_16x16x32_bf16 v[180:183], v[104:107], v[208:211], v[180:183]\n\t"
        "v_mfma_f32_16x16x32_bf16 v[188:191], v[104:107], v[212:215], v[188:191]\n\t"
        "s_waitcnt lgkmcnt(4)\n\t"
        "v_mfma_f32_16x16x32_bf16 v[176:179], v[28:31], v[216:219], v[176:179]\n\t"
        "ds_read_b128 v[208:211], %[xa] offset:6144\n\t"
        "v_mfma_f32_16x16x32_bf16 v[184:187], v[28:31], v[220:223], v[184:187]\n\t"
        "ds_read_b128 v[212:215], %[xa] offset:26624\n\t"
        "v_mfma_f32_16x16x32_bf16 v[180:183], v[108:111], v[216:219], v[180:183]\n\t"
        "v_mfma_f32_16x16x32_bf16 v[188:191], v[108:111], v[220:223], v[188:191]\n\t"
        "s_waitcnt lgkmcnt(4)\n\t"
        "v_mfma_f32_16x16x32_bf16 v[176:179], v[32:35], v[192:195], v[176:179]\n\t"
        "ds_read_b128 v[216:219], %[xa] offset:7168\n\t"
        "v_mfma_f32_16x16x32_bf16 v[184:187], v[32:35], v[196:199], v[184:187]\n\t"
        "ds_read_b128 v[220:223], %[xa] offset:27648\n\t"
        "v_mfma_f32_16x16x32_bf16 v[180:183], v[112:115], v[192:195], v[180:183]\n\t"
        "v_mfma_f32_16x16x32_bf16 v[188:191], v[112:115], v[196:199], v[188:191]\n\t"
        "s_waitcnt lgkmcnt(4)\n\t"
        "v_mfma_f32_16x16x32_bf16 v[176:179], v[36:39], v[200:203], v[176:179]\n\t"
        "ds_read_b128 v[192:195], %[xa] offset:8192\n\t"
        "v_mfma_f32_16x16x32_bf16 v[184:187], v[36:39], v[204:207], v[184:187]\n\t"
        "ds_read_b128 v[196:199], %[xa] offset:28672\n\t"
        "v_mfma_f32_16x16x32_bf16 v[180:183], v[116:119], v[200:203], v[180:183]\n\t"
        "v_mfma_f32_16x16x32_bf16 v[188:191], v[116:119], v[204:207], v[188:191]\n\t"
        "s_waitcnt lgkmcnt(4)\n\t"
        "v_mfma_f32_16x16x32_bf16 v[176:179], v[40:43], v[208:211], v[176:179]\n\t"
        "ds_read_b128 v[200:203], %[xa] offset:9216\n\t"
        "v_mfma_f32_16x16x32_bf16 v[184:187], v[40:43], v[212:215], v[184:187]\n\t"
        "ds_read_b128 v[204:207], %[xa] offset:29696\n\t"
        "v_mfma_f32_16x16x32_bf16 v[180:183], v[120:123], v[208:211], v[180:183]\n\t"
        "s_waitcnt vmcnt(0)\n\t"
        "s_mov_b64 exec, 1\n\t"
        "ds_add_rtn_u32 v233, v232, v233\n\t"
        "s_mov_b64 exec, -1\n\t"
        "v_mfma_f32_16x16x32_bf16 v[188:191], v[120:123], v[212:215], v[188:191]\n\t"
        "s_waitcnt lgkmcnt(5)\n\t"
        "v_mfma_f32_16x16x32_bf16 v[176:179], v[44:47], v[216:219], v[176:179]\n\t"
        "ds_read_b128 v[208:211], %[xa] offset:10240\n\t"
        "v_mfma_f32_16x16x32_bf16 v[184:187], v[44:47], v[220:223], v[184:187]\n\t"
        "ds_read_b128 v[212:215], %[xa] offset:30720\n\t"
        "v_mfma_f32_16x16x32_bf16 v[180:183], v[124:127], v[216:219], v[180:183]\n\t"
        "v_mfma_f32_16x16x32_bf16 v[188:191], v[124:127], v[220:223], v[188:191]\n\t"
        "s_waitcnt lgkmcnt(5)\n\t"
        "v_mfma_f32_16x16x32_bf16 v[176:179], v[48:51], v[192:195], v[176:179]\n\t"
        "ds_read_b128 v[216:219], %[xa] offset:11264\n\t"
        "v_mfma_f32_16x16x32_bf16 v[184:187], v[48:51], v[196:199], v[184:187]\n\t"
        "ds_read_b128 v[220:223], %[xa] offset:31744\n\t"
        "v_mfma_f32_16x16x32_bf16 v[180:183], v[128:131], v[192:195], v[180:183]\n\t"
        "s_waitcnt lgkmcnt(4)\n\t"
        "s_mov_b64 exec, 1\n\t"
        "v_cmpx_eq_u32 vcc, %[fc], v233\n\t"
        "buffer_store_dword v234, off, %[rs], %[fo] sc1\n\t"
        "s_mov_b64 exec, -1\n\t"
        "v_mfma_f32_16x16x32_bf16 v[188:191], v[128:131], v[196:199], v[188:191]\n\t"
        "s_waitcnt lgkmcnt(5)\n\t"
        "v_mfma_f32_16x16x32_bf16 v[176:179], v[52:55], v[200:203], v[176:179]\n\t"
        "ds_read_b128 v[192:195], %[xa] offset:12288\n\t"
        "v_mfma_f32_16x16x32_bf16 v[184:187], v[52:55], v[204:207], v[184:187]\n\t"
        "ds_read_b128 v[196:199], %[xa] offset:32768\n\t"
        "v_mfma_f32_16x16x32_bf16 v[180:183], v[132:135], v[200:203], v[180:183]\n\t"
        "v_mfma_f32_16x16x32_bf16 v[188:191], v[132:135], v[204:207], v[188:191]\n\t"
        "s_waitcnt lgkmcnt(4)\n\t"
        "v_mfma_f32_16x16x32_bf16 v[176:179], v[56:59], v[208:211], v[176:179]\n\t"
        "ds_read_b128 v[200:203], %[xa] offset:13312\n\t"
        "v_mfma_f32_16x16x32_bf16 v[184:187], v[56:59], v[212:215], v[184:187]\n\t"
        "ds_read_b128 v[204:207], %[xa] offset:33792\n\t"
        "v_mfma_f32_16x16x32_bf16 v[180:183], v[136:139], v[208:211], v[180:183]\n\t"
        "v_mfma_f32_16x16x32_bf16 v[188:191], v[136:139], v[212:215], v[188:191]\n\t"
        "s_waitcnt lgkmcnt(4)\n\t"
        "v_mfma_f32_16x16x32_bf16 v[176:179], v[60:63], v[216:219], v[176:179]\n\t"
        "ds_read_b128 v[208:211], %[xa] offset:14336\n\t"
        "v_mfma_f32_16x16x32_bf16 v[184:187], v[60:63], v[220:223], v[184:187]\n\t"
        "ds_read_b128 v[212:215], %[xa] offset:34816\n\t"
        "v_mfma_f32_16x16x32_bf16 v[180:183], v[140:143], v[216:219], v[180:183]\n\t"
        "v_mfma_f32_16x16x32_bf16 v[188:191], v[140:143], v[220:223], v[188:191]\n\t"
        "s_waitcnt lgkmcnt(4)\n\t"
        "v_mfma_f32_16x16x32_bf16 v[176:179], v[64:67], v[192:195], v[176:179]\n\t"
        "ds_read_b128 v[216:219], %[xa] offset:15360\n\t"
        "v_mfma_f32_16x16x32_bf16 v[184:187], v[64:67], v[196:199], v[184:187]\n\t"
        "ds_read_b128 v[220:223], %[xa] offset:35840\n\t"
        "v_mfma_f32_16x16x32_bf16 v[180:183], v[144:147], v[192:195], v[180:183]\n\t"
        "v_mfma_f32_16x16x32_bf16 v[188:191], v[144:147], v[196:199], v[188:191]\n\t"
        "s_waitcnt lgkmcnt(4)\n\t"
        "v_mfma_f32_16x16x32_bf16 v[176:179], v[68:71], v[200:203], v[176:179]\n\t"
        "ds_read_b128 v[192:195], %[xa] offset:16384\n\t"
        "v_mfma_f32_16x16x32_bf16 v[184:187], v[68:71], v[204:207], v[184:187]\n\t"
        "ds_read_b128 v[196:199], %[xa] offset:36864\n\t"
        "v_mfma_f32_16x16x32_bf16 v[180:183], v[148:151], v[200:203], v[180:183]\n\t"
        "v_mfma_f32_16x16x32_bf16 v[188:191], v[148:151], v[204:207], v[188:191]\n\t"
        "s_waitcnt lgkmcnt(4)\n\t"
        "v_mfma_f32_16x16x32_bf16 v[176:179], v[72:75], v[208:211], v[176:179]\n\t"
        "ds_read_b128 v[200:203], %[xa] offset:17408\n\t"
        "v_mfma_f32_16x16x32_bf16 v[184:187], v[72:75], v[212:215], v[184:187]\n\t"
        "ds_read_b128 v[204:207], %[xa] offset:37888\n\t"
        "v_mfma_f32_16x16x32_bf16 v[180:183], v[152:155], v[208:211], v[180:183]\n\t"
        "v_mfma_f32_16x16x32_bf16 v[188:191], v[152:155], v[212:215], v[188:191]\n\t"
        "s_waitcnt lgkmcnt(4)\n\t"
        "v_mfma_f32_16x16x32_bf16 v[176:179], v[76:79], v[216:219], v[176:179]\n\t"
        "ds_read_b128 v[208:211], %[xa] offset:18432\n\t"
        "v_mfma_f32_16x16x32_bf16 v[184:187], v[76:79], v[220:223], v[184:187]\n\t"
        "ds_read_b128 v[212:215], %[xa] offset:38912\n\t"
        "v_mfma_f32_16x16x32_bf16 v[180:183], v[156:159], v[216:219], v[180:183]\n\t"
        "v_mfma_f32_16x16x32_bf16 v[188:191], v[156:159], v[220:223], v[188:191]\n\t"
        "s_waitcnt lgkmcnt(4)\n\t"
        "v_mfma_f32_16x16x32_bf16 v[176:179], v[80:83], v[192:195], v[176:179]\n\t"
        "ds_read_b128 v[216:219], %[xa] offset:19456\n\t"
        "v_mfma_f32_16x16x32_bf16 v[184:187], v[80:83], v[196:199], v[184:187]\n\t"
        "ds_read_b128 v[220:223], %[xa] offset:39936\n\t"
        "v_mfma_f32_16x16x32_bf16 v[180:183], v[160:163], v[192:195], v[180:183]\n\t"
        "v_mfma_f32_16x16x32_bf16 v[188:191], v[160:163], v[196:199], v[188:191]\n\t"
        "s_waitcnt lgkmcnt(4)\n\t"
        "v_mfma_f32_16x16x32_bf16 v[176:179], v[84:87], v[200:203], v[176:179]\n\t"
        "v_mfma_f32_16x16x32_bf16 v[184:187], v[84:87], v[204:207], v[184:187]\n\t"
        "v_mfma_f32_16x16x32_bf16 v[180:183], v[164:167], v[200:203], v[180:183]\n\t"
        "v_mfma_f32_16x16x32_bf16 v[188:191], v[164:167], v[204:207], v[188:191]\n\t"
        "s_waitcnt lgkmcnt(2)\n\t"
        "v_mfma_f32_16x16x32_bf16 v[176:179], v[88:91], v[208:211], v[176:179]\n\t"
        "v_mfma_f32_16x16x32_bf16 v[184:187], v[88:91], v[212:215], v[184:187]\n\t"
        "v_mfma_f32_16x16x32_bf16 v[180:183], v[168:171], v[208:211], v[180:183]\n\t"
        "v_mfma_f32_16x16x32_bf16 v[188:191], v[168:171], v[212:215], v[188:191]\n\t"
        "s_waitcnt lgkmcnt(0)\n\t"
        "v_mfma_f32_16x16x32_bf16 v[176:179], v[92:95], v[216:219], v[176:179]\n\t"
        "v_mfma_f32_16x16x32_bf16 v[184:187], v[92:95], v[220:223], v[184:187]\n\t"
        "v_mfma_f32_16x16x32_bf16 v[180:183], v[172:175], v[216:219], v[180:183]\n\t"
        "v_mfma_f32_16x16x32_bf16 v[188:191], v[172:175], v[220:223], v[188:191]\n\t"
        "s_nop 0\n\t"
        "s_barrier\n\t"
        "s_nop 4\n\t"
        "v_mul_f32 v176, %[sc], v176\n\t"
        "v_mul_f32 v177, %[sc], v177\n\t"
        "v_mul_f32 v178, %[sc], v178\n\t"
        "v_mul_f32 v179, %[sc], v179\n\t"
        "v_mul_f32 v180, %[sc], v180\n\t"
        "v_mul_f32 v181, %[sc], v181\n\t"
        "v_mul_f32 v182, %[sc], v182\n\t"
        "v_mul_f32 v183, %[sc], v183\n\t"
        "v_cvt_pk_bf16_f32 v224, v176, v177\n\t"
        "v_cvt_pk_bf16_f32 v225, v178, v179\n\t"
        "v_cvt_pk_bf16_f32 v226, v180, v181\n\t"
        "v_cvt_pk_bf16_f32 v227, v182, v183\n\t"
        "buffer_store_dwordx4 v[224:227], %[oa], %[rs], %[o0] offen sc1\n\t"
        "v_mul_f32 v184, %[sc], v184\n\t"
        "v_mul_f32 v185, %[sc], v185\n\t"
        "v_mul_f32 v186, %[sc], v186\n\t"
        "v_mul_f32 v187, %[sc], v187\n\t"
        "v_mul_f32 v188, %[sc], v188\n\t"
        "v_mul_f32 v189, %[sc], v189\n\t"
        "v_mul_f32 v190, %[sc], v190\n\t"
        "v_mul_f32 v191, %[sc], v191\n\t"
        "v_cvt_pk_bf16_f32 v228, v184, v185\n\t"
        "v_cvt_pk_bf16_f32 v229, v186, v187\n\t"
        "v_cvt_pk_bf16_f32 v230, v188, v189\n\t"
        "v_cvt_pk_bf16_f32 v231, v190, v191\n\t"
        "buffer_store_dwordx4 v[228:231], %[oa], %[rs], %[o1] offen sc1\n\t"
        "s_nop 1\n\t"
        :
        : [w0] "{v[16:47]}"(W[0]), [w1] "{v[48:79]}"(W[1]), [w2] "{v[80:111]}"(W[2]), [w3] "{v[112:143]}"(W[3]), [w4] "{v[144:175]}"(W[4]), [xa] "v"(xa), [oa] "v"(oa), [rs] "s"(rs), [o0] "s"(o0), [o1] "s"(o1), [sc] "s"(sc), [ca] "s"(ca), [fc] "s"(fc), [fw] "s"(fw), [fo] "s"(fo), [xv] "v"(xv), [xs] "s"(xs), [xd] "s"(xd)
        : "s88", "s89", "v176", "v177", "v178", "v179", "v180", "v181", "v182", "v183", "v184", "v185", "v186", "v187", "v188", "v189", "v190", "v191", "v192", "v193", "v194", "v195", "v196", "v197", "v198", "v199", "v200", "v201", "v202", "v203", "v204", "v205", "v206", "v207", "v208", "v209", "v210", "v211", "v212", "v213", "v214", "v215", "v216", "v217", "v218", "v219", "v220", "v221", "v222", "v223", "v224", "v225", "v226", "v227", "v228", "v229", "v230", "v231", "v232", "v233", "v234", "vcc", "scc", "memory");
}

__device__ __forceinline__ void wb(const u32x32 (&W)[5], u32 xa, u32 oa, u32x4 rs, u32 bl, u32 wp, u32 it, u32 xv, u32 xs, u32 xd, u32 ys, u32 yd, u32 ca, u32 fc, u32 fw, u32 fo STP) {
    asm volatile(
        "ds_read_b128 v[192:195], %[xa] offset:0\n\t"
        "ds_read_b128 v[196:199], %[xa] offset:1024\n\t"
        "ds_read_b128 v[200:203], %[xa] offset:2048\n\t"
        "ds_read_b128 v[204:207], %[xa] offset:3072\n\t"
        "ds_read_b128 v[208:211], %[xa] offset:4096\n\t"
        "ds_read_b128 v[212:215], %[xa] offset:5120\n\t"
        "ds_read_b128 v[216:219], %[xa] offset:6144\n\t"
        "ds_read_b128 v[220:223], %[xa] offset:7168\n\t"
        "v_mov_b32 v232, %[ca]\n\t"
        "v_mov_b32 v233, 1\n\t"
        "v_mov_b32 v234, %[fw]\n\t"
        "s_andn2_b32 s91, %[wp], 15\n\t"
        "s_mov_b32 s92, %[bl]\n\t"
        "s_mov_b32 s88, m0\n\t"
        "s_mov_b32 m0, %[xd]\n\t"
        "s_add_u32 s89, %[xs], 0x1000\n\t"
        "buffer_load_dwordx4 %[xv], %[rs], %[xs] offen offset:0 sc1 lds\n\t"
        "buffer_load_dwordx4 %[xv], %[rs], %[xs] offen offset:1024 sc1 lds\n\t"
        "s_mov_b32 m0, s88\n\t"
        "s_setprio 1\n\t"
        "s_waitcnt lgkmcnt(0)\n\t"
        "v_mfma_f32_16x16x32_bf16 v[176:179], v[192:195], v[16:19], 0\n\t"
        "s_lshr_b32 s95, s92, 20\n\t"
        "s_xnor_b32 s95, s95, %[wp]\n\t"
        "v_mfma_f32_16x16x32_bf16 v[180:183], v[208:211], v[16:19], 0\n\t"
        "s_and_b32 s95, s95, 1\n\t"
        "s_lshl_b32 s95, s95, 13\n\t"
        "v_mfma_f32_16x16x32_bf16 v[176:179], v[196:199], v[20:23], v[176:179]\n\t"
        "v_mfma_f32_16x16x32_bf16 v[180:183], v[212:215], v[20:23], v[180:183]\n\t"
        "v_mfma_f32_16x16x32_bf16 v[176:179], v[200:203], v[24:27], v[176:179]\n\t"
        "v_mfma_f32_16x16x32_bf16 v[180:183], v[216:219], v[24:27], v[180:183]\n\t"
        "v_mfma_f32_16x16x32_bf16 v[176:179], v[204:207], v[28:31], v[176:179]\n\t"
        "v_mfma_f32_16x16x32_bf16 v[180:183], v[220:223], v[28:31], v[180:183]\n\t"
        "s_lshl1_add_u32 s93, s92, s91\n\t"
        "s_add_u32 s93, s93, s95\n\t"
        "v_mfma_f32_16x16x32_bf16 v[184:187], v[192:195], v[96:99], 0\n\t"
        "s_add_u32 s92, s92, 0x40000\n\t"
        "v_mfma_f32_16x16x32_bf16 v[188:191], v[208:211], v[96:99], 0\n\t"
        "s_add_u32 s94, s93, 0x10000\n\t"
        "v_mfma_f32_16x16x32_bf16 v[184:187], v[196:199], v[100:103], v[184:187]\n\t"
        "v_cvt_pk_bf16_f32 v224, v176, v177\n\t"
        "v_mfma_f32_16x16x32_bf16 v[188:191], v[212:215], v[100:103], v[188:191]\n\t"
        "v_cvt_pk_bf16_f32 v225, v178, v179\n\t"
        "v_mfma_f32_16x16x32_bf16 v[184:187], v[200:203], v[104:107], v[184:187]\n\t"
        "v_cvt_pk_bf16_f32 v226, v180, v181\n\t"
        "v_mfma_f32_16x16x32_bf16 v[188:191], v[216:219], v[104:107], v[188:191]\n\t"
        "v_cvt_pk_bf16_f32 v227, v182, v183\n\t"
        "v_mfma_f32_16x16x32_bf16 v[184:187], v[204:207], v[108:111], v[184:187]\n\t"
        "buffer_store_dwordx4 v[224:227], %[oa], %[rs], s93 offen sc1\n\t"
        "v_mfma_f32_16x16x32_bf16 v[188:191], v[220:223], v[108:111], v[188:191]\n\t"
        "v_mfma_f32_16x16x32_bf16 v[176:179], v[192:195], v[32:35], 0\n\t"
        "s_lshr_b32 s95, s92, 20\n\t"
        "s_xnor_b32 s95, s95, %[wp]\n\t"
        "v_mfma_f32_16x16x32_bf16 v[180:183], v[208:211], v[32:35], 0\n\t"
        "s_and_b32 s95, s95, 1\n\t"
        "s_lshl_b32 s95, s95, 13\n\t"
        "v_mfma_f32_16x16x32_bf16 v[176:179], v[196:199], v[36:39], v[176:179]\n\t"
        "v_cvt_pk_bf16_f32 v228, v184, v185\n\t"
        "v_mfma_f32_16x16x32_bf16 v[180:183], v[212:215], v[36:39], v[180:183]\n\t"
        "v_cvt_pk_bf16_f32 v229, v186, v187\n\t"
        "v_mfma_f32_16x16x32_bf16 v[176:179], v[200:203], v[40:43], v[176:179]\n\t"
        "v_cvt_pk_bf16_f32 v230, v188, v189\n\t"
        "v_mfma_f32_16x16x32_bf16 v[180:183], v[216:219], v[40:43], v[180:183]\n\t"
        "v_cvt_pk_bf16_f32 v231, v190, v191\n\t"
        "v_mfma_f32_16x16x32_bf16 v[176:179], v[204:207], v[44:47], v[176:179]\n\t"
        "buffer_store_dwordx4 v[228:231], %[oa], %[rs], s94 offen sc1\n\t"
        "v_mfma_f32_16x16x32_bf16 v[180:183], v[220:223], v[44:47], v[180:183]\n\t"
        "s_lshl1_add_u32 s93, s92, s91\n\t"
        "s_add_u32 s93, s93, s95\n\t"
        "v_mfma_f32_16x16x32_bf16 v[184:187], v[192:195], v[112:115], 0\n\t"
        "s_add_u32 s92, s92, 0x40000\n\t"
        "v_mfma_f32_16x16x32_bf16 v[188:191], v[208:211], v[112:115], 0\n\t"
        "s_add_u32 s94, s93, 0x10000\n\t"
        "v_mfma_f32_16x16x32_bf16 v[184:187], v[196:199], v[116:119], v[184:187]\n\t"
        "v_cvt_pk_bf16_f32 v224, v176, v177\n\t"
        "v_mfma_f32_16x16x32_bf16 v[188:191], v[212:215], v[116:119], v[188:191]\n\t"
        "v_cvt_pk_bf16_f32 v225, v178, v179\n\t"
        "v_mfma_f32_16x16x32_bf16 v[184:187], v[200:203], v[120:123], v[184:187]\n\t"
        "v_cvt_pk_bf16_f32 v226, v180, v181\n\t"
        "v_mfma_f32_16x16x32_bf16 v[188:191], v[216:219], v[120:123], v[188:191]\n\t"
        "v_cvt_pk_bf16_f32 v227, v182, v183\n\t"
        "v_mfma_f32_16x16x32_bf16 v[184:187], v[204:207], v[124:127], v[184:187]\n\t"
        "buffer_store_dwordx4 v[224:227], %[oa], %[rs], s93 offen sc1\n\t"
        "v_mfma_f32_16x16x32_bf16 v[188:191], v[220:223], v[124:127], v[188:191]\n\t"
        "s_waitcnt vmcnt(5)\n\t"
        "s_mov_b64 exec, 1\n\t"
        "ds_add_rtn_u32 v233, v232, v233\n\t"
        "s_mov_b64 exec, -1\n\t"
        "v_mfma_f32_16x16x32_bf16 v[176:179], v[192:195], v[48:51], 0\n\t"
        "s_lshr_b32 s95, s92, 20\n\t"
        "s_xnor_b32 s95, s95, %[wp]\n\t"
        "v_mfma_f32_16x16x32_bf16 v[180:183], v[208:211], v[48:51], 0\n\t"
        "s_and_b32 s95, s95, 1\n\t"
        "s_lshl_b32 s95, s95, 13\n\t"
        "v_mfma_f32_16x16x32_bf16 v[176:179], v[196:199], v[52:55], v[176:179]\n\t"
        "v_cvt_pk_bf16_f32 v228, v184, v185\n\t"
        "v_mfma_f32_16x16x32_bf16 v[180:183], v[212:215], v[52:55], v[180:183]\n\t"
        "v_cvt_pk_bf16_f32 v229, v186, v187\n\t"
        "v_mfma_f32_16x16x32_bf16 v[176:179], v[200:203], v[56:59], v[176:179]\n\t"
        "v_cvt_pk_bf16_f32 v230, v188, v189\n\t"
        "v_mfma_f32_16x16x32_bf16 v[180:183], v[216:219], v[56:59], v[180:183]\n\t"
        "v_cvt_pk_bf16_f32 v231, v190, v191\n\t"
        "v_mfma_f32_16x16x32_bf16 v[176:179], v[204:207], v[60:63], v[176:179]\n\t"
        "buffer_store_dwordx4 v[228:231], %[oa], %[rs], s94 offen sc1\n\t"
        "v_mfma_f32_16x16x32_bf16 v[180:183], v[220:223], v[60:63], v[180:183]\n\t"
        "s_lshl1_add_u32 s93, s92, s91\n\t"
        "s_add_u32 s93, s93, s95\n\t"
        "v_mfma_f32_16x16x32_bf16 v[184:187], v[192:195], v[128:131], 0\n\t"
        "s_add_u32 s92, s92, 0x40000\n\t"
        "v_mfma_f32_16x16x32_bf16 v[188:191], v[208:211], v[128:131], 0\n\t"
        "s_add_u32 s94, s93, 0x10000\n\t"
        "v_mfma_f32_16x16x32_bf16 v[184:187], v[196:199], v[132:135], v[184:187]\n\t"
        "v_cvt_pk_bf16_f32 v224, v176, v177\n\t"
        "v_mfma_f32_16x16x32_bf16 v[188:191], v[212:215], v[132:135], v[188:191]\n\t"
        "v_cvt_pk_bf16_f32 v225, v178, v179\n\t"
        "v_mfma_f32_16x16x32_bf16 v[184:187], v[200:203], v[136:139], v[184:187]\n\t"
        "v_cvt_pk_bf16_f32 v226, v180, v181\n\t"
        "v_mfma_f32_16x16x32_bf16 v[188:191], v[216:219], v[136:139], v[188:191]\n\t"
        "v_cvt_pk_bf16_f32 v227, v182, v183\n\t"
        "v_mfma_f32_16x16x32_bf16 v[184:187], v[204:207], v[140:143], v[184:187]\n\t"
        "buffer_store_dwordx4 v[224:227], %[oa], %[rs], s93 offen sc1\n\t"
        "v_mfma_f32_16x16x32_bf16 v[188:191], v[220:223], v[140:143], v[188:191]\n\t"
        "s_waitcnt lgkmcnt(0)\n\t"
        "s_mov_b64 exec, 1\n\t"
        "v_cmpx_eq_u32 vcc, %[fc], v233\n\t"
        "buffer_store_dword v234, off, %[rs], %[fo] sc1\n\t"
        "s_mov_b64 exec, -1\n\t"
        "s_cmp_le_u32 %[it], %[nowv]\n\t"
        "s_cbranch_scc1 Lp%=\n\t"
        "s_setprio 0\n\t"
        "Lp%=:\n\t"
        "s_mov_b32 s88, m0\n\t"
        "s_mov_b32 m0, %[yd]\n\t"
        "s_add_u32 s89, %[ys], 0x1000\n\t"
        "buffer_load_dwordx4 %[xv], %[rs], %[ys] offen offset:0 sc1 lds\n\t"
        "buffer_load_dwordx4 %[xv], %[rs], %[ys] offen offset:1024 sc1 lds\n\t"
        "buffer_load_dwordx4 %[xv], %[rs], %[ys] offen offset:2048 sc1 lds\n\t"
        "buffer_load_dwordx4 %[xv], %[rs], %[ys] offen offset:3072 sc1 lds\n\t"
        "s_add_u32 m0, m0, 0x1000\n\t"
        "s_nop 0\n\t"
        "buffer_load_dwordx4 %[xv], %[rs], s89 offen offset:0 sc1 lds\n\t"
        "buffer_load_dwordx4 %[xv], %[rs], s89 offen offset:1024 sc1 lds\n\t"
        "buffer_load_dwordx4 %[xv], %[rs], s89 offen offset:2048 sc1 lds\n\t"
        "buffer_load_dwordx4 %[xv], %[rs], s89 offen offset:3072 sc1 lds\n\t"
        "s_mov_b32 m0, s88\n\t"
        "v_mfma_f32_16x16x32_bf16 v[176:179], v[192:195], v[64:67], 0\n\t"
        "s_lshr_b32 s95, s92, 20\n\t"
        "s_xnor_b32 s95, s95, %[wp]\n\t"
        "v_mfma_f32_16x16x32_bf16 v[180:183], v[208:211], v[64:67], 0\n\t"
        "s_and_b32 s95, s95, 1\n\t"
        "s_lshl_b32 s95, s95, 13\n\t"
        "v_mfma_f32_16x16x32_bf16 v[176:179], v[196:199], v[68:71], v[176:179]\n\t"
        "v_cvt_pk_bf16_f32 v228, v184, v185\n\t"
        "v_mfma_f32_16x16x32_bf16 v[180:183], v[212:215], v[68:71], v[180:183]\n\t"
        "v_cvt_pk_bf16_f32 v229, v186, v187\n\t"
        "v_mfma_f32_16x16x32_bf16 v[176:179], v[200:203], v[72:75], v[176:179]\n\t"
        "v_cvt_pk_bf16_f32 v230, v188, v189\n\t"
        "v_mfma_f32_16x16x32_bf16 v[180:183], v[216:219], v[72:75], v[180:183]\n\t"
        "v_cvt_pk_bf16_f32 v231, v190, v191\n\t"
        "v_mfma_f32_16x16x32_bf16 v[176:179], v[204:207], v[76:79], v[176:179]\n\t"
        "buffer_store_dwordx4 v[228:231], %[oa], %[rs], s94 offen sc1\n\t"
        "v_mfma_f32_16x16x32_bf16 v[180:183], v[220:223], v[76:79], v[180:183]\n\t"
        "s_lshl1_add_u32 s93, s92, s91\n\t"
        "s_add_u32 s93, s93, s95\n\t"
        "v_mfma_f32_16x16x32_bf16 v[184:187], v[192:195], v[144:147], 0\n\t"
        "s_add_u32 s92, s92, 0x40000\n\t"
        "v_mfma_f32_16x16x32_bf16 v[188:191], v[208:211], v[144:147], 0\n\t"
        "s_add_u32 s94, s93, 0x10000\n\t"
        "v_mfma_f32_16x16x32_bf16 v[184:187], v[196:199], v[148:151], v[184:187]\n\t"
        "v_cvt_pk_bf16_f32 v224, v176, v177\n\t"
        "v_mfma_f32_16x16x32_bf16 v[188:191], v[212:215], v[148:151], v[188:191]\n\t"
        "v_cvt_pk_bf16_f32 v225, v178, v179\n\t"
        "v_mfma_f32_16x16x32_bf16 v[184:187], v[200:203], v[152:155], v[184:187]\n\t"
        "v_cvt_pk_bf16_f32 v226, v180, v181\n\t"
        "v_mfma_f32_16x16x32_bf16 v[188:191], v[216:219], v[152:155], v[188:191]\n\t"
        "v_cvt_pk_bf16_f32 v227, v182, v183\n\t"
        "v_mfma_f32_16x16x32_bf16 v[184:187], v[204:207], v[156:159], v[184:187]\n\t"
        "buffer_store_dwordx4 v[224:227], %[oa], %[rs], s93 offen sc1\n\t"
        "v_mfma_f32_16x16x32_bf16 v[188:191], v[220:223], v[156:159], v[188:191]\n\t"
        "v_mfma_f32_16x16x32_bf16 v[176:179], v[192:195], v[80:83], 0\n\t"
        "s_lshr_b32 s95, s92, 20\n\t"
        "s_xnor_b32 s95, s95, %[wp]\n\t"
        "v_mfma_f32_16x16x32_bf16 v[180:183], v[208:211], v[80:83], 0\n\t"
        "s_and_b32 s95, s95, 1\n\t"
        "s_lshl_b32 s95, s95, 13\n\t"
        "v_mfma_f32_16x16x32_bf16 v[176:179], v[196:199], v[84:87], v[176:179]\n\t"
        "v_cvt_pk_bf16_f32 v228, v184, v185\n\t"
        "v_mfma_f32_16x16x32_bf16 v[180:183], v[212:215], v[84:87], v[180:183]\n\t"
        "v_cvt_pk_bf16_f32 v229, v186, v187\n\t"
        "v_mfma_f32_16x16x32_bf16 v[176:179], v[200:203], v[88:91], v[176:179]\n\t"
        "v_cvt_pk_bf16_f32 v230, v188, v189\n\t"
        "v_mfma_f32_16x16x32_bf16 v[180:183], v[216:219], v[88:91], v[180:183]\n\t"
        "v_cvt_pk_bf16_f32 v231, v190, v191\n\t"
        "v_mfma_f32_16x16x32_bf16 v[176:179], v[204:207], v[92:95], v[176:179]\n\t"
        "buffer_store_dwordx4 v[228:231], %[oa], %[rs], s94 offen sc1\n\t"
        "v_mfma_f32_16x16x32_bf16 v[180:183], v[220:223], v[92:95], v[180:183]\n\t"
        "s_lshl1_add_u32 s93, s92, s91\n\t"
        "s_add_u32 s93, s93, s95\n\t"
        "v_mfma_f32_16x16x32_bf16 v[184:187], v[192:195], v[160:163], 0\n\t"
        "s_add_u32 s92, s92, 0x40000\n\t"
        "v_mfma_f32_16x16x32_bf16 v[188:191], v[208:211], v[160:163], 0\n\t"
        "s_add_u32 s94, s93, 0x10000\n\t"
        "v_mfma_f32_16x16x32_bf16 v[184:187], v[196:199], v[164:167], v[184:187]\n\t"
        "v_cvt_pk_bf16_f32 v224, v176, v177\n\t"
        "v_mfma_f32_16x16x32_bf16 v[188:191], v[212:215], v[164:167], v[188:191]\n\t"
        "v_cvt_pk_bf16_f32 v225, v178, v179\n\t"
        "v_mfma_f32_16x16x32_bf16 v[184:187], v[200:203], v[168:171], v[184:187]\n\t"
        "v_cvt_pk_bf16_f32 v226, v180, v181\n\t"
        "v_mfma_f32_16x16x32_bf16 v[188:191], v[216:219], v[168:171], v[188:191]\n\t"
        "v_cvt_pk_bf16_f32 v227, v182, v183\n\t"
        "v_mfma_f32_16x16x32_bf16 v[184:187], v[204:207], v[172:175], v[184:187]\n\t"
        "buffer_store_dwordx4 v[224:227], %[oa], %[rs], s93 offen sc1\n\t"
        "v_mfma_f32_16x16x32_bf16 v[188:191], v[220:223], v[172:175], v[188:191]\n\t"
        "s_and_b32 s95, %[it], 31\n\t"
        "s_cmp_le_u32 s95, %[nowv]\n\t"
        "s_cbranch_scc1 Lnb%=\n\t"
        "s_waitcnt vmcnt(17)\n\t"
        "s_barrier\n\t"
        "Lnb%=:\n\t"
        "s_setprio 0\n\t"
        "s_nop 3\n\t"
        "v_cvt_pk_bf16_f32 v228, v184, v185\n\t"
        "v_cvt_pk_bf16_f32 v229, v186, v187\n\t"
        "v_cvt_pk_bf16_f32 v230, v188, v189\n\t"
        "v_cvt_pk_bf16_f32 v231, v190, v191\n\t"
        "buffer_store_dwordx4 v[228:231], %[oa], %[rs], s94 offen sc1\n\t"
        "s_nop 1\n\t"
        :
        : [w0] "{v[16:47]}"(W[0]), [w1] "{v[48:79]}"(W[1]), [w2] "{v[80:111]}"(W[2]), [w3] "{v[112:143]}"(W[3]), [w4] "{v[144:175]}"(W[4]), [xa] "v"(xa), [oa] "v"(oa), [rs] "s"(rs), [bl] "s"(bl), [wp] "s"(wp), [it] "s"(it), [nowv] "n"(NOWV), [ca] "s"(ca), [fc] "s"(fc), [fw] "s"(fw), [fo] "s"(fo), [xv] "v"(xv), [xs] "s"(xs), [xd] "s"(xd), [ys] "s"(ys), [yd] "s"(yd)
        : "s88", "s89", "s91", "s92", "s93", "s94", "s95", "v176", "v177", "v178", "v179", "v180", "v181", "v182", "v183", "v184", "v185", "v186", "v187", "v188", "v189", "v190", "v191", "v192", "v193", "v194", "v195", "v196", "v197", "v198", "v199", "v200", "v201", "v202", "v203", "v204", "v205", "v206", "v207", "v208", "v209", "v210", "v211", "v212", "v213", "v214", "v215", "v216", "v217", "v218", "v219", "v220", "v221", "v222", "v223", "v224", "v225", "v226", "v227", "v228", "v229", "v230", "v231", "v232", "v233", "v234", "vcc", "scc", "memory");
}

#define TP0 198
#define TP1 16
#define TP2 184
#define TP3 30
#define TP4 0
#define TP5 130
#define TP6 112
#define TP7 208
#define TP8 4
#define TP9 24
#define TP10 4
#define TP11 2
#define TP12 2
#define TP13 150
#define TP14 70
#define TP15 4
#define WAHEAD 3
#define FILLG 4
#define WFIRST 2
#define EARLY_N 35
#define BUMP_N 3
#define BUMP_WIN 0xffu
#define BUMP_S0 8
#define BUMP_MAX 3
#define TV(idx) ((u32)(TP##idx) * 8u)
#define TLOAD() do { } while (0)

__device__ __forceinline__ void nap(u32 n) {
    u32 hi = n >> 6, lo = (n >> 4) & 3u;
    asm volatile("s_cmp_eq_u32 %0, 0\n\ts_cbranch_scc1 Ln%=\nLm%=:\n\ts_sleep 1\n\ts_sub_u32 %0, %0, 1\n\ts_cmp_lg_u32 %0, 0\n\ts_cbranch_scc1 Lm%=\nLn%=:\n\t"
                 "s_cmp_eq_u32 %1, 0\n\ts_cbranch_scc1 Lp%=\nLo%=:\n\ts_sub_u32 %1, %1, 1\n\ts_cmp_lg_u32 %1, 0\n\ts_cbranch_scc1 Lo%=\nLp%=:"
                 : "+s"(hi), "+s"(lo) :: "scc", "memory");
}
typedef __attribute__((address_space(3))) u32* lds_u32p;
typedef __attribute__((address_space(3))) volatile u32* lds_vu32p;
typedef __attribute__((address_space(3))) u16* lds_u16p;
typedef __attribute__((address_space(3))) u32x2* lds_u32x2p;
typedef __attribute__((address_space(3))) u32x4* lds_u32x4p;
__device__ __forceinline__ u32 rfl(u32 x) { return __builtin_amdgcn_readfirstlane(x); }
__device__ __forceinline__ bool has_sent(u32x4 v) { return (v[0] == SENT) | (v[1] == SENT) | (v[2] == SENT) | (v[3] == SENT); }
__device__ __forceinline__ u32 now32() { return (u32)__builtin_amdgcn_s_memtime(); }
__device__ __forceinline__ u32 step_tag(int s) { return (u32)(s + 1) * 0x10001u; }

template <int S0 = 0>
__device__ __forceinline__ u32 unless_neg(u32 v, int s) {
    u32 r;
    asm("s_cmp_lt_i32 %2, %3\n\ts_cselect_b32 %0, 0, %1" : "=s"(r) : "s"(v), "s"(s), "n"(S0) : "scc");
    return r;
}
template <int S0 = 0>
__device__ __forceinline__ u32x4 live(u32x4 srd, int s) { srd[2] = unless_neg<S0>(srd[2], s); return srd; }
__device__ __forceinline__ float shx(float v, int m, int lane) { return __int_as_float(__builtin_amdgcn_ds_bpermute((lane ^ m) << 2, __float_as_int(v))); }
__device__ __forceinline__ void mem0_image(const u16* __restrict__ mem0, char* smem, const int tid) {
    for (int c = tid; c < 640; c += 512) {
        u32x4 v = *(const u32x4*)(mem0 + (c & 7) * D_ + (c >> 3) * 8);
#pragma unroll
        for (int i = 0; i < 4; ++i) v[i] = v[i] == SENT ? ~0u : v[i];
        ((u32x4*)smem)[c + (c >= 576 ? 64 : 0)] = v;
    }
}
__device__ __forceinline__ u32 once_more(u32& left, u32 lbase, u32* errw) {
    if (__builtin_expect(left != 0, 1)) { --left; return 2u; }
    atomicOr(errw, 1u);
    const int lane = lane_id();
    for (int k = 0; k < 11; ++k) *(lds_u32x4p)(lbase + (u32)(k * 1024 + lane * 16)) = (u32x4){0u, 0u, 0u, 0u};
    asm volatile("s_waitcnt lgkmcnt(0)" ::: "memory");
    return 0u;
}
constexpr u32 TS_MISS = 0x10000u;
__device__ __forceinline__ u32 read_ts(rsrc_t rs, u32 off, int s, u32& m) {
    const int lane = lane_id();
    const u32 vo = lane < 8 ? (u32)lane * 128u : OOB, base = (u32)__builtin_amdgcn_s_memrealtime() * 24u - 0x100000u;
    u32x4 t;
#pragma unroll 1
    for (u32 spins = 1;; ++spins) {
        CFENCE();
        t = ldx4(rs, vo, off);
        m = (u32)__ballot(t[3] == (u32)s + 1u);
        if (((m | (m >> 4)) & 15u) == 15u) break;
        m = 0;
        if (spins >= 0x4000u) break;
        __builtin_amdgcn_s_sleep(4);
    }
    u32 v = t[3] == (u32)s + 1u ? t[1] - base : 0u;
    v = max(v, (u32)__builtin_amdgcn_update_dpp(0u, v, 0xb1, 0xf, 0xf, false));
    v = max(v, (u32)__builtin_amdgcn_update_dpp(0u, v, 0x4e, 0xf, 0xf, false));
    v = max(v, (u32)__builtin_amdgcn_update_dpp(0u, v, 0x141, 0xf, 0xf, false));
    return to_sgpr(v + base);
}
__device__ __forceinline__ u32 read_ts_late(rsrc_t rs, u32x4 srd, u32 off, int s, u32& m, u32 gap = 1024) {
    u32 t;
    ts_look(t, m, srd, off, (u32)s + 1u);
    if (m) return t;
    nap(gap);
    return read_ts(rs, off, s, m);
}
__device__ __forceinline__ void until(u32 t) { for (int q = 0; q < 256 && (int)(t - (u32)__builtin_amdgcn_s_memrealtime() * 24u) > 0; ++q) __builtin_amdgcn_s_sleep(1); }
__device__ __forceinline__ u32 umin3(u32 a, u32 b, u32 c) { return min(min(a, b), c); }
__device__ __forceinline__ u32 dist(u32 d, u32x4 v, u32 k) { return umin3(umin3(d, v[0] ^ k, v[1] ^ k), v[2] ^ k, v[3] ^ k); }
#define COUNT(i) do { } while (0)
#define COUNT0() do { } while (0)
#define COUNT1(i) do { } while (0)
__device__ __forceinline__ void touch(u32x4 srd, u32 wp, int s, u32 lds) {
    u32 sv;
    const u32 off = slot_off(wp, s) + SL_TS + 2048u + ((~((wp >> 3) ^ ((u32)s >> 5)) & 1u) << 18);
    asm volatile("s_mov_b32 %0, m0\n\ts_mov_b32 m0, %3\n\ts_nop 3\n\tbuffer_load_dword off, %1, %2 lds\n\ts_mov_b32 m0, %0" : "=&s"(sv) : "s"(srd), "s"(off), "s"(lds) : "memory");
}
__device__ __forceinline__ void track_rate(u32 c0, u32 r0, u32& pc, u32& pr, u32& kr) {
    const u32 dr = r0 - pr;
    if (dr >= 6000u) {
        if (dr < 40000u) kr = to_sgpr(min(max((u32)(4096.f * (float)(c0 - pc) * __builtin_amdgcn_rcpf((float)dr) + 0.5f), 2560u), 4352u));
        pc = c0; pr = r0;
    }
}

#define LANES() const int lane = lane_id(), n = lane & 15, g = lane >> 4; (void)n; (void)g
constexpr u32 L_Q = 12288, L_K = 18432, L_E = 24576, L_DR = 25088, L_MSK = 25600, L_KQ = 32768;
constexpr float LIM_X = 32.f, LIM_D = 1.0995116e12f;
__device__ __forceinline__ u32 kq_buf(int sg) { return L_KQ + (u32)(sg % 3) * 16384u; }

__device__ __forceinline__ u32 p_pos(int row, int g, int dg) { return (u32)((row >> 1) * 64 + (g & 1) * 32 + (row & 1) * 16 + dg * 8 + (g >> 1) * 4); }
__device__ __forceinline__ void s_slow(const int h, const int s, rsrc_t rs, u32 so, u32 lbase) {
    const int lane = lane_id(), row = lane & 7, j = lane >> 3;
    const float ce = 0.08838834764831845f * 1.4426950408889634f;
    float sc[5] = {0.f, 0.f, 0.f, 0.f, 0.f};
    const u32 kx = lbase + kq_buf(s) + (u32)((j >> 2) * 4096 + (j & 3) * 64);
#pragma unroll 1
    for (int e = 0; e < DH_; ++e) {
        const u32 qa = lbase + L_Q + (u32)((e >> 4) * 768 + (row >> 2) * 192 + (e & 15) * 8 + (row & 3) * 2);
        const float q = bf2f(*(lds_u16p)qa);
        const u32 ka = kx + (u32)((e >> 5) * 1024 + ((e >> 2) & 3) * 256 + ((e >> 4) & 1) * 8 + (e & 3) * 2);
#pragma unroll
        for (int i = 0; i < 4; ++i) sc[i] += q * bf2f(*(lds_u16p)(ka + i * 16));
        sc[4] += q * bf2f(*(lds_u16p)(lbase + L_K + (u32)((e >> 4) * 768 + (j >> 1) * 192 + (e & 15) * 8 + (j & 1) * 2)));
    }
    sc[4] *= ce;
    const u32x2 ev = *(lds_u32x2p)(lbase + L_E + (u32)((j >> 1) * 128 + row * 16 + (j & 1) * 8));
    float se = (bf2f((u16)ev[0]) + bf2f((u16)(ev[0] >> 16))) + (bf2f((u16)ev[1]) + bf2f((u16)(ev[1] >> 16)));
    float mx = fmaxf(fmaxf(fmaxf(sc[0], sc[1]), fmaxf(sc[2], sc[3])), sc[4]);
#pragma unroll
    for (int o = 8; o < 64; o <<= 1) { se += shx(se, o, lane); mx = fmaxf(mx, shx(mx, o, lane)); }
    const bool rew = ev[0] == ~0u || ev[1] == ~0u;
    u64 rw = __ballot(rew);
    rw |= rw >> 32; rw |= rw >> 16; rw |= rw >> 8;
    const bool again = (rw >> row) & 1u;
    float al = exp2f(-mx);
    if (again) {
        const u32x2 en = {pack2(exp2f(sc[0] - mx), exp2f(sc[1] - mx)), pack2(exp2f(sc[2] - mx), exp2f(sc[3] - mx))};
        se = (bf2f((u16)en[0]) + bf2f((u16)(en[0] >> 16))) + (bf2f((u16)en[1]) + bf2f((u16)(en[1] >> 16)));
        al = 1.f;
    }
    float sn = se;
#pragma unroll
    for (int o = 8; o < 64; o <<= 1) sn += shx(sn, o, lane);
    if (again) se = sn;
    const float em = exp2f(sc[4] - mx);
    float sm = em;
#pragma unroll
    for (int o = 8; o < 64; o <<= 1) sm += shx(sm, o, lane);
    const float rd = 1.f / (sm + al * se);
    if (rw & 255u) {
#pragma unroll 1
        for (u32 spins = 0; spins < SPIN_CAP && !__all(*(lds_vu32p)(lbase + L_DR + (u32)(lane & 1) * 4) == step_tag(s)); ++spins) __builtin_amdgcn_s_sleep(1);
        const u32x2 en = {pack2(exp2f(sc[0] - mx), exp2f(sc[1] - mx)), pack2(exp2f(sc[2] - mx), exp2f(sc[3] - mx))};
        const u32 eo = again ? (u32)((h >> 3) * 32768 + ((h >> 1) & 3) * 1024 + (j >> 1) * 256 + (h & 1) * 128 + row * 16 + (j & 1) * 8) : OOB;
#pragma unroll
        for (int c = 0; c < NCP; ++c) stx2(rs, eo, en, so + cpy(c));
    }
    DRAIN();
    const u32 pm = pack2(em * rd, shx(em * rd, 8, lane));
    const int g = j >> 1;
    u32 dg = pack2(row == 2 * g ? al * rd : 0.f, row == 2 * g + 1 ? al * rd : 0.f);
    if (again && (row >> 1) == (g ^ 1)) dg = MARK;
    const int gj = h >> 2;
    const u32 po = SL_P + (u32)((gj >> 2) * 32768 + (gj & 1) * 2048 + ((gj >> 1) & 1) * 1024 + (h & 3) * 256) + p_pos(row, g, (j ^ ~h) & 1);
    const bool done = ldx1(rs, po, so) != SENT;
#pragma unroll
    for (int c = 0; c < NCP; ++c) stx1(rs, done ? OOB : po, (j & 1) ? dg : pm, so + cpy(c));
}

__device__ __forceinline__ void s_role(const int wave, const int h, const int xg, const u64 tag, const u16* __restrict__ mem0, const u32x32 (&W)[5],
                                       char* __restrict__ ws, char* smem) {
    const int lane = lane_id(), tid = wave * 64 + lane, n = lane & 15, g = lane >> 4;
    u32* errw = (u32*)(ws + OFF_ERR);
    const rsrc_t rs = mk_rsrc(ws);
    const u32x4 srd = mk_srd(ws);
    const u32 lbase = (u32)(uintptr_t)(__attribute__((address_space(3))) char*)smem;
    TLOAD();
    int wslot = 0; const int wid = 128 + h;
    WSTAMP(2);
    mem0_image(mem0, smem, tid);
    if (tid < 448) *(u32x4*)(smem + L_K + tid * 16) = (u32x4){0u, 0u, 0u, 0u};
    const u32 wp = wait_init(rs, tag, 1);
    if (wave == 2) XSTAMP(wid, 224);
    const u32 sidx = (n & 2) ? 0xEEEE : 0x4444;
    const u32 va = lbase + (u32)(g * 256 + (n & 2) * 64 + (n >> 2) * 32 + (n & 1) * 16);
    const u32 ow = lbase + (u32)((wave & 3) * 768 + n * 8) + (wave >= 4 ? L_Q + (u32)((g >> 1) * 192 + (g & 1) * 4) : L_K + (u32)g * 192);
    u32 rnd = 0;
    const float ce = 0.08838834764831845f * 1.4426950408889634f;
    STAMPS();
    COUNT0();
#define BAR0() asm volatile("s_waitcnt vmcnt(0) lgkmcnt(0)\n\ts_barrier" ::: "memory")
#define Q_LAST() do { u32 redo; do s_l(rnd, redo, lbase); while (__builtin_expect(redo, 0)); } while (0)

    if ((wave >> 1) == 2) {
        LANES();
        const int kt = wave & 1;
        const u32 kxa = lbase + (u32)(kt * 4096 + lane * 16), qtr = lbase + L_Q + (u32)((lane & 1) * 192 + (lane >> 2) * 8);
        const u32 ep = (u32)((n & 7) * 16 + (g & 1) * 8 + (n >> 3) * 4);
        const u32 eo = (u32)((h >> 3) * 32768 + ((h >> 1) & 3) * 1024 + (2 * kt + (g >> 1)) * 256 + (h & 1) * 128) + ep;
        const u32 ew = lbase + L_E + (u32)((2 * kt + (g >> 1)) * 128) + ep;
        const u32 ts = slot_v(wp, lane) + put_off(wp, 2u, lane), tk = kq_buf(lane);
#pragma unroll 1
        for (int s = -1; s + 1 < NSEG; ++s) {
            BAR0();
            s_e(W, rnd, va, sidx, ow, lbase, kxa + tab(tk, s & 63), qtr, (u32)kt * 768u, eo, ew, LIM_X, live(srd, s), tab(ts, s & 63) STA);
            DRAIN();
            *(lds_u32p)(lbase + L_DR + (u32)kt * 4) = step_tag(s);
            FLUSH();
        }
        BAR0();
        Q_LAST();
    } else if (wave == 6) {
        LANES();
        const int gj = h >> 2;
        const u32 po = SL_P + (u32)((gj >> 2) * 32768 + (gj & 1) * 2048 + ((gj >> 1) & 1) * 1024 + (h & 3) * 256) + p_pos(n & 7, g, ((n >> 3) ^ ~h) & 1);
        const u32 er = lbase + L_E + (u32)(g * 128 + (n & 7) * 16);
        const u32 qtr = lbase + L_Q + (u32)((lane & 1) * 192 + (lane >> 2) * 8), ktr = lbase + L_K + (u32)((lane & 3) * 192 + (lane >> 2) * 8);
        const u32x4 ones = {0x3f803f80u, 0x3f803f80u, 0x3f803f80u, 0x3f803f80u};
        const u32 ts = slot_v(wp, lane) + put_off(wp, 2u, lane);
#pragma unroll 1
        for (int s = -1; s + 1 < NSEG; ++s) {
            const u32 so = tab(ts, s & 63);
            u32 bad;
            BAR0();
            s_f(W, rnd, va, sidx, ow, lbase, bad, ones, qtr, ktr, er, po, n < 8 ? 1.f : 0.f, n == 8 + 2 * g ? 1.f : 0.f, n == 9 + 2 * g ? 1.f : 0.f, live(srd, s), so, ce, LIM_D STA);
            if (__builtin_expect(bad && s >= 0, 0)) s_slow(h, s, rs, so, lbase);
            FLUSH();
        }
        BAR0();
        Q_LAST();
    } else if (wave == 7) {
        LANES();
        const u32 vm = (u32)lane * 16, cp = (u32)xg ^ (wp >> 1);
        u32 c0, r0, kr = 3950;
        calib(c0, r0);
        const u32 tc = slot_v(wp ^ 8u, lane) + copy_off(cp, lane);
        u32 pc = c0, pr = r0, tg = r0, sc = 0, left = SPIN_CAP;
        asm volatile("s_waitcnt lgkmcnt(0)" ::: "memory");
#pragma unroll 1
        for (int s = -1; s + 1 < NSEG; ++s) {
            const u32 so = sc;
            sc = tab(tc, s & 63);
            u32 poll = (0x12u >> (min(s + 1, 2) * 2)) & 3u;
            if (s == 0) BAR0();
            const u32 rn0 = rnd;
            u32 t1 = 0, tm = (u32)s + 1u;
            for (;;) {
                s_m(W, rnd, va, sidx, ow, lbase, left, c0, r0, t1, tm, tg, kr, vm, so, lbase + (poll & 2u) * 40960u, live(srd, s), poll, lane < 8 ? vm * 8u : OOB, sc + SL_TS STA);
                if (__builtin_expect(left != ~0u, 1)) break;
                left = 0;
                once_more(left, lbase, errw);
                poll = 0;
            }
            if (rnd - rn0 > 1u) COUNT(0);
            tg = t1;
            if (__builtin_expect(!tm && s >= 0, 0)) tg = read_ts(rs, sc + SL_TS, s, tm);
            track_rate(c0, r0, pc, pr, kr);
            FLUSH();
        }
        const u32 so = sc;
        for (u32 poll = 1;;) {
            if (poll) gather(tg, kr, vm, so, lbase, srd);
            if (poll == 1) LBAR();
            DRAIN();
            u32 redo;
            s_l(rnd, redo, lbase);
            if (__builtin_expect(!redo, 1)) break;
            poll = once_more(left, lbase, errw);
        }
    } else if (wave < 2) {
        LANES();
        const int nt = wave & 1;
        const u32 ktr = lbase + L_K + (u32)((lane & 3) * 192 + (lane >> 2) * 8);
        f32x4* msk = (f32x4*)(smem + L_MSK) + nt * 128 + lane;
#pragma unroll
        for (int q = 0; q < 2; ++q) {
            f32x4 m;
#pragma unroll
            for (int i = 0; i < 4; ++i) m[i] = q * 16 + 4 * g + i <= nt * 16 + n ? 0.f : -__builtin_huge_valf();
            msk[q * 64] = m;
        }
        const u32 vl = lbase + (u32)lane * 16;
        const u32 o0 = (u32)OFF_PX + (u32)((h * 2 + nt) * 1024 + lane * 16);
        const u32 o1 = (u32)OFF_PXM + (u32)(((h >> 2) * 2 + nt) * 1024 + ((h & 3) * 16 + n) * 16 + g * 4);
        u32x4 p0 = {0u, 0u, 0u, 0u};
        u32 p1 = 0;
#pragma unroll 1
        for (int s = -1; s < NSEG; ++s) {
            const u32 off = s > 0 ? 0u : OOB;
            BAR0();
            stx4(rs, (o0 + (u32)s * 65536 - 65536) | off, p0);
            stx1(rs, (o1 + (u32)s * 16384 - 16384) | off, p1);
            if (s + 1 < NSEG) s_k0(W, rnd, va, sidx, ow, lbase STA);
            else {
                s_k2(W, rnd, va, sidx, ow, lbase);
                DRAIN();
                stx1(rs, lane == 0 ? (u32)(OFF_FP + (h >> 2) * 128 + ((h & 3) * 2 + nt) * 4) : OOB, 1u);
            }
            px_step(p0, p1, vl + kq_buf(s + 3), vl + kq_buf(s + 3) + 8192 + (u32)nt * 4096, vl + L_MSK + (u32)nt * 2048, ktr, ce);
            STAMP(3);
            FLUSH();
        }
        stx4(rs, o0 + (NSEG - 1) * 65536, p0);
        stx1(rs, o1 + (NSEG - 1) * 16384, p1);
        DRAIN();
        stx1(rs, lane == 0 ? (u32)(OFF_FZ + ((h >> 2) * 2 + nt) * 128 + (h & 3) * 4) : OOB, 1u);
    } else if (wave == 2) {
        LANES();
        u32 cap = SPIN_CAP;
        u32 have = 0;
        auto seen = [&](u32x4 f) {
            u32 a = min(2 * rfl(f[0]), 2 * rfl(f[1]) + 1);
            if (h < 16) a = min(a, max(rfl(f[2]), 4u) - 1u);
            have = max(have, a);
        };
        const u32 tt = slot_v(wp ^ 8u, lane) + SL_TS + 2048u;
        *(lds_u32p)(lbase + 11520u + (u32)lane * 4) = 0u;
#pragma unroll 1
        for (int s = -1; s + 1 < NSEG; ++s) {
            const u32 pfo = far_off(wp ^ 1u, NEAR_KX + (u32)((s + 1) * H_ + h) * 8192);
            BAR0();
            seen(*(lds_u32x4p)(lbase + 11520u));
            s_k0(W, rnd, va, sidx, ow, lbase STA);
            u32 spins = 0;
#pragma unroll 1
            for (; have <= (u32)(s + 1) && spins < cap; ++spins) {
                CFENCE();
                seen(ldx4(rs, 0u, (u32)(OFF_FA + h * 128)));
                if (spins) __builtin_amdgcn_s_sleep(2);
            }
            if (spins >= cap) { atomicOr(errw, 8u); cap = 1; }
            CFENCE();
            if (s < 0) XSTAMP(wid, 226);
            s_fetch((u32)lane * 16, lane == s + 1 ? tt : OOB, srd, pfo, lbase + kq_buf(s + 1), lbase + 11264u);
            s_fetch((u32)lane * 16, have <= (u32)(s + 4) && lane < 4 ? (u32)(OFF_FA + h * 128 + lane * 4) : OOB, srd, pfo - 2u * (NEAR_KX - NEAR_QX),
                    lbase + kq_buf(s + 1) + 8192, lbase + 11520u);
            FLUSH();
        }
        BAR0();
        s_k2(W, rnd, va, sidx, ow, lbase);
    } else {
#pragma unroll 1
        for (int s = -1; s + 1 < NSEG; ++s) {
            BAR0();
            s_k0(W, rnd, va, sidx, ow, lbase STA);
            FLUSH();
        }
        BAR0();
        s_k2(W, rnd, va, sidx, ow, lbase);
    }
    COUNT1(0);
}

__device__ __forceinline__ void v_role(const int wave, const int h, const int xg, const u64 tag, const u16* __restrict__ mem0, const u32x32 (&W)[5],
                                       char* __restrict__ ws, char* smem) {
    const int lane = lane_id(), tid = wave * 64 + lane, n = lane & 15, g = lane >> 4;
    u32* errw = (u32*)(ws + OFF_ERR);
    const rsrc_t rs = mk_rsrc(ws);
    const u32x4 srd = mk_srd(ws);
    const u32 lbase = (u32)(uintptr_t)(__attribute__((address_space(3))) char*)smem;
    TLOAD();
    int wslot = 0; const int wid = 160 + h;
    WSTAMP(2);
    mem0_image(mem0, smem, tid);
    *(lds_u32p)(lbase + 14336) = 0u;
    const u32 wp = wait_init(rs, tag, 1);
    const u32 sidx = (n & (wave < 4 ? 2 : 8)) ? 0xEEEE : 0x4444;
    asm volatile("s_waitcnt vmcnt(0) lgkmcnt(0)" ::: "memory");
    STAMPS();
    COUNT0();
    u32 rnd = 0;
    if (wave < 4) {
        const u32 va = lbase + (u32)(g * 256 + (n & 2) * 64 + (n >> 2) * 32 + (n & 1) * 16), vw = lbase + 12288 + (u32)((wave * 16 + n) * 16 + g * 4);
        if (wave) {
#pragma unroll 1
            for (int s = 0; s < NSEG; ++s) {
                v_a0(W, rnd, va, sidx, vw, lbase STA);
                FLUSH();
                if (wave == 1 && s + 1 < NSEG) touch(srd, wp, s + 1, lbase + 15360u);
            }
        } else {
            const u32 tc = slot_v(wp ^ 8u, lane) + copy_off((u32)xg ^ (wp >> 1), lane);
            u32 c0, r0, kr = 3950;
            calib(c0, r0);
            u32 pc = c0, pr = r0, tg = r0, sc = 0, left = SPIN_CAP;
#pragma unroll 1
            for (int s = 0; s < NSEG; ++s) {
                const u32 so = sc, rn0 = rnd;
                sc = tab(tc, s);
                for (u32 poll = 2u - (((u32)s + 63u) >> 6);; poll = 0) {
                    v_a1(W, rnd, va, sidx, vw, lbase, left, c0, r0, tg + TV(15), kr, poll, (u32)lane * 16, so, lbase + (poll & 2u) * 16384u, live<1>(srd, s) STA);
                    if (__builtin_expect(left != ~0u, 1)) break;
                    left = 0;
                    once_more(left, lbase, errw);
                }
                const u32 look = rnd - rn0;
                if (look > 1) COUNT(3);
                u32 tm;
                if (s + 1 < NSEG) tg = look > 1 ? read_ts(rs, sc + SL_TS, s, tm) : read_ts_late(rs, srd, sc + SL_TS, s, tm, 0u);
                track_rate(c0, r0, pc, pr, kr);
                FLUSH();
            }
        }
    } else {
        const u32 vr = lbase + 12288 + (u32)((g * 8 + (n >> 2)) * 16 + (n & 1) * 8);
        u32 z[3];
#pragma unroll
        for (int p = 0; p < 3; ++p) z[p] = p == 2 && (g & 1) ? OOB : z_off(wp, (u32)((p * 2 + (g & 1)) * 8 + (wave & 3) * 2 + (g >> 1))) + (u32)(h * 256 + n * 16);
#pragma unroll 1
        for (int s = 0; s < NSEG; ++s) {
            const u32 sz = slot_off(wp, s);
            v_b(W, rnd, sidx, vr, z[0] + sz, z[1] + sz, z[2] + sz, srd, lbase STA);
            FLUSH();
        }
    }
    DRAIN();
    LBAR();
    stx1(rs, wave == 0 && lane_id() < 2 ? (u32)(OFF_FZ + ((h >> 2) * 2 + lane_id()) * 128 + 16 + (h & 3) * 4) : OOB, 1u);
    COUNT1(3);
}

constexpr u32 L_U = 8192, L_T = 16384;
__device__ __forceinline__ u32x4 sub4(const u32x32& v, const int i) { return (u32x4){v[i], v[i + 1], v[i + 2], v[i + 3]}; }
__device__ __forceinline__ void r_slow(const int s, rsrc_t rs, u32 so, u32 sz, u32 sm, u32 pv, u32 zv, u32 ra, u32 rb, u32 mo, u32 sidx, u32 lbase, u32& cap, u32* errw) {
    const int lane = lane_id(), n = lane & 15;
    u32 spins = 0;
    u32x4 p[8], z[8];
#pragma unroll 1
    for (; spins < cap; ++spins) {
        CFENCE();
        u32 d = 1;
#pragma unroll
        for (int j = 0; j < 8; ++j) p[j] = ldx4(rs, pv + (u32)((j & 1) * 256 + ((j >> 1) & 1) * 1024), so + (u32)(j >> 2) * 32768u);
#pragma unroll
        for (int j = 0; j < 8; ++j) z[j] = ldx4(rs, zv + (u32)j * 1024, sz);
#pragma unroll
        for (int j = 0; j < 8; ++j) d = dist(dist(d, p[j], SENT), z[j], SENT);
        if (!__any(d == 0)) break;
    }
    if (spins >= cap) { atomicOr(errw, 2u); cap = 2; }
#pragma unroll
    for (int j = 0; j < 8; ++j) {
#pragma unroll
        for (int i = 0; i < 4; ++i) p[j][i] = p[j][i] == MARK ? 0u : p[j][i];
    }
    *(lds_vu32p)(lbase + L_T + 12) = step_tag(s);
    LBAR();
    LBAR();
    f32x4 acc = {0.f, 0.f, 0.f, 0.f};
#pragma unroll
    for (int j = 0; j < 8; j += 2) {
        const u32 ua = (j == 6 ? rb : ra) + (u32)j * 1024;
        acc = smf((u32x4){p[j][2], p[j][3], p[j + 1][0], p[j + 1][1]}, z[j], z[j + 1], acc, (int)sidx);
        acc = smf((u32x4){p[j + 1][2], p[j + 1][3], p[(j + 2) & 7][0], p[(j + 2) & 7][1]}, *(lds_u32x4p)ua, *(lds_u32x4p)(ua + 1024), acc, (int)sidx);
    }
    const float x0 = acc[0] + acc[2], x1 = acc[1] + acc[3], o = __shfl_xor(n < 8 ? x1 : x0, 8);
    const u32 r = n < 8 ? pack2(x0, o) : pack2(o, x1);
#pragma unroll
    for (int c = 0; c < NCP; ++c) stx1(rs, mo, r, sm + cpy(c));
    LBAR();
}

__device__ __forceinline__ void r_role(const int wave, const int ct, const int xg, const u32 wp, char* __restrict__ ws, char* smem) {
    const int lane = lane_id(), tid = wave * 64 + lane, n = lane & 15, g = lane >> 4;
    u32* errw = (u32*)(ws + OFF_ERR);
    const rsrc_t rs = mk_rsrc(ws);
    const u32x4 srd = mk_srd(ws);
    const u32 lbase = (u32)(uintptr_t)(__attribute__((address_space(3))) char*)smem;
    if (tid < 6) *(u32*)(smem + L_T + 8 + tid * 4) = ~0u;
    asm volatile("s_waitcnt lgkmcnt(0)" ::: "memory");
    int wslot = 0; const int wid = 192 + ct;
    WSTAMP(1);
    TLOAD();
    const u32 cp = (u32)xg ^ (wp >> 1);
    u32 cap = SPIN_CAP;
    STAMPS();
    LOGS();
    COUNT0();
    if (wave == 0) {
        const int kp = ct >= 36 ? 10 : ct >> 2;
        const u32 mb = (u32)((kp >> 2) * 32768 + (kp & 3) * 1024 + (ct & 3) * 256);
        const u32 mo = mb + (u32)(((n & 7) >> 2) * 128 + (2 * g + (n >> 3)) * 16 + (n & 3) * 4);
        const u32 pv = (u32)((g >> 1) * 2048 + (g & 1) * 512 + n * 16), zv = (u32)(g * 256 + (n & 7) * 32 + (n >> 3) * 16), sidx = (n & 2) ? 0xEEEE : 0x4444;
        const u32 ra = lbase + L_U + (u32)(((g & 1) ? 2048 : 256) + (g >> 1) * 512 + (n & 7) * 32 + (n >> 3) * 16), rb = ra - (u32)(g & 1) * 8192;
        const u32 te = slot_v(wp, lane) + copy_off(cp, lane) + SL_P, tm = slot_v(wp ^ 8u, lane) + put_off(wp, 0u, lane), tz = slot_z(wp, lane) + z_off(wp, (u32)ct);
#pragma unroll 1
        for (int s = -1; s + 1 < NSEG; ++s) {
            const u32 so = tab(te, s & 63), sm = tab(tm, s & 63), sz = tab(tz, s & 63);
            u32 bad, pass;
            r_f(bad, pass, sidx, pv, zv, ra, rb, mo, lbase + L_T + 16 + (u32)(lane & 3) * 4, live(srd, s), so, sz, sm, lbase, step_tag(s), unless_neg(cap, s), min(TV(8), 256u) STA);
            if (__builtin_expect(bad, 0)) { if (s < 0) LBAR(); else r_slow(s, rs, so, sz, sm, pv, zv, ra, rb, mo, sidx, lbase, cap, errw); }
            if (pass) COUNT(2);
            touch(srd, wp, s + 1, lbase + 17408u);
            LOG();
        }
    } else if (wave == 4) {
        u32 c0, r0, kr = 3950;
        calib(c0, r0);
        const u32 tc = slot_v(wp ^ 8u, lane) + copy_off(cp, lane) + SL_TS;
        u32 pc = c0, pr = r0, tg = r0 - 0x10000u, t2 = 0;
#pragma unroll 1
        for (int s = -1; s + 1 < NSEG; ++s) {
            u32 t1 = tg, ok = 0;
            if (s) r_t0(c0, r0, t1, t2, ok, tg + TV(6), tg + (t2 & 0xffffu), kr, (u32)(s & 127) + 1u, lbase, live(srd, s), tab(tc, s & 63) STA);
            if (s < 0) { if (!ok) { LBAR(); LBAR(); } }
            else if (__builtin_expect(!ok, 0)) {
                until(tg + TV(6) + TV(7) - 256u);
                r_t1(c0, r0, t1, t2, lane < 8 ? (u32)lane * 128u : OOB, kr, (u32)s + 1u, lbase, srd, tab(tc, s) STA);
            }
            if (__builtin_expect(ok == 2u && s >= 0, 0)) t1 = read_ts(rs, tab(tc, s), s, ok);
            tg = t1;
            if (__builtin_expect(rfl(*(lds_vu32p)(lbase + L_T + 8)) == step_tag(s), 0)) {
                LBAR();
                if (rfl(*(lds_vu32p)(lbase + L_T + 12)) == step_tag(s)) { LBAR(); LBAR(); }
            }
            track_rate(c0, r0, pc, pr, kr);
            LOG();
        }
    } else {
        const int q = wave == 5 ? 3 : wave - 1;
        u32 have = 0;
        u32x32 bn = {};
        auto seen = [&](u32x2 f) {
            u32 a = min(2 * f[0], 2 * f[1] + 1);
            a = min(a, (u32)__builtin_amdgcn_update_dpp(0u, a, 0xb1, 0xf, 0xf, false));
            a = min(a, (u32)__builtin_amdgcn_update_dpp(0u, a, 0x4e, 0xf, 0xf, false));
            a = min(a, (u32)__builtin_amdgcn_update_dpp(0u, a, 0x124, 0xf, 0xf, false));
            have = max(have, rfl(a));
        };
        const u32 fo = (u32)(OFF_FB + (q * 8 + (lane & 7)) * 128);
        auto load_b = [&](int sg, u32x32& bx) {
            u32 spins = 0;
#pragma unroll 1
            for (; have <= (u32)sg && spins < cap; ++spins) {
                CFENCE();
                seen(ldx2(rs, fo));
                if (spins) __builtin_amdgcn_s_sleep(4);
            }
            if (spins >= cap) { atomicOr(errw, 16u); cap = 1; }
            CFENCE();
            const u32 bs = far_off(wp, FAR_BX + (u32)(sg * NRED + ct) * 32768 + (u32)q * 8192);
            pull((u32)lane * 128, bs, srd, 1u, 0u, lbase + 17664u);
            u32x4 t[8];
#pragma unroll
            for (int i = 0; i < 8; ++i) t[i] = ldx4(rs, (u32)(lane * 16 + i * 1024), bs);
#pragma unroll
            for (int i = 0; i < 8; ++i)
#pragma unroll
                for (int j = 0; j < 4; ++j) bx[(i >> 1) * 8 + j * 2 + (i & 1)] = t[i][j];
        };
        const u32 eo = (u32)(q * 32768 + lane * 16), te = slot_v(wp, lane) + copy_off(cp, lane), sidx = n >= 8 ? 0xEEEE : 0x4444;
        const u32 uw = lbase + L_U + (u32)(q * 2048 + (g >> 1) * 256 + n * 16 + (g & 1) * 8);
        const u32 tb = far_v(wp, FAR_BX + (u32)(min(lane + 1, NSEG - 1) * NRED + ct) * 32768 + (u32)q * 8192);
        u32 s = ~0u, why = 0, left = 0;
#pragma unroll 1
        for (;;) {
            if (why & 1u) {
#pragma unroll
                for (int i = 0; i < 4; ++i) {
                    const f32x4 u = smf(ldx4(rs, eo + (u32)i * 1024, tab(te, (int)s - 1)), sub4(bn, i * 8), sub4(bn, i * 8 + 4), (f32x4){0.f, 0.f, 0.f, 0.f}, (int)sidx);
                    *(lds_u32x2p)(uw + (u32)i * 512) = (u32x2){pack2(u[0], u[1]), pack2(u[2], u[3])};
                }
                LBAR();
                LBAR();
            }
            if (s + 1 == NSEG) break;
            if (s == 0) left = SPIN_CAP;
            if ((int)s >= 0) load_b((int)s, bn);
            have = to_sgpr(have); left = to_sgpr(left);
            r_u(bn, s, have, left, why, sidx, eo, uw, lbase + L_T + 16 + (u32)q * 4, (u32)lane * 16, fo, te, tb, live(srd, (int)s), (u32)q * 4u, lbase STA LGA);
            s = to_sgpr(s); have = to_sgpr(have); left = to_sgpr(left); why = to_sgpr(why);
        }
        if (left == 0) atomicOr(errw, 4u);
    }
    COUNT1(wave ? 1 : 2);
    LOGOUT(32 + ct);
}

__device__ __forceinline__ void relay_role(const u32 wp, const int xg, char* __restrict__ ws) {
    const int lane = lane_id();
    const rsrc_t rs = mk_rsrc(ws);
    const u32 tc = slot_v(wp ^ 8u, lane) + copy_off((u32)xg ^ (wp >> 1), lane) + SL_TS;
#pragma unroll 1
    for (int k = 0; k + 1 < NSEG; ++k) {
        u32 t;
#pragma unroll 1
        for (u32 spins = 0;; ++spins) {
            u32 ok;
            t = read_ts(rs, tab(tc, k), k, ok);
            if (ok) break;
            if (spins >= 16u || ldx1(rs, (u32)OFF_ERR) != 0u) return;
        }
        if (k >= 3) stx1(rs, lane < 8 ? (u32)(OFF_RDY + lane * 128) : OOB, (u32)k - 2u);
        for (int q = 0; q < 64 && (int)((u32)__builtin_amdgcn_s_memrealtime() * 24u - t) < 2400; ++q) __builtin_amdgcn_s_sleep(2);
    }
}

__device__ __forceinline__ void lead_role(const u32 wp, const int xg, char* __restrict__ ws, char* smem) {
    const int lane = lane_id();
    u32* errw = (u32*)(ws + OFF_ERR);
    const rsrc_t rs = mk_rsrc(ws);
    const u32x4 srd = mk_srd(ws);
    TLOAD();
    STAMPS();
    const u32 tsw = lane < 4 ? SL_TS + (u32)((lane & 1) * 4096 + (lane >> 1) * 16384 + xg * 128) : OOB;
    const u32 tm = slot_v(wp ^ 8u, lane), tc = tm + copy_off((u32)xg ^ (wp >> 1), lane);
    u32 c0, r0, kr = 3950, ki = 265, bump = 0, hist = 0, dp = 0;
    calib(c0, r0);
    u32 pc = c0, pr = r0, tg = r0, cap = SPIN_CAP;
#pragma unroll 1
    for (int s = 0; s + 1 < NSEG; ++s) {
        u32 sr = tsw + tab(tm, s);
        const u32 mg = bump + (s < EARLY_N ? TV(10) : 0u) + (s < 4 ? TV(3) : 0u);
        const u32 de = (TV(0) * ki >> 8) + mg, dz = TV(1) * ki >> 8, per = max(de, dp) + (TV(2) * ki >> 8) + mg;
        dp = de;
        u32 look = 0;
        DRAIN();
        {
            const u32 so = tab(tc, (s + 63) & 63);
#pragma unroll 1
            for (;; ++look) {
                u64 tl;
                u32 miss;
                l_g(miss, c0, r0, tl, tg + TV(15), look ? 0u : kr, (u32)((lane >> 5) * 32768 + (lane & 31) * 128), lane < 16 ? (u32)(65536 + (lane >> 3) * 2048 + (lane & 7) * 128) : OOB, so, live<1>(srd, s), (u32x4){tg + de, tg + per, de | (dz << 16), (u32)s + 1u}, look || !s ? OOB : sr STA);
                if (!s) break;
                if (look && (int)((u32)tl * 24u + TV(14) - tg) > 0) tg = (u32)tl * 24u + TV(14);
                if (__builtin_expect(!miss, 1)) break;
                if (!look) {
                    sr += 512u;
                    tg += TV(7);
                }
                if (look >= cap) { atomicOr(errw, 4u); cap = 1; break; }
            }
        }
        if (!s) {
            bool dead = false;
            wait_word(rs, (u32)(OFF_FA + lane * 128), true, 1u, dead, errw, 8u, lane);
            XSTAMP(192 + xg, 227);
            tg = (u32)__builtin_amdgcn_s_memrealtime() * 24u + TV(13) - 512u;
        }
        if (!s || look) stx4(rs, sr, (u32x4){tg + de, tg + per, de | (dz << 16), (u32)s + 1u});
        nap(384);
        u32 m;
        tg = look ? read_ts(rs, tab(tc, s) + SL_TS, s, m) : read_ts_late(rs, srd, tab(tc, s) + SL_TS, s, m);
        touch(srd, wp, s + 1, (u32)(uintptr_t)(__attribute__((address_space(3))) char*)smem);
        track_rate(c0, r0, pc, pr, kr);
        hist = ((hist << 1) | (s >= BUMP_S0 && m != 15u ? 1u : 0u)) & BUMP_WIN;
        if (__builtin_popcount(hist) >= BUMP_N && bump < BUMP_MAX * TV(11)) { bump += TV(11); hist = 0; }
        if (pc == c0) ki = to_sgpr((u32)(1048576.f * __builtin_amdgcn_rcpf((float)kr) + 0.5f));
        FLUSH();
    }
}

__device__ __forceinline__ void fill_role(const u32 wp, const int fi, char* __restrict__ ws) {
    const int lane = lane_id();
    const rsrc_t rs = mk_rsrc(ws);
    TLOAD();
    const u32 oz = (u32)(fi * 65536 + lane * 16);
    nap(TV(4) * 64u);
#pragma unroll 1
    for (int s = 5; s < NSEG; ++s) {
        const u32 un = 0x38u << ((~((wp >> 3) ^ ((u32)s >> 5)) & 1u) * 8u), a = oz + slot_off(wp, s);
        const u32 a0 = (un >> (2 * fi)) & 1u ? OOB : a, a1 = (un >> (2 * fi + 1)) & 1u ? OOB : a + 32768u;
#pragma unroll
        for (u32 i = 0; i < 32; ++i) {
            stx4(rs, (i < 16 ? a0 : a1) + ((i >> 3) & 1u) * 16384 + (i & 7) * 1024, (u32x4){SENT, SENT, SENT, SENT});
            if (i == 15) {
                asm volatile("s_waitcnt vmcnt(16)" ::: "memory");
                stx1(rs, lane == 0 ? (u32)(OFF_FA + fi * 128 + 8) : OOB, (u32)s - 1u);
            }
            if (i % FILLG == FILLG - 1) nap(TV(9));
        }
    }
    DRAIN();
    stx1(rs, lane == 0 ? (u32)(OFF_FA + fi * 128 + 8) : OOB, (u32)NSEG + 1u);
}

__device__ __forceinline__ int tile_of(const int xg, const int hl, const int i) {
    const u32 pieces = hl ? 0x85410u : 0x97632u;
    return CFG ? (int)((pieces >> ((i >> 1) * 4)) & 15u) * 4 + (i & 1) * 2 + (xg & 1) : (int)((pieces >> (i * 4)) & 15u) * 4 + xg;
}
__device__ __forceinline__ void warm_role(const u32 wp, const int xg, const int wi, char* __restrict__ ws, char* smem) {
    const int lane = lane_id();
    const rsrc_t rs = mk_rsrc(ws);
    const u32x4 srd = mk_srd(ws);
    const u32 lbase = (u32)(uintptr_t)(__attribute__((address_space(3))) char*)smem;
    TLOAD();
    u32 have = 0, ta = 0;
    int next = 1;
    const u32 tc = slot_v(wp ^ 8u, lane) + copy_off((u32)xg ^ (wp >> 1), lane) + SL_TS;
    const u32 wq = wi < 0 ? wp ^ 1u : wp, ls = wi < 0 ? H_ * 8192u : NRED * 32768u;
    const u32 l0 = wi < 0 ? (wi & 1 ? NEAR_KX : NEAR_QX) + (u32)(xg * 8 + (lane & 7)) * 8192 : FAR_BX + (u32)(min(lane, 9) * 4 + xg) * 32768 + (u32)wi * 8192;
    auto look = [&] {
        const u32x2 f = ldx2(rs, wi < 0 ? (u32)(OFF_FA + (xg * 8 + (lane & 7)) * 128) : (u32)(OFF_FB + (wi * 8 + (lane & 7)) * 128));
        u32 a = min(2 * f[0], 2 * f[1] + 1);
#pragma unroll
        for (int q = 1; q < 8; q <<= 1) a = min(a, (u32)__shfl_xor((int)a, q));
        have = rfl(a);
    };
#pragma unroll 1
    for (u32 spins = 0; next < WFIRST && spins < 0x4000u; ++spins) {
        CFENCE();
        if (have <= (u32)next) { __builtin_amdgcn_s_sleep(4); look(); }
        else { pull((u32)lane * 128, far_v(wq, l0 + (u32)next * ls), srd, wi < 0 ? 8u : 10u, 64u, lbase + 69632u); ++next; }
    }
#pragma unroll 1
    for (int s = 0; s + 2 < NSEG; ++s) {
        if (s) until(ta + 1000u);
        u32 tm;
        ta = read_ts_late(rs, srd, tab(tc, s), s, tm);
        if (have <= (u32)s + WAHEAD) look();
        if (next < s + 2) next = s + 2;
#pragma unroll 1
        for (; next <= s + WAHEAD && next < NSEG && have > (u32)next; ++next) {
            const u32 tb = far_v(wq, l0 + (u32)next * ls);
            until(ta + TV(5) - 1024u);
            pull((u32)lane * 128, tb, srd, wi < 0 ? 8u : 10u, min(TV(12), 256u), lbase + 69632u);
        }
    }
    DRAIN();
}

__device__ __forceinline__ void topk_role(const u64 tag, char* __restrict__ ws) {
    const int lane = lane_id();
    const rsrc_t rs = mk_rsrc(ws);
    wait_init(rs, tag, 2);
    u32 v[32];
#pragma unroll
    for (int i = 0; i < 8; ++i) {
        const u32x4 t = ldx4(rs, (u32)lane * 16, (u32)(OFF_R + i * 1024));
#pragma unroll
        for (int j = 0; j < 4; ++j) v[i * 4 + j] = ((t[j] ^ ((u32)((int)t[j] >> 31) | 0x80000000u)) & 0xffff0000u) | (u32)(2047 - i * 256 - j - lane * 4);
    }
    u32 top = ~0u, res = 0;
#pragma unroll 1
    for (int c = 0; c < TOPC; ++c) {
        u32 d = ~0u;
#pragma unroll
        for (int i = 0; i < 32; ++i) d = min(d, top - v[i]);
        d = min(d, (u32)__builtin_amdgcn_update_dpp(0u, d, 0xb1, 0xf, 0xf, false));
        d = min(d, (u32)__builtin_amdgcn_update_dpp(0u, d, 0x4e, 0xf, 0xf, false));
        d = min(d, (u32)__builtin_amdgcn_update_dpp(0u, d, 0x124, 0xf, 0xf, false));
        d = min(d, (u32)__builtin_amdgcn_update_dpp(0u, d, 0x128, 0xf, 0xf, false));
        const u32 dm = min(min((u32)__builtin_amdgcn_readlane(d, 0), (u32)__builtin_amdgcn_readlane(d, 16)), min((u32)__builtin_amdgcn_readlane(d, 32), (u32)__builtin_amdgcn_readlane(d, 48)));
        top -= dm;
        if (lane == c) res = 2048u - (top & 2047u);
        top -= 1u;
    }
    stx1(rs, lane < TOPC ? (u32)(OFF_IDX + lane * 4) : OOB, res);
    XSTAMP(192, 224);
}

#define MOEV 0
#define MOE_LD(p) __builtin_nontemporal_load(p)
#define MOE_NT " nt"
template <int NK>
__device__ __forceinline__ void moe_ld(const u16* __restrict__ wp, const int ldw, const int nvalid, u32 (&r)[NK][8]) {
#pragma unroll
    for (int k = 0; k < NK; ++k)
#pragma unroll
        for (int i = 0; i < 8; ++i) r[k][i] = k < nvalid ? MOE_LD((const u32*)(wp + (size_t)(k * 32 + i) * ldw)) : 0u;
}
template <int NK>
__device__ __forceinline__ void moe_mma(const u32 (&r)[NK][8], const u32x4 (&af)[NK], f32x4& ce, f32x4& co) {
#pragma unroll
    for (int k = 0; k < NK; ++k) {
        u32x4 be, bo;
#pragma unroll
        for (int j = 0; j < 4; ++j) {
            be[j] = __builtin_amdgcn_perm(r[k][2 * j + 1], r[k][2 * j], 0x05040100u);
            bo[j] = __builtin_amdgcn_perm(r[k][2 * j + 1], r[k][2 * j], 0x07060302u);
        }
        ce = mfma16(af[k], be, ce);
        co = mfma16(af[k], bo, co);
    }
}
constexpr int NGU = FF_ / 32, NDN = D_ / 32;
__device__ __forceinline__ void moe_part(const int w, const int tid, const u16* __restrict__ wg, const u16* __restrict__ wu,
                                         const u16* __restrict__ wd, char* __restrict__ ws, char* smem) {
    const int wave = __builtin_amdgcn_readfirstlane(tid >> 6), lane = tid & 63;
    const int n = lane & 15, g = lane >> 4;
    u32* errw = (u32*)(ws + OFF_ERR);
    const rsrc_t rs = mk_rsrc(ws);
    bool dead = false;
    f32x4 (*red)[2][64] = (f32x4(*)[2][64])smem;
    const f32x4 z = {0.f, 0.f, 0.f, 0.f};
    wait_word(rs, (u32)(OFF_IDX + (n & 7) * 4), true, 1u, dead, errw, 16u, lane);
    const u32 row = (ldx1(rs, (u32)(OFF_IDX + (n & 7) * 4)) - 1u) & (S_ - 1);
    u32 r1[5][8], r2[7][8];
    const int kq = wave >> 1, dg = w - (64 - NDN), k0 = wave * 7, nk = wave == 7 ? 5 : 7;
    if (w < NGU) moe_ld<5>(((wave & 1) ? wu : wg) + (size_t)(kq * 160 + g * 8) * FF_ + w * 32 + n * 2, FF_, 5, r1);
    if (dg >= 0) moe_ld<7>(wd + (size_t)(k0 * 32 + g * 8) * D_ + dg * 32 + n * 2, D_, nk, r2);
    if (w < NGU) {
        u32x4 af[5];
#pragma unroll
        for (int k = 0; k < 5; ++k) af[k] = ldx4(rs, (u32)OFF_XN + (row * D_ + (u32)((kq * 5 + k) * 32 + g * 8)) * 2);
        f32x4 ce = z, co = z;
        moe_mma<5>(r1, af, ce, co);
        red[wave][0][lane] = ce; red[wave][1][lane] = co;
        __syncthreads();
        if (wave == 0 && g < 2) {
            f32x4 t[4];
#pragma unroll
            for (int q = 0; q < 4; ++q) t[q] = (red[q >> 1][q & 1][lane] + red[2 + (q >> 1)][q & 1][lane]) + (red[4 + (q >> 1)][q & 1][lane] + red[6 + (q >> 1)][q & 1][lane]);
#pragma unroll
            for (int i = 0; i < 4; ++i) {
                float res[2];
#pragma unroll
                for (int q = 0; q < 2; ++q) {
                    float gg = bfr(t[q][i]), uu = bfr(t[2 + q][i]);
                    float sg = bfr(gg / (1.0f + __expf(-gg)));
                    res[q] = sg * uu;
                }
                stx1(rs, (u32)(OFF_HB + ((size_t)(4 * g + i) * FF_ + w * 32 + n * 2) * 2), pack2(res[0], res[1]));
            }
        }
        if (wave == 0) {
            DRAIN();
            if (lane == 0) __hip_atomic_fetch_add((u32*)(ws + OFF_CNT), 1u, __ATOMIC_RELAXED, __HIP_MEMORY_SCOPE_AGENT);
        }
        __syncthreads();
    }
    if (dg >= 0) {
        const float rv = __uint_as_float(ldx1(rs, (u32)OFF_R + row * 4));
        wait_word(rs, (u32)OFF_CNT, true, (u32)NGU, dead, errw, 16u, lane);
        u32x4 af[7];
#pragma unroll
        for (int k = 0; k < 7; ++k) af[k] = ldx4(rs, (u32)(OFF_HB + ((size_t)(n & 7) * FF_ + (k0 + (k < nk ? k : 0)) * 32 + g * 8) * 2));
        f32x4 ye = z, yo = z;
        moe_mma<7>(r2, af, ye, yo);
        red[wave][0][lane] = ye; red[wave][1][lane] = yo;
        __syncthreads();
        if (wave == 0) {
            f32x4 t[2];
#pragma unroll
            for (int q = 0; q < 2; ++q)
                t[q] = ((red[0][q][lane] + red[1][q][lane]) + (red[2][q][lane] + red[3][q][lane])) + ((red[4][q][lane] + red[5][q][lane]) + (red[6][q][lane] + red[7][q][lane]));
            const float rs1 = bfr(1.0f / (1.0f + __expf(-rv)));
#pragma unroll
            for (int i = 0; i < 4; ++i) {
                const float rsg = __shfl(rs1, (4 * g + i) & 7);
                stx1(rs, g < 2 ? (u32)(OFF_UPD + ((size_t)(4 * g + i) * D_ + dg * 32 + n * 2) * 2) : OOB, pack2(bfr(t[0][i]) * rsg, bfr(t[1][i]) * rsg));
            }
            DRAIN();
            if (lane == 0) __hip_atomic_fetch_add((u32*)(ws + OFF_CNT + 256), 1u, __ATOMIC_RELAXED, __HIP_MEMORY_SCOPE_AGENT);
        }
        __syncthreads();
    }
}

__device__ __forceinline__ void moe_ahead(const int w, const int t, const u16* __restrict__ wg, const u16* __restrict__ wu, const u16* __restrict__ wd, u32 lds) {
    const int lane = lane_id(), t2 = w < NGU ? t - 20 : t;
    if (t2 >= (w >= 64 - NDN ? 27 : 0)) return;
    const u16* p = t2 < 0 ? (t < 10 ? wg : wu - 640 * FF_) + (size_t)(t * 64 + lane) * FF_ + w * 32 : wd + (size_t)(t2 * 64 + lane) * D_ + (w - (64 - NDN)) * 32;
    u32 sv;
    asm volatile("s_mov_b32 %0, m0\n\ts_mov_b32 m0, %2\n\ts_nop 0\n\tglobal_load_lds_dword %1, off" MOE_NT "\n\ts_mov_b32 m0, %0" : "=&s"(sv) : "v"(p), "s"(lds) : "memory");
}

#define LOOKST 18
constexpr int NBIG = (NSEG - 1) * 10, NTASK = NBIG + 80;

struct OutCtx { rsrc_t rs; const u16* x; u16* out; u32* errw; u32 wp; int wid; char* ws; };
template <int OT, int NT>
__device__ __forceinline__ void out_task(const OutCtx& cx, const int sg, const int c0, const int n0, const int tid, const int (&sel_idx)[TOPC], bool& adone, u32& rdy,
                                         bool& dead, char* redp, u32* nextp, u32* tctr) {
    const int wave = __builtin_amdgcn_readfirstlane(tid >> 6), lane = tid & 63, n = lane & 15, g = lane >> 4;
    const rsrc_t rs = cx.rs;
    if (!adone && rdy <= (u32)sg) {
        wait_word(rs, (u32)(OFF_FB + (wave * 4 + (lane & 3)) * 128 + (sg & 1) * 4), true, (u32)(sg >> 1) + 1, dead, cx.errw, 32u, lane);
        u32x2 f = ldx2(rs, (u32)(OFF_FB + (wave * 4 + (lane & 3)) * 128));
        adone = __all(f[0] >= NSEG / 2 && f[1] >= NSEG / 2);
    }
    u32x4 af[OT][5], bq[NT][5];
    const u32 bo = amap((u32)(wave * 4096 + lane * 16));
#pragma unroll
    for (int c = 0; c < OT; ++c) {
        const u32 bb = far_off(cx.wp, FAR_BX + (u32)(sg * 40 + c0 + c) * 32768);
#pragma unroll
        for (int hh = 0; hh < 4; ++hh) af[c][hh] = ldx4(rs, bo + hh * 1024, bb);
    }
    const int ti = (tid >> 6) & (OT * NT - 1);
    const int token = sg * SEGL + (n0 + (ti & (NT - 1))) * 16 + n;
    const int col = (c0 + ti / NT) * 16 + 4 * g;
    int sel = -1;
#pragma unroll
    for (int c = 0; c < TOPC; ++c) if (sel_idx[c] == token) sel = c;
    const u32x2 xv = *(const u32x2*)(cx.x + (size_t)token * D_ + col);
    const u32x2 nv = ldx2(rs, (u32)(OFF_XN + ((size_t)token * D_ + col) * 2));
    if (__any(sel >= 0)) wait_word(rs, (u32)(OFF_CNT + 256), true, (u32)NDN, dead, cx.errw, 256u, lane);
    const u32x2 uv = ldx2(rs, sel >= 0 ? (u32)(OFF_UPD + ((size_t)sel * D_ + col) * 2) : OOB);
    __builtin_amdgcn_sched_barrier(0);
    const bool late = sg + 4 >= NSEG;
    if (late) wait_flags(rs, (u32)((OT == 1 ? OFF_FZ + (wave * 2 + n0) * 128 : OFF_FP + wave * 128) + (lane & 1) * 16), true, 1u, dead, cx.errw, 64u, lane);
    else if (rdy <= (u32)sg) {
#pragma unroll 1
        for (u32 spins = 0; !dead; ++spins) {
            CFENCE();
            rdy = __builtin_amdgcn_readfirstlane(ldx1(rs, (u32)(OFF_RDY + (cx.wid & 7) * 128)));
            if (rdy > (u32)sg) break;
            __builtin_amdgcn_s_sleep(8);
            if (spins > SPIN_CAP) { if (lane == 0) atomicOr(cx.errw, 64u); dead = true; }
            if ((spins & 63) == 63 && ldx1(rs, (u32)OFF_ERR) != 0u) dead = true;
        }
        CFENCE();
    }
#pragma unroll
    for (int c = 0; c < OT; ++c) af[c][4] = ldx4(rs, (u32)(wave * 64 + lane) * 16, slot_off(cx.wp, sg) + z_off(cx.wp, (u32)(c0 + c)));
#pragma unroll
    for (int nt = 0; nt < NT; ++nt) {
#pragma unroll
        for (int hh = 0; hh < 4; ++hh) bq[nt][hh] = ldx4(rs, (u32)lane * 16, (u32)OFF_PX + (u32)((sg * H_ + wave * 4 + hh) * 2 + n0 + nt) * 1024);
        bq[nt][4] = ldx4(rs, (u32)lane * 16, (u32)OFF_PXM + (u32)((sg * 8 + wave) * 2 + n0 + nt) * 1024);
    }
    CFENCE();
    u32 next = 0;
    if (tid == 0) next = !late ? __hip_atomic_fetch_add(tctr, 1u, __ATOMIC_RELAXED, __HIP_MEMORY_SCOPE_AGENT)
                                 : __hip_atomic_load(tctr, __ATOMIC_RELAXED, __HIP_MEMORY_SCOPE_AGENT);
    f32x4 (*red)[OT * NT][64] = (f32x4(*)[OT * NT][64])redp;
#pragma unroll
    for (int c = 0; c < OT; ++c)
#pragma unroll
        for (int nt = 0; nt < NT; ++nt) {
            f32x4 acc = {0.f, 0.f, 0.f, 0.f};
#pragma unroll
            for (int k = 0; k < 5; ++k) acc = mfma16(af[c][k], bq[nt][k], acc);
            red[wave][c * NT + nt][lane] = acc;
        }
    if (tid == 0) {
        if (late && next < (u32)NTASK) next = __hip_atomic_fetch_add(tctr, 1u, __ATOMIC_RELAXED, __HIP_MEMORY_SCOPE_AGENT);
        *nextp = next;
    }
    LBAR();
    if (tid < OT * NT * 64) {
        f32x4 sum = red[0][ti][lane];
#pragma unroll
        for (int wv = 1; wv < 8; ++wv) sum += red[wv][ti][lane];
        float res[4];
#pragma unroll
        for (int q = 0; q < 4; ++q) {
            float xx = bf2f((u16)((xv[q >> 1] >> ((q & 1) * 16)) & 0xffff));
            float nn = bf2f((u16)((nv[q >> 1] >> ((q & 1) * 16)) & 0xffff));
            float bo2 = nn;
            if (sel >= 0) bo2 = bfr(nn + bf2f((u16)((uv[q >> 1] >> ((q & 1) * 16)) & 0xffff)));
            float gg = bfr(bo2 / (1.0f + __expf(-bo2)));
            float sv = bfr(sum[q]);
            res[q] = bfr(xx + bfr(sv * gg));
        }
        *(u32x2*)(cx.out + (size_t)token * D_ + col) = (u32x2){pack2(res[0], res[1]), pack2(res[2], res[3])};
    }
}

constexpr u32 L_VS = 40960;
__device__ __forceinline__ u32 xbuf(int b) { return b == 0 ? 65536u : (b == 1 ? 106496u : 0u); }
__device__ __forceinline__ void worker_role(const int wave, const int wid, const u64 tag, const u32 wp, const u16* __restrict__ x, const u32x32 (&W)[5],
                                            const u16* __restrict__ wg, const u16* __restrict__ wu,
                                            const u16* __restrict__ wd, char* __restrict__ ws, u16* __restrict__ out, char* smem) {
    const int lane = lane_id(), tid = wave * 64 + lane;
    const int n = lane & 15, g = lane >> 4;
    const int par = (wid >> 2) & 1, kind = (wid >> 3) & 1, h = (wid & 3) * 8 + (wid >> 4), type = kind * 2 + par;
    u32* errw = (u32*)(ws + OFF_ERR);
    const rsrc_t rs = mk_rsrc(ws);
    bool dead = false;
    const bool isqk = type < 2;
    int wslot = 1;
    u32* sh = (u32*)(smem + L_MISC);

    {
        const u32 lbase = (u32)(uintptr_t)(__attribute__((address_space(3))) char*)smem;
        const u32x4 srd = mk_srd(ws);
        const u32 vl = lbase + (u32)lane * 16, xp = (u32)(((wave >> 1) & 1) * 20480 + (wave < 4 ? 4096 + (wave & 1) * 8192 : (wave & 1) * 2048)), xv = xp + (u32)lane * 16, ca = lbase + L_MISC + 64;
        const u32 fo = (u32)(OFF_FA + kind * 4096 + h * 128 + par * 4);
        auto xsrc = [&](int it) { return it < NSEG / 2 ? (u32)OFF_XNP + (u32)(it * 2 + par) * 40960u : (u32)OFF_XNP; };
        auto now = [&](u32 v) {
            DRAIN();
            if (lane_id() == 0 && __hip_atomic_fetch_add((u32*)(smem + L_MISC + 68), 1u, __ATOMIC_RELAXED, __HIP_MEMORY_SCOPE_WORKGROUP) == v * 4 - 1) { stx1(rs, fo, v); XSTAMP(wid, 224 + v); }
        };
        WSTAMP(4);
        STAMPS();
        u32 b0 = xbuf(0), b1 = xbuf(1), b2 = xbuf(2);
        auto rot = [&] { const u32 t = b0; b0 = b1; b1 = b2; b2 = t; };
        if (wave < 4) {
            const u32 oq = (u32)(wave * 64 + lane) * 16;
#pragma unroll 1
            for (int it = 0; it < NSEG / 2; ++it) {
                if (isqk) {
                    if ((it & ~4) == 2) wait_init(rs, tag, 1 + (it >> 2));
                    const u32 ob = far_off(wp ^ 1u, NEAR_QX + (u32)((it * 2 + par) * H_ + h) * 8192);
                    wx0(W, vl + b0, oq, srd, xv, xsrc(it + 2), lbase + b2 + xp, ob, ob + 4096 STA);
                } else wx1(W, vl + b0, lbase + L_VS + (u32)(it & 1) * 8192 + oq STA);
                rot();
                CFLUSH();
                if (it < 3 || (it & 7) == 7) WSTAMP(0x10 + it);
            }
            DRAIN();
            LBAR();
        } else if (isqk) {
            const u32 oq = (u32)(tid & 255) * 16;
            LBAR();
#pragma unroll 1
            for (int it = 0; it < NSEG / 2; ++it) {
                const int sg = it * 2 + par;
                if ((it & ~4) == 2) sh[18] = wait_init(rs, tag, 1 + (it >> 2));
                const u32 ob = far_off(wp ^ 1u, NEAR_KX + (u32)(sg * H_ + h) * 8192);
                wk(W, vl + b0, oq, srd, ob, ob + 4096, 0.08838834764831845f * 1.4426950408889634f, xv, xsrc(it + 2), lbase + b2 + xp,
                   ca, (u32)it * 4 + 3, (u32)it, fo STA);
                rot();
                CFLUSH();
                if (it < NOWV) now((u32)it + 1);
                if (MOEV && it >= 6 && (wave & 2) == 0) moe_ahead(h * 2 + par, (it - 6) * 2 + (wave & 1), wg, wu, wd, lbase + L_MISC + 256);
            }
        } else {
            const u32 ob = amap((u32)(h * 1024 + lane * 16)), yp = 4096u + (u32)(wave & 1) * 6144u;
            LBAR();
#pragma unroll
            for (int i = 0; i < 10; ++i) {
                const u32 o = i < 2 ? (u32)i * 1024 : yp + (u32)(i - 2) * 1024;
                dma16_sc1(ws + xsrc(2) + xv + o, __builtin_amdgcn_readfirstlane(lbase + b2 + xp + o));
            }
            DRAIN();
            LBAR();
#pragma unroll 1
            for (int it = 1; it <= NSEG / 2; ++it) {
                rot();
                if ((it & ~4) == 2) sh[18] = wait_init(rs, tag, 1 + (it >> 2));
                wb(W, vl + L_VS + (u32)(~it & 1) * 8192, ob, srd, FAR_BX + (u32)((it * 2 - 2 + par) * NRED + (wave & 3) * 2) * 32768, wp, (u32)it,
                   xv, xsrc(it + 2), lbase + b2 + xp, xsrc(it + 2) + yp, lbase + b2 + xp + yp, ca, (u32)it * 4 - 1, (u32)it - 1, fo STA);
                CFLUSH();
                if (it <= NOWV) { now((u32)it); LBAR(); }
            }
        }
        DRAIN();
        LBAR();
        stx1(rs, wave == 0 && lane_id() == 0 ? fo : OOB, NSEG / 2);
    }
    WSTAMP(2);

    {
    const int lane = lane_id(), tid = wave * 64 + lane;
    if (isqk) moe_part(h * 2 + par, tid, wg, wu, wd, ws, smem);
    WSTAMP(5);
    u32* nextp = (u32*)(smem + L_MISC + 96);
    u32* tctr = (u32*)(ws + OFF_CNT + 512);
    if (tid == 0) nextp[0] = __hip_atomic_fetch_add(tctr, 1u, __ATOMIC_RELAXED, __HIP_MEMORY_SCOPE_AGENT);
    wait_flags(rs, (u32)OFF_IDX + (lane & 1) * 16, true, 1u, dead, errw, 256u, lane);
    __syncthreads();
    int sel_idx[TOPC];
    {
        u32x4 i0 = ldx4(rs, (u32)OFF_IDX), i1 = ldx4(rs, (u32)OFF_IDX + 16);
#pragma unroll
        for (int c = 0; c < 4; ++c) { sel_idx[c] = (int)i0[c] - 1; sel_idx[4 + c] = (int)i1[c] - 1; }
    }
    u32 order;
    {
        bool hit = false;
#pragma unroll
        for (int c = 0; c < TOPC; ++c) hit |= (sel_idx[c] >> 5) == lane;
        const u64 m = __ballot(hit && lane < 40), below = ((u64)1 << lane) - 1;
        const int pos = lane >= 40 ? lane : ((m >> lane) & 1 ? 40 - __popcll(m) + __popcll(m & below) : lane - __popcll(m & below));
        order = (u32)__builtin_amdgcn_ds_permute(pos * 4, lane);
    }
    WSTAMP(3);
    const OutCtx cx = {rs, x, out, errw, to_sgpr(sh[18]), wid, ws};
    bool adone = false;
    u32 rdy = 0;
#pragma unroll 1
    for (int tc = 0;; ++tc) {
        const int task = __builtin_amdgcn_readfirstlane(nextp[tc & 1]);
        if (task >= NTASK) break;
        char* redp = smem + (tc & 1) * 65536;
        if (task < NBIG) {
            WSTAMP(0x100 + task / 10);
            out_task<4, 2>(cx, (int)tab(order, task / 10), (task % 10) * 4, 0, tid, sel_idx, adone, rdy, dead, redp, nextp + ((tc + 1) & 1), tctr);
        } else {
            WSTAMP(0x100 + NSEG - 1);
            out_task<1, 1>(cx, NSEG - 1, (task - NBIG) >> 1, (task - NBIG) & 1, tid, sel_idx, adone, rdy, dead, redp, nextp + ((tc + 1) & 1), tctr);
        }
        WSTAMP(0x300);
        if (task == NTASK - 1) {
            wait_word(rs, (u32)(OFF_CNT + 768), true, 8u, dead, errw, 64u, lane);
            if (tid < 216) stx2(rs, tid < 192 ? (u32)(OFF_INI + tid * 8) : (u32)(OFF_SUM + (tid - 192) * 128), (u32x2){~(u32)tag, ~cal_tag(tag, ~(u32)tag)});
        }
    }
    }
}

constexpr int FUSED_LDS = L_MISC + 1024;
#define ORD_A 0x143210
#define ORD_C 0x101234
#define KERNEL_SYM "fused_kernel"
#define CODE_KB 88
extern "C" __global__ __launch_bounds__(512, 2) void fused_kernel(const u16* __restrict__ x, const u16* __restrict__ mem0, const u16* __restrict__ w_r,
                                                        const u16* __restrict__ Wq, const u16* __restrict__ Wk,
                                                        const u16* __restrict__ Wv, const u16* __restrict__ Wo,
                                                        const u16* __restrict__ wg, const u16* __restrict__ wu,
                                                        const u16* __restrict__ wd, char* __restrict__ ws, u16* __restrict__ out) {
    extern __shared__ __attribute__((aligned(16))) char smem[];
    const int wave = __builtin_amdgcn_readfirstlane(threadIdx.x >> 6);
    u64 gen = 0, tag = 0, old = 0;
    u32 xcc, se;
    asm volatile("s_getreg_b32 %0, hwreg(HW_REG_XCC_ID, 0, 3)" : "=s"(xcc));
    asm volatile("s_getreg_b32 %0, hwreg(HW_REG_HW_ID, 13, 2)" : "=s"(se));
    u32 bid = blockIdx.x;
    asm volatile("" : "+s"(bid));
    const bool first = (int)bid >= 0;
    u32 bq = first ? (bid & ~31u) + se * 8u + xcc : bid & 255u;
    asm volatile("" : "+s"(bq));
    const int b = (int)bq, k = b >> 3, xg = b & 3;
    if (threadIdx.x >= 448 && threadIdx.x < 453) {
        const int lane = threadIdx.x & 63;
        const u32 pg = (bid >> 3 & 31u) + 32u * (u32)lane;
        const char* tp = lane < 4 ? ws + min(pg << 21, (u32)WS_TOTAL - 4u) : (const char*)out + (size_t)(bid >> 3 & 3u) * 655360;
        u32 sv;
        asm volatile("s_mov_b32 %0, m0\n\ts_mov_b32 m0, %2\n\ts_nop 0\n\tglobal_load_lds_dword %1, off\n\ts_mov_b32 m0, %0"
                     : "=&s"(sv) : "v"(tp), "s"((u32)(uintptr_t)(__attribute__((address_space(3))) char*)smem + L_MISC + 256u) : "memory");
    }
    if (wave == 6 && (bid >> 3 & 31u) < CODE_KB / 8) {
        u32 sv;
        asm volatile("s_getpc_b64 s[98:99]\nLc%=:\n\ts_sub_u32 s98, s98, Lc%=-" KERNEL_SYM "\n\ts_subb_u32 s99, s99, 0\n\t"
                     "s_mov_b32 %0, m0\n\ts_mov_b32 m0, %2\n\ts_nop 0\n\tglobal_load_lds_dword %1, s[98:99]\n\ts_mov_b32 m0, %0"
                     : "=&s"(sv) : "v"((bid >> 3 & 31u) * 8192u + (threadIdx.x & 63u) * 128u),
                       "s"((u32)(uintptr_t)(__attribute__((address_space(3))) char*)smem + L_MISC + 768u) : "s98", "s99", "scc", "memory");
    }
    u64* claim = (u64*)(ws + OFF_CLAIM);
    u32* sh = (u32*)(smem + L_MISC);
    auto try_claim = [&] {
        if (wave == 0 && first) {
            asm volatile("s_load_dwordx2 %0, %1, 0x0\n\ts_waitcnt lgkmcnt(0)" : "=s"(gen) : "s"(ws + OFF_GEN) : "memory");
            tag = (gen + 0x9E3779B97F4A7C15ull) * 0xBF58476D1CE4E5B9ull;
            tag ^= tag >> 31;
            tag *= 0x94D049BB133111EBull;
            tag ^= tag >> 29;
            if (lane_id() == 0) {
                old = __hip_atomic_exchange(claim + b * 8, tag, __ATOMIC_RELAXED, __HIP_MEMORY_SCOPE_AGENT);
                sh[1] = (u32)tag; sh[2] = (u32)(tag >> 32); sh[3] = (u32)gen; sh[4] = (u32)(gen >> 32);
            }
        }
    };
    auto settle = [&] {
        const int tid = wave * 64 + lane_id();
        if (tid == 0 && first) {
            u32 vb = (u32)b;
#pragma unroll 1
            for (int i = 0; i < 256 && old == tag; ++i) {
                vb = (vb + 1) & 255u;
                old = __hip_atomic_exchange(claim + vb * 8, tag, __ATOMIC_RELAXED, __HIP_MEMORY_SCOPE_AGENT);
            }
            sh[0] = vb;
        }
        LBAR();
        const u32 bc = __builtin_amdgcn_readfirstlane(sh[0]);
        asm volatile("s_cmp_eq_u32 %0, %1\n\ts_cbranch_scc1 Lgo%=\n\t"
                     "s_or_b32 s2, %0, 0x80000000\n\ts_mov_b64 s[0:1], %2\n\tv_mov_b32 v0, %3\n\ts_waitcnt vmcnt(0) lgkmcnt(0)\n\t"
                     "s_getpc_b64 s[4:5]\nLpc%=:\n\ts_sub_u32 s4, s4, Lpc%=-" KERNEL_SYM "\n\ts_subb_u32 s5, s5, 0\n\ts_setpc_b64 s[4:5]\nLgo%=:"
                     :: "s"(bc), "s"(b), "s"(__builtin_amdgcn_kernarg_segment_ptr()), "v"(tid) : "s2", "scc", "memory");
        tag = (u64)(u32)__builtin_amdgcn_readfirstlane(sh[1]) | ((u64)(u32)__builtin_amdgcn_readfirstlane(sh[2]) << 32);
    };
    if (b & 4) {
        const int wid = k * 4 + xg, h = (wid & 3) * 8 + (wid >> 4), par = (wid >> 2) & 1;
        const bool isqk = ((wid >> 3) & 1) == 0;
        const int lane = lane_id();
        u32x32 W[5];
        u32 wp = 0;
        u32x2 f0;
        u64 tg2 = 0;
        bool got = false;
        auto xtile = [&](int t) {
#pragma unroll
            for (int i = 0; i < 5; ++i)
                dma16_sc1(ws + OFF_XNP + (size_t)(par + 2 * t) * 40960 + (wave * 5 + i) * 1024 + lane * 16,
                          __builtin_amdgcn_readfirstlane((u32)(uintptr_t)(__attribute__((address_space(3))) char*)smem + xbuf(t) + (u32)(wave * 5 + i) * 1024));
        };
        load_weights<ORD_A>([&](int st) {
            if (st == 0) try_claim();
            if (st == LOOKST) {
                asm volatile("global_load_dwordx2 %0, %1, off sc1" : "=&v"(f0) : "v"(ws + OFF_SUM + xcc * 128u) : "memory");
                tg2 = (u64)(u32)__builtin_amdgcn_readfirstlane(sh[1]) | ((u64)(u32)__builtin_amdgcn_readfirstlane(sh[2]) << 32);
            }
            if (st == 3) {
                asm volatile("" : "+v"(f0));
                got = __all(f0[1] == cal_tag(tg2, f0[0]));
                if (got) { xtile(0); xtile(1); }
                wp = to_sgpr(f0[0]);
            }
        }, (isqk ? Wq : Wv) + h * DH_, isqk ? Wk + h * DH_ : Wo + (size_t)h * DH_ * D_, !isqk, smem, wave, lane,
           wave >> 2, 2 * (wave & 3), wave >> 2, 2 * (wave & 3) + 1, W);
        settle();
        const rsrc_t rs = mk_rsrc(ws);
        if (!got) { wp = wait_init(rs, tag, 0); xtile(0); xtile(1); }
        sh[16] = sh[17] = 0u;
        DRAIN();
        LBAR();
        PIN_W(W);
        worker_role(wave, wid, tag, wp, x, W, wg, wu, wd, ws, out, smem);
        __builtin_amdgcn_endpgm();
    }
    const int hl = ((k >> 1) ^ (xg >> 1)) & 1, j = (k >> 2) * 2 + (k & 1);
    if (CFG ? xg < 2 : j < 8) {
        const int h = CFG ? xg * 16 + (j >> 3) * 8 + hl * 4 + ((j >> 1) & 3) : xg * 8 + hl * 4 + (j >> 1);
        u32x32 W[5];
        auto hook = [&](int st) {
            if (st == 0) try_claim();
        };
        if ((j & 1) == 0) {
            load_weights<ORD_C>(hook, Wq + h * DH_, Wk + h * DH_, false, smem, wave, lane_id(), wave < 4, wave & 3, wave < 4, 4 + (wave & 3), W);
            settle();
            PIN_W(W);
            s_role(wave, h, xg, tag, mem0, W, ws, smem);
            __builtin_amdgcn_endpgm();
        }
        if (wave < 4) { load_weights<ORD_C>(hook, Wv + h * DH_, Wo + (size_t)h * DH_ * D_, true, smem, wave, lane_id(), 0, wave, 0, 4 + wave, W); PIN_W(W); }
        else { load_weights<ORD_C | (1 << 21)>(hook, Wv + h * DH_, Wo + (size_t)h * DH_ * D_, true, smem, wave, lane_id(), 1, 2 * (wave & 3), 1, 2 * (wave & 3) + 1, W); PIN_W(W); }
        settle();
        PIN_W(W);
        v_role(wave, h, xg, tag, mem0, W, ws, smem);
        __builtin_amdgcn_endpgm();
    }
    constexpr int NR = CFG ? 10 : 5, NS = CFG ? 6 : 3, NP = CFG ? 4 : 2;
    const int jr = CFG ? j : j - 8, hi = (CFG ? xg - 2 : xg) * 2 + hl;
    try_claim();
    init_role([&] { settle(); return tag; }, wave, jr < NR ? hi * NR + jr : jr == NR ? 40 + hi : 48 + hi * NP + jr - (NR + NS - NP), xcc, ((se ^ xcc) >> 1) & 1u, x, w_r, Wq, Wk, Wv, Wo, ws, smem);
    gen = (u64)sh[3] | ((u64)sh[4] << 32);
    tag = (u64)(u32)__builtin_amdgcn_readfirstlane(sh[1]) | ((u64)(u32)__builtin_amdgcn_readfirstlane(sh[2]) << 32);
    const int wi = hl * NS + jr - NR;
    const bool aux = wi == 2 * NS - 1;
    if (jr < NR) { if (wave < 6) r_role(wave, tile_of(xg, hl, jr), xg, wait_init(mk_rsrc(ws), tag, 1), ws, smem); }
    else if (!aux) {
        if (wave >= 4) { if (wi == 0 && xg == 0 && wave == 7) topk_role(tag, ws); return; }
        const u32 wp = wait_init(mk_rsrc(ws), tag, 1);
        LBAR();
        if (wave == 0 && wi == 0 && lane_id() == 0) __hip_atomic_fetch_add((u32*)(ws + OFF_CNT + 768), 1u, __ATOMIC_RELAXED, __HIP_MEMORY_SCOPE_AGENT);
        if (wave == 2 && wi) fill_role(wp, xg * 4 + wi - 1, ws);
        if (wave == 3 && xg == 3 && wi == 4) relay_role(wp, xg, ws);
        if (wave == 1 || (wave == 2 && !wi)) warm_role(wp, xg, wi - wave, ws, smem);
    }
    else if (wave == 1) {
        const u32 wp = wait_init(mk_rsrc(ws), tag, 1);
        if (lane_id() == 0) __hip_atomic_fetch_add((u32*)(ws + OFF_CNT + 768), 1u, __ATOMIC_RELAXED, __HIP_MEMORY_SCOPE_AGENT);
        lead_role(wp, xg, ws, smem);
    }
    else if (aux && xg == 2 && wave == 0) {
        const rsrc_t rs = mk_rsrc(ws);
        const int lane = lane_id();
#pragma unroll 1
        for (u32 spins = 0; spins < SPIN_CAP; ++spins) {
            CFENCE();
            bool ok = true;
#pragma unroll
            for (int q = 0; q < 4; ++q) {
                u32x2 f = ldx2(rs, (u32)(OFF_CLAIM + (q * 64 + lane) * 64));
                ok = ok && f[0] == (u32)tag && f[1] == (u32)(tag >> 32);
            }
            if (__all(ok)) break;
            __builtin_amdgcn_s_sleep(32);
        }
        if (lane == 0) __hip_atomic_store((u64*)(ws + OFF_GEN), gen + 1ull, __ATOMIC_RELAXED, __HIP_MEMORY_SCOPE_AGENT);
#pragma unroll
        for (int q = 0; q < 4; ++q) stx2(rs, (u32)(OFF_CLAIM + (q * 64 + lane) * 64), (u32x2){~(u32)tag, 0u});
    }
}

extern "C" void kernel_launch(void* const* d_in, const int* in_sizes, int n_in, void* d_out, int out_size,
                              void* d_ws, size_t ws_size, hipStream_t stream) {
    const u16* x = (const u16*)d_in[0];
    const u16* mem0 = (const u16*)d_in[1];
    const u16* Wq = (const u16*)d_in[2];
    const u16* Wk = (const u16*)d_in[3];
    const u16* Wv = (const u16*)d_in[4];
    const u16* Wo = (const u16*)d_in[5];
    const u16* w_r = (const u16*)d_in[8];
    const u16* w_gate = (const u16*)d_in[9];
    const u16* w_up = (const u16*)d_in[10];
    const u16* w_down = (const u16*)d_in[11];
    char* ws = (char*)d_ws;
    u16* out = (u16*)d_out;

    hipFuncSetAttribute((const void*)fused_kernel, hipFuncAttributeMaxDynamicSharedMemorySize, FUSED_LDS);
    fused_kernel<<<256, 512, FUSED_LDS, stream>>>(x, mem0, w_r, Wq, Wk, Wv, Wo, w_gate, w_up, w_down, ws, out);
}
```

```cpp
#define CFG 0
#include <hip/hip_runtime.h>
#include <hip/hip_bf16.h>
#include <stdint.h>

constexpr int S_ = 2048, D_ = 640, H_ = 32, DH_ = 128, NSEG = 64, SEGL = 32, FF_ = 1728, TOPC = 8;
constexpr int HD_ = H_ * DH_;
constexpr int NRED = 40;

typedef unsigned short u16;
typedef unsigned int u32;
typedef unsigned long long u64;
typedef __attribute__((ext_vector_type(8))) __bf16 bf16x8;
typedef __attribute__((ext_vector_type(16))) __bf16 bf16x16;
typedef __attribute__((ext_vector_type(4))) float f32x4;
typedef __attribute__((ext_vector_type(4))) unsigned int u32x4;
typedef __attribute__((ext_vector_type(8))) unsigned int u32x8;
typedef __attribute__((ext_vector_type(2))) unsigned int u32x2;

__device__ __forceinline__ float bf2f(u16 h) { return __uint_as_float(((u32)h) << 16); }
typedef __bf16 bf16x2_t __attribute__((ext_vector_type(2)));
typedef float f32x2_t __attribute__((ext_vector_type(2)));
__device__ __forceinline__ u16 f2bf(float f) {
    __bf16 h = (__bf16)f;
    return __builtin_bit_cast(u16, h);
}
__device__ __forceinline__ float bfr(float f) { return (float)(__bf16)f; }
__device__ __forceinline__ u32 pack2(float a, float b) {
    f32x2_t f = {a, b};
    bf16x2_t h = __builtin_convertvector(f, bf16x2_t);
    return __builtin_bit_cast(u32, h);
}
__device__ __forceinline__ bf16x8 as_bf(u32x4 v) { return __builtin_bit_cast(bf16x8, v); }
__device__ __forceinline__ f32x4 mfma16(u32x4 a, u32x4 b, f32x4 c) {
    return __builtin_amdgcn_mfma_f32_16x16x32_bf16(as_bf(a), as_bf(b), c, 0, 0, 0);
}
__device__ __forceinline__ f32x4 smf(u32x4 a, u32x4 b0, u32x4 b1, f32x4 c, int idx) {
    u32x8 b = {b0[0], b0[1], b0[2], b0[3], b1[0], b1[1], b1[2], b1[3]};
    return __builtin_amdgcn_smfmac_f32_16x16x64_bf16(as_bf(a), __builtin_bit_cast(bf16x16, b), c, idx, 0, 0);
}
__device__ __forceinline__ int dmap(int p) {
    int q = p & 15;
    return (p & ~15) | (q & 1) | ((q & 2) << 2) | ((q & 4) >> 1) | ((q & 8) >> 1);
}

constexpr size_t al256(size_t x) { return (x + 255) & ~(size_t)255; }
constexpr size_t OFF_XN = 0;
constexpr size_t OFF_XNP = OFF_XN + al256((size_t)S_ * D_ * 2);
constexpr size_t OFF_R = OFF_XNP + al256((size_t)S_ * D_ * 2);
constexpr size_t OFF_PX = OFF_R + al256((size_t)S_ * 4);
constexpr size_t OFF_PXM = OFF_PX + al256((size_t)NSEG * H_ * 2048);
constexpr size_t OFF_HB = OFF_PXM + al256((size_t)NSEG * 8 * 2048);
constexpr size_t OFF_UPD = OFF_HB + al256((size_t)8 * FF_ * 2);
constexpr size_t OFF_GEN = OFF_UPD + al256((size_t)8 * D_ * 2);
constexpr size_t OFF_CLAIM = OFF_GEN + 256;
constexpr size_t OFF_INI = OFF_CLAIM + 16384;
constexpr size_t OFF_SUM = OFF_INI + 2048;
constexpr size_t OFF_ERR = OFF_SUM + 3072;
constexpr size_t OFF_FA = OFF_ERR + 256;
constexpr size_t OFF_FB = OFF_FA + 4096;
constexpr size_t OFF_FP = OFF_FB + 4096;
constexpr size_t OFF_FZ = OFF_FP + 1024;
constexpr size_t OFF_RDY = OFF_FZ + 2048;
constexpr size_t OFF_IDX = OFF_RDY + 1024;
constexpr size_t OFF_CNT = OFF_IDX + 256;
constexpr size_t ZERO_BEGIN = OFF_ERR, ZERO_END = OFF_CNT + 1024;
constexpr size_t OFF_WIN = (size_t)12 << 20;
constexpr size_t WS_TOTAL = OFF_WIN + ((size_t)188 << 20);
static_assert(ZERO_END <= OFF_WIN && WS_TOTAL <= ((size_t)256 << 20), "ws");

constexpr u32 SLOT0 = 52u << 20;
constexpr u32 SL_P = 4 * 32768, SL_TS = 2 * 32768 + 1024, SL_PG = SL_TS + 2048 + 384;
constexpr u32 FAR_BX = 1u << 20;
constexpr u32 NEAR_QX = 59u << 20;
constexpr u32 NEAR_KX = NEAR_QX + NSEG * H_ * 8192;
static_assert(2 * ((size_t)NEAR_KX + (size_t)NSEG * H_ * 8192) + ((size_t)2 << 20) <= WS_TOTAL - OFF_WIN && SLOT0 + (64u << 20) <= 2 * NEAR_QX
              && 2 * ((size_t)FAR_BX + (size_t)NSEG * NRED * 32768) + ((size_t)2 << 20) <= WS_TOTAL - OFF_WIN, "far");
__device__ __forceinline__ u32 slot_off(u32 wp, int s) {
    u32 r, t;
    asm("s_lshr_b32 %0, %3, 1\n\ts_xor_b32 %0, %0, %2\n\ts_and_b32 %0, %0, 1\n\ts_lshl_b32 %0, %0, 13\n\ts_lshl_b32 %1, %3, 20\n\t"
        "s_add_u32 %0, %0, %1\n\ts_andn2_b32 %1, %2, 15\n\ts_add_u32 %0, %0, %1\n\ts_add_u32 %0, %0, %4" : "=&s"(r), "=&s"(t) : "s"(wp), "s"(s), "n"(SLOT0) : "scc");
    return r;
}
__device__ __forceinline__ u32 slot_z(u32 wp, u32 s) { return (wp & ~15u) + SLOT0 + (s << 20) + (((wp ^ (s >> 1)) & 1u) << 13); }
__device__ __forceinline__ u32 slot_v(u32 wp, u32 s) { return slot_z(wp, s) + ((((wp >> 3) ^ (s >> 5)) & 1u) << 18); }
__device__ __forceinline__ u32 far_v(u32 wp, u32 L) { return (wp & ~15u) + 2 * L + ((~(wp ^ (L >> 20)) & 1u) << 13); }
__device__ __forceinline__ u32 tab(u32 t, int s) { return __builtin_amdgcn_readlane(t, s); }
__device__ __forceinline__ u32 copy_off(u32 c, u32 s) { return ((c & 1u) << 14) | ((((c >> 1) ^ s) & 1u) << 12); }
__device__ __forceinline__ u32 z_off(u32 wp, u32 ct) {
    const u32 zb = ct >> 1, pg = zb < 16 ? zb + 16 : 6 + (zb & 1u) + (zb & 2u) * 4;
    return pg * 32768 + (((ct ^ (wp >> 1) ^ (pg >> 4)) & 1u) << 14);
}
constexpr int NCP = CFG ? 2 : 4;
__device__ __forceinline__ u32 put_off(u32 wp, u32 x0, u32 s) { return CFG ? copy_off(x0 ^ (wp >> 1), s) & 4096u : 0u; }
__device__ __forceinline__ u32 cpy(u32 c) { return CFG ? (c & 1u) * 16384u : (c & 1u) * 4096u + (c >> 1) * 16384u; }
__device__ __forceinline__ u32 far_off(u32 wp, u32 L) {
    u32 r, t;
    asm("s_lshr_b32 %0, %3, 20\n\ts_xnor_b32 %0, %0, %2\n\ts_and_b32 %0, %0, 1\n\ts_lshl_b32 %0, %0, 13\n\ts_lshl_b32 %1, %3, 1\n\t"
        "s_add_u32 %0, %0, %1\n\ts_andn2_b32 %1, %2, 15\n\ts_add_u32 %0, %0, %1" : "=&s"(r), "=&s"(t) : "s"(wp), "s"(L) : "scc");
    return r;
}
constexpr u32 SENT = 0xffbfffbfu;
constexpr u32 MARK = 0xffbeffbeu;
constexpr u32 OOB = 0x80000000u;
__device__ __forceinline__ u32 amap(u32 L) { return ((L >> 13) << 14) | (L & 8191u); }
__device__ __forceinline__ u32 win_off(const char* ws) {
    u64 a = (u64)(uintptr_t)ws;
    u64 w = (a + OFF_WIN + ((u64)2 << 20) - 1) & ~(((u64)2 << 20) - 1);
    return (u32)(w - a);
}

constexpr int AUX_SC1 = 16;
typedef __amdgpu_buffer_rsrc_t rsrc_t;
__device__ __forceinline__ rsrc_t mk_rsrc(const void* p) {
    return __builtin_amdgcn_make_buffer_rsrc((void*)p, 0, 0x7fffffff, 0x00020000);
}
__device__ __forceinline__ u32x4 ldx4(rsrc_t r, u32 off, u32 so = 0) { return __builtin_amdgcn_raw_buffer_load_b128(r, off, so, AUX_SC1); }
__device__ __forceinline__ u32x2 ldx2(rsrc_t r, u32 off, u32 so = 0) { return __builtin_amdgcn_raw_buffer_load_b64(r, off, so, AUX_SC1); }
__device__ __forceinline__ u32 ldx1(rsrc_t r, u32 off, u32 so = 0) { return __builtin_amdgcn_raw_buffer_load_b32(r, off, so, AUX_SC1); }
__device__ __forceinline__ void stx4(rsrc_t r, u32 off, u32x4 v, u32 so = 0) { __builtin_amdgcn_raw_buffer_store_b128(v, r, off + so, 0, AUX_SC1); }
__device__ __forceinline__ void stx2(rsrc_t r, u32 off, u32x2 v, u32 so = 0) { __builtin_amdgcn_raw_buffer_store_b64(v, r, off, so, AUX_SC1); }
__device__ __forceinline__ void stx1(rsrc_t r, u32 off, u32 v, u32 so = 0) { __builtin_amdgcn_raw_buffer_store_b32(v, r, off, so, AUX_SC1); }
#define DRAIN() asm volatile("s_waitcnt vmcnt(0)" ::: "memory")
#define CFENCE() asm volatile("" ::: "memory")
#define LBAR() asm volatile("s_waitcnt lgkmcnt(0)\n\ts_barrier" ::: "memory")

constexpr u32 L_MISC = 147456;
constexpr u32 SPIN_CAP = 1u << 18;
__device__ __forceinline__ int lane_id() {
    int l;
    asm volatile("v_mbcnt_lo_u32_b32 %0, -1, 0\n\tv_mbcnt_hi_u32_b32 %0, -1, %0" : "=v"(l));
    __builtin_assume(l >= 0 && l < 64);
    return l;
}

__device__ __forceinline__ float xmax16(float x) {
    u32 a = __float_as_uint(x);
    auto r = __builtin_amdgcn_permlane16_swap(a, a, false, false);
    return fmaxf(__uint_as_float(r[0]), __uint_as_float(r[1]));
}
__device__ __forceinline__ float xmax32(float x) {
    u32 a = __float_as_uint(x);
    auto r = __builtin_amdgcn_permlane32_swap(a, a, false, false);
    return fmaxf(__uint_as_float(r[0]), __uint_as_float(r[1]));
}
__device__ __forceinline__ float xadd16(float x) {
    u32 a = __float_as_uint(x);
    auto r = __builtin_amdgcn_permlane16_swap(a, a, false, false);
    return __uint_as_float(r[0]) + __uint_as_float(r[1]);
}
__device__ __forceinline__ float xadd32(float x) {
    u32 a = __float_as_uint(x);
    auto r = __builtin_amdgcn_permlane32_swap(a, a, false, false);
    return __uint_as_float(r[0]) + __uint_as_float(r[1]);
}
__device__ __forceinline__ u32 umax3(u32 a, u32 b, u32 c) { return max(max(a, b), c); }

__device__ __forceinline__ void wait_flags(rsrc_t rs, u32 off, bool act, u32 target, bool& dead, u32* errw, u32 code, int lane) {
    u32 spins = 0;
#pragma unroll 1
    while (!dead) {
        CFENCE();
        u32x4 f = ldx4(rs, off);
        bool ok = !act || ((f[0] >= target) & (f[1] >= target) & (f[2] >= target) & (f[3] >= target));
        if (__all(ok)) break;
        __builtin_amdgcn_s_sleep(2);
        if (++spins > SPIN_CAP) { if (lane == 0) atomicOr(errw, code); dead = true; }
        if ((spins & 63) == 0 && ldx1(rs, (u32)OFF_ERR) != 0u) dead = true;
    }
    CFENCE();
}
__device__ __forceinline__ void wait_word(rsrc_t rs, u32 off, bool act, u32 target, bool& dead, u32* errw, u32 code, int lane) {
    u32 spins = 0;
#pragma unroll 1
    while (!dead) {
        CFENCE();
        u32 f = ldx1(rs, off);
        if (__all(!act || f >= target)) break;
        __builtin_amdgcn_s_sleep(4);
        if (++spins > SPIN_CAP) { if (lane == 0) atomicOr(errw, code); dead = true; }
        if ((spins & 63) == 0 && ldx1(rs, (u32)OFF_ERR) != 0u) dead = true;
    }
    CFENCE();
}

__device__ __forceinline__ u32 cal_tag(u64 tag, u32 v) { return ((u32)tag ^ (u32)(tag >> 32)) + v * 0x9E3779B1u; }
__device__ __forceinline__ u32 to_sgpr(u32 x) {
    u32 r;
    asm volatile("s_nop 0\n\tv_readfirstlane_b32 %0, %1\n\ts_nop 4" : "=s"(r) : "v"(x));
    return r;
}
__device__ __forceinline__ u32 wp_of(const char* ws, u32 v) {
    v += __builtin_amdgcn_update_dpp(0u, v, 0xb1, 0xf, 0xf, false);
    v += __builtin_amdgcn_update_dpp(0u, v, 0x4e, 0xf, 0xf, false);
    v += __builtin_amdgcn_update_dpp(0u, v, 0x124, 0xf, 0xf, false);
    v += __builtin_amdgcn_update_dpp(0u, v, 0x128, 0xf, 0xf, false);
    const u32 r = __builtin_amdgcn_readlane(v, 48) + 0x0f0f0f0fu;
    return to_sgpr(win_off(ws) | ((r >> 7) & 1u) | ((r >> 14) & 2u) | ((r >> 21) & 4u) | ((r >> 28) & 8u));
}
__device__ __forceinline__ u32 sum_off(int level) {
    u32 xcc;
    asm volatile("s_getreg_b32 %0, hwreg(HW_REG_XCC_ID, 0, 3)" : "=s"(xcc));
    return (u32)(OFF_SUM + level * 1024) + xcc * 128u;
}
__device__ __forceinline__ u32 wait_init(rsrc_t rs, u64 tag, int level) {
    const u32 so = sum_off(level);
    u32x2 c = {0u, 0u};
#pragma unroll 1
    for (u32 spins = 0; spins < SPIN_CAP; ++spins) {
        CFENCE();
        c = ldx2(rs, 0u, so);
        if (__all(c[1] == cal_tag(tag, c[0]))) break;
        __builtin_amdgcn_s_sleep(6);
    }
    CFENCE();
    return to_sgpr(min(c[0], (u32)OFF_WIN + 0x20000fu));
}

#define STAMPS() do { } while (0)
#define STAMP(k) do { } while (0)
#define RSTAMP(k) do { } while (0)
#define STAMP2(k, r) do { } while (0)
#define FLUSH() do { } while (0)
#define LOGS() do { } while (0)
#define LOG() do { } while (0)
#define LOGOUT(cu) do { } while (0)
#define CFLUSH() do { } while (0)
#define XSTAMP(w, sl) do { } while (0)
#define WSTAMP(id) do { } while (0)
#define CSTAMP(id) do { } while (0)
#define NPROBE 2
constexpr u32 INI_ZERO_CHUNKS = (u32)((ZERO_END - ZERO_BEGIN) / 16);
static_assert(INI_ZERO_CHUNKS <= 8 * 256, "zero");

__device__ __forceinline__ float rsum16(float x) {
    x += __uint_as_float(__builtin_amdgcn_update_dpp(0u, __float_as_uint(x), 0xb1, 0xf, 0xf, false));
    x += __uint_as_float(__builtin_amdgcn_update_dpp(0u, __float_as_uint(x), 0x4e, 0xf, 0xf, false));
    x += __uint_as_float(__builtin_amdgcn_update_dpp(0u, __float_as_uint(x), 0x124, 0xf, 0xf, false));
    x += __uint_as_float(__builtin_amdgcn_update_dpp(0u, __float_as_uint(x), 0x128, 0xf, 0xf, false));
    return x;
}

__device__ __forceinline__ void ln_regs(u32x4 (&xv)[5]) {
    float sum = 0.f;
#pragma unroll
    for (int i = 0; i < 5; ++i)
#pragma unroll
        for (int j = 0; j < 4; ++j) sum += __uint_as_float(xv[i][j] << 16) + __uint_as_float(xv[i][j] & 0xffff0000u);
    const float mean = rsum16(sum) * (1.0f / D_);
    float vs = 0.f;
#pragma unroll
    for (int i = 0; i < 5; ++i)
#pragma unroll
        for (int j = 0; j < 4; ++j) {
            float d0 = __uint_as_float(xv[i][j] << 16) - mean, d1 = __uint_as_float(xv[i][j] & 0xffff0000u) - mean;
            vs += d0 * d0 + d1 * d1;
        }
    const float var = bfr(rsum16(vs) * (1.0f / D_));
    const float m_bf = bfr(mean);
    const float rden = 1.0f / bfr(sqrtf(bfr(var + 1e-05f)));
#pragma unroll
    for (int i = 0; i < 5; ++i)
#pragma unroll
        for (int j = 0; j < 4; ++j)
            xv[i][j] = pack2(bfr(__uint_as_float(xv[i][j] << 16) - m_bf) * rden, bfr(__uint_as_float(xv[i][j] & 0xffff0000u) - m_bf) * rden);
}
__device__ __forceinline__ u32x4 ln_perm(const u32x4 o, const int n) {
    const u32 s0 = (n & 1) ? o[0] : o[2], s1 = (n & 1) ? o[1] : o[3];
    const u32 t0 = __builtin_amdgcn_update_dpp(0u, s0, 0xb1, 0xf, 0xf, false), t1 = __builtin_amdgcn_update_dpp(0u, s1, 0xb1, 0xf, 0xf, false);
    return (n & 1) ? (u32x4){t0, o[2], t1, o[3]} : (u32x4){o[0], t0, o[1], t1};
}
__device__ __forceinline__ u32 img_off(const int row, const int n, const int i) {
    return (u32)((row >> 4) * 20480 + (4 * i + (n >> 2)) * 1024 + (n & 3) * 256 + (row & 15) * 16);
}
__device__ __forceinline__ void ln_load(const int row0, const int lane, const u16* __restrict__ x, const u16* __restrict__ w_r, u32x4 (&xv)[5], u32x4 (&wv)[5]) {
    const int n = lane & 15, row = row0 + (lane >> 4);
#pragma unroll
    for (int i = 0; i < 5; ++i) xv[i] = *(const u32x4*)(x + (size_t)row * D_ + (n + 16 * i) * 8);
#pragma unroll
    for (int i = 0; i < 5; ++i) wv[i] = *(const u32x4*)(w_r + (n + 16 * i) * 8);
}
__device__ __forceinline__ void ln_store(const int row0, const int lane, const u32x4 (&xv)[5], const u32x4 (&wv)[5], rsrc_t rs, const bool img = true) {
    const int n = lane & 15, row = row0 + (lane >> 4);
#pragma unroll
    for (int i = 0; i < 5; ++i) if (img) stx4(rs, (u32)OFF_XNP + img_off(row, n, i), ln_perm(xv[i], n));
    CFENCE();
    float dot = 0.f;
#pragma unroll
    for (int i = 0; i < 5; ++i) {
        stx4(rs, (u32)OFF_XN + (u32)(row * D_ + (n + 16 * i) * 8) * 2, xv[i]);
#pragma unroll
        for (int j = 0; j < 4; ++j)
            dot += __uint_as_float(xv[i][j] << 16) * __uint_as_float(wv[i][j] << 16) + __uint_as_float(xv[i][j] & 0xffff0000u) * __uint_as_float(wv[i][j] & 0xffff0000u);
    }
    dot = rsum16(dot);
    stx1(rs, n == 0 ? (u32)(OFF_R + row * 4) : OOB, __float_as_uint(bfr(dot)));
}
__device__ __forceinline__ u32x4 mk_srd(const char* ws) {
    const u64 a = (u64)(uintptr_t)ws;
    return (u32x4){(u32)a, (u32)(a >> 32), 0x7fffffffu, 0x00020000u};
}

template <typename F>
__device__ __forceinline__ u32 probe(F&& early, const int pi, const u32 xcc, const u32 hl, char* __restrict__ ws) {
    const u32x4 srd = mk_srd(ws);
    const u32 base = win_off(ws) + SLOT0 + (8u << 20) + 6u * 32768u + (u32)pi * 128u;
    const int lane = lane_id();
    const u32 zero = 0;
    asm volatile("buffer_atomic_or %0, %1, %2, %3 offen sc1\n\ts_waitcnt vmcnt(0)" :: "v"(zero), "v"(lane < 16 ? (u32)lane * 2048u : OOB), "s"(srd), "s"(base) : "memory");
    const u32 one = lane == 0 ? 0u : OOB;
    auto lat = [&](u32 q) {
        u64 t0, t1;
        asm volatile("s_memtime %0\n\tbuffer_atomic_or %2, %3, %4, %5 offen sc1\n\ts_waitcnt vmcnt(0)\n\ts_memtime %1\n\ts_waitcnt lgkmcnt(0)"
                     : "=&s"(t0), "=&s"(t1) : "v"(zero), "v"(one), "s"(srd), "s"(base + q * 2048u) : "memory");
        return (u32)t1 - (u32)t0;
    };
    const u32 q0 = (u32)pi & 11u, a0 = lat(q0), a1 = lat(q0 | 4u), a2 = lat(q0 ^ 8u), a3 = lat((q0 ^ 8u) | 4u), a4 = lat(q0 ^ 2u), a5 = lat((q0 ^ 2u) | 4u);
    const int d13 = (int)min(min(a0, a2), a4) - (int)min(min(a1, a3), a5), v13 = min(max(d13 >> 4, -7), 7);
    const u32 b13 = d13 > 0 ? 4u : 0u;
    const u32 flip = (xcc >> 2) | ((xcc & 3u) << 1) | (hl << 3);
    u32 votes = (u32)(7 + ((flip & 1u) ? -v13 : v13));
    early(votes);
    u32 best = ~0u;
#pragma unroll 1
    for (u32 i = 0; i < 8 * NPROBE; ++i) {
        const u32 c = (i + (u32)pi) & 7u, t = lat((c & 3u) | b13 | (c & 4u) * 2u);
        if ((u32)lane == c) best = min(best, t);
    }
#pragma unroll
    for (u32 i = 1; i < 4; ++i) {
        const u32 bit = 8u >> i;
        const int d = (int)best - __shfl_xor((int)best, (int)bit);
        int v = 0;
#pragma unroll
        for (u32 c = 0; c < 8; ++c) if (!(c & bit)) v += min(max(__builtin_amdgcn_readlane(d, c), -28), 28);
        v = (v + 8) >> 4;
        if ((flip >> i) & 1u) v = -v;
        votes |= (u32)(xcc >= 4 ? 7 : 7 + v) << (8 * i);
    }
    return votes;
}

typedef __attribute__((ext_vector_type(32))) unsigned int u32x32;
__device__ __forceinline__ u32x4 wfrag(const u32x32 (&W)[5], const int i) {
    return (u32x4){W[i >> 3][(i & 7) * 4], W[i >> 3][(i & 7) * 4 + 1], W[i >> 3][(i & 7) * 4 + 2], W[i >> 3][(i & 7) * 4 + 3]};
}
#define PIN_W(W) asm volatile("" : "+{v[16:47]}"(W[0]), "+{v[48:79]}"(W[1]), "+{v[80:111]}"(W[2]), "+{v[112:143]}"(W[3]), "+{v[144:175]}"(W[4]))
typedef __attribute__((ext_vector_type(4))) short s16x4;
typedef __attribute__((address_space(3))) s16x4* lds_s16x4p;
__device__ __forceinline__ u32x2 trd(u32 ldsaddr) {
    return __builtin_bit_cast(u32x2, __builtin_amdgcn_ds_read_tr16_b64_v4i16((lds_s16x4p)ldsaddr));
}
template <bool NT>
__device__ __forceinline__ void dma16(const void* g, u32 lds_base) {
    u32 sv;
    if (NT)
        asm volatile("s_mov_b32 %0, m0\n\ts_mov_b32 m0, %2\n\ts_nop 0\n\tglobal_load_lds_dwordx4 %1, off nt\n\ts_mov_b32 m0, %0"
                     : "=&s"(sv) : "v"(g), "s"(lds_base) : "memory");
    else
        asm volatile("s_mov_b32 %0, m0\n\ts_mov_b32 m0, %2\n\ts_nop 0\n\tglobal_load_lds_dwordx4 %1, off\n\ts_mov_b32 m0, %0"
                     : "=&s"(sv) : "v"(g), "s"(lds_base) : "memory");
}
__device__ __forceinline__ void dma16_sc1(const void* g, u32 lds_base) {
    u32 sv;
    asm volatile("s_mov_b32 %0, m0\n\ts_mov_b32 m0, %2\n\ts_nop 0\n\tglobal_load_lds_dwordx4 %1, off sc1\n\ts_mov_b32 m0, %0"
                 : "=&s"(sv) : "v"(g), "s"(lds_base) : "memory");
}
template <int ORD, typename F>
__device__ __forceinline__ void load_weights(F&& after, const u16* __restrict__ p0, const u16* __restrict__ p1, const bool wo1, char* smem,
                                             const int wave, const int lane, const int sa, const int ta, const int sb, const int tb,
                                             u32x32 (&W)[5]) {
    const u32 lbase = (u32)(uintptr_t)(__attribute__((address_space(3))) char*)smem;
    const int strm = wave >> 2;
    const bool wo = wo1 && strm == 1;
    const u16* src = strm ? p1 : p0;
    const u32 sstep = wo ? 128u : 128u * HD_;
    u32 so[8];
#pragma unroll
    for (int i = 0; i < 8; ++i) {
        const int j = (wave & 3) * 8 + i, r = 4 * j + (lane >> 4);
        const int f = (lane >> 4) | (((j >> 1) & 1) << 2);
        const int ch = (lane & 15) ^ (f << 1);
        const int e = (r & ~31) | (((r >> 2) & 1) << 4) | (((r >> 3) & 3) << 2) | (r & 3);
        so[i] = wo ? (u32)(e * D_ + ch * 8) : (u32)(dmap(r) * HD_ + ch * 8);
    }
    auto issue = [&](int st) {
        const u32 dst = __builtin_amdgcn_readfirstlane(lbase + (u32)((st & 1) * 65536 + strm * 32768 + (wave & 3) * 8192));
#pragma unroll
        for (int i = 0; i < 8; ++i) dma16<((ORD >> 20) & 1) != 0>(src + so[i] + (size_t)((ORD >> (4 * st)) & 15) * sstep, dst + i * 1024);
    };
    const int g = lane >> 4, q = (lane >> 2) & 3, p = lane & 3;
    const int f = q | ((g & 1) << 2);
    const u32 ra = lbase + (u32)(sa * 32768 + (8 * g + q) * 256 + (((ta ^ f) << 1) | (p >> 1)) * 16 + (p & 1) * 8);
    const u32 rb = lbase + (u32)(sb * 32768 + (8 * g + q) * 256 + (((tb ^ f) << 1) | (p >> 1)) * 16 + (p & 1) * 8);
    issue(0);
    issue(1);
#pragma unroll
    for (int st = 0; st < 5; ++st) {
        if (st < 4) asm volatile("s_waitcnt vmcnt(8)" ::: "memory"); else asm volatile("s_waitcnt vmcnt(0)" ::: "memory");
        LBAR();
#pragma unroll
        for (int k = 0; k < 4; ++k) {
            const u32 o = (u32)((st & 1) * 65536 + k * 8192);
            u32x2 a0 = trd(ra + o), a1 = trd(ra + o + 1024), b0 = trd(rb + o), b1 = trd(rb + o + 1024);
            const int ia = (((ORD >> (4 * st)) & 15) * 4 + k) * 4, ib = 80 + ia;
            if ((ORD >> 21) & 1) {
                u32x32& w = W[(ORD >> (4 * st)) & 15];
                w[k * 8] = a0[0]; w[k * 8 + 1] = b0[0]; w[k * 8 + 2] = a0[1]; w[k * 8 + 3] = b0[1];
                w[k * 8 + 4] = a1[0]; w[k * 8 + 5] = b1[0]; w[k * 8 + 6] = a1[1]; w[k * 8 + 7] = b1[1];
            } else {
                W[ia >> 5][ia & 31] = a0[0]; W[ia >> 5][(ia & 31) + 1] = a0[1]; W[ia >> 5][(ia & 31) + 2] = a1[0]; W[ia >> 5][(ia & 31) + 3] = a1[1];
                W[ib >> 5][ib & 31] = b0[0]; W[ib >> 5][(ib & 31) + 1] = b0[1]; W[ib >> 5][(ib & 31) + 2] = b1[0]; W[ib >> 5][(ib & 31) + 3] = b1[1];
            }
        }
        LBAR();
        after(st + 16);
        if (st + 2 < 5) issue(st + 2);
        after(st);
    }
}

__device__ __forceinline__ void early_lines(const int t0, const u64 wq, const u64 wk, const u64 wv, const u64 wo, const u32 lds) {
#pragma unroll
    for (int q = 0; q < 3; ++q) {
        const int t = t0 + q * 320, m = t / 180, r = (t - m * 180) * 64 + lane_id(), row = r / 18, c = r - row * 18;
        const u64 w = m == 0 ? wo : m == 1 ? wv : m == 2 ? wk : wq;
        const u32 blk = ((c < 16 ? (u32)(c >> 1) * 4u + 1u : 31u) - ((u32)w >> 8)) & 31u;
        u32 sv;
        if (t < 720)
            asm volatile("s_mov_b32 %0, m0\n\ts_mov_b32 m0, %3\n\ts_nop 0\n\tglobal_load_lds_dword %1, %2\n\ts_mov_b32 m0, %0"
                         : "=&s"(sv) : "v"((u32)row * 8192u + blk * 256u + (u32)(c & 1) * 128u), "s"(w), "s"(lds) : "memory");
    }
}
template <typename F>
__device__ __forceinline__ void init_role(F&& settle, const int wave, const int ii, const u32 xcc, const u32 hl, const u16* __restrict__ x, const u16* __restrict__ w_r,
                                          const u16* Wq, const u16* Wk, const u16* Wv, const u16* Wo, char* __restrict__ ws, char* smem) {
    const int lane = lane_id(), tid = wave * 64 + lane;
    const rsrc_t rs = mk_rsrc(ws);
    u32* cnt = (u32*)(smem + L_MISC + 128);
    if (tid < 3) cnt[tid] = 0u;
    u64 tag;
    auto arrive = [&](int level) {
        if (lane == 0 && __hip_atomic_fetch_add(cnt + level, 1u, __ATOMIC_RELAXED, __HIP_MEMORY_SCOPE_WORKGROUP) == 7u)
            stx2(rs, (u32)(OFF_INI + (level * 64 + ii) * 8), (u32x2){0u, cal_tag(tag, 0u)});
    };
    if (ii >= 48) {
        if (wave == 1 && ii < 63) {
            const u32 votes = probe([&](u32 v) {
                tag = settle();
                stx2(rs, lane == 0 ? (u32)(OFF_INI + ii * 8) : OOB, (u32x2){v, cal_tag(tag, v)});
                DRAIN();
                XSTAMP(192 + ii, 220);
            }, ii - 48, xcc, hl, ws);
            XSTAMP(192 + ii, 223);
            stx2(rs, lane == 0 ? (u32)(OFF_INI + (64 + ii) * 8) : OOB, (u32x2){votes, cal_tag(tag, votes)});
        } else tag = settle();
        if (wave == 2 && ii == 63) {
            u32 wp = 0;
#pragma unroll 1
            for (int level = 0; level < 3; ++level) {
                const bool act = level == 2 ? lane < 48 : lane < 63 && (lane >= 40 || level == 1);
                u32x2 c = {0u, 0u};
                u32 spins = 0;
#pragma unroll 1
                for (; spins < SPIN_CAP; ++spins) {
                    CFENCE();
                    c = ldx2(rs, (u32)(OFF_INI + level * 512 + lane * 8));
                    if (__all(!act || c[1] == cal_tag(tag, c[0]))) break;
                }
                if (spins == SPIN_CAP) break;
                if (level < 2) wp = wp_of(ws, lane < 63 ? c[0] : 0x07070707u);
                stx2(rs, lane < 8 ? (u32)(OFF_SUM + level * 1024 + lane * 128) : OOB, (u32x2){wp, cal_tag(tag, wp)});
                XSTAMP(192 + level, 229);
            }
        }
        if (ii < 63) LBAR();
        return;
    }
    u32x4 xv[5], wv[5];
    if (ii < 40) {
        early_lines(wave * 40 + ii, (u64)(uintptr_t)Wq, (u64)(uintptr_t)Wk, (u64)(uintptr_t)Wv, (u64)(uintptr_t)Wo, (u32)(uintptr_t)(__attribute__((address_space(3))) char*)smem + L_MISC + 512u);
        tag = settle();
        const u32 wp = wait_init(rs, tag, 0), st = (u32)(ii * 8 + wave), so = slot_off(wp, (int)(st >> 6)) + (st & 63u) * 16384u + (u32)lane * 16;
#pragma unroll
        for (int i = 0; i < 8; ++i) stx4(rs, so + i * 1024, (u32x4){SENT, SENT, SENT, SENT});
        DRAIN();
        XSTAMP(192 + ii, 225);
        arrive(1);
    } else {
        const int row = (ii - 40) * 32 + wave * 4;
        u32x4 xw[5];
        ln_load(row, lane, x, w_r, xv, wv);
#pragma unroll
        for (int i = 0; i < 5; ++i) xw[i] = *(const u32x4*)(x + (size_t)(row + 256 + (lane >> 4)) * D_ + ((lane & 15) + 16 * i) * 8);
        ln_regs(xv);
        tag = settle();
        const u32 i = (u32)((ii - 40) * 512 + tid);
        stx4(rs, i < INI_ZERO_CHUNKS ? (u32)ZERO_BEGIN + i * 16 : OOB, (u32x4){0u, 0u, 0u, 0u});
#pragma unroll
        for (int i = 0; i < 5; ++i) stx4(rs, (u32)OFF_XNP + img_off(row + (lane >> 4), lane & 15, i), ln_perm(xv[i], lane & 15));
        ln_regs(xw);
        DRAIN();
        XSTAMP(192 + ii, 220);
        arrive(0);
        ln_store(row + 256, lane, xw, wv, rs);
        asm volatile("s_waitcnt vmcnt(6)" ::: "memory");
        arrive(1);
        ln_store(row, lane, xv, wv, rs, false);
    }
    {
        const int row = 512 + (ii * 8 + wave) * 4;
        ln_load(row, lane, x, w_r, xv, wv);
        ln_regs(xv);
        ln_store(row, lane, xv, wv, rs);
    }
    DRAIN();
    XSTAMP(192 + ii, 222);
    arrive(2);
    LBAR();
}

#define NOWV 4
#define STP
#define STA
#define LGP
#define LGA
__device__ __forceinline__ void s_fetch(u32 vm, u32 xo, u32x4 rs, u32 pfo, u32 pfd, u32 xd) {
    asm volatile(
        "s_mov_b32 s92, m0\n\t"
        "s_mov_b32 m0, %[pfd]\n\t"
        "s_add_u32 s93, %[pfo], 0x1000\n\t"
        "s_nop 2\n\t"
        "buffer_load_dwordx4 %[vm], %[rs], %[pfo] offen offset:0 sc1 lds\n\t"
        "buffer_load_dwordx4 %[vm], %[rs], %[pfo] offen offset:1024 sc1 lds\n\t"
        "buffer_load_dwordx4 %[vm], %[rs], %[pfo] offen offset:2048 sc1 lds\n\t"
        "buffer_load_dwordx4 %[vm], %[rs], %[pfo] offen offset:3072 sc1 lds\n\t"
        "s_add_u32 m0, m0, 0x1000\n\t"
        "s_nop 0\n\t"
        "buffer_load_dwordx4 %[vm], %[rs], s93 offen offset:0 sc1 lds\n\t"
        "buffer_load_dwordx4 %[vm], %[rs], s93 offen offset:1024 sc1 lds\n\t"
        "buffer_load_dwordx4 %[vm], %[rs], s93 offen offset:2048 sc1 lds\n\t"
        "buffer_load_dwordx4 %[vm], %[rs], s93 offen offset:3072 sc1 lds\n\t"
        "s_mov_b32 m0, %[xd]\n\t"
        "s_nop 0\n\t"
        "buffer_load_dword %[xo], %[rs], 0 offen sc1 lds\n\t"
        "s_mov_b32 m0, s92\n\t"
        :
        : [vm] "v"(vm), [xo] "v"(xo), [rs] "s"(rs), [pfo] "s"(pfo), [pfd] "s"(pfd), [xd] "s"(xd)
        : "s92", "s93", "vcc", "scc", "memory");
}

__device__ __forceinline__ void s_k0(const u32x32 (&W)[5], u32& rnd, u32 va, u32 sidx, u32 ow, u32 lb STP) {
    asm volatile(
        "v_mov_b64 v[248:249], 0\n\t"
        "v_mov_b64 v[250:251], 0\n\t"
        "v_mov_b64 v[252:253], 0\n\t"
        "v_mov_b64 v[254:255], 0\n\t"
        "v_mov_b32 v200, 0xffbfffbf\n\t"
        "v_mov_b32 v206, %[lb]\n\t"
        "s_add_u32 %[rnd], %[rnd], 1\n\t"
        "s_setprio 1\n\t"
        "s_waitcnt vmcnt(0) lgkmcnt(0)\n\t"
        "s_barrier\n\t"
        "s_branch Lmk%=\n\t"
        "Ltop%=:\n\t"
        "v_mov_b64 v[248:249], 0\n\t"
        "v_mov_b64 v[250:251], 0\n\t"
        "v_mov_b64 v[252:253], 0\n\t"
        "v_mov_b64 v[254:255], 0\n\t"
        "v_mov_b32 v200, 0xffbfffbf\n\t"
        "v_mov_b32 v206, %[lb]\n\t"
        "s_add_u32 %[rnd], %[rnd], 1\n\t"
        "s_setprio 1\n\t"
        "Lmk%=:\n\t"
        "ds_write_b32 %[ow], v200\n\t"
        "ds_write_b32 %[ow], v200 offset:3072\n\t"
        "s_waitcnt lgkmcnt(0)\n\t"
        "s_barrier\n\t"
        "s_nop 15\n\t"
        "s_nop 15\n\t"
        "s_nop 3\n\t"
        "ds_read_b128 v[208:211], %[va] offset:0\n\t"
        "ds_read_b128 v[212:215], %[va] offset:1024\n\t"
        "ds_read_b128 v[216:219], %[va] offset:2048\n\t"
        "ds_read_b128 v[220:223], %[va] offset:3072\n\t"
        "ds_read_b128 v[224:227], %[va] offset:4096\n\t"
        "ds_read_b128 v[228:231], %[va] offset:5120\n\t"
        "ds_read_b128 v[232:235], %[va] offset:6144\n\t"
        "ds_read_b128 v[236:239], %[va] offset:7168\n\t"
        "ds_read_b128 v[240:243], %[va] offset:8192\n\t"
        "ds_read_b128 v[244:247], %[va] offset:10240\n\t"
        "s_waitcnt lgkmcnt(0)\n\t"
        "v_smfmac_f32_16x16x64_bf16 v[248:251], v[208:211], v[16:23], %[sidx]\n\t"
        "v_smfmac_f32_16x16x64_bf16 v[248:251], v[212:215], v[24:31], %[sidx]\n\t"
        "v_smfmac_f32_16x16x64_bf16 v[248:251], v[216:219], v[32:39], %[sidx]\n\t"
        "v_smfmac_f32_16x16x64_bf16 v[248:251], v[220:223], v[40:47], %[sidx]\n\t"
        "v_smfmac_f32_16x16x64_bf16 v[248:251], v[224:227], v[48:55], %[sidx]\n\t"
        "v_smfmac_f32_16x16x64_bf16 v[248:251], v[228:231], v[56:63], %[sidx]\n\t"
        "v_smfmac_f32_16x16x64_bf16 v[248:251], v[232:235], v[64:71], %[sidx]\n\t"
        "v_smfmac_f32_16x16x64_bf16 v[248:251], v[236:239], v[72:79], %[sidx]\n\t"
        "v_smfmac_f32_16x16x64_bf16 v[248:251], v[240:243], v[80:87], %[sidx]\n\t"
        "v_smfmac_f32_16x16x64_bf16 v[248:251], v[244:247], v[88:95], %[sidx]\n\t"
        "v_smfmac_f32_16x16x64_bf16 v[252:255], v[208:211], v[96:103], %[sidx]\n\t"
        "v_smfmac_f32_16x16x64_bf16 v[252:255], v[212:215], v[104:111], %[sidx]\n\t"
        "v_smfmac_f32_16x16x64_bf16 v[252:255], v[216:219], v[112:119], %[sidx]\n\t"
        "v_add_f32 v248, v248, v250\n\t"
        "v_smfmac_f32_16x16x64_bf16 v[252:255], v[220:223], v[120:127], %[sidx]\n\t"
        "v_add_f32 v249, v249, v251\n\t"
        "v_smfmac_f32_16x16x64_bf16 v[252:255], v[224:227], v[128:135], %[sidx]\n\t"
        "v_cvt_pk_bf16_f32 v200, v248, v249\n\t"
        "v_smfmac_f32_16x16x64_bf16 v[252:255], v[228:231], v[136:143], %[sidx]\n\t"
        "ds_write_b32 %[ow], v200\n\t"
        "v_smfmac_f32_16x16x64_bf16 v[252:255], v[232:235], v[144:151], %[sidx]\n\t"
        "v_cmp_u_f32 vcc, v248, v249\n\t"
        "v_smfmac_f32_16x16x64_bf16 v[252:255], v[236:239], v[152:159], %[sidx]\n\t"
        "v_smfmac_f32_16x16x64_bf16 v[252:255], v[240:243], v[160:167], %[sidx]\n\t"
        "v_smfmac_f32_16x16x64_bf16 v[252:255], v[244:247], v[168:175], %[sidx]\n\t"
        "s_nop 6\n\t"
        "s_cbranch_vccnz Lnan%=\n\t"
        "Lok%=:\n\t"
        "v_add_f32 v252, v252, v254\n\t"
        "v_add_f32 v253, v253, v255\n\t"
        "v_cvt_pk_bf16_f32 v201, v252, v253\n\t"
        "ds_write_b32 %[ow], v201 offset:3072\n\t"
        "s_barrier\n\t"
        "s_setprio 0\n\t"
        "ds_read_b32 v207, v206 offset:25104\n\t"
        "s_waitcnt lgkmcnt(0)\n\t"
        "v_cmp_eq_u32 vcc, %[rnd], v207\n\t"
        "s_barrier\n\t"
        "s_cbranch_vccnz Ltop%=\n\t"
        "s_branch Lend%=\n\t"
        "Lnan%=:\n\t"
        "v_max_u32 v207, v208, v209\n\t"
        "v_max3_u32 v207, v207, v210, v211\n\t"
        "v_max3_u32 v207, v207, v212, v213\n\t"
        "v_max3_u32 v207, v207, v214, v215\n\t"
        "v_max3_u32 v207, v207, v216, v217\n\t"
        "v_max3_u32 v207, v207, v218, v219\n\t"
        "v_max3_u32 v207, v207, v220, v221\n\t"
        "v_max3_u32 v207, v207, v222, v223\n\t"
        "v_max3_u32 v207, v207, v224, v225\n\t"
        "v_max3_u32 v207, v207, v226, v227\n\t"
        "v_max3_u32 v207, v207, v228, v229\n\t"
        "v_max3_u32 v207, v207, v230, v231\n\t"
        "v_max3_u32 v207, v207, v232, v233\n\t"
        "v_max3_u32 v207, v207, v234, v235\n\t"
        "v_max3_u32 v207, v207, v236, v237\n\t"
        "v_max3_u32 v207, v207, v238, v239\n\t"
        "v_max3_u32 v207, v207, v240, v241\n\t"
        "v_max3_u32 v207, v207, v242, v243\n\t"
        "v_max3_u32 v207, v207, v244, v245\n\t"
        "v_max3_u32 v207, v207, v246, v247\n\t"
        "v_cmp_eq_u32 vcc, 0xffbfffbf, v207\n\t"
        "v_mov_b32 v207, %[rnd]\n\t"
        "s_cbranch_vccz Lok%=\n\t"
        "ds_write_b32 v206, v207 offset:25104\n\t"
        "s_waitcnt lgkmcnt(0)\n\t"
        "s_barrier\n\t"
        "s_barrier\n\t"
        "s_branch Ltop%=\n\t"
        "Lend%=:\n\t"
        : [rnd] "+&s"(rnd)
        : [w0] "{v[16:47]}"(W[0]), [w1] "{v[48:79]}"(W[1]), [w2] "{v[80:111]}"(W[2]), [w3] "{v[112:143]}"(W[3]), [w4] "{v[144:175]}"(W[4]), [va] "v"(va), [sidx] "v"(sidx), [ow] "v"(ow), [lb] "s"(lb)
        : "v200", "v201", "v202", "v203", "v204", "v205", "v206", "v207", "v208", "v209", "v210", "v211", "v212", "v213", "v214", "v215", "v216", "v217", "v218", "v219", "v220", "v221", "v222", "v223", "v224", "v225", "v226", "v227", "v228", "v229", "v230", "v231", "v232", "v233", "v234", "v235", "v236", "v237", "v238", "v239", "v240", "v241", "v242", "v243", "v244", "v245", "v246", "v247", "v248", "v249", "v250", "v251", "v252", "v253", "v254", "v255", "vcc", "scc", "memory");
}

__device__ __forceinline__ void s_k1(const u32x32 (&W)[5], u32& rnd, u32 va, u32 sidx, u32 ow, u32 lb, u32x4& p0, u32& p1, u32 ka, u32 qa, u32 ma, u32 ktr, u32 o0, u32 o1, float ce, u32x4 rs STP) {
    asm volatile(
        "v_mov_b64 v[248:249], 0\n\t"
        "v_mov_b64 v[250:251], 0\n\t"
        "v_mov_b64 v[252:253], 0\n\t"
        "v_mov_b64 v[254:255], 0\n\t"
        "v_mov_b32 v200, 0xffbfffbf\n\t"
        "v_mov_b32 v206, %[lb]\n\t"
        "s_add_u32 %[rnd], %[rnd], 1\n\t"
        "s_setprio 1\n\t"
        "s_waitcnt vmcnt(0) lgkmcnt(0)\n\t"
        "s_barrier\n\t"
        "buffer_store_dwordx4 v[192:195], %[o0], %[rs], 0 offen sc1\n\t"
        "buffer_store_dword v196, %[o1], %[rs], 0 offen sc1\n\t"
        "s_branch Lmk%=\n\t"
        "Ltop%=:\n\t"
        "v_mov_b64 v[248:249], 0\n\t"
        "v_mov_b64 v[250:251], 0\n\t"
        "v_mov_b64 v[252:253], 0\n\t"
        "v_mov_b64 v[254:255], 0\n\t"
        "v_mov_b32 v200, 0xffbfffbf\n\t"
        "v_mov_b32 v206, %[lb]\n\t"
        "s_add_u32 %[rnd], %[rnd], 1\n\t"
        "s_setprio 1\n\t"
        "Lmk%=:\n\t"
        "ds_write_b32 %[ow], v200\n\t"
        "ds_write_b32 %[ow], v200 offset:3072\n\t"
        "s_waitcnt lgkmcnt(0)\n\t"
        "s_barrier\n\t"
        "s_nop 15\n\t"
        "s_nop 15\n\t"
        "s_nop 3\n\t"
        "ds_read_b128 v[208:211], %[va] offset:0\n\t"
        "ds_read_b128 v[212:215], %[va] offset:1024\n\t"
        "ds_read_b128 v[216:219], %[va] offset:2048\n\t"
        "ds_read_b128 v[220:223], %[va] offset:3072\n\t"
        "ds_read_b128 v[224:227], %[va] offset:4096\n\t"
        "ds_read_b128 v[228:231], %[va] offset:5120\n\t"
        "ds_read_b128 v[232:235], %[va] offset:6144\n\t"
        "ds_read_b128 v[236:239], %[va] offset:7168\n\t"
        "ds_read_b128 v[240:243], %[va] offset:8192\n\t"
        "ds_read_b128 v[244:247], %[va] offset:10240\n\t"
        "s_waitcnt lgkmcnt(0)\n\t"
        "v_smfmac_f32_16x16x64_bf16 v[248:251], v[208:211], v[16:23], %[sidx]\n\t"
        "v_smfmac_f32_16x16x64_bf16 v[248:251], v[212:215], v[24:31], %[sidx]\n\t"
        "v_smfmac_f32_16x16x64_bf16 v[248:251], v[216:219], v[32:39], %[sidx]\n\t"
        "v_smfmac_f32_16x16x64_bf16 v[248:251], v[220:223], v[40:47], %[sidx]\n\t"
        "v_smfmac_f32_16x16x64_bf16 v[248:251], v[224:227], v[48:55], %[sidx]\n\t"
        "v_smfmac_f32_16x16x64_bf16 v[248:251], v[228:231], v[56:63], %[sidx]\n\t"
        "v_smfmac_f32_16x16x64_bf16 v[248:251], v[232:235], v[64:71], %[sidx]\n\t"
        "v_smfmac_f32_16x16x64_bf16 v[248:251], v[236:239], v[72:79], %[sidx]\n\t"
        "v_smfmac_f32_16x16x64_bf16 v[248:251], v[240:243], v[80:87], %[sidx]\n\t"
        "v_smfmac_f32_16x16x64_bf16 v[248:251], v[244:247], v[88:95], %[sidx]\n\t"
        "v_smfmac_f32_16x16x64_bf16 v[252:255], v[208:211], v[96:103], %[sidx]\n\t"
        "v_smfmac_f32_16x16x64_bf16 v[252:255], v[212:215], v[104:111], %[sidx]\n\t"
        "v_smfmac_f32_16x16x64_bf16 v[252:255], v[216:219], v[112:119], %[sidx]\n\t"
        "v_add_f32 v248, v248, v250\n\t"
        "v_smfmac_f32_16x16x64_bf16 v[252:255], v[220:223], v[120:127], %[sidx]\n\t"
        "v_add_f32 v249, v249, v251\n\t"
        "v_smfmac_f32_16x16x64_bf16 v[252:255], v[224:227], v[128:135], %[sidx]\n\t"
        "v_cvt_pk_bf16_f32 v200, v248, v249\n\t"
        "v_smfmac_f32_16x16x64_bf16 v[252:255], v[228:231], v[136:143], %[sidx]\n\t"
        "ds_write_b32 %[ow], v200\n\t"
        "v_smfmac_f32_16x16x64_bf16 v[252:255], v[232:235], v[144:151], %[sidx]\n\t"
        "v_cmp_u_f32 vcc, v248, v249\n\t"
        "v_smfmac_f32_16x16x64_bf16 v[252:255], v[236:239], v[152:159], %[sidx]\n\t"
        "v_smfmac_f32_16x16x64_bf16 v[252:255], v[240:243], v[160:167], %[sidx]\n\t"
        "v_smfmac_f32_16x16x64_bf16 v[252:255], v[244:247], v[168:175], %[sidx]\n\t"
        "s_nop 6\n\t"
        "s_cbranch_vccnz Lnan%=\n\t"
        "Lok%=:\n\t"
        "v_add_f32 v252, v252, v254\n\t"
        "v_add_f32 v253, v253, v255\n\t"
        "v_cvt_pk_bf16_f32 v201, v252, v253\n\t"
        "ds_write_b32 %[ow], v201 offset:3072\n\t"
        "s_barrier\n\t"
        "s_sleep 1\n\t"
        "ds_read_b128 v[240:243], %[ma] offset:0\n\t"
        "ds_read_b128 v[244:247], %[ma] offset:1024\n\t"
        "ds_read_b128 v[176:179], %[qa] offset:0\n\t"
        "ds_read_b128 v[180:183], %[qa] offset:1024\n\t"
        "ds_read_b128 v[184:187], %[qa] offset:2048\n\t"
        "ds_read_b128 v[188:191], %[qa] offset:3072\n\t"
        "ds_read_b128 v[208:211], %[ka] offset:0\n\t"
        "ds_read_b128 v[212:215], %[ka] offset:1024\n\t"
        "ds_read_b128 v[216:219], %[ka] offset:2048\n\t"
        "ds_read_b128 v[220:223], %[ka] offset:3072\n\t"
        "ds_read_b128 v[224:227], %[ka] offset:4096\n\t"
        "ds_read_b128 v[228:231], %[ka] offset:5120\n\t"
        "ds_read_b128 v[232:235], %[ka] offset:6144\n\t"
        "ds_read_b128 v[236:239], %[ka] offset:7168\n\t"
        "s_waitcnt lgkmcnt(7)\n\t"
        "v_mfma_f32_16x16x32_bf16 v[240:243], v[208:211], v[176:179], v[240:243]\n\t"
        "s_waitcnt lgkmcnt(6)\n\t"
        "v_mfma_f32_16x16x32_bf16 v[240:243], v[212:215], v[180:183], v[240:243]\n\t"
        "s_waitcnt lgkmcnt(5)\n\t"
        "v_mfma_f32_16x16x32_bf16 v[240:243], v[216:219], v[184:187], v[240:243]\n\t"
        "s_waitcnt lgkmcnt(4)\n\t"
        "v_mfma_f32_16x16x32_bf16 v[240:243], v[220:223], v[188:191], v[240:243]\n\t"
        "s_waitcnt lgkmcnt(3)\n\t"
        "v_mfma_f32_16x16x32_bf16 v[244:247], v[224:227], v[176:179], v[244:247]\n\t"
        "s_waitcnt lgkmcnt(2)\n\t"
        "v_mfma_f32_16x16x32_bf16 v[244:247], v[228:231], v[180:183], v[244:247]\n\t"
        "s_waitcnt lgkmcnt(1)\n\t"
        "v_mfma_f32_16x16x32_bf16 v[244:247], v[232:235], v[184:187], v[244:247]\n\t"
        "s_waitcnt lgkmcnt(0)\n\t"
        "v_mfma_f32_16x16x32_bf16 v[244:247], v[236:239], v[188:191], v[244:247]\n\t"
        "s_setprio 0\n\t"
        "ds_read_b32 v207, v206 offset:25104\n\t"
        "s_waitcnt lgkmcnt(0)\n\t"
        "v_cmp_eq_u32 vcc, %[rnd], v207\n\t"
        "s_barrier\n\t"
        "s_cbranch_vccnz Ltop%=\n\t"
        "ds_read_b64_tr_b16 v[208:209], %[ktr] offset:0\n\t"
        "ds_read_b64_tr_b16 v[210:211], %[ktr] offset:768\n\t"
        "ds_read_b64_tr_b16 v[212:213], %[ktr] offset:1536\n\t"
        "ds_read_b64_tr_b16 v[214:215], %[ktr] offset:2304\n\t"
        "ds_read_b64_tr_b16 v[216:217], %[ktr] offset:3072\n\t"
        "ds_read_b64_tr_b16 v[218:219], %[ktr] offset:3840\n\t"
        "ds_read_b64_tr_b16 v[220:221], %[ktr] offset:4608\n\t"
        "ds_read_b64_tr_b16 v[222:223], %[ktr] offset:5376\n\t"
        "s_waitcnt lgkmcnt(6)\n\t"
        "v_mfma_f32_16x16x32_bf16 v[248:251], v[208:211], v[176:179], 0\n\t"
        "s_waitcnt lgkmcnt(4)\n\t"
        "v_mfma_f32_16x16x32_bf16 v[248:251], v[212:215], v[180:183], v[248:251]\n\t"
        "s_waitcnt lgkmcnt(2)\n\t"
        "v_mfma_f32_16x16x32_bf16 v[248:251], v[216:219], v[184:187], v[248:251]\n\t"
        "s_waitcnt lgkmcnt(0)\n\t"
        "v_mfma_f32_16x16x32_bf16 v[248:251], v[220:223], v[188:191], v[248:251]\n\t"
        "v_max3_f32 v253, v240, v241, v242\n\t"
        "v_max3_f32 v253, v253, v243, v244\n\t"
        "v_max3_f32 v253, v253, v245, v246\n\t"
        "v_max_f32 v253, v253, v247\n\t"
        "s_nop 3\n\t"
        "v_mul_f32 v248, %[ce], v248\n\t"
        "v_mul_f32 v249, %[ce], v249\n\t"
        "v_max3_f32 v253, v253, v248, v249\n\t"
        "v_mov_b32 v254, v253\n\t"
        "s_nop 1\n\t"
        "v_permlane16_swap_b32 v253, v254\n\t"
        "v_max_f32 v253, v253, v254\n\t"
        "v_mov_b32 v254, v253\n\t"
        "s_nop 1\n\t"
        "v_permlane32_swap_b32 v253, v254\n\t"
        "v_max_f32 v253, v253, v254\n\t"
        "v_sub_f32 v240, v240, v253\n\t"
        "v_sub_f32 v241, v241, v253\n\t"
        "v_sub_f32 v242, v242, v253\n\t"
        "v_sub_f32 v243, v243, v253\n\t"
        "v_sub_f32 v244, v244, v253\n\t"
        "v_sub_f32 v245, v245, v253\n\t"
        "v_sub_f32 v246, v246, v253\n\t"
        "v_sub_f32 v247, v247, v253\n\t"
        "v_sub_f32 v248, v248, v253\n\t"
        "v_sub_f32 v249, v249, v253\n\t"
        "v_exp_f32 v240, v240\n\t"
        "v_exp_f32 v241, v241\n\t"
        "v_exp_f32 v242, v242\n\t"
        "v_exp_f32 v243, v243\n\t"
        "v_exp_f32 v244, v244\n\t"
        "v_exp_f32 v245, v245\n\t"
        "v_exp_f32 v246, v246\n\t"
        "v_exp_f32 v247, v247\n\t"
        "v_exp_f32 v248, v248\n\t"
        "v_exp_f32 v249, v249\n\t"
        "v_pk_add_f32 v[252:253], v[240:241], v[242:243]\n\t"
        "v_pk_add_f32 v[254:255], v[244:245], v[246:247]\n\t"
        "v_pk_add_f32 v[252:253], v[252:253], v[248:249]\n\t"
        "v_pk_add_f32 v[252:253], v[252:253], v[254:255]\n\t"
        "v_add_f32 v252, v252, v253\n\t"
        "v_mov_b32 v253, v252\n\t"
        "s_nop 1\n\t"
        "v_permlane16_swap_b32 v252, v253\n\t"
        "v_add_f32 v252, v252, v253\n\t"
        "v_mov_b32 v253, v252\n\t"
        "s_nop 1\n\t"
        "v_permlane32_swap_b32 v252, v253\n\t"
        "v_add_f32 v252, v252, v253\n\t"
        "v_rcp_f32 v252, v252\n\t"
        "s_nop 0\n\t"
        "v_pk_mul_f32 v[240:241], v[240:241], v[252:253] op_sel_hi:[1,0]\n\t"
        "v_pk_mul_f32 v[242:243], v[242:243], v[252:253] op_sel_hi:[1,0]\n\t"
        "v_pk_mul_f32 v[244:245], v[244:245], v[252:253] op_sel_hi:[1,0]\n\t"
        "v_pk_mul_f32 v[246:247], v[246:247], v[252:253] op_sel_hi:[1,0]\n\t"
        "v_pk_mul_f32 v[248:249], v[248:249], v[252:253] op_sel_hi:[1,0]\n\t"
        "v_cvt_pk_bf16_f32 v192, v240, v241\n\t"
        "v_cvt_pk_bf16_f32 v193, v242, v243\n\t"
        "v_cvt_pk_bf16_f32 v194, v244, v245\n\t"
        "v_cvt_pk_bf16_f32 v195, v246, v247\n\t"
        "v_cvt_pk_bf16_f32 v196, v248, v249\n\t"
        "s_branch Lend%=\n\t"
        "Lnan%=:\n\t"
        "v_max_u32 v207, v208, v209\n\t"
        "v_max3_u32 v207, v207, v210, v211\n\t"
        "v_max3_u32 v207, v207, v212, v213\n\t"
        "v_max3_u32 v207, v207, v214, v215\n\t"
        "v_max3_u32 v207, v207, v216, v217\n\t"
        "v_max3_u32 v207, v207, v218, v219\n\t"
        "v_max3_u32 v207, v207, v220, v221\n\t"
        "v_max3_u32 v207, v207, v222, v223\n\t"
        "v_max3_u32 v207, v207, v224, v225\n\t"
        "v_max3_u32 v207, v207, v226, v227\n\t"
        "v_max3_u32 v207, v207, v228, v229\n\t"
        "v_max3_u32 v207, v207, v230, v231\n\t"
        "v_max3_u32 v207, v207, v232, v233\n\t"
        "v_max3_u32 v207, v207, v234, v235\n\t"
        "v_max3_u32 v207, v207, v236, v237\n\t"
        "v_max3_u32 v207, v207, v238, v239\n\t"
        "v_max3_u32 v207, v207, v240, v241\n\t"
        "v_max3_u32 v207, v207, v242, v243\n\t"
        "v_max3_u32 v207, v207, v244, v245\n\t"
        "v_max3_u32 v207, v207, v246, v247\n\t"
        "v_cmp_eq_u32 vcc, 0xffbfffbf, v207\n\t"
        "v_mov_b32 v207, %[rnd]\n\t"
        "s_cbranch_vccz Lok%=\n\t"
        "ds_write_b32 v206, v207 offset:25104\n\t"
        "s_waitcnt lgkmcnt(0)\n\t"
        "s_barrier\n\t"
        "s_barrier\n\t"
        "s_branch Ltop%=\n\t"
        "Lend%=:\n\t"
        : [rnd] "+&s"(rnd), [p0] "+{v[192:195]}"(p0), [p1] "+{v196}"(p1)
        : [w0] "{v[16:47]}"(W[0]), [w1] "{v[48:79]}"(W[1]), [w2] "{v[80:111]}"(W[2]), [w3] "{v[112:143]}"(W[3]), [w4] "{v[144:175]}"(W[4]), [va] "v"(va), [sidx] "v"(sidx), [ow] "v"(ow), [lb] "s"(lb), [ka] "v"(ka), [qa] "v"(qa), [ma] "v"(ma), [ktr] "v"(ktr), [o0] "v"(o0), [o1] "v"(o1), [ce] "s"(ce), [rs] "s"(rs)
        : "v176", "v177", "v178", "v179", "v180", "v181", "v182", "v183", "v184", "v185", "v186", "v187", "v188", "v189", "v190", "v191", "v200", "v201", "v202", "v203", "v204", "v205", "v206", "v207", "v208", "v209", "v210", "v211", "v212", "v213", "v214", "v215", "v216", "v217", "v218", "v219", "v220", "v221", "v222", "v223", "v224", "v225", "v226", "v227", "v228", "v229", "v230", "v231", "v232", "v233", "v234", "v235", "v236", "v237", "v238", "v239", "v240", "v241", "v242", "v243", "v244", "v245", "v246", "v247", "v248", "v249", "v250", "v251", "v252", "v253", "v254", "v255", "vcc", "scc", "memory");
}

__device__ __forceinline__ void s_k2(const u32x32 (&W)[5], u32& rnd, u32 va, u32 sidx, u32 ow, u32 lb) {
    asm volatile(
        "v_mov_b64 v[248:249], 0\n\t"
        "v_mov_b64 v[250:251], 0\n\t"
        "v_mov_b64 v[252:253], 0\n\t"
        "v_mov_b64 v[254:255], 0\n\t"
        "v_mov_b32 v200, 0xffbfffbf\n\t"
        "v_mov_b32 v206, %[lb]\n\t"
        "s_add_u32 %[rnd], %[rnd], 1\n\t"
        "s_setprio 1\n\t"
        "s_waitcnt vmcnt(0) lgkmcnt(0)\n\t"
        "s_barrier\n\t"
        "s_branch Lmk%=\n\t"
        "Ltop%=:\n\t"
        "v_mov_b64 v[248:249], 0\n\t"
        "v_mov_b64 v[250:251], 0\n\t"
        "v_mov_b64 v[252:253], 0\n\t"
        "v_mov_b64 v[254:255], 0\n\t"
        "v_mov_b32 v200, 0xffbfffbf\n\t"
        "v_mov_b32 v206, %[lb]\n\t"
        "s_add_u32 %[rnd], %[rnd], 1\n\t"
        "s_setprio 1\n\t"
        "Lmk%=:\n\t"
        "ds_write_b32 %[ow], v200\n\t"
        "ds_write_b32 %[ow], v200 offset:3072\n\t"
        "s_waitcnt lgkmcnt(0)\n\t"
        "s_barrier\n\t"
        "ds_read_b128 v[208:211], %[va] offset:0\n\t"
        "ds_read_b128 v[212:215], %[va] offset:1024\n\t"
        "ds_read_b128 v[216:219], %[va] offset:2048\n\t"
        "ds_read_b128 v[220:223], %[va] offset:3072\n\t"
        "ds_read_b128 v[224:227], %[va] offset:4096\n\t"
        "ds_read_b128 v[228:231], %[va] offset:5120\n\t"
        "ds_read_b128 v[232:235], %[va] offset:6144\n\t"
        "ds_read_b128 v[236:239], %[va] offset:7168\n\t"
        "ds_read_b128 v[240:243], %[va] offset:8192\n\t"
        "ds_read_b128 v[244:247], %[va] offset:10240\n\t"
        "s_waitcnt lgkmcnt(9)\n\t"
        "v_smfmac_f32_16x16x64_bf16 v[248:251], v[208:211], v[16:23], %[sidx]\n\t"
        "s_waitcnt lgkmcnt(8)\n\t"
        "v_smfmac_f32_16x16x64_bf16 v[248:251], v[212:215], v[24:31], %[sidx]\n\t"
        "s_waitcnt lgkmcnt(7)\n\t"
        "v_smfmac_f32_16x16x64_bf16 v[248:251], v[216:219], v[32:39], %[sidx]\n\t"
        "s_waitcnt lgkmcnt(6)\n\t"
        "v_smfmac_f32_16x16x64_bf16 v[248:251], v[220:223], v[40:47], %[sidx]\n\t"
        "s_waitcnt lgkmcnt(5)\n\t"
        "v_smfmac_f32_16x16x64_bf16 v[248:251], v[224:227], v[48:55], %[sidx]\n\t"
        "s_waitcnt lgkmcnt(4)\n\t"
        "v_smfmac_f32_16x16x64_bf16 v[248:251], v[228:231], v[56:63], %[sidx]\n\t"
        "s_waitcnt lgkmcnt(3)\n\t"
        "v_smfmac_f32_16x16x64_bf16 v[248:251], v[232:235], v[64:71], %[sidx]\n\t"
        "s_waitcnt lgkmcnt(2)\n\t"
        "v_smfmac_f32_16x16x64_bf16 v[248:251], v[236:239], v[72:79], %[sidx]\n\t"
        "s_waitcnt lgkmcnt(1)\n\t"
        "v_smfmac_f32_16x16x64_bf16 v[248:251], v[240:243], v[80:87], %[sidx]\n\t"
        "s_waitcnt lgkmcnt(0)\n\t"
        "v_smfmac_f32_16x16x64_bf16 v[248:251], v[244:247], v[88:95], %[sidx]\n\t"
        "v_smfmac_f32_16x16x64_bf16 v[252:255], v[208:211], v[96:103], %[sidx]\n\t"
        "v_smfmac_f32_16x16x64_bf16 v[252:255], v[212:215], v[104:111], %[sidx]\n\t"
        "v_smfmac_f32_16x16x64_bf16 v[252:255], v[216:219], v[112:119], %[sidx]\n\t"
        "v_add_f32 v248, v248, v250\n\t"
        "v_smfmac_f32_16x16x64_bf16 v[252:255], v[220:223], v[120:127], %[sidx]\n\t"
        "v_add_f32 v249, v249, v251\n\t"
        "v_smfmac_f32_16x16x64_bf16 v[252:255], v[224:227], v[128:135], %[sidx]\n\t"
        "v_cvt_pk_bf16_f32 v200, v248, v249\n\t"
        "v_smfmac_f32_16x16x64_bf16 v[252:255], v[228:231], v[136:143], %[sidx]\n\t"
        "ds_write_b32 %[ow], v200\n\t"
        "v_smfmac_f32_16x16x64_bf16 v[252:255], v[232:235], v[144:151], %[sidx]\n\t"
        "v_cmp_u_f32 vcc, v248, v249\n\t"
        "v_smfmac_f32_16x16x64_bf16 v[252:255], v[236:239], v[152:159], %[sidx]\n\t"
        "v_smfmac_f32_16x16x64_bf16 v[252:255], v[240:243], v[160:167], %[sidx]\n\t"
        "v_smfmac_f32_16x16x64_bf16 v[252:255], v[244:247], v[168:175], %[sidx]\n\t"
        "s_nop 6\n\t"
        "s_cbranch_vccnz Lnan%=\n\t"
        "Lok%=:\n\t"
        "v_add_f32 v252, v252, v254\n\t"
        "v_add_f32 v253, v253, v255\n\t"
        "v_cvt_pk_bf16_f32 v201, v252, v253\n\t"
        "ds_write_b32 %[ow], v201 offset:3072\n\t"
        "s_barrier\n\t"
        "s_setprio 0\n\t"
        "ds_read_b32 v207, v206 offset:25104\n\t"
        "s_waitcnt lgkmcnt(0)\n\t"
        "v_cmp_eq_u32 vcc, %[rnd], v207\n\t"
        "s_barrier\n\t"
        "s_cbranch_vccnz Ltop%=\n\t"
        "s_branch Lend%=\n\t"
        "Lnan%=:\n\t"
        "v_max_u32 v207, v208, v209\n\t"
        "v_max3_u32 v207, v207, v210, v211\n\t"
        "v_max3_u32 v207, v207, v212, v213\n\t"
        "v_max3_u32 v207, v207, v214, v215\n\t"
        "v_max3_u32 v207, v207, v216, v217\n\t"
        "v_max3_u32 v207, v207, v218, v219\n\t"
        "v_max3_u32 v207, v207, v220, v221\n\t"
        "v_max3_u32 v207, v207, v222, v223\n\t"
        "v_max3_u32 v207, v207, v224, v225\n\t"
        "v_max3_u32 v207, v207, v226, v227\n\t"
        "v_max3_u32 v207, v207, v228, v229\n\t"
        "v_max3_u32 v207, v207, v230, v231\n\t"
        "v_max3_u32 v207, v207, v232, v233\n\t"
        "v_max3_u32 v207, v207, v234, v235\n\t"
        "v_max3_u32 v207, v207, v236, v237\n\t"
        "v_max3_u32 v207, v207, v238, v239\n\t"
        "v_max3_u32 v207, v207, v240, v241\n\t"
        "v_max3_u32 v207, v207, v242, v243\n\t"
        "v_max3_u32 v207, v207, v244, v245\n\t"
        "v_max3_u32 v207, v207, v246, v247\n\t"
        "v_cmp_eq_u32 vcc, 0xffbfffbf, v207\n\t"
        "v_mov_b32 v207, %[rnd]\n\t"
        "s_cbranch_vccz Lok%=\n\t"
        "ds_write_b32 v206, v207 offset:25104\n\t"
        "s_waitcnt lgkmcnt(0)\n\t"
        "s_barrier\n\t"
        "s_barrier\n\t"
        "s_branch Ltop%=\n\t"
        "Lend%=:\n\t"
        : [rnd] "+&s"(rnd)
        : [w0] "{v[16:47]}"(W[0]), [w1] "{v[48:79]}"(W[1]), [w2] "{v[80:111]}"(W[2]), [w3] "{v[112:143]}"(W[3]), [w4] "{v[144:175]}"(W[4]), [va] "v"(va), [sidx] "v"(sidx), [ow] "v"(ow), [lb] "s"(lb)
        : "v200", "v201", "v202", "v203", "v204", "v205", "v206", "v207", "v208", "v209", "v210", "v211", "v212", "v213", "v214", "v215", "v216", "v217", "v218", "v219", "v220", "v221", "v222", "v223", "v224", "v225", "v226", "v227", "v228", "v229", "v230", "v231", "v232", "v233", "v234", "v235", "v236", "v237", "v238", "v239", "v240", "v241", "v242", "v243", "v244", "v245", "v246", "v247", "v248", "v249", "v250", "v251", "v252", "v253", "v254", "v255", "vcc", "scc", "memory");
}

__device__ __forceinline__ void s_k3(const u32x32 (&W)[5], u32& rnd, u32 va, u32 sidx, u32 ow, u32 lb, u32x4& p0, u32& p1, u32 ka, u32 qa, u32 ma, u32 ktr, u32 o0, u32 o1, float ce, u32x4 rs, u32 fo) {
    asm volatile(
        "v_mov_b64 v[248:249], 0\n\t"
        "v_mov_b64 v[250:251], 0\n\t"
        "v_mov_b64 v[252:253], 0\n\t"
        "v_mov_b64 v[254:255], 0\n\t"
        "v_mov_b32 v200, 0xffbfffbf\n\t"
        "v_mov_b32 v206, %[lb]\n\t"
        "s_add_u32 %[rnd], %[rnd], 1\n\t"
        "s_setprio 1\n\t"
        "s_waitcnt vmcnt(0) lgkmcnt(0)\n\t"
        "s_barrier\n\t"
        "buffer_store_dwordx4 v[192:195], %[o0], %[rs], 0 offen sc1\n\t"
        "buffer_store_dword v196, %[o1], %[rs], 0 offen sc1\n\t"
        "s_branch Lmk%=\n\t"
        "Ltop%=:\n\t"
        "v_mov_b64 v[248:249], 0\n\t"
        "v_mov_b64 v[250:251], 0\n\t"
        "v_mov_b64 v[252:253], 0\n\t"
        "v_mov_b64 v[254:255], 0\n\t"
        "v_mov_b32 v200, 0xffbfffbf\n\t"
        "v_mov_b32 v206, %[lb]\n\t"
        "s_add_u32 %[rnd], %[rnd], 1\n\t"
        "s_setprio 1\n\t"
        "Lmk%=:\n\t"
        "ds_write_b32 %[ow], v200\n\t"
        "ds_write_b32 %[ow], v200 offset:3072\n\t"
        "s_waitcnt lgkmcnt(0)\n\t"
        "s_barrier\n\t"
        "ds_read_b128 v[208:211], %[va] offset:0\n\t"
        "ds_read_b128 v[212:215], %[va] offset:1024\n\t"
        "ds_read_b128 v[216:219], %[va] offset:2048\n\t"
        "ds_read_b128 v[220:223], %[va] offset:3072\n\t"
        "ds_read_b128 v[224:227], %[va] offset:4096\n\t"
        "ds_read_b128 v[228:231], %[va] offset:5120\n\t"
        "ds_read_b128 v[232:235], %[va] offset:6144\n\t"
        "ds_read_b128 v[236:239], %[va] offset:7168\n\t"
        "ds_read_b128 v[240:243], %[va] offset:8192\n\t"
        "ds_read_b128 v[244:247], %[va] offset:10240\n\t"
        "s_waitcnt lgkmcnt(9)\n\t"
        "v_smfmac_f32_16x16x64_bf16 v[248:251], v[208:211], v[16:23], %[sidx]\n\t"
        "s_waitcnt lgkmcnt(8)\n\t"
        "v_smfmac_f32_16x16x64_bf16 v[248:251], v[212:215], v[24:31], %[sidx]\n\t"
        "s_waitcnt lgkmcnt(7)\n\t"
        "v_smfmac_f32_16x16x64_bf16 v[248:251], v[216:219], v[32:39], %[sidx]\n\t"
        "s_waitcnt lgkmcnt(6)\n\t"
        "v_smfmac_f32_16x16x64_bf16 v[248:251], v[220:223], v[40:47], %[sidx]\n\t"
        "s_waitcnt lgkmcnt(5)\n\t"
        "v_smfmac_f32_16x16x64_bf16 v[248:251], v[224:227], v[48:55], %[sidx]\n\t"
        "s_waitcnt lgkmcnt(4)\n\t"
        "v_smfmac_f32_16x16x64_bf16 v[248:251], v[228:231], v[56:63], %[sidx]\n\t"
        "s_waitcnt lgkmcnt(3)\n\t"
        "v_smfmac_f32_16x16x64_bf16 v[248:251], v[232:235], v[64:71], %[sidx]\n\t"
        "s_waitcnt lgkmcnt(2)\n\t"
        "v_smfmac_f32_16x16x64_bf16 v[248:251], v[236:239], v[72:79], %[sidx]\n\t"
        "s_waitcnt lgkmcnt(1)\n\t"
        "v_smfmac_f32_16x16x64_bf16 v[248:251], v[240:243], v[80:87], %[sidx]\n\t"
        "s_waitcnt lgkmcnt(0)\n\t"
        "v_smfmac_f32_16x16x64_bf16 v[248:251], v[244:247], v[88:95], %[sidx]\n\t"
        "v_smfmac_f32_16x16x64_bf16 v[252:255], v[208:211], v[96:103], %[sidx]\n\t"
        "v_smfmac_f32_16x16x64_bf16 v[252:255], v[212:215], v[104:111], %[sidx]\n\t"
        "v_smfmac_f32_16x16x64_bf16 v[252:255], v[216:219], v[112:119], %[sidx]\n\t"
        "v_add_f32 v248, v248, v250\n\t"
        "v_smfmac_f32_16x16x64_bf16 v[252:255], v[220:223], v[120:127], %[sidx]\n\t"
        "v_add_f32 v249, v249, v251\n\t"
        "v_smfmac_f32_16x16x64_bf16 v[252:255], v[224:227], v[128:135], %[sidx]\n\t"
        "v_cvt_pk_bf16_f32 v200, v248, v249\n\t"
        "v_smfmac_f32_16x16x64_bf16 v[252:255], v[228:231], v[136:143], %[sidx]\n\t"
        "ds_write_b32 %[ow], v200\n\t"
        "v_smfmac_f32_16x16x64_bf16 v[252:255], v[232:235], v[144:151], %[sidx]\n\t"
        "v_cmp_u_f32 vcc, v248, v249\n\t"
        "v_smfmac_f32_16x16x64_bf16 v[252:255], v[236:239], v[152:159], %[sidx]\n\t"
        "v_smfmac_f32_16x16x64_bf16 v[252:255], v[240:243], v[160:167], %[sidx]\n\t"
        "v_smfmac_f32_16x16x64_bf16 v[252:255], v[244:247], v[168:175], %[sidx]\n\t"
        "s_nop 6\n\t"
        "s_cbranch_vccnz Lnan%=\n\t"
        "Lok%=:\n\t"
        "v_add_f32 v252, v252, v254\n\t"
        "v_add_f32 v253, v253, v255\n\t"
        "v_cvt_pk_bf16_f32 v201, v252, v253\n\t"
        "ds_write_b32 %[ow], v201 offset:3072\n\t"
        "s_barrier\n\t"
        "ds_read_b128 v[240:243], %[ma] offset:0\n\t"
        "ds_read_b128 v[244:247], %[ma] offset:1024\n\t"
        "ds_read_b128 v[176:179], %[qa] offset:0\n\t"
        "ds_read_b128 v[180:183], %[qa] offset:1024\n\t"
        "ds_read_b128 v[184:187], %[qa] offset:2048\n\t"
        "ds_read_b128 v[188:191], %[qa] offset:3072\n\t"
        "ds_read_b128 v[208:211], %[ka] offset:0\n\t"
        "ds_read_b128 v[212:215], %[ka] offset:1024\n\t"
        "ds_read_b128 v[216:219], %[ka] offset:2048\n\t"
        "ds_read_b128 v[220:223], %[ka] offset:3072\n\t"
        "ds_read_b128 v[224:227], %[ka] offset:4096\n\t"
        "ds_read_b128 v[228:231], %[ka] offset:5120\n\t"
        "ds_read_b128 v[232:235], %[ka] offset:6144\n\t"
        "ds_read_b128 v[236:239], %[ka] offset:7168\n\t"
        "s_waitcnt lgkmcnt(7)\n\t"
        "v_mfma_f32_16x16x32_bf16 v[240:243], v[208:211], v[176:179], v[240:243]\n\t"
        "s_waitcnt lgkmcnt(6)\n\t"
        "v_mfma_f32_16x16x32_bf16 v[240:243], v[212:215], v[180:183], v[240:243]\n\t"
        "s_waitcnt lgkmcnt(5)\n\t"
        "v_mfma_f32_16x16x32_bf16 v[240:243], v[216:219], v[184:187], v[240:243]\n\t"
        "s_waitcnt lgkmcnt(4)\n\t"
        "v_mfma_f32_16x16x32_bf16 v[240:243], v[220:223], v[188:191], v[240:243]\n\t"
        "s_waitcnt lgkmcnt(3)\n\t"
        "v_mfma_f32_16x16x32_bf16 v[244:247], v[224:227], v[176:179], v[244:247]\n\t"
        "s_waitcnt lgkmcnt(2)\n\t"
        "v_mfma_f32_16x16x32_bf16 v[244:247], v[228:231], v[180:183], v[244:247]\n\t"
        "s_waitcnt lgkmcnt(1)\n\t"
        "v_mfma_f32_16x16x32_bf16 v[244:247], v[232:235], v[184:187], v[244:247]\n\t"
        "s_waitcnt lgkmcnt(0)\n\t"
        "v_mfma_f32_16x16x32_bf16 v[244:247], v[236:239], v[188:191], v[244:247]\n\t"
        "s_setprio 0\n\t"
        "ds_read_b32 v207, v206 offset:25104\n\t"
        "s_waitcnt lgkmcnt(0)\n\t"
        "v_cmp_eq_u32 vcc, %[rnd], v207\n\t"
        "s_barrier\n\t"
        "s_cbranch_vccnz Ltop%=\n\t"
        "ds_read_b64_tr_b16 v[208:209], %[ktr] offset:0\n\t"
        "ds_read_b64_tr_b16 v[210:211], %[ktr] offset:768\n\t"
        "ds_read_b64_tr_b16 v[212:213], %[ktr] offset:1536\n\t"
        "ds_read_b64_tr_b16 v[214:215], %[ktr] offset:2304\n\t"
        "ds_read_b64_tr_b16 v[216:217], %[ktr] offset:3072\n\t"
        "ds_read_b64_tr_b16 v[218:219], %[ktr] offset:3840\n\t"
        "ds_read_b64_tr_b16 v[220:221], %[ktr] offset:4608\n\t"
        "ds_read_b64_tr_b16 v[222:223], %[ktr] offset:5376\n\t"
        "v_mov_b32 v200, 1\n\t"
        "s_waitcnt vmcnt(0)\n\t"
        "buffer_store_dword v200, %[fo], %[rs], 0 offen sc1\n\t"
        "s_waitcnt lgkmcnt(6)\n\t"
        "v_mfma_f32_16x16x32_bf16 v[248:251], v[208:211], v[176:179], 0\n\t"
        "s_waitcnt lgkmcnt(4)\n\t"
        "v_mfma_f32_16x16x32_bf16 v[248:251], v[212:215], v[180:183], v[248:251]\n\t"
        "s_waitcnt lgkmcnt(2)\n\t"
        "v_mfma_f32_16x16x32_bf16 v[248:251], v[216:219], v[184:187], v[248:251]\n\t"
        "s_waitcnt lgkmcnt(0)\n\t"
        "v_mfma_f32_16x16x32_bf16 v[248:251], v[220:223], v[188:191], v[248:251]\n\t"
        "v_max3_f32 v253, v240, v241, v242\n\t"
        "v_max3_f32 v253, v253, v243, v244\n\t"
        "v_max3_f32 v253, v253, v245, v246\n\t"
        "v_max_f32 v253, v253, v247\n\t"
        "s_nop 3\n\t"
        "v_mul_f32 v248, %[ce], v248\n\t"
        "v_mul_f32 v249, %[ce], v249\n\t"
        "v_max3_f32 v253, v253, v248, v249\n\t"
        "v_mov_b32 v254, v253\n\t"
        "s_nop 1\n\t"
        "v_permlane16_swap_b32 v253, v254\n\t"
        "v_max_f32 v253, v253, v254\n\t"
        "v_mov_b32 v254, v253\n\t"
        "s_nop 1\n\t"
        "v_permlane32_swap_b32 v253, v254\n\t"
        "v_max_f32 v253, v253, v254\n\t"
        "v_sub_f32 v240, v240, v253\n\t"
        "v_sub_f32 v241, v241, v253\n\t"
        "v_sub_f32 v242, v242, v253\n\t"
        "v_sub_f32 v243, v243, v253\n\t"
        "v_sub_f32 v244, v244, v253\n\t"
        "v_sub_f32 v245, v245, v253\n\t"
        "v_sub_f32 v246, v246, v253\n\t"
        "v_sub_f32 v247, v247, v253\n\t"
        "v_sub_f32 v248, v248, v253\n\t"
        "v_sub_f32 v249, v249, v253\n\t"
        "v_exp_f32 v240, v240\n\t"
        "v_exp_f32 v241, v241\n\t"
        "v_exp_f32 v242, v242\n\t"
        "v_exp_f32 v243, v243\n\t"
        "v_exp_f32 v244, v244\n\t"
        "v_exp_f32 v245, v245\n\t"
        "v_exp_f32 v246, v246\n\t"
        "v_exp_f32 v247, v247\n\t"
        "v_exp_f32 v248, v248\n\t"
        "v_exp_f32 v249, v249\n\t"
        "v_pk_add_f32 v[252:253], v[240:241], v[242:243]\n\t"
        "v_pk_add_f32 v[254:255], v[244:245], v[246:247]\n\t"
        "v_pk_add_f32 v[252:253], v[252:253], v[248:249]\n\t"
        "v_pk_add_f32 v[252:253], v[252:253], v[254:255]\n\t"
        "v_add_f32 v252, v252, v253\n\t"
        "v_mov_b32 v253, v252\n\t"
        "s_nop 1\n\t"
        "v_permlane16_swap_b32 v252, v253\n\t"
        "v_add_f32 v252, v252, v253\n\t"
        "v_mov_b32 v253, v252\n\t"
        "s_nop 1\n\t"
        "v_permlane32_swap_b32 v252, v253\n\t"
        "v_add_f32 v252, v252, v253\n\t"
        "v_rcp_f32 v252, v252\n\t"
        "s_nop 0\n\t"
        "v_pk_mul_f32 v[240:241], v[240:241], v[252:253] op_sel_hi:[1,0]\n\t"
        "v_pk_mul_f32 v[242:243], v[242:243], v[252:253] op_sel_hi:[1,0]\n\t"
        "v_pk_mul_f32 v[244:245], v[244:245], v[252:253] op_sel_hi:[1,0]\n\t"
        "v_pk_mul_f32 v[246:247], v[246:247], v[252:253] op_sel_hi:[1,0]\n\t"
        "v_pk_mul_f32 v[248:249], v[248:249], v[252:253] op_sel_hi:[1,0]\n\t"
        "v_cvt_pk_bf16_f32 v192, v240, v241\n\t"
        "v_cvt_pk_bf16_f32 v193, v242, v243\n\t"
        "v_cvt_pk_bf16_f32 v194, v244, v245\n\t"
        "v_cvt_pk_bf16_f32 v195, v246, v247\n\t"
        "v_cvt_pk_bf16_f32 v196, v248, v249\n\t"
        "s_branch Lend%=\n\t"
        "Lnan%=:\n\t"
        "v_max_u32 v207, v208, v209\n\t"
        "v_max3_u32 v207, v207, v210, v211\n\t"
        "v_max3_u32 v207, v207, v212, v213\n\t"
        "v_max3_u32 v207, v207, v214, v215\n\t"
        "v_max3_u32 v207, v207, v216, v217\n\t"
        "v_max3_u32 v207, v207, v218, v219\n\t"
        "v_max3_u32 v207, v207, v220, v221\n\t"
        "v_max3_u32 v207, v207, v222, v223\n\t"
        "v_max3_u32 v207, v207, v224, v225\n\t"
        "v_max3_u32 v207, v207, v226, v227\n\t"
        "v_max3_u32 v207, v207, v228, v229\n\t"
        "v_max3_u32 v207, v207, v230, v231\n\t"
        "v_max3_u32 v207, v207, v232, v233\n\t"
        "v_max3_u32 v207, v207, v234, v235\n\t"
        "v_max3_u32 v207, v207, v236, v237\n\t"
        "v_max3_u32 v207, v207, v238, v239\n\t"
        "v_max3_u32 v207, v207, v240, v241\n\t"
        "v_max3_u32 v207, v207, v242, v243\n\t"
        "v_max3_u32 v207, v207, v244, v245\n\t"
        "v_max3_u32 v207, v207, v246, v247\n\t"
        "v_cmp_eq_u32 vcc, 0xffbfffbf, v207\n\t"
        "v_mov_b32 v207, %[rnd]\n\t"
        "s_cbranch_vccz Lok%=\n\t"
        "ds_write_b32 v206, v207 offset:25104\n\t"
        "s_waitcnt lgkmcnt(0)\n\t"
        "s_barrier\n\t"
        "s_barrier\n\t"
        "s_branch Ltop%=\n\t"
        "Lend%=:\n\t"
        : [rnd] "+&s"(rnd), [p0] "+{v[192:195]}"(p0), [p1] "+{v196}"(p1)
        : [w0] "{v[16:47]}"(W[0]), [w1] "{v[48:79]}"(W[1]), [w2] "{v[80:111]}"(W[2]), [w3] "{v[112:143]}"(W[3]), [w4] "{v[144:175]}"(W[4]), [va] "v"(va), [sidx] "v"(sidx), [ow] "v"(ow), [lb] "s"(lb), [ka] "v"(ka), [qa] "v"(qa), [ma] "v"(ma), [ktr] "v"(ktr), [o0] "v"(o0), [o1] "v"(o1), [ce] "s"(ce), [rs] "s"(rs), [fo] "v"(fo)
        : "v176", "v177", "v178", "v179", "v180", "v181", "v182", "v183", "v184", "v185", "v186", "v187", "v188", "v189", "v190", "v191", "v200", "v201", "v202", "v203", "v204", "v205", "v206", "v207", "v208", "v209", "v210", "v211", "v212", "v213", "v214", "v215", "v216", "v217", "v218", "v219", "v220", "v221", "v222", "v223", "v224", "v225", "v226", "v227", "v228", "v229", "v230", "v231", "v232", "v233", "v234", "v235", "v236", "v237", "v238", "v239", "v240", "v241", "v242", "v243", "v244", "v245", "v246", "v247", "v248", "v249", "v250", "v251", "v252", "v253", "v254", "v255", "vcc", "scc", "memory");
}

__device__ __forceinline__ void s_l(u32& rnd, u32& redo, u32 lb) {
    asm volatile(
        "v_mov_b32 v206, %[lb]\n\t"
        "s_add_u32 %[rnd], %[rnd], 1\n\t"
        "s_mov_b32 %[redo], 1\n\t"
        "s_barrier\n\t"
        "s_barrier\n\t"
        "s_setprio 0\n\t"
        "ds_read_b32 v207, v206 offset:25104\n\t"
        "s_waitcnt lgkmcnt(0)\n\t"
        "v_cmp_eq_u32 vcc, %[rnd], v207\n\t"
        "s_barrier\n\t"
        "s_cbranch_vccnz Lend%=\n\t"
        "s_mov_b32 %[redo], 0\n\t"
        "Lend%=:\n\t"
        : [rnd] "+&s"(rnd), [redo] "=&s"(redo)
        : [lb] "s"(lb)
        : "v206", "v207", "vcc", "scc", "memory");
}

__device__ __forceinline__ void s_e(const u32x32 (&W)[5], u32& rnd, u32 va, u32 sidx, u32 ow, u32 lb, u32 kxa, u32 qtr, u32 qo, u32 eo, u32 ew, float lim, u32x4 rs, u32 so STP) {
    asm volatile(
        "s_add_u32 s92, %[so], 0x1000\n\t"
        "s_add_u32 s93, %[so], 0x4000\n\t"
        "s_add_u32 s94, %[so], 0x5000\n\t"
        "v_mov_b64 v[248:249], 0\n\t"
        "v_mov_b64 v[250:251], 0\n\t"
        "v_mov_b64 v[252:253], 0\n\t"
        "v_mov_b64 v[254:255], 0\n\t"
        "v_mov_b32 v200, 0xffbfffbf\n\t"
        "v_mov_b32 v206, %[lb]\n\t"
        "s_add_u32 %[rnd], %[rnd], 1\n\t"
        "s_setprio 3\n\t"
        "s_mov_b32 s95, 1\n\t"
        "s_waitcnt vmcnt(0) lgkmcnt(0)\n\t"
        "s_barrier\n\t"
        "ds_read_b128 v[176:179], %[kxa] offset:0\n\t"
        "ds_read_b128 v[180:183], %[kxa] offset:1024\n\t"
        "ds_read_b128 v[184:187], %[kxa] offset:2048\n\t"
        "ds_read_b128 v[188:191], %[kxa] offset:3072\n\t"
        "s_branch Lmk%=\n\t"
        "Ltop%=:\n\t"
        "v_mov_b64 v[248:249], 0\n\t"
        "v_mov_b64 v[250:251], 0\n\t"
        "v_mov_b64 v[252:253], 0\n\t"
        "v_mov_b64 v[254:255], 0\n\t"
        "v_mov_b32 v200, 0xffbfffbf\n\t"
        "v_mov_b32 v206, %[lb]\n\t"
        "s_add_u32 %[rnd], %[rnd], 1\n\t"
        "s_setprio 3\n\t"
        "s_mov_b32 s95, 1\n\t"
        "Lmk%=:\n\t"
        "ds_write_b32 %[ow], v200\n\t"
        "ds_write_b32 %[ow], v200 offset:3072\n\t"
        "s_waitcnt lgkmcnt(0)\n\t"
        "s_barrier\n\t"
        "ds_read_b128 v[208:211], %[va] offset:0\n\t"
        "ds_read_b128 v[212:215], %[va] offset:1024\n\t"
        "ds_read_b128 v[216:219], %[va] offset:2048\n\t"
        "ds_read_b128 v[220:223], %[va] offset:3072\n\t"
        "ds_read_b128 v[224:227], %[va] offset:4096\n\t"
        "ds_read_b128 v[228:231], %[va] offset:5120\n\t"
        "s_waitcnt lgkmcnt(5)\n\t"
        "v_smfmac_f32_16x16x64_bf16 v[248:251], v[208:211], v[16:23], %[sidx]\n\t"
        "ds_read_b128 v[232:235], %[va] offset:6144\n\t"
        "v_smfmac_f32_16x16x64_bf16 v[252:255], v[208:211], v[96:103], %[sidx]\n\t"
        "s_waitcnt lgkmcnt(5)\n\t"
        "v_smfmac_f32_16x16x64_bf16 v[248:251], v[212:215], v[24:31], %[sidx]\n\t"
        "ds_read_b128 v[236:239], %[va] offset:7168\n\t"
        "v_smfmac_f32_16x16x64_bf16 v[252:255], v[212:215], v[104:111], %[sidx]\n\t"
        "s_waitcnt lgkmcnt(5)\n\t"
        "v_smfmac_f32_16x16x64_bf16 v[248:251], v[216:219], v[32:39], %[sidx]\n\t"
        "ds_read_b128 v[240:243], %[va] offset:8192\n\t"
        "v_smfmac_f32_16x16x64_bf16 v[252:255], v[216:219], v[112:119], %[sidx]\n\t"
        "s_waitcnt lgkmcnt(5)\n\t"
        "v_smfmac_f32_16x16x64_bf16 v[248:251], v[220:223], v[40:47], %[sidx]\n\t"
        "ds_read_b128 v[244:247], %[va] offset:10240\n\t"
        "v_smfmac_f32_16x16x64_bf16 v[252:255], v[220:223], v[120:127], %[sidx]\n\t"
        "s_waitcnt lgkmcnt(5)\n\t"
        "v_smfmac_f32_16x16x64_bf16 v[248:251], v[224:227], v[48:55], %[sidx]\n\t"
        "s_waitcnt lgkmcnt(4)\n\t"
        "v_smfmac_f32_16x16x64_bf16 v[248:251], v[228:231], v[56:63], %[sidx]\n\t"
        "s_waitcnt lgkmcnt(3)\n\t"
        "v_smfmac_f32_16x16x64_bf16 v[248:251], v[232:235], v[64:71], %[sidx]\n\t"
        "s_waitcnt lgkmcnt(2)\n\t"
        "v_smfmac_f32_16x16x64_bf16 v[248:251], v[236:239], v[72:79], %[sidx]\n\t"
        "s_waitcnt lgkmcnt(1)\n\t"
        "v_smfmac_f32_16x16x64_bf16 v[248:251], v[240:243], v[80:87], %[sidx]\n\t"
        "s_waitcnt lgkmcnt(0)\n\t"
        "v_smfmac_f32_16x16x64_bf16 v[248:251], v[244:247], v[88:95], %[sidx]\n\t"
        "v_smfmac_f32_16x16x64_bf16 v[252:255], v[224:227], v[128:135], %[sidx]\n\t"
        "v_smfmac_f32_16x16x64_bf16 v[252:255], v[228:231], v[136:143], %[sidx]\n\t"
        "v_add_f32 v248, v248, v250\n\t"
        "v_smfmac_f32_16x16x64_bf16 v[252:255], v[232:235], v[144:151], %[sidx]\n\t"
        "v_add_f32 v249, v249, v251\n\t"
        "v_smfmac_f32_16x16x64_bf16 v[252:255], v[236:239], v[152:159], %[sidx]\n\t"
        "v_cvt_pk_bf16_f32 v200, v248, v249\n\t"
        "v_smfmac_f32_16x16x64_bf16 v[252:255], v[240:243], v[160:167], %[sidx]\n\t"
        "ds_write_b32 %[ow], v200\n\t"
        "v_smfmac_f32_16x16x64_bf16 v[252:255], v[244:247], v[168:175], %[sidx]\n\t"
        "v_cmp_u_f32 vcc, v248, v249\n\t"
        "s_nop 2\n\t"
        "ds_read_b64_tr_b16 v[192:193], %[qtr] offset:0\n\t"
        "ds_read_b64_tr_b16 v[194:195], %[qtr] offset:768\n\t"
        "ds_read_b64_tr_b16 v[196:197], %[qtr] offset:1536\n\t"
        "ds_read_b64_tr_b16 v[198:199], %[qtr] offset:2304\n\t"
        "s_cbranch_vccnz Lnan%=\n\t"
        "Lok%=:\n\t"
        "v_add_f32 v252, v252, v254\n\t"
        "v_add_f32 v253, v253, v255\n\t"
        "v_cvt_pk_bf16_f32 v201, v252, v253\n\t"
        "ds_write_b32 %[ow], v201 offset:3072\n\t"
        "s_waitcnt lgkmcnt(3)\n\t"
        "v_mfma_f32_16x16x32_bf16 v[248:251], v[176:179], v[192:195], 0\n\t"
        "s_waitcnt lgkmcnt(1)\n\t"
        "v_mfma_f32_16x16x32_bf16 v[248:251], v[180:183], v[196:199], v[248:251]\n\t"
        "s_waitcnt lgkmcnt(0)\n\t"
        "s_nop 1\n\t"
        "Lre%=:\n\t"
        "ds_read_b64_tr_b16 v[208:209], %[qtr] offset:3072\n\t"
        "ds_read_b64_tr_b16 v[210:211], %[qtr] offset:3840\n\t"
        "ds_read_b64_tr_b16 v[212:213], %[qtr] offset:4608\n\t"
        "ds_read_b64_tr_b16 v[214:215], %[qtr] offset:5376\n\t"
        "s_waitcnt lgkmcnt(2)\n\t"
        "v_mfma_f32_16x16x32_bf16 v[248:251], v[184:187], v[208:211], v[248:251]\n\t"
        "s_waitcnt lgkmcnt(0)\n\t"
        "v_mfma_f32_16x16x32_bf16 v[248:251], v[188:191], v[212:215], v[248:251]\n\t"
        "s_nop 7\n\t"
        "v_mov_b32_dpp v248, v250 quad_perm:[0,1,2,3] row_mask:0xf bank_mask:0xc\n\t"
        "v_mov_b32_dpp v249, v251 quad_perm:[0,1,2,3] row_mask:0xf bank_mask:0xc\n\t"
        "v_exp_f32 v202, v248\n\t"
        "v_exp_f32 v203, v249\n\t"
        "v_max_f32_e64 v204, |v248|, |v249|\n\t"
        "v_cvt_pk_bf16_f32 v202, v202, v203\n\t"
        "v_cmpx_le_f32 vcc, v204, %[lim]\n\t"
        "buffer_store_dword v202, %[eo], %[rs], %[so] offen sc1\n\t"
        "buffer_store_dword v202, %[eo], %[rs], s92 offen sc1\n\t"
        "buffer_store_dword v202, %[eo], %[rs], s93 offen sc1\n\t"
        "buffer_store_dword v202, %[eo], %[rs], s94 offen sc1\n\t"
        "s_mov_b64 exec, -1\n\t"
        "v_cndmask_b32 v202, -1, v202, vcc\n\t"
        "s_cmp_lg_u64 vcc, -1\n\t"
        "ds_write_b32 %[ew], v202\n\t"
        "s_cbranch_scc1 Lag%=\n\t"
        "Lgo%=:\n\t"
        "s_waitcnt lgkmcnt(0)\n\t"
        "s_barrier\n\t"
        "s_setprio 0\n\t"
        "ds_read_b32 v207, v206 offset:25104\n\t"
        "s_waitcnt lgkmcnt(0)\n\t"
        "v_cmp_eq_u32 vcc, %[rnd], v207\n\t"
        "s_barrier\n\t"
        "s_cbranch_vccnz Ltop%=\n\t"
        "s_branch Lend%=\n\t"
        "Lag%=:\n\t"
        "s_sub_u32 s95, s95, 1\n\t"
        "s_cbranch_scc1 Lgo%=\n\t"
        "Lqw%=:\n\t"
        "v_subrev_u32 v205, %[qo], %[ow]\n\t"
        "ds_read_b32 v192, v205 offset:0\n\t"
        "ds_read_b32 v193, v205 offset:768\n\t"
        "ds_read_b32 v194, v205 offset:1536\n\t"
        "ds_read_b32 v195, v205 offset:2304\n\t"
        "ds_read_b32 v196, v205 offset:3072\n\t"
        "ds_read_b32 v197, v205 offset:3840\n\t"
        "ds_read_b32 v198, v205 offset:4608\n\t"
        "ds_read_b32 v199, v205 offset:5376\n\t"
        "ds_read_b32 v207, v206 offset:25104\n\t"
        "s_waitcnt lgkmcnt(0)\n\t"
        "v_cmp_eq_u32 vcc, %[rnd], v207\n\t"
        "s_cbranch_vccnz Lgo%=\n\t"
        "v_max_u32 v207, v192, v193\n\t"
        "v_max3_u32 v207, v207, v194, v195\n\t"
        "v_max3_u32 v207, v207, v196, v197\n\t"
        "v_max3_u32 v207, v207, v198, v199\n\t"
        "v_cmp_eq_u32 vcc, 0xffbfffbf, v207\n\t"
        "s_cbranch_vccnz Lqw%=\n\t"
        "ds_read_b64_tr_b16 v[192:193], %[qtr] offset:0\n\t"
        "ds_read_b64_tr_b16 v[194:195], %[qtr] offset:768\n\t"
        "ds_read_b64_tr_b16 v[196:197], %[qtr] offset:1536\n\t"
        "ds_read_b64_tr_b16 v[198:199], %[qtr] offset:2304\n\t"
        "s_waitcnt lgkmcnt(2)\n\t"
        "v_mfma_f32_16x16x32_bf16 v[248:251], v[176:179], v[192:195], 0\n\t"
        "s_waitcnt lgkmcnt(0)\n\t"
        "v_mfma_f32_16x16x32_bf16 v[248:251], v[180:183], v[196:199], v[248:251]\n\t"
        "s_branch Lre%=\n\t"
        "Lnan%=:\n\t"
        "v_max_u32 v207, v208, v209\n\t"
        "v_max3_u32 v207, v207, v210, v211\n\t"
        "v_max3_u32 v207, v207, v212, v213\n\t"
        "v_max3_u32 v207, v207, v214, v215\n\t"
        "v_max3_u32 v207, v207, v216, v217\n\t"
        "v_max3_u32 v207, v207, v218, v219\n\t"
        "v_max3_u32 v207, v207, v220, v221\n\t"
        "v_max3_u32 v207, v207, v222, v223\n\t"
        "v_max3_u32 v207, v207, v224, v225\n\t"
        "v_max3_u32 v207, v207, v226, v227\n\t"
        "v_max3_u32 v207, v207, v228, v229\n\t"
        "v_max3_u32 v207, v207, v230, v231\n\t"
        "v_max3_u32 v207, v207, v232, v233\n\t"
        "v_max3_u32 v207, v207, v234, v235\n\t"
        "v_max3_u32 v207, v207, v236, v237\n\t"
        "v_max3_u32 v207, v207, v238, v239\n\t"
        "v_max3_u32 v207, v207, v240, v241\n\t"
        "v_max3_u32 v207, v207, v242, v243\n\t"
        "v_max3_u32 v207, v207, v244, v245\n\t"
        "v_max3_u32 v207, v207, v246, v247\n\t"
        "v_cmp_eq_u32 vcc, 0xffbfffbf, v207\n\t"
        "v_mov_b32 v207, %[rnd]\n\t"
        "s_cbranch_vccz Lok%=\n\t"
        "ds_write_b32 v206, v207 offset:25104\n\t"
        "s_waitcnt lgkmcnt(0)\n\t"
        "s_barrier\n\t"
        "s_barrier\n\t"
        "s_branch Ltop%=\n\t"
        "Lend%=:\n\t"
        : [rnd] "+&s"(rnd)
        : [w0] "{v[16:47]}"(W[0]), [w1] "{v[48:79]}"(W[1]), [w2] "{v[80:111]}"(W[2]), [w3] "{v[112:143]}"(W[3]), [w4] "{v[144:175]}"(W[4]), [va] "v"(va), [sidx] "v"(sidx), [ow] "v"(ow), [lb] "s"(lb), [kxa] "v"(kxa), [qtr] "v"(qtr), [eo] "v"(eo), [ew] "v"(ew), [lim] "v"(lim), [rs] "s"(rs), [so] "s"(so), [qo] "s"(qo)
        : "s92", "s93", "s94", "s95", "v176", "v177", "v178", "v179", "v180", "v181", "v182", "v183", "v184", "v185", "v186", "v187", "v188", "v189", "v190", "v191", "v192", "v193", "v194", "v195", "v196", "v197", "v198", "v199", "v200", "v201", "v202", "v203", "v204", "v205", "v206", "v207", "v208", "v209", "v210", "v211", "v212", "v213", "v214", "v215", "v216", "v217", "v218", "v219", "v220", "v221", "v222", "v223", "v224", "v225", "v226", "v227", "v228", "v229", "v230", "v231", "v232", "v233", "v234", "v235", "v236", "v237", "v238", "v239", "v240", "v241", "v242", "v243", "v244", "v245", "v246", "v247", "v248", "v249", "v250", "v251", "v252", "v253", "v254", "v255", "vcc", "scc", "memory");
}

__device__ __forceinline__ void s_f(const u32x32 (&W)[5], u32& rnd, u32 va, u32 sidx, u32 ow, u32 lb, u32& bad, u32x4 ones, u32 qtr, u32 ktr, u32 er, u32 po, float c1, float ind0, float ind1, u32x4 rs, u32 so, float ce, float big STP) {
    asm volatile(
        "s_add_u32 s92, %[so], 0x1000\n\t"
        "s_add_u32 s93, %[so], 0x4000\n\t"
        "s_add_u32 s94, %[so], 0x5000\n\t"
        "v_mov_b64 v[248:249], 0\n\t"
        "v_mov_b64 v[250:251], 0\n\t"
        "v_mov_b64 v[252:253], 0\n\t"
        "v_mov_b64 v[254:255], 0\n\t"
        "v_mov_b32 v200, 0xffbfffbf\n\t"
        "v_mov_b32 v206, %[lb]\n\t"
        "s_add_u32 %[rnd], %[rnd], 1\n\t"
        "s_setprio 3\n\t"
        "s_mov_b32 %[bad], 2\n\t"
        "v_mov_b32 v197, 0\n\t"
        "s_waitcnt vmcnt(0) lgkmcnt(0)\n\t"
        "s_barrier\n\t"
        "s_branch Lmk%=\n\t"
        "Ltop%=:\n\t"
        "v_mov_b64 v[248:249], 0\n\t"
        "v_mov_b64 v[250:251], 0\n\t"
        "v_mov_b64 v[252:253], 0\n\t"
        "v_mov_b64 v[254:255], 0\n\t"
        "v_mov_b32 v200, 0xffbfffbf\n\t"
        "v_mov_b32 v206, %[lb]\n\t"
        "s_add_u32 %[rnd], %[rnd], 1\n\t"
        "s_setprio 3\n\t"
        "s_mov_b32 %[bad], 2\n\t"
        "v_mov_b32 v197, 0\n\t"
        "Lmk%=:\n\t"
        "ds_write_b32 %[ow], v200\n\t"
        "ds_write_b32 %[ow], v200 offset:3072\n\t"
        "s_waitcnt lgkmcnt(0)\n\t"
        "s_barrier\n\t"
        "ds_read_b128 v[208:211], %[va] offset:0\n\t"
        "ds_read_b128 v[212:215], %[va] offset:1024\n\t"
        "ds_read_b128 v[216:219], %[va] offset:2048\n\t"
        "ds_read_b128 v[220:223], %[va] offset:3072\n\t"
        "ds_read_b128 v[224:227], %[va] offset:4096\n\t"
        "ds_read_b128 v[228:231], %[va] offset:5120\n\t"
        "s_waitcnt lgkmcnt(5)\n\t"
        "v_smfmac_f32_16x16x64_bf16 v[248:251], v[208:211], v[16:23], %[sidx]\n\t"
        "ds_read_b128 v[232:235], %[va] offset:6144\n\t"
        "v_smfmac_f32_16x16x64_bf16 v[252:255], v[208:211], v[96:103], %[sidx]\n\t"
        "s_waitcnt lgkmcnt(5)\n\t"
        "v_smfmac_f32_16x16x64_bf16 v[248:251], v[212:215], v[24:31], %[sidx]\n\t"
        "ds_read_b128 v[236:239], %[va] offset:7168\n\t"
        "v_smfmac_f32_16x16x64_bf16 v[252:255], v[212:215], v[104:111], %[sidx]\n\t"
        "s_waitcnt lgkmcnt(5)\n\t"
        "v_smfmac_f32_16x16x64_bf16 v[248:251], v[216:219], v[32:39], %[sidx]\n\t"
        "ds_read_b128 v[240:243], %[va] offset:8192\n\t"
        "v_smfmac_f32_16x16x64_bf16 v[252:255], v[216:219], v[112:119], %[sidx]\n\t"
        "s_waitcnt lgkmcnt(5)\n\t"
        "v_smfmac_f32_16x16x64_bf16 v[248:251], v[220:223], v[40:47], %[sidx]\n\t"
        "ds_read_b128 v[244:247], %[va] offset:10240\n\t"
        "v_smfmac_f32_16x16x64_bf16 v[252:255], v[220:223], v[120:127], %[sidx]\n\t"
        "s_waitcnt lgkmcnt(5)\n\t"
        "v_smfmac_f32_16x16x64_bf16 v[248:251], v[224:227], v[48:55], %[sidx]\n\t"
        "s_waitcnt lgkmcnt(4)\n\t"
        "v_smfmac_f32_16x16x64_bf16 v[248:251], v[228:231], v[56:63], %[sidx]\n\t"
        "s_waitcnt lgkmcnt(3)\n\t"
        "v_smfmac_f32_16x16x64_bf16 v[248:251], v[232:235], v[64:71], %[sidx]\n\t"
        "s_waitcnt lgkmcnt(2)\n\t"
        "v_smfmac_f32_16x16x64_bf16 v[248:251], v[236:239], v[72:79], %[sidx]\n\t"
        "s_waitcnt lgkmcnt(1)\n\t"
        "v_smfmac_f32_16x16x64_bf16 v[248:251], v[240:243], v[80:87], %[sidx]\n\t"
        "s_waitcnt lgkmcnt(0)\n\t"
        "v_smfmac_f32_16x16x64_bf16 v[248:251], v[244:247], v[88:95], %[sidx]\n\t"
        "v_smfmac_f32_16x16x64_bf16 v[252:255], v[224:227], v[128:135], %[sidx]\n\t"
        "v_smfmac_f32_16x16x64_bf16 v[252:255], v[228:231], v[136:143], %[sidx]\n\t"
        "v_add_f32 v248, v248, v250\n\t"
        "v_smfmac_f32_16x16x64_bf16 v[252:255], v[232:235], v[144:151], %[sidx]\n\t"
        "v_add_f32 v249, v249, v251\n\t"
        "v_smfmac_f32_16x16x64_bf16 v[252:255], v[236:239], v[152:159], %[sidx]\n\t"
        "v_cvt_pk_bf16_f32 v200, v248, v249\n\t"
        "v_smfmac_f32_16x16x64_bf16 v[252:255], v[240:243], v[160:167], %[sidx]\n\t"
        "ds_write_b32 %[ow], v200\n\t"
        "v_smfmac_f32_16x16x64_bf16 v[252:255], v[244:247], v[168:175], %[sidx]\n\t"
        "v_cmp_u_f32 vcc, v248, v249\n\t"
        "s_nop 6\n\t"
        "s_cbranch_vccnz Lnan%=\n\t"
        "Lok%=:\n\t"
        "v_add_f32 v252, v252, v254\n\t"
        "v_add_f32 v253, v253, v255\n\t"
        "v_cvt_pk_bf16_f32 v201, v252, v253\n\t"
        "ds_write_b32 %[ow], v201 offset:3072\n\t"
        "s_waitcnt lgkmcnt(0)\n\t"
        "s_nop 15\n\t"
        "ds_read_b64_tr_b16 v[180:181], %[qtr] offset:0\n\t"
        "ds_read_b64_tr_b16 v[182:183], %[qtr] offset:768\n\t"
        "ds_read_b64_tr_b16 v[184:185], %[qtr] offset:1536\n\t"
        "ds_read_b64_tr_b16 v[186:187], %[qtr] offset:2304\n\t"
        "ds_read_b64_tr_b16 v[188:189], %[qtr] offset:3072\n\t"
        "ds_read_b64_tr_b16 v[190:191], %[qtr] offset:3840\n\t"
        "ds_read_b64_tr_b16 v[192:193], %[qtr] offset:4608\n\t"
        "ds_read_b64_tr_b16 v[194:195], %[qtr] offset:5376\n\t"
        "s_nop 15\n\t"
        "s_nop 7\n\t"
        "ds_read_b64_tr_b16 v[224:225], %[ktr] offset:0\n\t"
        "ds_read_b64_tr_b16 v[226:227], %[ktr] offset:768\n\t"
        "ds_read_b64_tr_b16 v[228:229], %[ktr] offset:1536\n\t"
        "ds_read_b64_tr_b16 v[230:231], %[ktr] offset:2304\n\t"
        "s_nop 7\n\t"
        "ds_read_b64_tr_b16 v[232:233], %[ktr] offset:4608\n\t"
        "ds_read_b64_tr_b16 v[234:235], %[ktr] offset:5376\n\t"
        "s_waitcnt lgkmcnt(4)\n\t"
        "v_mfma_f32_16x16x32_bf16 v[248:251], v[224:227], v[180:183], 0\n\t"
        "s_waitcnt lgkmcnt(2)\n\t"
        "v_mfma_f32_16x16x32_bf16 v[248:251], v[228:231], v[184:187], v[248:251]\n\t"
        "s_waitcnt lgkmcnt(0)\n\t"
        "v_mfma_f32_16x16x32_bf16 v[248:251], v[232:235], v[192:195], v[248:251]\n\t"
        "s_barrier\n\t"
        "Lre%=:\n\t"
        "ds_read_b64_tr_b16 v[208:209], %[ktr] offset:3072\n\t"
        "ds_read_b64_tr_b16 v[210:211], %[ktr] offset:3840\n\t"
        "ds_read_b128 v[240:243], %[er]\n\t"
        "ds_read_b32 v207, v206 offset:25104\n\t"
        "s_waitcnt lgkmcnt(2)\n\t"
        "v_mfma_f32_16x16x32_bf16 v[248:251], v[208:211], v[188:191], v[248:251]\n\t"
        "s_waitcnt lgkmcnt(1)\n\t"
        "v_mfma_f32_16x16x32_bf16 v[244:247], v[176:179], v[240:243], 0\n\t"
        "s_nop 3\n\t"
        "v_mul_f32 v248, %[ce], v248\n\t"
        "v_mul_f32 v249, %[ce], v249\n\t"
        "v_exp_f32 v250, v248\n\t"
        "v_exp_f32 v251, v249\n\t"
        "s_nop 0\n\t"
        "v_cvt_pk_bf16_f32 v196, v250, v251\n\t"
        "v_fma_f32 v250, v250, %[c1], %[ind0]\n\t"
        "v_fma_f32 v251, v251, %[c1], %[ind1]\n\t"
        "v_mfma_f32_16x16x16_bf16 v[244:247], v[176:177], v[196:197], v[244:247]\n\t"
        "s_nop 7\n\t"
        "v_rcp_f32 v246, v244\n\t"
        "v_cmpx_gt_f32 vcc, %[big], v244\n\t"
        "v_pk_mul_f32 v[250:251], v[250:251], v[246:247] op_sel_hi:[1,0]\n\t"
        "v_cvt_pk_bf16_f32 v250, v250, v251\n\t"
        "buffer_store_dword v250, %[po], %[rs], %[so] offen sc1\n\t"
        "buffer_store_dword v250, %[po], %[rs], s92 offen sc1\n\t"
        "buffer_store_dword v250, %[po], %[rs], s93 offen sc1\n\t"
        "buffer_store_dword v250, %[po], %[rs], s94 offen sc1\n\t"
        "s_mov_b64 exec, -1\n\t"
        "s_cmp_eq_u32 %[bad], 1\n\t"
        "s_cbranch_scc1 Ltw%=\n\t"
        "s_cmp_lg_u64 vcc, -1\n\t"
        "s_cselect_b32 %[bad], 1, 0\n\t"
        "s_waitcnt lgkmcnt(0)\n\t"
        "v_cmp_eq_u32 vcc, %[rnd], v207\n\t"
        "s_barrier\n\t"
        "s_cbranch_vccnz Ltop%=\n\t"
        "s_cbranch_scc0 Lend%=\n\t"
        "ds_read_b64_tr_b16 v[180:181], %[qtr] offset:0\n\t"
        "ds_read_b64_tr_b16 v[182:183], %[qtr] offset:768\n\t"
        "ds_read_b64_tr_b16 v[184:185], %[qtr] offset:1536\n\t"
        "ds_read_b64_tr_b16 v[186:187], %[qtr] offset:2304\n\t"
        "ds_read_b64_tr_b16 v[188:189], %[qtr] offset:3072\n\t"
        "ds_read_b64_tr_b16 v[190:191], %[qtr] offset:3840\n\t"
        "ds_read_b64_tr_b16 v[192:193], %[qtr] offset:4608\n\t"
        "ds_read_b64_tr_b16 v[194:195], %[qtr] offset:5376\n\t"
        "ds_read_b64_tr_b16 v[224:225], %[ktr] offset:0\n\t"
        "ds_read_b64_tr_b16 v[226:227], %[ktr] offset:768\n\t"
        "ds_read_b64_tr_b16 v[228:229], %[ktr] offset:1536\n\t"
        "ds_read_b64_tr_b16 v[230:231], %[ktr] offset:2304\n\t"
        "ds_read_b64_tr_b16 v[232:233], %[ktr] offset:4608\n\t"
        "ds_read_b64_tr_b16 v[234:235], %[ktr] offset:5376\n\t"
        "s_waitcnt lgkmcnt(0)\n\t"
        "v_mfma_f32_16x16x32_bf16 v[248:251], v[224:227], v[180:183], 0\n\t"
        "v_mfma_f32_16x16x32_bf16 v[248:251], v[228:231], v[184:187], v[248:251]\n\t"
        "v_mfma_f32_16x16x32_bf16 v[248:251], v[232:235], v[192:195], v[248:251]\n\t"
        "s_branch Lre%=\n\t"
        "Lnan%=:\n\t"
        "v_max_u32 v207, v208, v209\n\t"
        "v_max3_u32 v207, v207, v210, v211\n\t"
        "v_max3_u32 v207, v207, v212, v213\n\t"
        "v_max3_u32 v207, v207, v214, v215\n\t"
        "v_max3_u32 v207, v207, v216, v217\n\t"
        "v_max3_u32 v207, v207, v218, v219\n\t"
        "v_max3_u32 v207, v207, v220, v221\n\t"
        "v_max3_u32 v207, v207, v222, v223\n\t"
        "v_max3_u32 v207, v207, v224, v225\n\t"
        "v_max3_u32 v207, v207, v226, v227\n\t"
        "v_max3_u32 v207, v207, v228, v229\n\t"
        "v_max3_u32 v207, v207, v230, v231\n\t"
        "v_max3_u32 v207, v207, v232, v233\n\t"
        "v_max3_u32 v207, v207, v234, v235\n\t"
        "v_max3_u32 v207, v207, v236, v237\n\t"
        "v_max3_u32 v207, v207, v238, v239\n\t"
        "v_max3_u32 v207, v207, v240, v241\n\t"
        "v_max3_u32 v207, v207, v242, v243\n\t"
        "v_max3_u32 v207, v207, v244, v245\n\t"
        "v_max3_u32 v207, v207, v246, v247\n\t"
        "v_cmp_eq_u32 vcc, 0xffbfffbf, v207\n\t"
        "v_mov_b32 v207, %[rnd]\n\t"
        "s_cbranch_vccz Lok%=\n\t"
        "ds_write_b32 v206, v207 offset:25104\n\t"
        "s_waitcnt lgkmcnt(0)\n\t"
        "s_barrier\n\t"
        "s_barrier\n\t"
        "s_branch Ltop%=\n\t"
        "Ltw%=:\n\t"
        "s_cmp_lg_u64 vcc, -1\n\t"
        "s_cselect_b32 %[bad], 1, 0\n\t"
        "s_waitcnt lgkmcnt(0)\n\t"
        "Lend%=:\n\t"
        "s_setprio 0\n\t"
        : [rnd] "+&s"(rnd), [bad] "=&s"(bad)
        : [w0] "{v[16:47]}"(W[0]), [w1] "{v[48:79]}"(W[1]), [w2] "{v[80:111]}"(W[2]), [w3] "{v[112:143]}"(W[3]), [w4] "{v[144:175]}"(W[4]), [va] "v"(va), [sidx] "v"(sidx), [ow] "v"(ow), [lb] "s"(lb), [ones] "{v[176:179]}"(ones), [qtr] "v"(qtr), [ktr] "v"(ktr), [er] "v"(er), [po] "v"(po), [c1] "v"(c1), [ind0] "v"(ind0), [ind1] "v"(ind1), [rs] "s"(rs), [so] "s"(so), [ce] "s"(ce), [big] "s"(big)
        : "s92", "s93", "s94", "v180", "v181", "v182", "v183", "v184", "v185", "v186", "v187", "v188", "v189", "v190", "v191", "v192", "v193", "v194", "v195", "v196", "v197", "v198", "v199", "v200", "v201", "v202", "v203", "v204", "v205", "v206", "v207", "v208", "v209", "v210", "v211", "v212", "v213", "v214", "v215", "v216", "v217", "v218", "v219", "v220", "v221", "v222", "v223", "v224", "v225", "v226", "v227", "v228", "v229", "v230", "v231", "v232", "v233", "v234", "v235", "v236", "v237", "v238", "v239", "v240", "v241", "v242", "v243", "v244", "v245", "v246", "v247", "v248", "v249", "v250", "v251", "v252", "v253", "v254", "v255", "vcc", "scc", "memory");
}

__device__ __forceinline__ void s_m(const u32x32 (&W)[5], u32& rnd, u32 va, u32 sidx, u32 ow, u32 lb, u32& left, u32& s, u32& so, u32& tg, u32& kr, u32& c0, u32& r0, u32& ok, u32& cnt, u32 vm, u32 tc, u32 tv, u32 md, u32x4 rs, u32 poll, u32 last STP LGP) {
    asm volatile(
        "s_mov_b32 s94, m0\n\t"
        "Lst%=:\n\t"
        "v_readlane_b32 s60, %[tc], %[s]\n\t"
        "s_add_u32 s92, %[so], 0x8000\n\t"
        "s_add_u32 s93, %[so], 0x10000\n\t"
        "s_mov_b32 m0, %[md]\n\t"
        "s_add_u32 s62, %[s], 1\n\t"
        "s_mov_b32 s58, %[c0]\n\t"
        "s_mov_b32 s59, %[r0]\n\t"
        "s_mov_b32 s63, %[rnd]\n\t"
        "s_setprio 3\n\t"
        "s_add_u32 s61, s60, 0x10400\n\t"
        "s_mov_b32 s96, %[left]\n\t"
        "s_cmp_eq_u32 %[poll], 0\n\t"
        "s_cbranch_scc1 Ltop%=\n\t"
        "v_add_u32 v204, %[md], %[vm]\n\t"
        "v_mov_b32 v208, 0xffbfffbf\n\t"
        "v_mov_b32 v209, v208\n\t"
        "v_mov_b64 v[210:211], v[208:209]\n\t"
        "ds_write_b128 v204, v[208:211] offset:5120\n\t"
        "ds_write_b128 v204, v[208:211] offset:6144\n\t"
        "ds_write_b128 v204, v[208:211] offset:7168\n\t"
        "ds_write_b128 v204, v[208:211] offset:8192\n\t"
        "ds_write_b128 v204, v[208:211] offset:10240\n\t"
        "Lda_%=:\n\t"
        "s_memrealtime s[90:91]\n\t"
        "s_waitcnt lgkmcnt(0)\n\t"
        "s_mul_i32 s90, s90, 24\n\t"
        "s_sub_u32 s88, %[tg], s90\n\t"
        "s_sub_u32 s89, s88, 620\n\t"
        "s_cmp_gt_u32 s89, 0x4000\n\t"
        "s_cbranch_scc1 Lea_%=\n\t"
        "s_sleep 1\n\t"
        "s_branch Lda_%=\n\t"
        "Lea_%=:\n\t"
        "s_memrealtime s[64:65]\n\t"
        "s_nop 0\n\t"
        "s_memrealtime s[66:67]\n\t"
        "s_nop 0\n\t"
        "s_memrealtime s[68:69]\n\t"
        "s_nop 0\n\t"
        "s_memrealtime s[70:71]\n\t"
        "s_nop 0\n\t"
        "s_memrealtime s[72:73]\n\t"
        "s_nop 0\n\t"
        "s_memrealtime s[74:75]\n\t"
        "s_memtime s[90:91]\n\t"
        "s_memrealtime s[76:77]\n\t"
        "s_nop 0\n\t"
        "s_memrealtime s[78:79]\n\t"
        "s_nop 0\n\t"
        "s_memrealtime s[80:81]\n\t"
        "s_nop 0\n\t"
        "s_memrealtime s[82:83]\n\t"
        "s_nop 0\n\t"
        "s_memrealtime s[84:85]\n\t"
        "s_nop 0\n\t"
        "s_memrealtime s[86:87]\n\t"
        "s_nop 0\n\t"
        "s_nop 7\n\t"
        "s_waitcnt lgkmcnt(0)\n\t"
        "s_add_u32 s64, s64, s66\n\t"
        "s_add_u32 s64, s64, s68\n\t"
        "s_add_u32 s64, s64, s70\n\t"
        "s_add_u32 s64, s64, s72\n\t"
        "s_add_u32 s64, s64, s74\n\t"
        "s_add_u32 s64, s64, s76\n\t"
        "s_add_u32 s64, s64, s78\n\t"
        "s_add_u32 s64, s64, s80\n\t"
        "s_add_u32 s64, s64, s82\n\t"
        "s_add_u32 s64, s64, s84\n\t"
        "s_add_u32 s64, s64, s86\n\t"
        "s_lshl_b32 s64, s64, 1\n\t"
        "s_mov_b32 %[c0], s90\n\t"
        "s_mov_b32 %[r0], s64\n\t"
        "s_sub_u32 s88, %[tg], s64\n\t"
        "s_mul_i32 s88, s88, %[kr]\n\t"
        "s_ashr_i32 s88, s88, 12\n\t"
        "s_sub_u32 s88, s88, 244\n\t"
        "s_nop 1\n\t"
        "s_max_i32 s88, s88, 0\n\t"
        "s_min_i32 s88, s88, 384\n\t"
        "s_andn2_b32 s88, s88, 3\n\t"
        "s_getpc_b64 s[90:91]\n\t"
        "Lga_%=:\n\t"
        "s_sub_u32 s90, s90, s88\n\t"
        "s_subb_u32 s91, s91, 0\n\t"
        "s_add_u32 s90, s90, Lza_%=-Lga_%=\n\t"
        "s_addc_u32 s91, s91, 0\n\t"
        "s_setpc_b64 s[90:91]\n\t"
        "s_nop 0\n\ts_nop 0\n\ts_nop 0\n\ts_nop 0\n\t"
        "s_nop 0\n\ts_nop 0\n\ts_nop 0\n\ts_nop 0\n\t"
        "s_nop 0\n\ts_nop 0\n\ts_nop 0\n\ts_nop 0\n\t"
        "s_nop 0\n\ts_nop 0\n\ts_nop 0\n\ts_nop 0\n\t"
        "s_nop 0\n\ts_nop 0\n\ts_nop 0\n\ts_nop 0\n\t"
        "s_nop 0\n\ts_nop 0\n\ts_nop 0\n\ts_nop 0\n\t"
        "s_nop 0\n\ts_nop 0\n\ts_nop 0\n\ts_nop 0\n\t"
        "s_nop 0\n\ts_nop 0\n\ts_nop 0\n\ts_nop 0\n\t"
        "s_nop 0\n\ts_nop 0\n\ts_nop 0\n\ts_nop 0\n\t"
        "s_nop 0\n\ts_nop 0\n\ts_nop 0\n\ts_nop 0\n\t"
        "s_nop 0\n\ts_nop 0\n\ts_nop 0\n\ts_nop 0\n\t"
        "s_nop 0\n\ts_nop 0\n\ts_nop 0\n\ts_nop 0\n\t"
        "s_nop 0\n\ts_nop 0\n\ts_nop 0\n\ts_nop 0\n\t"
        "s_nop 0\n\ts_nop 0\n\ts_nop 0\n\ts_nop 0\n\t"
        "s_nop 0\n\ts_nop 0\n\ts_nop 0\n\ts_nop 0\n\t"
        "s_nop 0\n\ts_nop 0\n\ts_nop 0\n\ts_nop 0\n\t"
        "s_nop 0\n\ts_nop 0\n\ts_nop 0\n\ts_nop 0\n\t"
        "s_nop 0\n\ts_nop 0\n\ts_nop 0\n\ts_nop 0\n\t"
        "s_nop 0\n\ts_nop 0\n\ts_nop 0\n\ts_nop 0\n\t"
        "s_nop 0\n\ts_nop 0\n\ts_nop 0\n\ts_nop 0\n\t"
        "s_nop 0\n\ts_nop 0\n\ts_nop 0\n\ts_nop 0\n\t"
        "s_nop 0\n\ts_nop 0\n\ts_nop 0\n\ts_nop 0\n\t"
        "s_nop 0\n\ts_nop 0\n\ts_nop 0\n\ts_nop 0\n\t"
        "s_nop 0\n\ts_nop 0\n\ts_nop 0\n\ts_nop 0\n\t"
        "Lza_%=:\n\t"
        "buffer_load_dwordx4 %[vm], %[rs], %[so] offen offset:0 sc1 lds\n\t"
        "buffer_load_dwordx4 %[vm], %[rs], %[so] offen offset:1024 sc1 lds\n\t"
        "buffer_load_dwordx4 %[vm], %[rs], %[so] offen offset:2048 sc1 lds\n\t"
        "buffer_load_dwordx4 %[vm], %[rs], %[so] offen offset:3072 sc1 lds\n\t"
        "s_add_u32 m0, m0, 0x1000\n\t"
        "s_nop 0\n\t"
        "buffer_load_dwordx4 %[vm], %[rs], s92 offen offset:0 sc1 lds\n\t"
        "buffer_load_dwordx4 %[vm], %[rs], s92 offen offset:1024 sc1 lds\n\t"
        "buffer_load_dwordx4 %[vm], %[rs], s92 offen offset:2048 sc1 lds\n\t"
        "buffer_load_dwordx4 %[vm], %[rs], s92 offen offset:3072 sc1 lds\n\t"
        "s_add_u32 m0, m0, 0x1000\n\t"
        "s_nop 0\n\t"
        "buffer_load_dwordx4 %[vm], %[rs], s93 offen offset:0 sc1 lds\n\t"
        "buffer_load_dwordx4 %[vm], %[rs], s93 offen offset:2048 sc1 lds\n\t"
        "s_barrier\n\t"
        "s_sub_u32 s59, %[r0], s59\n\t"
        "s_sub_u32 s58, %[c0], s58\n\t"
        "s_mul_i32 s88, s59, %[kr]\n\t"
        "s_lshr_b32 s88, s88, 12\n\t"
        "s_sub_u32 s58, s58, s88\n\t"
        "s_mul_i32 s58, s58, 3\n\t"
        "s_ashr_i32 s58, s58, 2\n\t"
        "s_sub_u32 s59, s59, 2048\n\t"
        "s_cmp_lt_u32 s59, 2560\n\t"
        "s_cselect_b32 s58, s58, 0\n\t"
        "s_add_u32 %[kr], %[kr], s58\n\t"
        "s_max_i32 %[kr], %[kr], 2560\n\t"
        "s_min_i32 %[kr], %[kr], 4352\n\t"
        "Ltop%=:\n\t"
        "v_mov_b64 v[248:249], 0\n\t"
        "v_mov_b64 v[250:251], 0\n\t"
        "v_mov_b64 v[252:253], 0\n\t"
        "v_mov_b64 v[254:255], 0\n\t"
        "v_mov_b32 v200, 0xffbfffbf\n\t"
        "v_mov_b32 v206, %[lb]\n\t"
        "s_add_u32 %[rnd], %[rnd], 1\n\t"
        "s_setprio 3\n\t"
        "Lmk%=:\n\t"
        "ds_write_b32 %[ow], v200\n\t"
        "ds_write_b32 %[ow], v200 offset:3072\n\t"
        "s_mov_b32 s95, 0\n\t"
        "s_waitcnt vmcnt(5)\n\t"
        "s_waitcnt lgkmcnt(0)\n\t"
        "s_barrier\n\t"
        "ds_read_b128 v[208:211], %[va] offset:0\n\t"
        "ds_read_b128 v[212:215], %[va] offset:1024\n\t"
        "ds_read_b128 v[216:219], %[va] offset:2048\n\t"
        "ds_read_b128 v[220:223], %[va] offset:3072\n\t"
        "ds_read_b128 v[224:227], %[va] offset:4096\n\t"
        "ds_read_b128 v[228:231], %[va] offset:5120\n\t"
        "s_waitcnt lgkmcnt(5)\n\t"
        "v_smfmac_f32_16x16x64_bf16 v[248:251], v[208:211], v[16:23], %[sidx]\n\t"
        "ds_read_b128 v[232:235], %[va] offset:6144\n\t"
        "v_smfmac_f32_16x16x64_bf16 v[252:255], v[208:211], v[96:103], %[sidx]\n\t"
        "s_waitcnt lgkmcnt(5)\n\t"
        "v_smfmac_f32_16x16x64_bf16 v[248:251], v[212:215], v[24:31], %[sidx]\n\t"
        "ds_read_b128 v[236:239], %[va] offset:7168\n\t"
        "v_smfmac_f32_16x16x64_bf16 v[252:255], v[212:215], v[104:111], %[sidx]\n\t"
        "s_waitcnt lgkmcnt(5)\n\t"
        "v_smfmac_f32_16x16x64_bf16 v[248:251], v[216:219], v[32:39], %[sidx]\n\t"
        "ds_read_b128 v[240:243], %[va] offset:8192\n\t"
        "v_smfmac_f32_16x16x64_bf16 v[252:255], v[216:219], v[112:119], %[sidx]\n\t"
        "s_waitcnt lgkmcnt(5)\n\t"
        "v_smfmac_f32_16x16x64_bf16 v[248:251], v[220:223], v[40:47], %[sidx]\n\t"
        "ds_read_b128 v[244:247], %[va] offset:10240\n\t"
        "v_smfmac_f32_16x16x64_bf16 v[252:255], v[220:223], v[120:127], %[sidx]\n\t"
        "s_waitcnt lgkmcnt(5)\n\t"
        "v_smfmac_f32_16x16x64_bf16 v[248:251], v[224:227], v[48:55], %[sidx]\n\t"
        "s_waitcnt lgkmcnt(4)\n\t"
        "v_smfmac_f32_16x16x64_bf16 v[248:251], v[228:231], v[56:63], %[sidx]\n\t"
        "s_waitcnt lgkmcnt(3)\n\t"
        "v_smfmac_f32_16x16x64_bf16 v[248:251], v[232:235], v[64:71], %[sidx]\n\t"
        "s_waitcnt lgkmcnt(2)\n\t"
        "v_smfmac_f32_16x16x64_bf16 v[248:251], v[236:239], v[72:79], %[sidx]\n\t"
        "s_waitcnt lgkmcnt(1)\n\t"
        "v_smfmac_f32_16x16x64_bf16 v[248:251], v[240:243], v[80:87], %[sidx]\n\t"
        "s_waitcnt lgkmcnt(0)\n\t"
        "v_smfmac_f32_16x16x64_bf16 v[248:251], v[244:247], v[88:95], %[sidx]\n\t"
        "v_smfmac_f32_16x16x64_bf16 v[252:255], v[224:227], v[128:135], %[sidx]\n\t"
        "v_smfmac_f32_16x16x64_bf16 v[252:255], v[228:231], v[136:143], %[sidx]\n\t"
        "v_add_f32 v248, v248, v250\n\t"
        "v_smfmac_f32_16x16x64_bf16 v[252:255], v[232:235], v[144:151], %[sidx]\n\t"
        "v_add_f32 v249, v249, v251\n\t"
        "v_smfmac_f32_16x16x64_bf16 v[252:255], v[236:239], v[152:159], %[sidx]\n\t"
        "v_cvt_pk_bf16_f32 v200, v248, v249\n\t"
        "v_smfmac_f32_16x16x64_bf16 v[252:255], v[240:243], v[160:167], %[sidx]\n\t"
        "ds_write_b32 %[ow], v200\n\t"
        "v_smfmac_f32_16x16x64_bf16 v[252:255], v[244:247], v[168:175], %[sidx]\n\t"
        "v_cmp_u_f32 vcc, v248, v249\n\t"
        "s_nop 6\n\t"
        "s_cbranch_vccnz Lnan%=\n\t"
        "Lok%=:\n\t"
        "v_add_f32 v252, v252, v254\n\t"
        "v_add_f32 v253, v253, v255\n\t"
        "v_cvt_pk_bf16_f32 v201, v252, v253\n\t"
        "ds_write_b32 %[ow], v201 offset:3072\n\t"
        "s_waitcnt lgkmcnt(0)\n\t"
        "s_cmp_lg_u32 s96, %[left]\n\t"
        "s_cbranch_scc1 Lv%=\n\t"
        "s_buffer_load_dwordx4 s[64:67], %[rs], s61 offset:0x0\n\t"
        "s_buffer_load_dwordx4 s[68:71], %[rs], s61 offset:0x80\n\t"
        "s_buffer_load_dwordx4 s[72:75], %[rs], s61 offset:0x100\n\t"
        "s_buffer_load_dwordx4 s[76:79], %[rs], s61 offset:0x180\n\t"
        "s_barrier\n\t"
        "s_setprio 0\n\t"
        "ds_read_b32 v207, v206 offset:25104\n\t"
        "s_waitcnt lgkmcnt(0)\n\t"
        "v_cmp_eq_u32 vcc, %[rnd], v207\n\t"
        "s_barrier\n\t"
        "s_cbranch_vccnz Lck%=\n\t"
        "s_xor_b32 s67, s67, s62\n\t"
        "s_xor_b32 s71, s71, s62\n\t"
        "s_xor_b32 s75, s75, s62\n\t"
        "s_xor_b32 s79, s79, s62\n\t"
        "s_sub_u32 s65, s65, %[r0]\n\t"
        "s_cmp_eq_u32 s67, 0\n\t"
        "s_cselect_b32 s65, s65, 0\n\t"
        "s_sub_u32 s69, s69, %[r0]\n\t"
        "s_cmp_eq_u32 s71, 0\n\t"
        "s_cselect_b32 s69, s69, 0\n\t"
        "s_sub_u32 s73, s73, %[r0]\n\t"
        "s_cmp_eq_u32 s75, 0\n\t"
        "s_cselect_b32 s73, s73, 0\n\t"
        "s_sub_u32 s77, s77, %[r0]\n\t"
        "s_cmp_eq_u32 s79, 0\n\t"
        "s_cselect_b32 s77, s77, 0\n\t"
        "s_max_u32 s65, s65, s69\n\t"
        "s_max_u32 s73, s73, s77\n\t"
        "s_max_u32 s65, s65, s73\n\t"
        "s_mov_b32 s64, s65\n\t"
        "s_sub_u32 s67, s67, 1\n\t"
        "s_cmp_lt_u32 s67, 0x20000\n\t"
        "s_cselect_b32 s64, 0, s64\n\t"
        "s_sub_u32 s71, s71, 1\n\t"
        "s_cmp_lt_u32 s71, 0x20000\n\t"
        "s_cselect_b32 s64, 0, s64\n\t"
        "s_sub_u32 s75, s75, 1\n\t"
        "s_cmp_lt_u32 s75, 0x20000\n\t"
        "s_cselect_b32 s64, 0, s64\n\t"
        "s_sub_u32 s79, s79, 1\n\t"
        "s_cmp_lt_u32 s79, 0x20000\n\t"
        "s_cselect_b32 s64, 0, s64\n\t"
        "s_add_u32 s65, s65, %[r0]\n\t"
        "s_cmp_lg_u32 s64, 0\n\t"
        "Lnx%=:\n\t"
        "s_cselect_b32 %[ok], 1, 0\n\t"
        "s_mov_b32 %[tg], s65\n\t"
        "s_mov_b32 %[so], s60\n\t"
        "s_sub_u32 s63, %[rnd], s63\n\t"
        "s_cmp_gt_u32 s63, 1\n\t"
        "s_addc_u32 %[cnt], %[cnt], 0\n\t"
        "s_mov_b32 %[s], s62\n\t"
        "s_waitcnt vmcnt(0)\n\t"
        "s_and_b32 s63, %[ok], %[poll]\n\t"
        "s_cmp_lt_i32 s62, %[last]\n\t"
        "s_cselect_b32 s63, s63, 0\n\t"
        "s_cmp_eq_u32 s63, 1\n\t"
        "s_cbranch_scc1 Lst%=\n\t"
        "s_branch Lend%=\n\t"
        "Lv%=:\n\t"
        "s_barrier\n\t"
        "buffer_load_dwordx4 v[196:199], %[tv], %[rs], s61 offen sc1\n\t"
        "s_setprio 0\n\t"
        "ds_read_b32 v207, v206 offset:25104\n\t"
        "s_waitcnt lgkmcnt(0)\n\t"
        "v_cmp_eq_u32 vcc, %[rnd], v207\n\t"
        "s_barrier\n\t"
        "s_cbranch_vccnz Lck%=\n\t"
        "s_sub_u32 s97, %[r0], 0x100000\n\t"
        "s_waitcnt vmcnt(0)\n\t"
        "v_cmp_eq_u32 vcc, s62, v199\n\t"
        "v_subrev_u32 v196, s97, v196\n\t"
        "v_subrev_u32 v197, s97, v197\n\t"
        "v_cndmask_b32 v196, 0, v196, vcc\n\t"
        "v_cndmask_b32 v197, 0, v197, vcc\n\t"
        "v_cndmask_b32 v198, 0, v198, vcc\n\t"
        "v_max_u32_dpp v196, v196, v196 quad_perm:[1,0,3,2] row_mask:0xf bank_mask:0xf\n\t"
        "v_max_u32_dpp v197, v197, v197 quad_perm:[1,0,3,2] row_mask:0xf bank_mask:0xf\n\t"
        "v_max_u32_dpp v198, v198, v198 quad_perm:[1,0,3,2] row_mask:0xf bank_mask:0xf\n\t"
        "v_max_u32_dpp v196, v196, v196 quad_perm:[2,3,0,1] row_mask:0xf bank_mask:0xf\n\t"
        "v_max_u32_dpp v197, v197, v197 quad_perm:[2,3,0,1] row_mask:0xf bank_mask:0xf\n\t"
        "v_max_u32_dpp v198, v198, v198 quad_perm:[2,3,0,1] row_mask:0xf bank_mask:0xf\n\t"
        "v_max_u32_dpp v196, v196, v196 row_half_mirror row_mask:0xf bank_mask:0xf\n\t"
        "v_max_u32_dpp v197, v197, v197 row_half_mirror row_mask:0xf bank_mask:0xf\n\t"
        "v_max_u32_dpp v198, v198, v198 row_half_mirror row_mask:0xf bank_mask:0xf\n\t"
        "s_lshr_b32 s96, vcc_lo, 4\n\t"
        "v_readfirstlane_b32 s65, v197\n\t"
        "s_add_u32 s65, s65, s97\n\t"
        "s_or_b32 s96, s96, vcc_lo\n\t"
        "s_and_b32 s96, s96, 15\n\t"
        "s_cmp_eq_u32 s96, 15\n\t"
        "s_branch Lnx%=\n\t"
        "Lnan%=:\n\t"
        "s_cmp_lg_u32 %[poll], 1\n\t"
        "s_cbranch_scc1 Lok%=\n\t"
        "s_mov_b32 m0, %[lb]\n\t"
        "v_add_u32 v204, %[lb], %[vm]\n\t"
        "buffer_load_dwordx4 %[vm], %[rs], %[so] offen offset:0 sc1 lds\n\t"
        "buffer_load_dwordx4 %[vm], %[rs], %[so] offen offset:1024 sc1 lds\n\t"
        "buffer_load_dwordx4 %[vm], %[rs], %[so] offen offset:2048 sc1 lds\n\t"
        "buffer_load_dwordx4 %[vm], %[rs], %[so] offen offset:3072 sc1 lds\n\t"
        "s_add_u32 m0, m0, 0x1000\n\t"
        "s_nop 0\n\t"
        "buffer_load_dwordx4 %[vm], %[rs], s92 offen offset:0 sc1 lds\n\t"
        "buffer_load_dwordx4 %[vm], %[rs], s92 offen offset:1024 sc1 lds\n\t"
        "buffer_load_dwordx4 %[vm], %[rs], s92 offen offset:2048 sc1 lds\n\t"
        "buffer_load_dwordx4 %[vm], %[rs], s92 offen offset:3072 sc1 lds\n\t"
        "s_add_u32 m0, m0, 0x1000\n\t"
        "s_nop 0\n\t"
        "buffer_load_dwordx4 %[vm], %[rs], s93 offen offset:0 sc1 lds\n\t"
        "buffer_load_dwordx4 %[vm], %[rs], s93 offen offset:2048 sc1 lds\n\t"
        "v_max_u32 v207, v208, v209\n\t"
        "v_max3_u32 v207, v207, v210, v211\n\t"
        "v_max3_u32 v207, v207, v212, v213\n\t"
        "v_max3_u32 v207, v207, v214, v215\n\t"
        "v_max3_u32 v207, v207, v216, v217\n\t"
        "v_max3_u32 v207, v207, v218, v219\n\t"
        "v_max3_u32 v207, v207, v220, v221\n\t"
        "v_max3_u32 v207, v207, v222, v223\n\t"
        "v_max3_u32 v207, v207, v224, v225\n\t"
        "v_max3_u32 v207, v207, v226, v227\n\t"
        "v_max3_u32 v207, v207, v228, v229\n\t"
        "v_max3_u32 v207, v207, v230, v231\n\t"
        "v_max3_u32 v207, v207, v232, v233\n\t"
        "v_max3_u32 v207, v207, v234, v235\n\t"
        "v_max3_u32 v207, v207, v236, v237\n\t"
        "v_max3_u32 v207, v207, v238, v239\n\t"
        "v_max3_u32 v207, v207, v240, v241\n\t"
        "v_max3_u32 v207, v207, v242, v243\n\t"
        "v_max3_u32 v207, v207, v244, v245\n\t"
        "v_max3_u32 v207, v207, v246, v247\n\t"
        "v_cmp_eq_u32 vcc, 0xffbfffbf, v207\n\t"
        "v_mov_b32 v207, %[rnd]\n\t"
        "s_cbranch_vccz Lok%=\n\t"
        "ds_write_b32 v206, v207 offset:25104\n\t"
        "s_mov_b32 s95, 1\n\t"
        "s_waitcnt lgkmcnt(0)\n\t"
        "s_branch Lcv%=\n\t"
        "Lck%=:\n\t"
        "v_add_u32 v204, %[lb], %[vm]\n\t"
        "Lcl%=:\n\t"
        "s_mov_b32 m0, %[lb]\n\t"
        "s_nop 0\n\t"
        "buffer_load_dwordx4 %[vm], %[rs], %[so] offen offset:0 sc1 lds\n\t"
        "buffer_load_dwordx4 %[vm], %[rs], %[so] offen offset:1024 sc1 lds\n\t"
        "buffer_load_dwordx4 %[vm], %[rs], %[so] offen offset:2048 sc1 lds\n\t"
        "buffer_load_dwordx4 %[vm], %[rs], %[so] offen offset:3072 sc1 lds\n\t"
        "s_add_u32 m0, m0, 0x1000\n\t"
        "s_nop 0\n\t"
        "buffer_load_dwordx4 %[vm], %[rs], s92 offen offset:0 sc1 lds\n\t"
        "buffer_load_dwordx4 %[vm], %[rs], s92 offen offset:1024 sc1 lds\n\t"
        "buffer_load_dwordx4 %[vm], %[rs], s92 offen offset:2048 sc1 lds\n\t"
        "buffer_load_dwordx4 %[vm], %[rs], s92 offen offset:3072 sc1 lds\n\t"
        "s_add_u32 m0, m0, 0x1000\n\t"
        "s_nop 0\n\t"
        "buffer_load_dwordx4 %[vm], %[rs], s93 offen offset:0 sc1 lds\n\t"
        "buffer_load_dwordx4 %[vm], %[rs], s93 offen offset:2048 sc1 lds\n\t"
        "Lcv%=:\n\t"
        "s_sub_u32 %[left], %[left], 1\n\t"
        "s_waitcnt vmcnt(9)\n\t"
        "ds_read_b128 v[208:211], v204 offset:0\n\t"
        "s_waitcnt vmcnt(8)\n\t"
        "ds_read_b128 v[212:215], v204 offset:1024\n\t"
        "s_waitcnt vmcnt(7)\n\t"
        "ds_read_b128 v[216:219], v204 offset:2048\n\t"
        "s_waitcnt vmcnt(6)\n\t"
        "ds_read_b128 v[220:223], v204 offset:3072\n\t"
        "s_waitcnt vmcnt(5)\n\t"
        "ds_read_b128 v[224:227], v204 offset:4096\n\t"
        "s_waitcnt vmcnt(4)\n\t"
        "ds_read_b128 v[228:231], v204 offset:5120\n\t"
        "s_waitcnt lgkmcnt(5)\n\t"
        "v_max3_u32 v207, v208, v208, v209\n\t"
        "v_max3_u32 v207, v207, v210, v211\n\t"
        "s_waitcnt vmcnt(3)\n\t"
        "ds_read_b128 v[232:235], v204 offset:6144\n\t"
        "s_waitcnt lgkmcnt(5)\n\t"
        "v_max3_u32 v207, v207, v212, v213\n\t"
        "v_max3_u32 v207, v207, v214, v215\n\t"
        "s_waitcnt vmcnt(2)\n\t"
        "ds_read_b128 v[236:239], v204 offset:7168\n\t"
        "s_waitcnt lgkmcnt(5)\n\t"
        "v_max3_u32 v207, v207, v216, v217\n\t"
        "v_max3_u32 v207, v207, v218, v219\n\t"
        "s_waitcnt vmcnt(1)\n\t"
        "ds_read_b128 v[240:243], v204 offset:8192\n\t"
        "s_waitcnt lgkmcnt(5)\n\t"
        "v_max3_u32 v207, v207, v220, v221\n\t"
        "v_max3_u32 v207, v207, v222, v223\n\t"
        "s_waitcnt vmcnt(0)\n\t"
        "ds_read_b128 v[244:247], v204 offset:10240\n\t"
        "s_waitcnt lgkmcnt(5)\n\t"
        "v_max3_u32 v207, v207, v224, v225\n\t"
        "v_max3_u32 v207, v207, v226, v227\n\t"
        "s_waitcnt lgkmcnt(4)\n\t"
        "v_max3_u32 v207, v207, v228, v229\n\t"
        "v_max3_u32 v207, v207, v230, v231\n\t"
        "s_waitcnt lgkmcnt(3)\n\t"
        "v_max3_u32 v207, v207, v232, v233\n\t"
        "v_max3_u32 v207, v207, v234, v235\n\t"
        "s_waitcnt lgkmcnt(2)\n\t"
        "v_max3_u32 v207, v207, v236, v237\n\t"
        "v_max3_u32 v207, v207, v238, v239\n\t"
        "s_waitcnt lgkmcnt(1)\n\t"
        "v_max3_u32 v207, v207, v240, v241\n\t"
        "v_max3_u32 v207, v207, v242, v243\n\t"
        "s_waitcnt lgkmcnt(0)\n\t"
        "v_max3_u32 v207, v207, v244, v245\n\t"
        "v_max3_u32 v207, v207, v246, v247\n\t"
        "v_cmp_eq_u32 vcc, 0xffbfffbf, v207\n\t"
        "s_cbranch_vccz Lcd%=\n\t"
        "s_cbranch_scc0 Lcl%=\n\t"
        "Lcd%=:\n\t"
        "s_max_i32 %[left], %[left], 0\n\t"
        "s_cmp_eq_u32 s95, 0\n\t"
        "s_cbranch_scc1 Lce%=\n\t"
        "s_barrier\n\t"
        "s_barrier\n\t"
        "Lce%=:\n\t"
        "s_cbranch_vccz Ltop%=\n\t"
        "s_mov_b32 %[ok], 2\n\t"
        "Lend%=:\n\t"
        "s_mov_b32 m0, s94\n\t"
        "s_waitcnt vmcnt(0)\n\t"
        : [rnd] "+&s"(rnd), [s] "+&s"(s), [so] "+&s"(so), [tg] "+&s"(tg), [kr] "+&s"(kr), [c0] "+&s"(c0), [r0] "+&s"(r0), [ok] "=&s"(ok), [cnt] "+&s"(cnt), [left] "+&s"(left)
        : [w0] "{v[16:47]}"(W[0]), [w1] "{v[48:79]}"(W[1]), [w2] "{v[80:111]}"(W[2]), [w3] "{v[112:143]}"(W[3]), [w4] "{v[144:175]}"(W[4]), [va] "v"(va), [sidx] "v"(sidx), [ow] "v"(ow), [vm] "v"(vm), [tc] "v"(tc), [tv] "v"(tv), [rs] "s"(rs), [lb] "s"(lb), [md] "s"(md), [poll] "s"(poll), [last] "s"(last)
        : "s56", "s57", "s58", "s59", "s60", "s61", "s62", "s63", "s64", "s65", "s66", "s67", "s68", "s69", "s70", "s71", "s72", "s73", "s74", "s75", "s76", "s77", "s78", "s79", "s80", "s81", "s82", "s83", "s84", "s85", "s86", "s87", "s88", "s89", "s90", "s91", "s92", "s93", "s94", "s95", "s96", "s97", "s98", "s99", "v196", "v197", "v198", "v199", "v200", "v201", "v202", "v203", "v204", "v205", "v206", "v207", "v208", "v209", "v210", "v211", "v212", "v213", "v214", "v215", "v216", "v217", "v218", "v219", "v220", "v221", "v222", "v223", "v224", "v225", "v226", "v227", "v228", "v229", "v230", "v231", "v232", "v233", "v234", "v235", "v236", "v237", "v238", "v239", "v240", "v241", "v242", "v243", "v244", "v245", "v246", "v247", "v248", "v249", "v250", "v251", "v252", "v253", "v254", "v255", "vcc", "scc", "memory");
}

__device__ __forceinline__ void gather(u32 tg, u32 kr, u32 vm, u32 so, u32 lb, u32x4 rs) {
    asm volatile(
        "s_mov_b32 s94, m0\n\t"
        "s_add_u32 s92, %[so], 0x8000\n\t"
        "s_add_u32 s93, %[so], 0x10000\n\t"
        "s_mov_b32 m0, %[lb]\n\t"
        "Lda_%=:\n\t"
        "s_memrealtime s[90:91]\n\t"
        "s_waitcnt lgkmcnt(0)\n\t"
        "s_mul_i32 s90, s90, 24\n\t"
        "s_sub_u32 s88, %[tg], s90\n\t"
        "s_sub_u32 s89, s88, 620\n\t"
        "s_cmp_gt_u32 s89, 0x4000\n\t"
        "s_cbranch_scc1 Lea_%=\n\t"
        "s_sleep 1\n\t"
        "s_branch Lda_%=\n\t"
        "Lea_%=:\n\t"
        "s_cmp_lt_i32 s88, 120\n\t"
        "s_cbranch_scc1 Lza_%=\n\t"
        "s_memrealtime s[64:65]\n\t"
        "s_nop 0\n\t"
        "s_memrealtime s[66:67]\n\t"
        "s_nop 0\n\t"
        "s_memrealtime s[68:69]\n\t"
        "s_nop 0\n\t"
        "s_memrealtime s[70:71]\n\t"
        "s_nop 0\n\t"
        "s_memrealtime s[72:73]\n\t"
        "s_nop 0\n\t"
        "s_memrealtime s[74:75]\n\t"
        "s_memtime s[90:91]\n\t"
        "s_memrealtime s[76:77]\n\t"
        "s_nop 0\n\t"
        "s_memrealtime s[78:79]\n\t"
        "s_nop 0\n\t"
        "s_memrealtime s[80:81]\n\t"
        "s_nop 0\n\t"
        "s_memrealtime s[82:83]\n\t"
        "s_nop 0\n\t"
        "s_memrealtime s[84:85]\n\t"
        "s_nop 0\n\t"
        "s_memrealtime s[86:87]\n\t"
        "s_nop 0\n\t"
        "s_nop 7\n\t"
        "s_waitcnt lgkmcnt(0)\n\t"
        "s_add_u32 s64, s64, s66\n\t"
        "s_add_u32 s64, s64, s68\n\t"
        "s_add_u32 s64, s64, s70\n\t"
        "s_add_u32 s64, s64, s72\n\t"
        "s_add_u32 s64, s64, s74\n\t"
        "s_add_u32 s64, s64, s76\n\t"
        "s_add_u32 s64, s64, s78\n\t"
        "s_add_u32 s64, s64, s80\n\t"
        "s_add_u32 s64, s64, s82\n\t"
        "s_add_u32 s64, s64, s84\n\t"
        "s_add_u32 s64, s64, s86\n\t"
        "s_lshl_b32 s64, s64, 1\n\t"
        "s_nop 1\n\t"
        "s_sub_u32 s88, %[tg], s64\n\t"
        "s_mul_i32 s88, s88, %[kr]\n\t"
        "s_ashr_i32 s88, s88, 12\n\t"
        "s_sub_u32 s88, s88, 244\n\t"
        "s_nop 1\n\t"
        "s_max_i32 s88, s88, 0\n\t"
        "s_min_i32 s88, s88, 384\n\t"
        "s_andn2_b32 s88, s88, 3\n\t"
        "s_getpc_b64 s[90:91]\n\t"
        "Lga_%=:\n\t"
        "s_sub_u32 s90, s90, s88\n\t"
        "s_subb_u32 s91, s91, 0\n\t"
        "s_add_u32 s90, s90, Lza_%=-Lga_%=\n\t"
        "s_addc_u32 s91, s91, 0\n\t"
        "s_setpc_b64 s[90:91]\n\t"
        "s_nop 0\n\ts_nop 0\n\ts_nop 0\n\ts_nop 0\n\t"
        "s_nop 0\n\ts_nop 0\n\ts_nop 0\n\ts_nop 0\n\t"
        "s_nop 0\n\ts_nop 0\n\ts_nop 0\n\ts_nop 0\n\t"
        "s_nop 0\n\ts_nop 0\n\ts_nop 0\n\ts_nop 0\n\t"
        "s_nop 0\n\ts_nop 0\n\ts_nop 0\n\ts_nop 0\n\t"
        "s_nop 0\n\ts_nop 0\n\ts_nop 0\n\ts_nop 0\n\t"
        "s_nop 0\n\ts_nop 0\n\ts_nop 0\n\ts_nop 0\n\t"
        "s_nop 0\n\ts_nop 0\n\ts_nop 0\n\ts_nop 0\n\t"
        "s_nop 0\n\ts_nop 0\n\ts_nop 0\n\ts_nop 0\n\t"
        "s_nop 0\n\ts_nop 0\n\ts_nop 0\n\ts_nop 0\n\t"
        "s_nop 0\n\ts_nop 0\n\ts_nop 0\n\ts_nop 0\n\t"
        "s_nop 0\n\ts_nop 0\n\ts_nop 0\n\ts_nop 0\n\t"
        "s_nop 0\n\ts_nop 0\n\ts_nop 0\n\ts_nop 0\n\t"
        "s_nop 0\n\ts_nop 0\n\ts_nop 0\n\ts_nop 0\n\t"
        "s_nop 0\n\ts_nop 0\n\ts_nop 0\n\ts_nop 0\n\t"
        "s_nop 0\n\ts_nop 0\n\ts_nop 0\n\ts_nop 0\n\t"
        "s_nop 0\n\ts_nop 0\n\ts_nop 0\n\ts_nop 0\n\t"
        "s_nop 0\n\ts_nop 0\n\ts_nop 0\n\ts_nop 0\n\t"
        "s_nop 0\n\ts_nop 0\n\ts_nop 0\n\ts_nop 0\n\t"
        "s_nop 0\n\ts_nop 0\n\ts_nop 0\n\ts_nop 0\n\t"
        "s_nop 0\n\ts_nop 0\n\ts_nop 0\n\ts_nop 0\n\t"
        "s_nop 0\n\ts_nop 0\n\ts_nop 0\n\ts_nop 0\n\t"
        "s_nop 0\n\ts_nop 0\n\ts_nop 0\n\ts_nop 0\n\t"
        "s_nop 0\n\ts_nop 0\n\ts_nop 0\n\ts_nop 0\n\t"
        "Lza_%=:\n\t"
        "buffer_load_dwordx4 %[vm], %[rs], %[so] offen offset:0 sc1 lds\n\t"
        "buffer_load_dwordx4 %[vm], %[rs], %[so] offen offset:1024 sc1 lds\n\t"
        "buffer_load_dwordx4 %[vm], %[rs], %[so] offen offset:2048 sc1 lds\n\t"
        "buffer_load_dwordx4 %[vm], %[rs], %[so] offen offset:3072 sc1 lds\n\t"
        "s_add_u32 m0, m0, 0x1000\n\t"
        "s_nop 0\n\t"
        "buffer_load_dwordx4 %[vm], %[rs], s92 offen offset:0 sc1 lds\n\t"
        "buffer_load_dwordx4 %[vm], %[rs], s92 offen offset:1024 sc1 lds\n\t"
        "buffer_load_dwordx4 %[vm], %[rs], s92 offen offset:2048 sc1 lds\n\t"
        "buffer_load_dwordx4 %[vm], %[rs], s92 offen offset:3072 sc1 lds\n\t"
        "s_add_u32 m0, m0, 0x1000\n\t"
        "s_nop 0\n\t"
        "buffer_load_dwordx4 %[vm], %[rs], s93 offen offset:0 sc1 lds\n\t"
        "buffer_load_dwordx4 %[vm], %[rs], s93 offen offset:2048 sc1 lds\n\t"
        "s_mov_b32 m0, s94\n\t"
        :
        : [rs] "s"(rs), [tg] "s"(tg), [kr] "s"(kr), [vm] "v"(vm), [so] "s"(so), [lb] "s"(lb)
        : "s64", "s65", "s66", "s67", "s68", "s69", "s70", "s71", "s72", "s73", "s74", "s75", "s76", "s77", "s78", "s79", "s80", "s81", "s82", "s83", "s84", "s85", "s86", "s87", "s88", "s89", "s90", "s91", "s92", "s93", "s94", "vcc", "scc", "memory");
}

__device__ __forceinline__ void ts_look(u32& t1, u32& t2, u32& ok, u32x4 rs, u32 tsr, u32 want) {
    asm volatile(
        "s_nop 4\n\t"
        "s_buffer_load_dwordx4 s[64:67], %[rs], %[tsr] offset:0x0\n\t"
        "s_buffer_load_dwordx4 s[68:71], %[rs], %[tsr] offset:0x80\n\t"
        "s_buffer_load_dwordx4 s[72:75], %[rs], %[tsr] offset:0x100\n\t"
        "s_buffer_load_dwordx4 s[76:79], %[rs], %[tsr] offset:0x180\n\t"
        "s_waitcnt lgkmcnt(0)\n\t"
        "s_max_u32 %[t2], s66, s70\n\t"
        "s_max_u32 s64, s74, s78\n\t"
        "s_max_u32 %[t2], %[t2], s64\n\t"
        "s_xor_b32 s67, s67, %[want]\n\t"
        "s_xor_b32 s71, s71, %[want]\n\t"
        "s_xor_b32 s75, s75, %[want]\n\t"
        "s_xor_b32 s79, s79, %[want]\n\t"
        "s_sub_u32 s69, s69, s65\n\t"
        "s_sub_u32 s73, s73, s65\n\t"
        "s_sub_u32 s77, s77, s65\n\t"
        "s_max_i32 s69, s69, s73\n\t"
        "s_max_i32 s77, s77, 0\n\t"
        "s_max_i32 s69, s69, s77\n\t"
        "s_add_u32 s65, s65, s69\n\t"
        "s_or_b32 s66, s67, s71\n\t"
        "s_or_b32 s70, s75, s79\n\t"
        "s_or_b32 s70, s70, s66\n\t"
        "s_cmp_eq_u32 s70, 0\n\t"
        "s_cselect_b32 %[ok], 15, 0\n\t"
        "s_mov_b32 %[t1], s65\n\t"
        : [t1] "=&s"(t1), [t2] "=&s"(t2), [ok] "=&s"(ok)
        : [rs] "s"(rs), [tsr] "s"(tsr), [want] "s"(want)
        : "s64", "s65", "s66", "s67", "s68", "s69", "s70", "s71", "s72", "s73", "s74", "s75", "s76", "s77", "s78", "s79", "vcc", "scc", "memory");
}

__device__ __forceinline__ void pull(u32 vo, u32 tb, u32x4 rs, u32 n, u32 gap, u32 ld) {
    asm volatile(
        "s_mov_b32 s94, m0\n\t"
        "s_mov_b32 m0, %[ld]\n\t"
        "s_mov_b32 s92, 0\n\t"
        "Lp%=:\n\t"
        "v_readlane_b32 s93, %[tb], s92\n\t"
        "s_getpc_b64 s[90:91]\n\t"
        "Lgg_%=:\n\t"
        "s_sub_u32 s90, s90, %[gap]\n\t"
        "s_subb_u32 s91, s91, 0\n\t"
        "s_add_u32 s90, s90, Lzg_%=-Lgg_%=\n\t"
        "s_addc_u32 s91, s91, 0\n\t"
        "s_setpc_b64 s[90:91]\n\t"
        "s_nop 0\n\ts_nop 0\n\ts_nop 0\n\ts_nop 0\n\t"
        "s_nop 0\n\ts_nop 0\n\ts_nop 0\n\ts_nop 0\n\t"
        "s_nop 0\n\ts_nop 0\n\ts_nop 0\n\ts_nop 0\n\t"
        "s_nop 0\n\ts_nop 0\n\ts_nop 0\n\ts_nop 0\n\t"
        "s_nop 0\n\ts_nop 0\n\ts_nop 0\n\ts_nop 0\n\t"
        "s_nop 0\n\ts_nop 0\n\ts_nop 0\n\ts_nop 0\n\t"
        "s_nop 0\n\ts_nop 0\n\ts_nop 0\n\ts_nop 0\n\t"
        "s_nop 0\n\ts_nop 0\n\ts_nop 0\n\ts_nop 0\n\t"
        "s_nop 0\n\ts_nop 0\n\ts_nop 0\n\ts_nop 0\n\t"
        "s_nop 0\n\ts_nop 0\n\ts_nop 0\n\ts_nop 0\n\t"
        "s_nop 0\n\ts_nop 0\n\ts_nop 0\n\ts_nop 0\n\t"
        "s_nop 0\n\ts_nop 0\n\ts_nop 0\n\ts_nop 0\n\t"
        "s_nop 0\n\ts_nop 0\n\ts_nop 0\n\ts_nop 0\n\t"
        "s_nop 0\n\ts_nop 0\n\ts_nop 0\n\ts_nop 0\n\t"
        "s_nop 0\n\ts_nop 0\n\ts_nop 0\n\ts_nop 0\n\t"
        "s_nop 0\n\ts_nop 0\n\ts_nop 0\n\ts_nop 0\n\t"
        "Lzg_%=:\n\t"
        "buffer_load_dword %[vo], %[rs], s93 offen sc1 lds\n\t"
        "s_add_u32 s92, s92, 1\n\t"
        "s_cmp_lt_u32 s92, %[n]\n\t"
        "s_cbranch_scc1 Lp%=\n\t"
        "s_mov_b32 m0, s94\n\t"
        :
        : [vo] "v"(vo), [tb] "v"(tb), [rs] "s"(rs), [n] "s"(n), [gap] "s"(gap), [ld] "s"(ld)
        : "s88", "s89", "s90", "s91", "s92", "s93", "s94", "vcc", "scc", "memory");
}

__device__ __forceinline__ void calib(u32& c0, u32& r0) {
    asm volatile(
        "s_memrealtime s[64:65]\n\t"
        "s_nop 0\n\t"
        "s_memrealtime s[66:67]\n\t"
        "s_nop 0\n\t"
        "s_memrealtime s[68:69]\n\t"
        "s_nop 0\n\t"
        "s_memrealtime s[70:71]\n\t"
        "s_nop 0\n\t"
        "s_memrealtime s[72:73]\n\t"
        "s_nop 0\n\t"
        "s_memrealtime s[74:75]\n\t"
        "s_memtime s[90:91]\n\t"
        "s_memrealtime s[76:77]\n\t"
        "s_nop 0\n\t"
        "s_memrealtime s[78:79]\n\t"
        "s_nop 0\n\t"
        "s_memrealtime s[80:81]\n\t"
        "s_nop 0\n\t"
        "s_memrealtime s[82:83]\n\t"
        "s_nop 0\n\t"
        "s_memrealtime s[84:85]\n\t"
        "s_nop 0\n\t"
        "s_memrealtime s[86:87]\n\t"
        "s_nop 0\n\t"
        "s_nop 7\n\t"
        "s_waitcnt lgkmcnt(0)\n\t"
        "s_add_u32 s64, s64, s66\n\t"
        "s_add_u32 s64, s64, s68\n\t"
        "s_add_u32 s64, s64, s70\n\t"
        "s_add_u32 s64, s64, s72\n\t"
        "s_add_u32 s64, s64, s74\n\t"
        "s_add_u32 s64, s64, s76\n\t"
        "s_add_u32 s64, s64, s78\n\t"
        "s_add_u32 s64, s64, s80\n\t"
        "s_add_u32 s64, s64, s82\n\t"
        "s_add_u32 s64, s64, s84\n\t"
        "s_add_u32 s64, s64, s86\n\t"
        "s_lshl_b32 s64, s64, 1\n\t"
        "s_mov_b32 %[c0], s90\n\t"
        "s_mov_b32 %[r0], s64\n\t"
        : [c0] "=&s"(c0), [r0] "=&s"(r0)
        :
        : "s64", "s65", "s66", "s67", "s68", "s69", "s70", "s71", "s72", "s73", "s74", "s75", "s76", "s77", "s78", "s79", "s80", "s81", "s82", "s83", "s84", "s85", "s86", "s87", "s88", "s89", "s90", "s91", "vcc", "scc", "memory");
}

__device__ __forceinline__ void v_a0(const u32x32 (&W)[5], u32& rnd, u32 va, u32 sidx, u32 vw, u32 lb STP) {
    asm volatile(
        "Ltop%=:\n\t"
        "v_mov_b64 v[248:249], 0\n\t"
        "v_mov_b64 v[250:251], 0\n\t"
        "v_mov_b64 v[252:253], 0\n\t"
        "v_mov_b64 v[254:255], 0\n\t"
        "v_mov_b32 v200, 0xffbfffbf\n\t"
        "v_mov_b32 v206, %[lb]\n\t"
        "ds_write_b32 %[vw], v200\n\t"
        "ds_write_b32 %[vw], v200 offset:1024\n\t"
        "s_add_u32 %[rnd], %[rnd], 1\n\t"
        "s_setprio 3\n\t"
        "s_waitcnt lgkmcnt(0)\n\t"
        "s_barrier\n\t"
        "ds_read_b128 v[208:211], %[va] offset:0\n\t"
        "ds_read_b128 v[212:215], %[va] offset:1024\n\t"
        "ds_read_b128 v[216:219], %[va] offset:2048\n\t"
        "ds_read_b128 v[220:223], %[va] offset:3072\n\t"
        "ds_read_b128 v[224:227], %[va] offset:4096\n\t"
        "ds_read_b128 v[228:231], %[va] offset:5120\n\t"
        "s_waitcnt lgkmcnt(5)\n\t"
        "v_smfmac_f32_16x16x64_bf16 v[248:251], v[208:211], v[16:23], %[sidx]\n\t"
        "ds_read_b128 v[232:235], %[va] offset:6144\n\t"
        "v_smfmac_f32_16x16x64_bf16 v[252:255], v[208:211], v[96:103], %[sidx]\n\t"
        "s_waitcnt lgkmcnt(5)\n\t"
        "v_smfmac_f32_16x16x64_bf16 v[248:251], v[212:215], v[24:31], %[sidx]\n\t"
        "ds_read_b128 v[236:239], %[va] offset:7168\n\t"
        "v_smfmac_f32_16x16x64_bf16 v[252:255], v[212:215], v[104:111], %[sidx]\n\t"
        "s_waitcnt lgkmcnt(5)\n\t"
        "v_smfmac_f32_16x16x64_bf16 v[248:251], v[216:219], v[32:39], %[sidx]\n\t"
        "ds_read_b128 v[240:243], %[va] offset:8192\n\t"
        "s_waitcnt lgkmcnt(5)\n\t"
        "v_smfmac_f32_16x16x64_bf16 v[248:251], v[220:223], v[40:47], %[sidx]\n\t"
        "ds_read_b128 v[244:247], %[va] offset:10240\n\t"
        "s_waitcnt lgkmcnt(5)\n\t"
        "v_smfmac_f32_16x16x64_bf16 v[248:251], v[224:227], v[48:55], %[sidx]\n\t"
        "s_waitcnt lgkmcnt(4)\n\t"
        "v_smfmac_f32_16x16x64_bf16 v[248:251], v[228:231], v[56:63], %[sidx]\n\t"
        "s_waitcnt lgkmcnt(3)\n\t"
        "v_smfmac_f32_16x16x64_bf16 v[248:251], v[232:235], v[64:71], %[sidx]\n\t"
        "s_waitcnt lgkmcnt(2)\n\t"
        "v_smfmac_f32_16x16x64_bf16 v[248:251], v[236:239], v[72:79], %[sidx]\n\t"
        "s_waitcnt lgkmcnt(1)\n\t"
        "v_smfmac_f32_16x16x64_bf16 v[248:251], v[240:243], v[80:87], %[sidx]\n\t"
        "s_waitcnt lgkmcnt(0)\n\t"
        "v_smfmac_f32_16x16x64_bf16 v[248:251], v[244:247], v[88:95], %[sidx]\n\t"
        "v_smfmac_f32_16x16x64_bf16 v[252:255], v[216:219], v[112:119], %[sidx]\n\t"
        "v_smfmac_f32_16x16x64_bf16 v[252:255], v[220:223], v[120:127], %[sidx]\n\t"
        "v_add_f32 v248, v248, v250\n\t"
        "v_smfmac_f32_16x16x64_bf16 v[252:255], v[224:227], v[128:135], %[sidx]\n\t"
        "v_add_f32 v249, v249, v251\n\t"
        "v_smfmac_f32_16x16x64_bf16 v[252:255], v[228:231], v[136:143], %[sidx]\n\t"
        "v_cvt_pk_bf16_f32 v200, v248, v249\n\t"
        "v_smfmac_f32_16x16x64_bf16 v[252:255], v[232:235], v[144:151], %[sidx]\n\t"
        "ds_write_b32 %[vw], v200\n\t"
        "v_smfmac_f32_16x16x64_bf16 v[252:255], v[236:239], v[152:159], %[sidx]\n\t"
        "v_cmp_u_f32 vcc, v248, v249\n\t"
        "v_smfmac_f32_16x16x64_bf16 v[252:255], v[240:243], v[160:167], %[sidx]\n\t"
        "v_smfmac_f32_16x16x64_bf16 v[252:255], v[244:247], v[168:175], %[sidx]\n\t"
        "s_nop 7\n\t"
        "v_add_f32 v252, v252, v254\n\t"
        "v_add_f32 v253, v253, v255\n\t"
        "v_cvt_pk_bf16_f32 v201, v252, v253\n\t"
        "ds_write_b32 %[vw], v201 offset:1024\n\t"
        "s_setprio 0\n\t"
        "s_cbranch_vccnz Lnan%=\n\t"
        "Lok%=:\n\t"
        "s_waitcnt lgkmcnt(0)\n\t"
        "s_barrier\n\t"
        "ds_read_b32 v207, v206 offset:14336\n\t"
        "s_waitcnt lgkmcnt(0)\n\t"
        "v_cmp_eq_u32 vcc, %[rnd], v207\n\t"
        "s_barrier\n\t"
        "s_cbranch_vccnz Ltop%=\n\t"
        "s_branch Lend%=\n\t"
        "Lnan%=:\n\t"
        "v_max_u32 v207, v208, v209\n\t"
        "v_max3_u32 v207, v207, v210, v211\n\t"
        "v_max3_u32 v207, v207, v212, v213\n\t"
        "v_max3_u32 v207, v207, v214, v215\n\t"
        "v_max3_u32 v207, v207, v216, v217\n\t"
        "v_max3_u32 v207, v207, v218, v219\n\t"
        "v_max3_u32 v207, v207, v220, v221\n\t"
        "v_max3_u32 v207, v207, v222, v223\n\t"
        "v_max3_u32 v207, v207, v224, v225\n\t"
        "v_max3_u32 v207, v207, v226, v227\n\t"
        "v_max3_u32 v207, v207, v228, v229\n\t"
        "v_max3_u32 v207, v207, v230, v231\n\t"
        "v_max3_u32 v207, v207, v232, v233\n\t"
        "v_max3_u32 v207, v207, v234, v235\n\t"
        "v_max3_u32 v207, v207, v236, v237\n\t"
        "v_max3_u32 v207, v207, v238, v239\n\t"
        "v_max3_u32 v207, v207, v240, v241\n\t"
        "v_max3_u32 v207, v207, v242, v243\n\t"
        "v_max3_u32 v207, v207, v244, v245\n\t"
        "v_max3_u32 v207, v207, v246, v247\n\t"
        "v_cmp_eq_u32 vcc, 0xffbfffbf, v207\n\t"
        "v_mov_b32 v207, %[rnd]\n\t"
        "s_cbranch_vccz Lok%=\n\t"
        "ds_write_b32 v206, v207 offset:14336\n\t"
        "s_branch Lok%=\n\t"
        "Lend%=:\n\t"
        : [rnd] "+&s"(rnd)
        : [w0] "{v[16:47]}"(W[0]), [w1] "{v[48:79]}"(W[1]), [w2] "{v[80:111]}"(W[2]), [w3] "{v[112:143]}"(W[3]), [w4] "{v[144:175]}"(W[4]), [va] "v"(va), [sidx] "v"(sidx), [vw] "v"(vw), [lb] "s"(lb)
        : "v200", "v201", "v202", "v203", "v204", "v205", "v206", "v207", "v208", "v209", "v210", "v211", "v212", "v213", "v214", "v215", "v216", "v217", "v218", "v219", "v220", "v221", "v222", "v223", "v224", "v225", "v226", "v227", "v228", "v229", "v230", "v231", "v232", "v233", "v234", "v235", "v236", "v237", "v238", "v239", "v240", "v241", "v242", "v243", "v244", "v245", "v246", "v247", "v248", "v249", "v250", "v251", "v252", "v253", "v254", "v255", "vcc", "scc", "memory");
}

__device__ __forceinline__ void v_a1(const u32x32 (&W)[5], u32& rnd, u32 va, u32 sidx, u32 vw, u32 lb, u32& left, u32& s, u32& so, u32& tg, u32& kr, u32& c0, u32& r0, u32& ok, u32& cnt, u32 vm, u32 tc, u32 tv, u32 md, u32x4 rs, u32 poll, u32 last, u32 lag STP LGP) {
    asm volatile(
        "s_mov_b32 s94, m0\n\t"
        "Lst%=:\n\t"
        "v_readlane_b32 s60, %[tc], %[s]\n\t"
        "s_add_u32 s92, %[so], 0x8000\n\t"
        "s_add_u32 s93, %[so], 0x10000\n\t"
        "s_mov_b32 m0, %[md]\n\t"
        "s_add_u32 s62, %[s], 1\n\t"
        "s_mov_b32 s58, %[c0]\n\t"
        "s_mov_b32 s59, %[r0]\n\t"
        "s_mov_b32 s63, %[rnd]\n\t"
        "s_setprio 3\n\t"
        "s_add_u32 s61, s60, 0x10400\n\t"
        "s_mov_b32 s96, %[left]\n\t"
        "s_cmp_eq_u32 %[poll], 0\n\t"
        "s_cbranch_scc1 Ltop%=\n\t"
        "v_add_u32 v204, %[md], %[vm]\n\t"
        "v_mov_b32 v208, 0xffbfffbf\n\t"
        "v_mov_b32 v209, v208\n\t"
        "v_mov_b64 v[210:211], v[208:209]\n\t"
        "ds_write_b128 v204, v[208:211] offset:6144\n\t"
        "ds_write_b128 v204, v[208:211] offset:7168\n\t"
        "ds_write_b128 v204, v[208:211] offset:8192\n\t"
        "ds_write_b128 v204, v[208:211] offset:10240\n\t"
        "s_add_u32 %[tg], %[tg], %[lag]\n\t"
        "Lda_%=:\n\t"
        "s_memrealtime s[90:91]\n\t"
        "s_waitcnt lgkmcnt(0)\n\t"
        "s_mul_i32 s90, s90, 24\n\t"
        "s_sub_u32 s88, %[tg], s90\n\t"
        "s_sub_u32 s89, s88, 620\n\t"
        "s_cmp_gt_u32 s89, 0x4000\n\t"
        "s_cbranch_scc1 Lea_%=\n\t"
        "s_sleep 1\n\t"
        "s_branch Lda_%=\n\t"
        "Lea_%=:\n\t"
        "s_memrealtime s[64:65]\n\t"
        "s_nop 0\n\t"
        "s_memrealtime s[66:67]\n\t"
        "s_nop 0\n\t"
        "s_memrealtime s[68:69]\n\t"
        "s_nop 0\n\t"
        "s_memrealtime s[70:71]\n\t"
        "s_nop 0\n\t"
        "s_memrealtime s[72:73]\n\t"
        "s_nop 0\n\t"
        "s_memrealtime s[74:75]\n\t"
        "s_memtime s[90:91]\n\t"
        "s_memrealtime s[76:77]\n\t"
        "s_nop 0\n\t"
        "s_memrealtime s[78:79]\n\t"
        "s_nop 0\n\t"
        "s_memrealtime s[80:81]\n\t"
        "s_nop 0\n\t"
        "s_memrealtime s[82:83]\n\t"
        "s_nop 0\n\t"
        "s_memrealtime s[84:85]\n\t"
        "s_nop 0\n\t"
        "s_memrealtime s[86:87]\n\t"
        "s_nop 0\n\t"
        "s_nop 7\n\t"
        "s_waitcnt lgkmcnt(0)\n\t"
        "s_add_u32 s64, s64, s66\n\t"
        "s_add_u32 s64, s64, s68\n\t"
        "s_add_u32 s64, s64, s70\n\t"
        "s_add_u32 s64, s64, s72\n\t"
        "s_add_u32 s64, s64, s74\n\t"
        "s_add_u32 s64, s64, s76\n\t"
        "s_add_u32 s64, s64, s78\n\t"
        "s_add_u32 s64, s64, s80\n\t"
        "s_add_u32 s64, s64, s82\n\t"
        "s_add_u32 s64, s64, s84\n\t"
        "s_add_u32 s64, s64, s86\n\t"
        "s_lshl_b32 s64, s64, 1\n\t"
        "s_mov_b32 %[c0], s90\n\t"
        "s_mov_b32 %[r0], s64\n\t"
        "s_sub_u32 s88, %[tg], s64\n\t"
        "s_mul_i32 s88, s88, %[kr]\n\t"
        "s_ashr_i32 s88, s88, 12\n\t"
        "s_sub_u32 s88, s88, 244\n\t"
        "s_nop 1\n\t"
        "s_max_i32 s88, s88, 0\n\t"
        "s_min_i32 s88, s88, 384\n\t"
        "s_andn2_b32 s88, s88, 3\n\t"
        "s_getpc_b64 s[90:91]\n\t"
        "Lga_%=:\n\t"
        "s_sub_u32 s90, s90, s88\n\t"
        "s_subb_u32 s91, s91, 0\n\t"
        "s_add_u32 s90, s90, Lza_%=-Lga_%=\n\t"
        "s_addc_u32 s91, s91, 0\n\t"
        "s_setpc_b64 s[90:91]\n\t"
        "s_nop 0\n\ts_nop 0\n\ts_nop 0\n\ts_nop 0\n\t"
        "s_nop 0\n\ts_nop 0\n\ts_nop 0\n\ts_nop 0\n\t"
        "s_nop 0\n\ts_nop 0\n\ts_nop 0\n\ts_nop 0\n\t"
        "s_nop 0\n\ts_nop 0\n\ts_nop 0\n\ts_nop 0\n\t"
        "s_nop 0\n\ts_nop 0\n\ts_nop 0\n\ts_nop 0\n\t"
        "s_nop 0\n\ts_nop 0\n\ts_nop 0\n\ts_nop 0\n\t"
        "s_nop 0\n\ts_nop 0\n\ts_nop 0\n\ts_nop 0\n\t"
        "s_nop 0\n\ts_nop 0\n\ts_nop 0\n\ts_nop 0\n\t"
        "s_nop 0\n\ts_nop 0\n\ts_nop 0\n\ts_nop 0\n\t"
        "s_nop 0\n\ts_nop 0\n\ts_nop 0\n\ts_nop 0\n\t"
        "s_nop 0\n\ts_nop 0\n\ts_nop 0\n\ts_nop 0\n\t"
        "s_nop 0\n\ts_nop 0\n\ts_nop 0\n\ts_nop 0\n\t"
        "s_nop 0\n\ts_nop 0\n\ts_nop 0\n\ts_nop 0\n\t"
        "s_nop 0\n\ts_nop 0\n\ts_nop 0\n\ts_nop 0\n\t"
        "s_nop 0\n\ts_nop 0\n\ts_nop 0\n\ts_nop 0\n\t"
        "s_nop 0\n\ts_nop 0\n\ts_nop 0\n\ts_nop 0\n\t"
        "s_nop 0\n\ts_nop 0\n\ts_nop 0\n\ts_nop 0\n\t"
        "s_nop 0\n\ts_nop 0\n\ts_nop 0\n\ts_nop 0\n\t"
        "s_nop 0\n\ts_nop 0\n\ts_nop 0\n\ts_nop 0\n\t"
        "s_nop 0\n\ts_nop 0\n\ts_nop 0\n\ts_nop 0\n\t"
        "s_nop 0\n\ts_nop 0\n\ts_nop 0\n\ts_nop 0\n\t"
        "s_nop 0\n\ts_nop 0\n\ts_nop 0\n\ts_nop 0\n\t"
        "s_nop 0\n\ts_nop 0\n\ts_nop 0\n\ts_nop 0\n\t"
        "s_nop 0\n\ts_nop 0\n\ts_nop 0\n\ts_nop 0\n\t"
        "Lza_%=:\n\t"
        "buffer_load_dwordx4 %[vm], %[rs], %[so] offen offset:0 sc1 lds\n\t"
        "buffer_load_dwordx4 %[vm], %[rs], %[so] offen offset:1024 sc1 lds\n\t"
        "buffer_load_dwordx4 %[vm], %[rs], %[so] offen offset:2048 sc1 lds\n\t"
        "buffer_load_dwordx4 %[vm], %[rs], %[so] offen offset:3072 sc1 lds\n\t"
        "s_add_u32 m0, m0, 0x1000\n\t"
        "s_nop 0\n\t"
        "buffer_load_dwordx4 %[vm], %[rs], s92 offen offset:0 sc1 lds\n\t"
        "buffer_load_dwordx4 %[vm], %[rs], s92 offen offset:1024 sc1 lds\n\t"
        "buffer_load_dwordx4 %[vm], %[rs], s92 offen offset:2048 sc1 lds\n\t"
        "buffer_load_dwordx4 %[vm], %[rs], s92 offen offset:3072 sc1 lds\n\t"
        "s_add_u32 m0, m0, 0x1000\n\t"
        "s_nop 0\n\t"
        "buffer_load_dwordx4 %[vm], %[rs], s93 offen offset:0 sc1 lds\n\t"
        "buffer_load_dwordx4 %[vm], %[rs], s93 offen offset:2048 sc1 lds\n\t"
        "s_sub_u32 s59, %[r0], s59\n\t"
        "s_sub_u32 s58, %[c0], s58\n\t"
        "s_mul_i32 s88, s59, %[kr]\n\t"
        "s_lshr_b32 s88, s88, 12\n\t"
        "s_sub_u32 s58, s58, s88\n\t"
        "s_mul_i32 s58, s58, 3\n\t"
        "s_ashr_i32 s58, s58, 2\n\t"
        "s_sub_u32 s59, s59, 2048\n\t"
        "s_cmp_lt_u32 s59, 2560\n\t"
        "s_cselect_b32 s58, s58, 0\n\t"
        "s_add_u32 %[kr], %[kr], s58\n\t"
        "s_max_i32 %[kr], %[kr], 2560\n\t"
        "s_min_i32 %[kr], %[kr], 4352\n\t"
        "Ltop%=:\n\t"
        "v_mov_b64 v[248:249], 0\n\t"
        "v_mov_b64 v[250:251], 0\n\t"
        "v_mov_b64 v[252:253], 0\n\t"
        "v_mov_b64 v[254:255], 0\n\t"
        "v_mov_b32 v200, 0xffbfffbf\n\t"
        "v_mov_b32 v206, %[lb]\n\t"
        "ds_write_b32 %[vw], v200\n\t"
        "ds_write_b32 %[vw], v200 offset:1024\n\t"
        "s_add_u32 %[rnd], %[rnd], 1\n\t"
        "s_setprio 3\n\t"
        "s_mov_b32 s95, 0\n\t"
        "s_waitcnt vmcnt(4) lgkmcnt(0)\n\t"
        "s_barrier\n\t"
        "ds_read_b128 v[208:211], %[va] offset:0\n\t"
        "ds_read_b128 v[212:215], %[va] offset:1024\n\t"
        "ds_read_b128 v[216:219], %[va] offset:2048\n\t"
        "ds_read_b128 v[220:223], %[va] offset:3072\n\t"
        "ds_read_b128 v[224:227], %[va] offset:4096\n\t"
        "ds_read_b128 v[228:231], %[va] offset:5120\n\t"
        "s_waitcnt lgkmcnt(5)\n\t"
        "v_smfmac_f32_16x16x64_bf16 v[248:251], v[208:211], v[16:23], %[sidx]\n\t"
        "ds_read_b128 v[232:235], %[va] offset:6144\n\t"
        "v_smfmac_f32_16x16x64_bf16 v[252:255], v[208:211], v[96:103], %[sidx]\n\t"
        "s_waitcnt lgkmcnt(5)\n\t"
        "v_smfmac_f32_16x16x64_bf16 v[248:251], v[212:215], v[24:31], %[sidx]\n\t"
        "ds_read_b128 v[236:239], %[va] offset:7168\n\t"
        "v_smfmac_f32_16x16x64_bf16 v[252:255], v[212:215], v[104:111], %[sidx]\n\t"
        "s_waitcnt lgkmcnt(5)\n\t"
        "v_smfmac_f32_16x16x64_bf16 v[248:251], v[216:219], v[32:39], %[sidx]\n\t"
        "ds_read_b128 v[240:243], %[va] offset:8192\n\t"
        "s_waitcnt lgkmcnt(5)\n\t"
        "v_smfmac_f32_16x16x64_bf16 v[248:251], v[220:223], v[40:47], %[sidx]\n\t"
        "ds_read_b128 v[244:247], %[va] offset:10240\n\t"
        "s_waitcnt lgkmcnt(5)\n\t"
        "v_smfmac_f32_16x16x64_bf16 v[248:251], v[224:227], v[48:55], %[sidx]\n\t"
        "s_waitcnt lgkmcnt(4)\n\t"
        "v_smfmac_f32_16x16x64_bf16 v[248:251], v[228:231], v[56:63], %[sidx]\n\t"
        "s_waitcnt lgkmcnt(3)\n\t"
        "v_smfmac_f32_16x16x64_bf16 v[248:251], v[232:235], v[64:71], %[sidx]\n\t"
        "s_waitcnt lgkmcnt(2)\n\t"
        "v_smfmac_f32_16x16x64_bf16 v[248:251], v[236:239], v[72:79], %[sidx]\n\t"
        "s_waitcnt lgkmcnt(1)\n\t"
        "v_smfmac_f32_16x16x64_bf16 v[248:251], v[240:243], v[80:87], %[sidx]\n\t"
        "s_waitcnt lgkmcnt(0)\n\t"
        "v_smfmac_f32_16x16x64_bf16 v[248:251], v[244:247], v[88:95], %[sidx]\n\t"
        "v_smfmac_f32_16x16x64_bf16 v[252:255], v[216:219], v[112:119], %[sidx]\n\t"
        "v_smfmac_f32_16x16x64_bf16 v[252:255], v[220:223], v[120:127], %[sidx]\n\t"
        "v_add_f32 v248, v248, v250\n\t"
        "v_smfmac_f32_16x16x64_bf16 v[252:255], v[224:227], v[128:135], %[sidx]\n\t"
        "v_add_f32 v249, v249, v251\n\t"
        "v_smfmac_f32_16x16x64_bf16 v[252:255], v[228:231], v[136:143], %[sidx]\n\t"
        "v_cvt_pk_bf16_f32 v200, v248, v249\n\t"
        "v_smfmac_f32_16x16x64_bf16 v[252:255], v[232:235], v[144:151], %[sidx]\n\t"
        "ds_write_b32 %[vw], v200\n\t"
        "v_smfmac_f32_16x16x64_bf16 v[252:255], v[236:239], v[152:159], %[sidx]\n\t"
        "v_cmp_u_f32 vcc, v248, v249\n\t"
        "v_smfmac_f32_16x16x64_bf16 v[252:255], v[240:243], v[160:167], %[sidx]\n\t"
        "v_smfmac_f32_16x16x64_bf16 v[252:255], v[244:247], v[168:175], %[sidx]\n\t"
        "s_nop 7\n\t"
        "v_add_f32 v252, v252, v254\n\t"
        "v_add_f32 v253, v253, v255\n\t"
        "v_cvt_pk_bf16_f32 v201, v252, v253\n\t"
        "ds_write_b32 %[vw], v201 offset:1024\n\t"
        "s_setprio 0\n\t"
        "s_cbranch_vccnz Lnan%=\n\t"
        "Lok%=:\n\t"
        "s_cmp_lg_u32 s96, %[left]\n\t"
        "s_cbranch_scc1 Lv%=\n\t"
        "s_buffer_load_dwordx4 s[64:67], %[rs], s61 offset:0x0\n\t"
        "s_buffer_load_dwordx4 s[68:71], %[rs], s61 offset:0x80\n\t"
        "s_buffer_load_dwordx4 s[72:75], %[rs], s61 offset:0x100\n\t"
        "s_buffer_load_dwordx4 s[76:79], %[rs], s61 offset:0x180\n\t"
        "s_waitcnt lgkmcnt(0)\n\t"
        "s_barrier\n\t"
        "ds_read_b32 v207, v206 offset:14336\n\t"
        "s_waitcnt lgkmcnt(0)\n\t"
        "v_cmp_eq_u32 vcc, %[rnd], v207\n\t"
        "s_barrier\n\t"
        "s_cbranch_vccnz Lck%=\n\t"
        "s_xor_b32 s67, s67, s62\n\t"
        "s_xor_b32 s71, s71, s62\n\t"
        "s_xor_b32 s75, s75, s62\n\t"
        "s_xor_b32 s79, s79, s62\n\t"
        "s_sub_u32 s65, s65, %[r0]\n\t"
        "s_cmp_eq_u32 s67, 0\n\t"
        "s_cselect_b32 s65, s65, 0\n\t"
        "s_sub_u32 s69, s69, %[r0]\n\t"
        "s_cmp_eq_u32 s71, 0\n\t"
        "s_cselect_b32 s69, s69, 0\n\t"
        "s_sub_u32 s73, s73, %[r0]\n\t"
        "s_cmp_eq_u32 s75, 0\n\t"
        "s_cselect_b32 s73, s73, 0\n\t"
        "s_sub_u32 s77, s77, %[r0]\n\t"
        "s_cmp_eq_u32 s79, 0\n\t"
        "s_cselect_b32 s77, s77, 0\n\t"
        "s_max_u32 s65, s65, s69\n\t"
        "s_max_u32 s73, s73, s77\n\t"
        "s_max_u32 s65, s65, s73\n\t"
        "s_mov_b32 s64, s65\n\t"
        "s_sub_u32 s67, s67, 1\n\t"
        "s_cmp_lt_u32 s67, 0x20000\n\t"
        "s_cselect_b32 s64, 0, s64\n\t"
        "s_sub_u32 s71, s71, 1\n\t"
        "s_cmp_lt_u32 s71, 0x20000\n\t"
        "s_cselect_b32 s64, 0, s64\n\t"
        "s_sub_u32 s75, s75, 1\n\t"
        "s_cmp_lt_u32 s75, 0x20000\n\t"
        "s_cselect_b32 s64, 0, s64\n\t"
        "s_sub_u32 s79, s79, 1\n\t"
        "s_cmp_lt_u32 s79, 0x20000\n\t"
        "s_cselect_b32 s64, 0, s64\n\t"
        "s_add_u32 s65, s65, %[r0]\n\t"
        "s_cmp_lg_u32 s64, 0\n\t"
        "Lnx%=:\n\t"
        "s_cselect_b32 %[ok], 1, 0\n\t"
        "s_mov_b32 %[tg], s65\n\t"
        "s_mov_b32 %[so], s60\n\t"
        "s_sub_u32 s63, %[rnd], s63\n\t"
        "s_cmp_gt_u32 s63, 1\n\t"
        "s_addc_u32 %[cnt], %[cnt], 0\n\t"
        "s_mov_b32 %[s], s62\n\t"
        "s_waitcnt vmcnt(0)\n\t"
        "s_and_b32 s63, %[ok], %[poll]\n\t"
        "s_cmp_lt_i32 s62, %[last]\n\t"
        "s_cselect_b32 s63, s63, 0\n\t"
        "s_cmp_eq_u32 s63, 1\n\t"
        "s_cbranch_scc1 Lst%=\n\t"
        "s_branch Lend%=\n\t"
        "Lv%=:\n\t"
        "s_sleep 4\n\t"
        "buffer_load_dwordx4 v[196:199], %[tv], %[rs], s61 offen sc1\n\t"
        "s_waitcnt lgkmcnt(0)\n\t"
        "s_barrier\n\t"
        "ds_read_b32 v207, v206 offset:14336\n\t"
        "s_waitcnt lgkmcnt(0)\n\t"
        "v_cmp_eq_u32 vcc, %[rnd], v207\n\t"
        "s_barrier\n\t"
        "s_cbranch_vccnz Lck%=\n\t"
        "s_sub_u32 s97, %[r0], 0x100000\n\t"
        "s_waitcnt vmcnt(0)\n\t"
        "v_cmp_eq_u32 vcc, s62, v199\n\t"
        "v_subrev_u32 v196, s97, v196\n\t"
        "v_subrev_u32 v197, s97, v197\n\t"
        "v_cndmask_b32 v196, 0, v196, vcc\n\t"
        "v_cndmask_b32 v197, 0, v197, vcc\n\t"
        "v_cndmask_b32 v198, 0, v198, vcc\n\t"
        "v_max_u32_dpp v196, v196, v196 quad_perm:[1,0,3,2] row_mask:0xf bank_mask:0xf\n\t"
        "v_max_u32_dpp v197, v197, v197 quad_perm:[1,0,3,2] row_mask:0xf bank_mask:0xf\n\t"
        "v_max_u32_dpp v198, v198, v198 quad_perm:[1,0,3,2] row_mask:0xf bank_mask:0xf\n\t"
        "v_max_u32_dpp v196, v196, v196 quad_perm:[2,3,0,1] row_mask:0xf bank_mask:0xf\n\t"
        "v_max_u32_dpp v197, v197, v197 quad_perm:[2,3,0,1] row_mask:0xf bank_mask:0xf\n\t"
        "v_max_u32_dpp v198, v198, v198 quad_perm:[2,3,0,1] row_mask:0xf bank_mask:0xf\n\t"
        "v_max_u32_dpp v196, v196, v196 row_half_mirror row_mask:0xf bank_mask:0xf\n\t"
        "v_max_u32_dpp v197, v197, v197 row_half_mirror row_mask:0xf bank_mask:0xf\n\t"
        "v_max_u32_dpp v198, v198, v198 row_half_mirror row_mask:0xf bank_mask:0xf\n\t"
        "s_lshr_b32 s96, vcc_lo, 4\n\t"
        "v_readfirstlane_b32 s65, v197\n\t"
        "s_add_u32 s65, s65, s97\n\t"
        "s_or_b32 s96, s96, vcc_lo\n\t"
        "s_and_b32 s96, s96, 15\n\t"
        "s_cmp_eq_u32 s96, 15\n\t"
        "s_branch Lnx%=\n\t"
        "s_branch Lend%=\n\t"
        "Lnan%=:\n\t"
        "s_cmp_lg_u32 %[poll], 1\n\t"
        "s_cbranch_scc1 Lok%=\n\t"
        "s_mov_b32 m0, %[lb]\n\t"
        "v_add_u32 v204, %[lb], %[vm]\n\t"
        "buffer_load_dwordx4 %[vm], %[rs], %[so] offen offset:0 sc1 lds\n\t"
        "buffer_load_dwordx4 %[vm], %[rs], %[so] offen offset:1024 sc1 lds\n\t"
        "buffer_load_dwordx4 %[vm], %[rs], %[so] offen offset:2048 sc1 lds\n\t"
        "buffer_load_dwordx4 %[vm], %[rs], %[so] offen offset:3072 sc1 lds\n\t"
        "s_add_u32 m0, m0, 0x1000\n\t"
        "s_nop 0\n\t"
        "buffer_load_dwordx4 %[vm], %[rs], s92 offen offset:0 sc1 lds\n\t"
        "buffer_load_dwordx4 %[vm], %[rs], s92 offen offset:1024 sc1 lds\n\t"
        "buffer_load_dwordx4 %[vm], %[rs], s92 offen offset:2048 sc1 lds\n\t"
        "buffer_load_dwordx4 %[vm], %[rs], s92 offen offset:3072 sc1 lds\n\t"
        "s_add_u32 m0, m0, 0x1000\n\t"
        "s_nop 0\n\t"
        "buffer_load_dwordx4 %[vm], %[rs], s93 offen offset:0 sc1 lds\n\t"
        "buffer_load_dwordx4 %[vm], %[rs], s93 offen offset:2048 sc1 lds\n\t"
        "v_max_u32 v207, v208, v209\n\t"
        "v_max3_u32 v207, v207, v210, v211\n\t"
        "v_max3_u32 v207, v207, v212, v213\n\t"
        "v_max3_u32 v207, v207, v214, v215\n\t"
        "v_max3_u32 v207, v207, v216, v217\n\t"
        "v_max3_u32 v207, v207, v218, v219\n\t"
        "v_max3_u32 v207, v207, v220, v221\n\t"
        "v_max3_u32 v207, v207, v222, v223\n\t"
        "v_max3_u32 v207, v207, v224, v225\n\t"
        "v_max3_u32 v207, v207, v226, v227\n\t"
        "v_max3_u32 v207, v207, v228, v229\n\t"
        "v_max3_u32 v207, v207, v230, v231\n\t"
        "v_max3_u32 v207, v207, v232, v233\n\t"
        "v_max3_u32 v207, v207, v234, v235\n\t"
        "v_max3_u32 v207, v207, v236, v237\n\t"
        "v_max3_u32 v207, v207, v238, v239\n\t"
        "v_max3_u32 v207, v207, v240, v241\n\t"
        "v_max3_u32 v207, v207, v242, v243\n\t"
        "v_max3_u32 v207, v207, v244, v245\n\t"
        "v_max3_u32 v207, v207, v246, v247\n\t"
        "v_cmp_eq_u32 vcc, 0xffbfffbf, v207\n\t"
        "v_mov_b32 v207, %[rnd]\n\t"
        "s_cbranch_vccz Lok%=\n\t"
        "ds_write_b32 v206, v207 offset:14336\n\t"
        "s_mov_b32 s95, 1\n\t"
        "s_waitcnt lgkmcnt(0)\n\t"
        "s_branch Lcv%=\n\t"
        "Lck%=:\n\t"
        "v_add_u32 v204, %[lb], %[vm]\n\t"
        "Lcl%=:\n\t"
        "s_mov_b32 m0, %[lb]\n\t"
        "s_nop 0\n\t"
        "buffer_load_dwordx4 %[vm], %[rs], %[so] offen offset:0 sc1 lds\n\t"
        "buffer_load_dwordx4 %[vm], %[rs], %[so] offen offset:1024 sc1 lds\n\t"
        "buffer_load_dwordx4 %[vm], %[rs], %[so] offen offset:2048 sc1 lds\n\t"
        "buffer_load_dwordx4 %[vm], %[rs], %[so] offen offset:3072 sc1 lds\n\t"
        "s_add_u32 m0, m0, 0x1000\n\t"
        "s_nop 0\n\t"
        "buffer_load_dwordx4 %[vm], %[rs], s92 offen offset:0 sc1 lds\n\t"
        "buffer_load_dwordx4 %[vm], %[rs], s92 offen offset:1024 sc1 lds\n\t"
        "buffer_load_dwordx4 %[vm], %[rs], s92 offen offset:2048 sc1 lds\n\t"
        "buffer_load_dwordx4 %[vm], %[rs], s92 offen offset:3072 sc1 lds\n\t"
        "s_add_u32 m0, m0, 0x1000\n\t"
        "s_nop 0\n\t"
        "buffer_load_dwordx4 %[vm], %[rs], s93 offen offset:0 sc1 lds\n\t"
        "buffer_load_dwordx4 %[vm], %[rs], s93 offen offset:2048 sc1 lds\n\t"
        "Lcv%=:\n\t"
        "s_sub_u32 %[left], %[left], 1\n\t"
        "s_waitcnt vmcnt(9)\n\t"
        "ds_read_b128 v[208:211], v204 offset:0\n\t"
        "s_waitcnt vmcnt(8)\n\t"
        "ds_read_b128 v[212:215], v204 offset:1024\n\t"
        "s_waitcnt vmcnt(7)\n\t"
        "ds_read_b128 v[216:219], v204 offset:2048\n\t"
        "s_waitcnt vmcnt(6)\n\t"
        "ds_read_b128 v[220:223], v204 offset:3072\n\t"
        "s_waitcnt vmcnt(5)\n\t"
        "ds_read_b128 v[224:227], v204 offset:4096\n\t"
        "s_waitcnt vmcnt(4)\n\t"
        "ds_read_b128 v[228:231], v204 offset:5120\n\t"
        "s_waitcnt lgkmcnt(5)\n\t"
        "v_max3_u32 v207, v208, v208, v209\n\t"
        "v_max3_u32 v207, v207, v210, v211\n\t"
        "s_waitcnt vmcnt(3)\n\t"
        "ds_read_b128 v[232:235], v204 offset:6144\n\t"
        "s_waitcnt lgkmcnt(5)\n\t"
        "v_max3_u32 v207, v207, v212, v213\n\t"
        "v_max3_u32 v207, v207, v214, v215\n\t"
        "s_waitcnt vmcnt(2)\n\t"
        "ds_read_b128 v[236:239], v204 offset:7168\n\t"
        "s_waitcnt lgkmcnt(5)\n\t"
        "v_max3_u32 v207, v207, v216, v217\n\t"
        "v_max3_u32 v207, v207, v218, v219\n\t"
        "s_waitcnt vmcnt(1)\n\t"
        "ds_read_b128 v[240:243], v204 offset:8192\n\t"
        "s_waitcnt lgkmcnt(5)\n\t"
        "v_max3_u32 v207, v207, v220, v221\n\t"
        "v_max3_u32 v207, v207, v222, v223\n\t"
        "s_waitcnt vmcnt(0)\n\t"
        "ds_read_b128 v[244:247], v204 offset:10240\n\t"
        "s_waitcnt lgkmcnt(5)\n\t"
        "v_max3_u32 v207, v207, v224, v225\n\t"
        "v_max3_u32 v207, v207, v226, v227\n\t"
        "s_waitcnt lgkmcnt(4)\n\t"
        "v_max3_u32 v207, v207, v228, v229\n\t"
        "v_max3_u32 v207, v207, v230, v231\n\t"
        "s_waitcnt lgkmcnt(3)\n\t"
        "v_max3_u32 v207, v207, v232, v233\n\t"
        "v_max3_u32 v207, v207, v234, v235\n\t"
        "s_waitcnt lgkmcnt(2)\n\t"
        "v_max3_u32 v207, v207, v236, v237\n\t"
        "v_max3_u32 v207, v207, v238, v239\n\t"
        "s_waitcnt lgkmcnt(1)\n\t"
        "v_max3_u32 v207, v207, v240, v241\n\t"
        "v_max3_u32 v207, v207, v242, v243\n\t"
        "s_waitcnt lgkmcnt(0)\n\t"
        "v_max3_u32 v207, v207, v244, v245\n\t"
        "v_max3_u32 v207, v207, v246, v247\n\t"
        "v_cmp_eq_u32 vcc, 0xffbfffbf, v207\n\t"
        "s_cbranch_vccz Lcd%=\n\t"
        "s_cbranch_scc0 Lcl%=\n\t"
        "Lcd%=:\n\t"
        "s_max_i32 %[left], %[left], 0\n\t"
        "s_cmp_eq_u32 s95, 0\n\t"
        "s_cbranch_scc1 Lce%=\n\t"
        "s_barrier\n\t"
        "s_barrier\n\t"
        "Lce%=:\n\t"
        "s_cbranch_vccz Ltop%=\n\t"
        "s_mov_b32 %[ok], 2\n\t"
        "Lend%=:\n\t"
        "s_mov_b32 m0, s94\n\t"
        "s_waitcnt vmcnt(0)\n\t"
        : [rnd] "+&s"(rnd), [s] "+&s"(s), [so] "+&s"(so), [tg] "+&s"(tg), [kr] "+&s"(kr), [c0] "+&s"(c0), [r0] "+&s"(r0), [ok] "=&s"(ok), [cnt] "+&s"(cnt), [left] "+&s"(left)
        : [w0] "{v[16:47]}"(W[0]), [w1] "{v[48:79]}"(W[1]), [w2] "{v[80:111]}"(W[2]), [w3] "{v[112:143]}"(W[3]), [w4] "{v[144:175]}"(W[4]), [va] "v"(va), [sidx] "v"(sidx), [vw] "v"(vw), [vm] "v"(vm), [tc] "v"(tc), [tv] "v"(tv), [rs] "s"(rs), [lb] "s"(lb), [md] "s"(md), [poll] "s"(poll), [last] "s"(last), [lag] "s"(lag)
        : "s56", "s57", "s58", "s59", "s60", "s61", "s62", "s63", "s64", "s65", "s66", "s67", "s68", "s69", "s70", "s71", "s72", "s73", "s74", "s75", "s76", "s77", "s78", "s79", "s80", "s81", "s82", "s83", "s84", "s85", "s86", "s87", "s88", "s89", "s90", "s91", "s92", "s93", "s94", "s95", "s96", "s97", "s98", "s99", "v196", "v197", "v198", "v199", "v200", "v201", "v202", "v203", "v204", "v205", "v206", "v207", "v208", "v209", "v210", "v211", "v212", "v213", "v214", "v215", "v216", "v217", "v218", "v219", "v220", "v221", "v222", "v223", "v224", "v225", "v226", "v227", "v228", "v229", "v230", "v231", "v232", "v233", "v234", "v235", "v236", "v237", "v238", "v239", "v240", "v241", "v242", "v243", "v244", "v245", "v246", "v247", "v248", "v249", "v250", "v251", "v252", "v253", "v254", "v255", "vcc", "scc", "memory");
}

__device__ __forceinline__ void v_b(const u32x32 (&W)[5], u32& rnd, u32 sidx, u32 vr, u32 z0, u32 z1, u32 z2, u32x4 rs, u32 lb STP) {
    asm volatile(
        "Ltop%=:\n\t"
        "v_mov_b64 v[224:225], 0\n\t"
        "v_mov_b64 v[226:227], 0\n\t"
        "v_mov_b64 v[228:229], 0\n\t"
        "v_mov_b64 v[230:231], 0\n\t"
        "v_mov_b64 v[232:233], 0\n\t"
        "v_mov_b64 v[234:235], 0\n\t"
        "v_mov_b64 v[236:237], 0\n\t"
        "v_mov_b64 v[238:239], 0\n\t"
        "v_mov_b64 v[240:241], 0\n\t"
        "v_mov_b64 v[242:243], 0\n\t"
        "v_mov_b32 v206, %[lb]\n\t"
        "s_add_u32 %[rnd], %[rnd], 1\n\t"
        "s_mov_b32 s92, 0\n\t"
        "s_setprio 2\n\t"
        "s_barrier\n\t"
        "s_nop 15\n\t"
        "s_nop 15\n\t"
        "s_nop 15\n\t"
        "s_nop 15\n\t"
        "s_nop 15\n\t"
        "s_nop 7\n\t"
        "Lre%=:\n\t"
        "ds_read_b64_tr_b16 v[208:209], %[vr] offset:0\n\t"
        "ds_read_b64_tr_b16 v[210:211], %[vr] offset:64\n\t"
        "ds_read_b64_tr_b16 v[212:213], %[vr] offset:512\n\t"
        "ds_read_b64_tr_b16 v[214:215], %[vr] offset:576\n\t"
        "s_waitcnt lgkmcnt(0)\n\t"
        "v_smfmac_f32_16x16x64_bf16 v[224:227], v[208:211], v[16:23], %[sidx]\n\t"
        "v_smfmac_f32_16x16x64_bf16 v[228:231], v[208:211], v[48:55], %[sidx]\n\t"
        "v_smfmac_f32_16x16x64_bf16 v[232:235], v[208:211], v[80:87], %[sidx]\n\t"
        "v_smfmac_f32_16x16x64_bf16 v[236:239], v[208:211], v[112:119], %[sidx]\n\t"
        "v_smfmac_f32_16x16x64_bf16 v[240:243], v[208:211], v[144:151], %[sidx]\n\t"
        "ds_read_b64_tr_b16 v[216:217], %[vr] offset:1024\n\t"
        "v_smfmac_f32_16x16x64_bf16 v[224:227], v[212:215], v[24:31], %[sidx]\n\t"
        "ds_read_b64_tr_b16 v[218:219], %[vr] offset:1088\n\t"
        "v_smfmac_f32_16x16x64_bf16 v[228:231], v[212:215], v[56:63], %[sidx]\n\t"
        "ds_read_b64_tr_b16 v[220:221], %[vr] offset:1536\n\t"
        "v_smfmac_f32_16x16x64_bf16 v[232:235], v[212:215], v[88:95], %[sidx]\n\t"
        "ds_read_b64_tr_b16 v[222:223], %[vr] offset:1600\n\t"
        "v_smfmac_f32_16x16x64_bf16 v[236:239], v[212:215], v[120:127], %[sidx]\n\t"
        "v_smfmac_f32_16x16x64_bf16 v[240:243], v[212:215], v[152:159], %[sidx]\n\t"
        "s_waitcnt lgkmcnt(0)\n\t"
        "v_smfmac_f32_16x16x64_bf16 v[224:227], v[216:219], v[32:39], %[sidx]\n\t"
        "v_smfmac_f32_16x16x64_bf16 v[224:227], v[220:223], v[40:47], %[sidx]\n\t"
        "v_smfmac_f32_16x16x64_bf16 v[228:231], v[216:219], v[64:71], %[sidx]\n\t"
        "v_smfmac_f32_16x16x64_bf16 v[228:231], v[220:223], v[72:79], %[sidx]\n\t"
        "v_fma_f32 v205, v224, v225, v226\n\t"
        "v_smfmac_f32_16x16x64_bf16 v[232:235], v[216:219], v[96:103], %[sidx]\n\t"
        "v_cmp_u_f32 vcc, v205, v227\n\t"
        "v_smfmac_f32_16x16x64_bf16 v[232:235], v[220:223], v[104:111], %[sidx]\n\t"
        "v_cvt_pk_bf16_f32 v244, v224, v225\n\t"
        "v_smfmac_f32_16x16x64_bf16 v[236:239], v[216:219], v[128:135], %[sidx]\n\t"
        "v_cvt_pk_bf16_f32 v245, v226, v227\n\t"
        "v_smfmac_f32_16x16x64_bf16 v[236:239], v[220:223], v[136:143], %[sidx]\n\t"
        "v_cvt_pk_bf16_f32 v246, v228, v229\n\t"
        "v_smfmac_f32_16x16x64_bf16 v[240:243], v[216:219], v[160:167], %[sidx]\n\t"
        "v_cvt_pk_bf16_f32 v247, v230, v231\n\t"
        "v_smfmac_f32_16x16x64_bf16 v[240:243], v[220:223], v[168:175], %[sidx]\n\t"
        "s_cbranch_vccnz Lnan%=\n\t"
        "Lst%=:\n\t"
        "v_permlane16_swap_b32 v244, v246\n\t"
        "v_permlane16_swap_b32 v245, v247\n\t"
        "v_cvt_pk_bf16_f32 v248, v232, v233\n\t"
        "v_cvt_pk_bf16_f32 v249, v234, v235\n\t"
        "v_cvt_pk_bf16_f32 v250, v236, v237\n\t"
        "v_cvt_pk_bf16_f32 v251, v238, v239\n\t"
        "s_nop 0\n\t"
        "v_permlane16_swap_b32 v248, v250\n\t"
        "v_permlane16_swap_b32 v249, v251\n\t"
        "v_cvt_pk_bf16_f32 v252, v240, v241\n\t"
        "v_cvt_pk_bf16_f32 v254, v240, v241\n\t"
        "v_cvt_pk_bf16_f32 v253, v242, v243\n\t"
        "v_cvt_pk_bf16_f32 v255, v242, v243\n\t"
        "s_nop 0\n\t"
        "v_permlane16_swap_b32 v252, v254\n\t"
        "v_permlane16_swap_b32 v253, v255\n\t"
        "buffer_store_dwordx4 v[244:247], %[z0], %[rs], 0 offen sc1\n\t"
        "buffer_store_dwordx4 v[248:251], %[z1], %[rs], 0 offen sc1\n\t"
        "buffer_store_dwordx4 v[252:255], %[z2], %[rs], 0 offen sc1\n\t"
        "s_setprio 0\n\t"
        "s_cmp_eq_u32 s92, 2\n\t"
        "s_cbranch_scc1 Ly%=\n\t"
        "Lx%=:\n\t"
        "s_waitcnt lgkmcnt(0)\n\t"
        "s_barrier\n\t"
        "ds_read_b32 v207, v206 offset:14336\n\t"
        "s_waitcnt lgkmcnt(0)\n\t"
        "v_cmp_eq_u32 vcc, %[rnd], v207\n\t"
        "s_cmp_eq_u32 s92, 1\n\t"
        "s_cbranch_vccnz Lz%=\n\t"
        "s_cbranch_scc0 Ly%=\n\t"
        "s_mov_b32 s92, 2\n\t"
        "v_mov_b64 v[224:225], 0\n\t"
        "v_mov_b64 v[226:227], 0\n\t"
        "v_mov_b64 v[228:229], 0\n\t"
        "v_mov_b64 v[230:231], 0\n\t"
        "v_mov_b64 v[232:233], 0\n\t"
        "v_mov_b64 v[234:235], 0\n\t"
        "v_mov_b64 v[236:237], 0\n\t"
        "v_mov_b64 v[238:239], 0\n\t"
        "v_mov_b64 v[240:241], 0\n\t"
        "v_mov_b64 v[242:243], 0\n\t"
        "s_branch Lre%=\n\t"
        "Lnan%=:\n\t"
        "s_cmp_eq_u32 s92, 2\n\t"
        "s_cbranch_scc1 Lst%=\n\t"
        "s_mov_b32 s92, 1\n\t"
        "s_branch Lx%=\n\t"
        "Lz%=:\n\t"
        "s_barrier\n\t"
        "s_branch Ltop%=\n\t"
        "Ly%=:\n\t"
        "s_barrier\n\t"
        : [rnd] "+&s"(rnd)
        : [w0] "{v[16:47]}"(W[0]), [w1] "{v[48:79]}"(W[1]), [w2] "{v[80:111]}"(W[2]), [w3] "{v[112:143]}"(W[3]), [w4] "{v[144:175]}"(W[4]), [sidx] "v"(sidx), [vr] "v"(vr), [z0] "v"(z0), [z1] "v"(z1), [z2] "v"(z2), [rs] "s"(rs), [lb] "s"(lb)
        : "s92", "v205", "v206", "v207", "v208", "v209", "v210", "v211", "v212", "v213", "v214", "v215", "v216", "v217", "v218", "v219", "v220", "v221", "v222", "v223", "v224", "v225", "v226", "v227", "v228", "v229", "v230", "v231", "v232", "v233", "v234", "v235", "v236", "v237", "v238", "v239", "v240", "v241", "v242", "v243", "v244", "v245", "v246", "v247", "v248", "v249", "v250", "v251", "v252", "v253", "v254", "v255", "vcc", "scc", "memory");
}

__device__ __forceinline__ void l_g(u32& miss, u32& c0, u32& r0, u64& tl, u32 tg, u32 kr, u32 m0o, u32 so, u32x4 rs, u32x4 rec, u32 sr, u32 lim, u32 sh STP) {
    asm volatile(
        "s_mov_b32 %[miss], 1\n\t"
        "v_or_b32 v62, 0x10000, v59\n\t"
        "s_nop 1\n\t"
        "s_cmp_eq_u32 %[kr], 0\n\t"
        "s_cbranch_scc1 Lza_%=\n\t"
        "Lda_%=:\n\t"
        "s_memrealtime s[90:91]\n\t"
        "s_waitcnt lgkmcnt(0)\n\t"
        "s_mul_i32 s90, s90, 24\n\t"
        "s_sub_u32 s88, %[tg], s90\n\t"
        "s_sub_u32 s89, s88, 620\n\t"
        "s_cmp_gt_u32 s89, 0x4000\n\t"
        "s_cbranch_scc1 Lea_%=\n\t"
        "s_sleep 1\n\t"
        "s_branch Lda_%=\n\t"
        "Lea_%=:\n\t"
        "s_memrealtime s[64:65]\n\t"
        "s_nop 0\n\t"
        "s_memrealtime s[66:67]\n\t"
        "s_nop 0\n\t"
        "s_memrealtime s[68:69]\n\t"
        "s_nop 0\n\t"
        "s_memrealtime s[70:71]\n\t"
        "s_nop 0\n\t"
        "s_memrealtime s[72:73]\n\t"
        "s_nop 0\n\t"
        "s_memrealtime s[74:75]\n\t"
        "s_memtime s[90:91]\n\t"
        "s_memrealtime s[76:77]\n\t"
        "s_nop 0\n\t"
        "s_memrealtime s[78:79]\n\t"
        "s_nop 0\n\t"
        "s_memrealtime s[80:81]\n\t"
        "s_nop 0\n\t"
        "s_memrealtime s[82:83]\n\t"
        "s_nop 0\n\t"
        "s_memrealtime s[84:85]\n\t"
        "s_nop 0\n\t"
        "s_memrealtime s[86:87]\n\t"
        "s_nop 0\n\t"
        "s_nop 7\n\t"
        "s_waitcnt lgkmcnt(0)\n\t"
        "s_add_u32 s64, s64, s66\n\t"
        "s_add_u32 s64, s64, s68\n\t"
        "s_add_u32 s64, s64, s70\n\t"
        "s_add_u32 s64, s64, s72\n\t"
        "s_add_u32 s64, s64, s74\n\t"
        "s_add_u32 s64, s64, s76\n\t"
        "s_add_u32 s64, s64, s78\n\t"
        "s_add_u32 s64, s64, s80\n\t"
        "s_add_u32 s64, s64, s82\n\t"
        "s_add_u32 s64, s64, s84\n\t"
        "s_add_u32 s64, s64, s86\n\t"
        "s_lshl_b32 s64, s64, 1\n\t"
        "s_mov_b32 %[c0], s90\n\t"
        "s_mov_b32 %[r0], s64\n\t"
        "s_sub_u32 s88, %[tg], s64\n\t"
        "s_mul_i32 s88, s88, %[kr]\n\t"
        "s_ashr_i32 s88, s88, 12\n\t"
        "s_sub_u32 s88, s88, 244\n\t"
        "s_nop 1\n\t"
        "s_max_i32 s88, s88, 0\n\t"
        "s_min_i32 s88, s88, 384\n\t"
        "s_andn2_b32 s88, s88, 3\n\t"
        "s_getpc_b64 s[90:91]\n\t"
        "Lga_%=:\n\t"
        "s_sub_u32 s90, s90, s88\n\t"
        "s_subb_u32 s91, s91, 0\n\t"
        "s_add_u32 s90, s90, Lza_%=-Lga_%=\n\t"
        "s_addc_u32 s91, s91, 0\n\t"
        "s_setpc_b64 s[90:91]\n\t"
        "s_nop 0\n\ts_nop 0\n\ts_nop 0\n\ts_nop 0\n\t"
        "s_nop 0\n\ts_nop 0\n\ts_nop 0\n\ts_nop 0\n\t"
        "s_nop 0\n\ts_nop 0\n\ts_nop 0\n\ts_nop 0\n\t"
        "s_nop 0\n\ts_nop 0\n\ts_nop 0\n\ts_nop 0\n\t"
        "s_nop 0\n\ts_nop 0\n\ts_nop 0\n\ts_nop 0\n\t"
        "s_nop 0\n\ts_nop 0\n\ts_nop 0\n\ts_nop 0\n\t"
        "s_nop 0\n\ts_nop 0\n\ts_nop 0\n\ts_nop 0\n\t"
        "s_nop 0\n\ts_nop 0\n\ts_nop 0\n\ts_nop 0\n\t"
        "s_nop 0\n\ts_nop 0\n\ts_nop 0\n\ts_nop 0\n\t"
        "s_nop 0\n\ts_nop 0\n\ts_nop 0\n\ts_nop 0\n\t"
        "s_nop 0\n\ts_nop 0\n\ts_nop 0\n\ts_nop 0\n\t"
        "s_nop 0\n\ts_nop 0\n\ts_nop 0\n\ts_nop 0\n\t"
        "s_nop 0\n\ts_nop 0\n\ts_nop 0\n\ts_nop 0\n\t"
        "s_nop 0\n\ts_nop 0\n\ts_nop 0\n\ts_nop 0\n\t"
        "s_nop 0\n\ts_nop 0\n\ts_nop 0\n\ts_nop 0\n\t"
        "s_nop 0\n\ts_nop 0\n\ts_nop 0\n\ts_nop 0\n\t"
        "s_nop 0\n\ts_nop 0\n\ts_nop 0\n\ts_nop 0\n\t"
        "s_nop 0\n\ts_nop 0\n\ts_nop 0\n\ts_nop 0\n\t"
        "s_nop 0\n\ts_nop 0\n\ts_nop 0\n\ts_nop 0\n\t"
        "s_nop 0\n\ts_nop 0\n\ts_nop 0\n\ts_nop 0\n\t"
        "s_nop 0\n\ts_nop 0\n\ts_nop 0\n\ts_nop 0\n\t"
        "s_nop 0\n\ts_nop 0\n\ts_nop 0\n\ts_nop 0\n\t"
        "s_nop 0\n\ts_nop 0\n\ts_nop 0\n\ts_nop 0\n\t"
        "s_nop 0\n\ts_nop 0\n\ts_nop 0\n\ts_nop 0\n\t"
        "Lza_%=:\n\t"
        "buffer_load_dword v60, %[m0o], %[rs], %[so] offen sc1\n\t"
        "s_memtime s[92:93]\n\t"
        "s_memrealtime %[tl]\n\t"
        "v_add_u32 v64, %[sh], v56\n\t"
        "v_add_u32 v65, %[sh], v57\n\t"
        "v_mov_b64 v[66:67], v[58:59]\n\t"
        "v_or_b32 v61, 0x20000, v59\n\t"
        "s_waitcnt vmcnt(0)\n\t"
        "s_memtime s[94:95]\n\t"
        "v_cmp_eq_u32 vcc, 0xffbfffbf, v60\n\t"
        "s_cbranch_vccnz Lm%=\n\t"
        "s_waitcnt lgkmcnt(0)\n\t"
        "s_sub_u32 s92, s94, s92\n\t"
        "s_cmp_gt_u32 s92, %[lim]\n\t"
        "s_cbranch_scc1 Ls%=\n\t"
        "buffer_store_dwordx4 v[56:59], %[sr], %[rs], 0 offen sc1\n\t"
        "s_mov_b32 %[miss], 0\n\t"
        "s_branch Lend%=\n\t"
        "Ls%=:\n\t"
        "buffer_store_dwordx4 v[64:67], %[sr], %[rs], 0 offen offset:512 sc1\n\t"
        "buffer_store_dword v61, %[sr], %[rs], 0 offen offset:12 sc1\n\t"
        "s_mov_b32 %[miss], 0\n\t"
        "s_branch Lend%=\n\t"
        "Lm%=:\n\t"
        "buffer_store_dword v62, %[sr], %[rs], 0 offen offset:12 sc1\n\t"
        "Lend%=:\n\t"
        "s_waitcnt lgkmcnt(0)\n\t"
        : [c0] "+&s"(c0), [r0] "+&s"(r0), [tl] "=&s"(tl), [miss] "=&s"(miss)
        : [m0o] "v"(m0o), [sr] "v"(sr), [rs] "s"(rs), [so] "s"(so), [tg] "s"(tg), [kr] "s"(kr), [rec] "{v[56:59]}"(rec), [lim] "s"(lim), [sh] "s"(sh)
        : "s64", "s65", "s66", "s67", "s68", "s69", "s70", "s71", "s72", "s73", "s74", "s75", "s76", "s77", "s78", "s79", "s80", "s81", "s82", "s83", "s84", "s85", "s86", "s87", "s88", "s89", "s90", "s91", "s92", "s93", "s94", "s95", "v60", "v61", "v62", "v64", "v65", "v66", "v67", "vcc", "scc", "memory");
}

__device__ __forceinline__ void r_te(u32 lb, u32 fp) {
    asm volatile(
        "v_mov_b32 v20, %[lb]\n\t"
        "s_cmp_eq_u32 %[fp], 1\n\t"
        "s_cbranch_scc1 Lt%=\n\t"
        "s_barrier\n\t"
        "ds_read_b32 v21, v20 offset:16392\n\t"
        "s_waitcnt lgkmcnt(0)\n\t"
        "v_cmp_eq_u32 vcc, %[fp], v21\n\t"
        "s_cbranch_vccz Lt%=\n\t"
        "s_barrier\n\t"
        "ds_read_b32 v21, v20 offset:16396\n\t"
        "s_waitcnt lgkmcnt(0)\n\t"
        "v_cmp_eq_u32 vcc, %[fp], v21\n\t"
        "s_cbranch_vccz Lt%=\n\t"
        "s_barrier\n\t"
        "s_barrier\n\t"
        "Lt%=:\n\t"
        :
        : [lb] "s"(lb), [fp] "s"(fp)
        : "v20", "v21", "vcc", "scc", "memory");
}

__device__ __forceinline__ void r_t0(u32& c0, u32& r0, u32& t1, u32& t2, u32& ok, u32 tl, u32 t0, u32 kr, u32 fv, u32 fp, u32 lb, u32x4 rs, u32 so STP) {
    asm volatile(
        "s_setprio 3\n\t"
        "v_mov_b32 v20, %[lb]\n\t"
        "s_mov_b32 %[ok], 0\n\t"
        "s_or_b32 s92, %[fv], 0x10000\n\t"
        "s_mov_b32 s51, %[fv]\n\t"
        "s_mov_b32 s55, %[fv]\n\t"
        "s_mov_b32 s59, %[fv]\n\t"
        "s_mov_b32 s63, %[fv]\n\t"
        "s_cmp_eq_u32 %[fp], 1\n\t"
        "s_cbranch_scc1 Lt%=\n\t"
        "s_barrier\n\t"
        "ds_read_b32 v21, v20 offset:16392\n\t"
        "s_waitcnt lgkmcnt(0)\n\t"
        "v_cmp_eq_u32 vcc, %[fp], v21\n\t"
        "s_cbranch_vccz Lt%=\n\t"
        "s_barrier\n\t"
        "ds_read_b32 v21, v20 offset:16396\n\t"
        "s_waitcnt lgkmcnt(0)\n\t"
        "v_cmp_eq_u32 vcc, %[fp], v21\n\t"
        "s_cbranch_vccz Lt%=\n\t"
        "s_barrier\n\t"
        "s_barrier\n\t"
        "Lt%=:\n\t"
        "s_bfe_u32 s95, %[t2], 0x8000c\n\t"
        "s_mul_i32 s95, s95, %[kr]\n\t"
        "s_lshr_b32 s95, s95, 12\n\t"
        "s_min_u32 s95, s95, 512\n\t"
        "s_andn2_b32 s95, s95, 3\n\t"
        "v_mov_b32 v21, s95\n\t"
        "ds_write_b32 v20, v21 offset:16384\n\t"
        "Ldl_%=:\n\t"
        "s_memrealtime s[90:91]\n\t"
        "s_waitcnt lgkmcnt(0)\n\t"
        "s_mul_i32 s90, s90, 24\n\t"
        "s_sub_u32 s88, %[tl], s90\n\t"
        "s_sub_u32 s89, s88, 620\n\t"
        "s_cmp_gt_u32 s89, 0x4000\n\t"
        "s_cbranch_scc1 Lel_%=\n\t"
        "s_sleep 1\n\t"
        "s_branch Ldl_%=\n\t"
        "Lel_%=:\n\t"
        "s_memrealtime s[64:65]\n\t"
        "s_nop 0\n\t"
        "s_memrealtime s[66:67]\n\t"
        "s_nop 0\n\t"
        "s_memrealtime s[68:69]\n\t"
        "s_nop 0\n\t"
        "s_memrealtime s[70:71]\n\t"
        "s_nop 0\n\t"
        "s_memrealtime s[72:73]\n\t"
        "s_nop 0\n\t"
        "s_memrealtime s[74:75]\n\t"
        "s_memtime s[90:91]\n\t"
        "s_memrealtime s[76:77]\n\t"
        "s_nop 0\n\t"
        "s_memrealtime s[78:79]\n\t"
        "s_nop 0\n\t"
        "s_memrealtime s[80:81]\n\t"
        "s_nop 0\n\t"
        "s_memrealtime s[82:83]\n\t"
        "s_nop 0\n\t"
        "s_memrealtime s[84:85]\n\t"
        "s_nop 0\n\t"
        "s_memrealtime s[86:87]\n\t"
        "s_nop 0\n\t"
        "s_nop 7\n\t"
        "s_waitcnt lgkmcnt(0)\n\t"
        "s_add_u32 s64, s64, s66\n\t"
        "s_add_u32 s64, s64, s68\n\t"
        "s_add_u32 s64, s64, s70\n\t"
        "s_add_u32 s64, s64, s72\n\t"
        "s_add_u32 s64, s64, s74\n\t"
        "s_add_u32 s64, s64, s76\n\t"
        "s_add_u32 s64, s64, s78\n\t"
        "s_add_u32 s64, s64, s80\n\t"
        "s_add_u32 s64, s64, s82\n\t"
        "s_add_u32 s64, s64, s84\n\t"
        "s_add_u32 s64, s64, s86\n\t"
        "s_lshl_b32 s64, s64, 1\n\t"
        "s_mov_b32 %[c0], s90\n\t"
        "s_mov_b32 %[r0], s64\n\t"
        "s_sub_u32 s88, %[tl], s64\n\t"
        "s_mul_i32 s88, s88, %[kr]\n\t"
        "s_ashr_i32 s88, s88, 12\n\t"
        "s_sub_u32 s88, s88, 244\n\t"
        "s_min_i32 s89, s88, 0\n\t"
        "s_nop 0\n\t"
        "s_max_i32 s88, s88, 0\n\t"
        "s_min_i32 s88, s88, 384\n\t"
        "s_andn2_b32 s88, s88, 3\n\t"
        "s_getpc_b64 s[90:91]\n\t"
        "Lgl_%=:\n\t"
        "s_sub_u32 s90, s90, s88\n\t"
        "s_subb_u32 s91, s91, 0\n\t"
        "s_add_u32 s90, s90, Lzl_%=-Lgl_%=\n\t"
        "s_addc_u32 s91, s91, 0\n\t"
        "s_setpc_b64 s[90:91]\n\t"
        "s_nop 0\n\ts_nop 0\n\ts_nop 0\n\ts_nop 0\n\t"
        "s_nop 0\n\ts_nop 0\n\ts_nop 0\n\ts_nop 0\n\t"
        "s_nop 0\n\ts_nop 0\n\ts_nop 0\n\ts_nop 0\n\t"
        "s_nop 0\n\ts_nop 0\n\ts_nop 0\n\ts_nop 0\n\t"
        "s_nop 0\n\ts_nop 0\n\ts_nop 0\n\ts_nop 0\n\t"
        "s_nop 0\n\ts_nop 0\n\ts_nop 0\n\ts_nop 0\n\t"
        "s_nop 0\n\ts_nop 0\n\ts_nop 0\n\ts_nop 0\n\t"
        "s_nop 0\n\ts_nop 0\n\ts_nop 0\n\ts_nop 0\n\t"
        "s_nop 0\n\ts_nop 0\n\ts_nop 0\n\ts_nop 0\n\t"
        "s_nop 0\n\ts_nop 0\n\ts_nop 0\n\ts_nop 0\n\t"
        "s_nop 0\n\ts_nop 0\n\ts_nop 0\n\ts_nop 0\n\t"
        "s_nop 0\n\ts_nop 0\n\ts_nop 0\n\ts_nop 0\n\t"
        "s_nop 0\n\ts_nop 0\n\ts_nop 0\n\ts_nop 0\n\t"
        "s_nop 0\n\ts_nop 0\n\ts_nop 0\n\ts_nop 0\n\t"
        "s_nop 0\n\ts_nop 0\n\ts_nop 0\n\ts_nop 0\n\t"
        "s_nop 0\n\ts_nop 0\n\ts_nop 0\n\ts_nop 0\n\t"
        "s_nop 0\n\ts_nop 0\n\ts_nop 0\n\ts_nop 0\n\t"
        "s_nop 0\n\ts_nop 0\n\ts_nop 0\n\ts_nop 0\n\t"
        "s_nop 0\n\ts_nop 0\n\ts_nop 0\n\ts_nop 0\n\t"
        "s_nop 0\n\ts_nop 0\n\ts_nop 0\n\ts_nop 0\n\t"
        "s_nop 0\n\ts_nop 0\n\ts_nop 0\n\ts_nop 0\n\t"
        "s_nop 0\n\ts_nop 0\n\ts_nop 0\n\ts_nop 0\n\t"
        "s_nop 0\n\ts_nop 0\n\ts_nop 0\n\ts_nop 0\n\t"
        "s_nop 0\n\ts_nop 0\n\ts_nop 0\n\ts_nop 0\n\t"
        "Lzl_%=:\n\t"
        "s_buffer_load_dwordx4 s[48:51], %[rs], %[so] offset:0x0\n\t"
        "s_buffer_load_dwordx4 s[52:55], %[rs], %[so] offset:0x80\n\t"
        "s_buffer_load_dwordx4 s[56:59], %[rs], %[so] offset:0x100\n\t"
        "s_buffer_load_dwordx4 s[60:63], %[rs], %[so] offset:0x180\n\t"
        "s_sub_u32 s88, %[t0], %[tl]\n\t"
        "s_mul_i32 s88, s88, %[kr]\n\t"
        "s_ashr_i32 s88, s88, 12\n\t"
        "s_add_u32 s88, s88, s89\n\t"
        "s_sub_u32 s88, s88, 156\n\t"
        "s_max_i32 s88, s88, 0\n\t"
        "s_min_i32 s88, s88, 1280\n\t"
        "s_andn2_b32 s88, s88, 3\n\t"
        "s_cmp_lt_i32 s89, -64\n\t"
        "s_cbranch_scc1 Lw%=\n\t"
        "s_getpc_b64 s[90:91]\n\t"
        "Lga_%=:\n\t"
        "s_sub_u32 s90, s90, s88\n\t"
        "s_subb_u32 s91, s91, 0\n\t"
        "s_add_u32 s90, s90, Lza_%=-Lga_%=\n\t"
        "s_addc_u32 s91, s91, 0\n\t"
        "s_setpc_b64 s[90:91]\n\t"
        "s_nop 0\n\ts_nop 0\n\ts_nop 0\n\ts_nop 0\n\t"
        "s_nop 0\n\ts_nop 0\n\ts_nop 0\n\ts_nop 0\n\t"
        "s_nop 0\n\ts_nop 0\n\ts_nop 0\n\ts_nop 0\n\t"
        "s_nop 0\n\ts_nop 0\n\ts_nop 0\n\ts_nop 0\n\t"
        "s_nop 0\n\ts_nop 0\n\ts_nop 0\n\ts_nop 0\n\t"
        "s_nop 0\n\ts_nop 0\n\ts_nop 0\n\ts_nop 0\n\t"
        "s_nop 0\n\ts_nop 0\n\ts_nop 0\n\ts_nop 0\n\t"
        "s_nop 0\n\ts_nop 0\n\ts_nop 0\n\ts_nop 0\n\t"
        "s_nop 0\n\ts_nop 0\n\ts_nop 0\n\ts_nop 0\n\t"
        "s_nop 0\n\ts_nop 0\n\ts_nop 0\n\ts_nop 0\n\t"
        "s_nop 0\n\ts_nop 0\n\ts_nop 0\n\ts_nop 0\n\t"
        "s_nop 0\n\ts_nop 0\n\ts_nop 0\n\ts_nop 0\n\t"
        "s_nop 0\n\ts_nop 0\n\ts_nop 0\n\ts_nop 0\n\t"
        "s_nop 0\n\ts_nop 0\n\ts_nop 0\n\ts_nop 0\n\t"
        "s_nop 0\n\ts_nop 0\n\ts_nop 0\n\ts_nop 0\n\t"
        "s_nop 0\n\ts_nop 0\n\ts_nop 0\n\ts_nop 0\n\t"
        "s_nop 0\n\ts_nop 0\n\ts_nop 0\n\ts_nop 0\n\t"
        "s_nop 0\n\ts_nop 0\n\ts_nop 0\n\ts_nop 0\n\t"
        "s_nop 0\n\ts_nop 0\n\ts_nop 0\n\ts_nop 0\n\t"
        "s_nop 0\n\ts_nop 0\n\ts_nop 0\n\ts_nop 0\n\t"
        "s_nop 0\n\ts_nop 0\n\ts_nop 0\n\ts_nop 0\n\t"
        "s_nop 0\n\ts_nop 0\n\ts_nop 0\n\ts_nop 0\n\t"
        "s_nop 0\n\ts_nop 0\n\ts_nop 0\n\ts_nop 0\n\t"
        "s_nop 0\n\ts_nop 0\n\ts_nop 0\n\ts_nop 0\n\t"
        "s_nop 0\n\ts_nop 0\n\ts_nop 0\n\ts_nop 0\n\t"
        "s_nop 0\n\ts_nop 0\n\ts_nop 0\n\ts_nop 0\n\t"
        "s_nop 0\n\ts_nop 0\n\ts_nop 0\n\ts_nop 0\n\t"
        "s_nop 0\n\ts_nop 0\n\ts_nop 0\n\ts_nop 0\n\t"
        "s_nop 0\n\ts_nop 0\n\ts_nop 0\n\ts_nop 0\n\t"
        "s_nop 0\n\ts_nop 0\n\ts_nop 0\n\ts_nop 0\n\t"
        "s_nop 0\n\ts_nop 0\n\ts_nop 0\n\ts_nop 0\n\t"
        "s_nop 0\n\ts_nop 0\n\ts_nop 0\n\ts_nop 0\n\t"
        "s_nop 0\n\ts_nop 0\n\ts_nop 0\n\ts_nop 0\n\t"
        "s_nop 0\n\ts_nop 0\n\ts_nop 0\n\ts_nop 0\n\t"
        "s_nop 0\n\ts_nop 0\n\ts_nop 0\n\ts_nop 0\n\t"
        "s_nop 0\n\ts_nop 0\n\ts_nop 0\n\ts_nop 0\n\t"
        "s_nop 0\n\ts_nop 0\n\ts_nop 0\n\ts_nop 0\n\t"
        "s_nop 0\n\ts_nop 0\n\ts_nop 0\n\ts_nop 0\n\t"
        "s_nop 0\n\ts_nop 0\n\ts_nop 0\n\ts_nop 0\n\t"
        "s_nop 0\n\ts_nop 0\n\ts_nop 0\n\ts_nop 0\n\t"
        "s_nop 0\n\ts_nop 0\n\ts_nop 0\n\ts_nop 0\n\t"
        "s_nop 0\n\ts_nop 0\n\ts_nop 0\n\ts_nop 0\n\t"
        "s_nop 0\n\ts_nop 0\n\ts_nop 0\n\ts_nop 0\n\t"
        "s_nop 0\n\ts_nop 0\n\ts_nop 0\n\ts_nop 0\n\t"
        "s_nop 0\n\ts_nop 0\n\ts_nop 0\n\ts_nop 0\n\t"
        "s_nop 0\n\ts_nop 0\n\ts_nop 0\n\ts_nop 0\n\t"
        "s_nop 0\n\ts_nop 0\n\ts_nop 0\n\ts_nop 0\n\t"
        "s_nop 0\n\ts_nop 0\n\ts_nop 0\n\ts_nop 0\n\t"
        "s_nop 0\n\ts_nop 0\n\ts_nop 0\n\ts_nop 0\n\t"
        "s_nop 0\n\ts_nop 0\n\ts_nop 0\n\ts_nop 0\n\t"
        "s_nop 0\n\ts_nop 0\n\ts_nop 0\n\ts_nop 0\n\t"
        "s_nop 0\n\ts_nop 0\n\ts_nop 0\n\ts_nop 0\n\t"
        "s_nop 0\n\ts_nop 0\n\ts_nop 0\n\ts_nop 0\n\t"
        "s_nop 0\n\ts_nop 0\n\ts_nop 0\n\ts_nop 0\n\t"
        "s_nop 0\n\ts_nop 0\n\ts_nop 0\n\ts_nop 0\n\t"
        "s_nop 0\n\ts_nop 0\n\ts_nop 0\n\ts_nop 0\n\t"
        "s_nop 0\n\ts_nop 0\n\ts_nop 0\n\ts_nop 0\n\t"
        "s_nop 0\n\ts_nop 0\n\ts_nop 0\n\ts_nop 0\n\t"
        "s_nop 0\n\ts_nop 0\n\ts_nop 0\n\ts_nop 0\n\t"
        "s_nop 0\n\ts_nop 0\n\ts_nop 0\n\ts_nop 0\n\t"
        "s_nop 0\n\ts_nop 0\n\ts_nop 0\n\ts_nop 0\n\t"
        "s_nop 0\n\ts_nop 0\n\ts_nop 0\n\ts_nop 0\n\t"
        "s_nop 0\n\ts_nop 0\n\ts_nop 0\n\ts_nop 0\n\t"
        "s_nop 0\n\ts_nop 0\n\ts_nop 0\n\ts_nop 0\n\t"
        "s_nop 0\n\ts_nop 0\n\ts_nop 0\n\ts_nop 0\n\t"
        "s_nop 0\n\ts_nop 0\n\ts_nop 0\n\ts_nop 0\n\t"
        "s_nop 0\n\ts_nop 0\n\ts_nop 0\n\ts_nop 0\n\t"
        "s_nop 0\n\ts_nop 0\n\ts_nop 0\n\ts_nop 0\n\t"
        "s_nop 0\n\ts_nop 0\n\ts_nop 0\n\ts_nop 0\n\t"
        "s_nop 0\n\ts_nop 0\n\ts_nop 0\n\ts_nop 0\n\t"
        "s_nop 0\n\ts_nop 0\n\ts_nop 0\n\ts_nop 0\n\t"
        "s_nop 0\n\ts_nop 0\n\ts_nop 0\n\ts_nop 0\n\t"
        "s_nop 0\n\ts_nop 0\n\ts_nop 0\n\ts_nop 0\n\t"
        "s_nop 0\n\ts_nop 0\n\ts_nop 0\n\ts_nop 0\n\t"
        "s_nop 0\n\ts_nop 0\n\ts_nop 0\n\ts_nop 0\n\t"
        "s_nop 0\n\ts_nop 0\n\ts_nop 0\n\ts_nop 0\n\t"
        "s_nop 0\n\ts_nop 0\n\ts_nop 0\n\ts_nop 0\n\t"
        "s_nop 0\n\ts_nop 0\n\ts_nop 0\n\ts_nop 0\n\t"
        "s_nop 0\n\ts_nop 0\n\ts_nop 0\n\ts_nop 0\n\t"
        "s_nop 0\n\ts_nop 0\n\ts_nop 0\n\ts_nop 0\n\t"
        "Lza_%=:\n\t"
        "s_cmp_lg_u32 s51, %[fv]\n\t"
        "s_cbranch_scc1 Ln%=\n\t"
        "s_cmp_lg_u32 s55, %[fv]\n\t"
        "s_cbranch_scc1 Ln%=\n\t"
        "s_cmp_lg_u32 s59, %[fv]\n\t"
        "s_cbranch_scc1 Ln%=\n\t"
        "s_cmp_lg_u32 s63, %[fv]\n\t"
        "s_cbranch_scc1 Ln%=\n\t"
        "s_barrier\n\t"
        "s_setprio 0\n\t"
        "s_waitcnt lgkmcnt(0)\n\t"
        "s_xor_b32 s51, s51, %[fv]\n\t"
        "s_xor_b32 s55, s55, %[fv]\n\t"
        "s_xor_b32 s59, s59, %[fv]\n\t"
        "s_xor_b32 s63, s63, %[fv]\n\t"
        "s_sub_u32 s49, s49, %[tl]\n\t"
        "s_cmp_eq_u32 s51, 0\n\t"
        "s_cselect_b32 s49, s49, 0\n\t"
        "s_cselect_b32 s50, s50, 0\n\t"
        "s_sub_u32 s53, s53, %[tl]\n\t"
        "s_cmp_eq_u32 s55, 0\n\t"
        "s_cselect_b32 s53, s53, 0\n\t"
        "s_cselect_b32 s54, s54, 0\n\t"
        "s_sub_u32 s57, s57, %[tl]\n\t"
        "s_cmp_eq_u32 s59, 0\n\t"
        "s_cselect_b32 s57, s57, 0\n\t"
        "s_cselect_b32 s58, s58, 0\n\t"
        "s_sub_u32 s61, s61, %[tl]\n\t"
        "s_cmp_eq_u32 s63, 0\n\t"
        "s_cselect_b32 s61, s61, 0\n\t"
        "s_cselect_b32 s62, s62, 0\n\t"
        "s_max_u32 s49, s49, s53\n\t"
        "s_max_u32 s57, s57, s61\n\t"
        "s_max_u32 s49, s49, s57\n\t"
        "s_max_u32 s50, s50, s54\n\t"
        "s_max_u32 s58, s58, s62\n\t"
        "s_max_u32 s50, s50, s58\n\t"
        "s_mov_b32 s48, s49\n\t"
        "s_sub_u32 s51, s51, 1\n\t"
        "s_cmp_lt_u32 s51, 0x20000\n\t"
        "s_cselect_b32 s48, 0, s48\n\t"
        "s_sub_u32 s55, s55, 1\n\t"
        "s_cmp_lt_u32 s55, 0x20000\n\t"
        "s_cselect_b32 s48, 0, s48\n\t"
        "s_sub_u32 s59, s59, 1\n\t"
        "s_cmp_lt_u32 s59, 0x20000\n\t"
        "s_cselect_b32 s48, 0, s48\n\t"
        "s_sub_u32 s63, s63, 1\n\t"
        "s_cmp_lt_u32 s63, 0x20000\n\t"
        "s_cselect_b32 s48, 0, s48\n\t"
        "s_add_u32 s49, s49, %[tl]\n\t"
        "s_cmp_lg_u32 s48, 0\n\t"
        "s_cselect_b32 %[ok], 1, 2\n\t"
        "s_cselect_b32 %[t1], s49, %[t1]\n\t"
        "s_cselect_b32 %[t2], s50, %[t2]\n\t"
        "s_branch Lm%=\n\t"
        "Lw%=:\n\t"
        "s_waitcnt lgkmcnt(0)\n\t"
        "s_sub_u32 s95, %[t0], %[r0]\n\t"
        "s_mul_i32 s95, s95, %[kr]\n\t"
        "s_ashr_i32 s95, s95, 12\n\t"
        "s_add_u32 s95, s95, %[c0]\n\t"
        "s_sub_u32 s95, s95, 64\n\t"
        "Ldc_%=:\n\t"
        "s_memtime s[90:91]\n\t"
        "s_waitcnt lgkmcnt(0)\n\t"
        "s_sub_u32 s88, s95, s90\n\t"
        "s_sub_u32 s89, s88, 336\n\t"
        "s_cmp_gt_u32 s89, 0x4000\n\t"
        "s_cbranch_scc1 Lec_%=\n\t"
        "s_sleep 1\n\t"
        "s_branch Ldc_%=\n\t"
        "Lec_%=:\n\t"
        "s_sub_u32 s88, s88, 112\n\t"
        "s_max_i32 s88, s88, 0\n\t"
        "s_min_i32 s88, s88, 224\n\t"
        "s_andn2_b32 s88, s88, 3\n\t"
        "s_getpc_b64 s[90:91]\n\t"
        "Lgc_%=:\n\t"
        "s_sub_u32 s90, s90, s88\n\t"
        "s_subb_u32 s91, s91, 0\n\t"
        "s_add_u32 s90, s90, Lzc_%=-Lgc_%=\n\t"
        "s_addc_u32 s91, s91, 0\n\t"
        "s_setpc_b64 s[90:91]\n\t"
        "s_nop 0\n\ts_nop 0\n\ts_nop 0\n\ts_nop 0\n\t"
        "s_nop 0\n\ts_nop 0\n\ts_nop 0\n\ts_nop 0\n\t"
        "s_nop 0\n\ts_nop 0\n\ts_nop 0\n\ts_nop 0\n\t"
        "s_nop 0\n\ts_nop 0\n\ts_nop 0\n\ts_nop 0\n\t"
        "s_nop 0\n\ts_nop 0\n\ts_nop 0\n\ts_nop 0\n\t"
        "s_nop 0\n\ts_nop 0\n\ts_nop 0\n\ts_nop 0\n\t"
        "s_nop 0\n\ts_nop 0\n\ts_nop 0\n\ts_nop 0\n\t"
        "s_nop 0\n\ts_nop 0\n\ts_nop 0\n\ts_nop 0\n\t"
        "s_nop 0\n\ts_nop 0\n\ts_nop 0\n\ts_nop 0\n\t"
        "s_nop 0\n\ts_nop 0\n\ts_nop 0\n\ts_nop 0\n\t"
        "s_nop 0\n\ts_nop 0\n\ts_nop 0\n\ts_nop 0\n\t"
        "s_nop 0\n\ts_nop 0\n\ts_nop 0\n\ts_nop 0\n\t"
        "s_nop 0\n\ts_nop 0\n\ts_nop 0\n\ts_nop 0\n\t"
        "s_nop 0\n\ts_nop 0\n\ts_nop 0\n\ts_nop 0\n\t"
        "Lzc_%=:\n\t"
        "s_branch Lza_%=\n\t"
        "Ln%=:\n\t"
        "s_mov_b32 %[ok], 3\n\t"
        "s_waitcnt lgkmcnt(0)\n\t"
        "s_cmp_eq_u32 s51, s92\n\t"
        "s_cselect_b32 %[ok], 0, %[ok]\n\t"
        "s_cmp_eq_u32 s55, s92\n\t"
        "s_cselect_b32 %[ok], 0, %[ok]\n\t"
        "s_cmp_eq_u32 s59, s92\n\t"
        "s_cselect_b32 %[ok], 0, %[ok]\n\t"
        "s_cmp_eq_u32 s63, s92\n\t"
        "s_cselect_b32 %[ok], 0, %[ok]\n\t"
        "Lm%=:\n\t"
        "s_waitcnt lgkmcnt(0)\n\t"
        : [c0] "+&s"(c0), [r0] "+&s"(r0), [t1] "+&s"(t1), [t2] "+&s"(t2), [ok] "=&s"(ok)
        : [rs] "s"(rs), [so] "s"(so), [kr] "s"(kr), [fv] "s"(fv), [lb] "s"(lb), [tl] "s"(tl), [t0] "s"(t0), [fp] "s"(fp)
        : "s48", "s49", "s50", "s51", "s52", "s53", "s54", "s55", "s56", "s57", "s58", "s59", "s60", "s61", "s62", "s63", "s64", "s65", "s66", "s67", "s68", "s69", "s70", "s71", "s72", "s73", "s74", "s75", "s76", "s77", "s78", "s79", "s80", "s81", "s82", "s83", "s84", "s85", "s86", "s87", "s88", "s89", "s90", "s91", "s92", "s93", "s94", "s95", "v20", "v21", "vcc", "scc", "memory");
}

__device__ __forceinline__ void r_t1(u32& c0, u32& r0, u32& t1, u32& t2, u32 tv, u32 kr, u32 fv, u32 lb, u32x4 rs, u32 so STP) {
    asm volatile(
        "s_setprio 3\n\t"
        "s_mov_b32 s92, 0x4000\n\t"
        "v_mov_b32 v20, %[lb]\n\t"
        "s_nop 2\n\t"
        "Lr%=:\n\t"
        "buffer_load_dwordx4 v[16:19], %[tv], %[rs], %[so] offen sc1\n\t"
        "s_memrealtime s[64:65]\n\t"
        "s_nop 0\n\t"
        "s_memrealtime s[66:67]\n\t"
        "s_nop 0\n\t"
        "s_memrealtime s[68:69]\n\t"
        "s_nop 0\n\t"
        "s_memrealtime s[70:71]\n\t"
        "s_nop 0\n\t"
        "s_memrealtime s[72:73]\n\t"
        "s_nop 0\n\t"
        "s_memrealtime s[74:75]\n\t"
        "s_memtime s[90:91]\n\t"
        "s_memrealtime s[76:77]\n\t"
        "s_nop 0\n\t"
        "s_memrealtime s[78:79]\n\t"
        "s_nop 0\n\t"
        "s_memrealtime s[80:81]\n\t"
        "s_nop 0\n\t"
        "s_memrealtime s[82:83]\n\t"
        "s_nop 0\n\t"
        "s_memrealtime s[84:85]\n\t"
        "s_nop 0\n\t"
        "s_memrealtime s[86:87]\n\t"
        "s_nop 0\n\t"
        "s_nop 7\n\t"
        "s_waitcnt lgkmcnt(0)\n\t"
        "s_add_u32 s64, s64, s66\n\t"
        "s_add_u32 s64, s64, s68\n\t"
        "s_add_u32 s64, s64, s70\n\t"
        "s_add_u32 s64, s64, s72\n\t"
        "s_add_u32 s64, s64, s74\n\t"
        "s_add_u32 s64, s64, s76\n\t"
        "s_add_u32 s64, s64, s78\n\t"
        "s_add_u32 s64, s64, s80\n\t"
        "s_add_u32 s64, s64, s82\n\t"
        "s_add_u32 s64, s64, s84\n\t"
        "s_add_u32 s64, s64, s86\n\t"
        "s_lshl_b32 s64, s64, 1\n\t"
        "s_mov_b32 %[c0], s90\n\t"
        "s_mov_b32 %[r0], s64\n\t"
        "s_sub_u32 s96, %[r0], 0x100000\n\t"
        "s_waitcnt vmcnt(0)\n\t"
        "v_cmp_eq_u32 vcc, %[fv], v19\n\t"
        "v_subrev_u32 v16, s96, v16\n\t"
        "v_subrev_u32 v17, s96, v17\n\t"
        "v_cndmask_b32 v16, 0, v16, vcc\n\t"
        "v_cndmask_b32 v17, 0, v17, vcc\n\t"
        "v_cndmask_b32 v18, 0, v18, vcc\n\t"
        "v_max_u32_dpp v16, v16, v16 quad_perm:[1,0,3,2] row_mask:0xf bank_mask:0xf\n\t"
        "v_max_u32_dpp v17, v17, v17 quad_perm:[1,0,3,2] row_mask:0xf bank_mask:0xf\n\t"
        "v_max_u32_dpp v18, v18, v18 quad_perm:[1,0,3,2] row_mask:0xf bank_mask:0xf\n\t"
        "v_max_u32_dpp v16, v16, v16 quad_perm:[2,3,0,1] row_mask:0xf bank_mask:0xf\n\t"
        "v_max_u32_dpp v17, v17, v17 quad_perm:[2,3,0,1] row_mask:0xf bank_mask:0xf\n\t"
        "v_max_u32_dpp v18, v18, v18 quad_perm:[2,3,0,1] row_mask:0xf bank_mask:0xf\n\t"
        "v_max_u32_dpp v16, v16, v16 row_half_mirror row_mask:0xf bank_mask:0xf\n\t"
        "v_max_u32_dpp v17, v17, v17 row_half_mirror row_mask:0xf bank_mask:0xf\n\t"
        "v_max_u32_dpp v18, v18, v18 row_half_mirror row_mask:0xf bank_mask:0xf\n\t"
        "s_lshr_b32 s94, vcc_lo, 4\n\t"
        "v_readfirstlane_b32 s93, v16\n\t"
        "v_readfirstlane_b32 %[t1], v17\n\t"
        "v_readfirstlane_b32 %[t2], v18\n\t"
        "s_add_u32 s93, s93, s96\n\t"
        "s_add_u32 %[t1], %[t1], s96\n\t"
        "s_or_b32 s94, s94, vcc_lo\n\t"
        "s_and_b32 s94, s94, 15\n\t"
        "s_cmp_eq_u32 s94, 15\n\t"
        "s_cbranch_scc0 Lx%=\n\t"
        "Lgo%=:\n\t"
        "s_bfe_u32 s95, %[t2], 0x8000c\n\t"
        "s_mul_i32 s95, s95, %[kr]\n\t"
        "s_lshr_b32 s95, s95, 12\n\t"
        "s_min_u32 s95, s95, 512\n\t"
        "s_andn2_b32 s95, s95, 3\n\t"
        "v_mov_b32 v21, s95\n\t"
        "ds_write_b32 v20, v21 offset:16384\n\t"
        "s_sub_u32 s95, s93, %[r0]\n\t"
        "s_mul_i32 s95, s95, %[kr]\n\t"
        "s_ashr_i32 s95, s95, 12\n\t"
        "s_add_u32 s95, s95, %[c0]\n\t"
        "Lda_%=:\n\t"
        "s_memtime s[90:91]\n\t"
        "s_waitcnt lgkmcnt(0)\n\t"
        "s_sub_u32 s88, s95, s90\n\t"
        "s_sub_u32 s89, s88, 336\n\t"
        "s_cmp_gt_u32 s89, 0x4000\n\t"
        "s_cbranch_scc1 Lea_%=\n\t"
        "s_sleep 1\n\t"
        "s_branch Lda_%=\n\t"
        "Lea_%=:\n\t"
        "s_sub_u32 s88, s88, 112\n\t"
        "s_max_i32 s88, s88, 0\n\t"
        "s_min_i32 s88, s88, 224\n\t"
        "s_andn2_b32 s88, s88, 3\n\t"
        "s_getpc_b64 s[90:91]\n\t"
        "Lga_%=:\n\t"
        "s_sub_u32 s90, s90, s88\n\t"
        "s_subb_u32 s91, s91, 0\n\t"
        "s_add_u32 s90, s90, Lza_%=-Lga_%=\n\t"
        "s_addc_u32 s91, s91, 0\n\t"
        "s_setpc_b64 s[90:91]\n\t"
        "s_nop 0\n\ts_nop 0\n\ts_nop 0\n\ts_nop 0\n\t"
        "s_nop 0\n\ts_nop 0\n\ts_nop 0\n\ts_nop 0\n\t"
        "s_nop 0\n\ts_nop 0\n\ts_nop 0\n\ts_nop 0\n\t"
        "s_nop 0\n\ts_nop 0\n\ts_nop 0\n\ts_nop 0\n\t"
        "s_nop 0\n\ts_nop 0\n\ts_nop 0\n\ts_nop 0\n\t"
        "s_nop 0\n\ts_nop 0\n\ts_nop 0\n\ts_nop 0\n\t"
        "s_nop 0\n\ts_nop 0\n\ts_nop 0\n\ts_nop 0\n\t"
        "s_nop 0\n\ts_nop 0\n\ts_nop 0\n\ts_nop 0\n\t"
        "s_nop 0\n\ts_nop 0\n\ts_nop 0\n\ts_nop 0\n\t"
        "s_nop 0\n\ts_nop 0\n\ts_nop 0\n\ts_nop 0\n\t"
        "s_nop 0\n\ts_nop 0\n\ts_nop 0\n\ts_nop 0\n\t"
        "s_nop 0\n\ts_nop 0\n\ts_nop 0\n\ts_nop 0\n\t"
        "s_nop 0\n\ts_nop 0\n\ts_nop 0\n\ts_nop 0\n\t"
        "s_nop 0\n\ts_nop 0\n\ts_nop 0\n\ts_nop 0\n\t"
        "Lza_%=:\n\t"
        "s_barrier\n\t"
        "s_setprio 0\n\t"
        "s_branch Lend%=\n\t"
        "Lx%=:\n\t"
        "s_sub_u32 s92, s92, 1\n\t"
        "s_cbranch_scc1 Lgo%=\n\t"
        "s_branch Lr%=\n\t"
        "Lend%=:\n\t"
        : [c0] "+&s"(c0), [r0] "+&s"(r0), [t1] "+&s"(t1), [t2] "+&s"(t2)
        : [tv] "v"(tv), [rs] "s"(rs), [so] "s"(so), [kr] "s"(kr), [fv] "s"(fv), [lb] "s"(lb)
        : "s64", "s65", "s66", "s67", "s68", "s69", "s70", "s71", "s72", "s73", "s74", "s75", "s76", "s77", "s78", "s79", "s80", "s81", "s82", "s83", "s84", "s85", "s86", "s87", "s88", "s89", "s90", "s91", "s92", "s93", "s94", "s95", "s96", "v16", "v17", "v18", "v19", "v20", "v21", "vcc", "scc", "memory");
}

__device__ __forceinline__ void r_u(u32x32& bn, u32& s, u32& have, u32& left, u32& why, u32 sidx, u32 eo, u32 uw, u32 fa, u32 vm, u32 tp, u32 te, u32 tb, u32x4 rs, u32 lag, u32 lb STP LGP) {
    asm volatile(
        "v_mov_b32 v125, %[lb]\n\t"
        "v_mov_b32 v80, 0xffbfffbf\n\t"
        "v_mov_b32 v81, v80\n\t"
        "ds_write_b64 %[uw], v[80:81] offset:0\n\t"
        "ds_write_b64 %[uw], v[80:81] offset:512\n\t"
        "ds_write_b64 %[uw], v[80:81] offset:1024\n\t"
        "ds_write_b64 %[uw], v[80:81] offset:1536\n\t"
        "Lp%=:\n\t"
        "v_readlane_b32 s82, %[te], %[s]\n\t"
        "v_readlane_b32 s83, %[tb], %[s]\n\t"
        "s_add_u32 s92, %[s], 1\n\t"
        "s_mul_i32 s93, s92, 0x10001\n\t"
        "s_mov_b32 s95, %[left]\n\t"
        "s_mov_b32 s94, 0\n\t"
        "v_mov_b32 v124, s93\n\t"
        "v_readlane_b32 s85, %[tp], %[s]\n\t"
        "s_add_u32 s84, s83, 0x1000\n\t"
        "s_setprio 2\n\t"
        "s_waitcnt lgkmcnt(0)\n\t"
        "s_getpc_b64 s[90:91]\n\t"
        "Lj%=:\n\t"
        "s_sub_u32 s90, s90, %[lag]\n\t"
        "s_subb_u32 s91, s91, 0\n\t"
        "s_add_u32 s90, s90, Lg%=-Lj%=\n\t"
        "s_addc_u32 s91, s91, 0\n\t"
        "s_barrier\n\t"
        "s_setpc_b64 s[90:91]\n\t"
        "s_nop 10\n\t"
        "s_nop 10\n\t"
        "s_nop 10\n\t"
        "Lg%=:\n\t"
        "buffer_load_dwordx4 v[48:51], %[eo], %[rs], s82 offen offset:0 sc1\n\t"
        "buffer_load_dwordx4 v[52:55], %[eo], %[rs], s82 offen offset:1024 sc1\n\t"
        "buffer_load_dwordx4 v[56:59], %[eo], %[rs], s82 offen offset:2048 sc1\n\t"
        "buffer_load_dwordx4 v[60:63], %[eo], %[rs], s82 offen offset:3072 sc1\n\t"
        "v_mov_b64 v[64:65], 0\n\t"
        "v_mov_b64 v[66:67], 0\n\t"
        "v_mov_b64 v[68:69], 0\n\t"
        "v_mov_b64 v[70:71], 0\n\t"
        "v_mov_b64 v[72:73], 0\n\t"
        "v_mov_b64 v[74:75], 0\n\t"
        "v_mov_b64 v[76:77], 0\n\t"
        "v_mov_b64 v[78:79], 0\n\t"
        "s_waitcnt vmcnt(3)\n\t"
        "v_smfmac_f32_16x16x64_bf16 v[64:67], v[48:51], v[16:23], %[sidx]\n\t"
        "s_waitcnt vmcnt(2)\n\t"
        "v_smfmac_f32_16x16x64_bf16 v[68:71], v[52:55], v[24:31], %[sidx]\n\t"
        "s_waitcnt vmcnt(1)\n\t"
        "v_smfmac_f32_16x16x64_bf16 v[72:75], v[56:59], v[32:39], %[sidx]\n\t"
        "v_cvt_pk_bf16_f32 v80, v64, v65\n\t"
        "v_cvt_pk_bf16_f32 v81, v66, v67\n\t"
        "ds_write_b64 %[uw], v[80:81] offset:0\n\t"
        "s_waitcnt vmcnt(0)\n\t"
        "v_smfmac_f32_16x16x64_bf16 v[76:79], v[60:63], v[40:47], %[sidx]\n\t"
        "v_cvt_pk_bf16_f32 v82, v68, v69\n\t"
        "v_cvt_pk_bf16_f32 v83, v70, v71\n\t"
        "ds_write_b64 %[uw], v[82:83] offset:512\n\t"
        "v_cvt_pk_bf16_f32 v84, v72, v73\n\t"
        "v_cvt_pk_bf16_f32 v85, v74, v75\n\t"
        "ds_write_b64 %[uw], v[84:85] offset:1024\n\t"
        "s_nop 1\n\t"
        "v_cvt_pk_bf16_f32 v86, v76, v77\n\t"
        "v_cvt_pk_bf16_f32 v87, v78, v79\n\t"
        "ds_write_b64 %[uw], v[86:87] offset:1536\n\t"
        "s_setprio 0\n\t"
        "s_cmp_lg_u32 s94, 0\n\t"
        "s_cbranch_scc1 Lc%=\n\t"
        "s_waitcnt lgkmcnt(0)\n\t"
        "s_barrier\n\t"
        "ds_read_b32 v88, v125 offset:16392\n\t"
        "s_waitcnt lgkmcnt(0)\n\t"
        "v_cmp_eq_u32 vcc, s93, v88\n\t"
        "s_cbranch_vccnz Lc%=\n\t"
        "Lok%=:\n\t"
        "s_cmp_gt_u32 %[have], s92\n\t"
        "s_mov_b32 %[why], 0\n\t"
        "buffer_load_dword v122, off, %[rs], s85 sc1\n\t"
        "s_cbranch_scc0 Lh%=\n\t"
        "Li%=:\n\t"
        "buffer_load_dwordx4 v[90:93], %[vm], %[rs], s83 offen offset:0 sc1\n\t"
        "buffer_load_dwordx4 v[94:97], %[vm], %[rs], s83 offen offset:1024 sc1\n\t"
        "buffer_load_dwordx4 v[98:101], %[vm], %[rs], s83 offen offset:2048 sc1\n\t"
        "buffer_load_dwordx4 v[102:105], %[vm], %[rs], s83 offen offset:3072 sc1\n\t"
        "buffer_load_dwordx4 v[106:109], %[vm], %[rs], s84 offen offset:0 sc1\n\t"
        "buffer_load_dwordx4 v[110:113], %[vm], %[rs], s84 offen offset:1024 sc1\n\t"
        "buffer_load_dwordx4 v[114:117], %[vm], %[rs], s84 offen offset:2048 sc1\n\t"
        "buffer_load_dwordx4 v[118:121], %[vm], %[rs], s84 offen offset:3072 sc1\n\t"
        "Ln%=:\n\t"
        "ds_read_b32 v88, v125 offset:16396\n\t"
        "v_mov_b32 v80, 0xffbfffbf\n\t"
        "v_mov_b32 v81, v80\n\t"
        "s_sub_u32 s95, s95, %[left]\n\t"
        "s_mov_b32 %[s], s92\n\t"
        "s_waitcnt lgkmcnt(0)\n\t"
        "v_cmp_eq_u32 vcc, s93, v88\n\t"
        "s_cmp_eq_u32 %[s], 63\n\t"
        "s_cbranch_vccnz Lr%=\n\t"
        "s_cbranch_scc1 Lz%=\n\t"
        "s_cmp_lg_u32 %[why], 0\n\t"
        "s_cbranch_scc1 Lend%=\n\t"
        "ds_write_b64 %[uw], v[80:81] offset:0\n\t"
        "ds_write_b64 %[uw], v[80:81] offset:512\n\t"
        "ds_write_b64 %[uw], v[80:81] offset:1024\n\t"
        "ds_write_b64 %[uw], v[80:81] offset:1536\n\t"
        "s_waitcnt vmcnt(6)\n\t"
        "v_mov_b32 v16, v90\n\t"
        "v_mov_b32 v17, v94\n\t"
        "v_mov_b32 v18, v91\n\t"
        "v_mov_b32 v19, v95\n\t"
        "v_mov_b32 v20, v92\n\t"
        "v_mov_b32 v21, v96\n\t"
        "v_mov_b32 v22, v93\n\t"
        "v_mov_b32 v23, v97\n\t"
        "v_readfirstlane_b32 s96, v122\n\t"
        "s_waitcnt vmcnt(4)\n\t"
        "v_mov_b32 v24, v98\n\t"
        "v_mov_b32 v25, v102\n\t"
        "v_mov_b32 v26, v99\n\t"
        "v_mov_b32 v27, v103\n\t"
        "v_mov_b32 v28, v100\n\t"
        "v_mov_b32 v29, v104\n\t"
        "v_mov_b32 v30, v101\n\t"
        "v_mov_b32 v31, v105\n\t"
        "s_waitcnt vmcnt(2)\n\t"
        "v_mov_b32 v32, v106\n\t"
        "v_mov_b32 v33, v110\n\t"
        "v_mov_b32 v34, v107\n\t"
        "v_mov_b32 v35, v111\n\t"
        "v_mov_b32 v36, v108\n\t"
        "v_mov_b32 v37, v112\n\t"
        "v_mov_b32 v38, v109\n\t"
        "v_mov_b32 v39, v113\n\t"
        "s_max_i32 %[have], %[have], s96\n\t"
        "s_waitcnt vmcnt(0)\n\t"
        "v_mov_b32 v40, v114\n\t"
        "v_mov_b32 v41, v118\n\t"
        "v_mov_b32 v42, v115\n\t"
        "v_mov_b32 v43, v119\n\t"
        "v_mov_b32 v44, v116\n\t"
        "v_mov_b32 v45, v120\n\t"
        "v_mov_b32 v46, v117\n\t"
        "v_mov_b32 v47, v121\n\t"
        "s_branch Lp%=\n\t"
        "Lc%=:\n\t"
        "v_max_u32 v88, v48, v49\n\t"
        "v_max3_u32 v88, v88, v50, v51\n\t"
        "v_max3_u32 v88, v88, v52, v53\n\t"
        "v_max3_u32 v88, v88, v54, v55\n\t"
        "v_max3_u32 v88, v88, v56, v57\n\t"
        "v_max3_u32 v88, v88, v58, v59\n\t"
        "v_max3_u32 v88, v88, v60, v61\n\t"
        "v_max3_u32 v88, v88, v62, v63\n\t"
        "v_cmp_eq_u32 vcc, 0xffbfffbf, v88\n\t"
        "s_mov_b32 s94, 1\n\t"
        "s_cbranch_vccz Ld%=\n\t"
        "s_sub_u32 %[left], %[left], 1\n\t"
        "s_cbranch_scc0 Lg%=\n\t"
        "s_mov_b32 %[left], 0\n\t"
        "Ld%=:\n\t"
        "ds_write_b32 %[fa], v124\n\t"
        "s_waitcnt lgkmcnt(0)\n\t"
        "s_barrier\n\t"
        "s_branch Lok%=\n\t"
        "Lh%=:\n\t"
        "s_waitcnt vmcnt(0)\n\t"
        "v_readfirstlane_b32 s96, v122\n\t"
        "s_cmp_eq_u32 s92, 0\n\t"
        "s_cselect_b32 s96, 0, s96\n\t"
        "s_max_i32 %[have], %[have], s96\n\t"
        "s_cmp_gt_u32 %[have], s92\n\t"
        "s_cbranch_scc1 Li%=\n\t"
        "s_mov_b32 %[why], 2\n\t"
        "s_branch Ln%=\n\t"
        "Lr%=:\n\t"
        "s_mov_b32 %[why], 3\n\t"
        "s_branch Lend%=\n\t"
        "Lz%=:\n\t"
        "s_mov_b32 %[why], 0\n\t"
        "Lend%=:\n\t"
        "s_waitcnt vmcnt(0)\n\t"
        : [bn] "+{v[16:47]}"(bn), [s] "+&s"(s), [have] "+&s"(have), [left] "+&s"(left), [why] "=&s"(why)
        : [sidx] "v"(sidx), [eo] "v"(eo), [uw] "v"(uw), [fa] "v"(fa), [vm] "v"(vm), [tp] "v"(tp), [te] "v"(te), [tb] "v"(tb), [rs] "s"(rs), [lag] "s"(lag), [lb] "s"(lb)
        : "s82", "s83", "s84", "s85", "s86", "s87", "s88", "s89", "s90", "s91", "s92", "s93", "s94", "s95", "s96", "s97", "s98", "s99", "v48", "v49", "v50", "v51", "v52", "v53", "v54", "v55", "v56", "v57", "v58", "v59", "v60", "v61", "v62", "v63", "v64", "v65", "v66", "v67", "v68", "v69", "v70", "v71", "v72", "v73", "v74", "v75", "v76", "v77", "v78", "v79", "v80", "v81", "v82", "v83", "v84", "v85", "v86", "v87", "v88", "v89", "v90", "v91", "v92", "v93", "v94", "v95", "v96", "v97", "v98", "v99", "v100", "v101", "v102", "v103", "v104", "v105", "v106", "v107", "v108", "v109", "v110", "v111", "v112", "v113", "v114", "v115", "v116", "v117", "v118", "v119", "v120", "v121", "v122", "v123", "v124", "v125", "vcc", "scc", "memory");
}

__device__ __forceinline__ void r_f(u32& bad, u32& pass, u32 sidx, u32 pv, u32 zv, u32 ra, u32 rb, u32 mo, u32 uf, u32x4 rs, u32 so, u32 sz, u32 sm, u32 lb, u32 fv, u32 cap, u32 zp, u32 px, u32 zx STP) {
    asm volatile(
        "v_mov_b32 v120, %[lb]\n\t"
        "s_add_u32 s92, %[sm], 0x1000\n\t"
        "s_add_u32 s93, %[sm], 0x4000\n\t"
        "s_add_u32 s94, %[sm], 0x5000\n\t"
        "s_xor_b32 s80, %[px], 0x0\n\t"
        "s_add_u32 s80, s80, %[so]\n\t"
        "s_xor_b32 s84, %[zx], 0x0\n\t"
        "s_add_u32 s84, s84, %[sz]\n\t"
        "s_xor_b32 s81, %[px], 0x400\n\t"
        "s_add_u32 s81, s81, %[so]\n\t"
        "s_xor_b32 s85, %[zx], 0x800\n\t"
        "s_add_u32 s85, s85, %[sz]\n\t"
        "s_xor_b32 s82, %[px], 0x8000\n\t"
        "s_add_u32 s82, s82, %[so]\n\t"
        "s_xor_b32 s86, %[zx], 0x1000\n\t"
        "s_add_u32 s86, s86, %[sz]\n\t"
        "s_xor_b32 s83, %[px], 0x8400\n\t"
        "s_add_u32 s83, s83, %[so]\n\t"
        "s_xor_b32 s87, %[zx], 0x1800\n\t"
        "s_add_u32 s87, s87, %[sz]\n\t"
        "s_mov_b32 %[pass], 0\n\t"
        "s_mov_b32 s97, 0\n\t"
        "s_mov_b32 %[bad], 0\n\t"
        "s_setprio 3\n\t"
        "s_barrier\n\t"
        "ds_read_b32 v116, v120 offset:16384\n\t"
        "s_waitcnt lgkmcnt(0)\n\t"
        "v_readfirstlane_b32 s98, v116\n\t"
        "s_nop 3\n\t"
        "s_getpc_b64 s[90:91]\n\t"
        "Lgp_%=:\n\t"
        "s_sub_u32 s90, s90, s98\n\t"
        "s_subb_u32 s91, s91, 0\n\t"
        "s_add_u32 s90, s90, Lzp_%=-Lgp_%=\n\t"
        "s_addc_u32 s91, s91, 0\n\t"
        "s_setpc_b64 s[90:91]\n\t"
        "s_nop 0\n\ts_nop 0\n\ts_nop 0\n\ts_nop 0\n\t"
        "s_nop 0\n\ts_nop 0\n\ts_nop 0\n\ts_nop 0\n\t"
        "s_nop 0\n\ts_nop 0\n\ts_nop 0\n\ts_nop 0\n\t"
        "s_nop 0\n\ts_nop 0\n\ts_nop 0\n\ts_nop 0\n\t"
        "s_nop 0\n\ts_nop 0\n\ts_nop 0\n\ts_nop 0\n\t"
        "s_nop 0\n\ts_nop 0\n\ts_nop 0\n\ts_nop 0\n\t"
        "s_nop 0\n\ts_nop 0\n\ts_nop 0\n\ts_nop 0\n\t"
        "s_nop 0\n\ts_nop 0\n\ts_nop 0\n\ts_nop 0\n\t"
        "s_nop 0\n\ts_nop 0\n\ts_nop 0\n\ts_nop 0\n\t"
        "s_nop 0\n\ts_nop 0\n\ts_nop 0\n\ts_nop 0\n\t"
        "s_nop 0\n\ts_nop 0\n\ts_nop 0\n\ts_nop 0\n\t"
        "s_nop 0\n\ts_nop 0\n\ts_nop 0\n\ts_nop 0\n\t"
        "s_nop 0\n\ts_nop 0\n\ts_nop 0\n\ts_nop 0\n\t"
        "s_nop 0\n\ts_nop 0\n\ts_nop 0\n\ts_nop 0\n\t"
        "s_nop 0\n\ts_nop 0\n\ts_nop 0\n\ts_nop 0\n\t"
        "s_nop 0\n\ts_nop 0\n\ts_nop 0\n\ts_nop 0\n\t"
        "s_nop 0\n\ts_nop 0\n\ts_nop 0\n\ts_nop 0\n\t"
        "s_nop 0\n\ts_nop 0\n\ts_nop 0\n\ts_nop 0\n\t"
        "s_nop 0\n\ts_nop 0\n\ts_nop 0\n\ts_nop 0\n\t"
        "s_nop 0\n\ts_nop 0\n\ts_nop 0\n\ts_nop 0\n\t"
        "s_nop 0\n\ts_nop 0\n\ts_nop 0\n\ts_nop 0\n\t"
        "s_nop 0\n\ts_nop 0\n\ts_nop 0\n\ts_nop 0\n\t"
        "s_nop 0\n\ts_nop 0\n\ts_nop 0\n\ts_nop 0\n\t"
        "s_nop 0\n\ts_nop 0\n\ts_nop 0\n\ts_nop 0\n\t"
        "s_nop 0\n\ts_nop 0\n\ts_nop 0\n\ts_nop 0\n\t"
        "s_nop 0\n\ts_nop 0\n\ts_nop 0\n\ts_nop 0\n\t"
        "s_nop 0\n\ts_nop 0\n\ts_nop 0\n\ts_nop 0\n\t"
        "s_nop 0\n\ts_nop 0\n\ts_nop 0\n\ts_nop 0\n\t"
        "s_nop 0\n\ts_nop 0\n\ts_nop 0\n\ts_nop 0\n\t"
        "s_nop 0\n\ts_nop 0\n\ts_nop 0\n\ts_nop 0\n\t"
        "s_nop 0\n\ts_nop 0\n\ts_nop 0\n\ts_nop 0\n\t"
        "s_nop 0\n\ts_nop 0\n\ts_nop 0\n\ts_nop 0\n\t"
        "Lzp_%=:\n\t"
        "buffer_load_dwordx4 v[48:51], %[zv], %[rs], s84 offen offset:0 sc1\n\t"
        "buffer_load_dwordx4 v[52:55], %[zv], %[rs], s84 offen offset:1024 sc1\n\t"
        "buffer_load_dwordx4 v[56:59], %[zv], %[rs], s85 offen offset:0 sc1\n\t"
        "s_getpc_b64 s[90:91]\n\t"
        "Lgq_%=:\n\t"
        "s_sub_u32 s90, s90, %[zp]\n\t"
        "s_subb_u32 s91, s91, 0\n\t"
        "s_add_u32 s90, s90, Lzq_%=-Lgq_%=\n\t"
        "s_addc_u32 s91, s91, 0\n\t"
        "s_setpc_b64 s[90:91]\n\t"
        "s_nop 0\n\ts_nop 0\n\ts_nop 0\n\ts_nop 0\n\t"
        "s_nop 0\n\ts_nop 0\n\ts_nop 0\n\ts_nop 0\n\t"
        "s_nop 0\n\ts_nop 0\n\ts_nop 0\n\ts_nop 0\n\t"
        "s_nop 0\n\ts_nop 0\n\ts_nop 0\n\ts_nop 0\n\t"
        "s_nop 0\n\ts_nop 0\n\ts_nop 0\n\ts_nop 0\n\t"
        "s_nop 0\n\ts_nop 0\n\ts_nop 0\n\ts_nop 0\n\t"
        "s_nop 0\n\ts_nop 0\n\ts_nop 0\n\ts_nop 0\n\t"
        "s_nop 0\n\ts_nop 0\n\ts_nop 0\n\ts_nop 0\n\t"
        "s_nop 0\n\ts_nop 0\n\ts_nop 0\n\ts_nop 0\n\t"
        "s_nop 0\n\ts_nop 0\n\ts_nop 0\n\ts_nop 0\n\t"
        "s_nop 0\n\ts_nop 0\n\ts_nop 0\n\ts_nop 0\n\t"
        "s_nop 0\n\ts_nop 0\n\ts_nop 0\n\ts_nop 0\n\t"
        "s_nop 0\n\ts_nop 0\n\ts_nop 0\n\ts_nop 0\n\t"
        "s_nop 0\n\ts_nop 0\n\ts_nop 0\n\ts_nop 0\n\t"
        "s_nop 0\n\ts_nop 0\n\ts_nop 0\n\ts_nop 0\n\t"
        "s_nop 0\n\ts_nop 0\n\ts_nop 0\n\ts_nop 0\n\t"
        "Lzq_%=:\n\t"
        "buffer_load_dwordx4 v[14:17], %[pv], %[rs], s80 offen offset:0 sc1\n\t"
        "buffer_load_dwordx4 v[60:63], %[zv], %[rs], s85 offen offset:1024 sc1\n\t"
        "buffer_load_dwordx4 v[18:21], %[pv], %[rs], s80 offen offset:256 sc1\n\t"
        "buffer_load_dwordx4 v[64:67], %[zv], %[rs], s86 offen offset:0 sc1\n\t"
        "buffer_load_dwordx4 v[22:25], %[pv], %[rs], s81 offen offset:0 sc1\n\t"
        "buffer_load_dwordx4 v[68:71], %[zv], %[rs], s86 offen offset:1024 sc1\n\t"
        "buffer_load_dwordx4 v[26:29], %[pv], %[rs], s81 offen offset:256 sc1\n\t"
        "buffer_load_dwordx4 v[72:75], %[zv], %[rs], s87 offen offset:0 sc1\n\t"
        "buffer_load_dwordx4 v[30:33], %[pv], %[rs], s82 offen offset:0 sc1\n\t"
        "buffer_load_dwordx4 v[76:79], %[zv], %[rs], s87 offen offset:1024 sc1\n\t"
        "buffer_load_dwordx4 v[34:37], %[pv], %[rs], s82 offen offset:256 sc1\n\t"
        "buffer_load_dwordx4 v[38:41], %[pv], %[rs], s83 offen offset:0 sc1\n\t"
        "buffer_load_dwordx4 v[42:45], %[pv], %[rs], s83 offen offset:256 sc1\n\t"
        "v_mov_b64 v[112:113], 0\n\t"
        "v_mov_b64 v[114:115], 0\n\t"
        "s_cmp_lg_u32 s97, 0\n\t"
        "s_cbranch_scc1 Lwu%=\n\t"
        "Lrd%=:\n\t"
        "s_waitcnt vmcnt(13)\n\t"
        "ds_read_b128 v[80:83], %[ra] offset:0\n\t"
        "ds_read_b128 v[84:87], %[ra] offset:1024\n\t"
        "s_waitcnt vmcnt(12)\n\t"
        "v_mov_b64 v[46:47], v[14:15]\n\t"
        "s_waitcnt vmcnt(10)\n\t"
        "v_smfmac_f32_16x16x64_bf16 v[112:115], v[16:19], v[48:55], %[sidx]\n\t"
        "s_waitcnt vmcnt(9)\n\t"
        "ds_read_b128 v[88:91], %[ra] offset:2048\n\t"
        "ds_read_b128 v[92:95], %[ra] offset:3072\n\t"
        "s_waitcnt vmcnt(8) lgkmcnt(2)\n\t"
        "v_smfmac_f32_16x16x64_bf16 v[112:115], v[20:23], v[80:87], %[sidx]\n\t"
        "s_waitcnt vmcnt(6)\n\t"
        "v_smfmac_f32_16x16x64_bf16 v[112:115], v[24:27], v[56:63], %[sidx]\n\t"
        "s_waitcnt vmcnt(5)\n\t"
        "ds_read_b128 v[96:99], %[ra] offset:4096\n\t"
        "ds_read_b128 v[100:103], %[ra] offset:5120\n\t"
        "ds_read_b128 v[104:107], %[rb] offset:6144\n\t"
        "ds_read_b128 v[108:111], %[rb] offset:7168\n\t"
        "s_waitcnt vmcnt(4) lgkmcnt(4)\n\t"
        "v_smfmac_f32_16x16x64_bf16 v[112:115], v[28:31], v[88:95], %[sidx]\n\t"
        "s_waitcnt vmcnt(2)\n\t"
        "v_smfmac_f32_16x16x64_bf16 v[112:115], v[32:35], v[64:71], %[sidx]\n\t"
        "s_waitcnt vmcnt(1) lgkmcnt(2)\n\t"
        "v_smfmac_f32_16x16x64_bf16 v[112:115], v[36:39], v[96:103], %[sidx]\n\t"
        "s_waitcnt vmcnt(0)\n\t"
        "v_smfmac_f32_16x16x64_bf16 v[112:115], v[40:43], v[72:79], %[sidx]\n\t"
        "s_waitcnt vmcnt(0) lgkmcnt(0)\n\t"
        "v_smfmac_f32_16x16x64_bf16 v[112:115], v[44:47], v[104:111], %[sidx]\n\t"
        "s_nop 7\n\t"
        "v_add_f32 v116, v112, v114\n\t"
        "v_add_f32 v117, v113, v115\n\t"
        "v_add_f32 v118, v113, v115\n\t"
        "v_mov_b32_dpp v118, v116 row_ror:8 row_mask:0xf bank_mask:0x3\n\t"
        "v_mov_b32_dpp v116, v117 row_ror:8 row_mask:0xf bank_mask:0xc\n\t"
        "v_cvt_pk_bf16_f32 v119, v116, v118\n\t"
        "v_cmpx_o_f32 vcc, v116, v118\n\t"
        "buffer_store_dword v119, %[mo], %[rs], %[sm] offen sc1\n\t"
        "buffer_store_dword v119, %[mo], %[rs], s92 offen sc1\n\t"
        "buffer_store_dword v119, %[mo], %[rs], s93 offen sc1\n\t"
        "buffer_store_dword v119, %[mo], %[rs], s94 offen sc1\n\t"
        "s_nop 2\n\t"
        "s_cmp_eq_u64 exec, -1\n\t"
        "s_mov_b64 exec, -1\n\t"
        "s_cbranch_scc0 Lag%=\n\t"
        "s_barrier\n\t"
        "s_branch Lend%=\n\t"
        "Lag%=:\n\t"
        "s_add_u32 %[pass], %[pass], 1\n\t"
        "s_cmp_eq_u32 s97, 0\n\t"
        "s_cbranch_scc1 Lfi%=\n\t"
        "v_max_u32 v116, v16, v17\n\t"
        "v_max3_u32 v116, v116, v18, v19\n\t"
        "v_max3_u32 v116, v116, v20, v21\n\t"
        "v_max3_u32 v116, v116, v22, v23\n\t"
        "v_max3_u32 v116, v116, v24, v25\n\t"
        "v_max3_u32 v116, v116, v26, v27\n\t"
        "v_max3_u32 v116, v116, v28, v29\n\t"
        "v_max3_u32 v116, v116, v30, v31\n\t"
        "v_max3_u32 v116, v116, v32, v33\n\t"
        "v_max3_u32 v116, v116, v34, v35\n\t"
        "v_max3_u32 v116, v116, v36, v37\n\t"
        "v_max3_u32 v116, v116, v38, v39\n\t"
        "v_max3_u32 v116, v116, v40, v41\n\t"
        "v_max3_u32 v116, v116, v42, v43\n\t"
        "v_max3_u32 v116, v116, v44, v45\n\t"
        "v_max3_u32 v116, v116, v46, v47\n\t"
        "v_max3_u32 v116, v116, v48, v49\n\t"
        "v_max3_u32 v116, v116, v50, v51\n\t"
        "v_max3_u32 v116, v116, v52, v53\n\t"
        "v_max3_u32 v116, v116, v54, v55\n\t"
        "v_max3_u32 v116, v116, v56, v57\n\t"
        "v_max3_u32 v116, v116, v58, v59\n\t"
        "v_max3_u32 v116, v116, v60, v61\n\t"
        "v_max3_u32 v116, v116, v62, v63\n\t"
        "v_max3_u32 v116, v116, v64, v65\n\t"
        "v_max3_u32 v116, v116, v66, v67\n\t"
        "v_max3_u32 v116, v116, v68, v69\n\t"
        "v_max3_u32 v116, v116, v70, v71\n\t"
        "v_max3_u32 v116, v116, v72, v73\n\t"
        "v_max3_u32 v116, v116, v74, v75\n\t"
        "v_max3_u32 v116, v116, v76, v77\n\t"
        "v_max3_u32 v116, v116, v78, v79\n\t"
        "v_max3_u32 v116, v116, v80, v81\n\t"
        "v_max3_u32 v116, v116, v82, v83\n\t"
        "v_max3_u32 v116, v116, v84, v85\n\t"
        "v_max3_u32 v116, v116, v86, v87\n\t"
        "v_max3_u32 v116, v116, v88, v89\n\t"
        "v_max3_u32 v116, v116, v90, v91\n\t"
        "v_max3_u32 v116, v116, v92, v93\n\t"
        "v_max3_u32 v116, v116, v94, v95\n\t"
        "v_max3_u32 v116, v116, v96, v97\n\t"
        "v_max3_u32 v116, v116, v98, v99\n\t"
        "v_max3_u32 v116, v116, v100, v101\n\t"
        "v_max3_u32 v116, v116, v102, v103\n\t"
        "v_max3_u32 v116, v116, v104, v105\n\t"
        "v_max3_u32 v116, v116, v106, v107\n\t"
        "v_max3_u32 v116, v116, v108, v109\n\t"
        "v_max3_u32 v116, v116, v110, v111\n\t"
        "v_cmp_eq_u32 vcc, 0xffbfffbf, v116\n\t"
        "s_cmp_lt_u32 %[pass], %[cap]\n\t"
        "s_cbranch_vccz Lbad%=\n\t"
        "s_cbranch_scc1 Lzp_%=\n\t"
        "Lbad%=:\n\t"
        "s_mov_b32 %[bad], 1\n\t"
        "s_branch Lend%=\n\t"
        "Lfi%=:\n\t"
        "s_mov_b32 s97, 1\n\t"
        "v_mov_b32 v116, %[fv]\n\t"
        "ds_write_b32 v120, v116 offset:16392\n\t"
        "s_waitcnt lgkmcnt(0)\n\t"
        "s_barrier\n\t"
        "s_sleep 2\n\t"
        "s_branch Lzp_%=\n\t"
        "Lwu%=:\n\t"
        "ds_read_b32 v121, %[uf]\n\t"
        "s_waitcnt lgkmcnt(0)\n\t"
        "v_cmp_ne_u32 vcc, %[fv], v121\n\t"
        "s_cbranch_vccnz Lwu%=\n\t"
        "s_branch Lrd%=\n\t"
        "Lend%=:\n\t"
        "s_setprio 0\n\t"
        : [bad] "=&s"(bad), [pass] "=&s"(pass)
        : [sidx] "v"(sidx), [pv] "v"(pv), [zv] "v"(zv), [ra] "v"(ra), [rb] "v"(rb), [mo] "v"(mo), [uf] "v"(uf), [rs] "s"(rs), [so] "s"(so), [sz] "s"(sz), [sm] "s"(sm), [lb] "s"(lb), [fv] "s"(fv), [cap] "s"(cap), [zp] "s"(zp), [px] "s"(px), [zx] "s"(zx)
        : "s80", "s81", "s82", "s83", "s84", "s85", "s86", "s87", "s88", "s89", "s90", "s91", "s92", "s93", "s94", "s95", "s96", "s97", "s98", "v14", "v15", "v16", "v17", "v18", "v19", "v20", "v21", "v22", "v23", "v24", "v25", "v26", "v27", "v28", "v29", "v30", "v31", "v32", "v33", "v34", "v35", "v36", "v37", "v38", "v39", "v40", "v41", "v42", "v43", "v44", "v45", "v46", "v47", "v48", "v49", "v50", "v51", "v52", "v53", "v54", "v55", "v56", "v57", "v58", "v59", "v60", "v61", "v62", "v63", "v64", "v65", "v66", "v67", "v68", "v69", "v70", "v71", "v72", "v73", "v74", "v75", "v76", "v77", "v78", "v79", "v80", "v81", "v82", "v83", "v84", "v85", "v86", "v87", "v88", "v89", "v90", "v91", "v92", "v93", "v94", "v95", "v96", "v97", "v98", "v99", "v100", "v101", "v102", "v103", "v104", "v105", "v106", "v107", "v108", "v109", "v110", "v111", "v112", "v113", "v114", "v115", "v116", "v117", "v118", "v119", "v120", "v121", "vcc", "scc", "memory");
}

__device__ __forceinline__ void wx0(const u32x32 (&W)[5], u32 xa, u32 oa, u32x4 rs, u32 xv, u32 xs, u32 xd, u32 o0, u32 o1 STP) {
    asm volatile(
        "ds_read_b128 v[192:195], %[xa] offset:0\n\t"
        "ds_read_b128 v[196:199], %[xa] offset:20480\n\t"
        "ds_read_b128 v[200:203], %[xa] offset:1024\n\t"
        "ds_read_b128 v[204:207], %[xa] offset:21504\n\t"
        "ds_read_b128 v[208:211], %[xa] offset:2048\n\t"
        "ds_read_b128 v[212:215], %[xa] offset:22528\n\t"
        "s_waitcnt vmcnt(8)\n\t"
        "s_barrier\n\t"
        "s_waitcnt lgkmcnt(4)\n\t"
        "v_mfma_f32_16x16x32_bf16 v[176:179], v[16:19], v[192:195], 0\n\t"
        "ds_read_b128 v[216:219], %[xa] offset:3072\n\t"
        "v_mfma_f32_16x16x32_bf16 v[184:187], v[16:19], v[196:199], 0\n\t"
        "ds_read_b128 v[220:223], %[xa] offset:23552\n\t"
        "v_mfma_f32_16x16x32_bf16 v[180:183], v[96:99], v[192:195], 0\n\t"
        "v_mfma_f32_16x16x32_bf16 v[188:191], v[96:99], v[196:199], 0\n\t"
        "s_waitcnt lgkmcnt(4)\n\t"
        "v_mfma_f32_16x16x32_bf16 v[176:179], v[20:23], v[200:203], v[176:179]\n\t"
        "ds_read_b128 v[192:195], %[xa] offset:4096\n\t"
        "v_mfma_f32_16x16x32_bf16 v[184:187], v[20:23], v[204:207], v[184:187]\n\t"
        "ds_read_b128 v[196:199], %[xa] offset:24576\n\t"
        "v_mfma_f32_16x16x32_bf16 v[180:183], v[100:103], v[200:203], v[180:183]\n\t"
        "v_mfma_f32_16x16x32_bf16 v[188:191], v[100:103], v[204:207], v[188:191]\n\t"
        "s_waitcnt lgkmcnt(4)\n\t"
        "v_mfma_f32_16x16x32_bf16 v[176:179], v[24:27], v[208:211], v[176:179]\n\t"
        "ds_read_b128 v[200:203], %[xa] offset:5120\n\t"
        "v_mfma_f32_16x16x32_bf16 v[184:187], v[24:27], v[212:215], v[184:187]\n\t"
        "ds_read_b128 v[204:207], %[xa] offset:25600\n\t"
        "v_mfma_f32_16x16x32_bf16 v[180:183], v[104:107], v[208:211], v[180:183]\n\t"
        "v_mfma_f32_16x16x32_bf16 v[188:191], v[104:107], v[212:215], v[188:191]\n\t"
        "s_waitcnt lgkmcnt(4)\n\t"
        "v_mfma_f32_16x16x32_bf16 v[176:179], v[28:31], v[216:219], v[176:179]\n\t"
        "ds_read_b128 v[208:211], %[xa] offset:6144\n\t"
        "v_mfma_f32_16x16x32_bf16 v[184:187], v[28:31], v[220:223], v[184:187]\n\t"
        "ds_read_b128 v[212:215], %[xa] offset:26624\n\t"
        "v_mfma_f32_16x16x32_bf16 v[180:183], v[108:111], v[216:219], v[180:183]\n\t"
        "v_mfma_f32_16x16x32_bf16 v[188:191], v[108:111], v[220:223], v[188:191]\n\t"
        "s_waitcnt lgkmcnt(4)\n\t"
        "v_mfma_f32_16x16x32_bf16 v[176:179], v[32:35], v[192:195], v[176:179]\n\t"
        "ds_read_b128 v[216:219], %[xa] offset:7168\n\t"
        "v_mfma_f32_16x16x32_bf16 v[184:187], v[32:35], v[196:199], v[184:187]\n\t"
        "ds_read_b128 v[220:223], %[xa] offset:27648\n\t"
        "v_mfma_f32_16x16x32_bf16 v[180:183], v[112:115], v[192:195], v[180:183]\n\t"
        "v_mfma_f32_16x16x32_bf16 v[188:191], v[112:115], v[196:199], v[188:191]\n\t"
        "s_waitcnt lgkmcnt(4)\n\t"
        "v_mfma_f32_16x16x32_bf16 v[176:179], v[36:39], v[200:203], v[176:179]\n\t"
        "ds_read_b128 v[192:195], %[xa] offset:8192\n\t"
        "v_mfma_f32_16x16x32_bf16 v[184:187], v[36:39], v[204:207], v[184:187]\n\t"
        "ds_read_b128 v[196:199], %[xa] offset:28672\n\t"
        "v_mfma_f32_16x16x32_bf16 v[180:183], v[116:119], v[200:203], v[180:183]\n\t"
        "v_mfma_f32_16x16x32_bf16 v[188:191], v[116:119], v[204:207], v[188:191]\n\t"
        "s_waitcnt lgkmcnt(4)\n\t"
        "v_mfma_f32_16x16x32_bf16 v[176:179], v[40:43], v[208:211], v[176:179]\n\t"
        "ds_read_b128 v[200:203], %[xa] offset:9216\n\t"
        "v_mfma_f32_16x16x32_bf16 v[184:187], v[40:43], v[212:215], v[184:187]\n\t"
        "ds_read_b128 v[204:207], %[xa] offset:29696\n\t"
        "v_mfma_f32_16x16x32_bf16 v[180:183], v[120:123], v[208:211], v[180:183]\n\t"
        "v_mfma_f32_16x16x32_bf16 v[188:191], v[120:123], v[212:215], v[188:191]\n\t"
        "s_waitcnt lgkmcnt(4)\n\t"
        "v_mfma_f32_16x16x32_bf16 v[176:179], v[44:47], v[216:219], v[176:179]\n\t"
        "ds_read_b128 v[208:211], %[xa] offset:10240\n\t"
        "v_mfma_f32_16x16x32_bf16 v[184:187], v[44:47], v[220:223], v[184:187]\n\t"
        "ds_read_b128 v[212:215], %[xa] offset:30720\n\t"
        "v_mfma_f32_16x16x32_bf16 v[180:183], v[124:127], v[216:219], v[180:183]\n\t"
        "v_mfma_f32_16x16x32_bf16 v[188:191], v[124:127], v[220:223], v[188:191]\n\t"
        "s_waitcnt lgkmcnt(4)\n\t"
        "v_mfma_f32_16x16x32_bf16 v[176:179], v[48:51], v[192:195], v[176:179]\n\t"
        "ds_read_b128 v[216:219], %[xa] offset:11264\n\t"
        "v_mfma_f32_16x16x32_bf16 v[184:187], v[48:51], v[196:199], v[184:187]\n\t"
        "ds_read_b128 v[220:223], %[xa] offset:31744\n\t"
        "v_mfma_f32_16x16x32_bf16 v[180:183], v[128:131], v[192:195], v[180:183]\n\t"
        "v_mfma_f32_16x16x32_bf16 v[188:191], v[128:131], v[196:199], v[188:191]\n\t"
        "s_waitcnt lgkmcnt(4)\n\t"
        "v_mfma_f32_16x16x32_bf16 v[176:179], v[52:55], v[200:203], v[176:179]\n\t"
        "ds_read_b128 v[192:195], %[xa] offset:12288\n\t"
        "v_mfma_f32_16x16x32_bf16 v[184:187], v[52:55], v[204:207], v[184:187]\n\t"
        "ds_read_b128 v[196:199], %[xa] offset:32768\n\t"
        "v_mfma_f32_16x16x32_bf16 v[180:183], v[132:135], v[200:203], v[180:183]\n\t"
        "v_mfma_f32_16x16x32_bf16 v[188:191], v[132:135], v[204:207], v[188:191]\n\t"
        "s_waitcnt lgkmcnt(4)\n\t"
        "v_mfma_f32_16x16x32_bf16 v[176:179], v[56:59], v[208:211], v[176:179]\n\t"
        "ds_read_b128 v[200:203], %[xa] offset:13312\n\t"
        "v_mfma_f32_16x16x32_bf16 v[184:187], v[56:59], v[212:215], v[184:187]\n\t"
        "ds_read_b128 v[204:207], %[xa] offset:33792\n\t"
        "v_mfma_f32_16x16x32_bf16 v[180:183], v[136:139], v[208:211], v[180:183]\n\t"
        "v_mfma_f32_16x16x32_bf16 v[188:191], v[136:139], v[212:215], v[188:191]\n\t"
        "s_waitcnt lgkmcnt(4)\n\t"
        "v_mfma_f32_16x16x32_bf16 v[176:179], v[60:63], v[216:219], v[176:179]\n\t"
        "ds_read_b128 v[208:211], %[xa] offset:14336\n\t"
        "v_mfma_f32_16x16x32_bf16 v[184:187], v[60:63], v[220:223], v[184:187]\n\t"
        "ds_read_b128 v[212:215], %[xa] offset:34816\n\t"
        "v_mfma_f32_16x16x32_bf16 v[180:183], v[140:143], v[216:219], v[180:183]\n\t"
        "v_mfma_f32_16x16x32_bf16 v[188:191], v[140:143], v[220:223], v[188:191]\n\t"
        "s_waitcnt lgkmcnt(4)\n\t"
        "v_mfma_f32_16x16x32_bf16 v[176:179], v[64:67], v[192:195], v[176:179]\n\t"
        "ds_read_b128 v[216:219], %[xa] offset:15360\n\t"
        "v_mfma_f32_16x16x32_bf16 v[184:187], v[64:67], v[196:199], v[184:187]\n\t"
        "ds_read_b128 v[220:223], %[xa] offset:35840\n\t"
        "v_mfma_f32_16x16x32_bf16 v[180:183], v[144:147], v[192:195], v[180:183]\n\t"
        "v_mfma_f32_16x16x32_bf16 v[188:191], v[144:147], v[196:199], v[188:191]\n\t"
        "s_waitcnt lgkmcnt(4)\n\t"
        "v_mfma_f32_16x16x32_bf16 v[176:179], v[68:71], v[200:203], v[176:179]\n\t"
        "ds_read_b128 v[192:195], %[xa] offset:16384\n\t"
        "v_mfma_f32_16x16x32_bf16 v[184:187], v[68:71], v[204:207], v[184:187]\n\t"
        "ds_read_b128 v[196:199], %[xa] offset:36864\n\t"
        "v_mfma_f32_16x16x32_bf16 v[180:183], v[148:151], v[200:203], v[180:183]\n\t"
        "v_mfma_f32_16x16x32_bf16 v[188:191], v[148:151], v[204:207], v[188:191]\n\t"
        "s_waitcnt lgkmcnt(4)\n\t"
        "v_mfma_f32_16x16x32_bf16 v[176:179], v[72:75], v[208:211], v[176:179]\n\t"
        "ds_read_b128 v[200:203], %[xa] offset:17408\n\t"
        "v_mfma_f32_16x16x32_bf16 v[184:187], v[72:75], v[212:215], v[184:187]\n\t"
        "ds_read_b128 v[204:207], %[xa] offset:37888\n\t"
        "v_mfma_f32_16x16x32_bf16 v[180:183], v[152:155], v[208:211], v[180:183]\n\t"
        "v_mfma_f32_16x16x32_bf16 v[188:191], v[152:155], v[212:215], v[188:191]\n\t"
        "s_waitcnt lgkmcnt(4)\n\t"
        "v_mfma_f32_16x16x32_bf16 v[176:179], v[76:79], v[216:219], v[176:179]\n\t"
        "ds_read_b128 v[208:211], %[xa] offset:18432\n\t"
        "v_mfma_f32_16x16x32_bf16 v[184:187], v[76:79], v[220:223], v[184:187]\n\t"
        "ds_read_b128 v[212:215], %[xa] offset:38912\n\t"
        "v_mfma_f32_16x16x32_bf16 v[180:183], v[156:159], v[216:219], v[180:183]\n\t"
        "v_mfma_f32_16x16x32_bf16 v[188:191], v[156:159], v[220:223], v[188:191]\n\t"
        "s_waitcnt lgkmcnt(4)\n\t"
        "v_mfma_f32_16x16x32_bf16 v[176:179], v[80:83], v[192:195], v[176:179]\n\t"
        "ds_read_b128 v[216:219], %[xa] offset:19456\n\t"
        "v_mfma_f32_16x16x32_bf16 v[184:187], v[80:83], v[196:199], v[184:187]\n\t"
        "ds_read_b128 v[220:223], %[xa] offset:39936\n\t"
        "v_mfma_f32_16x16x32_bf16 v[180:183], v[160:163], v[192:195], v[180:183]\n\t"
        "v_mfma_f32_16x16x32_bf16 v[188:191], v[160:163], v[196:199], v[188:191]\n\t"
        "s_waitcnt lgkmcnt(4)\n\t"
        "v_mfma_f32_16x16x32_bf16 v[176:179], v[84:87], v[200:203], v[176:179]\n\t"
        "v_mfma_f32_16x16x32_bf16 v[184:187], v[84:87], v[204:207], v[184:187]\n\t"
        "v_mfma_f32_16x16x32_bf16 v[180:183], v[164:167], v[200:203], v[180:183]\n\t"
        "v_mfma_f32_16x16x32_bf16 v[188:191], v[164:167], v[204:207], v[188:191]\n\t"
        "s_waitcnt lgkmcnt(2)\n\t"
        "v_mfma_f32_16x16x32_bf16 v[176:179], v[88:91], v[208:211], v[176:179]\n\t"
        "v_mfma_f32_16x16x32_bf16 v[184:187], v[88:91], v[212:215], v[184:187]\n\t"
        "v_mfma_f32_16x16x32_bf16 v[180:183], v[168:171], v[208:211], v[180:183]\n\t"
        "v_mfma_f32_16x16x32_bf16 v[188:191], v[168:171], v[212:215], v[188:191]\n\t"
        "s_waitcnt lgkmcnt(0)\n\t"
        "v_mfma_f32_16x16x32_bf16 v[176:179], v[92:95], v[216:219], v[176:179]\n\t"
        "v_mfma_f32_16x16x32_bf16 v[184:187], v[92:95], v[220:223], v[184:187]\n\t"
        "v_mfma_f32_16x16x32_bf16 v[180:183], v[172:175], v[216:219], v[180:183]\n\t"
        "v_mfma_f32_16x16x32_bf16 v[188:191], v[172:175], v[220:223], v[188:191]\n\t"
        "s_nop 4\n\t"
        "v_cvt_pk_bf16_f32 v224, v176, v177\n\t"
        "v_cvt_pk_bf16_f32 v225, v178, v179\n\t"
        "v_cvt_pk_bf16_f32 v226, v180, v181\n\t"
        "v_cvt_pk_bf16_f32 v227, v182, v183\n\t"
        "buffer_store_dwordx4 v[224:227], %[oa], %[rs], %[o0] offen sc1\n\t"
        "v_cvt_pk_bf16_f32 v228, v184, v185\n\t"
        "v_cvt_pk_bf16_f32 v229, v186, v187\n\t"
        "v_cvt_pk_bf16_f32 v230, v188, v189\n\t"
        "v_cvt_pk_bf16_f32 v231, v190, v191\n\t"
        "buffer_store_dwordx4 v[228:231], %[oa], %[rs], %[o1] offen sc1\n\t"
        "s_mov_b32 s88, m0\n\t"
        "s_mov_b32 m0, %[xd]\n\t"
        "s_add_u32 s89, %[xs], 0x1000\n\t"
        "buffer_load_dwordx4 %[xv], %[rs], %[xs] offen offset:0 sc1 lds\n\t"
        "buffer_load_dwordx4 %[xv], %[rs], %[xs] offen offset:1024 sc1 lds\n\t"
        "buffer_load_dwordx4 %[xv], %[rs], %[xs] offen offset:2048 sc1 lds\n\t"
        "buffer_load_dwordx4 %[xv], %[rs], %[xs] offen offset:3072 sc1 lds\n\t"
        "s_add_u32 m0, m0, 0x1000\n\t"
        "s_nop 0\n\t"
        "buffer_load_dwordx4 %[xv], %[rs], s89 offen offset:0 sc1 lds\n\t"
        "buffer_load_dwordx4 %[xv], %[rs], s89 offen offset:1024 sc1 lds\n\t"
        "buffer_load_dwordx4 %[xv], %[rs], s89 offen offset:2048 sc1 lds\n\t"
        "buffer_load_dwordx4 %[xv], %[rs], s89 offen offset:3072 sc1 lds\n\t"
        "s_mov_b32 m0, s88\n\t"
        :
        : [w0] "{v[16:47]}"(W[0]), [w1] "{v[48:79]}"(W[1]), [w2] "{v[80:111]}"(W[2]), [w3] "{v[112:143]}"(W[3]), [w4] "{v[144:175]}"(W[4]), [xa] "v"(xa), [oa] "v"(oa), [xv] "v"(xv), [xs] "s"(xs), [xd] "s"(xd), [rs] "s"(rs), [o0] "s"(o0), [o1] "s"(o1)
        : "s88", "s89", "v176", "v177", "v178", "v179", "v180", "v181", "v182", "v183", "v184", "v185", "v186", "v187", "v188", "v189", "v190", "v191", "v192", "v193", "v194", "v195", "v196", "v197", "v198", "v199", "v200", "v201", "v202", "v203", "v204", "v205", "v206", "v207", "v208", "v209", "v210", "v211", "v212", "v213", "v214", "v215", "v216", "v217", "v218", "v219", "v220", "v221", "v222", "v223", "v224", "v225", "v226", "v227", "v228", "v229", "v230", "v231", "vcc", "scc", "memory");
}

__device__ __forceinline__ void wx1(const u32x32 (&W)[5], u32 xa, u32 oa STP) {
    asm volatile(
        "ds_read_b128 v[192:195], %[xa] offset:0\n\t"
        "ds_read_b128 v[196:199], %[xa] offset:20480\n\t"
        "ds_read_b128 v[200:203], %[xa] offset:1024\n\t"
        "ds_read_b128 v[204:207], %[xa] offset:21504\n\t"
        "ds_read_b128 v[208:211], %[xa] offset:2048\n\t"
        "ds_read_b128 v[212:215], %[xa] offset:22528\n\t"
        "s_waitcnt lgkmcnt(6)\n\t"
        "s_barrier\n\t"
        "s_waitcnt lgkmcnt(4)\n\t"
        "v_mfma_f32_16x16x32_bf16 v[176:179], v[16:19], v[192:195], 0\n\t"
        "ds_read_b128 v[216:219], %[xa] offset:3072\n\t"
        "v_mfma_f32_16x16x32_bf16 v[184:187], v[16:19], v[196:199], 0\n\t"
        "ds_read_b128 v[220:223], %[xa] offset:23552\n\t"
        "v_mfma_f32_16x16x32_bf16 v[180:183], v[96:99], v[192:195], 0\n\t"
        "v_mfma_f32_16x16x32_bf16 v[188:191], v[96:99], v[196:199], 0\n\t"
        "s_waitcnt lgkmcnt(4)\n\t"
        "v_mfma_f32_16x16x32_bf16 v[176:179], v[20:23], v[200:203], v[176:179]\n\t"
        "ds_read_b128 v[192:195], %[xa] offset:4096\n\t"
        "v_mfma_f32_16x16x32_bf16 v[184:187], v[20:23], v[204:207], v[184:187]\n\t"
        "ds_read_b128 v[196:199], %[xa] offset:24576\n\t"
        "v_mfma_f32_16x16x32_bf16 v[180:183], v[100:103], v[200:203], v[180:183]\n\t"
        "v_mfma_f32_16x16x32_bf16 v[188:191], v[100:103], v[204:207], v[188:191]\n\t"
        "s_waitcnt lgkmcnt(4)\n\t"
        "v_mfma_f32_16x16x32_bf16 v[176:179], v[24:27], v[208:211], v[176:179]\n\t"
        "ds_read_b128 v[200:203], %[xa] offset:5120\n\t"
        "v_mfma_f32_16x16x32_bf16 v[184:187], v[24:27], v[212:215], v[184:187]\n\t"
        "ds_read_b128 v[204:207], %[xa] offset:25600\n\t"
        "v_mfma_f32_16x16x32_bf16 v[180:183], v[104:107], v[208:211], v[180:183]\n\t"
        "v_mfma_f32_16x16x32_bf16 v[188:191], v[104:107], v[212:215], v[188:191]\n\t"
        "s_waitcnt lgkmcnt(4)\n\t"
        "v_mfma_f32_16x16x32_bf16 v[176:179], v[28:31], v[216:219], v[176:179]\n\t"
        "ds_read_b128 v[208:211], %[xa] offset:6144\n\t"
        "v_mfma_f32_16x16x32_bf16 v[184:187], v[28:31], v[220:223], v[184:187]\n\t"
        "ds_read_b128 v[212:215], %[xa] offset:26624\n\t"
        "v_mfma_f32_16x16x32_bf16 v[180:183], v[108:111], v[216:219], v[180:183]\n\t"
        "v_mfma_f32_16x16x32_bf16 v[188:191], v[108:111], v[220:223], v[188:191]\n\t"
        "s_waitcnt lgkmcnt(4)\n\t"
        "v_mfma_f32_16x16x32_bf16 v[176:179], v[32:35], v[192:195], v[176:179]\n\t"
        "ds_read_b128 v[216:219], %[xa] offset:7168\n\t"
        "v_mfma_f32_16x16x32_bf16 v[184:187], v[32:35], v[196:199], v[184:187]\n\t"
        "ds_read_b128 v[220:223], %[xa] offset:27648\n\t"
        "v_mfma_f32_16x16x32_bf16 v[180:183], v[112:115], v[192:195], v[180:183]\n\t"
        "v_mfma_f32_16x16x32_bf16 v[188:191], v[112:115], v[196:199], v[188:191]\n\t"
        "s_waitcnt lgkmcnt(4)\n\t"
        "v_mfma_f32_16x16x32_bf16 v[176:179], v[36:39], v[200:203], v[176:179]\n\t"
        "ds_read_b128 v[192:195], %[xa] offset:8192\n\t"
        "v_mfma_f32_16x16x32_bf16 v[184:187], v[36:39], v[204:207], v[184:187]\n\t"
        "ds_read_b128 v[196:199], %[xa] offset:28672\n\t"
        "v_mfma_f32_16x16x32_bf16 v[180:183], v[116:119], v[200:203], v[180:183]\n\t"
        "v_mfma_f32_16x16x32_bf16 v[188:191], v[116:119], v[204:207], v[188:191]\n\t"
        "s_waitcnt lgkmcnt(4)\n\t"
        "v_mfma_f32_16x16x32_bf16 v[176:179], v[40:43], v[208:211], v[176:179]\n\t"
        "ds_read_b128 v[200:203], %[xa] offset:9216\n\t"
        "v_mfma_f32_16x16x32_bf16 v[184:187], v[40:43], v[212:215], v[184:187]\n\t"
        "ds_read_b128 v[204:207], %[xa] offset:29696\n\t"
        "v_mfma_f32_16x16x32_bf16 v[180:183], v[120:123], v[208:211], v[180:183]\n\t"
        "v_mfma_f32_16x16x32_bf16 v[188:191], v[120:123], v[212:215], v[188:191]\n\t"
        "s_waitcnt lgkmcnt(4)\n\t"
        "v_mfma_f32_16x16x32_bf16 v[176:179], v[44:47], v[216:219], v[176:179]\n\t"
        "ds_read_b128 v[208:211], %[xa] offset:10240\n\t"
        "v_mfma_f32_16x16x32_bf16 v[184:187], v[44:47], v[220:223], v[184:187]\n\t"
        "ds_read_b128 v[212:215], %[xa] offset:30720\n\t"
        "v_mfma_f32_16x16x32_bf16 v[180:183], v[124:127], v[216:219], v[180:183]\n\t"
        "v_mfma_f32_16x16x32_bf16 v[188:191], v[124:127], v[220:223], v[188:191]\n\t"
        "s_waitcnt lgkmcnt(4)\n\t"
        "v_mfma_f32_16x16x32_bf16 v[176:179], v[48:51], v[192:195], v[176:179]\n\t"
        "ds_read_b128 v[216:219], %[xa] offset:11264\n\t"
        "v_mfma_f32_16x16x32_bf16 v[184:187], v[48:51], v[196:199], v[184:187]\n\t"
        "ds_read_b128 v[220:223], %[xa] offset:31744\n\t"
        "v_mfma_f32_16x16x32_bf16 v[180:183], v[128:131], v[192:195], v[180:183]\n\t"
        "v_mfma_f32_16x16x32_bf16 v[188:191], v[128:131], v[196:199], v[188:191]\n\t"
        "s_waitcnt lgkmcnt(4)\n\t"
        "v_mfma_f32_16x16x32_bf16 v[176:179], v[52:55], v[200:203], v[176:179]\n\t"
        "ds_read_b128 v[192:195], %[xa] offset:12288\n\t"
        "v_mfma_f32_16x16x32_bf16 v[184:187], v[52:55], v[204:207], v[184:187]\n\t"
        "ds_read_b128 v[196:199], %[xa] offset:32768\n\t"
        "v_mfma_f32_16x16x32_bf16 v[180:183], v[132:135], v[200:203], v[180:183]\n\t"
        "v_mfma_f32_16x16x32_bf16 v[188:191], v[132:135], v[204:207], v[188:191]\n\t"
        "s_waitcnt lgkmcnt(4)\n\t"
        "v_mfma_f32_16x16x32_bf16 v[176:179], v[56:59], v[208:211], v[176:179]\n\t"
        "ds_read_b128 v[200:203], %[xa] offset:13312\n\t"
        "v_mfma_f32_16x16x32_bf16 v[184:187], v[56:59], v[212:215], v[184:187]\n\t"
        "ds_read_b128 v[204:207], %[xa] offset:33792\n\t"
        "v_mfma_f32_16x16x32_bf16 v[180:183], v[136:139], v[208:211], v[180:183]\n\t"
        "v_mfma_f32_16x16x32_bf16 v[188:191], v[136:139], v[212:215], v[188:191]\n\t"
        "s_waitcnt lgkmcnt(4)\n\t"
        "v_mfma_f32_16x16x32_bf16 v[176:179], v[60:63], v[216:219], v[176:179]\n\t"
        "ds_read_b128 v[208:211], %[xa] offset:14336\n\t"
        "v_mfma_f32_16x16x32_bf16 v[184:187], v[60:63], v[220:223], v[184:187]\n\t"
        "ds_read_b128 v[212:215], %[xa] offset:34816\n\t"
        "v_mfma_f32_16x16x32_bf16 v[180:183], v[140:143], v[216:219], v[180:183]\n\t"
        "v_mfma_f32_16x16x32_bf16 v[188:191], v[140:143], v[220:223], v[188:191]\n\t"
        "s_waitcnt lgkmcnt(4)\n\t"
        "v_mfma_f32_16x16x32_bf16 v[176:179], v[64:67], v[192:195], v[176:179]\n\t"
        "ds_read_b128 v[216:219], %[xa] offset:15360\n\t"
        "v_mfma_f32_16x16x32_bf16 v[184:187], v[64:67], v[196:199], v[184:187]\n\t"
        "ds_read_b128 v[220:223], %[xa] offset:35840\n\t"
        "v_mfma_f32_16x16x32_bf16 v[180:183], v[144:147], v[192:195], v[180:183]\n\t"
        "v_mfma_f32_16x16x32_bf16 v[188:191], v[144:147], v[196:199], v[188:191]\n\t"
        "s_waitcnt lgkmcnt(4)\n\t"
        "v_mfma_f32_16x16x32_bf16 v[176:179], v[68:71], v[200:203], v[176:179]\n\t"
        "ds_read_b128 v[192:195], %[xa] offset:16384\n\t"
        "v_mfma_f32_16x16x32_bf16 v[184:187], v[68:71], v[204:207], v[184:187]\n\t"
        "ds_read_b128 v[196:199], %[xa] offset:36864\n\t"
        "v_mfma_f32_16x16x32_bf16 v[180:183], v[148:151], v[200:203], v[180:183]\n\t"
        "v_mfma_f32_16x16x32_bf16 v[188:191], v[148:151], v[204:207], v[188:191]\n\t"
        "s_waitcnt lgkmcnt(4)\n\t"
        "v_mfma_f32_16x16x32_bf16 v[176:179], v[72:75], v[208:211], v[176:179]\n\t"
        "ds_read_b128 v[200:203], %[xa] offset:17408\n\t"
        "v_mfma_f32_16x16x32_bf16 v[184:187], v[72:75], v[212:215], v[184:187]\n\t"
        "ds_read_b128 v[204:207], %[xa] offset:37888\n\t"
        "v_mfma_f32_16x16x32_bf16 v[180:183], v[152:155], v[208:211], v[180:183]\n\t"
        "v_mfma_f32_16x16x32_bf16 v[188:191], v[152:155], v[212:215], v[188:191]\n\t"
        "s_waitcnt lgkmcnt(4)\n\t"
        "v_mfma_f32_16x16x32_bf16 v[176:179], v[76:79], v[216:219], v[176:179]\n\t"
        "ds_read_b128 v[208:211], %[xa] offset:18432\n\t"
        "v_mfma_f32_16x16x32_bf16 v[184:187], v[76:79], v[220:223], v[184:187]\n\t"
        "ds_read_b128 v[212:215], %[xa] offset:38912\n\t"
        "v_mfma_f32_16x16x32_bf16 v[180:183], v[156:159], v[216:219], v[180:183]\n\t"
        "v_mfma_f32_16x16x32_bf16 v[188:191], v[156:159], v[220:223], v[188:191]\n\t"
        "s_waitcnt lgkmcnt(4)\n\t"
        "v_mfma_f32_16x16x32_bf16 v[176:179], v[80:83], v[192:195], v[176:179]\n\t"
        "ds_read_b128 v[216:219], %[xa] offset:19456\n\t"
        "v_mfma_f32_16x16x32_bf16 v[184:187], v[80:83], v[196:199], v[184:187]\n\t"
        "ds_read_b128 v[220:223], %[xa] offset:39936\n\t"
        "v_mfma_f32_16x16x32_bf16 v[180:183], v[160:163], v[192:195], v[180:183]\n\t"
        "v_mfma_f32_16x16x32_bf16 v[188:191], v[160:163], v[196:199], v[188:191]\n\t"
        "s_waitcnt lgkmcnt(4)\n\t"
        "v_mfma_f32_16x16x32_bf16 v[176:179], v[84:87], v[200:203], v[176:179]\n\t"
        "v_mfma_f32_16x16x32_bf16 v[184:187], v[84:87], v[204:207], v[184:187]\n\t"
        "v_mfma_f32_16x16x32_bf16 v[180:183], v[164:167], v[200:203], v[180:183]\n\t"
        "v_mfma_f32_16x16x32_bf16 v[188:191], v[164:167], v[204:207], v[188:191]\n\t"
        "s_waitcnt lgkmcnt(2)\n\t"
        "v_mfma_f32_16x16x32_bf16 v[176:179], v[88:91], v[208:211], v[176:179]\n\t"
        "v_mfma_f32_16x16x32_bf16 v[184:187], v[88:91], v[212:215], v[184:187]\n\t"
        "v_mfma_f32_16x16x32_bf16 v[180:183], v[168:171], v[208:211], v[180:183]\n\t"
        "v_mfma_f32_16x16x32_bf16 v[188:191], v[168:171], v[212:215], v[188:191]\n\t"
        "s_waitcnt lgkmcnt(0)\n\t"
        "v_mfma_f32_16x16x32_bf16 v[176:179], v[92:95], v[216:219], v[176:179]\n\t"
        "v_mfma_f32_16x16x32_bf16 v[184:187], v[92:95], v[220:223], v[184:187]\n\t"
        "v_mfma_f32_16x16x32_bf16 v[180:183], v[172:175], v[216:219], v[180:183]\n\t"
        "v_mfma_f32_16x16x32_bf16 v[188:191], v[172:175], v[220:223], v[188:191]\n\t"
        "s_nop 4\n\t"
        "v_cvt_pk_bf16_f32 v224, v176, v177\n\t"
        "v_cvt_pk_bf16_f32 v225, v178, v179\n\t"
        "v_cvt_pk_bf16_f32 v226, v180, v181\n\t"
        "v_cvt_pk_bf16_f32 v227, v182, v183\n\t"
        "ds_write_b128 %[oa], v[224:227] offset:0\n\t"
        "v_cvt_pk_bf16_f32 v228, v184, v185\n\t"
        "v_cvt_pk_bf16_f32 v229, v186, v187\n\t"
        "v_cvt_pk_bf16_f32 v230, v188, v189\n\t"
        "v_cvt_pk_bf16_f32 v231, v190, v191\n\t"
        "ds_write_b128 %[oa], v[228:231] offset:4096\n\t"
        :
        : [w0] "{v[16:47]}"(W[0]), [w1] "{v[48:79]}"(W[1]), [w2] "{v[80:111]}"(W[2]), [w3] "{v[112:143]}"(W[3]), [w4] "{v[144:175]}"(W[4]), [xa] "v"(xa), [oa] "v"(oa)
        : "v176", "v177", "v178", "v179", "v180", "v181", "v182", "v183", "v184", "v185", "v186", "v187", "v188", "v189", "v190", "v191", "v192", "v193", "v194", "v195", "v196", "v197", "v198", "v199", "v200", "v201", "v202", "v203", "v204", "v205", "v206", "v207", "v208", "v209", "v210", "v211", "v212", "v213", "v214", "v215", "v216", "v217", "v218", "v219", "v220", "v221", "v222", "v223", "v224", "v225", "v226", "v227", "v228", "v229", "v230", "v231", "vcc", "scc", "memory");
}

__device__ __forceinline__ void wk(const u32x32 (&W)[5], u32 xa, u32 oa, u32x4 rs, u32 o0, u32 o1, float sc, u32 xv, u32 xs, u32 xd, u32 ca, u32 fc, u32 fw, u32 fo STP) {
    asm volatile(
        "ds_read_b128 v[192:195], %[xa] offset:0\n\t"
        "ds_read_b128 v[196:199], %[xa] offset:20480\n\t"
        "ds_read_b128 v[200:203], %[xa] offset:1024\n\t"
        "ds_read_b128 v[204:207], %[xa] offset:21504\n\t"
        "ds_read_b128 v[208:211], %[xa] offset:2048\n\t"
        "ds_read_b128 v[212:215], %[xa] offset:22528\n\t"
        "v_mov_b32 v232, %[ca]\n\t"
        "v_mov_b32 v233, 1\n\t"
        "v_mov_b32 v234, %[fw]\n\t"
        "s_mov_b32 s88, m0\n\t"
        "s_mov_b32 m0, %[xd]\n\t"
        "s_add_u32 s89, %[xs], 0x1000\n\t"
        "buffer_load_dwordx4 %[xv], %[rs], %[xs] offen offset:0 sc1 lds\n\t"
        "buffer_load_dwordx4 %[xv], %[rs], %[xs] offen offset:1024 sc1 lds\n\t"
        "s_mov_b32 m0, s88\n\t"
        "s_waitcnt lgkmcnt(4)\n\t"
        "v_mfma_f32_16x16x32_bf16 v[176:179], v[16:19], v[192:195], 0\n\t"
        "ds_read_b128 v[216:219], %[xa] offset:3072\n\t"
        "v_mfma_f32_16x16x32_bf16 v[184:187], v[16:19], v[196:199], 0\n\t"
        "ds_read_b128 v[220:223], %[xa] offset:23552\n\t"
        "v_mfma_f32_16x16x32_bf16 v[180:183], v[96:99], v[192:195], 0\n\t"
        "v_mfma_f32_16x16x32_bf16 v[188:191], v[96:99], v[196:199], 0\n\t"
        "s_waitcnt lgkmcnt(4)\n\t"
        "v_mfma_f32_16x16x32_bf16 v[176:179], v[20:23], v[200:203], v[176:179]\n\t"
        "ds_read_b128 v[192:195], %[xa] offset:4096\n\t"
        "v_mfma_f32_16x16x32_bf16 v[184:187], v[20:23], v[204:207], v[184:187]\n\t"
        "ds_read_b128 v[196:199], %[xa] offset:24576\n\t"
        "v_mfma_f32_16x16x32_bf16 v[180:183], v[100:103], v[200:203], v[180:183]\n\t"
        "v_mfma_f32_16x16x32_bf16 v[188:191], v[100:103], v[204:207], v[188:191]\n\t"
        "s_waitcnt lgkmcnt(4)\n\t"
        "v_mfma_f32_16x16x32_bf16 v[176:179], v[24:27], v[208:211], v[176:179]\n\t"
        "ds_read_b128 v[200:203], %[xa] offset:5120\n\t"
        "v_mfma_f32_16x16x32_bf16 v[184:187], v[24:27], v[212:215], v[184:187]\n\t"
        "ds_read_b128 v[204:207], %[xa] offset:25600\n\t"
        "v_mfma_f32_16x16x32_bf16 v[180:183], v[104:107], v[208:211], v[180:183]\n\t"
        "v_mfma_f32_16x16x32_bf16 v[188:191], v[104:107], v[212:215], v[188:191]\n\t"
        "s_waitcnt lgkmcnt(4)\n\t"
        "v_mfma_f32_16x16x32_bf16 v[176:179], v[28:31], v[216:219], v[176:179]\n\t"
        "ds_read_b128 v[208:211], %[xa] offset:6144\n\t"
        "v_mfma_f32_16x16x32_bf16 v[184:187], v[28:31], v[220:223], v[184:187]\n\t"
        "ds_read_b128 v[212:215], %[xa] offset:26624\n\t"
        "v_mfma_f32_16x16x32_bf16 v[180:183], v[108:111], v[216:219], v[180:183]\n\t"
        "v_mfma_f32_16x16x32_bf16 v[188:191], v[108:111], v[220:223], v[188:191]\n\t"
        "s_waitcnt lgkmcnt(4)\n\t"
        "v_mfma_f32_16x16x32_bf16 v[176:179], v[32:35], v[192:195], v[176:179]\n\t"
        "ds_read_b128 v[216:219], %[xa] offset:7168\n\t"
        "v_mfma_f32_16x16x32_bf16 v[184:187], v[32:35], v[196:199], v[184:187]\n\t"
        "ds_read_b128 v[220:223], %[xa] offset:27648\n\t"
        "v_mfma_f32_16x16x32_bf16 v[180:183], v[112:115], v[192:195], v[180:183]\n\t"
        "v_mfma_f32_16x16x32_bf16 v[188:191], v[112:115], v[196:199], v[188:191]\n\t"
        "s_waitcnt lgkmcnt(4)\n\t"
        "v_mfma_f32_16x16x32_bf16 v[176:179], v[36:39], v[200:203], v[176:179]\n\t"
        "ds_read_b128 v[192:195], %[xa] offset:8192\n\t"
        "v_mfma_f32_16x16x32_bf16 v[184:187], v[36:39], v[204:207], v[184:187]\n\t"
        "ds_read_b128 v[196:199], %[xa] offset:28672\n\t"
        "v_mfma_f32_16x16x32_bf16 v[180:183], v[116:119], v[200:203], v[180:183]\n\t"
        "v_mfma_f32_16x16x32_bf16 v[188:191], v[116:119], v[204:207], v[188:191]\n\t"
        "s_waitcnt lgkmcnt(4)\n\t"
        "v_mfma_f32_16x16x32_bf16 v[176:179], v[40:43], v[208:211], v[176:179]\n\t"
        "ds_read_b128 v[200:203], %[xa] offset:9216\n\t"
        "v_mfma_f32_16x16x32_bf16 v[184:187], v[40:43], v[212:215], v[184:187]\n\t"
        "ds_read_b128 v[204:207], %[xa] offset:29696\n\t"
        "v_mfma_f32_16x16x32_bf16 v[180:183], v[120:123], v[208:211], v[180:183]\n\t"
        "s_waitcnt vmcnt(0)\n\t"
        "s_mov_b64 exec, 1\n\t"
        "ds_add_rtn_u32 v233, v232, v233\n\t"
        "s_mov_b64 exec, -1\n\t"
        "v_mfma_f32_16x16x32_bf16 v[188:191], v[120:123], v[212:215], v[188:191]\n\t"
        "s_waitcnt lgkmcnt(5)\n\t"
        "v_mfma_f32_16x16x32_bf16 v[176:179], v[44:47], v[216:219], v[176:179]\n\t"
        "ds_read_b128 v[208:211], %[xa] offset:10240\n\t"
        "v_mfma_f32_16x16x32_bf16 v[184:187], v[44:47], v[220:223], v[184:187]\n\t"
        "ds_read_b128 v[212:215], %[xa] offset:30720\n\t"
        "v_mfma_f32_16x16x32_bf16 v[180:183], v[124:127], v[216:219], v[180:183]\n\t"
        "v_mfma_f32_16x16x32_bf16 v[188:191], v[124:127], v[220:223], v[188:191]\n\t"
        "s_waitcnt lgkmcnt(5)\n\t"
        "v_mfma_f32_16x16x32_bf16 v[176:179], v[48:51], v[192:195], v[176:179]\n\t"
        "ds_read_b128 v[216:219], %[xa] offset:11264\n\t"
        "v_mfma_f32_16x16x32_bf16 v[184:187], v[48:51], v[196:199], v[184:187]\n\t"
        "ds_read_b128 v[220:223], %[xa] offset:31744\n\t"
        "v_mfma_f32_16x16x32_bf16 v[180:183], v[128:131], v[192:195], v[180:183]\n\t"
        "s_waitcnt lgkmcnt(4)\n\t"
        "s_mov_b64 exec, 1\n\t"
        "v_cmpx_eq_u32 vcc, %[fc], v233\n\t"
        "buffer_store_dword v234, off, %[rs], %[fo] sc1\n\t"
        "s_mov_b64 exec, -1\n\t"
        "v_mfma_f32_16x16x32_bf16 v[188:191], v[128:131], v[196:199], v[188:191]\n\t"
        "s_waitcnt lgkmcnt(5)\n\t"
        "v_mfma_f32_16x16x32_bf16 v[176:179], v[52:55], v[200:203], v[176:179]\n\t"
        "ds_read_b128 v[192:195], %[xa] offset:12288\n\t"
        "v_mfma_f32_16x16x32_bf16 v[184:187], v[52:55], v[204:207], v[184:187]\n\t"
        "ds_read_b128 v[196:199], %[xa] offset:32768\n\t"
        "v_mfma_f32_16x16x32_bf16 v[180:183], v[132:135], v[200:203], v[180:183]\n\t"
        "v_mfma_f32_16x16x32_bf16 v[188:191], v[132:135], v[204:207], v[188:191]\n\t"
        "s_waitcnt lgkmcnt(4)\n\t"
        "v_mfma_f32_16x16x32_bf16 v[176:179], v[56:59], v[208:211], v[176:179]\n\t"
        "ds_read_b128 v[200:203], %[xa] offset:13312\n\t"
        "v_mfma_f32_16x16x32_bf16 v[184:187], v[56:59], v[212:215], v[184:187]\n\t"
        "ds_read_b128 v[204:207], %[xa] offset:33792\n\t"
        "v_mfma_f32_16x16x32_bf16 v[180:183], v[136:139], v[208:211], v[180:183]\n\t"
        "v_mfma_f32_16x16x32_bf16 v[188:191], v[136:139], v[212:215], v[188:191]\n\t"
        "s_waitcnt lgkmcnt(4)\n\t"
        "v_mfma_f32_16x16x32_bf16 v[176:179], v[60:63], v[216:219], v[176:179]\n\t"
        "ds_read_b128 v[208:211], %[xa] offset:14336\n\t"
        "v_mfma_f32_16x16x32_bf16 v[184:187], v[60:63], v[220:223], v[184:187]\n\t"
        "ds_read_b128 v[212:215], %[xa] offset:34816\n\t"
        "v_mfma_f32_16x16x32_bf16 v[180:183], v[140:143], v[216:219], v[180:183]\n\t"
        "v_mfma_f32_16x16x32_bf16 v[188:191], v[140:143], v[220:223], v[188:191]\n\t"
        "s_waitcnt lgkmcnt(4)\n\t"
        "v_mfma_f32_16x16x32_bf16 v[176:179], v[64:67], v[192:195], v[176:179]\n\t"
        "ds_read_b128 v[216:219], %[xa] offset:15360\n\t"
        "v_mfma_f32_16x16x32_bf16 v[184:187], v[64:67], v[196:199], v[184:187]\n\t"
        "ds_read_b128 v[220:223], %[xa] offset:35840\n\t"
        "v_mfma_f32_16x16x32_bf16 v[180:183], v[144:147], v[192:195], v[180:183]\n\t"
        "v_mfma_f32_16x16x32_bf16 v[188:191], v[144:147], v[196:199], v[188:191]\n\t"
        "s_waitcnt lgkmcnt(4)\n\t"
        "v_mfma_f32_16x16x32_bf16 v[176:179], v[68:71], v[200:203], v[176:179]\n\t"
        "ds_read_b128 v[192:195], %[xa] offset:16384\n\t"
        "v_mfma_f32_16x16x32_bf16 v[184:187], v[68:71], v[204:207], v[184:187]\n\t"
        "ds_read_b128 v[196:199], %[xa] offset:36864\n\t"
        "v_mfma_f32_16x16x32_bf16 v[180:183], v[148:151], v[200:203], v[180:183]\n\t"
        "v_mfma_f32_16x16x32_bf16 v[188:191], v[148:151], v[204:207], v[188:191]\n\t"
        "s_waitcnt lgkmcnt(4)\n\t"
        "v_mfma_f32_16x16x32_bf16 v[176:179], v[72:75], v[208:211], v[176:179]\n\t"
        "ds_read_b128 v[200:203], %[xa] offset:17408\n\t"
        "v_mfma_f32_16x16x32_bf16 v[184:187], v[72:75], v[212:215], v[184:187]\n\t"
        "ds_read_b128 v[204:207], %[xa] offset:37888\n\t"
        "v_mfma_f32_16x16x32_bf16 v[180:183], v[152:155], v[208:211], v[180:183]\n\t"
        "v_mfma_f32_16x16x32_bf16 v[188:191], v[152:155], v[212:215], v[188:191]\n\t"
        "s_waitcnt lgkmcnt(4)\n\t"
        "v_mfma_f32_16x16x32_bf16 v[176:179], v[76:79], v[216:219], v[176:179]\n\t"
        "ds_read_b128 v[208:211], %[xa] offset:18432\n\t"
        "v_mfma_f32_16x16x32_bf16 v[184:187], v[76:79], v[220:223], v[184:187]\n\t"
        "ds_read_b128 v[212:215], %[xa] offset:38912\n\t"
        "v_mfma_f32_16x16x32_bf16 v[180:183], v[156:159], v[216:219], v[180:183]\n\t"
        "v_mfma_f32_16x16x32_bf16 v[188:191], v[156:159], v[220:223], v[188:191]\n\t"
        "s_waitcnt lgkmcnt(4)\n\t"
        "v_mfma_f32_16x16x32_bf16 v[176:179], v[80:83], v[192:195], v[176:179]\n\t"
        "ds_read_b128 v[216:219], %[xa] offset:19456\n\t"
        "v_mfma_f32_16x16x32_bf16 v[184:187], v[80:83], v[196:199], v[184:187]\n\t"
        "ds_read_b128 v[220:223], %[xa] offset:39936\n\t"
        "v_mfma_f32_16x16x32_bf16 v[180:183], v[160:163], v[192:195], v[180:183]\n\t"
        "v_mfma_f32_16x16x32_bf16 v[188:191], v[160:163], v[196:199], v[188:191]\n\t"
        "s_waitcnt lgkmcnt(4)\n\t"
        "v_mfma_f32_16x16x32_bf16 v[176:179], v[84:87], v[200:203], v[176:179]\n\t"
        "v_mfma_f32_16x16x32_bf16 v[184:187], v[84:87], v[204:207], v[184:187]\n\t"
        "v_mfma_f32_16x16x32_bf16 v[180:183], v[164:167], v[200:203], v[180:183]\n\t"
        "v_mfma_f32_16x16x32_bf16 v[188:191], v[164:167], v[204:207], v[188:191]\n\t"
        "s_waitcnt lgkmcnt(2)\n\t"
        "v_mfma_f32_16x16x32_bf16 v[176:179], v[88:91], v[208:211], v[176:179]\n\t"
        "v_mfma_f32_16x16x32_bf16 v[184:187], v[88:91], v[212:215], v[184:187]\n\t"
        "v_mfma_f32_16x16x32_bf16 v[180:183], v[168:171], v[208:211], v[180:183]\n\t"
        "v_mfma_f32_16x16x32_bf16 v[188:191], v[168:171], v[212:215], v[188:191]\n\t"
        "s_waitcnt lgkmcnt(0)\n\t"
        "v_mfma_f32_16x16x32_bf16 v[176:179], v[92:95], v[216:219], v[176:179]\n\t"
        "v_mfma_f32_16x16x32_bf16 v[184:187], v[92:95], v[220:223], v[184:187]\n\t"
        "v_mfma_f32_16x16x32_bf16 v[180:183], v[172:175], v[216:219], v[180:183]\n\t"
        "v_mfma_f32_16x16x32_bf16 v[188:191], v[172:175], v[220:223], v[188:191]\n\t"
        "s_nop 0\n\t"
        "s_barrier\n\t"
        "s_nop 4\n\t"
        "v_mul_f32 v176, %[sc], v176\n\t"
        "v_mul_f32 v177, %[sc], v177\n\t"
        "v_mul_f32 v178, %[sc], v178\n\t"
        "v_mul_f32 v179, %[sc], v179\n\t"
        "v_mul_f32 v180, %[sc], v180\n\t"
        "v_mul_f32 v181, %[sc], v181\n\t"
        "v_mul_f32 v182, %[sc], v182\n\t"
        "v_mul_f32 v183, %[sc], v183\n\t"
        "v_cvt_pk_bf16_f32 v224, v176, v177\n\t"
        "v_cvt_pk_bf16_f32 v225, v178, v179\n\t"
        "v_cvt_pk_bf16_f32 v226, v180, v181\n\t"
        "v_cvt_pk_bf16_f32 v227, v182, v183\n\t"
        "buffer_store_dwordx4 v[224:227], %[oa], %[rs], %[o0] offen sc1\n\t"
        "v_mul_f32 v184, %[sc], v184\n\t"
        "v_mul_f32 v185, %[sc], v185\n\t"
        "v_mul_f32 v186, %[sc], v186\n\t"
        "v_mul_f32 v187, %[sc], v187\n\t"
        "v_mul_f32 v188, %[sc], v188\n\t"
        "v_mul_f32 v189, %[sc], v189\n\t"
        "v_mul_f32 v190, %[sc], v190\n\t"
        "v_mul_f32 v191, %[sc], v191\n\t"
        "v_cvt_pk_bf16_f32 v228, v184, v185\n\t"
        "v_cvt_pk_bf16_f32 v229, v186, v187\n\t"
        "v_cvt_pk_bf16_f32 v230, v188, v189\n\t"
        "v_cvt_pk_bf16_f32 v231, v190, v191\n\t"
        "buffer_store_dwordx4 v[228:231], %[oa], %[rs], %[o1] offen sc1\n\t"
        "s_nop 1\n\t"
        :
        : [w0] "{v[16:47]}"(W[0]), [w1] "{v[48:79]}"(W[1]), [w2] "{v[80:111]}"(W[2]), [w3] "{v[112:143]}"(W[3]), [w4] "{v[144:175]}"(W[4]), [xa] "v"(xa), [oa] "v"(oa), [rs] "s"(rs), [o0] "s"(o0), [o1] "s"(o1), [sc] "s"(sc), [ca] "s"(ca), [fc] "s"(fc), [fw] "s"(fw), [fo] "s"(fo), [xv] "v"(xv), [xs] "s"(xs), [xd] "s"(xd)
        : "s88", "s89", "v176", "v177", "v178", "v179", "v180", "v181", "v182", "v183", "v184", "v185", "v186", "v187", "v188", "v189", "v190", "v191", "v192", "v193", "v194", "v195", "v196", "v197", "v198", "v199", "v200", "v201", "v202", "v203", "v204", "v205", "v206", "v207", "v208", "v209", "v210", "v211", "v212", "v213", "v214", "v215", "v216", "v217", "v218", "v219", "v220", "v221", "v222", "v223", "v224", "v225", "v226", "v227", "v228", "v229", "v230", "v231", "v232", "v233", "v234", "vcc", "scc", "memory");
}

__device__ __forceinline__ void wb(const u32x32 (&W)[5], u32 xa, u32 oa, u32x4 rs, u32 bl, u32 wp, u32 it, u32 xv, u32 xs, u32 xd, u32 ys, u32 yd, u32 ca, u32 fc, u32 fw, u32 fo STP) {
    asm volatile(
        "ds_read_b128 v[192:195], %[xa] offset:0\n\t"
        "ds_read_b128 v[196:199], %[xa] offset:1024\n\t"
        "ds_read_b128 v[200:203], %[xa] offset:2048\n\t"
        "ds_read_b128 v[204:207], %[xa] offset:3072\n\t"
        "ds_read_b128 v[208:211], %[xa] offset:4096\n\t"
        "ds_read_b128 v[212:215], %[xa] offset:5120\n\t"
        "ds_read_b128 v[216:219], %[xa] offset:6144\n\t"
        "ds_read_b128 v[220:223], %[xa] offset:7168\n\t"
        "v_mov_b32 v232, %[ca]\n\t"
        "v_mov_b32 v233, 1\n\t"
        "v_mov_b32 v234, %[fw]\n\t"
        "s_andn2_b32 s91, %[wp], 15\n\t"
        "s_mov_b32 s92, %[bl]\n\t"
        "s_mov_b32 s88, m0\n\t"
        "s_mov_b32 m0, %[xd]\n\t"
        "s_add_u32 s89, %[xs], 0x1000\n\t"
        "buffer_load_dwordx4 %[xv], %[rs], %[xs] offen offset:0 sc1 lds\n\t"
        "buffer_load_dwordx4 %[xv], %[rs], %[xs] offen offset:1024 sc1 lds\n\t"
        "s_mov_b32 m0, s88\n\t"
        "s_setprio 1\n\t"
        "s_waitcnt lgkmcnt(0)\n\t"
        "v_mfma_f32_16x16x32_bf16 v[176:179], v[192:195], v[16:19], 0\n\t"
        "s_lshr_b32 s95, s92, 20\n\t"
        "s_xnor_b32 s95, s95, %[wp]\n\t"
        "v_mfma_f32_16x16x32_bf16 v[180:183], v[208:211], v[16:19], 0\n\t"
        "s_and_b32 s95, s95, 1\n\t"
        "s_lshl_b32 s95, s95, 13\n\t"
        "v_mfma_f32_16x16x32_bf16 v[176:179], v[196:199], v[20:23], v[176:179]\n\t"
        "v_mfma_f32_16x16x32_bf16 v[180:183], v[212:215], v[20:23], v[180:183]\n\t"
        "v_mfma_f32_16x16x32_bf16 v[176:179], v[200:203], v[24:27], v[176:179]\n\t"
        "v_mfma_f32_16x16x32_bf16 v[180:183], v[216:219], v[24:27], v[180:183]\n\t"
        "v_mfma_f32_16x16x32_bf16 v[176:179], v[204:207], v[28:31], v[176:179]\n\t"
        "v_mfma_f32_16x16x32_bf16 v[180:183], v[220:223], v[28:31], v[180:183]\n\t"
        "s_lshl1_add_u32 s93, s92, s91\n\t"
        "s_add_u32 s93, s93, s95\n\t"
        "v_mfma_f32_16x16x32_bf16 v[184:187], v[192:195], v[96:99], 0\n\t"
        "s_add_u32 s92, s92, 0x40000\n\t"
        "v_mfma_f32_16x16x32_bf16 v[188:191], v[208:211], v[96:99], 0\n\t"
        "s_add_u32 s94, s93, 0x10000\n\t"
        "v_mfma_f32_16x16x32_bf16 v[184:187], v[196:199], v[100:103], v[184:187]\n\t"
        "v_cvt_pk_bf16_f32 v224, v176, v177\n\t"
        "v_mfma_f32_16x16x32_bf16 v[188:191], v[212:215], v[100:103], v[188:191]\n\t"
        "v_cvt_pk_bf16_f32 v225, v178, v179\n\t"
        "v_mfma_f32_16x16x32_bf16 v[184:187], v[200:203], v[104:107], v[184:187]\n\t"
        "v_cvt_pk_bf16_f32 v226, v180, v181\n\t"
        "v_mfma_f32_16x16x32_bf16 v[188:191], v[216:219], v[104:107], v[188:191]\n\t"
        "v_cvt_pk_bf16_f32 v227, v182, v183\n\t"
        "v_mfma_f32_16x16x32_bf16 v[184:187], v[204:207], v[108:111], v[184:187]\n\t"
        "buffer_store_dwordx4 v[224:227], %[oa], %[rs], s93 offen sc1\n\t"
        "v_mfma_f32_16x16x32_bf16 v[188:191], v[220:223], v[108:111], v[188:191]\n\t"
        "v_mfma_f32_16x16x32_bf16 v[176:179], v[192:195], v[32:35], 0\n\t"
        "s_lshr_b32 s95, s92, 20\n\t"
        "s_xnor_b32 s95, s95, %[wp]\n\t"
        "v_mfma_f32_16x16x32_bf16 v[180:183], v[208:211], v[32:35], 0\n\t"
        "s_and_b32 s95, s95, 1\n\t"
        "s_lshl_b32 s95, s95, 13\n\t"
        "v_mfma_f32_16x16x32_bf16 v[176:179], v[196:199], v[36:39], v[176:179]\n\t"
        "v_cvt_pk_bf16_f32 v228, v184, v185\n\t"
        "v_mfma_f32_16x16x32_bf16 v[180:183], v[212:215], v[36:39], v[180:183]\n\t"
        "v_cvt_pk_bf16_f32 v229, v186, v187\n\t"
        "v_mfma_f32_16x16x32_bf16 v[176:179], v[200:203], v[40:43], v[176:179]\n\t"
        "v_cvt_pk_bf16_f32 v230, v188, v189\n\t"
        "v_mfma_f32_16x16x32_bf16 v[180:183], v[216:219], v[40:43], v[180:183]\n\t"
        "v_cvt_pk_bf16_f32 v231, v190, v191\n\t"
        "v_mfma_f32_16x16x32_bf16 v[176:179], v[204:207], v[44:47], v[176:179]\n\t"
        "buffer_store_dwordx4 v[228:231], %[oa], %[rs], s94 offen sc1\n\t"
        "v_mfma_f32_16x16x32_bf16 v[180:183], v[220:223], v[44:47], v[180:183]\n\t"
        "s_lshl1_add_u32 s93, s92, s91\n\t"
        "s_add_u32 s93, s93, s95\n\t"
        "v_mfma_f32_16x16x32_bf16 v[184:187], v[192:195], v[112:115], 0\n\t"
        "s_add_u32 s92, s92, 0x40000\n\t"
        "v_mfma_f32_16x16x32_bf16 v[188:191], v[208:211], v[112:115], 0\n\t"
        "s_add_u32 s94, s93, 0x10000\n\t"
        "v_mfma_f32_16x16x32_bf16 v[184:187], v[196:199], v[116:119], v[184:187]\n\t"
        "v_cvt_pk_bf16_f32 v224, v176, v177\n\t"
        "v_mfma_f32_16x16x32_bf16 v[188:191], v[212:215], v[116:119], v[188:191]\n\t"
        "v_cvt_pk_bf16_f32 v225, v178, v179\n\t"
        "v_mfma_f32_16x16x32_bf16 v[184:187], v[200:203], v[120:123], v[184:187]\n\t"
        "v_cvt_pk_bf16_f32 v226, v180, v181\n\t"
        "v_mfma_f32_16x16x32_bf16 v[188:191], v[216:219], v[120:123], v[188:191]\n\t"
        "v_cvt_pk_bf16_f32 v227, v182, v183\n\t"
        "v_mfma_f32_16x16x32_bf16 v[184:187], v[204:207], v[124:127], v[184:187]\n\t"
        "buffer_store_dwordx4 v[224:227], %[oa], %[rs], s93 offen sc1\n\t"
        "v_mfma_f32_16x16x32_bf16 v[188:191], v[220:223], v[124:127], v[188:191]\n\t"
        "s_waitcnt vmcnt(5)\n\t"
        "s_mov_b64 exec, 1\n\t"
        "ds_add_rtn_u32 v233, v232, v233\n\t"
        "s_mov_b64 exec, -1\n\t"
        "v_mfma_f32_16x16x32_bf16 v[176:179], v[192:195], v[48:51], 0\n\t"
        "s_lshr_b32 s95, s92, 20\n\t"
        "s_xnor_b32 s95, s95, %[wp]\n\t"
        "v_mfma_f32_16x16x32_bf16 v[180:183], v[208:211], v[48:51], 0\n\t"
        "s_and_b32 s95, s95, 1\n\t"
        "s_lshl_b32 s95, s95, 13\n\t"
        "v_mfma_f32_16x16x32_bf16 v[176:179], v[196:199], v[52:55], v[176:179]\n\t"
        "v_cvt_pk_bf16_f32 v228, v184, v185\n\t"
        "v_mfma_f32_16x16x32_bf16 v[180:183], v[212:215], v[52:55], v[180:183]\n\t"
        "v_cvt_pk_bf16_f32 v229, v186, v187\n\t"
        "v_mfma_f32_16x16x32_bf16 v[176:179], v[200:203], v[56:59], v[176:179]\n\t"
        "v_cvt_pk_bf16_f32 v230, v188, v189\n\t"
        "v_mfma_f32_16x16x32_bf16 v[180:183], v[216:219], v[56:59], v[180:183]\n\t"
        "v_cvt_pk_bf16_f32 v231, v190, v191\n\t"
        "v_mfma_f32_16x16x32_bf16 v[176:179], v[204:207], v[60:63], v[176:179]\n\t"
        "buffer_store_dwordx4 v[228:231], %[oa], %[rs], s94 offen sc1\n\t"
        "v_mfma_f32_16x16x32_bf16 v[180:183], v[220:223], v[60:63], v[180:183]\n\t"
        "s_lshl1_add_u32 s93, s92, s91\n\t"
        "s_add_u32 s93, s93, s95\n\t"
        "v_mfma_f32_16x16x32_bf16 v[184:187], v[192:195], v[128:131], 0\n\t"
        "s_add_u32 s92, s92, 0x40000\n\t"
        "v_mfma_f32_16x16x32_bf16 v[188:191], v[208:211], v[128:131], 0\n\t"
        "s_add_u32 s94, s93, 0x10000\n\t"
        "v_mfma_f32_16x16x32_bf16 v[184:187], v[196:199], v[132:135], v[184:187]\n\t"
        "v_cvt_pk_bf16_f32 v224, v176, v177\n\t"
        "v_mfma_f32_16x16x32_bf16 v[188:191], v[212:215], v[132:135], v[188:191]\n\t"
        "v_cvt_pk_bf16_f32 v225, v178, v179\n\t"
        "v_mfma_f32_16x16x32_bf16 v[184:187], v[200:203], v[136:139], v[184:187]\n\t"
        "v_cvt_pk_bf16_f32 v226, v180, v181\n\t"
        "v_mfma_f32_16x16x32_bf16 v[188:191], v[216:219], v[136:139], v[188:191]\n\t"
        "v_cvt_pk_bf16_f32 v227, v182, v183\n\t"
        "v_mfma_f32_16x16x32_bf16 v[184:187], v[204:207], v[140:143], v[184:187]\n\t"
        "buffer_store_dwordx4 v[224:227], %[oa], %[rs], s93 offen sc1\n\t"
        "v_mfma_f32_16x16x32_bf16 v[188:191], v[220:223], v[140:143], v[188:191]\n\t"
        "s_waitcnt lgkmcnt(0)\n\t"
        "s_mov_b64 exec, 1\n\t"
        "v_cmpx_eq_u32 vcc, %[fc], v233\n\t"
        "buffer_store_dword v234, off, %[rs], %[fo] sc1\n\t"
        "s_mov_b64 exec, -1\n\t"
        "s_cmp_le_u32 %[it], %[nowv]\n\t"
        "s_cbranch_scc1 Lp%=\n\t"
        "s_setprio 0\n\t"
        "Lp%=:\n\t"
        "s_mov_b32 s88, m0\n\t"
        "s_mov_b32 m0, %[yd]\n\t"
        "s_add_u32 s89, %[ys], 0x1000\n\t"
        "buffer_load_dwordx4 %[xv], %[rs], %[ys] offen offset:0 sc1 lds\n\t"
        "buffer_load_dwordx4 %[xv], %[rs], %[ys] offen offset:1024 sc1 lds\n\t"
        "buffer_load_dwordx4 %[xv], %[rs], %[ys] offen offset:2048 sc1 lds\n\t"
        "buffer_load_dwordx4 %[xv], %[rs], %[ys] offen offset:3072 sc1 lds\n\t"
        "s_add_u32 m0, m0, 0x1000\n\t"
        "s_nop 0\n\t"
        "buffer_load_dwordx4 %[xv], %[rs], s89 offen offset:0 sc1 lds\n\t"
        "buffer_load_dwordx4 %[xv], %[rs], s89 offen offset:1024 sc1 lds\n\t"
        "buffer_load_dwordx4 %[xv], %[rs], s89 offen offset:2048 sc1 lds\n\t"
        "buffer_load_dwordx4 %[xv], %[rs], s89 offen offset:3072 sc1 lds\n\t"
        "s_mov_b32 m0, s88\n\t"
        "v_mfma_f32_16x16x32_bf16 v[176:179], v[192:195], v[64:67], 0\n\t"
        "s_lshr_b32 s95, s92, 20\n\t"
        "s_xnor_b32 s95, s95, %[wp]\n\t"
        "v_mfma_f32_16x16x32_bf16 v[180:183], v[208:211], v[64:67], 0\n\t"
        "s_and_b32 s95, s95, 1\n\t"
        "s_lshl_b32 s95, s95, 13\n\t"
        "v_mfma_f32_16x16x32_bf16 v[176:179], v[196:199], v[68:71], v[176:179]\n\t"
        "v_cvt_pk_bf16_f32 v228, v184, v185\n\t"
        "v_mfma_f32_16x16x32_bf16 v[180:183], v[212:215], v[68:71], v[180:183]\n\t"
        "v_cvt_pk_bf16_f32 v229, v186, v187\n\t"
        "v_mfma_f32_16x16x32_bf16 v[176:179], v[200:203], v[72:75], v[176:179]\n\t"
        "v_cvt_pk_bf16_f32 v230, v188, v189\n\t"
        "v_mfma_f32_16x16x32_bf16 v[180:183], v[216:219], v[72:75], v[180:183]\n\t"
        "v_cvt_pk_bf16_f32 v231, v190, v191\n\t"
        "v_mfma_f32_16x16x32_bf16 v[176:179], v[204:207], v[76:79], v[176:179]\n\t"
        "buffer_store_dwordx4 v[228:231], %[oa], %[rs], s94 offen sc1\n\t"
        "v_mfma_f32_16x16x32_bf16 v[180:183], v[220:223], v[76:79], v[180:183]\n\t"
        "s_lshl1_add_u32 s93, s92, s91\n\t"
        "s_add_u32 s93, s93, s95\n\t"
        "v_mfma_f32_16x16x32_bf16 v[184:187], v[192:195], v[144:147], 0\n\t"
        "s_add_u32 s92, s92, 0x40000\n\t"
        "v_mfma_f32_16x16x32_bf16 v[188:191], v[208:211], v[144:147], 0\n\t"
        "s_add_u32 s94, s93, 0x10000\n\t"
        "v_mfma_f32_16x16x32_bf16 v[184:187], v[196:199], v[148:151], v[184:187]\n\t"
        "v_cvt_pk_bf16_f32 v224, v176, v177\n\t"
        "v_mfma_f32_16x16x32_bf16 v[188:191], v[212:215], v[148:151], v[188:191]\n\t"
        "v_cvt_pk_bf16_f32 v225, v178, v179\n\t"
        "v_mfma_f32_16x16x32_bf16 v[184:187], v[200:203], v[152:155], v[184:187]\n\t"
        "v_cvt_pk_bf16_f32 v226, v180, v181\n\t"
        "v_mfma_f32_16x16x32_bf16 v[188:191], v[216:219], v[152:155], v[188:191]\n\t"
        "v_cvt_pk_bf16_f32 v227, v182, v183\n\t"
        "v_mfma_f32_16x16x32_bf16 v[184:187], v[204:207], v[156:159], v[184:187]\n\t"
        "buffer_store_dwordx4 v[224:227], %[oa], %[rs], s93 offen sc1\n\t"
        "v_mfma_f32_16x16x32_bf16 v[188:191], v[220:223], v[156:159], v[188:191]\n\t"
        "v_mfma_f32_16x16x32_bf16 v[176:179], v[192:195], v[80:83], 0\n\t"
        "s_lshr_b32 s95, s92, 20\n\t"
        "s_xnor_b32 s95, s95, %[wp]\n\t"
        "v_mfma_f32_16x16x32_bf16 v[180:183], v[208:211], v[80:83], 0\n\t"
        "s_and_b32 s95, s95, 1\n\t"
        "s_lshl_b32 s95, s95, 13\n\t"
        "v_mfma_f32_16x16x32_bf16 v[176:179], v[196:199], v[84:87], v[176:179]\n\t"
        "v_cvt_pk_bf16_f32 v228, v184, v185\n\t"
        "v_mfma_f32_16x16x32_bf16 v[180:183], v[212:215], v[84:87], v[180:183]\n\t"
        "v_cvt_pk_bf16_f32 v229, v186, v187\n\t"
        "v_mfma_f32_16x16x32_bf16 v[176:179], v[200:203], v[88:91], v[176:179]\n\t"
        "v_cvt_pk_bf16_f32 v230, v188, v189\n\t"
        "v_mfma_f32_16x16x32_bf16 v[180:183], v[216:219], v[88:91], v[180:183]\n\t"
        "v_cvt_pk_bf16_f32 v231, v190, v191\n\t"
        "v_mfma_f32_16x16x32_bf16 v[176:179], v[204:207], v[92:95], v[176:179]\n\t"
        "buffer_store_dwordx4 v[228:231], %[oa], %[rs], s94 offen sc1\n\t"
        "v_mfma_f32_16x16x32_bf16 v[180:183], v[220:223], v[92:95], v[180:183]\n\t"
        "s_lshl1_add_u32 s93, s92, s91\n\t"
        "s_add_u32 s93, s93, s95\n\t"
        "v_mfma_f32_16x16x32_bf16 v[184:187], v[192:195], v[160:163], 0\n\t"
        "s_add_u32 s92, s92, 0x40000\n\t"
        "v_mfma_f32_16x16x32_bf16 v[188:191], v[208:211], v[160:163], 0\n\t"
        "s_add_u32 s94, s93, 0x10000\n\t"
        "v_mfma_f32_16x16x32_bf16 v[184:187], v[196:199], v[164:167], v[184:187]\n\t"
        "v_cvt_pk_bf16_f32 v224, v176, v177\n\t"
        "v_mfma_f32_16x16x32_bf16 v[188:191], v[212:215], v[164:167], v[188:191]\n\t"
        "v_cvt_pk_bf16_f32 v225, v178, v179\n\t"
        "v_mfma_f32_16x16x32_bf16 v[184:187], v[200:203], v[168:171], v[184:187]\n\t"
        "v_cvt_pk_bf16_f32 v226, v180, v181\n\t"
        "v_mfma_f32_16x16x32_bf16 v[188:191], v[216:219], v[168:171], v[188:191]\n\t"
        "v_cvt_pk_bf16_f32 v227, v182, v183\n\t"
        "v_mfma_f32_16x16x32_bf16 v[184:187], v[204:207], v[172:175], v[184:187]\n\t"
        "buffer_store_dwordx4 v[224:227], %[oa], %[rs], s93 offen sc1\n\t"
        "v_mfma_f32_16x16x32_bf16 v[188:191], v[220:223], v[172:175], v[188:191]\n\t"
        "s_and_b32 s95, %[it], 31\n\t"
        "s_cmp_le_u32 s95, %[nowv]\n\t"
        "s_cbranch_scc1 Lnb%=\n\t"
        "s_waitcnt vmcnt(17)\n\t"
        "s_barrier\n\t"
        "Lnb%=:\n\t"
        "s_setprio 0\n\t"
        "s_nop 3\n\t"
        "v_cvt_pk_bf16_f32 v228, v184, v185\n\t"
        "v_cvt_pk_bf16_f32 v229, v186, v187\n\t"
        "v_cvt_pk_bf16_f32 v230, v188, v189\n\t"
        "v_cvt_pk_bf16_f32 v231, v190, v191\n\t"
        "buffer_store_dwordx4 v[228:231], %[oa], %[rs], s94 offen sc1\n\t"
        "s_nop 1\n\t"
        :
        : [w0] "{v[16:47]}"(W[0]), [w1] "{v[48:79]}"(W[1]), [w2] "{v[80:111]}"(W[2]), [w3] "{v[112:143]}"(W[3]), [w4] "{v[144:175]}"(W[4]), [xa] "v"(xa), [oa] "v"(oa), [rs] "s"(rs), [bl] "s"(bl), [wp] "s"(wp), [it] "s"(it), [nowv] "n"(NOWV), [ca] "s"(ca), [fc] "s"(fc), [fw] "s"(fw), [fo] "s"(fo), [xv] "v"(xv), [xs] "s"(xs), [xd] "s"(xd), [ys] "s"(ys), [yd] "s"(yd)
        : "s88", "s89", "s91", "s92", "s93", "s94", "s95", "v176", "v177", "v178", "v179", "v180", "v181", "v182", "v183", "v184", "v185", "v186", "v187", "v188", "v189", "v190", "v191", "v192", "v193", "v194", "v195", "v196", "v197", "v198", "v199", "v200", "v201", "v202", "v203", "v204", "v205", "v206", "v207", "v208", "v209", "v210", "v211", "v212", "v213", "v214", "v215", "v216", "v217", "v218", "v219", "v220", "v221", "v222", "v223", "v224", "v225", "v226", "v227", "v228", "v229", "v230", "v231", "v232", "v233", "v234", "vcc", "scc", "memory");
}

#define TP0 186
#define TP1 18
#define TP2 184
#define TP3 30
#define TP4 150
#define TP5 130
#define TP6 104
#define TP7 240
#define TP8 8
#define TP9 14
#define TP10 3
#define TP11 2
#define TP12 1
#define TP13 150
#define TP14 110
#define TP15 4
#define WAHEAD 3
#define FILLG 4
#define WFIRST 2
#define ROT xg
#define SLOW_RT 620u
#define WLATE 600u
#define EARLY_N 35
#define BUMP_N 3
#define BUMP_WIN 0xffu
#define BUMP_S0 8
#define BUMP_MAX 3
#define TV(idx) ((u32)(TP##idx) * 8u)
#define TLOAD() do { } while (0)

__device__ __forceinline__ void nap(u32 n) {
    u32 hi = n >> 6, lo = (n >> 4) & 3u;
    asm volatile("s_cmp_eq_u32 %0, 0\n\ts_cbranch_scc1 Ln%=\nLm%=:\n\ts_sleep 1\n\ts_sub_u32 %0, %0, 1\n\ts_cmp_lg_u32 %0, 0\n\ts_cbranch_scc1 Lm%=\nLn%=:\n\t"
                 "s_cmp_eq_u32 %1, 0\n\ts_cbranch_scc1 Lp%=\nLo%=:\n\ts_sub_u32 %1, %1, 1\n\ts_cmp_lg_u32 %1, 0\n\ts_cbranch_scc1 Lo%=\nLp%=:"
                 : "+s"(hi), "+s"(lo) :: "scc", "memory");
}
typedef __attribute__((address_space(3))) u32* lds_u32p;
typedef __attribute__((address_space(3))) volatile u32* lds_vu32p;
typedef __attribute__((address_space(3))) u16* lds_u16p;
typedef __attribute__((address_space(3))) u32x2* lds_u32x2p;
typedef __attribute__((address_space(3))) u32x4* lds_u32x4p;
__device__ __forceinline__ u32 rfl(u32 x) { return __builtin_amdgcn_readfirstlane(x); }
__device__ __forceinline__ bool has_sent(u32x4 v) { return (v[0] == SENT) | (v[1] == SENT) | (v[2] == SENT) | (v[3] == SENT); }
__device__ __forceinline__ u32 now32() { return (u32)__builtin_amdgcn_s_memtime(); }
__device__ __forceinline__ u32 step_tag(int s) { return (u32)(s + 1) * 0x10001u; }

#define UNI(a, b, c, d, e, f, g, h, i, j) do { } while (0)
template <int S0 = 0>
__device__ __forceinline__ u32 unless_neg(u32 v, int s) {
    u32 r;
    asm("s_cmp_lt_i32 %2, %3\n\ts_cselect_b32 %0, 0, %1" : "=s"(r) : "s"(v), "s"(s), "n"(S0) : "scc");
    return r;
}
template <int S0 = 0>
__device__ __forceinline__ u32x4 live(u32x4 srd, int s) { srd[2] = unless_neg<S0>(srd[2], s); return srd; }
__device__ __forceinline__ float shx(float v, int m, int lane) { return __int_as_float(__builtin_amdgcn_ds_bpermute((lane ^ m) << 2, __float_as_int(v))); }
__device__ __forceinline__ void mem0_image(const u16* __restrict__ mem0, char* smem, const int tid) {
    for (int c = tid; c < 640; c += 512) {
        u32x4 v = *(const u32x4*)(mem0 + (c & 7) * D_ + (c >> 3) * 8);
#pragma unroll
        for (int i = 0; i < 4; ++i) v[i] = v[i] == SENT ? ~0u : v[i];
        ((u32x4*)smem)[c + (c >= 576 ? 64 : 0)] = v;
    }
}
__device__ __forceinline__ u32 once_more(u32& left, u32 lbase, u32* errw) {
    if (__builtin_expect(left != 0, 1)) { --left; return 2u; }
    atomicOr(errw, 1u);
    const int lane = lane_id();
    for (int k = 0; k < 11; ++k) *(lds_u32x4p)(lbase + (u32)(k * 1024 + lane * 16)) = (u32x4){0u, 0u, 0u, 0u};
    asm volatile("s_waitcnt lgkmcnt(0)" ::: "memory");
    return 0u;
}
constexpr u32 TS_MISS = 0x10000u;
__device__ __forceinline__ u32 read_ts(rsrc_t rs, u32 off, int s, u32& m, u32* t2 = nullptr) {
    const int lane = lane_id();
    const u32 vo = lane < 8 ? (u32)lane * 128u : OOB, base = (u32)__builtin_amdgcn_s_memrealtime() * 24u - 0x100000u;
    u32x4 t;
#pragma unroll 1
    for (u32 spins = 1;; ++spins) {
        CFENCE();
        t = ldx4(rs, vo, off);
        m = (u32)__ballot(t[3] == (u32)s + 1u);
        if (((m | (m >> 4)) & 15u) == 15u) break;
        m = 0;
        if (spins >= 0x4000u) break;
        __builtin_amdgcn_s_sleep(4);
    }
    u32 v = t[3] == (u32)s + 1u ? t[1] - base : 0u;
    v = max(v, (u32)__builtin_amdgcn_update_dpp(0u, v, 0xb1, 0xf, 0xf, false));
    v = max(v, (u32)__builtin_amdgcn_update_dpp(0u, v, 0x4e, 0xf, 0xf, false));
    v = max(v, (u32)__builtin_amdgcn_update_dpp(0u, v, 0x141, 0xf, 0xf, false));
    if (t2) {
        u32 w = t[3] == (u32)s + 1u ? t[2] : 0u;
        w = max(w, (u32)__builtin_amdgcn_update_dpp(0u, w, 0xb1, 0xf, 0xf, false));
        w = max(w, (u32)__builtin_amdgcn_update_dpp(0u, w, 0x4e, 0xf, 0xf, false));
        w = max(w, (u32)__builtin_amdgcn_update_dpp(0u, w, 0x141, 0xf, 0xf, false));
        *t2 = to_sgpr(w);
    }
    return to_sgpr(v + base);
}
__device__ __forceinline__ u32 read_ts_late(rsrc_t rs, u32x4 srd, u32 off, int s, u32& m, u32& t2, u32 gap = 1024) {
    u32 t;
    ts_look(t, t2, m, srd, off, (u32)s + 1u);
    if (m) return t;
    nap(gap);
    return read_ts(rs, off, s, m, &t2);
}
__device__ __forceinline__ void until(u32 t) { for (int q = 0; q < 256 && (int)(t - (u32)__builtin_amdgcn_s_memrealtime() * 24u) > 0; ++q) __builtin_amdgcn_s_sleep(1); }
__device__ __forceinline__ u32 umin3(u32 a, u32 b, u32 c) { return min(min(a, b), c); }
__device__ __forceinline__ u32 dist(u32 d, u32x4 v, u32 k) { return umin3(umin3(d, v[0] ^ k, v[1] ^ k), v[2] ^ k, v[3] ^ k); }
#define COUNT(i) do { } while (0)
#define COUNT0() do { } while (0)
#define COUNT1(i) do { } while (0)
__device__ __forceinline__ void touch(u32x4 srd, u32 wp, int s, u32 lds) {
    u32 sv;
    const u32 off = slot_off(wp, s) + SL_TS + 2048u + ((~((wp >> 3) ^ ((u32)s >> 5)) & 1u) << 18);
    asm volatile("s_mov_b32 %0, m0\n\ts_mov_b32 m0, %3\n\ts_nop 3\n\tbuffer_load_dword off, %1, %2 lds\n\ts_mov_b32 m0, %0" : "=&s"(sv) : "s"(srd), "s"(off), "s"(lds) : "memory");
}
__device__ __forceinline__ void track_rate(u32 c0, u32 r0, u32& pc, u32& pr, u32& kr) {
    const u32 dr = r0 - pr;
    if (dr >= 6000u) {
        if (dr < 40000u) kr = to_sgpr(min(max((u32)(4096.f * (float)(c0 - pc) * __builtin_amdgcn_rcpf((float)dr) + 0.5f), 2560u), 4352u));
        pc = c0; pr = r0;
    }
}

#define LANES() const int lane = lane_id(), n = lane & 15, g = lane >> 4; (void)n; (void)g
constexpr u32 L_Q = 12288, L_K = 18432, L_E = 24576, L_DR = 25088, L_MSK = 25600, L_KQ = 32768;
constexpr float LIM_X = 32.f, LIM_D = 1.0995116e12f;
__device__ __forceinline__ u32 kq_buf(int sg) { return L_KQ + (u32)(sg % 3) * 16384u; }

__device__ __forceinline__ u32 p_pos(int row, int g, int dg) { return (u32)((row >> 1) * 64 + (g & 1) * 32 + (row & 1) * 16 + dg * 8 + (g >> 1) * 4); }
__device__ __forceinline__ void s_slow(const int h, const int s, rsrc_t rs, u32 so, u32 lbase) {
    const int lane = lane_id(), row = lane & 7, j = lane >> 3;
    const float ce = 0.08838834764831845f * 1.4426950408889634f;
    float sc[5] = {0.f, 0.f, 0.f, 0.f, 0.f};
    const u32 kx = lbase + kq_buf(s) + (u32)((j >> 2) * 4096 + (j & 3) * 64);
#pragma unroll 1
    for (int e = 0; e < DH_; ++e) {
        const u32 qa = lbase + L_Q + (u32)((e >> 4) * 768 + (row >> 2) * 192 + (e & 15) * 8 + (row & 3) * 2);
        const float q = bf2f(*(lds_u16p)qa);
        const u32 ka = kx + (u32)((e >> 5) * 1024 + ((e >> 2) & 3) * 256 + ((e >> 4) & 1) * 8 + (e & 3) * 2);
#pragma unroll
        for (int i = 0; i < 4; ++i) sc[i] += q * bf2f(*(lds_u16p)(ka + i * 16));
        sc[4] += q * bf2f(*(lds_u16p)(lbase + L_K + (u32)((e >> 4) * 768 + (j >> 1) * 192 + (e & 15) * 8 + (j & 1) * 2)));
    }
    sc[4] *= ce;
    const u32x2 ev = *(lds_u32x2p)(lbase + L_E + (u32)((j >> 1) * 128 + row * 16 + (j & 1) * 8));
    float se = (bf2f((u16)ev[0]) + bf2f((u16)(ev[0] >> 16))) + (bf2f((u16)ev[1]) + bf2f((u16)(ev[1] >> 16)));
    float mx = fmaxf(fmaxf(fmaxf(sc[0], sc[1]), fmaxf(sc[2], sc[3])), sc[4]);
#pragma unroll
    for (int o = 8; o < 64; o <<= 1) { se += shx(se, o, lane); mx = fmaxf(mx, shx(mx, o, lane)); }
    const bool rew = ev[0] == ~0u || ev[1] == ~0u;
    u64 rw = __ballot(rew);
    rw |= rw >> 32; rw |= rw >> 16; rw |= rw >> 8;
    const bool again = (rw >> row) & 1u;
    float al = exp2f(-mx);
    if (again) {
        const u32x2 en = {pack2(exp2f(sc[0] - mx), exp2f(sc[1] - mx)), pack2(exp2f(sc[2] - mx), exp2f(sc[3] - mx))};
        se = (bf2f((u16)en[0]) + bf2f((u16)(en[0] >> 16))) + (bf2f((u16)en[1]) + bf2f((u16)(en[1] >> 16)));
        al = 1.f;
    }
    float sn = se;
#pragma unroll
    for (int o = 8; o < 64; o <<= 1) sn += shx(sn, o, lane);
    if (again) se = sn;
    const float em = exp2f(sc[4] - mx);
    float sm = em;
#pragma unroll
    for (int o = 8; o < 64; o <<= 1) sm += shx(sm, o, lane);
    const float rd = 1.f / (sm + al * se);
    if (rw & 255u) {
#pragma unroll 1
        for (u32 spins = 0; spins < SPIN_CAP && !__all(*(lds_vu32p)(lbase + L_DR + (u32)(lane & 1) * 4) == step_tag(s)); ++spins) __builtin_amdgcn_s_sleep(1);
        const u32x2 en = {pack2(exp2f(sc[0] - mx), exp2f(sc[1] - mx)), pack2(exp2f(sc[2] - mx), exp2f(sc[3] - mx))};
        const u32 eo = again ? (u32)((h >> 3) * 32768 + ((h >> 1) & 3) * 1024 + (j >> 1) * 256 + (h & 1) * 128 + row * 16 + (j & 1) * 8) : OOB;
#pragma unroll
        for (int c = 0; c < NCP; ++c) stx2(rs, eo, en, so + cpy(c));
    }
    DRAIN();
    const u32 pm = pack2(em * rd, shx(em * rd, 8, lane));
    const int g = j >> 1;
    u32 dg = pack2(row == 2 * g ? al * rd : 0.f, row == 2 * g + 1 ? al * rd : 0.f);
    if (again && (row >> 1) == (g ^ 1)) dg = MARK;
    const int gj = h >> 2;
    const u32 po = SL_P + (u32)((gj >> 2) * 32768 + (gj & 1) * 2048 + ((gj >> 1) & 1) * 1024 + (h & 3) * 256) + p_pos(row, g, (j ^ ~h) & 1);
    const bool done = ldx1(rs, po, so) != SENT;
#pragma unroll
    for (int c = 0; c < NCP; ++c) stx1(rs, done ? OOB : po, (j & 1) ? dg : pm, so + cpy(c));
}

__device__ __forceinline__ void s_role(const int wave, const int h, const int xg, const u64 tag, const u16* __restrict__ mem0, const u32x32 (&W)[5],
                                       char* __restrict__ ws, char* smem) {
    const int lane = lane_id(), tid = wave * 64 + lane, n = lane & 15, g = lane >> 4;
    u32* errw = (u32*)(ws + OFF_ERR);
    const rsrc_t rs = mk_rsrc(ws);
    const u32x4 srd = mk_srd(ws);
    const u32 lbase = (u32)(uintptr_t)(__attribute__((address_space(3))) char*)smem;
    TLOAD();
    int wslot = 0; const int wid = 128 + h;
    WSTAMP(2);
    mem0_image(mem0, smem, tid);
    if (tid < 448) *(u32x4*)(smem + L_K + tid * 16) = (u32x4){0u, 0u, 0u, 0u};
    const u32 wp = wait_init(rs, tag, 1);
    if (wave == 2) XSTAMP(wid, 224);
    const u32 sidx = (n & 2) ? 0xEEEE : 0x4444;
    const u32 va = lbase + (u32)(g * 256 + (n & 2) * 64 + (n >> 2) * 32 + (n & 1) * 16);
    const u32 ow = lbase + (u32)((wave & 3) * 768 + n * 8) + (wave >= 4 ? L_Q + (u32)((g >> 1) * 192 + (g & 1) * 4) : L_K + (u32)g * 192);
    u32 rnd = 0;
    const float ce = 0.08838834764831845f * 1.4426950408889634f;
    STAMPS();
    COUNT0();
#define BAR0() asm volatile("s_waitcnt vmcnt(0) lgkmcnt(0)\n\ts_barrier" ::: "memory")
#define Q_LAST() do { u32 redo; do s_l(rnd, redo, lbase); while (__builtin_expect(redo, 0)); } while (0)

    if ((wave >> 1) == 2) {
        LANES();
        const int kt = wave & 1;
        const u32 kxa = lbase + (u32)(kt * 4096 + lane * 16), qtr = lbase + L_Q + (u32)((lane & 1) * 192 + (lane >> 2) * 8);
        const u32 ep = (u32)((n & 7) * 16 + (g & 1) * 8 + (n >> 3) * 4);
        const u32 eo = (u32)((h >> 3) * 32768 + ((h >> 1) & 3) * 1024 + (2 * kt + (g >> 1)) * 256 + (h & 1) * 128) + ep;
        const u32 ew = lbase + L_E + (u32)((2 * kt + (g >> 1)) * 128) + ep;
        const u32 ts = slot_v(wp, lane) + put_off(wp, 2u, lane), tk = kq_buf(lane);
#pragma unroll 1
        for (int s = -1; s + 1 < NSEG; ++s) {
            s_e(W, rnd, va, sidx, ow, lbase, kxa + tab(tk, s & 63), qtr, (u32)kt * 768u, eo, ew, LIM_X, live(srd, s), tab(ts, s & 63) STA);
            DRAIN();
            *(lds_u32p)(lbase + L_DR + (u32)kt * 4) = step_tag(s);
            FLUSH();
        }
        BAR0();
        Q_LAST();
    } else if (wave == 6) {
        LANES();
        const int gj = h >> 2;
        const u32 po = SL_P + (u32)((gj >> 2) * 32768 + (gj & 1) * 2048 + ((gj >> 1) & 1) * 1024 + (h & 3) * 256) + p_pos(n & 7, g, ((n >> 3) ^ ~h) & 1);
        const u32 er = lbase + L_E + (u32)(g * 128 + (n & 7) * 16);
        const u32 qtr = lbase + L_Q + (u32)((lane & 1) * 192 + (lane >> 2) * 8), ktr = lbase + L_K + (u32)((lane & 3) * 192 + (lane >> 2) * 8);
        const u32x4 ones = {0x3f803f80u, 0x3f803f80u, 0x3f803f80u, 0x3f803f80u};
        const u32 ts = slot_v(wp, lane) + put_off(wp, 2u, lane);
#pragma unroll 1
        for (int s = -1; s + 1 < NSEG; ++s) {
            const u32 so = tab(ts, s & 63);
            u32 bad;
            s_f(W, rnd, va, sidx, ow, lbase, bad, ones, qtr, ktr, er, po, n < 8 ? 1.f : 0.f, n == 8 + 2 * g ? 1.f : 0.f, n == 9 + 2 * g ? 1.f : 0.f, live(srd, s), so, ce, LIM_D STA);
            if (__builtin_expect(bad && s >= 0, 0)) s_slow(h, s, rs, so, lbase);
            FLUSH();
        }
        BAR0();
        Q_LAST();
    } else if (wave == 7) {
        LANES();
        const u32 vm = (u32)lane * 16, cp = (u32)xg ^ (wp >> 1);
        u32 c0, r0, kr = 3950;
        calib(c0, r0);
        const u32 tc = slot_v(wp ^ 8u, lane) + copy_off(cp, lane);
        u32 tg = r0, so = 0, left = SPIN_CAP, s = ~0u, poll = 2, cnt = 0;
        LOGS();
        asm volatile("s_waitcnt lgkmcnt(0)" ::: "memory");
#pragma unroll 1
        while (s + 1 != NSEG) {
            u32 ok;
            s_m(W, rnd, va, sidx, ow, lbase, left, s, so, tg, kr, c0, r0, ok, cnt, vm, tc, lane < 8 ? vm * 8u : OOB, lbase + (poll & 2u) * 40960u, live(srd, (int)s), poll,
                NSEG - 1 STA LGA);
            UNI(rnd, left, s, so, tg, kr, c0, r0, ok, cnt);
            if (__builtin_expect(ok == 2u, 0)) { once_more(left, lbase, errw); poll = 0; continue; }
            if (s == 0) { BAR0(); poll = 0; continue; }
            poll = 1;
            if (!ok) tg = read_ts(rs, so + SL_TS, (int)s - 1, ok);
        }
        for (u32 poll = 1;;) {
            if (poll) gather(tg, kr, vm, so, lbase, srd);
            if (poll == 1) LBAR();
            DRAIN();
            u32 redo;
            s_l(rnd, redo, lbase);
            if (__builtin_expect(!redo, 1)) break;
            poll = once_more(left, lbase, errw);
        }
    } else if (wave < 2) {
        LANES();
        const int nt = wave & 1;
        const u32 ktr = lbase + L_K + (u32)((lane & 3) * 192 + (lane >> 2) * 8);
        f32x4* msk = (f32x4*)(smem + L_MSK) + nt * 128 + lane;
#pragma unroll
        for (int q = 0; q < 2; ++q) {
            f32x4 m;
#pragma unroll
            for (int i = 0; i < 4; ++i) m[i] = q * 16 + 4 * g + i <= nt * 16 + n ? 0.f : -__builtin_huge_valf();
            msk[q * 64] = m;
        }
        const u32 vl = lbase + (u32)lane * 16;
        const u32 o0 = (u32)OFF_PX + (u32)((h * 2 + nt) * 1024 + lane * 16);
        const u32 o1 = (u32)OFF_PXM + (u32)(((h >> 2) * 2 + nt) * 1024 + ((h & 3) * 16 + n) * 16 + g * 4);
        u32x4 p0 = {0u, 0u, 0u, 0u};
        u32 p1 = 0;
#pragma unroll 1
        for (int s = -1; s + 1 < NSEG; ++s) {
            const u32 kb = vl + kq_buf(s + 3);
            s_k1(W, rnd, va, sidx, ow, lbase, p0, p1, kb, kb + 8192 + (u32)nt * 4096, vl + L_MSK + (u32)nt * 2048, ktr, o0 + (u32)max(s - 1, 0) * 65536,
                 o1 + (u32)max(s - 1, 0) * 16384, ce, live<1>(srd, s) STA);
            rnd = to_sgpr(rnd);
            FLUSH();
        }
        s_k3(W, rnd, va, sidx, ow, lbase, p0, p1, vl + kq_buf(NSEG - 1), vl + kq_buf(NSEG - 1) + 8192 + (u32)nt * 4096, vl + L_MSK + (u32)nt * 2048, ktr,
             o0 + (NSEG - 2) * 65536, o1 + (NSEG - 2) * 16384, ce, srd, lane == 0 ? (u32)(OFF_FP + (h >> 2) * 128 + ((h & 3) * 2 + nt) * 4) : OOB);
        stx4(rs, o0 + (NSEG - 1) * 65536, p0);
        stx1(rs, o1 + (NSEG - 1) * 16384, p1);
        DRAIN();
        stx1(rs, lane == 0 ? (u32)(OFF_FZ + ((h >> 2) * 2 + nt) * 128 + (h & 3) * 4) : OOB, 1u);
    } else if (wave == 2) {
        LANES();
        u32 cap = SPIN_CAP;
        u32 have = 0;
        auto seen = [&](u32x4 f) {
            u32 a = min(2 * rfl(f[0]), 2 * rfl(f[1]) + 1);
            if (h < 16) a = min(a, max(rfl(f[2]), 4u) - 1u);
            have = max(have, a);
        };
        const u32 tt = slot_v(wp ^ 8u, lane) + SL_TS + 2048u;
        const u32 tp = slot_v(wp ^ 8u, lane) + copy_off((u32)xg ^ (wp >> 1), lane) + SL_PG - 128u;
        *(lds_u32p)(lbase + 11520u + (u32)lane * 4) = 0u;
#pragma unroll 1
        for (int s = -1; s + 1 < NSEG; ++s) {
            const u32 pfo = far_off(wp ^ 1u, NEAR_KX + (u32)((s + 1) * H_ + h) * 8192);
            s_k0(W, rnd, va, sidx, ow, lbase STA);
            have = (u32)max((int)have, (int)rfl(*(lds_vu32p)(lbase + 11520u + (u32)((s + 63) & 63) * 4)));
            u32 spins = 0;
#pragma unroll 1
            for (; have <= (u32)(s + 1) && spins < cap; ++spins) {
                CFENCE();
                seen(ldx4(rs, 0u, (u32)(OFF_FA + h * 128)));
                if (spins) __builtin_amdgcn_s_sleep(2);
            }
            if (spins >= cap) { atomicOr(errw, 8u); cap = 1; }
            CFENCE();
            if (s < 0) XSTAMP(wid, 226);
            s_fetch((u32)lane * 16, lane == s + 1 ? tt : OOB, srd, pfo, lbase + kq_buf(s + 1), lbase + 11264u);
            s_fetch((u32)lane * 16, lane == s ? tp : OOB, srd, pfo - 2u * (NEAR_KX - NEAR_QX),
                    lbase + kq_buf(s + 1) + 8192, lbase + 11520u);
            FLUSH();
        }
        s_k2(W, rnd, va, sidx, ow, lbase);
    } else {
#pragma unroll 1
        for (int s = -1; s + 1 < NSEG; ++s) {
            s_k0(W, rnd, va, sidx, ow, lbase STA);
            FLUSH();
        }
        s_k2(W, rnd, va, sidx, ow, lbase);
    }
    COUNT1(0);
}

__device__ __forceinline__ void v_role(const int wave, const int h, const int xg, const u64 tag, const u16* __restrict__ mem0, const u32x32 (&W)[5],
                                       char* __restrict__ ws, char* smem) {
    const int lane = lane_id(), tid = wave * 64 + lane, n = lane & 15, g = lane >> 4;
    u32* errw = (u32*)(ws + OFF_ERR);
    const rsrc_t rs = mk_rsrc(ws);
    const u32x4 srd = mk_srd(ws);
    const u32 lbase = (u32)(uintptr_t)(__attribute__((address_space(3))) char*)smem;
    TLOAD();
    int wslot = 0; const int wid = 160 + h;
    WSTAMP(2);
    mem0_image(mem0, smem, tid);
    *(lds_u32p)(lbase + 14336) = 0u;
    const u32 wp = wait_init(rs, tag, 1);
    const u32 sidx = (n & (wave < 4 ? 2 : 8)) ? 0xEEEE : 0x4444;
    asm volatile("s_waitcnt vmcnt(0) lgkmcnt(0)" ::: "memory");
    STAMPS();
    COUNT0();
    u32 rnd = 0;
    if (wave < 4) {
        const u32 va = lbase + (u32)(g * 256 + (n & 2) * 64 + (n >> 2) * 32 + (n & 1) * 16), vw = lbase + 12288 + (u32)((wave * 16 + n) * 16 + g * 4);
        if (wave) {
#pragma unroll 1
            for (int s = 0; s < NSEG; ++s) {
                v_a0(W, rnd, va, sidx, vw, lbase STA);
                FLUSH();
                if (wave == 1 && s + 1 < NSEG) touch(srd, wp, s + 1, lbase + 15360u);
            }
        } else {
            const u32 tc = slot_v(wp ^ 8u, lane) + copy_off((u32)xg ^ (wp >> 1), lane);
            u32 c0, r0, kr = 3950;
            calib(c0, r0);
            u32 tg = r0, so = 0, left = SPIN_CAP, s = 0, poll = 2, cnt = 0;
            LOGS();
#pragma unroll 1
            while (s != NSEG) {
                u32 ok;
                v_a1(W, rnd, va, sidx, vw, lbase, left, s, so, tg, kr, c0, r0, ok, cnt, (u32)lane * 16, tc, lane < 8 ? (u32)lane * 128u : OOB, lbase + (poll & 2u) * 16384u,
                     live<1>(srd, (int)s), poll, NSEG, TV(15) STA LGA);
                UNI(rnd, left, s, so, tg, kr, c0, r0, ok, cnt);
                if (__builtin_expect(ok == 2u, 0)) { once_more(left, lbase, errw); poll = 0; continue; }
                poll = 1;
                if (!ok && s != NSEG) tg = read_ts(rs, so + SL_TS, (int)s - 1, ok);
            }
        }
    } else {
        const u32 vr = lbase + 12288 + (u32)((g * 8 + (n >> 2)) * 16 + (n & 1) * 8);
        u32 z[3];
#pragma unroll
        for (int p = 0; p < 3; ++p) z[p] = p == 2 && (g & 1) ? OOB : z_off(wp, (u32)((p * 2 + (g & 1)) * 8 + (wave & 3) * 2 + (g >> 1))) + (u32)(h * 256 + n * 16);
#pragma unroll 1
        for (int s = 0; s < NSEG; ++s) {
            const u32 sz = slot_off(wp, s);
            v_b(W, rnd, sidx, vr, z[0] + sz, z[1] + sz, z[2] + sz, srd, lbase STA);
            FLUSH();
        }
    }
    DRAIN();
    LBAR();
    stx1(rs, wave == 0 && lane_id() < 2 ? (u32)(OFF_FZ + ((h >> 2) * 2 + lane_id()) * 128 + 16 + (h & 3) * 4) : OOB, 1u);
    COUNT1(3);
}

constexpr u32 L_U = 8192, L_T = 16384;
__device__ __forceinline__ u32x4 sub4(const u32x32& v, const int i) { return (u32x4){v[i], v[i + 1], v[i + 2], v[i + 3]}; }
__device__ __forceinline__ void r_slow(const int s, rsrc_t rs, u32 so, u32 sz, u32 sm, u32 pv, u32 zv, u32 ra, u32 rb, u32 mo, u32 sidx, u32 lbase, u32 gx, u32& cap, u32* errw) {
    const int lane = lane_id(), n = lane & 15;
    u32 spins = 0;
    u32x4 p[8], z[8];
#pragma unroll 1
    for (; spins < cap; ++spins) {
        CFENCE();
        u32 d = 1;
#pragma unroll
        for (int j = 0; j < 8; ++j) p[j] = ldx4(rs, pv + (u32)(j & 1) * 256, so + ((gx ^ (u32)(j >> 1)) & 1u) * 1024u + ((gx ^ (u32)(j >> 1)) >> 1) * 32768u);
#pragma unroll
        for (int j = 0; j < 8; ++j) z[j] = ldx4(rs, zv + (u32)(j & 1) * 1024, sz + (gx ^ (u32)(j >> 1)) * 2048u);
#pragma unroll
        for (int j = 0; j < 8; ++j) d = dist(dist(d, p[j], SENT), z[j], SENT);
        if (!__any(d == 0)) break;
    }
    if (spins >= cap) { atomicOr(errw, 2u); cap = 2; }
#pragma unroll
    for (int j = 0; j < 8; ++j) {
#pragma unroll
        for (int i = 0; i < 4; ++i) p[j][i] = p[j][i] == MARK ? 0u : p[j][i];
    }
    *(lds_vu32p)(lbase + L_T + 12) = step_tag(s);
    LBAR();
    LBAR();
    f32x4 acc = {0.f, 0.f, 0.f, 0.f};
#pragma unroll
    for (int j = 0; j < 8; j += 2) {
        const u32 ua = (j == 6 ? rb : ra) + (u32)j * 1024;
        acc = smf((u32x4){p[j][2], p[j][3], p[j + 1][0], p[j + 1][1]}, z[j], z[j + 1], acc, (int)sidx);
        acc = smf((u32x4){p[j + 1][2], p[j + 1][3], p[(j + 2) & 7][0], p[(j + 2) & 7][1]}, *(lds_u32x4p)ua, *(lds_u32x4p)(ua + 1024), acc, (int)sidx);
    }
    const float x0 = acc[0] + acc[2], x1 = acc[1] + acc[3], o = __shfl_xor(n < 8 ? x1 : x0, 8);
    const u32 r = n < 8 ? pack2(x0, o) : pack2(o, x1);
#pragma unroll
    for (int c = 0; c < NCP; ++c) stx1(rs, mo, r, sm + cpy(c));
    LBAR();
}

__device__ __forceinline__ void r_role(const int wave, const int ct, const int xg, const u32 wp, char* __restrict__ ws, char* smem) {
    const int lane = lane_id(), tid = wave * 64 + lane, n = lane & 15, g = lane >> 4;
    u32* errw = (u32*)(ws + OFF_ERR);
    const rsrc_t rs = mk_rsrc(ws);
    const u32x4 srd = mk_srd(ws);
    const u32 lbase = (u32)(uintptr_t)(__attribute__((address_space(3))) char*)smem;
    if (tid < 6) *(u32*)(smem + L_T + 8 + tid * 4) = ~0u;
    asm volatile("s_waitcnt lgkmcnt(0)" ::: "memory");
    int wslot = 0; const int wid = 192 + ct;
    WSTAMP(1);
    TLOAD();
    const u32 cp = (u32)xg ^ (wp >> 1);
    u32 cap = SPIN_CAP;
    STAMPS();
    LOGS();
    COUNT0();
    if (wave == 0) {
        const int kp = ct >= 36 ? 10 : ct >> 2;
        const u32 mb = (u32)((kp >> 2) * 32768 + (kp & 3) * 1024 + (ct & 3) * 256);
        const u32 mo = mb + (u32)(((n & 7) >> 2) * 128 + (2 * g + (n >> 3)) * 16 + (n & 3) * 4);
        const u32 pv = (u32)((g >> 1) * 2048 + (g & 1) * 512 + n * 16), zv = (u32)(g * 256 + (n & 7) * 32 + (n >> 3) * 16), sidx = (n & 2) ? 0xEEEE : 0x4444;
        const u32 ra = lbase + L_U + (u32)(((g & 1) ? 2048 : 256) + (g >> 1) * 512 + (n & 7) * 32 + (n >> 3) * 16), rb = ra - (u32)(g & 1) * 8192;
        const u32 te = slot_v(wp, lane) + copy_off(cp, lane) + SL_P, tm = slot_v(wp ^ 8u, lane) + put_off(wp, 0u, lane), tz = slot_z(wp, lane) + z_off(wp, (u32)ct);
#pragma unroll 1
        for (int s = -1; s + 1 < NSEG; ++s) {
            const u32 so = tab(te, s & 63), sm = tab(tm, s & 63), sz = tab(tz, s & 63);
            u32 bad, pass;
            r_f(bad, pass, sidx, pv, zv, ra, rb, mo, lbase + L_T + 16 + (u32)(lane & 3) * 4, live(srd, s), so, sz, sm, lbase, step_tag(s), unless_neg(cap, s), min(TV(8), 256u), (u32)((ROT >> 1) * 32768 + (ROT & 1) * 1024), (u32)ROT * 2048u STA);
            if (__builtin_expect(bad, 0)) { if (s < 0) LBAR(); else r_slow(s, rs, so, sz, sm, pv, zv, ra, rb, mo, sidx, lbase, (u32)ROT, cap, errw); }
            if (pass) COUNT(2);
            touch(srd, wp, s + 1, lbase + 17408u);
            LOG();
        }
    } else if (wave == 4) {
        u32 c0, r0, kr = 3950;
        calib(c0, r0);
        const u32 tc = slot_v(wp ^ 8u, lane) + copy_off(cp, lane) + SL_TS;
        u32 pc = c0, pr = r0, tg = r0 - 0x10000u, t2 = 0, fp = 1;
#pragma unroll 1
        for (int s = -1; s + 1 < NSEG; ++s) {
            u32 t1 = tg, ok = 0;
            if (s) r_t0(c0, r0, t1, t2, ok, tg + TV(6), tg + (t2 & 0xfffu), kr, (u32)(s & 127) + 1u, fp, lbase, live(srd, s), tab(tc, s & 63) STA);
            else r_te(lbase, fp);
            fp = step_tag(s);
            if (s < 0) { if (ok == 0u || ok == 3u) LBAR(); }
            else if (__builtin_expect(ok == 0u || ok == 3u, 0)) {
                if (!ok) until(tg + TV(6) + TV(7) - 256u);
                r_t1(c0, r0, t1, t2, lane < 8 ? (u32)lane * 128u : OOB, kr, (u32)s + 1u, lbase, srd, tab(tc, s) STA);
            }
            if (__builtin_expect(ok == 2u && s >= 0, 0)) { r_te(lbase, fp); fp = 1; t1 = read_ts(rs, tab(tc, s), s, ok); }
            tg = t1;
            track_rate(c0, r0, pc, pr, kr);
            LOG();
        }
        r_te(lbase, fp);
    } else {
        const int o = wave == 5 ? 3 : wave - 1, q = o ^ ROT;
        u32 have = 0;
        u32x32 bn = {};
        auto seen = [&](u32x2 f) {
            u32 a = min(2 * f[0], 2 * f[1] + 1);
            a = min(a, (u32)__builtin_amdgcn_update_dpp(0u, a, 0xb1, 0xf, 0xf, false));
            a = min(a, (u32)__builtin_amdgcn_update_dpp(0u, a, 0x4e, 0xf, 0xf, false));
            a = min(a, (u32)__builtin_amdgcn_update_dpp(0u, a, 0x124, 0xf, 0xf, false));
            have = max(have, rfl(a));
        };
        const u32 fo = (u32)(OFF_FB + (q * 8 + (lane & 7)) * 128);
        auto load_b = [&](int sg, u32x32& bx) {
            u32 spins = 0;
#pragma unroll 1
            for (; have <= (u32)sg && spins < cap; ++spins) {
                CFENCE();
                seen(ldx2(rs, fo));
                if (spins) __builtin_amdgcn_s_sleep(4);
            }
            if (spins >= cap) { atomicOr(errw, 16u); cap = 1; }
            CFENCE();
            const u32 bs = far_off(wp, FAR_BX + (u32)(sg * NRED + ct) * 32768 + (u32)q * 8192);
            pull((u32)lane * 128, bs, srd, 1u, 0u, lbase + 17664u);
            u32x4 t[8];
#pragma unroll
            for (int i = 0; i < 8; ++i) t[i] = ldx4(rs, (u32)(lane * 16 + i * 1024), bs);
#pragma unroll
            for (int i = 0; i < 8; ++i)
#pragma unroll
                for (int j = 0; j < 4; ++j) bx[(i >> 1) * 8 + j * 2 + (i & 1)] = t[i][j];
        };
        const u32 eo = (u32)(q * 32768 + lane * 16), te = slot_v(wp, lane) + copy_off(cp, lane), sidx = n >= 8 ? 0xEEEE : 0x4444;
        const u32 uw = lbase + L_U + (u32)(o * 2048 + (g >> 1) * 256 + n * 16 + (g & 1) * 8);
        const u32 tb = far_v(wp, FAR_BX + (u32)(min(lane + 1, NSEG - 1) * NRED + ct) * 32768 + (u32)q * 8192);
        const u32 tp = slot_v(wp ^ 8u, lane) + copy_off(cp, lane) + SL_PG + (u32)q * 128;
        u32 s = ~0u, why = 0, left = 0;
#pragma unroll 1
        for (;;) {
            if (why & 1u) {
#pragma unroll
                for (int i = 0; i < 4; ++i) {
                    const f32x4 u = smf(ldx4(rs, eo + (u32)i * 1024, tab(te, (int)s - 1)), sub4(bn, i * 8), sub4(bn, i * 8 + 4), (f32x4){0.f, 0.f, 0.f, 0.f}, (int)sidx);
                    *(lds_u32x2p)(uw + (u32)i * 512) = (u32x2){pack2(u[0], u[1]), pack2(u[2], u[3])};
                }
                LBAR();
                LBAR();
            }
            if (s + 1 == NSEG) break;
            if (s == 0) left = SPIN_CAP;
            if ((int)s >= 0) load_b((int)s, bn);
            have = to_sgpr(have); left = to_sgpr(left);
            r_u(bn, s, have, left, why, sidx, eo, uw, lbase + L_T + 16 + (u32)q * 4, (u32)lane * 16, tp, te, tb, live(srd, (int)s), (u32)o * 4u, lbase STA LGA);
            s = to_sgpr(s); have = to_sgpr(have); left = to_sgpr(left); why = to_sgpr(why);
        }
        if (left == 0) atomicOr(errw, 4u);
    }
    COUNT1(wave ? 1 : 2);
    LOGOUT(32 + ct);
}

__device__ __forceinline__ void relay_role(const u32 wp, const int xg, char* __restrict__ ws) {
    const int lane = lane_id();
    const rsrc_t rs = mk_rsrc(ws);
    const u32 tc = slot_v(wp ^ 8u, lane) + copy_off((u32)xg ^ (wp >> 1), lane) + SL_TS;
#pragma unroll 1
    for (int k = 0; k + 1 < NSEG; ++k) {
        u32 t;
#pragma unroll 1
        for (u32 spins = 0;; ++spins) {
            u32 ok;
            t = read_ts(rs, tab(tc, k), k, ok);
            if (ok) break;
            if (spins >= 16u || ldx1(rs, (u32)OFF_ERR) != 0u) return;
        }
        if (k >= 3) stx1(rs, lane < 8 ? (u32)(OFF_RDY + lane * 128) : OOB, (u32)k - 2u);
        for (int q = 0; q < 64 && (int)((u32)__builtin_amdgcn_s_memrealtime() * 24u - t) < 2400; ++q) __builtin_amdgcn_s_sleep(2);
    }
}

__device__ __forceinline__ void lead_role(const u32 wp, const int xg, char* __restrict__ ws, char* smem) {
    const int lane = lane_id();
    u32* errw = (u32*)(ws + OFF_ERR);
    const rsrc_t rs = mk_rsrc(ws);
    const u32x4 srd = mk_srd(ws);
    TLOAD();
    STAMPS();
    const u32 tsw = lane < 4 ? SL_TS + (u32)((lane & 1) * 4096 + (lane >> 1) * 16384 + xg * 128) : OOB;
    const u32 tm = slot_v(wp ^ 8u, lane), tc = tm + copy_off((u32)xg ^ (wp >> 1), lane);
    const u32 m0o = lane < 40 ? (u32)((lane >> 4) * 32768 + (lane & 15) * 256 + (lane >= 36 ? 1024 : 0) + 128) : OOB;
    u32 c0, r0, kr = 3950, ki = 4240, bump = 0, hist = 0, dp = 0;
    calib(c0, r0);
    u32 pc = c0, pr = r0, tg = r0, cap = SPIN_CAP, m = 15, de, dd, per, kc = ki;
    auto plan = [&](int s) {
        const u32 mg = bump + (s < EARLY_N ? TV(10) : 0u) + (s < 4 ? TV(3) : 0u);
        de = (TV(0) * kc >> 12) + mg;
        dd = de | ((TV(1) * kc >> 12) << 12) | ((ki - 2048u) << 20);
        per = max(de, dp) + (TV(2) * kc >> 12) + mg;
        dp = de;
    };
    plan(0);
    LOGS();
#pragma unroll 1
    for (int s = 0; s + 1 < NSEG; ++s) {
        u32 sr = tsw + tab(tm, s), look = 0;
        const u32 so = tab(tc, (s + 63) & 63);
#pragma unroll 1
        for (;; ++look) {
            u64 tl;
            u32 miss;
            l_g(miss, c0, r0, tl, tg, look ? 0u : kr, m0o, so, live<1>(srd, s), (u32x4){tg + de, tg + per, dd, (u32)s + 1u}, look || !s ? OOB : sr, SLOW_RT, TV(4) STA);
            if (!s) break;
            if (look && (int)((u32)tl * 24u + TV(14) - tg) > 0) tg = (u32)tl * 24u + TV(14);
            if (__builtin_expect(!miss, 1)) break;
            if (!look) {
                sr += 512u;
                tg += TV(7);
            }
            if (look >= cap) { atomicOr(errw, 4u); cap = 1; break; }
        }
        if (!s) {
            bool dead = false;
            wait_word(rs, (u32)(OFF_FA + lane * 128), true, 1u, dead, errw, 8u, lane);
            XSTAMP(192 + xg, 227);
            tg = (u32)__builtin_amdgcn_s_memrealtime() * 24u + TV(13) - 512u;
        }
        if (!s || look) stx4(rs, sr, (u32x4){tg + de, tg + per, dd, (u32)s + 1u});
        touch(srd, wp, s + 1, (u32)(uintptr_t)(__attribute__((address_space(3))) char*)smem);
        track_rate(c0, r0, pc, pr, kr);
        if (pc == c0) { const u32 kn = to_sgpr((u32)(16777216.f * __builtin_amdgcn_rcpf((float)kr) + 0.5f)); ki = min(max(kn, (ki * 3u + kn) >> 2), 6000u); }
        hist = ((hist << 1) | (s > BUMP_S0 && m != 15u ? 1u : 0u)) & BUMP_WIN;
        if (__builtin_popcount(hist) >= BUMP_N && bump < BUMP_MAX * TV(11)) { bump += TV(11); hist = 0; }
        plan(s + 1);
        asm volatile("s_sleep 3" : "+s"(de), "+s"(dd), "+s"(per));
        u32 t2;
        tg = look ? read_ts(rs, tab(tc, s) + SL_TS, s, m, &t2) : read_ts_late(rs, srd, tab(tc, s) + SL_TS, s, m, t2, 256u);
        kc = max(t2 >> 20, 1800u) + 2048u;
        LOG();
    }
    if (xg == 0) LOGOUT(104);
}

__device__ __forceinline__ void fill_role(const u32 wp, const int fi, char* __restrict__ ws) {
    const int lane = lane_id();
    const rsrc_t rs = mk_rsrc(ws);
    TLOAD();
    const u32 oz = (u32)(fi * 65536 + lane * 16);
#pragma unroll 1
    for (int s = 5; s < NSEG; ++s) {
        const u32 un = 0x38u << ((~((wp >> 3) ^ ((u32)s >> 5)) & 1u) * 8u), a = oz + slot_off(wp, s);
        const u32 a0 = (un >> (2 * fi)) & 1u ? OOB : a, a1 = (un >> (2 * fi + 1)) & 1u ? OOB : a + 32768u;
#pragma unroll
        for (u32 i = 0; i < 32; ++i) {
            stx4(rs, (i < 16 ? a0 : a1) + ((i >> 3) & 1u) * 16384 + (i & 7) * 1024, (u32x4){SENT, SENT, SENT, SENT});
            if (i == 15) {
                asm volatile("s_waitcnt vmcnt(16)" ::: "memory");
                stx1(rs, lane == 0 ? (u32)(OFF_FA + fi * 128 + 8) : OOB, (u32)s - 1u);
            }
            if (i % FILLG == FILLG - 1) nap(TV(9));
        }
    }
    DRAIN();
    stx1(rs, lane == 0 ? (u32)(OFF_FA + fi * 128 + 8) : OOB, (u32)NSEG + 1u);
}

__device__ __forceinline__ int tile_of(const int xg, const int hl, const int i) {
    const u32 pieces = hl ? 0x85410u : 0x97632u;
    return CFG ? (int)((pieces >> ((i >> 1) * 4)) & 15u) * 4 + (i & 1) * 2 + (xg & 1) : (int)((pieces >> (i * 4)) & 15u) * 4 + xg;
}
__device__ __forceinline__ void warm_role(const u32 wp, const int xg, const int wi, char* __restrict__ ws, char* smem) {
    const int lane = lane_id();
    const rsrc_t rs = mk_rsrc(ws);
    const u32x4 srd = mk_srd(ws);
    const u32 lbase = (u32)(uintptr_t)(__attribute__((address_space(3))) char*)smem;
    TLOAD();
    u32 have = 0, ta = 0;
    int next = 1;
    const u32 tc = slot_v(wp ^ 8u, lane) + copy_off((u32)xg ^ (wp >> 1), lane) + SL_TS;
    const u32 wq = wi < 0 ? wp ^ 1u : wp, ls = wi < 0 ? H_ * 8192u : NRED * 32768u;
    const u32 l0 = wi < 0 ? (wi & 1 ? NEAR_KX : NEAR_QX) + (u32)(xg * 8 + (lane & 7)) * 8192 : FAR_BX + (u32)(min(lane, 9) * 4 + xg) * 32768 + (u32)wi * 8192;
    auto look = [&] {
        const u32x4 f = ldx4(rs, wi < 0 ? (u32)(OFF_FA + (xg * 8 + (lane & 7)) * 128) : (u32)(OFF_FB + (wi * 8 + (lane & 7)) * 128));
        u32 a = min(2 * f[0], 2 * f[1] + 1);
        if (wi < 0 && xg < 2) a = min(a, max(f[2], 4u) - 1u);
#pragma unroll
        for (int q = 1; q < 8; q <<= 1) a = min(a, (u32)__shfl_xor((int)a, q));
        have = rfl(a);
    };
#pragma unroll 1
    for (u32 spins = 0; next < WFIRST && spins < 0x4000u; ++spins) {
        CFENCE();
        if (have <= (u32)next) { __builtin_amdgcn_s_sleep(4); look(); }
        else { pull((u32)lane * 128, far_v(wq, l0 + (u32)next * ls), srd, wi < 0 ? 8u : 10u, 64u, lbase + 69632u); ++next; }
    }
#pragma unroll 1
    for (int s = 0; s + 2 < NSEG; ++s) {
        if (s) until(ta + 1000u);
        u32 tm, t2;
        ta = read_ts_late(rs, srd, tab(tc, s), s, tm, t2);
        if (have <= (u32)s + 8u) look();
        for (int q = 0; q < 64 && have <= (u32)min(s + WAHEAD, NSEG - 1) && (int)(ta + TV(5) - 1024u + WLATE - (u32)__builtin_amdgcn_s_memrealtime() * 24u) > 0; ++q) { CFENCE(); look(); }
        stx1(rs, lane == 0 ? (u32)(SL_PG - SL_TS + wi * 128) : OOB, have, tab(tc, s + 1));
        if (next < s + 2) next = s + 2;
#pragma unroll 1
        for (; next <= s + WAHEAD && next < NSEG && have > (u32)next; ++next) {
            const u32 tb = far_v(wq, l0 + (u32)next * ls);
            until(ta + TV(5) - 1024u);
            pull((u32)lane * 128, tb, srd, wi < 0 ? 8u : 10u, min(TV(12), 256u), lbase + 69632u);
        }
    }
    DRAIN();
}

__device__ __forceinline__ void topk_role(const u64 tag, char* __restrict__ ws) {
    const int lane = lane_id();
    const rsrc_t rs = mk_rsrc(ws);
    wait_init(rs, tag, 2);
    u32 v[32];
#pragma unroll
    for (int i = 0; i < 8; ++i) {
        const u32x4 t = ldx4(rs, (u32)lane * 16, (u32)(OFF_R + i * 1024));
#pragma unroll
        for (int j = 0; j < 4; ++j) v[i * 4 + j] = ((t[j] ^ ((u32)((int)t[j] >> 31) | 0x80000000u)) & 0xffff0000u) | (u32)(2047 - i * 256 - j - lane * 4);
    }
    u32 top = ~0u, res = 0;
#pragma unroll 1
    for (int c = 0; c < TOPC; ++c) {
        u32 d = ~0u;
#pragma unroll
        for (int i = 0; i < 32; ++i) d = min(d, top - v[i]);
        d = min(d, (u32)__builtin_amdgcn_update_dpp(0u, d, 0xb1, 0xf, 0xf, false));
        d = min(d, (u32)__builtin_amdgcn_update_dpp(0u, d, 0x4e, 0xf, 0xf, false));
        d = min(d, (u32)__builtin_amdgcn_update_dpp(0u, d, 0x124, 0xf, 0xf, false));
        d = min(d, (u32)__builtin_amdgcn_update_dpp(0u, d, 0x128, 0xf, 0xf, false));
        const u32 dm = min(min((u32)__builtin_amdgcn_readlane(d, 0), (u32)__builtin_amdgcn_readlane(d, 16)), min((u32)__builtin_amdgcn_readlane(d, 32), (u32)__builtin_amdgcn_readlane(d, 48)));
        top -= dm;
        if (lane == c) res = 2048u - (top & 2047u);
        top -= 1u;
    }
    stx1(rs, lane < TOPC ? (u32)(OFF_IDX + lane * 4) : OOB, res);
    XSTAMP(192, 224);
}

#define MOEV 0
#define MOE_LD(p) __builtin_nontemporal_load(p)
#define MOE_NT " nt"
template <int NK>
__device__ __forceinline__ void moe_ld(const u16* __restrict__ wp, const int ldw, const int nvalid, u32 (&r)[NK][8]) {
#pragma unroll
    for (int k = 0; k < NK; ++k)
#pragma unroll
        for (int i = 0; i < 8; ++i) r[k][i] = k < nvalid ? MOE_LD((const u32*)(wp + (size_t)(k * 32 + i) * ldw)) : 0u;
}
template <int NK>
__device__ __forceinline__ void moe_mma(const u32 (&r)[NK][8], const u32x4 (&af)[NK], f32x4& ce, f32x4& co) {
#pragma unroll
    for (int k = 0; k < NK; ++k) {
        u32x4 be, bo;
#pragma unroll
        for (int j = 0; j < 4; ++j) {
            be[j] = __builtin_amdgcn_perm(r[k][2 * j + 1], r[k][2 * j], 0x05040100u);
            bo[j] = __builtin_amdgcn_perm(r[k][2 * j + 1], r[k][2 * j], 0x07060302u);
        }
        ce = mfma16(af[k], be, ce);
        co = mfma16(af[k], bo, co);
    }
}
constexpr int NGU = FF_ / 32, NDN = D_ / 32;
__device__ __forceinline__ void moe_part(const int w, const int tid, const u16* __restrict__ wg, const u16* __restrict__ wu,
                                         const u16* __restrict__ wd, char* __restrict__ ws, char* smem) {
    const int wave = __builtin_amdgcn_readfirstlane(tid >> 6), lane = tid & 63;
    const int n = lane & 15, g = lane >> 4;
    u32* errw = (u32*)(ws + OFF_ERR);
    const rsrc_t rs = mk_rsrc(ws);
    bool dead = false;
    f32x4 (*red)[2][64] = (f32x4(*)[2][64])smem;
    const f32x4 z = {0.f, 0.f, 0.f, 0.f};
    wait_word(rs, (u32)(OFF_IDX + (n & 7) * 4), true, 1u, dead, errw, 16u, lane);
    const u32 row = (ldx1(rs, (u32)(OFF_IDX + (n & 7) * 4)) - 1u) & (S_ - 1);
    u32 r1[5][8], r2[7][8];
    const int kq = wave >> 1, dg = w - (64 - NDN), k0 = wave * 7, nk = wave == 7 ? 5 : 7;
    if (w < NGU) moe_ld<5>(((wave & 1) ? wu : wg) + (size_t)(kq * 160 + g * 8) * FF_ + w * 32 + n * 2, FF_, 5, r1);
    if (dg >= 0) moe_ld<7>(wd + (size_t)(k0 * 32 + g * 8) * D_ + dg * 32 + n * 2, D_, nk, r2);
    if (w < NGU) {
        u32x4 af[5];
#pragma unroll
        for (int k = 0; k < 5; ++k) af[k] = ldx4(rs, (u32)OFF_XN + (row * D_ + (u32)((kq * 5 + k) * 32 + g * 8)) * 2);
        f32x4 ce = z, co = z;
        moe_mma<5>(r1, af, ce, co);
        red[wave][0][lane] = ce; red[wave][1][lane] = co;
        __syncthreads();
        if (wave == 0 && g < 2) {
            f32x4 t[4];
#pragma unroll
            for (int q = 0; q < 4; ++q) t[q] = (red[q >> 1][q & 1][lane] + red[2 + (q >> 1)][q & 1][lane]) + (red[4 + (q >> 1)][q & 1][lane] + red[6 + (q >> 1)][q & 1][lane]);
#pragma unroll
            for (int i = 0; i < 4; ++i) {
                float res[2];
#pragma unroll
                for (int q = 0; q < 2; ++q) {
                    float gg = bfr(t[q][i]), uu = bfr(t[2 + q][i]);
                    float sg = bfr(gg / (1.0f + __expf(-gg)));
                    res[q] = sg * uu;
                }
                stx1(rs, (u32)(OFF_HB + ((size_t)(4 * g + i) * FF_ + w * 32 + n * 2) * 2), pack2(res[0], res[1]));
            }
        }
        if (wave == 0) {
            DRAIN();
            if (lane == 0) __hip_atomic_fetch_add((u32*)(ws + OFF_CNT), 1u, __ATOMIC_RELAXED, __HIP_MEMORY_SCOPE_AGENT);
        }
        __syncthreads();
    }
    if (dg >= 0) {
        const float rv = __uint_as_float(ldx1(rs, (u32)OFF_R + row * 4));
        wait_word(rs, (u32)OFF_CNT, true, (u32)NGU, dead, errw, 16u, lane);
        u32x4 af[7];
#pragma unroll
        for (int k = 0; k < 7; ++k) af[k] = ldx4(rs, (u32)(OFF_HB + ((size_t)(n & 7) * FF_ + (k0 + (k < nk ? k : 0)) * 32 + g * 8) * 2));
        f32x4 ye = z, yo = z;
        moe_mma<7>(r2, af, ye, yo);
        red[wave][0][lane] = ye; red[wave][1][lane] = yo;
        __syncthreads();
        if (wave == 0) {
            f32x4 t[2];
#pragma unroll
            for (int q = 0; q < 2; ++q)
                t[q] = ((red[0][q][lane] + red[1][q][lane]) + (red[2][q][lane] + red[3][q][lane])) + ((red[4][q][lane] + red[5][q][lane]) + (red[6][q][lane] + red[7][q][lane]));
            const float rs1 = bfr(1.0f / (1.0f + __expf(-rv)));
#pragma unroll
            for (int i = 0; i < 4; ++i) {
                const float rsg = __shfl(rs1, (4 * g + i) & 7);
                stx1(rs, g < 2 ? (u32)(OFF_UPD + ((size_t)(4 * g + i) * D_ + dg * 32 + n * 2) * 2) : OOB, pack2(bfr(t[0][i]) * rsg, bfr(t[1][i]) * rsg));
            }
            DRAIN();
            if (lane == 0) __hip_atomic_fetch_add((u32*)(ws + OFF_CNT + 256), 1u, __ATOMIC_RELAXED, __HIP_MEMORY_SCOPE_AGENT);
        }
        __syncthreads();
    }
}

__device__ __forceinline__ void moe_ahead(const int w, const int t, const u16* __restrict__ wg, const u16* __restrict__ wu, const u16* __restrict__ wd, u32 lds) {
    const int lane = lane_id(), t2 = w < NGU ? t - 20 : t;
    if (t2 >= (w >= 64 - NDN ? 27 : 0)) return;
    const u16* p = t2 < 0 ? (t < 10 ? wg : wu - 640 * FF_) + (size_t)(t * 64 + lane) * FF_ + w * 32 : wd + (size_t)(t2 * 64 + lane) * D_ + (w - (64 - NDN)) * 32;
    u32 sv;
    asm volatile("s_mov_b32 %0, m0\n\ts_mov_b32 m0, %2\n\ts_nop 0\n\tglobal_load_lds_dword %1, off" MOE_NT "\n\ts_mov_b32 m0, %0" : "=&s"(sv) : "v"(p), "s"(lds) : "memory");
}

#define LOOKST 18
constexpr int NBIG = (NSEG - 1) * 10, NTASK = NBIG + 80;

struct OutCtx { rsrc_t rs; const u16* x; u16* out; u32* errw; u32 wp; int wid; char* ws; };
template <int OT, int NT>
__device__ __forceinline__ void out_task(const OutCtx& cx, const int sg, const int c0, const int n0, const int tid, const int (&sel_idx)[TOPC], bool& adone, u32& rdy,
                                         bool& dead, char* redp, u32* nextp, u32* tctr) {
    const int wave = __builtin_amdgcn_readfirstlane(tid >> 6), lane = tid & 63, n = lane & 15, g = lane >> 4;
    const rsrc_t rs = cx.rs;
    if (!adone && rdy <= (u32)sg) {
        wait_word(rs, (u32)(OFF_FB + (wave * 4 + (lane & 3)) * 128 + (sg & 1) * 4), true, (u32)(sg >> 1) + 1, dead, cx.errw, 32u, lane);
        u32x2 f = ldx2(rs, (u32)(OFF_FB + (wave * 4 + (lane & 3)) * 128));
        adone = __all(f[0] >= NSEG / 2 && f[1] >= NSEG / 2);
    }
    u32x4 af[OT][5], bq[NT][5];
    const u32 bo = amap((u32)(wave * 4096 + lane * 16));
#pragma unroll
    for (int c = 0; c < OT; ++c) {
        const u32 bb = far_off(cx.wp, FAR_BX + (u32)(sg * 40 + c0 + c) * 32768);
#pragma unroll
        for (int hh = 0; hh < 4; ++hh) af[c][hh] = ldx4(rs, bo + hh * 1024, bb);
    }
    const int ti = (tid >> 6) & (OT * NT - 1);
    const int token = sg * SEGL + (n0 + (ti & (NT - 1))) * 16 + n;
    const int col = (c0 + ti / NT) * 16 + 4 * g;
    int sel = -1;
#pragma unroll
    for (int c = 0; c < TOPC; ++c) if (sel_idx[c] == token) sel = c;
    const u32x2 xv = *(const u32x2*)(cx.x + (size_t)token * D_ + col);
    const u32x2 nv = ldx2(rs, (u32)(OFF_XN + ((size_t)token * D_ + col) * 2));
    if (__any(sel >= 0)) wait_word(rs, (u32)(OFF_CNT + 256), true, (u32)NDN, dead, cx.errw, 256u, lane);
    const u32x2 uv = ldx2(rs, sel >= 0 ? (u32)(OFF_UPD + ((size_t)sel * D_ + col) * 2) : OOB);
    __builtin_amdgcn_sched_barrier(0);
    const bool late = sg + 4 >= NSEG;
    if (late) wait_flags(rs, (u32)((OT == 1 ? OFF_FZ + (wave * 2 + n0) * 128 : OFF_FP + wave * 128) + (lane & 1) * 16), true, 1u, dead, cx.errw, 64u, lane);
    else if (rdy <= (u32)sg) {
#pragma unroll 1
        for (u32 spins = 0; !dead; ++spins) {
            CFENCE();
            rdy = __builtin_amdgcn_readfirstlane(ldx1(rs, (u32)(OFF_RDY + (cx.wid & 7) * 128)));
            if (rdy > (u32)sg) break;
            __builtin_amdgcn_s_sleep(8);
            if (spins > SPIN_CAP) { if (lane == 0) atomicOr(cx.errw, 64u); dead = true; }
            if ((spins & 63) == 63 && ldx1(rs, (u32)OFF_ERR) != 0u) dead = true;
        }
        CFENCE();
    }
#pragma unroll
    for (int c = 0; c < OT; ++c) af[c][4] = ldx4(rs, (u32)(wave * 64 + lane) * 16, slot_off(cx.wp, sg) + z_off(cx.wp, (u32)(c0 + c)));
#pragma unroll
    for (int nt = 0; nt < NT; ++nt) {
#pragma unroll
        for (int hh = 0; hh < 4; ++hh) bq[nt][hh] = ldx4(rs, (u32)lane * 16, (u32)OFF_PX + (u32)((sg * H_ + wave * 4 + hh) * 2 + n0 + nt) * 1024);
        bq[nt][4] = ldx4(rs, (u32)lane * 16, (u32)OFF_PXM + (u32)((sg * 8 + wave) * 2 + n0 + nt) * 1024);
    }
    CFENCE();
    u32 next = 0;
    if (tid == 0) next = !late ? __hip_atomic_fetch_add(tctr, 1u, __ATOMIC_RELAXED, __HIP_MEMORY_SCOPE_AGENT)
                                 : __hip_atomic_load(tctr, __ATOMIC_RELAXED, __HIP_MEMORY_SCOPE_AGENT);
    f32x4 (*red)[OT * NT][64] = (f32x4(*)[OT * NT][64])redp;
#pragma unroll
    for (int c = 0; c < OT; ++c)
#pragma unroll
        for (int nt = 0; nt < NT; ++nt) {
            f32x4 acc = {0.f, 0.f, 0.f, 0.f};
#pragma unroll
            for (int k = 0; k < 5; ++k) acc = mfma16(af[c][k], bq[nt][k], acc);
            red[wave][c * NT + nt][lane] = acc;
        }
    if (tid == 0) {
        if (late && next < (u32)NTASK) next = __hip_atomic_fetch_add(tctr, 1u, __ATOMIC_RELAXED, __HIP_MEMORY_SCOPE_AGENT);
        *nextp = next;
    }
    LBAR();
    if (tid < OT * NT * 64) {
        f32x4 sum = red[0][ti][lane];
#pragma unroll
        for (int wv = 1; wv < 8; ++wv) sum += red[wv][ti][lane];
        float res[4];
#pragma unroll
        for (int q = 0; q < 4; ++q) {
            float xx = bf2f((u16)((xv[q >> 1] >> ((q & 1) * 16)) & 0xffff));
            float nn = bf2f((u16)((nv[q >> 1] >> ((q & 1) * 16)) & 0xffff));
            float bo2 = nn;
            if (sel >= 0) bo2 = bfr(nn + bf2f((u16)((uv[q >> 1] >> ((q & 1) * 16)) & 0xffff)));
            float gg = bfr(bo2 / (1.0f + __expf(-bo2)));
            float sv = bfr(sum[q]);
            res[q] = bfr(xx + bfr(sv * gg));
        }
        *(u32x2*)(cx.out + (size_t)token * D_ + col) = (u32x2){pack2(res[0], res[1]), pack2(res[2], res[3])};
    }
}

constexpr u32 L_VS = 40960;
__device__ __forceinline__ u32 xbuf(int b) { return b == 0 ? 65536u : (b == 1 ? 106496u : 0u); }
__device__ __forceinline__ void worker_role(const int wave, const int wid, const u64 tag, const u32 wp, const u16* __restrict__ x, const u32x32 (&W)[5],
                                            const u16* __restrict__ wg, const u16* __restrict__ wu,
                                            const u16* __restrict__ wd, char* __restrict__ ws, u16* __restrict__ out, char* smem) {
    const int lane = lane_id(), tid = wave * 64 + lane;
    const int n = lane & 15, g = lane >> 4;
    const int par = (wid >> 2) & 1, kind = (wid >> 3) & 1, h = (wid & 3) * 8 + (wid >> 4), type = kind * 2 + par;
    u32* errw = (u32*)(ws + OFF_ERR);
    const rsrc_t rs = mk_rsrc(ws);
    bool dead = false;
    const bool isqk = type < 2;
    int wslot = 1;
    u32* sh = (u32*)(smem + L_MISC);

    {
        const u32 lbase = (u32)(uintptr_t)(__attribute__((address_space(3))) char*)smem;
        const u32x4 srd = mk_srd(ws);
        const u32 vl = lbase + (u32)lane * 16, xp = (u32)(((wave >> 1) & 1) * 20480 + (wave < 4 ? 4096 + (wave & 1) * 8192 : (wave & 1) * 2048)), xv = xp + (u32)lane * 16, ca = lbase + L_MISC + 64;
        const u32 fo = (u32)(OFF_FA + kind * 4096 + h * 128 + par * 4);
        auto xsrc = [&](int it) { return it < NSEG / 2 ? (u32)OFF_XNP + (u32)(it * 2 + par) * 40960u : (u32)OFF_XNP; };
        auto now = [&](u32 v) {
            DRAIN();
            if (lane_id() == 0 && __hip_atomic_fetch_add((u32*)(smem + L_MISC + 68), 1u, __ATOMIC_RELAXED, __HIP_MEMORY_SCOPE_WORKGROUP) == v * 4 - 1) { stx1(rs, fo, v); XSTAMP(wid, 224 + v); }
        };
        WSTAMP(4);
        STAMPS();
        u32 b0 = xbuf(0), b1 = xbuf(1), b2 = xbuf(2);
        auto rot = [&] { const u32 t = b0; b0 = b1; b1 = b2; b2 = t; };
        if (wave < 4) {
            const u32 oq = (u32)(wave * 64 + lane) * 16;
#pragma unroll 1
            for (int it = 0; it < NSEG / 2; ++it) {
                if (isqk) {
                    if ((it & ~4) == 2) wait_init(rs, tag, 1 + (it >> 2));
                    const u32 ob = far_off(wp ^ 1u, NEAR_QX + (u32)((it * 2 + par) * H_ + h) * 8192);
                    wx0(W, vl + b0, oq, srd, xv, xsrc(it + 2), lbase + b2 + xp, ob, ob + 4096 STA);
                } else wx1(W, vl + b0, lbase + L_VS + (u32)(it & 1) * 8192 + oq STA);
                rot();
                CFLUSH();
                if (it < 3 || (it & 7) == 7) WSTAMP(0x10 + it);
            }
            DRAIN();
            LBAR();
        } else if (isqk) {
            const u32 oq = (u32)(tid & 255) * 16;
            LBAR();
#pragma unroll 1
            for (int it = 0; it < NSEG / 2; ++it) {
                const int sg = it * 2 + par;
                if ((it & ~4) == 2) sh[18] = wait_init(rs, tag, 1 + (it >> 2));
                const u32 ob = far_off(wp ^ 1u, NEAR_KX + (u32)(sg * H_ + h) * 8192);
                wk(W, vl + b0, oq, srd, ob, ob + 4096, 0.08838834764831845f * 1.4426950408889634f, xv, xsrc(it + 2), lbase + b2 + xp,
                   ca, (u32)it * 4 + 3, (u32)it, fo STA);
                rot();
                CFLUSH();
                if (it < NOWV) now((u32)it + 1);
                if (MOEV && it >= 6 && (wave & 2) == 0) moe_ahead(h * 2 + par, (it - 6) * 2 + (wave & 1), wg, wu, wd, lbase + L_MISC + 256);
            }
        } else {
            const u32 ob = amap((u32)(h * 1024 + lane * 16)), yp = 4096u + (u32)(wave & 1) * 6144u;
            LBAR();
#pragma unroll
            for (int i = 0; i < 10; ++i) {
                const u32 o = i < 2 ? (u32)i * 1024 : yp + (u32)(i - 2) * 1024;
                dma16_sc1(ws + xsrc(2) + xv + o, __builtin_amdgcn_readfirstlane(lbase + b2 + xp + o));
            }
            DRAIN();
            LBAR();
#pragma unroll 1
            for (int it = 1; it <= NSEG / 2; ++it) {
                rot();
                if ((it & ~4) == 2) sh[18] = wait_init(rs, tag, 1 + (it >> 2));
                wb(W, vl + L_VS + (u32)(~it & 1) * 8192, ob, srd, FAR_BX + (u32)((it * 2 - 2 + par) * NRED + (wave & 3) * 2) * 32768, wp, (u32)it,
                   xv, xsrc(it + 2), lbase + b2 + xp, xsrc(it + 2) + yp, lbase + b2 + xp + yp, ca, (u32)it * 4 - 1, (u32)it - 1, fo STA);
                CFLUSH();
                if (it <= NOWV) { now((u32)it); LBAR(); }
            }
        }
        DRAIN();
        LBAR();
        stx1(rs, wave == 0 && lane_id() == 0 ? fo : OOB, NSEG / 2);
    }
    WSTAMP(2);

    {
    const int lane = lane_id(), tid = wave * 64 + lane;
    if (isqk) moe_part(h * 2 + par, tid, wg, wu, wd, ws, smem);
    WSTAMP(5);
    u32* nextp = (u32*)(smem + L_MISC + 96);
    u32* tctr = (u32*)(ws + OFF_CNT + 512);
    if (tid == 0) nextp[0] = __hip_atomic_fetch_add(tctr, 1u, __ATOMIC_RELAXED, __HIP_MEMORY_SCOPE_AGENT);
    wait_flags(rs, (u32)OFF_IDX + (lane & 1) * 16, true, 1u, dead, errw, 256u, lane);
    __syncthreads();
    int sel_idx[TOPC];
    {
        u32x4 i0 = ldx4(rs, (u32)OFF_IDX), i1 = ldx4(rs, (u32)OFF_IDX + 16);
#pragma unroll
        for (int c = 0; c < 4; ++c) { sel_idx[c] = (int)i0[c] - 1; sel_idx[4 + c] = (int)i1[c] - 1; }
    }
    u32 order;
    {
        bool hit = false;
#pragma unroll
        for (int c = 0; c < TOPC; ++c) hit |= (sel_idx[c] >> 5) == lane;
        const u64 m = __ballot(hit && lane < 40), below = ((u64)1 << lane) - 1;
        const int pos = lane >= 40 ? lane : ((m >> lane) & 1 ? 40 - __popcll(m) + __popcll(m & below) : lane - __popcll(m & below));
        order = (u32)__builtin_amdgcn_ds_permute(pos * 4, lane);
    }
    WSTAMP(3);
    const OutCtx cx = {rs, x, out, errw, to_sgpr(sh[18]), wid, ws};
    bool adone = false;
    u32 rdy = 0;
#pragma unroll 1
    for (int tc = 0;; ++tc) {
        const int task = __builtin_amdgcn_readfirstlane(nextp[tc & 1]);
        if (task >= NTASK) break;
        char* redp = smem + (tc & 1) * 65536;
        if (task < NBIG) {
            WSTAMP(0x100 + task / 10);
            out_task<4, 2>(cx, (int)tab(order, task / 10), (task % 10) * 4, 0, tid, sel_idx, adone, rdy, dead, redp, nextp + ((tc + 1) & 1), tctr);
        } else {
            WSTAMP(0x100 + NSEG - 1);
            out_task<1, 1>(cx, NSEG - 1, (task - NBIG) >> 1, (task - NBIG) & 1, tid, sel_idx, adone, rdy, dead, redp, nextp + ((tc + 1) & 1), tctr);
        }
        WSTAMP(0x300);
        if (task == NTASK - 1) {
            wait_word(rs, (u32)(OFF_CNT + 768), true, 8u, dead, errw, 64u, lane);
            if (tid < 216) stx2(rs, tid < 192 ? (u32)(OFF_INI + tid * 8) : (u32)(OFF_SUM + (tid - 192) * 128), (u32x2){~(u32)tag, ~cal_tag(tag, ~(u32)tag)});
        }
    }
    }
}

constexpr int FUSED_LDS = L_MISC + 1024;
#define ORD_A 0x143210
#define ORD_C 0x101234
#define KERNEL_SYM "fused_kernel"
#define CODE_KB 88
extern "C" __global__ __launch_bounds__(512, 2) void fused_kernel(const u16* __restrict__ x, const u16* __restrict__ mem0, const u16* __restrict__ w_r,
                                                        const u16* __restrict__ Wq, const u16* __restrict__ Wk,
                                                        const u16* __restrict__ Wv, const u16* __restrict__ Wo,
                                                        const u16* __restrict__ wg, const u16* __restrict__ wu,
                                                        const u16* __restrict__ wd, char* __restrict__ ws, u16* __restrict__ out) {
    extern __shared__ __attribute__((aligned(16))) char smem[];
    const int wave = __builtin_amdgcn_readfirstlane(threadIdx.x >> 6);
    u64 gen = 0, tag = 0, old = 0;
    u32 xcc, se;
    asm volatile("s_getreg_b32 %0, hwreg(HW_REG_XCC_ID, 0, 3)" : "=s"(xcc));
    asm volatile("s_getreg_b32 %0, hwreg(HW_REG_HW_ID, 13, 2)" : "=s"(se));
    u32 bid = blockIdx.x;
    asm volatile("" : "+s"(bid));
    const bool first = (int)bid >= 0;
    u32 bq = first ? (bid & ~31u) + se * 8u + xcc : bid & 255u;
    asm volatile("" : "+s"(bq));
    const int b = (int)bq, k = b >> 3, xg = b & 3;
    if (threadIdx.x >= 448 && threadIdx.x < 453) {
        const int lane = threadIdx.x & 63;
        const u32 pg = (bid >> 3 & 31u) + 32u * (u32)lane;
        const char* tp = lane < 4 ? ws + min(pg << 21, (u32)WS_TOTAL - 4u) : (const char*)out + (size_t)(bid >> 3 & 3u) * 655360;
        u32 sv;
        asm volatile("s_mov_b32 %0, m0\n\ts_mov_b32 m0, %2\n\ts_nop 0\n\tglobal_load_lds_dword %1, off\n\ts_mov_b32 m0, %0"
                     : "=&s"(sv) : "v"(tp), "s"((u32)(uintptr_t)(__attribute__((address_space(3))) char*)smem + L_MISC + 256u) : "memory");
    }
    if (wave == 6 && (bid >> 3 & 31u) < CODE_KB / 8) {
        u32 sv;
        asm volatile("s_getpc_b64 s[98:99]\nLc%=:\n\ts_sub_u32 s98, s98, Lc%=-" KERNEL_SYM "\n\ts_subb_u32 s99, s99, 0\n\t"
                     "s_mov_b32 %0, m0\n\ts_mov_b32 m0, %2\n\ts_nop 0\n\tglobal_load_lds_dword %1, s[98:99]\n\ts_mov_b32 m0, %0"
                     : "=&s"(sv) : "v"((bid >> 3 & 31u) * 8192u + (threadIdx.x & 63u) * 128u),
                       "s"((u32)(uintptr_t)(__attribute__((address_space(3))) char*)smem + L_MISC + 768u) : "s98", "s99", "scc", "memory");
    }
    u64* claim = (u64*)(ws + OFF_CLAIM);
    u32* sh = (u32*)(smem + L_MISC);
    auto try_claim = [&] {
        if (wave == 0 && first) {
            asm volatile("s_load_dwordx2 %0, %1, 0x0\n\ts_waitcnt lgkmcnt(0)" : "=s"(gen) : "s"(ws + OFF_GEN) : "memory");
            tag = (gen + 0x9E3779B97F4A7C15ull) * 0xBF58476D1CE4E5B9ull;
            tag ^= tag >> 31;
            tag *= 0x94D049BB133111EBull;
            tag ^= tag >> 29;
            if (lane_id() == 0) {
                old = __hip_atomic_exchange(claim + b * 8, tag, __ATOMIC_RELAXED, __HIP_MEMORY_SCOPE_AGENT);
                sh[1] = (u32)tag; sh[2] = (u32)(tag >> 32); sh[3] = (u32)gen; sh[4] = (u32)(gen >> 32);
            }
        }
    };
    auto settle = [&] {
        const int tid = wave * 64 + lane_id();
        if (tid == 0 && first) {
            u32 vb = (u32)b;
#pragma unroll 1
            for (int i = 0; i < 256 && old == tag; ++i) {
                vb = (vb + 1) & 255u;
                old = __hip_atomic_exchange(claim + vb * 8, tag, __ATOMIC_RELAXED, __HIP_MEMORY_SCOPE_AGENT);
            }
            sh[0] = vb;
        }
        LBAR();
        const u32 bc = __builtin_amdgcn_readfirstlane(sh[0]);
        asm volatile("s_cmp_eq_u32 %0, %1\n\ts_cbranch_scc1 Lgo%=\n\t"
                     "s_or_b32 s2, %0, 0x80000000\n\ts_mov_b64 s[0:1], %2\n\tv_mov_b32 v0, %3\n\ts_waitcnt vmcnt(0) lgkmcnt(0)\n\t"
                     "s_getpc_b64 s[4:5]\nLpc%=:\n\ts_sub_u32 s4, s4, Lpc%=-" KERNEL_SYM "\n\ts_subb_u32 s5, s5, 0\n\ts_setpc_b64 s[4:5]\nLgo%=:"
                     :: "s"(bc), "s"(b), "s"(__builtin_amdgcn_kernarg_segment_ptr()), "v"(tid) : "s2", "scc", "memory");
        tag = (u64)(u32)__builtin_amdgcn_readfirstlane(sh[1]) | ((u64)(u32)__builtin_amdgcn_readfirstlane(sh[2]) << 32);
    };
    if (b & 4) {
        const int wid = k * 4 + xg, h = (wid & 3) * 8 + (wid >> 4), par = (wid >> 2) & 1;
        const bool isqk = ((wid >> 3) & 1) == 0;
        const int lane = lane_id();
        u32x32 W[5];
        u32 wp = 0;
        u32x2 f0;
        u64 tg2 = 0;
        bool got = false;
        auto xtile = [&](int t) {
#pragma unroll
            for (int i = 0; i < 5; ++i)
                dma16_sc1(ws + OFF_XNP + (size_t)(par + 2 * t) * 40960 + (wave * 5 + i) * 1024 + lane * 16,
                          __builtin_amdgcn_readfirstlane((u32)(uintptr_t)(__attribute__((address_space(3))) char*)smem + xbuf(t) + (u32)(wave * 5 + i) * 1024));
        };
        load_weights<ORD_A>([&](int st) {
            if (st == 0) try_claim();
            if (st == LOOKST) {
                asm volatile("global_load_dwordx2 %0, %1, off sc1" : "=&v"(f0) : "v"(ws + OFF_SUM + xcc * 128u) : "memory");
                tg2 = (u64)(u32)__builtin_amdgcn_readfirstlane(sh[1]) | ((u64)(u32)__builtin_amdgcn_readfirstlane(sh[2]) << 32);
            }
            if (st == 3) {
                asm volatile("" : "+v"(f0));
                got = __all(f0[1] == cal_tag(tg2, f0[0]));
                if (got) { xtile(0); xtile(1); }
                wp = to_sgpr(f0[0]);
            }
        }, (isqk ? Wq : Wv) + h * DH_, isqk ? Wk + h * DH_ : Wo + (size_t)h * DH_ * D_, !isqk, smem, wave, lane,
           wave >> 2, 2 * (wave & 3), wave >> 2, 2 * (wave & 3) + 1, W);
        settle();
        const rsrc_t rs = mk_rsrc(ws);
        if (!got) { wp = wait_init(rs, tag, 0); xtile(0); xtile(1); }
        sh[16] = sh[17] = 0u;
        DRAIN();
        LBAR();
        PIN_W(W);
        worker_role(wave, wid, tag, wp, x, W, wg, wu, wd, ws, out, smem);
        __builtin_amdgcn_endpgm();
    }
    const int hl = ((k >> 1) ^ (xg >> 1)) & 1, j = (k >> 2) * 2 + (k & 1);
    if (CFG ? xg < 2 : j < 8) {
        const int h = CFG ? xg * 16 + (j >> 3) * 8 + hl * 4 + ((j >> 1) & 3) : xg * 8 + hl * 4 + (j >> 1);
        u32x32 W[5];
        auto hook = [&](int st) {
            if (st == 0) try_claim();
        };
        if ((j & 1) == 0) {
            load_weights<ORD_C>(hook, Wq + h * DH_, Wk + h * DH_, false, smem, wave, lane_id(), wave < 4, wave & 3, wave < 4, 4 + (wave & 3), W);
            settle();
            PIN_W(W);
            s_role(wave, h, xg, tag, mem0, W, ws, smem);
            __builtin_amdgcn_endpgm();
        }
        if (wave < 4) { load_weights<ORD_C>(hook, Wv + h * DH_, Wo + (size_t)h * DH_ * D_, true, smem, wave, lane_id(), 0, wave, 0, 4 + wave, W); PIN_W(W); }
        else { load_weights<ORD_C | (1 << 21)>(hook, Wv + h * DH_, Wo + (size_t)h * DH_ * D_, true, smem, wave, lane_id(), 1, 2 * (wave & 3), 1, 2 * (wave & 3) + 1, W); PIN_W(W); }
        settle();
        PIN_W(W);
        v_role(wave, h, xg, tag, mem0, W, ws, smem);
        __builtin_amdgcn_endpgm();
    }
    constexpr int NR = CFG ? 10 : 5, NS = CFG ? 6 : 3, NP = CFG ? 4 : 2;
    const int jr = CFG ? j : j - 8, hi = (CFG ? xg - 2 : xg) * 2 + hl;
    try_claim();
    init_role([&] { settle(); return tag; }, wave, jr < NR ? hi * NR + jr : jr == NR ? 40 + hi : 48 + hi * NP + jr - (NR + NS - NP), xcc, ((se ^ xcc) >> 1) & 1u, x, w_r, Wq, Wk, Wv, Wo, ws, smem);
    gen = (u64)sh[3] | ((u64)sh[4] << 32);
    tag = (u64)(u32)__builtin_amdgcn_readfirstlane(sh[1]) | ((u64)(u32)__builtin_amdgcn_readfirstlane(sh[2]) << 32);
    const int wi = hl * NS + jr - NR;
    const bool aux = wi == 2 * NS - 1;
    if (jr < NR) { if (wave < 6) r_role(wave, tile_of(xg, hl, jr), xg, wait_init(mk_rsrc(ws), tag, 1), ws, smem); }
    else if (!aux) {
        if (wave >= 4) { if (wi == 0 && xg == 0 && wave == 7) topk_role(tag, ws); return; }
        const u32 wp = wait_init(mk_rsrc(ws), tag, 1);
        LBAR();
        if (wave == 0 && wi == 0 && lane_id() == 0) __hip_atomic_fetch_add((u32*)(ws + OFF_CNT + 768), 1u, __ATOMIC_RELAXED, __HIP_MEMORY_SCOPE_AGENT);
        if (wave == 2 && wi) fill_role(wp, xg * 4 + wi - 1, ws);
        if (wave == 3 && xg == 3 && wi == 4) relay_role(wp, xg, ws);
        if (wave == 1 || (wave == 2 && !wi)) warm_role(wp, xg, wi - wave, ws, smem);
    }
    else if (wave == 1) {
        const u32 wp = wait_init(mk_rsrc(ws), tag, 1);
        if (lane_id() == 0) __hip_atomic_fetch_add((u32*)(ws + OFF_CNT + 768), 1u, __ATOMIC_RELAXED, __HIP_MEMORY_SCOPE_AGENT);
        lead_role(wp, xg, ws, smem);
    }
    else if (aux && xg == 2 && wave == 0) {
        const rsrc_t rs = mk_rsrc(ws);
        const int lane = lane_id();
#pragma unroll 1
        for (u32 spins = 0; spins < SPIN_CAP; ++spins) {
            CFENCE();
            bool ok = true;
#pragma unroll
            for (int q = 0; q < 4; ++q) {
                u32x2 f = ldx2(rs, (u32)(OFF_CLAIM + (q * 64 + lane) * 64));
                ok = ok && f[0] == (u32)tag && f[1] == (u32)(tag >> 32);
            }
            if (__all(ok)) break;
            __builtin_amdgcn_s_sleep(32);
        }
        if (lane == 0) __hip_atomic_store((u64*)(ws + OFF_GEN), gen + 1ull, __ATOMIC_RELAXED, __HIP_MEMORY_SCOPE_AGENT);
#pragma unroll
        for (int q = 0; q < 4; ++q) stx2(rs, (u32)(OFF_CLAIM + (q * 64 + lane) * 64), (u32x2){~(u32)tag, 0u});
    }
}

extern "C" void kernel_launch(void* const* d_in, const int* in_sizes, int n_in, void* d_out, int out_size,
                              void* d_ws, size_t ws_size, hipStream_t stream) {
    const u16* x = (const u16*)d_in[0];
    const u16* mem0 = (const u16*)d_in[1];
    const u16* Wq = (const u16*)d_in[2];
    const u16* Wk = (const u16*)d_in[3];
    const u16* Wv = (const u16*)d_in[4];
    const u16* Wo = (const u16*)d_in[5];
    const u16* w_r = (const u16*)d_in[8];
    const u16* w_gate = (const u16*)d_in[9];
    const u16* w_up = (const u16*)d_in[10];
    const u16* w_down = (const u16*)d_in[11];
    char* ws = (char*)d_ws;
    u16* out = (u16*)d_out;

    hipFuncSetAttribute((const void*)fused_kernel, hipFuncAttributeMaxDynamicSharedMemorySize, FUSED_LDS);
    fused_kernel<<<256, 512, FUSED_LDS, stream>>>(x, mem0, w_r, Wq, Wk, Wv, Wo, w_gate, w_up, w_down, ws, out);
}
```

```cpp
#define CFG 0
#include <hip/hip_runtime.h>
#include <hip/hip_bf16.h>
#include <stdint.h>

constexpr int S_ = 2048, D_ = 640, H_ = 32, DH_ = 128, NSEG = 64, SEGL = 32, FF_ = 1728, TOPC = 8;
constexpr int HD_ = H_ * DH_;
constexpr int NRED = 40;

typedef unsigned short u16;
typedef unsigned int u32;
typedef unsigned long long u64;
typedef __attribute__((ext_vector_type(8))) __bf16 bf16x8;
typedef __attribute__((ext_vector_type(16))) __bf16 bf16x16;
typedef __attribute__((ext_vector_type(4))) float f32x4;
typedef __attribute__((ext_vector_type(4))) unsigned int u32x4;
typedef __attribute__((ext_vector_type(8))) unsigned int u32x8;
typedef __attribute__((ext_vector_type(2))) unsigned int u32x2;

__device__ __forceinline__ float bf2f(u16 h) { return __uint_as_float(((u32)h) << 16); }
typedef __bf16 bf16x2_t __attribute__((ext_vector_type(2)));
typedef float f32x2_t __attribute__((ext_vector_type(2)));
__device__ __forceinline__ u16 f2bf(float f) {
    __bf16 h = (__bf16)f;
    return __builtin_bit_cast(u16, h);
}
__device__ __forceinline__ float bfr(float f) { return (float)(__bf16)f; }
__device__ __forceinline__ u32 pack2(float a, float b) {
    f32x2_t f = {a, b};
    bf16x2_t h = __builtin_convertvector(f, bf16x2_t);
    return __builtin_bit_cast(u32, h);
}
__device__ __forceinline__ bf16x8 as_bf(u32x4 v) { return __builtin_bit_cast(bf16x8, v); }
__device__ __forceinline__ f32x4 mfma16(u32x4 a, u32x4 b, f32x4 c) {
    return __builtin_amdgcn_mfma_f32_16x16x32_bf16(as_bf(a), as_bf(b), c, 0, 0, 0);
}
__device__ __forceinline__ f32x4 smf(u32x4 a, u32x4 b0, u32x4 b1, f32x4 c, int idx) {
    u32x8 b = {b0[0], b0[1], b0[2], b0[3], b1[0], b1[1], b1[2], b1[3]};
    return __builtin_amdgcn_smfmac_f32_16x16x64_bf16(as_bf(a), __builtin_bit_cast(bf16x16, b), c, idx, 0, 0);
}
__device__ __forceinline__ int dmap(int p) {
    int q = p & 15;
    return (p & ~15) | (q & 1) | ((q & 2) << 2) | ((q & 4) >> 1) | ((q & 8) >> 1);
}

constexpr size_t al256(size_t x) { return (x + 255) & ~(size_t)255; }
constexpr size_t OFF_XN = 0;
constexpr size_t OFF_XNP = OFF_XN + al256((size_t)S_ * D_ * 2);
constexpr size_t OFF_R = OFF_XNP + al256((size_t)S_ * D_ * 2);
constexpr size_t OFF_PX = OFF_R + al256((size_t)S_ * 4);
constexpr size_t OFF_PXM = OFF_PX + al256((size_t)NSEG * H_ * 2048);
constexpr size_t OFF_HB = OFF_PXM + al256((size_t)NSEG * 8 * 2048);
constexpr size_t OFF_UPD = OFF_HB + al256((size_t)16 * FF_ * 2);
constexpr size_t OFF_GEN = OFF_UPD + al256((size_t)24 * D_ * 4);
constexpr size_t OFF_CLAIM = OFF_GEN + 256;
constexpr size_t OFF_INI = OFF_CLAIM + 16384;
constexpr size_t OFF_SUM = OFF_INI + 2048;
constexpr size_t OFF_ERR = OFF_SUM + 3072;
constexpr size_t OFF_FA = OFF_ERR + 256;
constexpr size_t OFF_FB = OFF_FA + 4096;
constexpr size_t OFF_FP = OFF_FB + 4096;
constexpr size_t OFF_FZ = OFF_FP + 1024;
constexpr size_t OFF_RDY = OFF_FZ + 2048;
constexpr size_t OFF_IDX = OFF_RDY + 1024;
constexpr size_t OFF_CNT = OFF_IDX + 256;
constexpr size_t ZERO_BEGIN = OFF_ERR, ZERO_END = OFF_CNT + 1024;
constexpr size_t OFF_WIN = (size_t)12 << 20;
constexpr size_t WS_TOTAL = OFF_WIN + ((size_t)188 << 20);
static_assert(ZERO_END <= OFF_WIN && WS_TOTAL <= ((size_t)256 << 20), "ws");

constexpr u32 SLOT0 = 52u << 20;
constexpr u32 SL_P = 4 * 32768, SL_TS = 2 * 32768 + 1024, SL_PG = SL_TS + 2048 + 384;
constexpr u32 FAR_BX = 1u << 20;
#define KQX 1u
constexpr u32 NEAR_QX = 59u << 20;
constexpr u32 NEAR_KX = NEAR_QX + NSEG * H_ * 8192;
static_assert(2 * ((size_t)NEAR_KX + (size_t)NSEG * H_ * 8192) + ((size_t)2 << 20) <= WS_TOTAL - OFF_WIN && SLOT0 + (64u << 20) <= 2 * NEAR_QX
              && 2 * ((size_t)FAR_BX + (size_t)NSEG * NRED * 32768) + ((size_t)2 << 20) <= WS_TOTAL - OFF_WIN, "far");
__device__ __forceinline__ u32 slot_off(u32 wp, int s) {
    u32 r, t;
    asm("s_lshr_b32 %0, %3, 1\n\ts_xor_b32 %0, %0, %2\n\ts_and_b32 %0, %0, 1\n\ts_lshl_b32 %0, %0, 13\n\ts_lshl_b32 %1, %3, 20\n\t"
        "s_add_u32 %0, %0, %1\n\ts_andn2_b32 %1, %2, 15\n\ts_add_u32 %0, %0, %1\n\ts_add_u32 %0, %0, %4" : "=&s"(r), "=&s"(t) : "s"(wp), "s"(s), "n"(SLOT0) : "scc");
    return r;
}
__device__ __forceinline__ u32 slot_z(u32 wp, u32 s) { return (wp & ~15u) + SLOT0 + (s << 20) + (((wp ^ (s >> 1)) & 1u) << 13); }
__device__ __forceinline__ u32 slot_v(u32 wp, u32 s) { return slot_z(wp, s) + ((((wp >> 3) ^ (s >> 5)) & 1u) << 18); }
__device__ __forceinline__ u32 far_v(u32 wp, u32 L) { return (wp & ~15u) + 2 * L + ((~(wp ^ (L >> 20)) & 1u) << 13); }
__device__ __forceinline__ u32 tab(u32 t, int s) { return __builtin_amdgcn_readlane(t, s); }
__device__ __forceinline__ u32 copy_off(u32 c, u32 s) { return ((c & 1u) << 14) | ((((c >> 1) ^ s) & 1u) << 12); }
__device__ __forceinline__ u32 z_off(u32 wp, u32 ct) {
    const u32 zb = ct >> 1, pg = zb < 16 ? zb + 16 : 6 + (zb & 1u) + (zb & 2u) * 4;
    return pg * 32768 + (((ct ^ (wp >> 1) ^ (pg >> 4)) & 1u) << 14);
}
constexpr int NCP = CFG ? 2 : 4;
__device__ __forceinline__ u32 put_off(u32 wp, u32 x0, u32 s) { return CFG ? copy_off(x0 ^ (wp >> 1), s) & 4096u : 0u; }
__device__ __forceinline__ u32 cpy(u32 c) { return CFG ? (c & 1u) * 16384u : (c & 1u) * 4096u + (c >> 1) * 16384u; }
__device__ __forceinline__ u32 far_off(u32 wp, u32 L) {
    u32 r, t;
    asm("s_lshr_b32 %0, %3, 20\n\ts_xnor_b32 %0, %0, %2\n\ts_and_b32 %0, %0, 1\n\ts_lshl_b32 %0, %0, 13\n\ts_lshl_b32 %1, %3, 1\n\t"
        "s_add_u32 %0, %0, %1\n\ts_andn2_b32 %1, %2, 15\n\ts_add_u32 %0, %0, %1" : "=&s"(r), "=&s"(t) : "s"(wp), "s"(L) : "scc");
    return r;
}
constexpr u32 SENT = 0xffbfffbfu;
constexpr u32 MARK = 0xffbeffbeu;
constexpr u32 OOB = 0x80000000u;
__device__ __forceinline__ u32 amap(u32 L) { return ((L >> 13) << 14) | (L & 8191u); }
__device__ __forceinline__ u32 win_off(const char* ws) {
    u64 a = (u64)(uintptr_t)ws;
    u64 w = (a + OFF_WIN + ((u64)2 << 20) - 1) & ~(((u64)2 << 20) - 1);
    return (u32)(w - a);
}

constexpr int AUX_SC1 = 16;
typedef __amdgpu_buffer_rsrc_t rsrc_t;
__device__ __forceinline__ rsrc_t mk_rsrc(const void* p) {
    return __builtin_amdgcn_make_buffer_rsrc((void*)p, 0, (int)WS_TOTAL, 0x00020000);
}
__device__ __forceinline__ u32x4 ldx4(rsrc_t r, u32 off, u32 so = 0) { return __builtin_amdgcn_raw_buffer_load_b128(r, off, so, AUX_SC1); }
__device__ __forceinline__ u32x2 ldx2(rsrc_t r, u32 off, u32 so = 0) { return __builtin_amdgcn_raw_buffer_load_b64(r, off, so, AUX_SC1); }
__device__ __forceinline__ u32 ldx1(rsrc_t r, u32 off, u32 so = 0) { return __builtin_amdgcn_raw_buffer_load_b32(r, off, so, AUX_SC1); }
__device__ __forceinline__ void stx4(rsrc_t r, u32 off, u32x4 v, u32 so = 0) { __builtin_amdgcn_raw_buffer_store_b128(v, r, off + so, 0, AUX_SC1); }
__device__ __forceinline__ void stx2(rsrc_t r, u32 off, u32x2 v, u32 so = 0) { __builtin_amdgcn_raw_buffer_store_b64(v, r, off, so, AUX_SC1); }
__device__ __forceinline__ void stx1(rsrc_t r, u32 off, u32 v, u32 so = 0) { __builtin_amdgcn_raw_buffer_store_b32(v, r, off, so, AUX_SC1); }
#define DRAIN() asm volatile("s_waitcnt vmcnt(0)" ::: "memory")
#define CFENCE() asm volatile("" ::: "memory")
#define LBAR() asm volatile("s_waitcnt lgkmcnt(0)\n\ts_barrier" ::: "memory")

constexpr u32 L_MISC = 147456;
constexpr u32 SPIN_CAP = 1u << 18;
__device__ __forceinline__ int lane_id() {
    int l;
    asm volatile("v_mbcnt_lo_u32_b32 %0, -1, 0\n\tv_mbcnt_hi_u32_b32 %0, -1, %0" : "=v"(l));
    __builtin_assume(l >= 0 && l < 64);
    return l;
}

__device__ __forceinline__ float xmax16(float x) {
    u32 a = __float_as_uint(x);
    auto r = __builtin_amdgcn_permlane16_swap(a, a, false, false);
    return fmaxf(__uint_as_float(r[0]), __uint_as_float(r[1]));
}
__device__ __forceinline__ float xmax32(float x) {
    u32 a = __float_as_uint(x);
    auto r = __builtin_amdgcn_permlane32_swap(a, a, false, false);
    return fmaxf(__uint_as_float(r[0]), __uint_as_float(r[1]));
}
__device__ __forceinline__ float xadd16(float x) {
    u32 a = __float_as_uint(x);
    auto r = __builtin_amdgcn_permlane16_swap(a, a, false, false);
    return __uint_as_float(r[0]) + __uint_as_float(r[1]);
}
__device__ __forceinline__ float xadd32(float x) {
    u32 a = __float_as_uint(x);
    auto r = __builtin_amdgcn_permlane32_swap(a, a, false, false);
    return __uint_as_float(r[0]) + __uint_as_float(r[1]);
}
__device__ __forceinline__ u32 umax3(u32 a, u32 b, u32 c) { return max(max(a, b), c); }

__device__ __forceinline__ void wait_flags(rsrc_t rs, u32 off, bool act, u32 target, bool& dead, u32* errw, u32 code, int lane) {
    u32 spins = 0;
#pragma unroll 1
    while (!dead) {
        CFENCE();
        u32x4 f = ldx4(rs, off);
        bool ok = !act || ((f[0] >= target) & (f[1] >= target) & (f[2] >= target) & (f[3] >= target));
        if (__all(ok)) break;
        __builtin_amdgcn_s_sleep(2);
        if (++spins > SPIN_CAP) { if (lane == 0) atomicOr(errw, code); dead = true; }
        if ((spins & 63) == 0 && ldx1(rs, (u32)OFF_ERR) != 0u) dead = true;
    }
    CFENCE();
}
__device__ __forceinline__ void wait_word(rsrc_t rs, u32 off, bool act, u32 target, bool& dead, u32* errw, u32 code, int lane) {
    u32 spins = 0;
#pragma unroll 1
    while (!dead) {
        CFENCE();
        u32 f = ldx1(rs, off);
        if (__all(!act || f >= target)) break;
        __builtin_amdgcn_s_sleep(4);
        if (++spins > SPIN_CAP) { if (lane == 0) atomicOr(errw, code); dead = true; }
        if ((spins & 63) == 0 && ldx1(rs, (u32)OFF_ERR) != 0u) dead = true;
    }
    CFENCE();
}

__device__ __forceinline__ u32 cal_tag(u64 tag, u32 v) { return ((u32)tag ^ (u32)(tag >> 32)) + v * 0x9E3779B1u; }
__device__ __forceinline__ u32 to_sgpr(u32 x) {
    u32 r;
    asm volatile("s_nop 0\n\tv_readfirstlane_b32 %0, %1\n\ts_nop 4" : "=s"(r) : "v"(x));
    return r;
}
__device__ __forceinline__ u32 wp_of(const char* ws, u32 v) {
    v += __builtin_amdgcn_update_dpp(0u, v, 0xb1, 0xf, 0xf, false);
    v += __builtin_amdgcn_update_dpp(0u, v, 0x4e, 0xf, 0xf, false);
    v += __builtin_amdgcn_update_dpp(0u, v, 0x124, 0xf, 0xf, false);
    v += __builtin_amdgcn_update_dpp(0u, v, 0x128, 0xf, 0xf, false);
    const u32 r = __builtin_amdgcn_readlane(v, 48) + 0x0f0f0f0fu;
    return to_sgpr(win_off(ws) | ((r >> 7) & 1u) | ((r >> 14) & 2u) | ((r >> 21) & 4u) | ((r >> 28) & 8u));
}
__device__ __forceinline__ u32 sum_off(int level) {
    u32 xcc;
    asm volatile("s_getreg_b32 %0, hwreg(HW_REG_XCC_ID, 0, 3)" : "=s"(xcc));
    return (u32)(OFF_SUM + level * 1024) + xcc * 128u;
}
__device__ __forceinline__ u32 wait_init(rsrc_t rs, u64 tag, int level) {
    const u32 so = sum_off(level);
    u32x2 c = {0u, 0u};
#pragma unroll 1
    for (u32 spins = 0; spins < SPIN_CAP; ++spins) {
        CFENCE();
        c = ldx2(rs, 0u, so);
        if (__all(c[1] == cal_tag(tag, c[0]))) break;
        __builtin_amdgcn_s_sleep(6);
    }
    CFENCE();
    return to_sgpr(min(c[0], (u32)OFF_WIN + 0x20000fu));
}

#define STAMPS() do { } while (0)
#define STAMP(k) do { } while (0)
#define RSTAMP(k) do { } while (0)
#define STAMP2(k, r) do { } while (0)
#define FLUSH() do { } while (0)
#define LOGS() do { } while (0)
#define LOG() do { } while (0)
#define LOGOUT(cu) do { } while (0)
#define CFLUSH() do { } while (0)
#define XSTAMP(w, sl) do { } while (0)
#define WSTAMP(id) do { } while (0)
#define CSTAMP(id) do { } while (0)
#define NPROBE 2
constexpr u32 INI_ZERO_CHUNKS = (u32)((ZERO_END - ZERO_BEGIN) / 16);
static_assert(INI_ZERO_CHUNKS <= 8 * 256, "zero");

__device__ __forceinline__ float rsum16(float x) {
    x += __uint_as_float(__builtin_amdgcn_update_dpp(0u, __float_as_uint(x), 0xb1, 0xf, 0xf, false));
    x += __uint_as_float(__builtin_amdgcn_update_dpp(0u, __float_as_uint(x), 0x4e, 0xf, 0xf, false));
    x += __uint_as_float(__builtin_amdgcn_update_dpp(0u, __float_as_uint(x), 0x124, 0xf, 0xf, false));
    x += __uint_as_float(__builtin_amdgcn_update_dpp(0u, __float_as_uint(x), 0x128, 0xf, 0xf, false));
    return x;
}

__device__ __forceinline__ void ln_regs(u32x4 (&xv)[5]) {
    float sum = 0.f;
#pragma unroll
    for (int i = 0; i < 5; ++i)
#pragma unroll
        for (int j = 0; j < 4; ++j) sum += __uint_as_float(xv[i][j] << 16) + __uint_as_float(xv[i][j] & 0xffff0000u);
    const float mean = rsum16(sum) * (1.0f / D_);
    float vs = 0.f;
#pragma unroll
    for (int i = 0; i < 5; ++i)
#pragma unroll
        for (int j = 0; j < 4; ++j) {
            float d0 = __uint_as_float(xv[i][j] << 16) - mean, d1 = __uint_as_float(xv[i][j] & 0xffff0000u) - mean;
            vs += d0 * d0 + d1 * d1;
        }
    const float var = bfr(rsum16(vs) * (1.0f / D_));
    const float m_bf = bfr(mean);
    const float rden = 1.0f / bfr(sqrtf(bfr(var + 1e-05f)));
#pragma unroll
    for (int i = 0; i < 5; ++i)
#pragma unroll
        for (int j = 0; j < 4; ++j)
            xv[i][j] = pack2(bfr(__uint_as_float(xv[i][j] << 16) - m_bf) * rden, bfr(__uint_as_float(xv[i][j] & 0xffff0000u) - m_bf) * rden);
}
__device__ __forceinline__ u32x4 ln_perm(const u32x4 o, const int n) {
    const u32 s0 = (n & 1) ? o[0] : o[2], s1 = (n & 1) ? o[1] : o[3];
    const u32 t0 = __builtin_amdgcn_update_dpp(0u, s0, 0xb1, 0xf, 0xf, false), t1 = __builtin_amdgcn_update_dpp(0u, s1, 0xb1, 0xf, 0xf, false);
    return (n & 1) ? (u32x4){t0, o[2], t1, o[3]} : (u32x4){o[0], t0, o[1], t1};
}
__device__ __forceinline__ u32 img_off(const int row, const int n, const int i) {
    return (u32)((row >> 4) * 20480 + (4 * i + (n >> 2)) * 1024 + (n & 3) * 256 + (row & 15) * 16);
}
__device__ __forceinline__ void ln_load(const int row0, const int lane, const u16* __restrict__ x, const u16* __restrict__ w_r, u32x4 (&xv)[5], u32x4 (&wv)[5]) {
    const int n = lane & 15, row = row0 + (lane >> 4);
#pragma unroll
    for (int i = 0; i < 5; ++i) xv[i] = *(const u32x4*)(x + (size_t)row * D_ + (n + 16 * i) * 8);
#pragma unroll
    for (int i = 0; i < 5; ++i) wv[i] = *(const u32x4*)(w_r + (n + 16 * i) * 8);
}
__device__ __forceinline__ void ln_store(const int row0, const int lane, const u32x4 (&xv)[5], const u32x4 (&wv)[5], rsrc_t rs, const bool img = true) {
    const int n = lane & 15, row = row0 + (lane >> 4);
#pragma unroll
    for (int i = 0; i < 5; ++i) if (img) stx4(rs, (u32)OFF_XNP + img_off(row, n, i), ln_perm(xv[i], n));
    CFENCE();
    float dot = 0.f;
#pragma unroll
    for (int i = 0; i < 5; ++i) {
        stx4(rs, (u32)OFF_XN + (u32)(row * D_ + (n + 16 * i) * 8) * 2, xv[i]);
#pragma unroll
        for (int j = 0; j < 4; ++j)
            dot += __uint_as_float(xv[i][j] << 16) * __uint_as_float(wv[i][j] << 16) + __uint_as_float(xv[i][j] & 0xffff0000u) * __uint_as_float(wv[i][j] & 0xffff0000u);
    }
    dot = rsum16(dot);
    stx1(rs, n == 0 ? (u32)(OFF_R + row * 4) : OOB, __float_as_uint(bfr(dot)));
}
__device__ __forceinline__ u32x4 mk_srd(const char* ws) {
    const u64 a = (u64)(uintptr_t)ws;
    return (u32x4){(u32)a, (u32)(a >> 32), (u32)WS_TOTAL, 0x00020000u};
}

template <typename F>
__device__ __forceinline__ u32 probe(F&& early, const int pi, const u32 xcc, const u32 hl, char* __restrict__ ws) {
    const u32x4 srd = mk_srd(ws);
    const u32 base = win_off(ws) + SLOT0 + (8u << 20) + 6u * 32768u + (u32)pi * 128u;
    const int lane = lane_id();
    const u32 zero = 0;
    asm volatile("buffer_atomic_or %0, %1, %2, %3 offen sc1\n\ts_waitcnt vmcnt(0)" :: "v"(zero), "v"(lane < 16 ? (u32)lane * 2048u : OOB), "s"(srd), "s"(base) : "memory");
    const u32 one = lane == 0 ? 0u : OOB;
    auto lat = [&](u32 q) {
        u64 t0, t1;
        asm volatile("s_memtime %0\n\tbuffer_atomic_or %2, %3, %4, %5 offen sc1\n\ts_waitcnt vmcnt(0)\n\ts_memtime %1\n\ts_waitcnt lgkmcnt(0)"
                     : "=&s"(t0), "=&s"(t1) : "v"(zero), "v"(one), "s"(srd), "s"(base + q * 2048u) : "memory");
        return (u32)t1 - (u32)t0;
    };
    const u32 q0 = (u32)pi & 11u, a0 = lat(q0), a1 = lat(q0 | 4u), a2 = lat(q0 ^ 8u), a3 = lat((q0 ^ 8u) | 4u), a4 = lat(q0 ^ 2u), a5 = lat((q0 ^ 2u) | 4u);
    const int d13 = (int)min(min(a0, a2), a4) - (int)min(min(a1, a3), a5), v13 = min(max(d13 >> 4, -7), 7);
    const u32 b13 = d13 > 0 ? 4u : 0u;
    const u32 flip = (xcc >> 2) | ((xcc & 3u) << 1) | (hl << 3);
    u32 votes = (u32)(7 + ((flip & 1u) ? -v13 : v13));
    early(votes);
    u32 best = ~0u;
#pragma unroll 1
    for (u32 i = 0; i < 8 * NPROBE; ++i) {
        const u32 c = (i + (u32)pi) & 7u, t = lat((c & 3u) | b13 | (c & 4u) * 2u);
        if ((u32)lane == c) best = min(best, t);
    }
#pragma unroll
    for (u32 i = 1; i < 4; ++i) {
        const u32 bit = 8u >> i;
        const int d = (int)best - __shfl_xor((int)best, (int)bit);
        int v = 0;
#pragma unroll
        for (u32 c = 0; c < 8; ++c) if (!(c & bit)) v += min(max(__builtin_amdgcn_readlane(d, c), -28), 28);
        v = (v + 8) >> 4;
        if ((flip >> i) & 1u) v = -v;
        votes |= (u32)(xcc >= 4 ? 7 : 7 + v) << (8 * i);
    }
    return votes;
}

typedef __attribute__((ext_vector_type(32))) unsigned int u32x32;
__device__ __forceinline__ u32x4 wfrag(const u32x32 (&W)[5], const int i) {
    return (u32x4){W[i >> 3][(i & 7) * 4], W[i >> 3][(i & 7) * 4 + 1], W[i >> 3][(i & 7) * 4 + 2], W[i >> 3][(i & 7) * 4 + 3]};
}
#define PIN_W(W) asm volatile("" : "+{v[16:47]}"(W[0]), "+{v[48:79]}"(W[1]), "+{v[80:111]}"(W[2]), "+{v[112:143]}"(W[3]), "+{v[144:175]}"(W[4]))
typedef __attribute__((ext_vector_type(4))) short s16x4;
typedef __attribute__((address_space(3))) s16x4* lds_s16x4p;
__device__ __forceinline__ u32x2 trd(u32 ldsaddr) {
    return __builtin_bit_cast(u32x2, __builtin_amdgcn_ds_read_tr16_b64_v4i16((lds_s16x4p)ldsaddr));
}
template <bool NT>
__device__ __forceinline__ void dma16(const void* g, u32 lds_base) {
    u32 sv;
    if (NT)
        asm volatile("s_mov_b32 %0, m0\n\ts_mov_b32 m0, %2\n\ts_nop 0\n\tglobal_load_lds_dwordx4 %1, off nt\n\ts_mov_b32 m0, %0"
                     : "=&s"(sv) : "v"(g), "s"(lds_base) : "memory");
    else
        asm volatile("s_mov_b32 %0, m0\n\ts_mov_b32 m0, %2\n\ts_nop 0\n\tglobal_load_lds_dwordx4 %1, off\n\ts_mov_b32 m0, %0"
                     : "=&s"(sv) : "v"(g), "s"(lds_base) : "memory");
}
__device__ __forceinline__ void dma16_sc1(const void* g, u32 lds_base) {
    u32 sv;
    asm volatile("s_mov_b32 %0, m0\n\ts_mov_b32 m0, %2\n\ts_nop 0\n\tglobal_load_lds_dwordx4 %1, off sc1\n\ts_mov_b32 m0, %0"
                 : "=&s"(sv) : "v"(g), "s"(lds_base) : "memory");
}
template <int ORD, typename F>
__device__ __forceinline__ void load_weights(F&& after, const u16* __restrict__ p0, const u16* __restrict__ p1, const bool wo1, char* smem,
                                             const int wave, const int lane, const int sa, const int ta, const int sb, const int tb,
                                             u32x32 (&W)[5]) {
    const u32 lbase = (u32)(uintptr_t)(__attribute__((address_space(3))) char*)smem;
    const int strm = wave >> 2;
    const bool wo = wo1 && strm == 1;
    const u16* src = strm ? p1 : p0;
    const u32 sstep = wo ? 128u : 128u * HD_;
    u32 so[8];
#pragma unroll
    for (int i = 0; i < 8; ++i) {
        const int j = (wave & 3) * 8 + i, r = 4 * j + (lane >> 4);
        const int f = (lane >> 4) | (((j >> 1) & 1) << 2);
        const int ch = (lane & 15) ^ (f << 1);
        const int e = (r & ~31) | (((r >> 2) & 1) << 4) | (((r >> 3) & 3) << 2) | (r & 3);
        so[i] = wo ? (u32)(e * D_ + ch * 8) : (u32)(dmap(r) * HD_ + ch * 8);
    }
    auto issue = [&](int st) {
        const u32 dst = __builtin_amdgcn_readfirstlane(lbase + (u32)((st & 1) * 65536 + strm * 32768 + (wave & 3) * 8192));
#pragma unroll
        for (int i = 0; i < 8; ++i) dma16<((ORD >> 20) & 1) != 0>(src + so[i] + (size_t)((ORD >> (4 * st)) & 15) * sstep, dst + i * 1024);
    };
    const int g = lane >> 4, q = (lane >> 2) & 3, p = lane & 3;
    const int f = q | ((g & 1) << 2);
    const u32 ra = lbase + (u32)(sa * 32768 + (8 * g + q) * 256 + (((ta ^ f) << 1) | (p >> 1)) * 16 + (p & 1) * 8);
    const u32 rb = lbase + (u32)(sb * 32768 + (8 * g + q) * 256 + (((tb ^ f) << 1) | (p >> 1)) * 16 + (p & 1) * 8);
    issue(0);
    issue(1);
#pragma unroll
    for (int st = 0; st < 5; ++st) {
        if (st < 4) asm volatile("s_waitcnt vmcnt(8)" ::: "memory"); else asm volatile("s_waitcnt vmcnt(0)" ::: "memory");
        LBAR();
#pragma unroll
        for (int k = 0; k < 4; ++k) {
            const u32 o = (u32)((st & 1) * 65536 + k * 8192);
            u32x2 a0 = trd(ra + o), a1 = trd(ra + o + 1024), b0 = trd(rb + o), b1 = trd(rb + o + 1024);
            const int ia = (((ORD >> (4 * st)) & 15) * 4 + k) * 4, ib = 80 + ia;
            if ((ORD >> 21) & 1) {
                u32x32& w = W[(ORD >> (4 * st)) & 15];
                w[k * 8] = a0[0]; w[k * 8 + 1] = b0[0]; w[k * 8 + 2] = a0[1]; w[k * 8 + 3] = b0[1];
                w[k * 8 + 4] = a1[0]; w[k * 8 + 5] = b1[0]; w[k * 8 + 6] = a1[1]; w[k * 8 + 7] = b1[1];
            } else {
                W[ia >> 5][ia & 31] = a0[0]; W[ia >> 5][(ia & 31) + 1] = a0[1]; W[ia >> 5][(ia & 31) + 2] = a1[0]; W[ia >> 5][(ia & 31) + 3] = a1[1];
                W[ib >> 5][ib & 31] = b0[0]; W[ib >> 5][(ib & 31) + 1] = b0[1]; W[ib >> 5][(ib & 31) + 2] = b1[0]; W[ib >> 5][(ib & 31) + 3] = b1[1];
            }
        }
        LBAR();
        after(st + 16);
        if (st + 2 < 5) issue(st + 2);
        after(st);
    }
}

__device__ __forceinline__ void early_lines(const int t0, const u64 wq, const u64 wk, const u64 wv, const u64 wo, const u32 lds) {
#pragma unroll
    for (int q = 0; q < 3; ++q) {
        const int t = t0 + q * 320, m = t / 180, r = (t - m * 180) * 64 + lane_id(), row = r / 18, c = r - row * 18;
        const u64 w = m == 0 ? wo : m == 1 ? wv : m == 2 ? wk : wq;
        const u32 blk = ((c < 16 ? (u32)(c >> 1) * 4u + 1u : 31u) - ((u32)w >> 8)) & 31u;
        u32 sv;
        if (t < 720)
            asm volatile("s_mov_b32 %0, m0\n\ts_mov_b32 m0, %3\n\ts_nop 0\n\tglobal_load_lds_dword %1, %2\n\ts_mov_b32 m0, %0"
                         : "=&s"(sv) : "v"((u32)row * 8192u + blk * 256u + (u32)(c & 1) * 128u), "s"(w), "s"(lds) : "memory");
    }
}
template <typename F>
__device__ __forceinline__ void init_role(F&& settle, const int wave, const int ii, const u32 xcc, const u32 hl, const u16* __restrict__ x, const u16* __restrict__ w_r,
                                          const u16* Wq, const u16* Wk, const u16* Wv, const u16* Wo, char* __restrict__ ws, char* smem) {
    const int lane = lane_id(), tid = wave * 64 + lane;
    const rsrc_t rs = mk_rsrc(ws);
    u32* cnt = (u32*)(smem + L_MISC + 128);
    if (tid < 3) cnt[tid] = 0u;
    u64 tag;
    auto arrive = [&](int level) {
        if (lane == 0 && __hip_atomic_fetch_add(cnt + level, 1u, __ATOMIC_RELAXED, __HIP_MEMORY_SCOPE_WORKGROUP) == 7u)
            stx2(rs, (u32)(OFF_INI + (level * 64 + ii) * 8), (u32x2){0u, cal_tag(tag, 0u)});
    };
    if (ii >= 48) {
        if (wave == 1 && ii < 63) {
            const u32 votes = probe([&](u32 v) {
                tag = settle();
                stx2(rs, lane == 0 ? (u32)(OFF_INI + ii * 8) : OOB, (u32x2){v, cal_tag(tag, v)});
                DRAIN();
                XSTAMP(192 + ii, 220);
            }, ii - 48, xcc, hl, ws);
            XSTAMP(192 + ii, 223);
            stx2(rs, lane == 0 ? (u32)(OFF_INI + (64 + ii) * 8) : OOB, (u32x2){votes, cal_tag(tag, votes)});
        } else tag = settle();
        if (wave == 2 && ii == 63) {
            u32 wp = 0;
#pragma unroll 1
            for (int level = 0; level < 3; ++level) {
                const bool act = level == 2 ? lane < 48 : lane < 63 && (lane >= 40 || level == 1);
                u32x2 c = {0u, 0u};
                u32 spins = 0;
#pragma unroll 1
                for (; spins < SPIN_CAP; ++spins) {
                    CFENCE();
                    c = ldx2(rs, (u32)(OFF_INI + level * 512 + lane * 8));
                    if (__all(!act || c[1] == cal_tag(tag, c[0]))) break;
                }
                if (spins == SPIN_CAP) break;
                if (level < 2) wp = wp_of(ws, lane < 63 ? c[0] : 0x07070707u);
                stx2(rs, lane < 8 ? (u32)(OFF_SUM + level * 1024 + lane * 128) : OOB, (u32x2){wp, cal_tag(tag, wp)});
                XSTAMP(192 + level, 229);
            }
        }
        if (ii < 63) LBAR();
        return;
    }
    u32x4 xv[5], wv[5];
    if (ii < 40) {
        early_lines(wave * 40 + ii, (u64)(uintptr_t)Wq, (u64)(uintptr_t)Wk, (u64)(uintptr_t)Wv, (u64)(uintptr_t)Wo, (u32)(uintptr_t)(__attribute__((address_space(3))) char*)smem + L_MISC + 512u);
        tag = settle();
        const u32 wp = wait_init(rs, tag, 0), st = (u32)(ii * 8 + wave), so = slot_off(wp, (int)(st >> 6)) + (st & 63u) * 16384u + (u32)lane * 16;
#pragma unroll
        for (int i = 0; i < 8; ++i) stx4(rs, so + i * 1024, (u32x4){SENT, SENT, SENT, SENT});
        DRAIN();
        XSTAMP(192 + ii, 225);
        arrive(1);
    } else {
        const int row = (ii - 40) * 32 + wave * 4;
        u32x4 xw[5];
        ln_load(row, lane, x, w_r, xv, wv);
#pragma unroll
        for (int i = 0; i < 5; ++i) xw[i] = *(const u32x4*)(x + (size_t)(row + 256 + (lane >> 4)) * D_ + ((lane & 15) + 16 * i) * 8);
        ln_regs(xv);
        tag = settle();
        const u32 i = (u32)((ii - 40) * 512 + tid);
        stx4(rs, i < INI_ZERO_CHUNKS ? (u32)ZERO_BEGIN + i * 16 : OOB, (u32x4){0u, 0u, 0u, 0u});
#pragma unroll
        for (int i = 0; i < 5; ++i) stx4(rs, (u32)OFF_XNP + img_off(row + (lane >> 4), lane & 15, i), ln_perm(xv[i], lane & 15));
        ln_regs(xw);
        DRAIN();
        XSTAMP(192 + ii, 220);
        arrive(0);
        ln_store(row + 256, lane, xw, wv, rs);
        asm volatile("s_waitcnt vmcnt(6)" ::: "memory");
        arrive(1);
        ln_store(row, lane, xv, wv, rs, false);
    }
    {
        const int row = 512 + (ii * 8 + wave) * 4;
        ln_load(row, lane, x, w_r, xv, wv);
        ln_regs(xv);
        ln_store(row, lane, xv, wv, rs);
    }
    DRAIN();
    XSTAMP(192 + ii, 222);
    arrive(2);
    LBAR();
}

#define NOWV 4
#define STP
#define STA
#define LGP
#define LGA
__device__ __forceinline__ void s_fetch(u32 vm, u32 xo, u32x4 rs, u32 pfo, u32 pfd, u32 xd) {
    asm volatile(
        "s_mov_b32 s92, m0\n\t"
        "s_mov_b32 m0, %[pfd]\n\t"
        "s_add_u32 s93, %[pfo], 0x1000\n\t"
        "s_nop 2\n\t"
        "buffer_load_dwordx4 %[vm], %[rs], %[pfo] offen offset:0 sc1 lds\n\t"
        "buffer_load_dwordx4 %[vm], %[rs], %[pfo] offen offset:1024 sc1 lds\n\t"
        "buffer_load_dwordx4 %[vm], %[rs], %[pfo] offen offset:2048 sc1 lds\n\t"
        "buffer_load_dwordx4 %[vm], %[rs], %[pfo] offen offset:3072 sc1 lds\n\t"
        "s_add_u32 m0, m0, 0x1000\n\t"
        "s_nop 0\n\t"
        "buffer_load_dwordx4 %[vm], %[rs], s93 offen offset:0 sc1 lds\n\t"
        "buffer_load_dwordx4 %[vm], %[rs], s93 offen offset:1024 sc1 lds\n\t"
        "buffer_load_dwordx4 %[vm], %[rs], s93 offen offset:2048 sc1 lds\n\t"
        "buffer_load_dwordx4 %[vm], %[rs], s93 offen offset:3072 sc1 lds\n\t"
        "s_mov_b32 m0, %[xd]\n\t"
        "s_nop 0\n\t"
        "buffer_load_dword %[xo], %[rs], 0 offen sc1 lds\n\t"
        "s_mov_b32 m0, s92\n\t"
        :
        : [vm] "v"(vm), [xo] "v"(xo), [rs] "s"(rs), [pfo] "s"(pfo), [pfd] "s"(pfd), [xd] "s"(xd)
        : "s92", "s93", "vcc", "scc", "memory");
}

__device__ __forceinline__ void s_k0(const u32x32 (&W)[5], u32& rnd, u32 va, u32 sidx, u32 ow, u32 lb STP) {
    asm volatile(
        "v_mov_b64 v[248:249], 0\n\t"
        "v_mov_b64 v[250:251], 0\n\t"
        "v_mov_b64 v[252:253], 0\n\t"
        "v_mov_b64 v[254:255], 0\n\t"
        "v_mov_b32 v200, 0xffbfffbf\n\t"
        "v_mov_b32 v206, %[lb]\n\t"
        "s_add_u32 %[rnd], %[rnd], 1\n\t"
        "s_setprio 1\n\t"
        "s_waitcnt vmcnt(0) lgkmcnt(0)\n\t"
        "s_barrier\n\t"
        "s_branch Lmk%=\n\t"
        "Ltop%=:\n\t"
        "v_mov_b64 v[248:249], 0\n\t"
        "v_mov_b64 v[250:251], 0\n\t"
        "v_mov_b64 v[252:253], 0\n\t"
        "v_mov_b64 v[254:255], 0\n\t"
        "v_mov_b32 v200, 0xffbfffbf\n\t"
        "v_mov_b32 v206, %[lb]\n\t"
        "s_add_u32 %[rnd], %[rnd], 1\n\t"
        "s_setprio 1\n\t"
        "Lmk%=:\n\t"
        "ds_write_b32 %[ow], v200\n\t"
        "ds_write_b32 %[ow], v200 offset:3072\n\t"
        "s_waitcnt lgkmcnt(0)\n\t"
        "s_barrier\n\t"
        "s_nop 15\n\t"
        "s_nop 15\n\t"
        "s_nop 3\n\t"
        "ds_read_b128 v[208:211], %[va] offset:0\n\t"
        "ds_read_b128 v[212:215], %[va] offset:1024\n\t"
        "ds_read_b128 v[216:219], %[va] offset:2048\n\t"
        "ds_read_b128 v[220:223], %[va] offset:3072\n\t"
        "ds_read_b128 v[224:227], %[va] offset:4096\n\t"
        "ds_read_b128 v[228:231], %[va] offset:5120\n\t"
        "ds_read_b128 v[232:235], %[va] offset:6144\n\t"
        "ds_read_b128 v[236:239], %[va] offset:7168\n\t"
        "ds_read_b128 v[240:243], %[va] offset:8192\n\t"
        "ds_read_b128 v[244:247], %[va] offset:10240\n\t"
        "s_waitcnt lgkmcnt(0)\n\t"
        "v_smfmac_f32_16x16x64_bf16 v[248:251], v[208:211], v[16:23], %[sidx]\n\t"
        "v_smfmac_f32_16x16x64_bf16 v[248:251], v[212:215], v[24:31], %[sidx]\n\t"
        "v_smfmac_f32_16x16x64_bf16 v[248:251], v[216:219], v[32:39], %[sidx]\n\t"
        "v_smfmac_f32_16x16x64_bf16 v[248:251], v[220:223], v[40:47], %[sidx]\n\t"
        "v_smfmac_f32_16x16x64_bf16 v[248:251], v[224:227], v[48:55], %[sidx]\n\t"
        "v_smfmac_f32_16x16x64_bf16 v[248:251], v[228:231], v[56:63], %[sidx]\n\t"
        "v_smfmac_f32_16x16x64_bf16 v[248:251], v[232:235], v[64:71], %[sidx]\n\t"
        "v_smfmac_f32_16x16x64_bf16 v[248:251], v[236:239], v[72:79], %[sidx]\n\t"
        "v_smfmac_f32_16x16x64_bf16 v[248:251], v[240:243], v[80:87], %[sidx]\n\t"
        "v_smfmac_f32_16x16x64_bf16 v[248:251], v[244:247], v[88:95], %[sidx]\n\t"
        "v_smfmac_f32_16x16x64_bf16 v[252:255], v[208:211], v[96:103], %[sidx]\n\t"
        "v_smfmac_f32_16x16x64_bf16 v[252:255], v[212:215], v[104:111], %[sidx]\n\t"
        "v_smfmac_f32_16x16x64_bf16 v[252:255], v[216:219], v[112:119], %[sidx]\n\t"
        "v_add_f32 v248, v248, v250\n\t"
        "v_smfmac_f32_16x16x64_bf16 v[252:255], v[220:223], v[120:127], %[sidx]\n\t"
        "v_add_f32 v249, v249, v251\n\t"
        "v_smfmac_f32_16x16x64_bf16 v[252:255], v[224:227], v[128:135], %[sidx]\n\t"
        "v_cvt_pk_bf16_f32 v200, v248, v249\n\t"
        "v_smfmac_f32_16x16x64_bf16 v[252:255], v[228:231], v[136:143], %[sidx]\n\t"
        "ds_write_b32 %[ow], v200\n\t"
        "v_smfmac_f32_16x16x64_bf16 v[252:255], v[232:235], v[144:151], %[sidx]\n\t"
        "v_cmp_u_f32 vcc, v248, v249\n\t"
        "v_smfmac_f32_16x16x64_bf16 v[252:255], v[236:239], v[152:159], %[sidx]\n\t"
        "v_smfmac_f32_16x16x64_bf16 v[252:255], v[240:243], v[160:167], %[sidx]\n\t"
        "v_smfmac_f32_16x16x64_bf16 v[252:255], v[244:247], v[168:175], %[sidx]\n\t"
        "s_nop 6\n\t"
        "s_cbranch_vccnz Lnan%=\n\t"
        "Lok%=:\n\t"
        "v_add_f32 v252, v252, v254\n\t"
        "v_add_f32 v253, v253, v255\n\t"
        "v_cvt_pk_bf16_f32 v201, v252, v253\n\t"
        "ds_write_b32 %[ow], v201 offset:3072\n\t"
        "s_barrier\n\t"
        "s_setprio 0\n\t"
        "ds_read_b32 v207, v206 offset:25104\n\t"
        "s_waitcnt lgkmcnt(0)\n\t"
        "v_cmp_eq_u32 vcc, %[rnd], v207\n\t"
        "s_barrier\n\t"
        "s_cbranch_vccnz Ltop%=\n\t"
        "s_branch Lend%=\n\t"
        "Lnan%=:\n\t"
        "v_max_u32 v207, v208, v209\n\t"
        "v_max3_u32 v207, v207, v210, v211\n\t"
        "v_max3_u32 v207, v207, v212, v213\n\t"
        "v_max3_u32 v207, v207, v214, v215\n\t"
        "v_max3_u32 v207, v207, v216, v217\n\t"
        "v_max3_u32 v207, v207, v218, v219\n\t"
        "v_max3_u32 v207, v207, v220, v221\n\t"
        "v_max3_u32 v207, v207, v222, v223\n\t"
        "v_max3_u32 v207, v207, v224, v225\n\t"
        "v_max3_u32 v207, v207, v226, v227\n\t"
        "v_max3_u32 v207, v207, v228, v229\n\t"
        "v_max3_u32 v207, v207, v230, v231\n\t"
        "v_max3_u32 v207, v207, v232, v233\n\t"
        "v_max3_u32 v207, v207, v234, v235\n\t"
        "v_max3_u32 v207, v207, v236, v237\n\t"
        "v_max3_u32 v207, v207, v238, v239\n\t"
        "v_max3_u32 v207, v207, v240, v241\n\t"
        "v_max3_u32 v207, v207, v242, v243\n\t"
        "v_max3_u32 v207, v207, v244, v245\n\t"
        "v_max3_u32 v207, v207, v246, v247\n\t"
        "v_cmp_eq_u32 vcc, 0xffbfffbf, v207\n\t"
        "v_mov_b32 v207, %[rnd]\n\t"
        "s_cbranch_vccz Lok%=\n\t"
        "ds_write_b32 v206, v207 offset:25104\n\t"
        "s_waitcnt lgkmcnt(0)\n\t"
        "s_barrier\n\t"
        "s_barrier\n\t"
        "s_branch Ltop%=\n\t"
        "Lend%=:\n\t"
        : [rnd] "+&s"(rnd)
        : [w0] "{v[16:47]}"(W[0]), [w1] "{v[48:79]}"(W[1]), [w2] "{v[80:111]}"(W[2]), [w3] "{v[112:143]}"(W[3]), [w4] "{v[144:175]}"(W[4]), [va] "v"(va), [sidx] "v"(sidx), [ow] "v"(ow), [lb] "s"(lb)
        : "v200", "v201", "v202", "v203", "v204", "v205", "v206", "v207", "v208", "v209", "v210", "v211", "v212", "v213", "v214", "v215", "v216", "v217", "v218", "v219", "v220", "v221", "v222", "v223", "v224", "v225", "v226", "v227", "v228", "v229", "v230", "v231", "v232", "v233", "v234", "v235", "v236", "v237", "v238", "v239", "v240", "v241", "v242", "v243", "v244", "v245", "v246", "v247", "v248", "v249", "v250", "v251", "v252", "v253", "v254", "v255", "vcc", "scc", "memory");
}

__device__ __forceinline__ void s_k1(const u32x32 (&W)[5], u32& rnd, u32 va, u32 sidx, u32 ow, u32 lb, u32x4& p0, u32& p1, u32 ka, u32 qa, u32 ma, u32 ktr, u32 o0, u32 o1, float ce, u32x4 rs STP) {
    asm volatile(
        "v_mov_b64 v[248:249], 0\n\t"
        "v_mov_b64 v[250:251], 0\n\t"
        "v_mov_b64 v[252:253], 0\n\t"
        "v_mov_b64 v[254:255], 0\n\t"
        "v_mov_b32 v200, 0xffbfffbf\n\t"
        "v_mov_b32 v206, %[lb]\n\t"
        "s_add_u32 %[rnd], %[rnd], 1\n\t"
        "s_setprio 1\n\t"
        "s_waitcnt vmcnt(0) lgkmcnt(0)\n\t"
        "s_barrier\n\t"
        "buffer_store_dwordx4 v[192:195], %[o0], %[rs], 0 offen sc1\n\t"
        "buffer_store_dword v196, %[o1], %[rs], 0 offen sc1\n\t"
        "s_branch Lmk%=\n\t"
        "Ltop%=:\n\t"
        "v_mov_b64 v[248:249], 0\n\t"
        "v_mov_b64 v[250:251], 0\n\t"
        "v_mov_b64 v[252:253], 0\n\t"
        "v_mov_b64 v[254:255], 0\n\t"
        "v_mov_b32 v200, 0xffbfffbf\n\t"
        "v_mov_b32 v206, %[lb]\n\t"
        "s_add_u32 %[rnd], %[rnd], 1\n\t"
        "s_setprio 1\n\t"
        "Lmk%=:\n\t"
        "ds_write_b32 %[ow], v200\n\t"
        "ds_write_b32 %[ow], v200 offset:3072\n\t"
        "s_waitcnt lgkmcnt(0)\n\t"
        "s_barrier\n\t"
        "s_nop 15\n\t"
        "s_nop 15\n\t"
        "s_nop 3\n\t"
        "ds_read_b128 v[208:211], %[va] offset:0\n\t"
        "ds_read_b128 v[212:215], %[va] offset:1024\n\t"
        "ds_read_b128 v[216:219], %[va] offset:2048\n\t"
        "ds_read_b128 v[220:223], %[va] offset:3072\n\t"
        "ds_read_b128 v[224:227], %[va] offset:4096\n\t"
        "ds_read_b128 v[228:231], %[va] offset:5120\n\t"
        "ds_read_b128 v[232:235], %[va] offset:6144\n\t"
        "ds_read_b128 v[236:239], %[va] offset:7168\n\t"
        "ds_read_b128 v[240:243], %[va] offset:8192\n\t"
        "ds_read_b128 v[244:247], %[va] offset:10240\n\t"
        "s_waitcnt lgkmcnt(0)\n\t"
        "v_smfmac_f32_16x16x64_bf16 v[248:251], v[208:211], v[16:23], %[sidx]\n\t"
        "v_smfmac_f32_16x16x64_bf16 v[248:251], v[212:215], v[24:31], %[sidx]\n\t"
        "v_smfmac_f32_16x16x64_bf16 v[248:251], v[216:219], v[32:39], %[sidx]\n\t"
        "v_smfmac_f32_16x16x64_bf16 v[248:251], v[220:223], v[40:47], %[sidx]\n\t"
        "v_smfmac_f32_16x16x64_bf16 v[248:251], v[224:227], v[48:55], %[sidx]\n\t"
        "v_smfmac_f32_16x16x64_bf16 v[248:251], v[228:231], v[56:63], %[sidx]\n\t"
        "v_smfmac_f32_16x16x64_bf16 v[248:251], v[232:235], v[64:71], %[sidx]\n\t"
        "v_smfmac_f32_16x16x64_bf16 v[248:251], v[236:239], v[72:79], %[sidx]\n\t"
        "v_smfmac_f32_16x16x64_bf16 v[248:251], v[240:243], v[80:87], %[sidx]\n\t"
        "v_smfmac_f32_16x16x64_bf16 v[248:251], v[244:247], v[88:95], %[sidx]\n\t"
        "v_smfmac_f32_16x16x64_bf16 v[252:255], v[208:211], v[96:103], %[sidx]\n\t"
        "v_smfmac_f32_16x16x64_bf16 v[252:255], v[212:215], v[104:111], %[sidx]\n\t"
        "v_smfmac_f32_16x16x64_bf16 v[252:255], v[216:219], v[112:119], %[sidx]\n\t"
        "v_add_f32 v248, v248, v250\n\t"
        "v_smfmac_f32_16x16x64_bf16 v[252:255], v[220:223], v[120:127], %[sidx]\n\t"
        "v_add_f32 v249, v249, v251\n\t"
        "v_smfmac_f32_16x16x64_bf16 v[252:255], v[224:227], v[128:135], %[sidx]\n\t"
        "v_cvt_pk_bf16_f32 v200, v248, v249\n\t"
        "v_smfmac_f32_16x16x64_bf16 v[252:255], v[228:231], v[136:143], %[sidx]\n\t"
        "ds_write_b32 %[ow], v200\n\t"
        "v_smfmac_f32_16x16x64_bf16 v[252:255], v[232:235], v[144:151], %[sidx]\n\t"
        "v_cmp_u_f32 vcc, v248, v249\n\t"
        "v_smfmac_f32_16x16x64_bf16 v[252:255], v[236:239], v[152:159], %[sidx]\n\t"
        "v_smfmac_f32_16x16x64_bf16 v[252:255], v[240:243], v[160:167], %[sidx]\n\t"
        "v_smfmac_f32_16x16x64_bf16 v[252:255], v[244:247], v[168:175], %[sidx]\n\t"
        "s_nop 6\n\t"
        "s_cbranch_vccnz Lnan%=\n\t"
        "Lok%=:\n\t"
        "v_add_f32 v252, v252, v254\n\t"
        "v_add_f32 v253, v253, v255\n\t"
        "v_cvt_pk_bf16_f32 v201, v252, v253\n\t"
        "ds_write_b32 %[ow], v201 offset:3072\n\t"
        "s_barrier\n\t"
        "s_sleep 1\n\t"
        "ds_read_b128 v[240:243], %[ma] offset:0\n\t"
        "ds_read_b128 v[244:247], %[ma] offset:1024\n\t"
        "ds_read_b128 v[176:179], %[qa] offset:0\n\t"
        "ds_read_b128 v[180:183], %[qa] offset:1024\n\t"
        "ds_read_b128 v[184:187], %[qa] offset:2048\n\t"
        "ds_read_b128 v[188:191], %[qa] offset:3072\n\t"
        "ds_read_b128 v[208:211], %[ka] offset:0\n\t"
        "ds_read_b128 v[212:215], %[ka] offset:1024\n\t"
        "ds_read_b128 v[216:219], %[ka] offset:2048\n\t"
        "ds_read_b128 v[220:223], %[ka] offset:3072\n\t"
        "ds_read_b128 v[224:227], %[ka] offset:4096\n\t"
        "ds_read_b128 v[228:231], %[ka] offset:5120\n\t"
        "ds_read_b128 v[232:235], %[ka] offset:6144\n\t"
        "ds_read_b128 v[236:239], %[ka] offset:7168\n\t"
        "s_waitcnt lgkmcnt(7)\n\t"
        "v_mfma_f32_16x16x32_bf16 v[240:243], v[208:211], v[176:179], v[240:243]\n\t"
        "s_waitcnt lgkmcnt(6)\n\t"
        "v_mfma_f32_16x16x32_bf16 v[240:243], v[212:215], v[180:183], v[240:243]\n\t"
        "s_waitcnt lgkmcnt(5)\n\t"
        "v_mfma_f32_16x16x32_bf16 v[240:243], v[216:219], v[184:187], v[240:243]\n\t"
        "s_waitcnt lgkmcnt(4)\n\t"
        "v_mfma_f32_16x16x32_bf16 v[240:243], v[220:223], v[188:191], v[240:243]\n\t"
        "s_waitcnt lgkmcnt(3)\n\t"
        "v_mfma_f32_16x16x32_bf16 v[244:247], v[224:227], v[176:179], v[244:247]\n\t"
        "s_waitcnt lgkmcnt(2)\n\t"
        "v_mfma_f32_16x16x32_bf16 v[244:247], v[228:231], v[180:183], v[244:247]\n\t"
        "s_waitcnt lgkmcnt(1)\n\t"
        "v_mfma_f32_16x16x32_bf16 v[244:247], v[232:235], v[184:187], v[244:247]\n\t"
        "s_waitcnt lgkmcnt(0)\n\t"
        "v_mfma_f32_16x16x32_bf16 v[244:247], v[236:239], v[188:191], v[244:247]\n\t"
        "s_setprio 0\n\t"
        "ds_read_b32 v207, v206 offset:25104\n\t"
        "s_waitcnt lgkmcnt(0)\n\t"
        "v_cmp_eq_u32 vcc, %[rnd], v207\n\t"
        "s_barrier\n\t"
        "s_cbranch_vccnz Ltop%=\n\t"
        "ds_read_b64_tr_b16 v[208:209], %[ktr] offset:0\n\t"
        "ds_read_b64_tr_b16 v[210:211], %[ktr] offset:768\n\t"
        "ds_read_b64_tr_b16 v[212:213], %[ktr] offset:1536\n\t"
        "ds_read_b64_tr_b16 v[214:215], %[ktr] offset:2304\n\t"
        "ds_read_b64_tr_b16 v[216:217], %[ktr] offset:3072\n\t"
        "ds_read_b64_tr_b16 v[218:219], %[ktr] offset:3840\n\t"
        "ds_read_b64_tr_b16 v[220:221], %[ktr] offset:4608\n\t"
        "ds_read_b64_tr_b16 v[222:223], %[ktr] offset:5376\n\t"
        "s_waitcnt lgkmcnt(6)\n\t"
        "v_mfma_f32_16x16x32_bf16 v[248:251], v[208:211], v[176:179], 0\n\t"
        "s_waitcnt lgkmcnt(4)\n\t"
        "v_mfma_f32_16x16x32_bf16 v[248:251], v[212:215], v[180:183], v[248:251]\n\t"
        "s_waitcnt lgkmcnt(2)\n\t"
        "v_mfma_f32_16x16x32_bf16 v[248:251], v[216:219], v[184:187], v[248:251]\n\t"
        "s_waitcnt lgkmcnt(0)\n\t"
        "v_mfma_f32_16x16x32_bf16 v[248:251], v[220:223], v[188:191], v[248:251]\n\t"
        "v_max3_f32 v253, v240, v241, v242\n\t"
        "v_max3_f32 v253, v253, v243, v244\n\t"
        "v_max3_f32 v253, v253, v245, v246\n\t"
        "v_max_f32 v253, v253, v247\n\t"
        "s_nop 3\n\t"
        "v_mul_f32 v248, %[ce], v248\n\t"
        "v_mul_f32 v249, %[ce], v249\n\t"
        "v_max3_f32 v253, v253, v248, v249\n\t"
        "v_mov_b32 v254, v253\n\t"
        "s_nop 1\n\t"
        "v_permlane16_swap_b32 v253, v254\n\t"
        "v_max_f32 v253, v253, v254\n\t"
        "v_mov_b32 v254, v253\n\t"
        "s_nop 1\n\t"
        "v_permlane32_swap_b32 v253, v254\n\t"
        "v_max_f32 v253, v253, v254\n\t"
        "v_sub_f32 v240, v240, v253\n\t"
        "v_sub_f32 v241, v241, v253\n\t"
        "v_sub_f32 v242, v242, v253\n\t"
        "v_sub_f32 v243, v243, v253\n\t"
        "v_sub_f32 v244, v244, v253\n\t"
        "v_sub_f32 v245, v245, v253\n\t"
        "v_sub_f32 v246, v246, v253\n\t"
        "v_sub_f32 v247, v247, v253\n\t"
        "v_sub_f32 v248, v248, v253\n\t"
        "v_sub_f32 v249, v249, v253\n\t"
        "v_exp_f32 v240, v240\n\t"
        "v_exp_f32 v241, v241\n\t"
        "v_exp_f32 v242, v242\n\t"
        "v_exp_f32 v243, v243\n\t"
        "v_exp_f32 v244, v244\n\t"
        "v_exp_f32 v245, v245\n\t"
        "v_exp_f32 v246, v246\n\t"
        "v_exp_f32 v247, v247\n\t"
        "v_exp_f32 v248, v248\n\t"
        "v_exp_f32 v249, v249\n\t"
        "v_pk_add_f32 v[252:253], v[240:241], v[242:243]\n\t"
        "v_pk_add_f32 v[254:255], v[244:245], v[246:247]\n\t"
        "v_pk_add_f32 v[252:253], v[252:253], v[248:249]\n\t"
        "v_pk_add_f32 v[252:253], v[252:253], v[254:255]\n\t"
        "v_add_f32 v252, v252, v253\n\t"
        "v_mov_b32 v253, v252\n\t"
        "s_nop 1\n\t"
        "v_permlane16_swap_b32 v252, v253\n\t"
        "v_add_f32 v252, v252, v253\n\t"
        "v_mov_b32 v253, v252\n\t"
        "s_nop 1\n\t"
        "v_permlane32_swap_b32 v252, v253\n\t"
        "v_add_f32 v252, v252, v253\n\t"
        "v_rcp_f32 v252, v252\n\t"
        "s_nop 0\n\t"
        "v_pk_mul_f32 v[240:241], v[240:241], v[252:253] op_sel_hi:[1,0]\n\t"
        "v_pk_mul_f32 v[242:243], v[242:243], v[252:253] op_sel_hi:[1,0]\n\t"
        "v_pk_mul_f32 v[244:245], v[244:245], v[252:253] op_sel_hi:[1,0]\n\t"
        "v_pk_mul_f32 v[246:247], v[246:247], v[252:253] op_sel_hi:[1,0]\n\t"
        "v_pk_mul_f32 v[248:249], v[248:249], v[252:253] op_sel_hi:[1,0]\n\t"
        "v_cvt_pk_bf16_f32 v192, v240, v241\n\t"
        "v_cvt_pk_bf16_f32 v193, v242, v243\n\t"
        "v_cvt_pk_bf16_f32 v194, v244, v245\n\t"
        "v_cvt_pk_bf16_f32 v195, v246, v247\n\t"
        "v_cvt_pk_bf16_f32 v196, v248, v249\n\t"
        "s_branch Lend%=\n\t"
        "Lnan%=:\n\t"
        "v_max_u32 v207, v208, v209\n\t"
        "v_max3_u32 v207, v207, v210, v211\n\t"
        "v_max3_u32 v207, v207, v212, v213\n\t"
        "v_max3_u32 v207, v207, v214, v215\n\t"
        "v_max3_u32 v207, v207, v216, v217\n\t"
        "v_max3_u32 v207, v207, v218, v219\n\t"
        "v_max3_u32 v207, v207, v220, v221\n\t"
        "v_max3_u32 v207, v207, v222, v223\n\t"
        "v_max3_u32 v207, v207, v224, v225\n\t"
        "v_max3_u32 v207, v207, v226, v227\n\t"
        "v_max3_u32 v207, v207, v228, v229\n\t"
        "v_max3_u32 v207, v207, v230, v231\n\t"
        "v_max3_u32 v207, v207, v232, v233\n\t"
        "v_max3_u32 v207, v207, v234, v235\n\t"
        "v_max3_u32 v207, v207, v236, v237\n\t"
        "v_max3_u32 v207, v207, v238, v239\n\t"
        "v_max3_u32 v207, v207, v240, v241\n\t"
        "v_max3_u32 v207, v207, v242, v243\n\t"
        "v_max3_u32 v207, v207, v244, v245\n\t"
        "v_max3_u32 v207, v207, v246, v247\n\t"
        "v_cmp_eq_u32 vcc, 0xffbfffbf, v207\n\t"
        "v_mov_b32 v207, %[rnd]\n\t"
        "s_cbranch_vccz Lok%=\n\t"
        "ds_write_b32 v206, v207 offset:25104\n\t"
        "s_waitcnt lgkmcnt(0)\n\t"
        "s_barrier\n\t"
        "s_barrier\n\t"
        "s_branch Ltop%=\n\t"
        "Lend%=:\n\t"
        : [rnd] "+&s"(rnd), [p0] "+{v[192:195]}"(p0), [p1] "+{v196}"(p1)
        : [w0] "{v[16:47]}"(W[0]), [w1] "{v[48:79]}"(W[1]), [w2] "{v[80:111]}"(W[2]), [w3] "{v[112:143]}"(W[3]), [w4] "{v[144:175]}"(W[4]), [va] "v"(va), [sidx] "v"(sidx), [ow] "v"(ow), [lb] "s"(lb), [ka] "v"(ka), [qa] "v"(qa), [ma] "v"(ma), [ktr] "v"(ktr), [o0] "v"(o0), [o1] "v"(o1), [ce] "s"(ce), [rs] "s"(rs)
        : "v176", "v177", "v178", "v179", "v180", "v181", "v182", "v183", "v184", "v185", "v186", "v187", "v188", "v189", "v190", "v191", "v200", "v201", "v202", "v203", "v204", "v205", "v206", "v207", "v208", "v209", "v210", "v211", "v212", "v213", "v214", "v215", "v216", "v217", "v218", "v219", "v220", "v221", "v222", "v223", "v224", "v225", "v226", "v227", "v228", "v229", "v230", "v231", "v232", "v233", "v234", "v235", "v236", "v237", "v238", "v239", "v240", "v241", "v242", "v243", "v244", "v245", "v246", "v247", "v248", "v249", "v250", "v251", "v252", "v253", "v254", "v255", "vcc", "scc", "memory");
}

__device__ __forceinline__ void s_k2(const u32x32 (&W)[5], u32& rnd, u32 va, u32 sidx, u32 ow, u32 lb) {
    asm volatile(
        "v_mov_b64 v[248:249], 0\n\t"
        "v_mov_b64 v[250:251], 0\n\t"
        "v_mov_b64 v[252:253], 0\n\t"
        "v_mov_b64 v[254:255], 0\n\t"
        "v_mov_b32 v200, 0xffbfffbf\n\t"
        "v_mov_b32 v206, %[lb]\n\t"
        "s_add_u32 %[rnd], %[rnd], 1\n\t"
        "s_setprio 1\n\t"
        "s_waitcnt vmcnt(0) lgkmcnt(0)\n\t"
        "s_barrier\n\t"
        "s_branch Lmk%=\n\t"
        "Ltop%=:\n\t"
        "v_mov_b64 v[248:249], 0\n\t"
        "v_mov_b64 v[250:251], 0\n\t"
        "v_mov_b64 v[252:253], 0\n\t"
        "v_mov_b64 v[254:255], 0\n\t"
        "v_mov_b32 v200, 0xffbfffbf\n\t"
        "v_mov_b32 v206, %[lb]\n\t"
        "s_add_u32 %[rnd], %[rnd], 1\n\t"
        "s_setprio 1\n\t"
        "Lmk%=:\n\t"
        "ds_write_b32 %[ow], v200\n\t"
        "ds_write_b32 %[ow], v200 offset:3072\n\t"
        "s_waitcnt lgkmcnt(0)\n\t"
        "s_barrier\n\t"
        "ds_read_b128 v[208:211], %[va] offset:0\n\t"
        "ds_read_b128 v[212:215], %[va] offset:1024\n\t"
        "ds_read_b128 v[216:219], %[va] offset:2048\n\t"
        "ds_read_b128 v[220:223], %[va] offset:3072\n\t"
        "ds_read_b128 v[224:227], %[va] offset:4096\n\t"
        "ds_read_b128 v[228:231], %[va] offset:5120\n\t"
        "ds_read_b128 v[232:235], %[va] offset:6144\n\t"
        "ds_read_b128 v[236:239], %[va] offset:7168\n\t"
        "ds_read_b128 v[240:243], %[va] offset:8192\n\t"
        "ds_read_b128 v[244:247], %[va] offset:10240\n\t"
        "s_waitcnt lgkmcnt(9)\n\t"
        "v_smfmac_f32_16x16x64_bf16 v[248:251], v[208:211], v[16:23], %[sidx]\n\t"
        "s_waitcnt lgkmcnt(8)\n\t"
        "v_smfmac_f32_16x16x64_bf16 v[248:251], v[212:215], v[24:31], %[sidx]\n\t"
        "s_waitcnt lgkmcnt(7)\n\t"
        "v_smfmac_f32_16x16x64_bf16 v[248:251], v[216:219], v[32:39], %[sidx]\n\t"
        "s_waitcnt lgkmcnt(6)\n\t"
        "v_smfmac_f32_16x16x64_bf16 v[248:251], v[220:223], v[40:47], %[sidx]\n\t"
        "s_waitcnt lgkmcnt(5)\n\t"
        "v_smfmac_f32_16x16x64_bf16 v[248:251], v[224:227], v[48:55], %[sidx]\n\t"
        "s_waitcnt lgkmcnt(4)\n\t"
        "v_smfmac_f32_16x16x64_bf16 v[248:251], v[228:231], v[56:63], %[sidx]\n\t"
        "s_waitcnt lgkmcnt(3)\n\t"
        "v_smfmac_f32_16x16x64_bf16 v[248:251], v[232:235], v[64:71], %[sidx]\n\t"
        "s_waitcnt lgkmcnt(2)\n\t"
        "v_smfmac_f32_16x16x64_bf16 v[248:251], v[236:239], v[72:79], %[sidx]\n\t"
        "s_waitcnt lgkmcnt(1)\n\t"
        "v_smfmac_f32_16x16x64_bf16 v[248:251], v[240:243], v[80:87], %[sidx]\n\t"
        "s_waitcnt lgkmcnt(0)\n\t"
        "v_smfmac_f32_16x16x64_bf16 v[248:251], v[244:247], v[88:95], %[sidx]\n\t"
        "v_smfmac_f32_16x16x64_bf16 v[252:255], v[208:211], v[96:103], %[sidx]\n\t"
        "v_smfmac_f32_16x16x64_bf16 v[252:255], v[212:215], v[104:111], %[sidx]\n\t"
        "v_smfmac_f32_16x16x64_bf16 v[252:255], v[216:219], v[112:119], %[sidx]\n\t"
        "v_add_f32 v248, v248, v250\n\t"
        "v_smfmac_f32_16x16x64_bf16 v[252:255], v[220:223], v[120:127], %[sidx]\n\t"
        "v_add_f32 v249, v249, v251\n\t"
        "v_smfmac_f32_16x16x64_bf16 v[252:255], v[224:227], v[128:135], %[sidx]\n\t"
        "v_cvt_pk_bf16_f32 v200, v248, v249\n\t"
        "v_smfmac_f32_16x16x64_bf16 v[252:255], v[228:231], v[136:143], %[sidx]\n\t"
        "ds_write_b32 %[ow], v200\n\t"
        "v_smfmac_f32_16x16x64_bf16 v[252:255], v[232:235], v[144:151], %[sidx]\n\t"
        "v_cmp_u_f32 vcc, v248, v249\n\t"
        "v_smfmac_f32_16x16x64_bf16 v[252:255], v[236:239], v[152:159], %[sidx]\n\t"
        "v_smfmac_f32_16x16x64_bf16 v[252:255], v[240:243], v[160:167], %[sidx]\n\t"
        "v_smfmac_f32_16x16x64_bf16 v[252:255], v[244:247], v[168:175], %[sidx]\n\t"
        "s_nop 6\n\t"
        "s_cbranch_vccnz Lnan%=\n\t"
        "Lok%=:\n\t"
        "v_add_f32 v252, v252, v254\n\t"
        "v_add_f32 v253, v253, v255\n\t"
        "v_cvt_pk_bf16_f32 v201, v252, v253\n\t"
        "ds_write_b32 %[ow], v201 offset:3072\n\t"
        "s_barrier\n\t"
        "s_setprio 0\n\t"
        "ds_read_b32 v207, v206 offset:25104\n\t"
        "s_waitcnt lgkmcnt(0)\n\t"
        "v_cmp_eq_u32 vcc, %[rnd], v207\n\t"
        "s_barrier\n\t"
        "s_cbranch_vccnz Ltop%=\n\t"
        "s_branch Lend%=\n\t"
        "Lnan%=:\n\t"
        "v_max_u32 v207, v208, v209\n\t"
        "v_max3_u32 v207, v207, v210, v211\n\t"
        "v_max3_u32 v207, v207, v212, v213\n\t"
        "v_max3_u32 v207, v207, v214, v215\n\t"
        "v_max3_u32 v207, v207, v216, v217\n\t"
        "v_max3_u32 v207, v207, v218, v219\n\t"
        "v_max3_u32 v207, v207, v220, v221\n\t"
        "v_max3_u32 v207, v207, v222, v223\n\t"
        "v_max3_u32 v207, v207, v224, v225\n\t"
        "v_max3_u32 v207, v207, v226, v227\n\t"
        "v_max3_u32 v207, v207, v228, v229\n\t"
        "v_max3_u32 v207, v207, v230, v231\n\t"
        "v_max3_u32 v207, v207, v232, v233\n\t"
        "v_max3_u32 v207, v207, v234, v235\n\t"
        "v_max3_u32 v207, v207, v236, v237\n\t"
        "v_max3_u32 v207, v207, v238, v239\n\t"
        "v_max3_u32 v207, v207, v240, v241\n\t"
        "v_max3_u32 v207, v207, v242, v243\n\t"
        "v_max3_u32 v207, v207, v244, v245\n\t"
        "v_max3_u32 v207, v207, v246, v247\n\t"
        "v_cmp_eq_u32 vcc, 0xffbfffbf, v207\n\t"
        "v_mov_b32 v207, %[rnd]\n\t"
        "s_cbranch_vccz Lok%=\n\t"
        "ds_write_b32 v206, v207 offset:25104\n\t"
        "s_waitcnt lgkmcnt(0)\n\t"
        "s_barrier\n\t"
        "s_barrier\n\t"
        "s_branch Ltop%=\n\t"
        "Lend%=:\n\t"
        : [rnd] "+&s"(rnd)
        : [w0] "{v[16:47]}"(W[0]), [w1] "{v[48:79]}"(W[1]), [w2] "{v[80:111]}"(W[2]), [w3] "{v[112:143]}"(W[3]), [w4] "{v[144:175]}"(W[4]), [va] "v"(va), [sidx] "v"(sidx), [ow] "v"(ow), [lb] "s"(lb)
        : "v200", "v201", "v202", "v203", "v204", "v205", "v206", "v207", "v208", "v209", "v210", "v211", "v212", "v213", "v214", "v215", "v216", "v217", "v218", "v219", "v220", "v221", "v222", "v223", "v224", "v225", "v226", "v227", "v228", "v229", "v230", "v231", "v232", "v233", "v234", "v235", "v236", "v237", "v238", "v239", "v240", "v241", "v242", "v243", "v244", "v245", "v246", "v247", "v248", "v249", "v250", "v251", "v252", "v253", "v254", "v255", "vcc", "scc", "memory");
}

__device__ __forceinline__ void s_k3(const u32x32 (&W)[5], u32& rnd, u32 va, u32 sidx, u32 ow, u32 lb, u32x4& p0, u32& p1, u32 ka, u32 qa, u32 ma, u32 ktr, u32 o0, u32 o1, float ce, u32x4 rs, u32 fo) {
    asm volatile(
        "v_mov_b64 v[248:249], 0\n\t"
        "v_mov_b64 v[250:251], 0\n\t"
        "v_mov_b64 v[252:253], 0\n\t"
        "v_mov_b64 v[254:255], 0\n\t"
        "v_mov_b32 v200, 0xffbfffbf\n\t"
        "v_mov_b32 v206, %[lb]\n\t"
        "s_add_u32 %[rnd], %[rnd], 1\n\t"
        "s_setprio 1\n\t"
        "s_waitcnt vmcnt(0) lgkmcnt(0)\n\t"
        "s_barrier\n\t"
        "buffer_store_dwordx4 v[192:195], %[o0], %[rs], 0 offen sc1\n\t"
        "buffer_store_dword v196, %[o1], %[rs], 0 offen sc1\n\t"
        "s_branch Lmk%=\n\t"
        "Ltop%=:\n\t"
        "v_mov_b64 v[248:249], 0\n\t"
        "v_mov_b64 v[250:251], 0\n\t"
        "v_mov_b64 v[252:253], 0\n\t"
        "v_mov_b64 v[254:255], 0\n\t"
        "v_mov_b32 v200, 0xffbfffbf\n\t"
        "v_mov_b32 v206, %[lb]\n\t"
        "s_add_u32 %[rnd], %[rnd], 1\n\t"
        "s_setprio 1\n\t"
        "Lmk%=:\n\t"
        "ds_write_b32 %[ow], v200\n\t"
        "ds_write_b32 %[ow], v200 offset:3072\n\t"
        "s_waitcnt lgkmcnt(0)\n\t"
        "s_barrier\n\t"
        "ds_read_b128 v[208:211], %[va] offset:0\n\t"
        "ds_read_b128 v[212:215], %[va] offset:1024\n\t"
        "ds_read_b128 v[216:219], %[va] offset:2048\n\t"
        "ds_read_b128 v[220:223], %[va] offset:3072\n\t"
        "ds_read_b128 v[224:227], %[va] offset:4096\n\t"
        "ds_read_b128 v[228:231], %[va] offset:5120\n\t"
        "ds_read_b128 v[232:235], %[va] offset:6144\n\t"
        "ds_read_b128 v[236:239], %[va] offset:7168\n\t"
        "ds_read_b128 v[240:243], %[va] offset:8192\n\t"
        "ds_read_b128 v[244:247], %[va] offset:10240\n\t"
        "s_waitcnt lgkmcnt(9)\n\t"
        "v_smfmac_f32_16x16x64_bf16 v[248:251], v[208:211], v[16:23], %[sidx]\n\t"
        "s_waitcnt lgkmcnt(8)\n\t"
        "v_smfmac_f32_16x16x64_bf16 v[248:251], v[212:215], v[24:31], %[sidx]\n\t"
        "s_waitcnt lgkmcnt(7)\n\t"
        "v_smfmac_f32_16x16x64_bf16 v[248:251], v[216:219], v[32:39], %[sidx]\n\t"
        "s_waitcnt lgkmcnt(6)\n\t"
        "v_smfmac_f32_16x16x64_bf16 v[248:251], v[220:223], v[40:47], %[sidx]\n\t"
        "s_waitcnt lgkmcnt(5)\n\t"
        "v_smfmac_f32_16x16x64_bf16 v[248:251], v[224:227], v[48:55], %[sidx]\n\t"
        "s_waitcnt lgkmcnt(4)\n\t"
        "v_smfmac_f32_16x16x64_bf16 v[248:251], v[228:231], v[56:63], %[sidx]\n\t"
        "s_waitcnt lgkmcnt(3)\n\t"
        "v_smfmac_f32_16x16x64_bf16 v[248:251], v[232:235], v[64:71], %[sidx]\n\t"
        "s_waitcnt lgkmcnt(2)\n\t"
        "v_smfmac_f32_16x16x64_bf16 v[248:251], v[236:239], v[72:79], %[sidx]\n\t"
        "s_waitcnt lgkmcnt(1)\n\t"
        "v_smfmac_f32_16x16x64_bf16 v[248:251], v[240:243], v[80:87], %[sidx]\n\t"
        "s_waitcnt lgkmcnt(0)\n\t"
        "v_smfmac_f32_16x16x64_bf16 v[248:251], v[244:247], v[88:95], %[sidx]\n\t"
        "v_smfmac_f32_16x16x64_bf16 v[252:255], v[208:211], v[96:103], %[sidx]\n\t"
        "v_smfmac_f32_16x16x64_bf16 v[252:255], v[212:215], v[104:111], %[sidx]\n\t"
        "v_smfmac_f32_16x16x64_bf16 v[252:255], v[216:219], v[112:119], %[sidx]\n\t"
        "v_add_f32 v248, v248, v250\n\t"
        "v_smfmac_f32_16x16x64_bf16 v[252:255], v[220:223], v[120:127], %[sidx]\n\t"
        "v_add_f32 v249, v249, v251\n\t"
        "v_smfmac_f32_16x16x64_bf16 v[252:255], v[224:227], v[128:135], %[sidx]\n\t"
        "v_cvt_pk_bf16_f32 v200, v248, v249\n\t"
        "v_smfmac_f32_16x16x64_bf16 v[252:255], v[228:231], v[136:143], %[sidx]\n\t"
        "ds_write_b32 %[ow], v200\n\t"
        "v_smfmac_f32_16x16x64_bf16 v[252:255], v[232:235], v[144:151], %[sidx]\n\t"
        "v_cmp_u_f32 vcc, v248, v249\n\t"
        "v_smfmac_f32_16x16x64_bf16 v[252:255], v[236:239], v[152:159], %[sidx]\n\t"
        "v_smfmac_f32_16x16x64_bf16 v[252:255], v[240:243], v[160:167], %[sidx]\n\t"
        "v_smfmac_f32_16x16x64_bf16 v[252:255], v[244:247], v[168:175], %[sidx]\n\t"
        "s_nop 6\n\t"
        "s_cbranch_vccnz Lnan%=\n\t"
        "Lok%=:\n\t"
        "v_add_f32 v252, v252, v254\n\t"
        "v_add_f32 v253, v253, v255\n\t"
        "v_cvt_pk_bf16_f32 v201, v252, v253\n\t"
        "ds_write_b32 %[ow], v201 offset:3072\n\t"
        "s_barrier\n\t"
        "ds_read_b128 v[240:243], %[ma] offset:0\n\t"
        "ds_read_b128 v[244:247], %[ma] offset:1024\n\t"
        "ds_read_b128 v[176:179], %[qa] offset:0\n\t"
        "ds_read_b128 v[180:183], %[qa] offset:1024\n\t"
        "ds_read_b128 v[184:187], %[qa] offset:2048\n\t"
        "ds_read_b128 v[188:191], %[qa] offset:3072\n\t"
        "ds_read_b128 v[208:211], %[ka] offset:0\n\t"
        "ds_read_b128 v[212:215], %[ka] offset:1024\n\t"
        "ds_read_b128 v[216:219], %[ka] offset:2048\n\t"
        "ds_read_b128 v[220:223], %[ka] offset:3072\n\t"
        "ds_read_b128 v[224:227], %[ka] offset:4096\n\t"
        "ds_read_b128 v[228:231], %[ka] offset:5120\n\t"
        "ds_read_b128 v[232:235], %[ka] offset:6144\n\t"
        "ds_read_b128 v[236:239], %[ka] offset:7168\n\t"
        "s_waitcnt lgkmcnt(7)\n\t"
        "v_mfma_f32_16x16x32_bf16 v[240:243], v[208:211], v[176:179], v[240:243]\n\t"
        "s_waitcnt lgkmcnt(6)\n\t"
        "v_mfma_f32_16x16x32_bf16 v[240:243], v[212:215], v[180:183], v[240:243]\n\t"
        "s_waitcnt lgkmcnt(5)\n\t"
        "v_mfma_f32_16x16x32_bf16 v[240:243], v[216:219], v[184:187], v[240:243]\n\t"
        "s_waitcnt lgkmcnt(4)\n\t"
        "v_mfma_f32_16x16x32_bf16 v[240:243], v[220:223], v[188:191], v[240:243]\n\t"
        "s_waitcnt lgkmcnt(3)\n\t"
        "v_mfma_f32_16x16x32_bf16 v[244:247], v[224:227], v[176:179], v[244:247]\n\t"
        "s_waitcnt lgkmcnt(2)\n\t"
        "v_mfma_f32_16x16x32_bf16 v[244:247], v[228:231], v[180:183], v[244:247]\n\t"
        "s_waitcnt lgkmcnt(1)\n\t"
        "v_mfma_f32_16x16x32_bf16 v[244:247], v[232:235], v[184:187], v[244:247]\n\t"
        "s_waitcnt lgkmcnt(0)\n\t"
        "v_mfma_f32_16x16x32_bf16 v[244:247], v[236:239], v[188:191], v[244:247]\n\t"
        "s_setprio 0\n\t"
        "ds_read_b32 v207, v206 offset:25104\n\t"
        "s_waitcnt lgkmcnt(0)\n\t"
        "v_cmp_eq_u32 vcc, %[rnd], v207\n\t"
        "s_barrier\n\t"
        "s_cbranch_vccnz Ltop%=\n\t"
        "ds_read_b64_tr_b16 v[208:209], %[ktr] offset:0\n\t"
        "ds_read_b64_tr_b16 v[210:211], %[ktr] offset:768\n\t"
        "ds_read_b64_tr_b16 v[212:213], %[ktr] offset:1536\n\t"
        "ds_read_b64_tr_b16 v[214:215], %[ktr] offset:2304\n\t"
        "ds_read_b64_tr_b16 v[216:217], %[ktr] offset:3072\n\t"
        "ds_read_b64_tr_b16 v[218:219], %[ktr] offset:3840\n\t"
        "ds_read_b64_tr_b16 v[220:221], %[ktr] offset:4608\n\t"
        "ds_read_b64_tr_b16 v[222:223], %[ktr] offset:5376\n\t"
        "v_mov_b32 v200, 1\n\t"
        "s_waitcnt vmcnt(0)\n\t"
        "buffer_store_dword v200, %[fo], %[rs], 0 offen sc1\n\t"
        "s_waitcnt lgkmcnt(6)\n\t"
        "v_mfma_f32_16x16x32_bf16 v[248:251], v[208:211], v[176:179], 0\n\t"
        "s_waitcnt lgkmcnt(4)\n\t"
        "v_mfma_f32_16x16x32_bf16 v[248:251], v[212:215], v[180:183], v[248:251]\n\t"
        "s_waitcnt lgkmcnt(2)\n\t"
        "v_mfma_f32_16x16x32_bf16 v[248:251], v[216:219], v[184:187], v[248:251]\n\t"
        "s_waitcnt lgkmcnt(0)\n\t"
        "v_mfma_f32_16x16x32_bf16 v[248:251], v[220:223], v[188:191], v[248:251]\n\t"
        "v_max3_f32 v253, v240, v241, v242\n\t"
        "v_max3_f32 v253, v253, v243, v244\n\t"
        "v_max3_f32 v253, v253, v245, v246\n\t"
        "v_max_f32 v253, v253, v247\n\t"
        "s_nop 3\n\t"
        "v_mul_f32 v248, %[ce], v248\n\t"
        "v_mul_f32 v249, %[ce], v249\n\t"
        "v_max3_f32 v253, v253, v248, v249\n\t"
        "v_mov_b32 v254, v253\n\t"
        "s_nop 1\n\t"
        "v_permlane16_swap_b32 v253, v254\n\t"
        "v_max_f32 v253, v253, v254\n\t"
        "v_mov_b32 v254, v253\n\t"
        "s_nop 1\n\t"
        "v_permlane32_swap_b32 v253, v254\n\t"
        "v_max_f32 v253, v253, v254\n\t"
        "v_sub_f32 v240, v240, v253\n\t"
        "v_sub_f32 v241, v241, v253\n\t"
        "v_sub_f32 v242, v242, v253\n\t"
        "v_sub_f32 v243, v243, v253\n\t"
        "v_sub_f32 v244, v244, v253\n\t"
        "v_sub_f32 v245, v245, v253\n\t"
        "v_sub_f32 v246, v246, v253\n\t"
        "v_sub_f32 v247, v247, v253\n\t"
        "v_sub_f32 v248, v248, v253\n\t"
        "v_sub_f32 v249, v249, v253\n\t"
        "v_exp_f32 v240, v240\n\t"
        "v_exp_f32 v241, v241\n\t"
        "v_exp_f32 v242, v242\n\t"
        "v_exp_f32 v243, v243\n\t"
        "v_exp_f32 v244, v244\n\t"
        "v_exp_f32 v245, v245\n\t"
        "v_exp_f32 v246, v246\n\t"
        "v_exp_f32 v247, v247\n\t"
        "v_exp_f32 v248, v248\n\t"
        "v_exp_f32 v249, v249\n\t"
        "v_pk_add_f32 v[252:253], v[240:241], v[242:243]\n\t"
        "v_pk_add_f32 v[254:255], v[244:245], v[246:247]\n\t"
        "v_pk_add_f32 v[252:253], v[252:253], v[248:249]\n\t"
        "v_pk_add_f32 v[252:253], v[252:253], v[254:255]\n\t"
        "v_add_f32 v252, v252, v253\n\t"
        "v_mov_b32 v253, v252\n\t"
        "s_nop 1\n\t"
        "v_permlane16_swap_b32 v252, v253\n\t"
        "v_add_f32 v252, v252, v253\n\t"
        "v_mov_b32 v253, v252\n\t"
        "s_nop 1\n\t"
        "v_permlane32_swap_b32 v252, v253\n\t"
        "v_add_f32 v252, v252, v253\n\t"
        "v_rcp_f32 v252, v252\n\t"
        "s_nop 0\n\t"
        "v_pk_mul_f32 v[240:241], v[240:241], v[252:253] op_sel_hi:[1,0]\n\t"
        "v_pk_mul_f32 v[242:243], v[242:243], v[252:253] op_sel_hi:[1,0]\n\t"
        "v_pk_mul_f32 v[244:245], v[244:245], v[252:253] op_sel_hi:[1,0]\n\t"
        "v_pk_mul_f32 v[246:247], v[246:247], v[252:253] op_sel_hi:[1,0]\n\t"
        "v_pk_mul_f32 v[248:249], v[248:249], v[252:253] op_sel_hi:[1,0]\n\t"
        "v_cvt_pk_bf16_f32 v192, v240, v241\n\t"
        "v_cvt_pk_bf16_f32 v193, v242, v243\n\t"
        "v_cvt_pk_bf16_f32 v194, v244, v245\n\t"
        "v_cvt_pk_bf16_f32 v195, v246, v247\n\t"
        "v_cvt_pk_bf16_f32 v196, v248, v249\n\t"
        "s_branch Lend%=\n\t"
        "Lnan%=:\n\t"
        "v_max_u32 v207, v208, v209\n\t"
        "v_max3_u32 v207, v207, v210, v211\n\t"
        "v_max3_u32 v207, v207, v212, v213\n\t"
        "v_max3_u32 v207, v207, v214, v215\n\t"
        "v_max3_u32 v207, v207, v216, v217\n\t"
        "v_max3_u32 v207, v207, v218, v219\n\t"
        "v_max3_u32 v207, v207, v220, v221\n\t"
        "v_max3_u32 v207, v207, v222, v223\n\t"
        "v_max3_u32 v207, v207, v224, v225\n\t"
        "v_max3_u32 v207, v207, v226, v227\n\t"
        "v_max3_u32 v207, v207, v228, v229\n\t"
        "v_max3_u32 v207, v207, v230, v231\n\t"
        "v_max3_u32 v207, v207, v232, v233\n\t"
        "v_max3_u32 v207, v207, v234, v235\n\t"
        "v_max3_u32 v207, v207, v236, v237\n\t"
        "v_max3_u32 v207, v207, v238, v239\n\t"
        "v_max3_u32 v207, v207, v240, v241\n\t"
        "v_max3_u32 v207, v207, v242, v243\n\t"
        "v_max3_u32 v207, v207, v244, v245\n\t"
        "v_max3_u32 v207, v207, v246, v247\n\t"
        "v_cmp_eq_u32 vcc, 0xffbfffbf, v207\n\t"
        "v_mov_b32 v207, %[rnd]\n\t"
        "s_cbranch_vccz Lok%=\n\t"
        "ds_write_b32 v206, v207 offset:25104\n\t"
        "s_waitcnt lgkmcnt(0)\n\t"
        "s_barrier\n\t"
        "s_barrier\n\t"
        "s_branch Ltop%=\n\t"
        "Lend%=:\n\t"
        : [rnd] "+&s"(rnd), [p0] "+{v[192:195]}"(p0), [p1] "+{v196}"(p1)
        : [w0] "{v[16:47]}"(W[0]), [w1] "{v[48:79]}"(W[1]), [w2] "{v[80:111]}"(W[2]), [w3] "{v[112:143]}"(W[3]), [w4] "{v[144:175]}"(W[4]), [va] "v"(va), [sidx] "v"(sidx), [ow] "v"(ow), [lb] "s"(lb), [ka] "v"(ka), [qa] "v"(qa), [ma] "v"(ma), [ktr] "v"(ktr), [o0] "v"(o0), [o1] "v"(o1), [ce] "s"(ce), [rs] "s"(rs), [fo] "v"(fo)
        : "v176", "v177", "v178", "v179", "v180", "v181", "v182", "v183", "v184", "v185", "v186", "v187", "v188", "v189", "v190", "v191", "v200", "v201", "v202", "v203", "v204", "v205", "v206", "v207", "v208", "v209", "v210", "v211", "v212", "v213", "v214", "v215", "v216", "v217", "v218", "v219", "v220", "v221", "v222", "v223", "v224", "v225", "v226", "v227", "v228", "v229", "v230", "v231", "v232", "v233", "v234", "v235", "v236", "v237", "v238", "v239", "v240", "v241", "v242", "v243", "v244", "v245", "v246", "v247", "v248", "v249", "v250", "v251", "v252", "v253", "v254", "v255", "vcc", "scc", "memory");
}

__device__ __forceinline__ void s_l(u32& rnd, u32& redo, u32 lb) {
    asm volatile(
        "v_mov_b32 v206, %[lb]\n\t"
        "s_add_u32 %[rnd], %[rnd], 1\n\t"
        "s_mov_b32 %[redo], 1\n\t"
        "s_barrier\n\t"
        "s_barrier\n\t"
        "s_setprio 0\n\t"
        "ds_read_b32 v207, v206 offset:25104\n\t"
        "s_waitcnt lgkmcnt(0)\n\t"
        "v_cmp_eq_u32 vcc, %[rnd], v207\n\t"
        "s_barrier\n\t"
        "s_cbranch_vccnz Lend%=\n\t"
        "s_mov_b32 %[redo], 0\n\t"
        "Lend%=:\n\t"
        : [rnd] "+&s"(rnd), [redo] "=&s"(redo)
        : [lb] "s"(lb)
        : "v206", "v207", "vcc", "scc", "memory");
}

__device__ __forceinline__ void s_e(const u32x32 (&W)[5], u32& rnd, u32 va, u32 sidx, u32 ow, u32 lb, u32 kxa, u32 qtr, u32 qo, u32 eo, u32 ew, float lim, u32x4 rs, u32 so STP) {
    asm volatile(
        "s_add_u32 s92, %[so], 0x1000\n\t"
        "s_add_u32 s93, %[so], 0x4000\n\t"
        "s_add_u32 s94, %[so], 0x5000\n\t"
        "v_mov_b64 v[248:249], 0\n\t"
        "v_mov_b64 v[250:251], 0\n\t"
        "v_mov_b64 v[252:253], 0\n\t"
        "v_mov_b64 v[254:255], 0\n\t"
        "v_mov_b32 v200, 0xffbfffbf\n\t"
        "v_mov_b32 v206, %[lb]\n\t"
        "s_add_u32 %[rnd], %[rnd], 1\n\t"
        "s_setprio 3\n\t"
        "s_mov_b32 s95, 1\n\t"
        "s_waitcnt vmcnt(0) lgkmcnt(0)\n\t"
        "s_barrier\n\t"
        "ds_read_b128 v[176:179], %[kxa] offset:0\n\t"
        "ds_read_b128 v[180:183], %[kxa] offset:1024\n\t"
        "ds_read_b128 v[184:187], %[kxa] offset:2048\n\t"
        "ds_read_b128 v[188:191], %[kxa] offset:3072\n\t"
        "s_branch Lmk%=\n\t"
        "Ltop%=:\n\t"
        "v_mov_b64 v[248:249], 0\n\t"
        "v_mov_b64 v[250:251], 0\n\t"
        "v_mov_b64 v[252:253], 0\n\t"
        "v_mov_b64 v[254:255], 0\n\t"
        "v_mov_b32 v200, 0xffbfffbf\n\t"
        "v_mov_b32 v206, %[lb]\n\t"
        "s_add_u32 %[rnd], %[rnd], 1\n\t"
        "s_setprio 3\n\t"
        "s_mov_b32 s95, 1\n\t"
        "Lmk%=:\n\t"
        "ds_write_b32 %[ow], v200\n\t"
        "ds_write_b32 %[ow], v200 offset:3072\n\t"
        "s_waitcnt lgkmcnt(0)\n\t"
        "s_barrier\n\t"
        "ds_read_b128 v[208:211], %[va] offset:0\n\t"
        "ds_read_b128 v[212:215], %[va] offset:1024\n\t"
        "ds_read_b128 v[216:219], %[va] offset:2048\n\t"
        "ds_read_b128 v[220:223], %[va] offset:3072\n\t"
        "ds_read_b128 v[224:227], %[va] offset:4096\n\t"
        "ds_read_b128 v[228:231], %[va] offset:5120\n\t"
        "s_waitcnt lgkmcnt(5)\n\t"
        "v_smfmac_f32_16x16x64_bf16 v[248:251], v[208:211], v[16:23], %[sidx]\n\t"
        "ds_read_b128 v[232:235], %[va] offset:6144\n\t"
        "v_smfmac_f32_16x16x64_bf16 v[252:255], v[208:211], v[96:103], %[sidx]\n\t"
        "s_waitcnt lgkmcnt(5)\n\t"
        "v_smfmac_f32_16x16x64_bf16 v[248:251], v[212:215], v[24:31], %[sidx]\n\t"
        "ds_read_b128 v[236:239], %[va] offset:7168\n\t"
        "v_smfmac_f32_16x16x64_bf16 v[252:255], v[212:215], v[104:111], %[sidx]\n\t"
        "s_waitcnt lgkmcnt(5)\n\t"
        "v_smfmac_f32_16x16x64_bf16 v[248:251], v[216:219], v[32:39], %[sidx]\n\t"
        "ds_read_b128 v[240:243], %[va] offset:8192\n\t"
        "v_smfmac_f32_16x16x64_bf16 v[252:255], v[216:219], v[112:119], %[sidx]\n\t"
        "s_waitcnt lgkmcnt(5)\n\t"
        "v_smfmac_f32_16x16x64_bf16 v[248:251], v[220:223], v[40:47], %[sidx]\n\t"
        "ds_read_b128 v[244:247], %[va] offset:10240\n\t"
        "v_smfmac_f32_16x16x64_bf16 v[252:255], v[220:223], v[120:127], %[sidx]\n\t"
        "s_waitcnt lgkmcnt(5)\n\t"
        "v_smfmac_f32_16x16x64_bf16 v[248:251], v[224:227], v[48:55], %[sidx]\n\t"
        "s_waitcnt lgkmcnt(4)\n\t"
        "v_smfmac_f32_16x16x64_bf16 v[248:251], v[228:231], v[56:63], %[sidx]\n\t"
        "s_waitcnt lgkmcnt(3)\n\t"
        "v_smfmac_f32_16x16x64_bf16 v[248:251], v[232:235], v[64:71], %[sidx]\n\t"
        "s_waitcnt lgkmcnt(2)\n\t"
        "v_smfmac_f32_16x16x64_bf16 v[248:251], v[236:239], v[72:79], %[sidx]\n\t"
        "s_waitcnt lgkmcnt(1)\n\t"
        "v_smfmac_f32_16x16x64_bf16 v[248:251], v[240:243], v[80:87], %[sidx]\n\t"
        "s_waitcnt lgkmcnt(0)\n\t"
        "v_smfmac_f32_16x16x64_bf16 v[248:251], v[244:247], v[88:95], %[sidx]\n\t"
        "v_smfmac_f32_16x16x64_bf16 v[252:255], v[224:227], v[128:135], %[sidx]\n\t"
        "v_smfmac_f32_16x16x64_bf16 v[252:255], v[228:231], v[136:143], %[sidx]\n\t"
        "v_add_f32 v248, v248, v250\n\t"
        "v_smfmac_f32_16x16x64_bf16 v[252:255], v[232:235], v[144:151], %[sidx]\n\t"
        "v_add_f32 v249, v249, v251\n\t"
        "v_smfmac_f32_16x16x64_bf16 v[252:255], v[236:239], v[152:159], %[sidx]\n\t"
        "v_cvt_pk_bf16_f32 v200, v248, v249\n\t"
        "v_smfmac_f32_16x16x64_bf16 v[252:255], v[240:243], v[160:167], %[sidx]\n\t"
        "ds_write_b32 %[ow], v200\n\t"
        "v_smfmac_f32_16x16x64_bf16 v[252:255], v[244:247], v[168:175], %[sidx]\n\t"
        "v_cmp_u_f32 vcc, v248, v249\n\t"
        "s_nop 2\n\t"
        "ds_read_b64_tr_b16 v[192:193], %[qtr] offset:0\n\t"
        "ds_read_b64_tr_b16 v[194:195], %[qtr] offset:768\n\t"
        "ds_read_b64_tr_b16 v[196:197], %[qtr] offset:1536\n\t"
        "ds_read_b64_tr_b16 v[198:199], %[qtr] offset:2304\n\t"
        "s_cbranch_vccnz Lnan%=\n\t"
        "Lok%=:\n\t"
        "v_add_f32 v252, v252, v254\n\t"
        "v_add_f32 v253, v253, v255\n\t"
        "v_cvt_pk_bf16_f32 v201, v252, v253\n\t"
        "ds_write_b32 %[ow], v201 offset:3072\n\t"
        "s_waitcnt lgkmcnt(3)\n\t"
        "v_mfma_f32_16x16x32_bf16 v[248:251], v[176:179], v[192:195], 0\n\t"
        "s_waitcnt lgkmcnt(1)\n\t"
        "v_mfma_f32_16x16x32_bf16 v[248:251], v[180:183], v[196:199], v[248:251]\n\t"
        "s_waitcnt lgkmcnt(0)\n\t"
        "s_nop 1\n\t"
        "Lre%=:\n\t"
        "ds_read_b64_tr_b16 v[208:209], %[qtr] offset:3072\n\t"
        "ds_read_b64_tr_b16 v[210:211], %[qtr] offset:3840\n\t"
        "ds_read_b64_tr_b16 v[212:213], %[qtr] offset:4608\n\t"
        "ds_read_b64_tr_b16 v[214:215], %[qtr] offset:5376\n\t"
        "s_waitcnt lgkmcnt(2)\n\t"
        "v_mfma_f32_16x16x32_bf16 v[248:251], v[184:187], v[208:211], v[248:251]\n\t"
        "s_waitcnt lgkmcnt(0)\n\t"
        "v_mfma_f32_16x16x32_bf16 v[248:251], v[188:191], v[212:215], v[248:251]\n\t"
        "s_nop 7\n\t"
        "v_mov_b32_dpp v248, v250 quad_perm:[0,1,2,3] row_mask:0xf bank_mask:0xc\n\t"
        "v_mov_b32_dpp v249, v251 quad_perm:[0,1,2,3] row_mask:0xf bank_mask:0xc\n\t"
        "v_exp_f32 v202, v248\n\t"
        "v_exp_f32 v203, v249\n\t"
        "v_max_f32_e64 v204, |v248|, |v249|\n\t"
        "v_cvt_pk_bf16_f32 v202, v202, v203\n\t"
        "v_cmpx_le_f32 vcc, v204, %[lim]\n\t"
        "buffer_store_dword v202, %[eo], %[rs], %[so] offen sc1\n\t"
        "buffer_store_dword v202, %[eo], %[rs], s92 offen sc1\n\t"
        "buffer_store_dword v202, %[eo], %[rs], s93 offen sc1\n\t"
        "buffer_store_dword v202, %[eo], %[rs], s94 offen sc1\n\t"
        "s_mov_b64 exec, -1\n\t"
        "v_cndmask_b32 v202, -1, v202, vcc\n\t"
        "s_cmp_lg_u64 vcc, -1\n\t"
        "ds_write_b32 %[ew], v202\n\t"
        "s_cbranch_scc1 Lag%=\n\t"
        "Lgo%=:\n\t"
        "s_waitcnt lgkmcnt(0)\n\t"
        "s_barrier\n\t"
        "s_setprio 0\n\t"
        "ds_read_b32 v207, v206 offset:25104\n\t"
        "s_waitcnt lgkmcnt(0)\n\t"
        "v_cmp_eq_u32 vcc, %[rnd], v207\n\t"
        "s_barrier\n\t"
        "s_cbranch_vccnz Ltop%=\n\t"
        "s_branch Lend%=\n\t"
        "Lag%=:\n\t"
        "s_sub_u32 s95, s95, 1\n\t"
        "s_cbranch_scc1 Lgo%=\n\t"
        "Lqw%=:\n\t"
        "v_subrev_u32 v205, %[qo], %[ow]\n\t"
        "ds_read_b32 v192, v205 offset:0\n\t"
        "ds_read_b32 v193, v205 offset:768\n\t"
        "ds_read_b32 v194, v205 offset:1536\n\t"
        "ds_read_b32 v195, v205 offset:2304\n\t"
        "ds_read_b32 v196, v205 offset:3072\n\t"
        "ds_read_b32 v197, v205 offset:3840\n\t"
        "ds_read_b32 v198, v205 offset:4608\n\t"
        "ds_read_b32 v199, v205 offset:5376\n\t"
        "ds_read_b32 v207, v206 offset:25104\n\t"
        "s_waitcnt lgkmcnt(0)\n\t"
        "v_cmp_eq_u32 vcc, %[rnd], v207\n\t"
        "s_cbranch_vccnz Lgo%=\n\t"
        "v_max_u32 v207, v192, v193\n\t"
        "v_max3_u32 v207, v207, v194, v195\n\t"
        "v_max3_u32 v207, v207, v196, v197\n\t"
        "v_max3_u32 v207, v207, v198, v199\n\t"
        "v_cmp_eq_u32 vcc, 0xffbfffbf, v207\n\t"
        "s_cbranch_vccnz Lqw%=\n\t"
        "ds_read_b64_tr_b16 v[192:193], %[qtr] offset:0\n\t"
        "ds_read_b64_tr_b16 v[194:195], %[qtr] offset:768\n\t"
        "ds_read_b64_tr_b16 v[196:197], %[qtr] offset:1536\n\t"
        "ds_read_b64_tr_b16 v[198:199], %[qtr] offset:2304\n\t"
        "s_waitcnt lgkmcnt(2)\n\t"
        "v_mfma_f32_16x16x32_bf16 v[248:251], v[176:179], v[192:195], 0\n\t"
        "s_waitcnt lgkmcnt(0)\n\t"
        "v_mfma_f32_16x16x32_bf16 v[248:251], v[180:183], v[196:199], v[248:251]\n\t"
        "s_branch Lre%=\n\t"
        "Lnan%=:\n\t"
        "v_max_u32 v207, v208, v209\n\t"
        "v_max3_u32 v207, v207, v210, v211\n\t"
        "v_max3_u32 v207, v207, v212, v213\n\t"
        "v_max3_u32 v207, v207, v214, v215\n\t"
        "v_max3_u32 v207, v207, v216, v217\n\t"
        "v_max3_u32 v207, v207, v218, v219\n\t"
        "v_max3_u32 v207, v207, v220, v221\n\t"
        "v_max3_u32 v207, v207, v222, v223\n\t"
        "v_max3_u32 v207, v207, v224, v225\n\t"
        "v_max3_u32 v207, v207, v226, v227\n\t"
        "v_max3_u32 v207, v207, v228, v229\n\t"
        "v_max3_u32 v207, v207, v230, v231\n\t"
        "v_max3_u32 v207, v207, v232, v233\n\t"
        "v_max3_u32 v207, v207, v234, v235\n\t"
        "v_max3_u32 v207, v207, v236, v237\n\t"
        "v_max3_u32 v207, v207, v238, v239\n\t"
        "v_max3_u32 v207, v207, v240, v241\n\t"
        "v_max3_u32 v207, v207, v242, v243\n\t"
        "v_max3_u32 v207, v207, v244, v245\n\t"
        "v_max3_u32 v207, v207, v246, v247\n\t"
        "v_cmp_eq_u32 vcc, 0xffbfffbf, v207\n\t"
        "v_mov_b32 v207, %[rnd]\n\t"
        "s_cbranch_vccz Lok%=\n\t"
        "ds_write_b32 v206, v207 offset:25104\n\t"
        "s_waitcnt lgkmcnt(0)\n\t"
        "s_barrier\n\t"
        "s_barrier\n\t"
        "s_branch Ltop%=\n\t"
        "Lend%=:\n\t"
        : [rnd] "+&s"(rnd)
        : [w0] "{v[16:47]}"(W[0]), [w1] "{v[48:79]}"(W[1]), [w2] "{v[80:111]}"(W[2]), [w3] "{v[112:143]}"(W[3]), [w4] "{v[144:175]}"(W[4]), [va] "v"(va), [sidx] "v"(sidx), [ow] "v"(ow), [lb] "s"(lb), [kxa] "v"(kxa), [qtr] "v"(qtr), [eo] "v"(eo), [ew] "v"(ew), [lim] "v"(lim), [rs] "s"(rs), [so] "s"(so), [qo] "s"(qo)
        : "s92", "s93", "s94", "s95", "v176", "v177", "v178", "v179", "v180", "v181", "v182", "v183", "v184", "v185", "v186", "v187", "v188", "v189", "v190", "v191", "v192", "v193", "v194", "v195", "v196", "v197", "v198", "v199", "v200", "v201", "v202", "v203", "v204", "v205", "v206", "v207", "v208", "v209", "v210", "v211", "v212", "v213", "v214", "v215", "v216", "v217", "v218", "v219", "v220", "v221", "v222", "v223", "v224", "v225", "v226", "v227", "v228", "v229", "v230", "v231", "v232", "v233", "v234", "v235", "v236", "v237", "v238", "v239", "v240", "v241", "v242", "v243", "v244", "v245", "v246", "v247", "v248", "v249", "v250", "v251", "v252", "v253", "v254", "v255", "vcc", "scc", "memory");
}

__device__ __forceinline__ void s_f(const u32x32 (&W)[5], u32& rnd, u32 va, u32 sidx, u32 ow, u32 lb, u32& bad, u32x4 ones, u32 qtr, u32 ktr, u32 er, u32 po, float c1, float ind0, float ind1, u32x4 rs, u32 so, float ce, float big STP) {
    asm volatile(
        "s_add_u32 s92, %[so], 0x1000\n\t"
        "s_add_u32 s93, %[so], 0x4000\n\t"
        "s_add_u32 s94, %[so], 0x5000\n\t"
        "v_mov_b64 v[248:249], 0\n\t"
        "v_mov_b64 v[250:251], 0\n\t"
        "v_mov_b64 v[252:253], 0\n\t"
        "v_mov_b64 v[254:255], 0\n\t"
        "v_mov_b32 v200, 0xffbfffbf\n\t"
        "v_mov_b32 v206, %[lb]\n\t"
        "s_add_u32 %[rnd], %[rnd], 1\n\t"
        "s_setprio 3\n\t"
        "s_mov_b32 %[bad], 2\n\t"
        "v_mov_b32 v197, 0\n\t"
        "s_waitcnt vmcnt(0) lgkmcnt(0)\n\t"
        "s_barrier\n\t"
        "s_branch Lmk%=\n\t"
        "Ltop%=:\n\t"
        "v_mov_b64 v[248:249], 0\n\t"
        "v_mov_b64 v[250:251], 0\n\t"
        "v_mov_b64 v[252:253], 0\n\t"
        "v_mov_b64 v[254:255], 0\n\t"
        "v_mov_b32 v200, 0xffbfffbf\n\t"
        "v_mov_b32 v206, %[lb]\n\t"
        "s_add_u32 %[rnd], %[rnd], 1\n\t"
        "s_setprio 3\n\t"
        "s_mov_b32 %[bad], 2\n\t"
        "v_mov_b32 v197, 0\n\t"
        "Lmk%=:\n\t"
        "ds_write_b32 %[ow], v200\n\t"
        "ds_write_b32 %[ow], v200 offset:3072\n\t"
        "s_waitcnt lgkmcnt(0)\n\t"
        "s_barrier\n\t"
        "ds_read_b128 v[208:211], %[va] offset:0\n\t"
        "ds_read_b128 v[212:215], %[va] offset:1024\n\t"
        "ds_read_b128 v[216:219], %[va] offset:2048\n\t"
        "ds_read_b128 v[220:223], %[va] offset:3072\n\t"
        "ds_read_b128 v[224:227], %[va] offset:4096\n\t"
        "ds_read_b128 v[228:231], %[va] offset:5120\n\t"
        "s_waitcnt lgkmcnt(5)\n\t"
        "v_smfmac_f32_16x16x64_bf16 v[248:251], v[208:211], v[16:23], %[sidx]\n\t"
        "ds_read_b128 v[232:235], %[va] offset:6144\n\t"
        "v_smfmac_f32_16x16x64_bf16 v[252:255], v[208:211], v[96:103], %[sidx]\n\t"
        "s_waitcnt lgkmcnt(5)\n\t"
        "v_smfmac_f32_16x16x64_bf16 v[248:251], v[212:215], v[24:31], %[sidx]\n\t"
        "ds_read_b128 v[236:239], %[va] offset:7168\n\t"
        "v_smfmac_f32_16x16x64_bf16 v[252:255], v[212:215], v[104:111], %[sidx]\n\t"
        "s_waitcnt lgkmcnt(5)\n\t"
        "v_smfmac_f32_16x16x64_bf16 v[248:251], v[216:219], v[32:39], %[sidx]\n\t"
        "ds_read_b128 v[240:243], %[va] offset:8192\n\t"
        "v_smfmac_f32_16x16x64_bf16 v[252:255], v[216:219], v[112:119], %[sidx]\n\t"
        "s_waitcnt lgkmcnt(5)\n\t"
        "v_smfmac_f32_16x16x64_bf16 v[248:251], v[220:223], v[40:47], %[sidx]\n\t"
        "ds_read_b128 v[244:247], %[va] offset:10240\n\t"
        "v_smfmac_f32_16x16x64_bf16 v[252:255], v[220:223], v[120:127], %[sidx]\n\t"
        "s_waitcnt lgkmcnt(5)\n\t"
        "v_smfmac_f32_16x16x64_bf16 v[248:251], v[224:227], v[48:55], %[sidx]\n\t"
        "s_waitcnt lgkmcnt(4)\n\t"
        "v_smfmac_f32_16x16x64_bf16 v[248:251], v[228:231], v[56:63], %[sidx]\n\t"
        "s_waitcnt lgkmcnt(3)\n\t"
        "v_smfmac_f32_16x16x64_bf16 v[248:251], v[232:235], v[64:71], %[sidx]\n\t"
        "s_waitcnt lgkmcnt(2)\n\t"
        "v_smfmac_f32_16x16x64_bf16 v[248:251], v[236:239], v[72:79], %[sidx]\n\t"
        "s_waitcnt lgkmcnt(1)\n\t"
        "v_smfmac_f32_16x16x64_bf16 v[248:251], v[240:243], v[80:87], %[sidx]\n\t"
        "s_waitcnt lgkmcnt(0)\n\t"
        "v_smfmac_f32_16x16x64_bf16 v[248:251], v[244:247], v[88:95], %[sidx]\n\t"
        "v_smfmac_f32_16x16x64_bf16 v[252:255], v[224:227], v[128:135], %[sidx]\n\t"
        "v_smfmac_f32_16x16x64_bf16 v[252:255], v[228:231], v[136:143], %[sidx]\n\t"
        "v_add_f32 v248, v248, v250\n\t"
        "v_smfmac_f32_16x16x64_bf16 v[252:255], v[232:235], v[144:151], %[sidx]\n\t"
        "v_add_f32 v249, v249, v251\n\t"
        "v_smfmac_f32_16x16x64_bf16 v[252:255], v[236:239], v[152:159], %[sidx]\n\t"
        "v_cvt_pk_bf16_f32 v200, v248, v249\n\t"
        "v_smfmac_f32_16x16x64_bf16 v[252:255], v[240:243], v[160:167], %[sidx]\n\t"
        "ds_write_b32 %[ow], v200\n\t"
        "v_smfmac_f32_16x16x64_bf16 v[252:255], v[244:247], v[168:175], %[sidx]\n\t"
        "v_cmp_u_f32 vcc, v248, v249\n\t"
        "s_nop 6\n\t"
        "s_cbranch_vccnz Lnan%=\n\t"
        "Lok%=:\n\t"
        "v_add_f32 v252, v252, v254\n\t"
        "v_add_f32 v253, v253, v255\n\t"
        "v_cvt_pk_bf16_f32 v201, v252, v253\n\t"
        "ds_write_b32 %[ow], v201 offset:3072\n\t"
        "s_waitcnt lgkmcnt(0)\n\t"
        "s_nop 15\n\t"
        "ds_read_b64_tr_b16 v[180:181], %[qtr] offset:0\n\t"
        "ds_read_b64_tr_b16 v[182:183], %[qtr] offset:768\n\t"
        "ds_read_b64_tr_b16 v[184:185], %[qtr] offset:1536\n\t"
        "ds_read_b64_tr_b16 v[186:187], %[qtr] offset:2304\n\t"
        "ds_read_b64_tr_b16 v[188:189], %[qtr] offset:3072\n\t"
        "ds_read_b64_tr_b16 v[190:191], %[qtr] offset:3840\n\t"
        "ds_read_b64_tr_b16 v[192:193], %[qtr] offset:4608\n\t"
        "ds_read_b64_tr_b16 v[194:195], %[qtr] offset:5376\n\t"
        "s_nop 15\n\t"
        "s_nop 7\n\t"
        "ds_read_b64_tr_b16 v[224:225], %[ktr] offset:0\n\t"
        "ds_read_b64_tr_b16 v[226:227], %[ktr] offset:768\n\t"
        "ds_read_b64_tr_b16 v[228:229], %[ktr] offset:1536\n\t"
        "ds_read_b64_tr_b16 v[230:231], %[ktr] offset:2304\n\t"
        "s_nop 7\n\t"
        "ds_read_b64_tr_b16 v[232:233], %[ktr] offset:4608\n\t"
        "ds_read_b64_tr_b16 v[234:235], %[ktr] offset:5376\n\t"
        "s_waitcnt lgkmcnt(4)\n\t"
        "v_mfma_f32_16x16x32_bf16 v[248:251], v[224:227], v[180:183], 0\n\t"
        "s_waitcnt lgkmcnt(2)\n\t"
        "v_mfma_f32_16x16x32_bf16 v[248:251], v[228:231], v[184:187], v[248:251]\n\t"
        "s_waitcnt lgkmcnt(0)\n\t"
        "v_mfma_f32_16x16x32_bf16 v[248:251], v[232:235], v[192:195], v[248:251]\n\t"
        "s_barrier\n\t"
        "Lre%=:\n\t"
        "ds_read_b64_tr_b16 v[208:209], %[ktr] offset:3072\n\t"
        "ds_read_b64_tr_b16 v[210:211], %[ktr] offset:3840\n\t"
        "ds_read_b128 v[240:243], %[er]\n\t"
        "ds_read_b32 v207, v206 offset:25104\n\t"
        "s_waitcnt lgkmcnt(2)\n\t"
        "v_mfma_f32_16x16x32_bf16 v[248:251], v[208:211], v[188:191], v[248:251]\n\t"
        "s_waitcnt lgkmcnt(1)\n\t"
        "v_mfma_f32_16x16x32_bf16 v[244:247], v[176:179], v[240:243], 0\n\t"
        "s_nop 3\n\t"
        "v_mul_f32 v248, %[ce], v248\n\t"
        "v_mul_f32 v249, %[ce], v249\n\t"
        "v_exp_f32 v250, v248\n\t"
        "v_exp_f32 v251, v249\n\t"
        "s_nop 0\n\t"
        "v_cvt_pk_bf16_f32 v196, v250, v251\n\t"
        "v_fma_f32 v250, v250, %[c1], %[ind0]\n\t"
        "v_fma_f32 v251, v251, %[c1], %[ind1]\n\t"
        "v_mfma_f32_16x16x16_bf16 v[244:247], v[176:177], v[196:197], v[244:247]\n\t"
        "s_nop 7\n\t"
        "v_rcp_f32 v246, v244\n\t"
        "v_cmpx_gt_f32 vcc, %[big], v244\n\t"
        "v_pk_mul_f32 v[250:251], v[250:251], v[246:247] op_sel_hi:[1,0]\n\t"
        "v_cvt_pk_bf16_f32 v250, v250, v251\n\t"
        "buffer_store_dword v250, %[po], %[rs], %[so] offen sc1\n\t"
        "buffer_store_dword v250, %[po], %[rs], s92 offen sc1\n\t"
        "buffer_store_dword v250, %[po], %[rs], s93 offen sc1\n\t"
        "buffer_store_dword v250, %[po], %[rs], s94 offen sc1\n\t"
        "s_mov_b64 exec, -1\n\t"
        "s_cmp_eq_u32 %[bad], 1\n\t"
        "s_cbranch_scc1 Ltw%=\n\t"
        "s_cmp_lg_u64 vcc, -1\n\t"
        "s_cselect_b32 %[bad], 1, 0\n\t"
        "s_waitcnt lgkmcnt(0)\n\t"
        "v_cmp_eq_u32 vcc, %[rnd], v207\n\t"
        "s_barrier\n\t"
        "s_cbranch_vccnz Ltop%=\n\t"
        "s_cbranch_scc0 Lend%=\n\t"
        "ds_read_b64_tr_b16 v[180:181], %[qtr] offset:0\n\t"
        "ds_read_b64_tr_b16 v[182:183], %[qtr] offset:768\n\t"
        "ds_read_b64_tr_b16 v[184:185], %[qtr] offset:1536\n\t"
        "ds_read_b64_tr_b16 v[186:187], %[qtr] offset:2304\n\t"
        "ds_read_b64_tr_b16 v[188:189], %[qtr] offset:3072\n\t"
        "ds_read_b64_tr_b16 v[190:191], %[qtr] offset:3840\n\t"
        "ds_read_b64_tr_b16 v[192:193], %[qtr] offset:4608\n\t"
        "ds_read_b64_tr_b16 v[194:195], %[qtr] offset:5376\n\t"
        "ds_read_b64_tr_b16 v[224:225], %[ktr] offset:0\n\t"
        "ds_read_b64_tr_b16 v[226:227], %[ktr] offset:768\n\t"
        "ds_read_b64_tr_b16 v[228:229], %[ktr] offset:1536\n\t"
        "ds_read_b64_tr_b16 v[230:231], %[ktr] offset:2304\n\t"
        "ds_read_b64_tr_b16 v[232:233], %[ktr] offset:4608\n\t"
        "ds_read_b64_tr_b16 v[234:235], %[ktr] offset:5376\n\t"
        "s_waitcnt lgkmcnt(0)\n\t"
        "v_mfma_f32_16x16x32_bf16 v[248:251], v[224:227], v[180:183], 0\n\t"
        "v_mfma_f32_16x16x32_bf16 v[248:251], v[228:231], v[184:187], v[248:251]\n\t"
        "v_mfma_f32_16x16x32_bf16 v[248:251], v[232:235], v[192:195], v[248:251]\n\t"
        "s_branch Lre%=\n\t"
        "Lnan%=:\n\t"
        "v_max_u32 v207, v208, v209\n\t"
        "v_max3_u32 v207, v207, v210, v211\n\t"
        "v_max3_u32 v207, v207, v212, v213\n\t"
        "v_max3_u32 v207, v207, v214, v215\n\t"
        "v_max3_u32 v207, v207, v216, v217\n\t"
        "v_max3_u32 v207, v207, v218, v219\n\t"
        "v_max3_u32 v207, v207, v220, v221\n\t"
        "v_max3_u32 v207, v207, v222, v223\n\t"
        "v_max3_u32 v207, v207, v224, v225\n\t"
        "v_max3_u32 v207, v207, v226, v227\n\t"
        "v_max3_u32 v207, v207, v228, v229\n\t"
        "v_max3_u32 v207, v207, v230, v231\n\t"
        "v_max3_u32 v207, v207, v232, v233\n\t"
        "v_max3_u32 v207, v207, v234, v235\n\t"
        "v_max3_u32 v207, v207, v236, v237\n\t"
        "v_max3_u32 v207, v207, v238, v239\n\t"
        "v_max3_u32 v207, v207, v240, v241\n\t"
        "v_max3_u32 v207, v207, v242, v243\n\t"
        "v_max3_u32 v207, v207, v244, v245\n\t"
        "v_max3_u32 v207, v207, v246, v247\n\t"
        "v_cmp_eq_u32 vcc, 0xffbfffbf, v207\n\t"
        "v_mov_b32 v207, %[rnd]\n\t"
        "s_cbranch_vccz Lok%=\n\t"
        "ds_write_b32 v206, v207 offset:25104\n\t"
        "s_waitcnt lgkmcnt(0)\n\t"
        "s_barrier\n\t"
        "s_barrier\n\t"
        "s_branch Ltop%=\n\t"
        "Ltw%=:\n\t"
        "s_cmp_lg_u64 vcc, -1\n\t"
        "s_cselect_b32 %[bad], 1, 0\n\t"
        "s_waitcnt lgkmcnt(0)\n\t"
        "Lend%=:\n\t"
        "s_setprio 0\n\t"
        : [rnd] "+&s"(rnd), [bad] "=&s"(bad)
        : [w0] "{v[16:47]}"(W[0]), [w1] "{v[48:79]}"(W[1]), [w2] "{v[80:111]}"(W[2]), [w3] "{v[112:143]}"(W[3]), [w4] "{v[144:175]}"(W[4]), [va] "v"(va), [sidx] "v"(sidx), [ow] "v"(ow), [lb] "s"(lb), [ones] "{v[176:179]}"(ones), [qtr] "v"(qtr), [ktr] "v"(ktr), [er] "v"(er), [po] "v"(po), [c1] "v"(c1), [ind0] "v"(ind0), [ind1] "v"(ind1), [rs] "s"(rs), [so] "s"(so), [ce] "s"(ce), [big] "s"(big)
        : "s92", "s93", "s94", "v180", "v181", "v182", "v183", "v184", "v185", "v186", "v187", "v188", "v189", "v190", "v191", "v192", "v193", "v194", "v195", "v196", "v197", "v198", "v199", "v200", "v201", "v202", "v203", "v204", "v205", "v206", "v207", "v208", "v209", "v210", "v211", "v212", "v213", "v214", "v215", "v216", "v217", "v218", "v219", "v220", "v221", "v222", "v223", "v224", "v225", "v226", "v227", "v228", "v229", "v230", "v231", "v232", "v233", "v234", "v235", "v236", "v237", "v238", "v239", "v240", "v241", "v242", "v243", "v244", "v245", "v246", "v247", "v248", "v249", "v250", "v251", "v252", "v253", "v254", "v255", "vcc", "scc", "memory");
}

__device__ __forceinline__ void s_m(const u32x32 (&W)[5], u32& rnd, u32 va, u32 sidx, u32 ow, u32 lb, u32& left, u32& s, u32& so, u32& tg, u32& kr, u32& c0, u32& r0, u32& ok, u32& cnt, u32 vm, u32 tc, u32 tv, u32 md, u32x4 rs, u32 poll, u32 last STP LGP) {
    asm volatile(
        "s_mov_b32 s94, m0\n\t"
        "Lst%=:\n\t"
        "v_readlane_b32 s60, %[tc], %[s]\n\t"
        "s_add_u32 s92, %[so], 0x8000\n\t"
        "s_add_u32 s93, %[so], 0x10000\n\t"
        "s_mov_b32 m0, %[md]\n\t"
        "s_add_u32 s62, %[s], 1\n\t"
        "s_mov_b32 s58, %[c0]\n\t"
        "s_mov_b32 s59, %[r0]\n\t"
        "s_mov_b32 s63, %[rnd]\n\t"
        "s_setprio 3\n\t"
        "s_add_u32 s61, s60, 0x10400\n\t"
        "s_mov_b32 s96, %[left]\n\t"
        "s_cmp_eq_u32 %[poll], 0\n\t"
        "s_cbranch_scc1 Ltop%=\n\t"
        "v_add_u32 v204, %[md], %[vm]\n\t"
        "v_mov_b32 v208, 0xffbfffbf\n\t"
        "v_mov_b32 v209, v208\n\t"
        "v_mov_b64 v[210:211], v[208:209]\n\t"
        "ds_write_b128 v204, v[208:211] offset:5120\n\t"
        "ds_write_b128 v204, v[208:211] offset:6144\n\t"
        "ds_write_b128 v204, v[208:211] offset:7168\n\t"
        "ds_write_b128 v204, v[208:211] offset:8192\n\t"
        "ds_write_b128 v204, v[208:211] offset:10240\n\t"
        "Lda_%=:\n\t"
        "s_memrealtime s[90:91]\n\t"
        "s_waitcnt lgkmcnt(0)\n\t"
        "s_mul_i32 s90, s90, 24\n\t"
        "s_sub_u32 s88, %[tg], s90\n\t"
        "s_sub_u32 s89, s88, 620\n\t"
        "s_cmp_gt_u32 s89, 0x4000\n\t"
        "s_cbranch_scc1 Lea_%=\n\t"
        "s_sleep 1\n\t"
        "s_branch Lda_%=\n\t"
        "Lea_%=:\n\t"
        "s_memrealtime s[64:65]\n\t"
        "s_nop 0\n\t"
        "s_memrealtime s[66:67]\n\t"
        "s_nop 0\n\t"
        "s_memrealtime s[68:69]\n\t"
        "s_nop 0\n\t"
        "s_memrealtime s[70:71]\n\t"
        "s_nop 0\n\t"
        "s_memrealtime s[72:73]\n\t"
        "s_nop 0\n\t"
        "s_memrealtime s[74:75]\n\t"
        "s_memtime s[90:91]\n\t"
        "s_memrealtime s[76:77]\n\t"
        "s_nop 0\n\t"
        "s_memrealtime s[78:79]\n\t"
        "s_nop 0\n\t"
        "s_memrealtime s[80:81]\n\t"
        "s_nop 0\n\t"
        "s_memrealtime s[82:83]\n\t"
        "s_nop 0\n\t"
        "s_memrealtime s[84:85]\n\t"
        "s_nop 0\n\t"
        "s_memrealtime s[86:87]\n\t"
        "s_nop 0\n\t"
        "s_nop 7\n\t"
        "s_waitcnt lgkmcnt(0)\n\t"
        "s_add_u32 s64, s64, s66\n\t"
        "s_add_u32 s64, s64, s68\n\t"
        "s_add_u32 s64, s64, s70\n\t"
        "s_add_u32 s64, s64, s72\n\t"
        "s_add_u32 s64, s64, s74\n\t"
        "s_add_u32 s64, s64, s76\n\t"
        "s_add_u32 s64, s64, s78\n\t"
        "s_add_u32 s64, s64, s80\n\t"
        "s_add_u32 s64, s64, s82\n\t"
        "s_add_u32 s64, s64, s84\n\t"
        "s_add_u32 s64, s64, s86\n\t"
        "s_lshl_b32 s64, s64, 1\n\t"
        "s_mov_b32 %[c0], s90\n\t"
        "s_mov_b32 %[r0], s64\n\t"
        "s_sub_u32 s88, %[tg], s64\n\t"
        "s_mul_i32 s88, s88, %[kr]\n\t"
        "s_ashr_i32 s88, s88, 12\n\t"
        "s_sub_u32 s88, s88, 244\n\t"
        "s_nop 1\n\t"
        "s_max_i32 s88, s88, 0\n\t"
        "s_min_i32 s88, s88, 384\n\t"
        "s_andn2_b32 s88, s88, 3\n\t"
        "s_getpc_b64 s[90:91]\n\t"
        "Lga_%=:\n\t"
        "s_sub_u32 s90, s90, s88\n\t"
        "s_subb_u32 s91, s91, 0\n\t"
        "s_add_u32 s90, s90, Lza_%=-Lga_%=\n\t"
        "s_addc_u32 s91, s91, 0\n\t"
        "s_setpc_b64 s[90:91]\n\t"
        "s_nop 0\n\ts_nop 0\n\ts_nop 0\n\ts_nop 0\n\t"
        "s_nop 0\n\ts_nop 0\n\ts_nop 0\n\ts_nop 0\n\t"
        "s_nop 0\n\ts_nop 0\n\ts_nop 0\n\ts_nop 0\n\t"
        "s_nop 0\n\ts_nop 0\n\ts_nop 0\n\ts_nop 0\n\t"
        "s_nop 0\n\ts_nop 0\n\ts_nop 0\n\ts_nop 0\n\t"
        "s_nop 0\n\ts_nop 0\n\ts_nop 0\n\ts_nop 0\n\t"
        "s_nop 0\n\ts_nop 0\n\ts_nop 0\n\ts_nop 0\n\t"
        "s_nop 0\n\ts_nop 0\n\ts_nop 0\n\ts_nop 0\n\t"
        "s_nop 0\n\ts_nop 0\n\ts_nop 0\n\ts_nop 0\n\t"
        "s_nop 0\n\ts_nop 0\n\ts_nop 0\n\ts_nop 0\n\t"
        "s_nop 0\n\ts_nop 0\n\ts_nop 0\n\ts_nop 0\n\t"
        "s_nop 0\n\ts_nop 0\n\ts_nop 0\n\ts_nop 0\n\t"
        "s_nop 0\n\ts_nop 0\n\ts_nop 0\n\ts_nop 0\n\t"
        "s_nop 0\n\ts_nop 0\n\ts_nop 0\n\ts_nop 0\n\t"
        "s_nop 0\n\ts_nop 0\n\ts_nop 0\n\ts_nop 0\n\t"
        "s_nop 0\n\ts_nop 0\n\ts_nop 0\n\ts_nop 0\n\t"
        "s_nop 0\n\ts_nop 0\n\ts_nop 0\n\ts_nop 0\n\t"
        "s_nop 0\n\ts_nop 0\n\ts_nop 0\n\ts_nop 0\n\t"
        "s_nop 0\n\ts_nop 0\n\ts_nop 0\n\ts_nop 0\n\t"
        "s_nop 0\n\ts_nop 0\n\ts_nop 0\n\ts_nop 0\n\t"
        "s_nop 0\n\ts_nop 0\n\ts_nop 0\n\ts_nop 0\n\t"
        "s_nop 0\n\ts_nop 0\n\ts_nop 0\n\ts_nop 0\n\t"
        "s_nop 0\n\ts_nop 0\n\ts_nop 0\n\ts_nop 0\n\t"
        "s_nop 0\n\ts_nop 0\n\ts_nop 0\n\ts_nop 0\n\t"
        "Lza_%=:\n\t"
        "buffer_load_dwordx4 %[vm], %[rs], %[so] offen offset:0 sc1 lds\n\t"
        "buffer_load_dwordx4 %[vm], %[rs], %[so] offen offset:1024 sc1 lds\n\t"
        "buffer_load_dwordx4 %[vm], %[rs], %[so] offen offset:2048 sc1 lds\n\t"
        "buffer_load_dwordx4 %[vm], %[rs], %[so] offen offset:3072 sc1 lds\n\t"
        "s_add_u32 m0, m0, 0x1000\n\t"
        "s_nop 0\n\t"
        "buffer_load_dwordx4 %[vm], %[rs], s92 offen offset:0 sc1 lds\n\t"
        "buffer_load_dwordx4 %[vm], %[rs], s92 offen offset:1024 sc1 lds\n\t"
        "buffer_load_dwordx4 %[vm], %[rs], s92 offen offset:2048 sc1 lds\n\t"
        "buffer_load_dwordx4 %[vm], %[rs], s92 offen offset:3072 sc1 lds\n\t"
        "s_add_u32 m0, m0, 0x1000\n\t"
        "s_nop 0\n\t"
        "buffer_load_dwordx4 %[vm], %[rs], s93 offen offset:0 sc1 lds\n\t"
        "buffer_load_dwordx4 %[vm], %[rs], s93 offen offset:2048 sc1 lds\n\t"
        "s_barrier\n\t"
        "s_sub_u32 s59, %[r0], s59\n\t"
        "s_sub_u32 s58, %[c0], s58\n\t"
        "s_mul_i32 s88, s59, %[kr]\n\t"
        "s_lshr_b32 s88, s88, 12\n\t"
        "s_sub_u32 s58, s58, s88\n\t"
        "s_mul_i32 s58, s58, 3\n\t"
        "s_ashr_i32 s58, s58, 2\n\t"
        "s_sub_u32 s59, s59, 2048\n\t"
        "s_cmp_lt_u32 s59, 2560\n\t"
        "s_cselect_b32 s58, s58, 0\n\t"
        "s_add_u32 %[kr], %[kr], s58\n\t"
        "s_max_i32 %[kr], %[kr], 2560\n\t"
        "s_min_i32 %[kr], %[kr], 4352\n\t"
        "Ltop%=:\n\t"
        "v_mov_b64 v[248:249], 0\n\t"
        "v_mov_b64 v[250:251], 0\n\t"
        "v_mov_b64 v[252:253], 0\n\t"
        "v_mov_b64 v[254:255], 0\n\t"
        "v_mov_b32 v200, 0xffbfffbf\n\t"
        "v_mov_b32 v206, %[lb]\n\t"
        "s_add_u32 %[rnd], %[rnd], 1\n\t"
        "s_setprio 3\n\t"
        "Lmk%=:\n\t"
        "ds_write_b32 %[ow], v200\n\t"
        "ds_write_b32 %[ow], v200 offset:3072\n\t"
        "s_mov_b32 s95, 0\n\t"
        "s_waitcnt vmcnt(5)\n\t"
        "s_waitcnt lgkmcnt(0)\n\t"
        "s_barrier\n\t"
        "ds_read_b128 v[208:211], %[va] offset:0\n\t"
        "ds_read_b128 v[212:215], %[va] offset:1024\n\t"
        "ds_read_b128 v[216:219], %[va] offset:2048\n\t"
        "ds_read_b128 v[220:223], %[va] offset:3072\n\t"
        "ds_read_b128 v[224:227], %[va] offset:4096\n\t"
        "ds_read_b128 v[228:231], %[va] offset:5120\n\t"
        "s_waitcnt lgkmcnt(5)\n\t"
        "v_smfmac_f32_16x16x64_bf16 v[248:251], v[208:211], v[16:23], %[sidx]\n\t"
        "ds_read_b128 v[232:235], %[va] offset:6144\n\t"
        "v_smfmac_f32_16x16x64_bf16 v[252:255], v[208:211], v[96:103], %[sidx]\n\t"
        "s_waitcnt lgkmcnt(5)\n\t"
        "v_smfmac_f32_16x16x64_bf16 v[248:251], v[212:215], v[24:31], %[sidx]\n\t"
        "ds_read_b128 v[236:239], %[va] offset:7168\n\t"
        "v_smfmac_f32_16x16x64_bf16 v[252:255], v[212:215], v[104:111], %[sidx]\n\t"
        "s_waitcnt lgkmcnt(5)\n\t"
        "v_smfmac_f32_16x16x64_bf16 v[248:251], v[216:219], v[32:39], %[sidx]\n\t"
        "ds_read_b128 v[240:243], %[va] offset:8192\n\t"
        "v_smfmac_f32_16x16x64_bf16 v[252:255], v[216:219], v[112:119], %[sidx]\n\t"
        "s_waitcnt lgkmcnt(5)\n\t"
        "v_smfmac_f32_16x16x64_bf16 v[248:251], v[220:223], v[40:47], %[sidx]\n\t"
        "ds_read_b128 v[244:247], %[va] offset:10240\n\t"
        "v_smfmac_f32_16x16x64_bf16 v[252:255], v[220:223], v[120:127], %[sidx]\n\t"
        "s_waitcnt lgkmcnt(5)\n\t"
        "v_smfmac_f32_16x16x64_bf16 v[248:251], v[224:227], v[48:55], %[sidx]\n\t"
        "s_waitcnt lgkmcnt(4)\n\t"
        "v_smfmac_f32_16x16x64_bf16 v[248:251], v[228:231], v[56:63], %[sidx]\n\t"
        "s_waitcnt lgkmcnt(3)\n\t"
        "v_smfmac_f32_16x16x64_bf16 v[248:251], v[232:235], v[64:71], %[sidx]\n\t"
        "s_waitcnt lgkmcnt(2)\n\t"
        "v_smfmac_f32_16x16x64_bf16 v[248:251], v[236:239], v[72:79], %[sidx]\n\t"
        "s_waitcnt lgkmcnt(1)\n\t"
        "v_smfmac_f32_16x16x64_bf16 v[248:251], v[240:243], v[80:87], %[sidx]\n\t"
        "s_waitcnt lgkmcnt(0)\n\t"
        "v_smfmac_f32_16x16x64_bf16 v[248:251], v[244:247], v[88:95], %[sidx]\n\t"
        "v_smfmac_f32_16x16x64_bf16 v[252:255], v[224:227], v[128:135], %[sidx]\n\t"
        "v_smfmac_f32_16x16x64_bf16 v[252:255], v[228:231], v[136:143], %[sidx]\n\t"
        "v_add_f32 v248, v248, v250\n\t"
        "v_smfmac_f32_16x16x64_bf16 v[252:255], v[232:235], v[144:151], %[sidx]\n\t"
        "v_add_f32 v249, v249, v251\n\t"
        "v_smfmac_f32_16x16x64_bf16 v[252:255], v[236:239], v[152:159], %[sidx]\n\t"
        "v_cvt_pk_bf16_f32 v200, v248, v249\n\t"
        "v_smfmac_f32_16x16x64_bf16 v[252:255], v[240:243], v[160:167], %[sidx]\n\t"
        "ds_write_b32 %[ow], v200\n\t"
        "v_smfmac_f32_16x16x64_bf16 v[252:255], v[244:247], v[168:175], %[sidx]\n\t"
        "v_cmp_u_f32 vcc, v248, v249\n\t"
        "s_nop 6\n\t"
        "s_cbranch_vccnz Lnan%=\n\t"
        "Lok%=:\n\t"
        "v_add_f32 v252, v252, v254\n\t"
        "v_add_f32 v253, v253, v255\n\t"
        "v_cvt_pk_bf16_f32 v201, v252, v253\n\t"
        "ds_write_b32 %[ow], v201 offset:3072\n\t"
        "s_waitcnt lgkmcnt(0)\n\t"
        "s_cmp_lg_u32 s96, %[left]\n\t"
        "s_cbranch_scc1 Lv%=\n\t"
        "s_buffer_load_dwordx4 s[64:67], %[rs], s61 offset:0x0\n\t"
        "s_buffer_load_dwordx4 s[68:71], %[rs], s61 offset:0x80\n\t"
        "s_buffer_load_dwordx4 s[72:75], %[rs], s61 offset:0x100\n\t"
        "s_buffer_load_dwordx4 s[76:79], %[rs], s61 offset:0x180\n\t"
        "s_barrier\n\t"
        "s_setprio 0\n\t"
        "ds_read_b32 v207, v206 offset:25104\n\t"
        "s_waitcnt lgkmcnt(0)\n\t"
        "v_cmp_eq_u32 vcc, %[rnd], v207\n\t"
        "s_barrier\n\t"
        "s_cbranch_vccnz Lck%=\n\t"
        "s_xor_b32 s67, s67, s62\n\t"
        "s_xor_b32 s71, s71, s62\n\t"
        "s_xor_b32 s75, s75, s62\n\t"
        "s_xor_b32 s79, s79, s62\n\t"
        "s_sub_u32 s65, s65, %[r0]\n\t"
        "s_cmp_eq_u32 s67, 0\n\t"
        "s_cselect_b32 s65, s65, 0\n\t"
        "s_sub_u32 s69, s69, %[r0]\n\t"
        "s_cmp_eq_u32 s71, 0\n\t"
        "s_cselect_b32 s69, s69, 0\n\t"
        "s_sub_u32 s73, s73, %[r0]\n\t"
        "s_cmp_eq_u32 s75, 0\n\t"
        "s_cselect_b32 s73, s73, 0\n\t"
        "s_sub_u32 s77, s77, %[r0]\n\t"
        "s_cmp_eq_u32 s79, 0\n\t"
        "s_cselect_b32 s77, s77, 0\n\t"
        "s_max_u32 s65, s65, s69\n\t"
        "s_max_u32 s73, s73, s77\n\t"
        "s_max_u32 s65, s65, s73\n\t"
        "s_mov_b32 s64, s65\n\t"
        "s_sub_u32 s67, s67, 1\n\t"
        "s_cmp_lt_u32 s67, 0x20000\n\t"
        "s_cselect_b32 s64, 0, s64\n\t"
        "s_sub_u32 s71, s71, 1\n\t"
        "s_cmp_lt_u32 s71, 0x20000\n\t"
        "s_cselect_b32 s64, 0, s64\n\t"
        "s_sub_u32 s75, s75, 1\n\t"
        "s_cmp_lt_u32 s75, 0x20000\n\t"
        "s_cselect_b32 s64, 0, s64\n\t"
        "s_sub_u32 s79, s79, 1\n\t"
        "s_cmp_lt_u32 s79, 0x20000\n\t"
        "s_cselect_b32 s64, 0, s64\n\t"
        "s_add_u32 s65, s65, %[r0]\n\t"
        "s_cmp_lg_u32 s64, 0\n\t"
        "Lnx%=:\n\t"
        "s_cselect_b32 %[ok], 1, 0\n\t"
        "s_mov_b32 %[tg], s65\n\t"
        "s_mov_b32 %[so], s60\n\t"
        "s_sub_u32 s63, %[rnd], s63\n\t"
        "s_cmp_gt_u32 s63, 1\n\t"
        "s_addc_u32 %[cnt], %[cnt], 0\n\t"
        "s_mov_b32 %[s], s62\n\t"
        "s_waitcnt vmcnt(0)\n\t"
        "s_and_b32 s63, %[ok], %[poll]\n\t"
        "s_cmp_lt_i32 s62, %[last]\n\t"
        "s_cselect_b32 s63, s63, 0\n\t"
        "s_cmp_eq_u32 s63, 1\n\t"
        "s_cbranch_scc1 Lst%=\n\t"
        "s_branch Lend%=\n\t"
        "Lv%=:\n\t"
        "s_barrier\n\t"
        "buffer_load_dwordx4 v[196:199], %[tv], %[rs], s61 offen sc1\n\t"
        "s_setprio 0\n\t"
        "ds_read_b32 v207, v206 offset:25104\n\t"
        "s_waitcnt lgkmcnt(0)\n\t"
        "v_cmp_eq_u32 vcc, %[rnd], v207\n\t"
        "s_barrier\n\t"
        "s_cbranch_vccnz Lck%=\n\t"
        "s_sub_u32 s97, %[r0], 0x100000\n\t"
        "s_waitcnt vmcnt(0)\n\t"
        "v_cmp_eq_u32 vcc, s62, v199\n\t"
        "v_subrev_u32 v196, s97, v196\n\t"
        "v_subrev_u32 v197, s97, v197\n\t"
        "v_cndmask_b32 v196, 0, v196, vcc\n\t"
        "v_cndmask_b32 v197, 0, v197, vcc\n\t"
        "v_cndmask_b32 v198, 0, v198, vcc\n\t"
        "v_max_u32_dpp v196, v196, v196 quad_perm:[1,0,3,2] row_mask:0xf bank_mask:0xf\n\t"
        "v_max_u32_dpp v197, v197, v197 quad_perm:[1,0,3,2] row_mask:0xf bank_mask:0xf\n\t"
        "v_max_u32_dpp v198, v198, v198 quad_perm:[1,0,3,2] row_mask:0xf bank_mask:0xf\n\t"
        "v_max_u32_dpp v196, v196, v196 quad_perm:[2,3,0,1] row_mask:0xf bank_mask:0xf\n\t"
        "v_max_u32_dpp v197, v197, v197 quad_perm:[2,3,0,1] row_mask:0xf bank_mask:0xf\n\t"
        "v_max_u32_dpp v198, v198, v198 quad_perm:[2,3,0,1] row_mask:0xf bank_mask:0xf\n\t"
        "v_max_u32_dpp v196, v196, v196 row_half_mirror row_mask:0xf bank_mask:0xf\n\t"
        "v_max_u32_dpp v197, v197, v197 row_half_mirror row_mask:0xf bank_mask:0xf\n\t"
        "v_max_u32_dpp v198, v198, v198 row_half_mirror row_mask:0xf bank_mask:0xf\n\t"
        "s_lshr_b32 s96, vcc_lo, 4\n\t"
        "v_readfirstlane_b32 s65, v197\n\t"
        "s_add_u32 s65, s65, s97\n\t"
        "s_or_b32 s96, s96, vcc_lo\n\t"
        "s_and_b32 s96, s96, 15\n\t"
        "s_cmp_eq_u32 s96, 15\n\t"
        "s_branch Lnx%=\n\t"
        "Lnan%=:\n\t"
        "s_cmp_lg_u32 %[poll], 1\n\t"
        "s_cbranch_scc1 Lok%=\n\t"
        "s_mov_b32 m0, %[lb]\n\t"
        "v_add_u32 v204, %[lb], %[vm]\n\t"
        "buffer_load_dwordx4 %[vm], %[rs], %[so] offen offset:0 sc1 lds\n\t"
        "buffer_load_dwordx4 %[vm], %[rs], %[so] offen offset:1024 sc1 lds\n\t"
        "buffer_load_dwordx4 %[vm], %[rs], %[so] offen offset:2048 sc1 lds\n\t"
        "buffer_load_dwordx4 %[vm], %[rs], %[so] offen offset:3072 sc1 lds\n\t"
        "s_add_u32 m0, m0, 0x1000\n\t"
        "s_nop 0\n\t"
        "buffer_load_dwordx4 %[vm], %[rs], s92 offen offset:0 sc1 lds\n\t"
        "buffer_load_dwordx4 %[vm], %[rs], s92 offen offset:1024 sc1 lds\n\t"
        "buffer_load_dwordx4 %[vm], %[rs], s92 offen offset:2048 sc1 lds\n\t"
        "buffer_load_dwordx4 %[vm], %[rs], s92 offen offset:3072 sc1 lds\n\t"
        "s_add_u32 m0, m0, 0x1000\n\t"
        "s_nop 0\n\t"
        "buffer_load_dwordx4 %[vm], %[rs], s93 offen offset:0 sc1 lds\n\t"
        "buffer_load_dwordx4 %[vm], %[rs], s93 offen offset:2048 sc1 lds\n\t"
        "v_max_u32 v207, v208, v209\n\t"
        "v_max3_u32 v207, v207, v210, v211\n\t"
        "v_max3_u32 v207, v207, v212, v213\n\t"
        "v_max3_u32 v207, v207, v214, v215\n\t"
        "v_max3_u32 v207, v207, v216, v217\n\t"
        "v_max3_u32 v207, v207, v218, v219\n\t"
        "v_max3_u32 v207, v207, v220, v221\n\t"
        "v_max3_u32 v207, v207, v222, v223\n\t"
        "v_max3_u32 v207, v207, v224, v225\n\t"
        "v_max3_u32 v207, v207, v226, v227\n\t"
        "v_max3_u32 v207, v207, v228, v229\n\t"
        "v_max3_u32 v207, v207, v230, v231\n\t"
        "v_max3_u32 v207, v207, v232, v233\n\t"
        "v_max3_u32 v207, v207, v234, v235\n\t"
        "v_max3_u32 v207, v207, v236, v237\n\t"
        "v_max3_u32 v207, v207, v238, v239\n\t"
        "v_max3_u32 v207, v207, v240, v241\n\t"
        "v_max3_u32 v207, v207, v242, v243\n\t"
        "v_max3_u32 v207, v207, v244, v245\n\t"
        "v_max3_u32 v207, v207, v246, v247\n\t"
        "v_cmp_eq_u32 vcc, 0xffbfffbf, v207\n\t"
        "v_mov_b32 v207, %[rnd]\n\t"
        "s_cbranch_vccz Lok%=\n\t"
        "ds_write_b32 v206, v207 offset:25104\n\t"
        "s_mov_b32 s95, 1\n\t"
        "s_waitcnt lgkmcnt(0)\n\t"
        "s_branch Lcv%=\n\t"
        "Lck%=:\n\t"
        "v_add_u32 v204, %[lb], %[vm]\n\t"
        "Lcl%=:\n\t"
        "s_mov_b32 m0, %[lb]\n\t"
        "s_nop 0\n\t"
        "buffer_load_dwordx4 %[vm], %[rs], %[so] offen offset:0 sc1 lds\n\t"
        "buffer_load_dwordx4 %[vm], %[rs], %[so] offen offset:1024 sc1 lds\n\t"
        "buffer_load_dwordx4 %[vm], %[rs], %[so] offen offset:2048 sc1 lds\n\t"
        "buffer_load_dwordx4 %[vm], %[rs], %[so] offen offset:3072 sc1 lds\n\t"
        "s_add_u32 m0, m0, 0x1000\n\t"
        "s_nop 0\n\t"
        "buffer_load_dwordx4 %[vm], %[rs], s92 offen offset:0 sc1 lds\n\t"
        "buffer_load_dwordx4 %[vm], %[rs], s92 offen offset:1024 sc1 lds\n\t"
        "buffer_load_dwordx4 %[vm], %[rs], s92 offen offset:2048 sc1 lds\n\t"
        "buffer_load_dwordx4 %[vm], %[rs], s92 offen offset:3072 sc1 lds\n\t"
        "s_add_u32 m0, m0, 0x1000\n\t"
        "s_nop 0\n\t"
        "buffer_load_dwordx4 %[vm], %[rs], s93 offen offset:0 sc1 lds\n\t"
        "buffer_load_dwordx4 %[vm], %[rs], s93 offen offset:2048 sc1 lds\n\t"
        "Lcv%=:\n\t"
        "s_sub_u32 %[left], %[left], 1\n\t"
        "s_waitcnt vmcnt(9)\n\t"
        "ds_read_b128 v[208:211], v204 offset:0\n\t"
        "s_waitcnt vmcnt(8)\n\t"
        "ds_read_b128 v[212:215], v204 offset:1024\n\t"
        "s_waitcnt vmcnt(7)\n\t"
        "ds_read_b128 v[216:219], v204 offset:2048\n\t"
        "s_waitcnt vmcnt(6)\n\t"
        "ds_read_b128 v[220:223], v204 offset:3072\n\t"
        "s_waitcnt vmcnt(5)\n\t"
        "ds_read_b128 v[224:227], v204 offset:4096\n\t"
        "s_waitcnt vmcnt(4)\n\t"
        "ds_read_b128 v[228:231], v204 offset:5120\n\t"
        "s_waitcnt lgkmcnt(5)\n\t"
        "v_max3_u32 v207, v208, v208, v209\n\t"
        "v_max3_u32 v207, v207, v210, v211\n\t"
        "s_waitcnt vmcnt(3)\n\t"
        "ds_read_b128 v[232:235], v204 offset:6144\n\t"
        "s_waitcnt lgkmcnt(5)\n\t"
        "v_max3_u32 v207, v207, v212, v213\n\t"
        "v_max3_u32 v207, v207, v214, v215\n\t"
        "s_waitcnt vmcnt(2)\n\t"
        "ds_read_b128 v[236:239], v204 offset:7168\n\t"
        "s_waitcnt lgkmcnt(5)\n\t"
        "v_max3_u32 v207, v207, v216, v217\n\t"
        "v_max3_u32 v207, v207, v218, v219\n\t"
        "s_waitcnt vmcnt(1)\n\t"
        "ds_read_b128 v[240:243], v204 offset:8192\n\t"
        "s_waitcnt lgkmcnt(5)\n\t"
        "v_max3_u32 v207, v207, v220, v221\n\t"
        "v_max3_u32 v207, v207, v222, v223\n\t"
        "s_waitcnt vmcnt(0)\n\t"
        "ds_read_b128 v[244:247], v204 offset:10240\n\t"
        "s_waitcnt lgkmcnt(5)\n\t"
        "v_max3_u32 v207, v207, v224, v225\n\t"
        "v_max3_u32 v207, v207, v226, v227\n\t"
        "s_waitcnt lgkmcnt(4)\n\t"
        "v_max3_u32 v207, v207, v228, v229\n\t"
        "v_max3_u32 v207, v207, v230, v231\n\t"
        "s_waitcnt lgkmcnt(3)\n\t"
        "v_max3_u32 v207, v207, v232, v233\n\t"
        "v_max3_u32 v207, v207, v234, v235\n\t"
        "s_waitcnt lgkmcnt(2)\n\t"
        "v_max3_u32 v207, v207, v236, v237\n\t"
        "v_max3_u32 v207, v207, v238, v239\n\t"
        "s_waitcnt lgkmcnt(1)\n\t"
        "v_max3_u32 v207, v207, v240, v241\n\t"
        "v_max3_u32 v207, v207, v242, v243\n\t"
        "s_waitcnt lgkmcnt(0)\n\t"
        "v_max3_u32 v207, v207, v244, v245\n\t"
        "v_max3_u32 v207, v207, v246, v247\n\t"
        "v_cmp_eq_u32 vcc, 0xffbfffbf, v207\n\t"
        "s_cbranch_vccz Lcd%=\n\t"
        "s_cbranch_scc0 Lcl%=\n\t"
        "Lcd%=:\n\t"
        "s_max_i32 %[left], %[left], 0\n\t"
        "s_cmp_eq_u32 s95, 0\n\t"
        "s_cbranch_scc1 Lce%=\n\t"
        "s_barrier\n\t"
        "s_barrier\n\t"
        "Lce%=:\n\t"
        "s_cbranch_vccz Ltop%=\n\t"
        "s_mov_b32 %[ok], 2\n\t"
        "Lend%=:\n\t"
        "s_mov_b32 m0, s94\n\t"
        "s_waitcnt vmcnt(0)\n\t"
        : [rnd] "+&s"(rnd), [s] "+&s"(s), [so] "+&s"(so), [tg] "+&s"(tg), [kr] "+&s"(kr), [c0] "+&s"(c0), [r0] "+&s"(r0), [ok] "=&s"(ok), [cnt] "+&s"(cnt), [left] "+&s"(left)
        : [w0] "{v[16:47]}"(W[0]), [w1] "{v[48:79]}"(W[1]), [w2] "{v[80:111]}"(W[2]), [w3] "{v[112:143]}"(W[3]), [w4] "{v[144:175]}"(W[4]), [va] "v"(va), [sidx] "v"(sidx), [ow] "v"(ow), [vm] "v"(vm), [tc] "v"(tc), [tv] "v"(tv), [rs] "s"(rs), [lb] "s"(lb), [md] "s"(md), [poll] "s"(poll), [last] "s"(last)
        : "s56", "s57", "s58", "s59", "s60", "s61", "s62", "s63", "s64", "s65", "s66", "s67", "s68", "s69", "s70", "s71", "s72", "s73", "s74", "s75", "s76", "s77", "s78", "s79", "s80", "s81", "s82", "s83", "s84", "s85", "s86", "s87", "s88", "s89", "s90", "s91", "s92", "s93", "s94", "s95", "s96", "s97", "s98", "s99", "v196", "v197", "v198", "v199", "v200", "v201", "v202", "v203", "v204", "v205", "v206", "v207", "v208", "v209", "v210", "v211", "v212", "v213", "v214", "v215", "v216", "v217", "v218", "v219", "v220", "v221", "v222", "v223", "v224", "v225", "v226", "v227", "v228", "v229", "v230", "v231", "v232", "v233", "v234", "v235", "v236", "v237", "v238", "v239", "v240", "v241", "v242", "v243", "v244", "v245", "v246", "v247", "v248", "v249", "v250", "v251", "v252", "v253", "v254", "v255", "vcc", "scc", "memory");
}

__device__ __forceinline__ void gather(u32 tg, u32 kr, u32 vm, u32 so, u32 lb, u32x4 rs) {
    asm volatile(
        "s_mov_b32 s94, m0\n\t"
        "s_add_u32 s92, %[so], 0x8000\n\t"
        "s_add_u32 s93, %[so], 0x10000\n\t"
        "s_mov_b32 m0, %[lb]\n\t"
        "Lda_%=:\n\t"
        "s_memrealtime s[90:91]\n\t"
        "s_waitcnt lgkmcnt(0)\n\t"
        "s_mul_i32 s90, s90, 24\n\t"
        "s_sub_u32 s88, %[tg], s90\n\t"
        "s_sub_u32 s89, s88, 620\n\t"
        "s_cmp_gt_u32 s89, 0x4000\n\t"
        "s_cbranch_scc1 Lea_%=\n\t"
        "s_sleep 1\n\t"
        "s_branch Lda_%=\n\t"
        "Lea_%=:\n\t"
        "s_cmp_lt_i32 s88, 120\n\t"
        "s_cbranch_scc1 Lza_%=\n\t"
        "s_memrealtime s[64:65]\n\t"
        "s_nop 0\n\t"
        "s_memrealtime s[66:67]\n\t"
        "s_nop 0\n\t"
        "s_memrealtime s[68:69]\n\t"
        "s_nop 0\n\t"
        "s_memrealtime s[70:71]\n\t"
        "s_nop 0\n\t"
        "s_memrealtime s[72:73]\n\t"
        "s_nop 0\n\t"
        "s_memrealtime s[74:75]\n\t"
        "s_memtime s[90:91]\n\t"
        "s_memrealtime s[76:77]\n\t"
        "s_nop 0\n\t"
        "s_memrealtime s[78:79]\n\t"
        "s_nop 0\n\t"
        "s_memrealtime s[80:81]\n\t"
        "s_nop 0\n\t"
        "s_memrealtime s[82:83]\n\t"
        "s_nop 0\n\t"
        "s_memrealtime s[84:85]\n\t"
        "s_nop 0\n\t"
        "s_memrealtime s[86:87]\n\t"
        "s_nop 0\n\t"
        "s_nop 7\n\t"
        "s_waitcnt lgkmcnt(0)\n\t"
        "s_add_u32 s64, s64, s66\n\t"
        "s_add_u32 s64, s64, s68\n\t"
        "s_add_u32 s64, s64, s70\n\t"
        "s_add_u32 s64, s64, s72\n\t"
        "s_add_u32 s64, s64, s74\n\t"
        "s_add_u32 s64, s64, s76\n\t"
        "s_add_u32 s64, s64, s78\n\t"
        "s_add_u32 s64, s64, s80\n\t"
        "s_add_u32 s64, s64, s82\n\t"
        "s_add_u32 s64, s64, s84\n\t"
        "s_add_u32 s64, s64, s86\n\t"
        "s_lshl_b32 s64, s64, 1\n\t"
        "s_nop 1\n\t"
        "s_sub_u32 s88, %[tg], s64\n\t"
        "s_mul_i32 s88, s88, %[kr]\n\t"
        "s_ashr_i32 s88, s88, 12\n\t"
        "s_sub_u32 s88, s88, 244\n\t"
        "s_nop 1\n\t"
        "s_max_i32 s88, s88, 0\n\t"
        "s_min_i32 s88, s88, 384\n\t"
        "s_andn2_b32 s88, s88, 3\n\t"
        "s_getpc_b64 s[90:91]\n\t"
        "Lga_%=:\n\t"
        "s_sub_u32 s90, s90, s88\n\t"
        "s_subb_u32 s91, s91, 0\n\t"
        "s_add_u32 s90, s90, Lza_%=-Lga_%=\n\t"
        "s_addc_u32 s91, s91, 0\n\t"
        "s_setpc_b64 s[90:91]\n\t"
        "s_nop 0\n\ts_nop 0\n\ts_nop 0\n\ts_nop 0\n\t"
        "s_nop 0\n\ts_nop 0\n\ts_nop 0\n\ts_nop 0\n\t"
        "s_nop 0\n\ts_nop 0\n\ts_nop 0\n\ts_nop 0\n\t"
        "s_nop 0\n\ts_nop 0\n\ts_nop 0\n\ts_nop 0\n\t"
        "s_nop 0\n\ts_nop 0\n\ts_nop 0\n\ts_nop 0\n\t"
        "s_nop 0\n\ts_nop 0\n\ts_nop 0\n\ts_nop 0\n\t"
        "s_nop 0\n\ts_nop 0\n\ts_nop 0\n\ts_nop 0\n\t"
        "s_nop 0\n\ts_nop 0\n\ts_nop 0\n\ts_nop 0\n\t"
        "s_nop 0\n\ts_nop 0\n\ts_nop 0\n\ts_nop 0\n\t"
        "s_nop 0\n\ts_nop 0\n\ts_nop 0\n\ts_nop 0\n\t"
        "s_nop 0\n\ts_nop 0\n\ts_nop 0\n\ts_nop 0\n\t"
        "s_nop 0\n\ts_nop 0\n\ts_nop 0\n\ts_nop 0\n\t"
        "s_nop 0\n\ts_nop 0\n\ts_nop 0\n\ts_nop 0\n\t"
        "s_nop 0\n\ts_nop 0\n\ts_nop 0\n\ts_nop 0\n\t"
        "s_nop 0\n\ts_nop 0\n\ts_nop 0\n\ts_nop 0\n\t"
        "s_nop 0\n\ts_nop 0\n\ts_nop 0\n\ts_nop 0\n\t"
        "s_nop 0\n\ts_nop 0\n\ts_nop 0\n\ts_nop 0\n\t"
        "s_nop 0\n\ts_nop 0\n\ts_nop 0\n\ts_nop 0\n\t"
        "s_nop 0\n\ts_nop 0\n\ts_nop 0\n\ts_nop 0\n\t"
        "s_nop 0\n\ts_nop 0\n\ts_nop 0\n\ts_nop 0\n\t"
        "s_nop 0\n\ts_nop 0\n\ts_nop 0\n\ts_nop 0\n\t"
        "s_nop 0\n\ts_nop 0\n\ts_nop 0\n\ts_nop 0\n\t"
        "s_nop 0\n\ts_nop 0\n\ts_nop 0\n\ts_nop 0\n\t"
        "s_nop 0\n\ts_nop 0\n\ts_nop 0\n\ts_nop 0\n\t"
        "Lza_%=:\n\t"
        "buffer_load_dwordx4 %[vm], %[rs], %[so] offen offset:0 sc1 lds\n\t"
        "buffer_load_dwordx4 %[vm], %[rs], %[so] offen offset:1024 sc1 lds\n\t"
        "buffer_load_dwordx4 %[vm], %[rs], %[so] offen offset:2048 sc1 lds\n\t"
        "buffer_load_dwordx4 %[vm], %[rs], %[so] offen offset:3072 sc1 lds\n\t"
        "s_add_u32 m0, m0, 0x1000\n\t"
        "s_nop 0\n\t"
        "buffer_load_dwordx4 %[vm], %[rs], s92 offen offset:0 sc1 lds\n\t"
        "buffer_load_dwordx4 %[vm], %[rs], s92 offen offset:1024 sc1 lds\n\t"
        "buffer_load_dwordx4 %[vm], %[rs], s92 offen offset:2048 sc1 lds\n\t"
        "buffer_load_dwordx4 %[vm], %[rs], s92 offen offset:3072 sc1 lds\n\t"
        "s_add_u32 m0, m0, 0x1000\n\t"
        "s_nop 0\n\t"
        "buffer_load_dwordx4 %[vm], %[rs], s93 offen offset:0 sc1 lds\n\t"
        "buffer_load_dwordx4 %[vm], %[rs], s93 offen offset:2048 sc1 lds\n\t"
        "s_mov_b32 m0, s94\n\t"
        :
        : [rs] "s"(rs), [tg] "s"(tg), [kr] "s"(kr), [vm] "v"(vm), [so] "s"(so), [lb] "s"(lb)
        : "s64", "s65", "s66", "s67", "s68", "s69", "s70", "s71", "s72", "s73", "s74", "s75", "s76", "s77", "s78", "s79", "s80", "s81", "s82", "s83", "s84", "s85", "s86", "s87", "s88", "s89", "s90", "s91", "s92", "s93", "s94", "vcc", "scc", "memory");
}

__device__ __forceinline__ void ts_look(u32& t1, u32& t2, u32& ok, u32x4 rs, u32 tsr, u32 want) {
    asm volatile(
        "s_nop 4\n\t"
        "s_buffer_load_dwordx4 s[64:67], %[rs], %[tsr] offset:0x0\n\t"
        "s_buffer_load_dwordx4 s[68:71], %[rs], %[tsr] offset:0x80\n\t"
        "s_buffer_load_dwordx4 s[72:75], %[rs], %[tsr] offset:0x100\n\t"
        "s_buffer_load_dwordx4 s[76:79], %[rs], %[tsr] offset:0x180\n\t"
        "s_waitcnt lgkmcnt(0)\n\t"
        "s_max_u32 %[t2], s66, s70\n\t"
        "s_max_u32 s64, s74, s78\n\t"
        "s_max_u32 %[t2], %[t2], s64\n\t"
        "s_xor_b32 s67, s67, %[want]\n\t"
        "s_xor_b32 s71, s71, %[want]\n\t"
        "s_xor_b32 s75, s75, %[want]\n\t"
        "s_xor_b32 s79, s79, %[want]\n\t"
        "s_sub_u32 s69, s69, s65\n\t"
        "s_sub_u32 s73, s73, s65\n\t"
        "s_sub_u32 s77, s77, s65\n\t"
        "s_max_i32 s69, s69, s73\n\t"
        "s_max_i32 s77, s77, 0\n\t"
        "s_max_i32 s69, s69, s77\n\t"
        "s_add_u32 s65, s65, s69\n\t"
        "s_or_b32 s66, s67, s71\n\t"
        "s_or_b32 s70, s75, s79\n\t"
        "s_or_b32 s70, s70, s66\n\t"
        "s_cmp_eq_u32 s70, 0\n\t"
        "s_cselect_b32 %[ok], 15, 0\n\t"
        "s_mov_b32 %[t1], s65\n\t"
        : [t1] "=&s"(t1), [t2] "=&s"(t2), [ok] "=&s"(ok)
        : [rs] "s"(rs), [tsr] "s"(tsr), [want] "s"(want)
        : "s64", "s65", "s66", "s67", "s68", "s69", "s70", "s71", "s72", "s73", "s74", "s75", "s76", "s77", "s78", "s79", "vcc", "scc", "memory");
}

__device__ __forceinline__ void pull(u32 vo, u32 tb, u32x4 rs, u32 n, u32 gap, u32 ld) {
    asm volatile(
        "s_mov_b32 s94, m0\n\t"
        "s_mov_b32 m0, %[ld]\n\t"
        "s_mov_b32 s92, 0\n\t"
        "Lp%=:\n\t"
        "v_readlane_b32 s93, %[tb], s92\n\t"
        "s_getpc_b64 s[90:91]\n\t"
        "Lgg_%=:\n\t"
        "s_sub_u32 s90, s90, %[gap]\n\t"
        "s_subb_u32 s91, s91, 0\n\t"
        "s_add_u32 s90, s90, Lzg_%=-Lgg_%=\n\t"
        "s_addc_u32 s91, s91, 0\n\t"
        "s_setpc_b64 s[90:91]\n\t"
        "s_nop 0\n\ts_nop 0\n\ts_nop 0\n\ts_nop 0\n\t"
        "s_nop 0\n\ts_nop 0\n\ts_nop 0\n\ts_nop 0\n\t"
        "s_nop 0\n\ts_nop 0\n\ts_nop 0\n\ts_nop 0\n\t"
        "s_nop 0\n\ts_nop 0\n\ts_nop 0\n\ts_nop 0\n\t"
        "s_nop 0\n\ts_nop 0\n\ts_nop 0\n\ts_nop 0\n\t"
        "s_nop 0\n\ts_nop 0\n\ts_nop 0\n\ts_nop 0\n\t"
        "s_nop 0\n\ts_nop 0\n\ts_nop 0\n\ts_nop 0\n\t"
        "s_nop 0\n\ts_nop 0\n\ts_nop 0\n\ts_nop 0\n\t"
        "s_nop 0\n\ts_nop 0\n\ts_nop 0\n\ts_nop 0\n\t"
        "s_nop 0\n\ts_nop 0\n\ts_nop 0\n\ts_nop 0\n\t"
        "s_nop 0\n\ts_nop 0\n\ts_nop 0\n\ts_nop 0\n\t"
        "s_nop 0\n\ts_nop 0\n\ts_nop 0\n\ts_nop 0\n\t"
        "s_nop 0\n\ts_nop 0\n\ts_nop 0\n\ts_nop 0\n\t"
        "s_nop 0\n\ts_nop 0\n\ts_nop 0\n\ts_nop 0\n\t"
        "s_nop 0\n\ts_nop 0\n\ts_nop 0\n\ts_nop 0\n\t"
        "s_nop 0\n\ts_nop 0\n\ts_nop 0\n\ts_nop 0\n\t"
        "Lzg_%=:\n\t"
        "buffer_load_dword %[vo], %[rs], s93 offen sc1 lds\n\t"
        "s_add_u32 s92, s92, 1\n\t"
        "s_cmp_lt_u32 s92, %[n]\n\t"
        "s_cbranch_scc1 Lp%=\n\t"
        "s_mov_b32 m0, s94\n\t"
        :
        : [vo] "v"(vo), [tb] "v"(tb), [rs] "s"(rs), [n] "s"(n), [gap] "s"(gap), [ld] "s"(ld)
        : "s88", "s89", "s90", "s91", "s92", "s93", "s94", "vcc", "scc", "memory");
}

__device__ __forceinline__ void calib(u32& c0, u32& r0) {
    asm volatile(
        "s_memrealtime s[64:65]\n\t"
        "s_nop 0\n\t"
        "s_memrealtime s[66:67]\n\t"
        "s_nop 0\n\t"
        "s_memrealtime s[68:69]\n\t"
        "s_nop 0\n\t"
        "s_memrealtime s[70:71]\n\t"
        "s_nop 0\n\t"
        "s_memrealtime s[72:73]\n\t"
        "s_nop 0\n\t"
        "s_memrealtime s[74:75]\n\t"
        "s_memtime s[90:91]\n\t"
        "s_memrealtime s[76:77]\n\t"
        "s_nop 0\n\t"
        "s_memrealtime s[78:79]\n\t"
        "s_nop 0\n\t"
        "s_memrealtime s[80:81]\n\t"
        "s_nop 0\n\t"
        "s_memrealtime s[82:83]\n\t"
        "s_nop 0\n\t"
        "s_memrealtime s[84:85]\n\t"
        "s_nop 0\n\t"
        "s_memrealtime s[86:87]\n\t"
        "s_nop 0\n\t"
        "s_nop 7\n\t"
        "s_waitcnt lgkmcnt(0)\n\t"
        "s_add_u32 s64, s64, s66\n\t"
        "s_add_u32 s64, s64, s68\n\t"
        "s_add_u32 s64, s64, s70\n\t"
        "s_add_u32 s64, s64, s72\n\t"
        "s_add_u32 s64, s64, s74\n\t"
        "s_add_u32 s64, s64, s76\n\t"
        "s_add_u32 s64, s64, s78\n\t"
        "s_add_u32 s64, s64, s80\n\t"
        "s_add_u32 s64, s64, s82\n\t"
        "s_add_u32 s64, s64, s84\n\t"
        "s_add_u32 s64, s64, s86\n\t"
        "s_lshl_b32 s64, s64, 1\n\t"
        "s_mov_b32 %[c0], s90\n\t"
        "s_mov_b32 %[r0], s64\n\t"
        : [c0] "=&s"(c0), [r0] "=&s"(r0)
        :
        : "s64", "s65", "s66", "s67", "s68", "s69", "s70", "s71", "s72", "s73", "s74", "s75", "s76", "s77", "s78", "s79", "s80", "s81", "s82", "s83", "s84", "s85", "s86", "s87", "s88", "s89", "s90", "s91", "vcc", "scc", "memory");
}

__device__ __forceinline__ void v_a0(const u32x32 (&W)[5], u32& rnd, u32 va, u32 sidx, u32 vw, u32 lb STP) {
    asm volatile(
        "Ltop%=:\n\t"
        "v_mov_b64 v[248:249], 0\n\t"
        "v_mov_b64 v[250:251], 0\n\t"
        "v_mov_b64 v[252:253], 0\n\t"
        "v_mov_b64 v[254:255], 0\n\t"
        "v_mov_b32 v200, 0xffbfffbf\n\t"
        "v_mov_b32 v206, %[lb]\n\t"
        "ds_write_b32 %[vw], v200\n\t"
        "ds_write_b32 %[vw], v200 offset:1024\n\t"
        "s_add_u32 %[rnd], %[rnd], 1\n\t"
        "s_setprio 3\n\t"
        "s_waitcnt lgkmcnt(0)\n\t"
        "s_barrier\n\t"
        "ds_read_b128 v[208:211], %[va] offset:0\n\t"
        "ds_read_b128 v[212:215], %[va] offset:1024\n\t"
        "ds_read_b128 v[216:219], %[va] offset:2048\n\t"
        "ds_read_b128 v[220:223], %[va] offset:3072\n\t"
        "ds_read_b128 v[224:227], %[va] offset:4096\n\t"
        "ds_read_b128 v[228:231], %[va] offset:5120\n\t"
        "s_waitcnt lgkmcnt(5)\n\t"
        "v_smfmac_f32_16x16x64_bf16 v[248:251], v[208:211], v[16:23], %[sidx]\n\t"
        "ds_read_b128 v[232:235], %[va] offset:6144\n\t"
        "v_smfmac_f32_16x16x64_bf16 v[252:255], v[208:211], v[96:103], %[sidx]\n\t"
        "s_waitcnt lgkmcnt(5)\n\t"
        "v_smfmac_f32_16x16x64_bf16 v[248:251], v[212:215], v[24:31], %[sidx]\n\t"
        "ds_read_b128 v[236:239], %[va] offset:7168\n\t"
        "v_smfmac_f32_16x16x64_bf16 v[252:255], v[212:215], v[104:111], %[sidx]\n\t"
        "s_waitcnt lgkmcnt(5)\n\t"
        "v_smfmac_f32_16x16x64_bf16 v[248:251], v[216:219], v[32:39], %[sidx]\n\t"
        "ds_read_b128 v[240:243], %[va] offset:8192\n\t"
        "s_waitcnt lgkmcnt(5)\n\t"
        "v_smfmac_f32_16x16x64_bf16 v[248:251], v[220:223], v[40:47], %[sidx]\n\t"
        "ds_read_b128 v[244:247], %[va] offset:10240\n\t"
        "s_waitcnt lgkmcnt(5)\n\t"
        "v_smfmac_f32_16x16x64_bf16 v[248:251], v[224:227], v[48:55], %[sidx]\n\t"
        "s_waitcnt lgkmcnt(4)\n\t"
        "v_smfmac_f32_16x16x64_bf16 v[248:251], v[228:231], v[56:63], %[sidx]\n\t"
        "s_waitcnt lgkmcnt(3)\n\t"
        "v_smfmac_f32_16x16x64_bf16 v[248:251], v[232:235], v[64:71], %[sidx]\n\t"
        "s_waitcnt lgkmcnt(2)\n\t"
        "v_smfmac_f32_16x16x64_bf16 v[248:251], v[236:239], v[72:79], %[sidx]\n\t"
        "s_waitcnt lgkmcnt(1)\n\t"
        "v_smfmac_f32_16x16x64_bf16 v[248:251], v[240:243], v[80:87], %[sidx]\n\t"
        "s_waitcnt lgkmcnt(0)\n\t"
        "v_smfmac_f32_16x16x64_bf16 v[248:251], v[244:247], v[88:95], %[sidx]\n\t"
        "v_smfmac_f32_16x16x64_bf16 v[252:255], v[216:219], v[112:119], %[sidx]\n\t"
        "v_smfmac_f32_16x16x64_bf16 v[252:255], v[220:223], v[120:127], %[sidx]\n\t"
        "v_add_f32 v248, v248, v250\n\t"
        "v_smfmac_f32_16x16x64_bf16 v[252:255], v[224:227], v[128:135], %[sidx]\n\t"
        "v_add_f32 v249, v249, v251\n\t"
        "v_smfmac_f32_16x16x64_bf16 v[252:255], v[228:231], v[136:143], %[sidx]\n\t"
        "v_cvt_pk_bf16_f32 v200, v248, v249\n\t"
        "v_smfmac_f32_16x16x64_bf16 v[252:255], v[232:235], v[144:151], %[sidx]\n\t"
        "ds_write_b32 %[vw], v200\n\t"
        "v_smfmac_f32_16x16x64_bf16 v[252:255], v[236:239], v[152:159], %[sidx]\n\t"
        "v_cmp_u_f32 vcc, v248, v249\n\t"
        "v_smfmac_f32_16x16x64_bf16 v[252:255], v[240:243], v[160:167], %[sidx]\n\t"
        "v_smfmac_f32_16x16x64_bf16 v[252:255], v[244:247], v[168:175], %[sidx]\n\t"
        "s_nop 7\n\t"
        "v_add_f32 v252, v252, v254\n\t"
        "v_add_f32 v253, v253, v255\n\t"
        "v_cvt_pk_bf16_f32 v201, v252, v253\n\t"
        "ds_write_b32 %[vw], v201 offset:1024\n\t"
        "s_setprio 0\n\t"
        "s_cbranch_vccnz Lnan%=\n\t"
        "Lok%=:\n\t"
        "s_waitcnt lgkmcnt(0)\n\t"
        "s_barrier\n\t"
        "ds_read_b32 v207, v206 offset:14336\n\t"
        "s_waitcnt lgkmcnt(0)\n\t"
        "v_cmp_eq_u32 vcc, %[rnd], v207\n\t"
        "s_barrier\n\t"
        "s_cbranch_vccnz Ltop%=\n\t"
        "s_branch Lend%=\n\t"
        "Lnan%=:\n\t"
        "v_max_u32 v207, v208, v209\n\t"
        "v_max3_u32 v207, v207, v210, v211\n\t"
        "v_max3_u32 v207, v207, v212, v213\n\t"
        "v_max3_u32 v207, v207, v214, v215\n\t"
        "v_max3_u32 v207, v207, v216, v217\n\t"
        "v_max3_u32 v207, v207, v218, v219\n\t"
        "v_max3_u32 v207, v207, v220, v221\n\t"
        "v_max3_u32 v207, v207, v222, v223\n\t"
        "v_max3_u32 v207, v207, v224, v225\n\t"
        "v_max3_u32 v207, v207, v226, v227\n\t"
        "v_max3_u32 v207, v207, v228, v229\n\t"
        "v_max3_u32 v207, v207, v230, v231\n\t"
        "v_max3_u32 v207, v207, v232, v233\n\t"
        "v_max3_u32 v207, v207, v234, v235\n\t"
        "v_max3_u32 v207, v207, v236, v237\n\t"
        "v_max3_u32 v207, v207, v238, v239\n\t"
        "v_max3_u32 v207, v207, v240, v241\n\t"
        "v_max3_u32 v207, v207, v242, v243\n\t"
        "v_max3_u32 v207, v207, v244, v245\n\t"
        "v_max3_u32 v207, v207, v246, v247\n\t"
        "v_cmp_eq_u32 vcc, 0xffbfffbf, v207\n\t"
        "v_mov_b32 v207, %[rnd]\n\t"
        "s_cbranch_vccz Lok%=\n\t"
        "ds_write_b32 v206, v207 offset:14336\n\t"
        "s_branch Lok%=\n\t"
        "Lend%=:\n\t"
        : [rnd] "+&s"(rnd)
        : [w0] "{v[16:47]}"(W[0]), [w1] "{v[48:79]}"(W[1]), [w2] "{v[80:111]}"(W[2]), [w3] "{v[112:143]}"(W[3]), [w4] "{v[144:175]}"(W[4]), [va] "v"(va), [sidx] "v"(sidx), [vw] "v"(vw), [lb] "s"(lb)
        : "v200", "v201", "v202", "v203", "v204", "v205", "v206", "v207", "v208", "v209", "v210", "v211", "v212", "v213", "v214", "v215", "v216", "v217", "v218", "v219", "v220", "v221", "v222", "v223", "v224", "v225", "v226", "v227", "v228", "v229", "v230", "v231", "v232", "v233", "v234", "v235", "v236", "v237", "v238", "v239", "v240", "v241", "v242", "v243", "v244", "v245", "v246", "v247", "v248", "v249", "v250", "v251", "v252", "v253", "v254", "v255", "vcc", "scc", "memory");
}

__device__ __forceinline__ void v_a1(const u32x32 (&W)[5], u32& rnd, u32 va, u32 sidx, u32 vw, u32 lb, u32& left, u32& s, u32& so, u32& tg, u32& kr, u32& c0, u32& r0, u32& ok, u32& cnt, u32 vm, u32 tc, u32 tv, u32 md, u32x4 rs, u32 poll, u32 last, u32 lag STP LGP) {
    asm volatile(
        "s_mov_b32 s94, m0\n\t"
        "Lst%=:\n\t"
        "v_readlane_b32 s60, %[tc], %[s]\n\t"
        "s_add_u32 s92, %[so], 0x8000\n\t"
        "s_add_u32 s93, %[so], 0x10000\n\t"
        "s_mov_b32 m0, %[md]\n\t"
        "s_add_u32 s62, %[s], 1\n\t"
        "s_mov_b32 s58, %[c0]\n\t"
        "s_mov_b32 s59, %[r0]\n\t"
        "s_mov_b32 s63, %[rnd]\n\t"
        "s_setprio 3\n\t"
        "s_add_u32 s61, s60, 0x10400\n\t"
        "s_mov_b32 s96, %[left]\n\t"
        "s_cmp_eq_u32 %[poll], 0\n\t"
        "s_cbranch_scc1 Ltop%=\n\t"
        "v_add_u32 v204, %[md], %[vm]\n\t"
        "v_mov_b32 v208, 0xffbfffbf\n\t"
        "v_mov_b32 v209, v208\n\t"
        "v_mov_b64 v[210:211], v[208:209]\n\t"
        "ds_write_b128 v204, v[208:211] offset:6144\n\t"
        "ds_write_b128 v204, v[208:211] offset:7168\n\t"
        "ds_write_b128 v204, v[208:211] offset:8192\n\t"
        "ds_write_b128 v204, v[208:211] offset:10240\n\t"
        "s_add_u32 %[tg], %[tg], %[lag]\n\t"
        "Lda_%=:\n\t"
        "s_memrealtime s[90:91]\n\t"
        "s_waitcnt lgkmcnt(0)\n\t"
        "s_mul_i32 s90, s90, 24\n\t"
        "s_sub_u32 s88, %[tg], s90\n\t"
        "s_sub_u32 s89, s88, 620\n\t"
        "s_cmp_gt_u32 s89, 0x4000\n\t"
        "s_cbranch_scc1 Lea_%=\n\t"
        "s_sleep 1\n\t"
        "s_branch Lda_%=\n\t"
        "Lea_%=:\n\t"
        "s_memrealtime s[64:65]\n\t"
        "s_nop 0\n\t"
        "s_memrealtime s[66:67]\n\t"
        "s_nop 0\n\t"
        "s_memrealtime s[68:69]\n\t"
        "s_nop 0\n\t"
        "s_memrealtime s[70:71]\n\t"
        "s_nop 0\n\t"
        "s_memrealtime s[72:73]\n\t"
        "s_nop 0\n\t"
        "s_memrealtime s[74:75]\n\t"
        "s_memtime s[90:91]\n\t"
        "s_memrealtime s[76:77]\n\t"
        "s_nop 0\n\t"
        "s_memrealtime s[78:79]\n\t"
        "s_nop 0\n\t"
        "s_memrealtime s[80:81]\n\t"
        "s_nop 0\n\t"
        "s_memrealtime s[82:83]\n\t"
        "s_nop 0\n\t"
        "s_memrealtime s[84:85]\n\t"
        "s_nop 0\n\t"
        "s_memrealtime s[86:87]\n\t"
        "s_nop 0\n\t"
        "s_nop 7\n\t"
        "s_waitcnt lgkmcnt(0)\n\t"
        "s_add_u32 s64, s64, s66\n\t"
        "s_add_u32 s64, s64, s68\n\t"
        "s_add_u32 s64, s64, s70\n\t"
        "s_add_u32 s64, s64, s72\n\t"
        "s_add_u32 s64, s64, s74\n\t"
        "s_add_u32 s64, s64, s76\n\t"
        "s_add_u32 s64, s64, s78\n\t"
        "s_add_u32 s64, s64, s80\n\t"
        "s_add_u32 s64, s64, s82\n\t"
        "s_add_u32 s64, s64, s84\n\t"
        "s_add_u32 s64, s64, s86\n\t"
        "s_lshl_b32 s64, s64, 1\n\t"
        "s_mov_b32 %[c0], s90\n\t"
        "s_mov_b32 %[r0], s64\n\t"
        "s_sub_u32 s88, %[tg], s64\n\t"
        "s_mul_i32 s88, s88, %[kr]\n\t"
        "s_ashr_i32 s88, s88, 12\n\t"
        "s_sub_u32 s88, s88, 244\n\t"
        "s_nop 1\n\t"
        "s_max_i32 s88, s88, 0\n\t"
        "s_min_i32 s88, s88, 384\n\t"
        "s_andn2_b32 s88, s88, 3\n\t"
        "s_getpc_b64 s[90:91]\n\t"
        "Lga_%=:\n\t"
        "s_sub_u32 s90, s90, s88\n\t"
        "s_subb_u32 s91, s91, 0\n\t"
        "s_add_u32 s90, s90, Lza_%=-Lga_%=\n\t"
        "s_addc_u32 s91, s91, 0\n\t"
        "s_setpc_b64 s[90:91]\n\t"
        "s_nop 0\n\ts_nop 0\n\ts_nop 0\n\ts_nop 0\n\t"
        "s_nop 0\n\ts_nop 0\n\ts_nop 0\n\ts_nop 0\n\t"
        "s_nop 0\n\ts_nop 0\n\ts_nop 0\n\ts_nop 0\n\t"
        "s_nop 0\n\ts_nop 0\n\ts_nop 0\n\ts_nop 0\n\t"
        "s_nop 0\n\ts_nop 0\n\ts_nop 0\n\ts_nop 0\n\t"
        "s_nop 0\n\ts_nop 0\n\ts_nop 0\n\ts_nop 0\n\t"
        "s_nop 0\n\ts_nop 0\n\ts_nop 0\n\ts_nop 0\n\t"
        "s_nop 0\n\ts_nop 0\n\ts_nop 0\n\ts_nop 0\n\t"
        "s_nop 0\n\ts_nop 0\n\ts_nop 0\n\ts_nop 0\n\t"
        "s_nop 0\n\ts_nop 0\n\ts_nop 0\n\ts_nop 0\n\t"
        "s_nop 0\n\ts_nop 0\n\ts_nop 0\n\ts_nop 0\n\t"
        "s_nop 0\n\ts_nop 0\n\ts_nop 0\n\ts_nop 0\n\t"
        "s_nop 0\n\ts_nop 0\n\ts_nop 0\n\ts_nop 0\n\t"
        "s_nop 0\n\ts_nop 0\n\ts_nop 0\n\ts_nop 0\n\t"
        "s_nop 0\n\ts_nop 0\n\ts_nop 0\n\ts_nop 0\n\t"
        "s_nop 0\n\ts_nop 0\n\ts_nop 0\n\ts_nop 0\n\t"
        "s_nop 0\n\ts_nop 0\n\ts_nop 0\n\ts_nop 0\n\t"
        "s_nop 0\n\ts_nop 0\n\ts_nop 0\n\ts_nop 0\n\t"
        "s_nop 0\n\ts_nop 0\n\ts_nop 0\n\ts_nop 0\n\t"
        "s_nop 0\n\ts_nop 0\n\ts_nop 0\n\ts_nop 0\n\t"
        "s_nop 0\n\ts_nop 0\n\ts_nop 0\n\ts_nop 0\n\t"
        "s_nop 0\n\ts_nop 0\n\ts_nop 0\n\ts_nop 0\n\t"
        "s_nop 0\n\ts_nop 0\n\ts_nop 0\n\ts_nop 0\n\t"
        "s_nop 0\n\ts_nop 0\n\ts_nop 0\n\ts_nop 0\n\t"
        "Lza_%=:\n\t"
        "buffer_load_dwordx4 %[vm], %[rs], %[so] offen offset:0 sc1 lds\n\t"
        "buffer_load_dwordx4 %[vm], %[rs], %[so] offen offset:1024 sc1 lds\n\t"
        "buffer_load_dwordx4 %[vm], %[rs], %[so] offen offset:2048 sc1 lds\n\t"
        "buffer_load_dwordx4 %[vm], %[rs], %[so] offen offset:3072 sc1 lds\n\t"
        "s_add_u32 m0, m0, 0x1000\n\t"
        "s_nop 0\n\t"
        "buffer_load_dwordx4 %[vm], %[rs], s92 offen offset:0 sc1 lds\n\t"
        "buffer_load_dwordx4 %[vm], %[rs], s92 offen offset:1024 sc1 lds\n\t"
        "buffer_load_dwordx4 %[vm], %[rs], s92 offen offset:2048 sc1 lds\n\t"
        "buffer_load_dwordx4 %[vm], %[rs], s92 offen offset:3072 sc1 lds\n\t"
        "s_add_u32 m0, m0, 0x1000\n\t"
        "s_nop 0\n\t"
        "buffer_load_dwordx4 %[vm], %[rs], s93 offen offset:0 sc1 lds\n\t"
        "buffer_load_dwordx4 %[vm], %[rs], s93 offen offset:2048 sc1 lds\n\t"
        "s_sub_u32 s59, %[r0], s59\n\t"
        "s_sub_u32 s58, %[c0], s58\n\t"
        "s_mul_i32 s88, s59, %[kr]\n\t"
        "s_lshr_b32 s88, s88, 12\n\t"
        "s_sub_u32 s58, s58, s88\n\t"
        "s_mul_i32 s58, s58, 3\n\t"
        "s_ashr_i32 s58, s58, 2\n\t"
        "s_sub_u32 s59, s59, 2048\n\t"
        "s_cmp_lt_u32 s59, 2560\n\t"
        "s_cselect_b32 s58, s58, 0\n\t"
        "s_add_u32 %[kr], %[kr], s58\n\t"
        "s_max_i32 %[kr], %[kr], 2560\n\t"
        "s_min_i32 %[kr], %[kr], 4352\n\t"
        "Ltop%=:\n\t"
        "v_mov_b64 v[248:249], 0\n\t"
        "v_mov_b64 v[250:251], 0\n\t"
        "v_mov_b64 v[252:253], 0\n\t"
        "v_mov_b64 v[254:255], 0\n\t"
        "v_mov_b32 v200, 0xffbfffbf\n\t"
        "v_mov_b32 v206, %[lb]\n\t"
        "ds_write_b32 %[vw], v200\n\t"
        "ds_write_b32 %[vw], v200 offset:1024\n\t"
        "s_add_u32 %[rnd], %[rnd], 1\n\t"
        "s_setprio 3\n\t"
        "s_mov_b32 s95, 0\n\t"
        "s_waitcnt vmcnt(4) lgkmcnt(0)\n\t"
        "s_barrier\n\t"
        "ds_read_b128 v[208:211], %[va] offset:0\n\t"
        "ds_read_b128 v[212:215], %[va] offset:1024\n\t"
        "ds_read_b128 v[216:219], %[va] offset:2048\n\t"
        "ds_read_b128 v[220:223], %[va] offset:3072\n\t"
        "ds_read_b128 v[224:227], %[va] offset:4096\n\t"
        "ds_read_b128 v[228:231], %[va] offset:5120\n\t"
        "s_waitcnt lgkmcnt(5)\n\t"
        "v_smfmac_f32_16x16x64_bf16 v[248:251], v[208:211], v[16:23], %[sidx]\n\t"
        "ds_read_b128 v[232:235], %[va] offset:6144\n\t"
        "v_smfmac_f32_16x16x64_bf16 v[252:255], v[208:211], v[96:103], %[sidx]\n\t"
        "s_waitcnt lgkmcnt(5)\n\t"
        "v_smfmac_f32_16x16x64_bf16 v[248:251], v[212:215], v[24:31], %[sidx]\n\t"
        "ds_read_b128 v[236:239], %[va] offset:7168\n\t"
        "v_smfmac_f32_16x16x64_bf16 v[252:255], v[212:215], v[104:111], %[sidx]\n\t"
        "s_waitcnt lgkmcnt(5)\n\t"
        "v_smfmac_f32_16x16x64_bf16 v[248:251], v[216:219], v[32:39], %[sidx]\n\t"
        "ds_read_b128 v[240:243], %[va] offset:8192\n\t"
        "s_waitcnt lgkmcnt(5)\n\t"
        "v_smfmac_f32_16x16x64_bf16 v[248:251], v[220:223], v[40:47], %[sidx]\n\t"
        "ds_read_b128 v[244:247], %[va] offset:10240\n\t"
        "s_waitcnt lgkmcnt(5)\n\t"
        "v_smfmac_f32_16x16x64_bf16 v[248:251], v[224:227], v[48:55], %[sidx]\n\t"
        "s_waitcnt lgkmcnt(4)\n\t"
        "v_smfmac_f32_16x16x64_bf16 v[248:251], v[228:231], v[56:63], %[sidx]\n\t"
        "s_waitcnt lgkmcnt(3)\n\t"
        "v_smfmac_f32_16x16x64_bf16 v[248:251], v[232:235], v[64:71], %[sidx]\n\t"
        "s_waitcnt lgkmcnt(2)\n\t"
        "v_smfmac_f32_16x16x64_bf16 v[248:251], v[236:239], v[72:79], %[sidx]\n\t"
        "s_waitcnt lgkmcnt(1)\n\t"
        "v_smfmac_f32_16x16x64_bf16 v[248:251], v[240:243], v[80:87], %[sidx]\n\t"
        "s_waitcnt lgkmcnt(0)\n\t"
        "v_smfmac_f32_16x16x64_bf16 v[248:251], v[244:247], v[88:95], %[sidx]\n\t"
        "v_smfmac_f32_16x16x64_bf16 v[252:255], v[216:219], v[112:119], %[sidx]\n\t"
        "v_smfmac_f32_16x16x64_bf16 v[252:255], v[220:223], v[120:127], %[sidx]\n\t"
        "v_add_f32 v248, v248, v250\n\t"
        "v_smfmac_f32_16x16x64_bf16 v[252:255], v[224:227], v[128:135], %[sidx]\n\t"
        "v_add_f32 v249, v249, v251\n\t"
        "v_smfmac_f32_16x16x64_bf16 v[252:255], v[228:231], v[136:143], %[sidx]\n\t"
        "v_cvt_pk_bf16_f32 v200, v248, v249\n\t"
        "v_smfmac_f32_16x16x64_bf16 v[252:255], v[232:235], v[144:151], %[sidx]\n\t"
        "ds_write_b32 %[vw], v200\n\t"
        "v_smfmac_f32_16x16x64_bf16 v[252:255], v[236:239], v[152:159], %[sidx]\n\t"
        "v_cmp_u_f32 vcc, v248, v249\n\t"
        "v_smfmac_f32_16x16x64_bf16 v[252:255], v[240:243], v[160:167], %[sidx]\n\t"
        "v_smfmac_f32_16x16x64_bf16 v[252:255], v[244:247], v[168:175], %[sidx]\n\t"
        "s_nop 7\n\t"
        "v_add_f32 v252, v252, v254\n\t"
        "v_add_f32 v253, v253, v255\n\t"
        "v_cvt_pk_bf16_f32 v201, v252, v253\n\t"
        "ds_write_b32 %[vw], v201 offset:1024\n\t"
        "s_setprio 0\n\t"
        "s_cbranch_vccnz Lnan%=\n\t"
        "Lok%=:\n\t"
        "s_cmp_lg_u32 s96, %[left]\n\t"
        "s_cbranch_scc1 Lv%=\n\t"
        "s_buffer_load_dwordx4 s[64:67], %[rs], s61 offset:0x0\n\t"
        "s_buffer_load_dwordx4 s[68:71], %[rs], s61 offset:0x80\n\t"
        "s_buffer_load_dwordx4 s[72:75], %[rs], s61 offset:0x100\n\t"
        "s_buffer_load_dwordx4 s[76:79], %[rs], s61 offset:0x180\n\t"
        "s_waitcnt lgkmcnt(0)\n\t"
        "s_barrier\n\t"
        "ds_read_b32 v207, v206 offset:14336\n\t"
        "s_waitcnt lgkmcnt(0)\n\t"
        "v_cmp_eq_u32 vcc, %[rnd], v207\n\t"
        "s_barrier\n\t"
        "s_cbranch_vccnz Lck%=\n\t"
        "s_xor_b32 s67, s67, s62\n\t"
        "s_xor_b32 s71, s71, s62\n\t"
        "s_xor_b32 s75, s75, s62\n\t"
        "s_xor_b32 s79, s79, s62\n\t"
        "s_sub_u32 s65, s65, %[r0]\n\t"
        "s_cmp_eq_u32 s67, 0\n\t"
        "s_cselect_b32 s65, s65, 0\n\t"
        "s_sub_u32 s69, s69, %[r0]\n\t"
        "s_cmp_eq_u32 s71, 0\n\t"
        "s_cselect_b32 s69, s69, 0\n\t"
        "s_sub_u32 s73, s73, %[r0]\n\t"
        "s_cmp_eq_u32 s75, 0\n\t"
        "s_cselect_b32 s73, s73, 0\n\t"
        "s_sub_u32 s77, s77, %[r0]\n\t"
        "s_cmp_eq_u32 s79, 0\n\t"
        "s_cselect_b32 s77, s77, 0\n\t"
        "s_max_u32 s65, s65, s69\n\t"
        "s_max_u32 s73, s73, s77\n\t"
        "s_max_u32 s65, s65, s73\n\t"
        "s_mov_b32 s64, s65\n\t"
        "s_sub_u32 s67, s67, 1\n\t"
        "s_cmp_lt_u32 s67, 0x20000\n\t"
        "s_cselect_b32 s64, 0, s64\n\t"
        "s_sub_u32 s71, s71, 1\n\t"
        "s_cmp_lt_u32 s71, 0x20000\n\t"
        "s_cselect_b32 s64, 0, s64\n\t"
        "s_sub_u32 s75, s75, 1\n\t"
        "s_cmp_lt_u32 s75, 0x20000\n\t"
        "s_cselect_b32 s64, 0, s64\n\t"
        "s_sub_u32 s79, s79, 1\n\t"
        "s_cmp_lt_u32 s79, 0x20000\n\t"
        "s_cselect_b32 s64, 0, s64\n\t"
        "s_add_u32 s65, s65, %[r0]\n\t"
        "s_cmp_lg_u32 s64, 0\n\t"
        "Lnx%=:\n\t"
        "s_cselect_b32 %[ok], 1, 0\n\t"
        "s_mov_b32 %[tg], s65\n\t"
        "s_mov_b32 %[so], s60\n\t"
        "s_sub_u32 s63, %[rnd], s63\n\t"
        "s_cmp_gt_u32 s63, 1\n\t"
        "s_addc_u32 %[cnt], %[cnt], 0\n\t"
        "s_mov_b32 %[s], s62\n\t"
        "s_waitcnt vmcnt(0)\n\t"
        "s_and_b32 s63, %[ok], %[poll]\n\t"
        "s_cmp_lt_i32 s62, %[last]\n\t"
        "s_cselect_b32 s63, s63, 0\n\t"
        "s_cmp_eq_u32 s63, 1\n\t"
        "s_cbranch_scc1 Lst%=\n\t"
        "s_branch Lend%=\n\t"
        "Lv%=:\n\t"
        "s_sleep 4\n\t"
        "buffer_load_dwordx4 v[196:199], %[tv], %[rs], s61 offen sc1\n\t"
        "s_waitcnt lgkmcnt(0)\n\t"
        "s_barrier\n\t"
        "ds_read_b32 v207, v206 offset:14336\n\t"
        "s_waitcnt lgkmcnt(0)\n\t"
        "v_cmp_eq_u32 vcc, %[rnd], v207\n\t"
        "s_barrier\n\t"
        "s_cbranch_vccnz Lck%=\n\t"
        "s_sub_u32 s97, %[r0], 0x100000\n\t"
        "s_waitcnt vmcnt(0)\n\t"
        "v_cmp_eq_u32 vcc, s62, v199\n\t"
        "v_subrev_u32 v196, s97, v196\n\t"
        "v_subrev_u32 v197, s97, v197\n\t"
        "v_cndmask_b32 v196, 0, v196, vcc\n\t"
        "v_cndmask_b32 v197, 0, v197, vcc\n\t"
        "v_cndmask_b32 v198, 0, v198, vcc\n\t"
        "v_max_u32_dpp v196, v196, v196 quad_perm:[1,0,3,2] row_mask:0xf bank_mask:0xf\n\t"
        "v_max_u32_dpp v197, v197, v197 quad_perm:[1,0,3,2] row_mask:0xf bank_mask:0xf\n\t"
        "v_max_u32_dpp v198, v198, v198 quad_perm:[1,0,3,2] row_mask:0xf bank_mask:0xf\n\t"
        "v_max_u32_dpp v196, v196, v196 quad_perm:[2,3,0,1] row_mask:0xf bank_mask:0xf\n\t"
        "v_max_u32_dpp v197, v197, v197 quad_perm:[2,3,0,1] row_mask:0xf bank_mask:0xf\n\t"
        "v_max_u32_dpp v198, v198, v198 quad_perm:[2,3,0,1] row_mask:0xf bank_mask:0xf\n\t"
        "v_max_u32_dpp v196, v196, v196 row_half_mirror row_mask:0xf bank_mask:0xf\n\t"
        "v_max_u32_dpp v197, v197, v197 row_half_mirror row_mask:0xf bank_mask:0xf\n\t"
        "v_max_u32_dpp v198, v198, v198 row_half_mirror row_mask:0xf bank_mask:0xf\n\t"
        "s_lshr_b32 s96, vcc_lo, 4\n\t"
        "v_readfirstlane_b32 s65, v197\n\t"
        "s_add_u32 s65, s65, s97\n\t"
        "s_or_b32 s96, s96, vcc_lo\n\t"
        "s_and_b32 s96, s96, 15\n\t"
        "s_cmp_eq_u32 s96, 15\n\t"
        "s_branch Lnx%=\n\t"
        "s_branch Lend%=\n\t"
        "Lnan%=:\n\t"
        "s_cmp_lg_u32 %[poll], 1\n\t"
        "s_cbranch_scc1 Lok%=\n\t"
        "s_mov_b32 m0, %[lb]\n\t"
        "v_add_u32 v204, %[lb], %[vm]\n\t"
        "buffer_load_dwordx4 %[vm], %[rs], %[so] offen offset:0 sc1 lds\n\t"
        "buffer_load_dwordx4 %[vm], %[rs], %[so] offen offset:1024 sc1 lds\n\t"
        "buffer_load_dwordx4 %[vm], %[rs], %[so] offen offset:2048 sc1 lds\n\t"
        "buffer_load_dwordx4 %[vm], %[rs], %[so] offen offset:3072 sc1 lds\n\t"
        "s_add_u32 m0, m0, 0x1000\n\t"
        "s_nop 0\n\t"
        "buffer_load_dwordx4 %[vm], %[rs], s92 offen offset:0 sc1 lds\n\t"
        "buffer_load_dwordx4 %[vm], %[rs], s92 offen offset:1024 sc1 lds\n\t"
        "buffer_load_dwordx4 %[vm], %[rs], s92 offen offset:2048 sc1 lds\n\t"
        "buffer_load_dwordx4 %[vm], %[rs], s92 offen offset:3072 sc1 lds\n\t"
        "s_add_u32 m0, m0, 0x1000\n\t"
        "s_nop 0\n\t"
        "buffer_load_dwordx4 %[vm], %[rs], s93 offen offset:0 sc1 lds\n\t"
        "buffer_load_dwordx4 %[vm], %[rs], s93 offen offset:2048 sc1 lds\n\t"
        "v_max_u32 v207, v208, v209\n\t"
        "v_max3_u32 v207, v207, v210, v211\n\t"
        "v_max3_u32 v207, v207, v212, v213\n\t"
        "v_max3_u32 v207, v207, v214, v215\n\t"
        "v_max3_u32 v207, v207, v216, v217\n\t"
        "v_max3_u32 v207, v207, v218, v219\n\t"
        "v_max3_u32 v207, v207, v220, v221\n\t"
        "v_max3_u32 v207, v207, v222, v223\n\t"
        "v_max3_u32 v207, v207, v224, v225\n\t"
        "v_max3_u32 v207, v207, v226, v227\n\t"
        "v_max3_u32 v207, v207, v228, v229\n\t"
        "v_max3_u32 v207, v207, v230, v231\n\t"
        "v_max3_u32 v207, v207, v232, v233\n\t"
        "v_max3_u32 v207, v207, v234, v235\n\t"
        "v_max3_u32 v207, v207, v236, v237\n\t"
        "v_max3_u32 v207, v207, v238, v239\n\t"
        "v_max3_u32 v207, v207, v240, v241\n\t"
        "v_max3_u32 v207, v207, v242, v243\n\t"
        "v_max3_u32 v207, v207, v244, v245\n\t"
        "v_max3_u32 v207, v207, v246, v247\n\t"
        "v_cmp_eq_u32 vcc, 0xffbfffbf, v207\n\t"
        "v_mov_b32 v207, %[rnd]\n\t"
        "s_cbranch_vccz Lok%=\n\t"
        "ds_write_b32 v206, v207 offset:14336\n\t"
        "s_mov_b32 s95, 1\n\t"
        "s_waitcnt lgkmcnt(0)\n\t"
        "s_branch Lcv%=\n\t"
        "Lck%=:\n\t"
        "v_add_u32 v204, %[lb], %[vm]\n\t"
        "Lcl%=:\n\t"
        "s_mov_b32 m0, %[lb]\n\t"
        "s_nop 0\n\t"
        "buffer_load_dwordx4 %[vm], %[rs], %[so] offen offset:0 sc1 lds\n\t"
        "buffer_load_dwordx4 %[vm], %[rs], %[so] offen offset:1024 sc1 lds\n\t"
        "buffer_load_dwordx4 %[vm], %[rs], %[so] offen offset:2048 sc1 lds\n\t"
        "buffer_load_dwordx4 %[vm], %[rs], %[so] offen offset:3072 sc1 lds\n\t"
        "s_add_u32 m0, m0, 0x1000\n\t"
        "s_nop 0\n\t"
        "buffer_load_dwordx4 %[vm], %[rs], s92 offen offset:0 sc1 lds\n\t"
        "buffer_load_dwordx4 %[vm], %[rs], s92 offen offset:1024 sc1 lds\n\t"
        "buffer_load_dwordx4 %[vm], %[rs], s92 offen offset:2048 sc1 lds\n\t"
        "buffer_load_dwordx4 %[vm], %[rs], s92 offen offset:3072 sc1 lds\n\t"
        "s_add_u32 m0, m0, 0x1000\n\t"
        "s_nop 0\n\t"
        "buffer_load_dwordx4 %[vm], %[rs], s93 offen offset:0 sc1 lds\n\t"
        "buffer_load_dwordx4 %[vm], %[rs], s93 offen offset:2048 sc1 lds\n\t"
        "Lcv%=:\n\t"
        "s_sub_u32 %[left], %[left], 1\n\t"
        "s_waitcnt vmcnt(9)\n\t"
        "ds_read_b128 v[208:211], v204 offset:0\n\t"
        "s_waitcnt vmcnt(8)\n\t"
        "ds_read_b128 v[212:215], v204 offset:1024\n\t"
        "s_waitcnt vmcnt(7)\n\t"
        "ds_read_b128 v[216:219], v204 offset:2048\n\t"
        "s_waitcnt vmcnt(6)\n\t"
        "ds_read_b128 v[220:223], v204 offset:3072\n\t"
        "s_waitcnt vmcnt(5)\n\t"
        "ds_read_b128 v[224:227], v204 offset:4096\n\t"
        "s_waitcnt vmcnt(4)\n\t"
        "ds_read_b128 v[228:231], v204 offset:5120\n\t"
        "s_waitcnt lgkmcnt(5)\n\t"
        "v_max3_u32 v207, v208, v208, v209\n\t"
        "v_max3_u32 v207, v207, v210, v211\n\t"
        "s_waitcnt vmcnt(3)\n\t"
        "ds_read_b128 v[232:235], v204 offset:6144\n\t"
        "s_waitcnt lgkmcnt(5)\n\t"
        "v_max3_u32 v207, v207, v212, v213\n\t"
        "v_max3_u32 v207, v207, v214, v215\n\t"
        "s_waitcnt vmcnt(2)\n\t"
        "ds_read_b128 v[236:239], v204 offset:7168\n\t"
        "s_waitcnt lgkmcnt(5)\n\t"
        "v_max3_u32 v207, v207, v216, v217\n\t"
        "v_max3_u32 v207, v207, v218, v219\n\t"
        "s_waitcnt vmcnt(1)\n\t"
        "ds_read_b128 v[240:243], v204 offset:8192\n\t"
        "s_waitcnt lgkmcnt(5)\n\t"
        "v_max3_u32 v207, v207, v220, v221\n\t"
        "v_max3_u32 v207, v207, v222, v223\n\t"
        "s_waitcnt vmcnt(0)\n\t"
        "ds_read_b128 v[244:247], v204 offset:10240\n\t"
        "s_waitcnt lgkmcnt(5)\n\t"
        "v_max3_u32 v207, v207, v224, v225\n\t"
        "v_max3_u32 v207, v207, v226, v227\n\t"
        "s_waitcnt lgkmcnt(4)\n\t"
        "v_max3_u32 v207, v207, v228, v229\n\t"
        "v_max3_u32 v207, v207, v230, v231\n\t"
        "s_waitcnt lgkmcnt(3)\n\t"
        "v_max3_u32 v207, v207, v232, v233\n\t"
        "v_max3_u32 v207, v207, v234, v235\n\t"
        "s_waitcnt lgkmcnt(2)\n\t"
        "v_max3_u32 v207, v207, v236, v237\n\t"
        "v_max3_u32 v207, v207, v238, v239\n\t"
        "s_waitcnt lgkmcnt(1)\n\t"
        "v_max3_u32 v207, v207, v240, v241\n\t"
        "v_max3_u32 v207, v207, v242, v243\n\t"
        "s_waitcnt lgkmcnt(0)\n\t"
        "v_max3_u32 v207, v207, v244, v245\n\t"
        "v_max3_u32 v207, v207, v246, v247\n\t"
        "v_cmp_eq_u32 vcc, 0xffbfffbf, v207\n\t"
        "s_cbranch_vccz Lcd%=\n\t"
        "s_cbranch_scc0 Lcl%=\n\t"
        "Lcd%=:\n\t"
        "s_max_i32 %[left], %[left], 0\n\t"
        "s_cmp_eq_u32 s95, 0\n\t"
        "s_cbranch_scc1 Lce%=\n\t"
        "s_barrier\n\t"
        "s_barrier\n\t"
        "Lce%=:\n\t"
        "s_cbranch_vccz Ltop%=\n\t"
        "s_mov_b32 %[ok], 2\n\t"
        "Lend%=:\n\t"
        "s_mov_b32 m0, s94\n\t"
        "s_waitcnt vmcnt(0)\n\t"
        : [rnd] "+&s"(rnd), [s] "+&s"(s), [so] "+&s"(so), [tg] "+&s"(tg), [kr] "+&s"(kr), [c0] "+&s"(c0), [r0] "+&s"(r0), [ok] "=&s"(ok), [cnt] "+&s"(cnt), [left] "+&s"(left)
        : [w0] "{v[16:47]}"(W[0]), [w1] "{v[48:79]}"(W[1]), [w2] "{v[80:111]}"(W[2]), [w3] "{v[112:143]}"(W[3]), [w4] "{v[144:175]}"(W[4]), [va] "v"(va), [sidx] "v"(sidx), [vw] "v"(vw), [vm] "v"(vm), [tc] "v"(tc), [tv] "v"(tv), [rs] "s"(rs), [lb] "s"(lb), [md] "s"(md), [poll] "s"(poll), [last] "s"(last), [lag] "s"(lag)
        : "s56", "s57", "s58", "s59", "s60", "s61", "s62", "s63", "s64", "s65", "s66", "s67", "s68", "s69", "s70", "s71", "s72", "s73", "s74", "s75", "s76", "s77", "s78", "s79", "s80", "s81", "s82", "s83", "s84", "s85", "s86", "s87", "s88", "s89", "s90", "s91", "s92", "s93", "s94", "s95", "s96", "s97", "s98", "s99", "v196", "v197", "v198", "v199", "v200", "v201", "v202", "v203", "v204", "v205", "v206", "v207", "v208", "v209", "v210", "v211", "v212", "v213", "v214", "v215", "v216", "v217", "v218", "v219", "v220", "v221", "v222", "v223", "v224", "v225", "v226", "v227", "v228", "v229", "v230", "v231", "v232", "v233", "v234", "v235", "v236", "v237", "v238", "v239", "v240", "v241", "v242", "v243", "v244", "v245", "v246", "v247", "v248", "v249", "v250", "v251", "v252", "v253", "v254", "v255", "vcc", "scc", "memory");
}

__device__ __forceinline__ void v_b(const u32x32 (&W)[5], u32& rnd, u32 sidx, u32 vr, u32 z0, u32 z1, u32 z2, u32x4 rs, u32 lb STP) {
    asm volatile(
        "Ltop%=:\n\t"
        "v_mov_b64 v[224:225], 0\n\t"
        "v_mov_b64 v[226:227], 0\n\t"
        "v_mov_b64 v[228:229], 0\n\t"
        "v_mov_b64 v[230:231], 0\n\t"
        "v_mov_b64 v[232:233], 0\n\t"
        "v_mov_b64 v[234:235], 0\n\t"
        "v_mov_b64 v[236:237], 0\n\t"
        "v_mov_b64 v[238:239], 0\n\t"
        "v_mov_b64 v[240:241], 0\n\t"
        "v_mov_b64 v[242:243], 0\n\t"
        "v_mov_b32 v206, %[lb]\n\t"
        "s_add_u32 %[rnd], %[rnd], 1\n\t"
        "s_mov_b32 s92, 0\n\t"
        "s_setprio 2\n\t"
        "s_barrier\n\t"
        "s_nop 15\n\t"
        "s_nop 15\n\t"
        "s_nop 15\n\t"
        "s_nop 15\n\t"
        "s_nop 15\n\t"
        "s_nop 7\n\t"
        "Lre%=:\n\t"
        "ds_read_b64_tr_b16 v[208:209], %[vr] offset:0\n\t"
        "ds_read_b64_tr_b16 v[210:211], %[vr] offset:64\n\t"
        "ds_read_b64_tr_b16 v[212:213], %[vr] offset:512\n\t"
        "ds_read_b64_tr_b16 v[214:215], %[vr] offset:576\n\t"
        "s_waitcnt lgkmcnt(0)\n\t"
        "v_smfmac_f32_16x16x64_bf16 v[224:227], v[208:211], v[16:23], %[sidx]\n\t"
        "v_smfmac_f32_16x16x64_bf16 v[228:231], v[208:211], v[48:55], %[sidx]\n\t"
        "v_smfmac_f32_16x16x64_bf16 v[232:235], v[208:211], v[80:87], %[sidx]\n\t"
        "v_smfmac_f32_16x16x64_bf16 v[236:239], v[208:211], v[112:119], %[sidx]\n\t"
        "v_smfmac_f32_16x16x64_bf16 v[240:243], v[208:211], v[144:151], %[sidx]\n\t"
        "ds_read_b64_tr_b16 v[216:217], %[vr] offset:1024\n\t"
        "v_smfmac_f32_16x16x64_bf16 v[224:227], v[212:215], v[24:31], %[sidx]\n\t"
        "ds_read_b64_tr_b16 v[218:219], %[vr] offset:1088\n\t"
        "v_smfmac_f32_16x16x64_bf16 v[228:231], v[212:215], v[56:63], %[sidx]\n\t"
        "ds_read_b64_tr_b16 v[220:221], %[vr] offset:1536\n\t"
        "v_smfmac_f32_16x16x64_bf16 v[232:235], v[212:215], v[88:95], %[sidx]\n\t"
        "ds_read_b64_tr_b16 v[222:223], %[vr] offset:1600\n\t"
        "v_smfmac_f32_16x16x64_bf16 v[236:239], v[212:215], v[120:127], %[sidx]\n\t"
        "v_smfmac_f32_16x16x64_bf16 v[240:243], v[212:215], v[152:159], %[sidx]\n\t"
        "s_waitcnt lgkmcnt(0)\n\t"
        "v_smfmac_f32_16x16x64_bf16 v[224:227], v[216:219], v[32:39], %[sidx]\n\t"
        "v_smfmac_f32_16x16x64_bf16 v[224:227], v[220:223], v[40:47], %[sidx]\n\t"
        "v_smfmac_f32_16x16x64_bf16 v[228:231], v[216:219], v[64:71], %[sidx]\n\t"
        "v_smfmac_f32_16x16x64_bf16 v[228:231], v[220:223], v[72:79], %[sidx]\n\t"
        "v_fma_f32 v205, v224, v225, v226\n\t"
        "v_smfmac_f32_16x16x64_bf16 v[232:235], v[216:219], v[96:103], %[sidx]\n\t"
        "v_cmp_u_f32 vcc, v205, v227\n\t"
        "v_smfmac_f32_16x16x64_bf16 v[232:235], v[220:223], v[104:111], %[sidx]\n\t"
        "v_cvt_pk_bf16_f32 v244, v224, v225\n\t"
        "v_smfmac_f32_16x16x64_bf16 v[236:239], v[216:219], v[128:135], %[sidx]\n\t"
        "v_cvt_pk_bf16_f32 v245, v226, v227\n\t"
        "v_smfmac_f32_16x16x64_bf16 v[236:239], v[220:223], v[136:143], %[sidx]\n\t"
        "v_cvt_pk_bf16_f32 v246, v228, v229\n\t"
        "v_smfmac_f32_16x16x64_bf16 v[240:243], v[216:219], v[160:167], %[sidx]\n\t"
        "v_cvt_pk_bf16_f32 v247, v230, v231\n\t"
        "v_smfmac_f32_16x16x64_bf16 v[240:243], v[220:223], v[168:175], %[sidx]\n\t"
        "s_cbranch_vccnz Lnan%=\n\t"
        "Lst%=:\n\t"
        "v_permlane16_swap_b32 v244, v246\n\t"
        "v_permlane16_swap_b32 v245, v247\n\t"
        "v_cvt_pk_bf16_f32 v248, v232, v233\n\t"
        "v_cvt_pk_bf16_f32 v249, v234, v235\n\t"
        "v_cvt_pk_bf16_f32 v250, v236, v237\n\t"
        "v_cvt_pk_bf16_f32 v251, v238, v239\n\t"
        "s_nop 0\n\t"
        "v_permlane16_swap_b32 v248, v250\n\t"
        "v_permlane16_swap_b32 v249, v251\n\t"
        "v_cvt_pk_bf16_f32 v252, v240, v241\n\t"
        "v_cvt_pk_bf16_f32 v254, v240, v241\n\t"
        "v_cvt_pk_bf16_f32 v253, v242, v243\n\t"
        "v_cvt_pk_bf16_f32 v255, v242, v243\n\t"
        "s_nop 0\n\t"
        "v_permlane16_swap_b32 v252, v254\n\t"
        "v_permlane16_swap_b32 v253, v255\n\t"
        "buffer_store_dwordx4 v[244:247], %[z0], %[rs], 0 offen sc1\n\t"
        "buffer_store_dwordx4 v[248:251], %[z1], %[rs], 0 offen sc1\n\t"
        "buffer_store_dwordx4 v[252:255], %[z2], %[rs], 0 offen sc1\n\t"
        "s_setprio 0\n\t"
        "s_cmp_eq_u32 s92, 2\n\t"
        "s_cbranch_scc1 Ly%=\n\t"
        "Lx%=:\n\t"
        "s_waitcnt lgkmcnt(0)\n\t"
        "s_barrier\n\t"
        "ds_read_b32 v207, v206 offset:14336\n\t"
        "s_waitcnt lgkmcnt(0)\n\t"
        "v_cmp_eq_u32 vcc, %[rnd], v207\n\t"
        "s_cmp_eq_u32 s92, 1\n\t"
        "s_cbranch_vccnz Lz%=\n\t"
        "s_cbranch_scc0 Ly%=\n\t"
        "s_mov_b32 s92, 2\n\t"
        "v_mov_b64 v[224:225], 0\n\t"
        "v_mov_b64 v[226:227], 0\n\t"
        "v_mov_b64 v[228:229], 0\n\t"
        "v_mov_b64 v[230:231], 0\n\t"
        "v_mov_b64 v[232:233], 0\n\t"
        "v_mov_b64 v[234:235], 0\n\t"
        "v_mov_b64 v[236:237], 0\n\t"
        "v_mov_b64 v[238:239], 0\n\t"
        "v_mov_b64 v[240:241], 0\n\t"
        "v_mov_b64 v[242:243], 0\n\t"
        "s_branch Lre%=\n\t"
        "Lnan%=:\n\t"
        "s_cmp_eq_u32 s92, 2\n\t"
        "s_cbranch_scc1 Lst%=\n\t"
        "s_mov_b32 s92, 1\n\t"
        "s_branch Lx%=\n\t"
        "Lz%=:\n\t"
        "s_barrier\n\t"
        "s_branch Ltop%=\n\t"
        "Ly%=:\n\t"
        "s_barrier\n\t"
        : [rnd] "+&s"(rnd)
        : [w0] "{v[16:47]}"(W[0]), [w1] "{v[48:79]}"(W[1]), [w2] "{v[80:111]}"(W[2]), [w3] "{v[112:143]}"(W[3]), [w4] "{v[144:175]}"(W[4]), [sidx] "v"(sidx), [vr] "v"(vr), [z0] "v"(z0), [z1] "v"(z1), [z2] "v"(z2), [rs] "s"(rs), [lb] "s"(lb)
        : "s92", "v205", "v206", "v207", "v208", "v209", "v210", "v211", "v212", "v213", "v214", "v215", "v216", "v217", "v218", "v219", "v220", "v221", "v222", "v223", "v224", "v225", "v226", "v227", "v228", "v229", "v230", "v231", "v232", "v233", "v234", "v235", "v236", "v237", "v238", "v239", "v240", "v241", "v242", "v243", "v244", "v245", "v246", "v247", "v248", "v249", "v250", "v251", "v252", "v253", "v254", "v255", "vcc", "scc", "memory");
}

__device__ __forceinline__ void l_g(u32& miss, u32& c0, u32& r0, u64& tl, u32 tg, u32 kr, u32 m0o, u32 so, u32x4 rs, u32x4 rec, u32 sr, u32 lim, u32 sh STP) {
    asm volatile(
        "s_mov_b32 %[miss], 1\n\t"
        "v_or_b32 v62, 0x10000, v59\n\t"
        "s_nop 1\n\t"
        "s_cmp_eq_u32 %[kr], 0\n\t"
        "s_cbranch_scc1 Lza_%=\n\t"
        "Lda_%=:\n\t"
        "s_memrealtime s[90:91]\n\t"
        "s_waitcnt lgkmcnt(0)\n\t"
        "s_mul_i32 s90, s90, 24\n\t"
        "s_sub_u32 s88, %[tg], s90\n\t"
        "s_sub_u32 s89, s88, 620\n\t"
        "s_cmp_gt_u32 s89, 0x4000\n\t"
        "s_cbranch_scc1 Lea_%=\n\t"
        "s_sleep 1\n\t"
        "s_branch Lda_%=\n\t"
        "Lea_%=:\n\t"
        "s_memrealtime s[64:65]\n\t"
        "s_nop 0\n\t"
        "s_memrealtime s[66:67]\n\t"
        "s_nop 0\n\t"
        "s_memrealtime s[68:69]\n\t"
        "s_nop 0\n\t"
        "s_memrealtime s[70:71]\n\t"
        "s_nop 0\n\t"
        "s_memrealtime s[72:73]\n\t"
        "s_nop 0\n\t"
        "s_memrealtime s[74:75]\n\t"
        "s_memtime s[90:91]\n\t"
        "s_memrealtime s[76:77]\n\t"
        "s_nop 0\n\t"
        "s_memrealtime s[78:79]\n\t"
        "s_nop 0\n\t"
        "s_memrealtime s[80:81]\n\t"
        "s_nop 0\n\t"
        "s_memrealtime s[82:83]\n\t"
        "s_nop 0\n\t"
        "s_memrealtime s[84:85]\n\t"
        "s_nop 0\n\t"
        "s_memrealtime s[86:87]\n\t"
        "s_nop 0\n\t"
        "s_nop 7\n\t"
        "s_waitcnt lgkmcnt(0)\n\t"
        "s_add_u32 s64, s64, s66\n\t"
        "s_add_u32 s64, s64, s68\n\t"
        "s_add_u32 s64, s64, s70\n\t"
        "s_add_u32 s64, s64, s72\n\t"
        "s_add_u32 s64, s64, s74\n\t"
        "s_add_u32 s64, s64, s76\n\t"
        "s_add_u32 s64, s64, s78\n\t"
        "s_add_u32 s64, s64, s80\n\t"
        "s_add_u32 s64, s64, s82\n\t"
        "s_add_u32 s64, s64, s84\n\t"
        "s_add_u32 s64, s64, s86\n\t"
        "s_lshl_b32 s64, s64, 1\n\t"
        "s_mov_b32 %[c0], s90\n\t"
        "s_mov_b32 %[r0], s64\n\t"
        "s_sub_u32 s88, %[tg], s64\n\t"
        "s_mul_i32 s88, s88, %[kr]\n\t"
        "s_ashr_i32 s88, s88, 12\n\t"
        "s_sub_u32 s88, s88, 244\n\t"
        "s_nop 1\n\t"
        "s_max_i32 s88, s88, 0\n\t"
        "s_min_i32 s88, s88, 384\n\t"
        "s_andn2_b32 s88, s88, 3\n\t"
        "s_getpc_b64 s[90:91]\n\t"
        "Lga_%=:\n\t"
        "s_sub_u32 s90, s90, s88\n\t"
        "s_subb_u32 s91, s91, 0\n\t"
        "s_add_u32 s90, s90, Lza_%=-Lga_%=\n\t"
        "s_addc_u32 s91, s91, 0\n\t"
        "s_setpc_b64 s[90:91]\n\t"
        "s_nop 0\n\ts_nop 0\n\ts_nop 0\n\ts_nop 0\n\t"
        "s_nop 0\n\ts_nop 0\n\ts_nop 0\n\ts_nop 0\n\t"
        "s_nop 0\n\ts_nop 0\n\ts_nop 0\n\ts_nop 0\n\t"
        "s_nop 0\n\ts_nop 0\n\ts_nop 0\n\ts_nop 0\n\t"
        "s_nop 0\n\ts_nop 0\n\ts_nop 0\n\ts_nop 0\n\t"
        "s_nop 0\n\ts_nop 0\n\ts_nop 0\n\ts_nop 0\n\t"
        "s_nop 0\n\ts_nop 0\n\ts_nop 0\n\ts_nop 0\n\t"
        "s_nop 0\n\ts_nop 0\n\ts_nop 0\n\ts_nop 0\n\t"
        "s_nop 0\n\ts_nop 0\n\ts_nop 0\n\ts_nop 0\n\t"
        "s_nop 0\n\ts_nop 0\n\ts_nop 0\n\ts_nop 0\n\t"
        "s_nop 0\n\ts_nop 0\n\ts_nop 0\n\ts_nop 0\n\t"
        "s_nop 0\n\ts_nop 0\n\ts_nop 0\n\ts_nop 0\n\t"
        "s_nop 0\n\ts_nop 0\n\ts_nop 0\n\ts_nop 0\n\t"
        "s_nop 0\n\ts_nop 0\n\ts_nop 0\n\ts_nop 0\n\t"
        "s_nop 0\n\ts_nop 0\n\ts_nop 0\n\ts_nop 0\n\t"
        "s_nop 0\n\ts_nop 0\n\ts_nop 0\n\ts_nop 0\n\t"
        "s_nop 0\n\ts_nop 0\n\ts_nop 0\n\ts_nop 0\n\t"
        "s_nop 0\n\ts_nop 0\n\ts_nop 0\n\ts_nop 0\n\t"
        "s_nop 0\n\ts_nop 0\n\ts_nop 0\n\ts_nop 0\n\t"
        "s_nop 0\n\ts_nop 0\n\ts_nop 0\n\ts_nop 0\n\t"
        "s_nop 0\n\ts_nop 0\n\ts_nop 0\n\ts_nop 0\n\t"
        "s_nop 0\n\ts_nop 0\n\ts_nop 0\n\ts_nop 0\n\t"
        "s_nop 0\n\ts_nop 0\n\ts_nop 0\n\ts_nop 0\n\t"
        "s_nop 0\n\ts_nop 0\n\ts_nop 0\n\ts_nop 0\n\t"
        "Lza_%=:\n\t"
        "buffer_load_dword v60, %[m0o], %[rs], %[so] offen sc1\n\t"
        "s_memtime s[92:93]\n\t"
        "s_memrealtime %[tl]\n\t"
        "v_add_u32 v64, %[sh], v56\n\t"
        "v_add_u32 v65, %[sh], v57\n\t"
        "v_mov_b64 v[66:67], v[58:59]\n\t"
        "v_or_b32 v61, 0x20000, v59\n\t"
        "s_waitcnt vmcnt(0)\n\t"
        "s_memtime s[94:95]\n\t"
        "v_cmp_eq_u32 vcc, 0xffbfffbf, v60\n\t"
        "s_cbranch_vccnz Lm%=\n\t"
        "s_waitcnt lgkmcnt(0)\n\t"
        "s_sub_u32 s92, s94, s92\n\t"
        "s_cmp_gt_u32 s92, %[lim]\n\t"
        "s_cbranch_scc1 Ls%=\n\t"
        "buffer_store_dwordx4 v[56:59], %[sr], %[rs], 0 offen sc1\n\t"
        "s_mov_b32 %[miss], 0\n\t"
        "s_branch Lend%=\n\t"
        "Ls%=:\n\t"
        "buffer_store_dwordx4 v[64:67], %[sr], %[rs], 0 offen offset:512 sc1\n\t"
        "buffer_store_dword v61, %[sr], %[rs], 0 offen offset:12 sc1\n\t"
        "s_mov_b32 %[miss], 0\n\t"
        "s_branch Lend%=\n\t"
        "Lm%=:\n\t"
        "buffer_store_dword v62, %[sr], %[rs], 0 offen offset:12 sc1\n\t"
        "Lend%=:\n\t"
        "s_waitcnt lgkmcnt(0)\n\t"
        : [c0] "+&s"(c0), [r0] "+&s"(r0), [tl] "=&s"(tl), [miss] "=&s"(miss)
        : [m0o] "v"(m0o), [sr] "v"(sr), [rs] "s"(rs), [so] "s"(so), [tg] "s"(tg), [kr] "s"(kr), [rec] "{v[56:59]}"(rec), [lim] "s"(lim), [sh] "s"(sh)
        : "s64", "s65", "s66", "s67", "s68", "s69", "s70", "s71", "s72", "s73", "s74", "s75", "s76", "s77", "s78", "s79", "s80", "s81", "s82", "s83", "s84", "s85", "s86", "s87", "s88", "s89", "s90", "s91", "s92", "s93", "s94", "s95", "v60", "v61", "v62", "v64", "v65", "v66", "v67", "vcc", "scc", "memory");
}

__device__ __forceinline__ void r_te(u32 lb, u32 fp) {
    asm volatile(
        "v_mov_b32 v20, %[lb]\n\t"
        "s_cmp_eq_u32 %[fp], 1\n\t"
        "s_cbranch_scc1 Lt%=\n\t"
        "s_barrier\n\t"
        "ds_read_b32 v21, v20 offset:16392\n\t"
        "s_waitcnt lgkmcnt(0)\n\t"
        "v_cmp_eq_u32 vcc, %[fp], v21\n\t"
        "s_cbranch_vccz Lt%=\n\t"
        "s_barrier\n\t"
        "ds_read_b32 v21, v20 offset:16396\n\t"
        "s_waitcnt lgkmcnt(0)\n\t"
        "v_cmp_eq_u32 vcc, %[fp], v21\n\t"
        "s_cbranch_vccz Lt%=\n\t"
        "s_barrier\n\t"
        "s_barrier\n\t"
        "Lt%=:\n\t"
        :
        : [lb] "s"(lb), [fp] "s"(fp)
        : "v20", "v21", "vcc", "scc", "memory");
}

__device__ __forceinline__ void r_t0(u32& c0, u32& r0, u32& t1, u32& t2, u32& ok, u32 tl, u32 t0, u32 kr, u32 fv, u32 fp, u32 lb, u32x4 rs, u32 so STP) {
    asm volatile(
        "s_setprio 3\n\t"
        "v_mov_b32 v20, %[lb]\n\t"
        "s_mov_b32 %[ok], 0\n\t"
        "s_or_b32 s92, %[fv], 0x10000\n\t"
        "s_mov_b32 s51, %[fv]\n\t"
        "s_mov_b32 s55, %[fv]\n\t"
        "s_mov_b32 s59, %[fv]\n\t"
        "s_mov_b32 s63, %[fv]\n\t"
        "s_cmp_eq_u32 %[fp], 1\n\t"
        "s_cbranch_scc1 Lt%=\n\t"
        "s_barrier\n\t"
        "ds_read_b32 v21, v20 offset:16392\n\t"
        "s_waitcnt lgkmcnt(0)\n\t"
        "v_cmp_eq_u32 vcc, %[fp], v21\n\t"
        "s_cbranch_vccz Lt%=\n\t"
        "s_barrier\n\t"
        "ds_read_b32 v21, v20 offset:16396\n\t"
        "s_waitcnt lgkmcnt(0)\n\t"
        "v_cmp_eq_u32 vcc, %[fp], v21\n\t"
        "s_cbranch_vccz Lt%=\n\t"
        "s_barrier\n\t"
        "s_barrier\n\t"
        "Lt%=:\n\t"
        "s_bfe_u32 s95, %[t2], 0x8000c\n\t"
        "s_mul_i32 s95, s95, %[kr]\n\t"
        "s_lshr_b32 s95, s95, 12\n\t"
        "s_min_u32 s95, s95, 512\n\t"
        "s_andn2_b32 s95, s95, 3\n\t"
        "v_mov_b32 v21, s95\n\t"
        "ds_write_b32 v20, v21 offset:16384\n\t"
        "Ldl_%=:\n\t"
        "s_memrealtime s[90:91]\n\t"
        "s_waitcnt lgkmcnt(0)\n\t"
        "s_mul_i32 s90, s90, 24\n\t"
        "s_sub_u32 s88, %[tl], s90\n\t"
        "s_sub_u32 s89, s88, 620\n\t"
        "s_cmp_gt_u32 s89, 0x4000\n\t"
        "s_cbranch_scc1 Lel_%=\n\t"
        "s_sleep 1\n\t"
        "s_branch Ldl_%=\n\t"
        "Lel_%=:\n\t"
        "s_memrealtime s[64:65]\n\t"
        "s_nop 0\n\t"
        "s_memrealtime s[66:67]\n\t"
        "s_nop 0\n\t"
        "s_memrealtime s[68:69]\n\t"
        "s_nop 0\n\t"
        "s_memrealtime s[70:71]\n\t"
        "s_nop 0\n\t"
        "s_memrealtime s[72:73]\n\t"
        "s_nop 0\n\t"
        "s_memrealtime s[74:75]\n\t"
        "s_memtime s[90:91]\n\t"
        "s_memrealtime s[76:77]\n\t"
        "s_nop 0\n\t"
        "s_memrealtime s[78:79]\n\t"
        "s_nop 0\n\t"
        "s_memrealtime s[80:81]\n\t"
        "s_nop 0\n\t"
        "s_memrealtime s[82:83]\n\t"
        "s_nop 0\n\t"
        "s_memrealtime s[84:85]\n\t"
        "s_nop 0\n\t"
        "s_memrealtime s[86:87]\n\t"
        "s_nop 0\n\t"
        "s_nop 7\n\t"
        "s_waitcnt lgkmcnt(0)\n\t"
        "s_add_u32 s64, s64, s66\n\t"
        "s_add_u32 s64, s64, s68\n\t"
        "s_add_u32 s64, s64, s70\n\t"
        "s_add_u32 s64, s64, s72\n\t"
        "s_add_u32 s64, s64, s74\n\t"
        "s_add_u32 s64, s64, s76\n\t"
        "s_add_u32 s64, s64, s78\n\t"
        "s_add_u32 s64, s64, s80\n\t"
        "s_add_u32 s64, s64, s82\n\t"
        "s_add_u32 s64, s64, s84\n\t"
        "s_add_u32 s64, s64, s86\n\t"
        "s_lshl_b32 s64, s64, 1\n\t"
        "s_mov_b32 %[c0], s90\n\t"
        "s_mov_b32 %[r0], s64\n\t"
        "s_sub_u32 s88, %[tl], s64\n\t"
        "s_mul_i32 s88, s88, %[kr]\n\t"
        "s_ashr_i32 s88, s88, 12\n\t"
        "s_sub_u32 s88, s88, 244\n\t"
        "s_min_i32 s89, s88, 0\n\t"
        "s_nop 0\n\t"
        "s_max_i32 s88, s88, 0\n\t"
        "s_min_i32 s88, s88, 384\n\t"
        "s_andn2_b32 s88, s88, 3\n\t"
        "s_getpc_b64 s[90:91]\n\t"
        "Lgl_%=:\n\t"
        "s_sub_u32 s90, s90, s88\n\t"
        "s_subb_u32 s91, s91, 0\n\t"
        "s_add_u32 s90, s90, Lzl_%=-Lgl_%=\n\t"
        "s_addc_u32 s91, s91, 0\n\t"
        "s_setpc_b64 s[90:91]\n\t"
        "s_nop 0\n\ts_nop 0\n\ts_nop 0\n\ts_nop 0\n\t"
        "s_nop 0\n\ts_nop 0\n\ts_nop 0\n\ts_nop 0\n\t"
        "s_nop 0\n\ts_nop 0\n\ts_nop 0\n\ts_nop 0\n\t"
        "s_nop 0\n\ts_nop 0\n\ts_nop 0\n\ts_nop 0\n\t"
        "s_nop 0\n\ts_nop 0\n\ts_nop 0\n\ts_nop 0\n\t"
        "s_nop 0\n\ts_nop 0\n\ts_nop 0\n\ts_nop 0\n\t"
        "s_nop 0\n\ts_nop 0\n\ts_nop 0\n\ts_nop 0\n\t"
        "s_nop 0\n\ts_nop 0\n\ts_nop 0\n\ts_nop 0\n\t"
        "s_nop 0\n\ts_nop 0\n\ts_nop 0\n\ts_nop 0\n\t"
        "s_nop 0\n\ts_nop 0\n\ts_nop 0\n\ts_nop 0\n\t"
        "s_nop 0\n\ts_nop 0\n\ts_nop 0\n\ts_nop 0\n\t"
        "s_nop 0\n\ts_nop 0\n\ts_nop 0\n\ts_nop 0\n\t"
        "s_nop 0\n\ts_nop 0\n\ts_nop 0\n\ts_nop 0\n\t"
        "s_nop 0\n\ts_nop 0\n\ts_nop 0\n\ts_nop 0\n\t"
        "s_nop 0\n\ts_nop 0\n\ts_nop 0\n\ts_nop 0\n\t"
        "s_nop 0\n\ts_nop 0\n\ts_nop 0\n\ts_nop 0\n\t"
        "s_nop 0\n\ts_nop 0\n\ts_nop 0\n\ts_nop 0\n\t"
        "s_nop 0\n\ts_nop 0\n\ts_nop 0\n\ts_nop 0\n\t"
        "s_nop 0\n\ts_nop 0\n\ts_nop 0\n\ts_nop 0\n\t"
        "s_nop 0\n\ts_nop 0\n\ts_nop 0\n\ts_nop 0\n\t"
        "s_nop 0\n\ts_nop 0\n\ts_nop 0\n\ts_nop 0\n\t"
        "s_nop 0\n\ts_nop 0\n\ts_nop 0\n\ts_nop 0\n\t"
        "s_nop 0\n\ts_nop 0\n\ts_nop 0\n\ts_nop 0\n\t"
        "s_nop 0\n\ts_nop 0\n\ts_nop 0\n\ts_nop 0\n\t"
        "Lzl_%=:\n\t"
        "s_buffer_load_dwordx4 s[48:51], %[rs], %[so] offset:0x0\n\t"
        "s_buffer_load_dwordx4 s[52:55], %[rs], %[so] offset:0x80\n\t"
        "s_buffer_load_dwordx4 s[56:59], %[rs], %[so] offset:0x100\n\t"
        "s_buffer_load_dwordx4 s[60:63], %[rs], %[so] offset:0x180\n\t"
        "s_sub_u32 s88, %[t0], %[tl]\n\t"
        "s_mul_i32 s88, s88, %[kr]\n\t"
        "s_ashr_i32 s88, s88, 12\n\t"
        "s_add_u32 s88, s88, s89\n\t"
        "s_sub_u32 s88, s88, 156\n\t"
        "s_max_i32 s88, s88, 0\n\t"
        "s_min_i32 s88, s88, 1280\n\t"
        "s_andn2_b32 s88, s88, 3\n\t"
        "s_cmp_lt_i32 s89, -64\n\t"
        "s_cbranch_scc1 Lw%=\n\t"
        "s_getpc_b64 s[90:91]\n\t"
        "Lga_%=:\n\t"
        "s_sub_u32 s90, s90, s88\n\t"
        "s_subb_u32 s91, s91, 0\n\t"
        "s_add_u32 s90, s90, Lza_%=-Lga_%=\n\t"
        "s_addc_u32 s91, s91, 0\n\t"
        "s_setpc_b64 s[90:91]\n\t"
        "s_nop 0\n\ts_nop 0\n\ts_nop 0\n\ts_nop 0\n\t"
        "s_nop 0\n\ts_nop 0\n\ts_nop 0\n\ts_nop 0\n\t"
        "s_nop 0\n\ts_nop 0\n\ts_nop 0\n\ts_nop 0\n\t"
        "s_nop 0\n\ts_nop 0\n\ts_nop 0\n\ts_nop 0\n\t"
        "s_nop 0\n\ts_nop 0\n\ts_nop 0\n\ts_nop 0\n\t"
        "s_nop 0\n\ts_nop 0\n\ts_nop 0\n\ts_nop 0\n\t"
        "s_nop 0\n\ts_nop 0\n\ts_nop 0\n\ts_nop 0\n\t"
        "s_nop 0\n\ts_nop 0\n\ts_nop 0\n\ts_nop 0\n\t"
        "s_nop 0\n\ts_nop 0\n\ts_nop 0\n\ts_nop 0\n\t"
        "s_nop 0\n\ts_nop 0\n\ts_nop 0\n\ts_nop 0\n\t"
        "s_nop 0\n\ts_nop 0\n\ts_nop 0\n\ts_nop 0\n\t"
        "s_nop 0\n\ts_nop 0\n\ts_nop 0\n\ts_nop 0\n\t"
        "s_nop 0\n\ts_nop 0\n\ts_nop 0\n\ts_nop 0\n\t"
        "s_nop 0\n\ts_nop 0\n\ts_nop 0\n\ts_nop 0\n\t"
        "s_nop 0\n\ts_nop 0\n\ts_nop 0\n\ts_nop 0\n\t"
        "s_nop 0\n\ts_nop 0\n\ts_nop 0\n\ts_nop 0\n\t"
        "s_nop 0\n\ts_nop 0\n\ts_nop 0\n\ts_nop 0\n\t"
        "s_nop 0\n\ts_nop 0\n\ts_nop 0\n\ts_nop 0\n\t"
        "s_nop 0\n\ts_nop 0\n\ts_nop 0\n\ts_nop 0\n\t"
        "s_nop 0\n\ts_nop 0\n\ts_nop 0\n\ts_nop 0\n\t"
        "s_nop 0\n\ts_nop 0\n\ts_nop 0\n\ts_nop 0\n\t"
        "s_nop 0\n\ts_nop 0\n\ts_nop 0\n\ts_nop 0\n\t"
        "s_nop 0\n\ts_nop 0\n\ts_nop 0\n\ts_nop 0\n\t"
        "s_nop 0\n\ts_nop 0\n\ts_nop 0\n\ts_nop 0\n\t"
        "s_nop 0\n\ts_nop 0\n\ts_nop 0\n\ts_nop 0\n\t"
        "s_nop 0\n\ts_nop 0\n\ts_nop 0\n\ts_nop 0\n\t"
        "s_nop 0\n\ts_nop 0\n\ts_nop 0\n\ts_nop 0\n\t"
        "s_nop 0\n\ts_nop 0\n\ts_nop 0\n\ts_nop 0\n\t"
        "s_nop 0\n\ts_nop 0\n\ts_nop 0\n\ts_nop 0\n\t"
        "s_nop 0\n\ts_nop 0\n\ts_nop 0\n\ts_nop 0\n\t"
        "s_nop 0\n\ts_nop 0\n\ts_nop 0\n\ts_nop 0\n\t"
        "s_nop 0\n\ts_nop 0\n\ts_nop 0\n\ts_nop 0\n\t"
        "s_nop 0\n\ts_nop 0\n\ts_nop 0\n\ts_nop 0\n\t"
        "s_nop 0\n\ts_nop 0\n\ts_nop 0\n\ts_nop 0\n\t"
        "s_nop 0\n\ts_nop 0\n\ts_nop 0\n\ts_nop 0\n\t"
        "s_nop 0\n\ts_nop 0\n\ts_nop 0\n\ts_nop 0\n\t"
        "s_nop 0\n\ts_nop 0\n\ts_nop 0\n\ts_nop 0\n\t"
        "s_nop 0\n\ts_nop 0\n\ts_nop 0\n\ts_nop 0\n\t"
        "s_nop 0\n\ts_nop 0\n\ts_nop 0\n\ts_nop 0\n\t"
        "s_nop 0\n\ts_nop 0\n\ts_nop 0\n\ts_nop 0\n\t"
        "s_nop 0\n\ts_nop 0\n\ts_nop 0\n\ts_nop 0\n\t"
        "s_nop 0\n\ts_nop 0\n\ts_nop 0\n\ts_nop 0\n\t"
        "s_nop 0\n\ts_nop 0\n\ts_nop 0\n\ts_nop 0\n\t"
        "s_nop 0\n\ts_nop 0\n\ts_nop 0\n\ts_nop 0\n\t"
        "s_nop 0\n\ts_nop 0\n\ts_nop 0\n\ts_nop 0\n\t"
        "s_nop 0\n\ts_nop 0\n\ts_nop 0\n\ts_nop 0\n\t"
        "s_nop 0\n\ts_nop 0\n\ts_nop 0\n\ts_nop 0\n\t"
        "s_nop 0\n\ts_nop 0\n\ts_nop 0\n\ts_nop 0\n\t"
        "s_nop 0\n\ts_nop 0\n\ts_nop 0\n\ts_nop 0\n\t"
        "s_nop 0\n\ts_nop 0\n\ts_nop 0\n\ts_nop 0\n\t"
        "s_nop 0\n\ts_nop 0\n\ts_nop 0\n\ts_nop 0\n\t"
        "s_nop 0\n\ts_nop 0\n\ts_nop 0\n\ts_nop 0\n\t"
        "s_nop 0\n\ts_nop 0\n\ts_nop 0\n\ts_nop 0\n\t"
        "s_nop 0\n\ts_nop 0\n\ts_nop 0\n\ts_nop 0\n\t"
        "s_nop 0\n\ts_nop 0\n\ts_nop 0\n\ts_nop 0\n\t"
        "s_nop 0\n\ts_nop 0\n\ts_nop 0\n\ts_nop 0\n\t"
        "s_nop 0\n\ts_nop 0\n\ts_nop 0\n\ts_nop 0\n\t"
        "s_nop 0\n\ts_nop 0\n\ts_nop 0\n\ts_nop 0\n\t"
        "s_nop 0\n\ts_nop 0\n\ts_nop 0\n\ts_nop 0\n\t"
        "s_nop 0\n\ts_nop 0\n\ts_nop 0\n\ts_nop 0\n\t"
        "s_nop 0\n\ts_nop 0\n\ts_nop 0\n\ts_nop 0\n\t"
        "s_nop 0\n\ts_nop 0\n\ts_nop 0\n\ts_nop 0\n\t"
        "s_nop 0\n\ts_nop 0\n\ts_nop 0\n\ts_nop 0\n\t"
        "s_nop 0\n\ts_nop 0\n\ts_nop 0\n\ts_nop 0\n\t"
        "s_nop 0\n\ts_nop 0\n\ts_nop 0\n\ts_nop 0\n\t"
        "s_nop 0\n\ts_nop 0\n\ts_nop 0\n\ts_nop 0\n\t"
        "s_nop 0\n\ts_nop 0\n\ts_nop 0\n\ts_nop 0\n\t"
        "s_nop 0\n\ts_nop 0\n\ts_nop 0\n\ts_nop 0\n\t"
        "s_nop 0\n\ts_nop 0\n\ts_nop 0\n\ts_nop 0\n\t"
        "s_nop 0\n\ts_nop 0\n\ts_nop 0\n\ts_nop 0\n\t"
        "s_nop 0\n\ts_nop 0\n\ts_nop 0\n\ts_nop 0\n\t"
        "s_nop 0\n\ts_nop 0\n\ts_nop 0\n\ts_nop 0\n\t"
        "s_nop 0\n\ts_nop 0\n\ts_nop 0\n\ts_nop 0\n\t"
        "s_nop 0\n\ts_nop 0\n\ts_nop 0\n\ts_nop 0\n\t"
        "s_nop 0\n\ts_nop 0\n\ts_nop 0\n\ts_nop 0\n\t"
        "s_nop 0\n\ts_nop 0\n\ts_nop 0\n\ts_nop 0\n\t"
        "s_nop 0\n\ts_nop 0\n\ts_nop 0\n\ts_nop 0\n\t"
        "s_nop 0\n\ts_nop 0\n\ts_nop 0\n\ts_nop 0\n\t"
        "s_nop 0\n\ts_nop 0\n\ts_nop 0\n\ts_nop 0\n\t"
        "s_nop 0\n\ts_nop 0\n\ts_nop 0\n\ts_nop 0\n\t"
        "Lza_%=:\n\t"
        "s_cmp_lg_u32 s51, %[fv]\n\t"
        "s_cbranch_scc1 Ln%=\n\t"
        "s_cmp_lg_u32 s55, %[fv]\n\t"
        "s_cbranch_scc1 Ln%=\n\t"
        "s_cmp_lg_u32 s59, %[fv]\n\t"
        "s_cbranch_scc1 Ln%=\n\t"
        "s_cmp_lg_u32 s63, %[fv]\n\t"
        "s_cbranch_scc1 Ln%=\n\t"
        "s_barrier\n\t"
        "s_setprio 0\n\t"
        "s_waitcnt lgkmcnt(0)\n\t"
        "s_xor_b32 s51, s51, %[fv]\n\t"
        "s_xor_b32 s55, s55, %[fv]\n\t"
        "s_xor_b32 s59, s59, %[fv]\n\t"
        "s_xor_b32 s63, s63, %[fv]\n\t"
        "s_sub_u32 s49, s49, %[tl]\n\t"
        "s_cmp_eq_u32 s51, 0\n\t"
        "s_cselect_b32 s49, s49, 0\n\t"
        "s_cselect_b32 s50, s50, 0\n\t"
        "s_sub_u32 s53, s53, %[tl]\n\t"
        "s_cmp_eq_u32 s55, 0\n\t"
        "s_cselect_b32 s53, s53, 0\n\t"
        "s_cselect_b32 s54, s54, 0\n\t"
        "s_sub_u32 s57, s57, %[tl]\n\t"
        "s_cmp_eq_u32 s59, 0\n\t"
        "s_cselect_b32 s57, s57, 0\n\t"
        "s_cselect_b32 s58, s58, 0\n\t"
        "s_sub_u32 s61, s61, %[tl]\n\t"
        "s_cmp_eq_u32 s63, 0\n\t"
        "s_cselect_b32 s61, s61, 0\n\t"
        "s_cselect_b32 s62, s62, 0\n\t"
        "s_max_u32 s49, s49, s53\n\t"
        "s_max_u32 s57, s57, s61\n\t"
        "s_max_u32 s49, s49, s57\n\t"
        "s_max_u32 s50, s50, s54\n\t"
        "s_max_u32 s58, s58, s62\n\t"
        "s_max_u32 s50, s50, s58\n\t"
        "s_mov_b32 s48, s49\n\t"
        "s_sub_u32 s51, s51, 1\n\t"
        "s_cmp_lt_u32 s51, 0x20000\n\t"
        "s_cselect_b32 s48, 0, s48\n\t"
        "s_sub_u32 s55, s55, 1\n\t"
        "s_cmp_lt_u32 s55, 0x20000\n\t"
        "s_cselect_b32 s48, 0, s48\n\t"
        "s_sub_u32 s59, s59, 1\n\t"
        "s_cmp_lt_u32 s59, 0x20000\n\t"
        "s_cselect_b32 s48, 0, s48\n\t"
        "s_sub_u32 s63, s63, 1\n\t"
        "s_cmp_lt_u32 s63, 0x20000\n\t"
        "s_cselect_b32 s48, 0, s48\n\t"
        "s_add_u32 s49, s49, %[tl]\n\t"
        "s_cmp_lg_u32 s48, 0\n\t"
        "s_cselect_b32 %[ok], 1, 2\n\t"
        "s_cselect_b32 %[t1], s49, %[t1]\n\t"
        "s_cselect_b32 %[t2], s50, %[t2]\n\t"
        "s_branch Lm%=\n\t"
        "Lw%=:\n\t"
        "s_waitcnt lgkmcnt(0)\n\t"
        "s_sub_u32 s95, %[t0], %[r0]\n\t"
        "s_mul_i32 s95, s95, %[kr]\n\t"
        "s_ashr_i32 s95, s95, 12\n\t"
        "s_add_u32 s95, s95, %[c0]\n\t"
        "s_sub_u32 s95, s95, 64\n\t"
        "Ldc_%=:\n\t"
        "s_memtime s[90:91]\n\t"
        "s_waitcnt lgkmcnt(0)\n\t"
        "s_sub_u32 s88, s95, s90\n\t"
        "s_sub_u32 s89, s88, 336\n\t"
        "s_cmp_gt_u32 s89, 0x4000\n\t"
        "s_cbranch_scc1 Lec_%=\n\t"
        "s_sleep 1\n\t"
        "s_branch Ldc_%=\n\t"
        "Lec_%=:\n\t"
        "s_sub_u32 s88, s88, 112\n\t"
        "s_max_i32 s88, s88, 0\n\t"
        "s_min_i32 s88, s88, 224\n\t"
        "s_andn2_b32 s88, s88, 3\n\t"
        "s_getpc_b64 s[90:91]\n\t"
        "Lgc_%=:\n\t"
        "s_sub_u32 s90, s90, s88\n\t"
        "s_subb_u32 s91, s91, 0\n\t"
        "s_add_u32 s90, s90, Lzc_%=-Lgc_%=\n\t"
        "s_addc_u32 s91, s91, 0\n\t"
        "s_setpc_b64 s[90:91]\n\t"
        "s_nop 0\n\ts_nop 0\n\ts_nop 0\n\ts_nop 0\n\t"
        "s_nop 0\n\ts_nop 0\n\ts_nop 0\n\ts_nop 0\n\t"
        "s_nop 0\n\ts_nop 0\n\ts_nop 0\n\ts_nop 0\n\t"
        "s_nop 0\n\ts_nop 0\n\ts_nop 0\n\ts_nop 0\n\t"
        "s_nop 0\n\ts_nop 0\n\ts_nop 0\n\ts_nop 0\n\t"
        "s_nop 0\n\ts_nop 0\n\ts_nop 0\n\ts_nop 0\n\t"
        "s_nop 0\n\ts_nop 0\n\ts_nop 0\n\ts_nop 0\n\t"
        "s_nop 0\n\ts_nop 0\n\ts_nop 0\n\ts_nop 0\n\t"
        "s_nop 0\n\ts_nop 0\n\ts_nop 0\n\ts_nop 0\n\t"
        "s_nop 0\n\ts_nop 0\n\ts_nop 0\n\ts_nop 0\n\t"
        "s_nop 0\n\ts_nop 0\n\ts_nop 0\n\ts_nop 0\n\t"
        "s_nop 0\n\ts_nop 0\n\ts_nop 0\n\ts_nop 0\n\t"
        "s_nop 0\n\ts_nop 0\n\ts_nop 0\n\ts_nop 0\n\t"
        "s_nop 0\n\ts_nop 0\n\ts_nop 0\n\ts_nop 0\n\t"
        "Lzc_%=:\n\t"
        "s_branch Lza_%=\n\t"
        "Ln%=:\n\t"
        "s_mov_b32 %[ok], 3\n\t"
        "s_waitcnt lgkmcnt(0)\n\t"
        "s_cmp_eq_u32 s51, s92\n\t"
        "s_cselect_b32 %[ok], 0, %[ok]\n\t"
        "s_cmp_eq_u32 s55, s92\n\t"
        "s_cselect_b32 %[ok], 0, %[ok]\n\t"
        "s_cmp_eq_u32 s59, s92\n\t"
        "s_cselect_b32 %[ok], 0, %[ok]\n\t"
        "s_cmp_eq_u32 s63, s92\n\t"
        "s_cselect_b32 %[ok], 0, %[ok]\n\t"
        "Lm%=:\n\t"
        "s_waitcnt lgkmcnt(0)\n\t"
        : [c0] "+&s"(c0), [r0] "+&s"(r0), [t1] "+&s"(t1), [t2] "+&s"(t2), [ok] "=&s"(ok)
        : [rs] "s"(rs), [so] "s"(so), [kr] "s"(kr), [fv] "s"(fv), [lb] "s"(lb), [tl] "s"(tl), [t0] "s"(t0), [fp] "s"(fp)
        : "s48", "s49", "s50", "s51", "s52", "s53", "s54", "s55", "s56", "s57", "s58", "s59", "s60", "s61", "s62", "s63", "s64", "s65", "s66", "s67", "s68", "s69", "s70", "s71", "s72", "s73", "s74", "s75", "s76", "s77", "s78", "s79", "s80", "s81", "s82", "s83", "s84", "s85", "s86", "s87", "s88", "s89", "s90", "s91", "s92", "s93", "s94", "s95", "v20", "v21", "vcc", "scc", "memory");
}

__device__ __forceinline__ void r_t1(u32& c0, u32& r0, u32& t1, u32& t2, u32 tv, u32 kr, u32 fv, u32 lb, u32x4 rs, u32 so STP) {
    asm volatile(
        "s_setprio 3\n\t"
        "s_mov_b32 s92, 0x4000\n\t"
        "v_mov_b32 v20, %[lb]\n\t"
        "s_nop 2\n\t"
        "Lr%=:\n\t"
        "buffer_load_dwordx4 v[16:19], %[tv], %[rs], %[so] offen sc1\n\t"
        "s_memrealtime s[64:65]\n\t"
        "s_nop 0\n\t"
        "s_memrealtime s[66:67]\n\t"
        "s_nop 0\n\t"
        "s_memrealtime s[68:69]\n\t"
        "s_nop 0\n\t"
        "s_memrealtime s[70:71]\n\t"
        "s_nop 0\n\t"
        "s_memrealtime s[72:73]\n\t"
        "s_nop 0\n\t"
        "s_memrealtime s[74:75]\n\t"
        "s_memtime s[90:91]\n\t"
        "s_memrealtime s[76:77]\n\t"
        "s_nop 0\n\t"
        "s_memrealtime s[78:79]\n\t"
        "s_nop 0\n\t"
        "s_memrealtime s[80:81]\n\t"
        "s_nop 0\n\t"
        "s_memrealtime s[82:83]\n\t"
        "s_nop 0\n\t"
        "s_memrealtime s[84:85]\n\t"
        "s_nop 0\n\t"
        "s_memrealtime s[86:87]\n\t"
        "s_nop 0\n\t"
        "s_nop 7\n\t"
        "s_waitcnt lgkmcnt(0)\n\t"
        "s_add_u32 s64, s64, s66\n\t"
        "s_add_u32 s64, s64, s68\n\t"
        "s_add_u32 s64, s64, s70\n\t"
        "s_add_u32 s64, s64, s72\n\t"
        "s_add_u32 s64, s64, s74\n\t"
        "s_add_u32 s64, s64, s76\n\t"
        "s_add_u32 s64, s64, s78\n\t"
        "s_add_u32 s64, s64, s80\n\t"
        "s_add_u32 s64, s64, s82\n\t"
        "s_add_u32 s64, s64, s84\n\t"
        "s_add_u32 s64, s64, s86\n\t"
        "s_lshl_b32 s64, s64, 1\n\t"
        "s_mov_b32 %[c0], s90\n\t"
        "s_mov_b32 %[r0], s64\n\t"
        "s_sub_u32 s96, %[r0], 0x100000\n\t"
        "s_waitcnt vmcnt(0)\n\t"
        "v_cmp_eq_u32 vcc, %[fv], v19\n\t"
        "v_subrev_u32 v16, s96, v16\n\t"
        "v_subrev_u32 v17, s96, v17\n\t"
        "v_cndmask_b32 v16, 0, v16, vcc\n\t"
        "v_cndmask_b32 v17, 0, v17, vcc\n\t"
        "v_cndmask_b32 v18, 0, v18, vcc\n\t"
        "v_max_u32_dpp v16, v16, v16 quad_perm:[1,0,3,2] row_mask:0xf bank_mask:0xf\n\t"
        "v_max_u32_dpp v17, v17, v17 quad_perm:[1,0,3,2] row_mask:0xf bank_mask:0xf\n\t"
        "v_max_u32_dpp v18, v18, v18 quad_perm:[1,0,3,2] row_mask:0xf bank_mask:0xf\n\t"
        "v_max_u32_dpp v16, v16, v16 quad_perm:[2,3,0,1] row_mask:0xf bank_mask:0xf\n\t"
        "v_max_u32_dpp v17, v17, v17 quad_perm:[2,3,0,1] row_mask:0xf bank_mask:0xf\n\t"
        "v_max_u32_dpp v18, v18, v18 quad_perm:[2,3,0,1] row_mask:0xf bank_mask:0xf\n\t"
        "v_max_u32_dpp v16, v16, v16 row_half_mirror row_mask:0xf bank_mask:0xf\n\t"
        "v_max_u32_dpp v17, v17, v17 row_half_mirror row_mask:0xf bank_mask:0xf\n\t"
        "v_max_u32_dpp v18, v18, v18 row_half_mirror row_mask:0xf bank_mask:0xf\n\t"
        "s_lshr_b32 s94, vcc_lo, 4\n\t"
        "v_readfirstlane_b32 s93, v16\n\t"
        "v_readfirstlane_b32 %[t1], v17\n\t"
        "v_readfirstlane_b32 %[t2], v18\n\t"
        "s_add_u32 s93, s93, s96\n\t"
        "s_add_u32 %[t1], %[t1], s96\n\t"
        "s_or_b32 s94, s94, vcc_lo\n\t"
        "s_and_b32 s94, s94, 15\n\t"
        "s_cmp_eq_u32 s94, 15\n\t"
        "s_cbranch_scc0 Lx%=\n\t"
        "Lgo%=:\n\t"
        "s_bfe_u32 s95, %[t2], 0x8000c\n\t"
        "s_mul_i32 s95, s95, %[kr]\n\t"
        "s_lshr_b32 s95, s95, 12\n\t"
        "s_min_u32 s95, s95, 512\n\t"
        "s_andn2_b32 s95, s95, 3\n\t"
        "v_mov_b32 v21, s95\n\t"
        "ds_write_b32 v20, v21 offset:16384\n\t"
        "s_sub_u32 s95, s93, %[r0]\n\t"
        "s_mul_i32 s95, s95, %[kr]\n\t"
        "s_ashr_i32 s95, s95, 12\n\t"
        "s_add_u32 s95, s95, %[c0]\n\t"
        "Lda_%=:\n\t"
        "s_memtime s[90:91]\n\t"
        "s_waitcnt lgkmcnt(0)\n\t"
        "s_sub_u32 s88, s95, s90\n\t"
        "s_sub_u32 s89, s88, 336\n\t"
        "s_cmp_gt_u32 s89, 0x4000\n\t"
        "s_cbranch_scc1 Lea_%=\n\t"
        "s_sleep 1\n\t"
        "s_branch Lda_%=\n\t"
        "Lea_%=:\n\t"
        "s_sub_u32 s88, s88, 112\n\t"
        "s_max_i32 s88, s88, 0\n\t"
        "s_min_i32 s88, s88, 224\n\t"
        "s_andn2_b32 s88, s88, 3\n\t"
        "s_getpc_b64 s[90:91]\n\t"
        "Lga_%=:\n\t"
        "s_sub_u32 s90, s90, s88\n\t"
        "s_subb_u32 s91, s91, 0\n\t"
        "s_add_u32 s90, s90, Lza_%=-Lga_%=\n\t"
        "s_addc_u32 s91, s91, 0\n\t"
        "s_setpc_b64 s[90:91]\n\t"
        "s_nop 0\n\ts_nop 0\n\ts_nop 0\n\ts_nop 0\n\t"
        "s_nop 0\n\ts_nop 0\n\ts_nop 0\n\ts_nop 0\n\t"
        "s_nop 0\n\ts_nop 0\n\ts_nop 0\n\ts_nop 0\n\t"
        "s_nop 0\n\ts_nop 0\n\ts_nop 0\n\ts_nop 0\n\t"
        "s_nop 0\n\ts_nop 0\n\ts_nop 0\n\ts_nop 0\n\t"
        "s_nop 0\n\ts_nop 0\n\ts_nop 0\n\ts_nop 0\n\t"
        "s_nop 0\n\ts_nop 0\n\ts_nop 0\n\ts_nop 0\n\t"
        "s_nop 0\n\ts_nop 0\n\ts_nop 0\n\ts_nop 0\n\t"
        "s_nop 0\n\ts_nop 0\n\ts_nop 0\n\ts_nop 0\n\t"
        "s_nop 0\n\ts_nop 0\n\ts_nop 0\n\ts_nop 0\n\t"
        "s_nop 0\n\ts_nop 0\n\ts_nop 0\n\ts_nop 0\n\t"
        "s_nop 0\n\ts_nop 0\n\ts_nop 0\n\ts_nop 0\n\t"
        "s_nop 0\n\ts_nop 0\n\ts_nop 0\n\ts_nop 0\n\t"
        "s_nop 0\n\ts_nop 0\n\ts_nop 0\n\ts_nop 0\n\t"
        "Lza_%=:\n\t"
        "s_barrier\n\t"
        "s_setprio 0\n\t"
        "s_branch Lend%=\n\t"
        "Lx%=:\n\t"
        "s_sub_u32 s92, s92, 1\n\t"
        "s_cbranch_scc1 Lgo%=\n\t"
        "s_branch Lr%=\n\t"
        "Lend%=:\n\t"
        : [c0] "+&s"(c0), [r0] "+&s"(r0), [t1] "+&s"(t1), [t2] "+&s"(t2)
        : [tv] "v"(tv), [rs] "s"(rs), [so] "s"(so), [kr] "s"(kr), [fv] "s"(fv), [lb] "s"(lb)
        : "s64", "s65", "s66", "s67", "s68", "s69", "s70", "s71", "s72", "s73", "s74", "s75", "s76", "s77", "s78", "s79", "s80", "s81", "s82", "s83", "s84", "s85", "s86", "s87", "s88", "s89", "s90", "s91", "s92", "s93", "s94", "s95", "s96", "v16", "v17", "v18", "v19", "v20", "v21", "vcc", "scc", "memory");
}

__device__ __forceinline__ void r_u(u32x32& bn, u32& s, u32& have, u32& left, u32& why, u32 sidx, u32 eo, u32 uw, u32 fa, u32 vm, u32 tp, u32 te, u32 tb, u32x4 rs, u32 lag, u32 lb STP LGP) {
    asm volatile(
        "v_mov_b32 v125, %[lb]\n\t"
        "v_mov_b32 v80, 0xffbfffbf\n\t"
        "v_mov_b32 v81, v80\n\t"
        "ds_write_b64 %[uw], v[80:81] offset:0\n\t"
        "ds_write_b64 %[uw], v[80:81] offset:512\n\t"
        "ds_write_b64 %[uw], v[80:81] offset:1024\n\t"
        "ds_write_b64 %[uw], v[80:81] offset:1536\n\t"
        "Lp%=:\n\t"
        "v_readlane_b32 s82, %[te], %[s]\n\t"
        "v_readlane_b32 s83, %[tb], %[s]\n\t"
        "s_add_u32 s92, %[s], 1\n\t"
        "s_mul_i32 s93, s92, 0x10001\n\t"
        "s_mov_b32 s95, %[left]\n\t"
        "s_mov_b32 s94, 0\n\t"
        "v_mov_b32 v124, s93\n\t"
        "v_readlane_b32 s85, %[tp], %[s]\n\t"
        "s_add_u32 s84, s83, 0x1000\n\t"
        "s_setprio 2\n\t"
        "s_waitcnt lgkmcnt(0)\n\t"
        "s_getpc_b64 s[90:91]\n\t"
        "Lj%=:\n\t"
        "s_sub_u32 s90, s90, %[lag]\n\t"
        "s_subb_u32 s91, s91, 0\n\t"
        "s_add_u32 s90, s90, Lg%=-Lj%=\n\t"
        "s_addc_u32 s91, s91, 0\n\t"
        "s_barrier\n\t"
        "s_setpc_b64 s[90:91]\n\t"
        "s_nop 10\n\t"
        "s_nop 10\n\t"
        "s_nop 10\n\t"
        "Lg%=:\n\t"
        "buffer_load_dwordx4 v[48:51], %[eo], %[rs], s82 offen offset:0 sc1\n\t"
        "buffer_load_dwordx4 v[52:55], %[eo], %[rs], s82 offen offset:1024 sc1\n\t"
        "buffer_load_dwordx4 v[56:59], %[eo], %[rs], s82 offen offset:2048 sc1\n\t"
        "buffer_load_dwordx4 v[60:63], %[eo], %[rs], s82 offen offset:3072 sc1\n\t"
        "v_mov_b64 v[64:65], 0\n\t"
        "v_mov_b64 v[66:67], 0\n\t"
        "v_mov_b64 v[68:69], 0\n\t"
        "v_mov_b64 v[70:71], 0\n\t"
        "v_mov_b64 v[72:73], 0\n\t"
        "v_mov_b64 v[74:75], 0\n\t"
        "v_mov_b64 v[76:77], 0\n\t"
        "v_mov_b64 v[78:79], 0\n\t"
        "s_waitcnt vmcnt(3)\n\t"
        "v_smfmac_f32_16x16x64_bf16 v[64:67], v[48:51], v[16:23], %[sidx]\n\t"
        "s_waitcnt vmcnt(2)\n\t"
        "v_smfmac_f32_16x16x64_bf16 v[68:71], v[52:55], v[24:31], %[sidx]\n\t"
        "s_waitcnt vmcnt(1)\n\t"
        "v_smfmac_f32_16x16x64_bf16 v[72:75], v[56:59], v[32:39], %[sidx]\n\t"
        "v_cvt_pk_bf16_f32 v80, v64, v65\n\t"
        "v_cvt_pk_bf16_f32 v81, v66, v67\n\t"
        "ds_write_b64 %[uw], v[80:81] offset:0\n\t"
        "s_waitcnt vmcnt(0)\n\t"
        "v_smfmac_f32_16x16x64_bf16 v[76:79], v[60:63], v[40:47], %[sidx]\n\t"
        "v_cvt_pk_bf16_f32 v82, v68, v69\n\t"
        "v_cvt_pk_bf16_f32 v83, v70, v71\n\t"
        "ds_write_b64 %[uw], v[82:83] offset:512\n\t"
        "v_cvt_pk_bf16_f32 v84, v72, v73\n\t"
        "v_cvt_pk_bf16_f32 v85, v74, v75\n\t"
        "ds_write_b64 %[uw], v[84:85] offset:1024\n\t"
        "s_nop 1\n\t"
        "v_cvt_pk_bf16_f32 v86, v76, v77\n\t"
        "v_cvt_pk_bf16_f32 v87, v78, v79\n\t"
        "ds_write_b64 %[uw], v[86:87] offset:1536\n\t"
        "s_setprio 0\n\t"
        "s_cmp_lg_u32 s94, 0\n\t"
        "s_cbranch_scc1 Lc%=\n\t"
        "s_waitcnt lgkmcnt(0)\n\t"
        "s_barrier\n\t"
        "ds_read_b32 v88, v125 offset:16392\n\t"
        "s_waitcnt lgkmcnt(0)\n\t"
        "v_cmp_eq_u32 vcc, s93, v88\n\t"
        "s_cbranch_vccnz Lc%=\n\t"
        "Lok%=:\n\t"
        "s_cmp_gt_u32 %[have], s92\n\t"
        "s_mov_b32 %[why], 0\n\t"
        "buffer_load_dword v122, off, %[rs], s85 sc1\n\t"
        "s_cbranch_scc0 Lh%=\n\t"
        "Li%=:\n\t"
        "buffer_load_dwordx4 v[90:93], %[vm], %[rs], s83 offen offset:0 sc1\n\t"
        "buffer_load_dwordx4 v[94:97], %[vm], %[rs], s83 offen offset:1024 sc1\n\t"
        "buffer_load_dwordx4 v[98:101], %[vm], %[rs], s83 offen offset:2048 sc1\n\t"
        "buffer_load_dwordx4 v[102:105], %[vm], %[rs], s83 offen offset:3072 sc1\n\t"
        "buffer_load_dwordx4 v[106:109], %[vm], %[rs], s84 offen offset:0 sc1\n\t"
        "buffer_load_dwordx4 v[110:113], %[vm], %[rs], s84 offen offset:1024 sc1\n\t"
        "buffer_load_dwordx4 v[114:117], %[vm], %[rs], s84 offen offset:2048 sc1\n\t"
        "buffer_load_dwordx4 v[118:121], %[vm], %[rs], s84 offen offset:3072 sc1\n\t"
        "Ln%=:\n\t"
        "ds_read_b32 v88, v125 offset:16396\n\t"
        "v_mov_b32 v80, 0xffbfffbf\n\t"
        "v_mov_b32 v81, v80\n\t"
        "s_sub_u32 s95, s95, %[left]\n\t"
        "s_mov_b32 %[s], s92\n\t"
        "s_waitcnt lgkmcnt(0)\n\t"
        "v_cmp_eq_u32 vcc, s93, v88\n\t"
        "s_cmp_eq_u32 %[s], 63\n\t"
        "s_cbranch_vccnz Lr%=\n\t"
        "s_cbranch_scc1 Lz%=\n\t"
        "s_cmp_lg_u32 %[why], 0\n\t"
        "s_cbranch_scc1 Lend%=\n\t"
        "ds_write_b64 %[uw], v[80:81] offset:0\n\t"
        "ds_write_b64 %[uw], v[80:81] offset:512\n\t"
        "ds_write_b64 %[uw], v[80:81] offset:1024\n\t"
        "ds_write_b64 %[uw], v[80:81] offset:1536\n\t"
        "s_waitcnt vmcnt(6)\n\t"
        "v_mov_b32 v16, v90\n\t"
        "v_mov_b32 v17, v94\n\t"
        "v_mov_b32 v18, v91\n\t"
        "v_mov_b32 v19, v95\n\t"
        "v_mov_b32 v20, v92\n\t"
        "v_mov_b32 v21, v96\n\t"
        "v_mov_b32 v22, v93\n\t"
        "v_mov_b32 v23, v97\n\t"
        "v_readfirstlane_b32 s96, v122\n\t"
        "s_waitcnt vmcnt(4)\n\t"
        "v_mov_b32 v24, v98\n\t"
        "v_mov_b32 v25, v102\n\t"
        "v_mov_b32 v26, v99\n\t"
        "v_mov_b32 v27, v103\n\t"
        "v_mov_b32 v28, v100\n\t"
        "v_mov_b32 v29, v104\n\t"
        "v_mov_b32 v30, v101\n\t"
        "v_mov_b32 v31, v105\n\t"
        "s_waitcnt vmcnt(2)\n\t"
        "v_mov_b32 v32, v106\n\t"
        "v_mov_b32 v33, v110\n\t"
        "v_mov_b32 v34, v107\n\t"
        "v_mov_b32 v35, v111\n\t"
        "v_mov_b32 v36, v108\n\t"
        "v_mov_b32 v37, v112\n\t"
        "v_mov_b32 v38, v109\n\t"
        "v_mov_b32 v39, v113\n\t"
        "s_max_i32 %[have], %[have], s96\n\t"
        "s_waitcnt vmcnt(0)\n\t"
        "v_mov_b32 v40, v114\n\t"
        "v_mov_b32 v41, v118\n\t"
        "v_mov_b32 v42, v115\n\t"
        "v_mov_b32 v43, v119\n\t"
        "v_mov_b32 v44, v116\n\t"
        "v_mov_b32 v45, v120\n\t"
        "v_mov_b32 v46, v117\n\t"
        "v_mov_b32 v47, v121\n\t"
        "s_branch Lp%=\n\t"
        "Lc%=:\n\t"
        "v_max_u32 v88, v48, v49\n\t"
        "v_max3_u32 v88, v88, v50, v51\n\t"
        "v_max3_u32 v88, v88, v52, v53\n\t"
        "v_max3_u32 v88, v88, v54, v55\n\t"
        "v_max3_u32 v88, v88, v56, v57\n\t"
        "v_max3_u32 v88, v88, v58, v59\n\t"
        "v_max3_u32 v88, v88, v60, v61\n\t"
        "v_max3_u32 v88, v88, v62, v63\n\t"
        "v_cmp_eq_u32 vcc, 0xffbfffbf, v88\n\t"
        "s_mov_b32 s94, 1\n\t"
        "s_cbranch_vccz Ld%=\n\t"
        "s_sub_u32 %[left], %[left], 1\n\t"
        "s_cbranch_scc0 Lg%=\n\t"
        "s_mov_b32 %[left], 0\n\t"
        "Ld%=:\n\t"
        "ds_write_b32 %[fa], v124\n\t"
        "s_waitcnt lgkmcnt(0)\n\t"
        "s_barrier\n\t"
        "s_branch Lok%=\n\t"
        "Lh%=:\n\t"
        "s_waitcnt vmcnt(0)\n\t"
        "v_readfirstlane_b32 s96, v122\n\t"
        "s_cmp_eq_u32 s92, 0\n\t"
        "s_cselect_b32 s96, 0, s96\n\t"
        "s_max_i32 %[have], %[have], s96\n\t"
        "s_cmp_gt_u32 %[have], s92\n\t"
        "s_cbranch_scc1 Li%=\n\t"
        "s_mov_b32 %[why], 2\n\t"
        "s_branch Ln%=\n\t"
        "Lr%=:\n\t"
        "s_mov_b32 %[why], 3\n\t"
        "s_branch Lend%=\n\t"
        "Lz%=:\n\t"
        "s_mov_b32 %[why], 0\n\t"
        "Lend%=:\n\t"
        "s_waitcnt vmcnt(0)\n\t"
        : [bn] "+{v[16:47]}"(bn), [s] "+&s"(s), [have] "+&s"(have), [left] "+&s"(left), [why] "=&s"(why)
        : [sidx] "v"(sidx), [eo] "v"(eo), [uw] "v"(uw), [fa] "v"(fa), [vm] "v"(vm), [tp] "v"(tp), [te] "v"(te), [tb] "v"(tb), [rs] "s"(rs), [lag] "s"(lag), [lb] "s"(lb)
        : "s82", "s83", "s84", "s85", "s86", "s87", "s88", "s89", "s90", "s91", "s92", "s93", "s94", "s95", "s96", "s97", "s98", "s99", "v48", "v49", "v50", "v51", "v52", "v53", "v54", "v55", "v56", "v57", "v58", "v59", "v60", "v61", "v62", "v63", "v64", "v65", "v66", "v67", "v68", "v69", "v70", "v71", "v72", "v73", "v74", "v75", "v76", "v77", "v78", "v79", "v80", "v81", "v82", "v83", "v84", "v85", "v86", "v87", "v88", "v89", "v90", "v91", "v92", "v93", "v94", "v95", "v96", "v97", "v98", "v99", "v100", "v101", "v102", "v103", "v104", "v105", "v106", "v107", "v108", "v109", "v110", "v111", "v112", "v113", "v114", "v115", "v116", "v117", "v118", "v119", "v120", "v121", "v122", "v123", "v124", "v125", "vcc", "scc", "memory");
}

__device__ __forceinline__ void r_f(u32& bad, u32& pass, u32 sidx, u32 pv, u32 zv, u32 ra, u32 rb, u32 mo, u32 uf, u32x4 rs, u32 so, u32 sz, u32 sm, u32 lb, u32 fv, u32 cap, u32 zp, u32 px, u32 zx STP) {
    asm volatile(
        "v_mov_b32 v120, %[lb]\n\t"
        "s_add_u32 s92, %[sm], 0x1000\n\t"
        "s_add_u32 s93, %[sm], 0x4000\n\t"
        "s_add_u32 s94, %[sm], 0x5000\n\t"
        "s_xor_b32 s80, %[px], 0x0\n\t"
        "s_add_u32 s80, s80, %[so]\n\t"
        "s_xor_b32 s84, %[zx], 0x0\n\t"
        "s_add_u32 s84, s84, %[sz]\n\t"
        "s_xor_b32 s81, %[px], 0x400\n\t"
        "s_add_u32 s81, s81, %[so]\n\t"
        "s_xor_b32 s85, %[zx], 0x800\n\t"
        "s_add_u32 s85, s85, %[sz]\n\t"
        "s_xor_b32 s82, %[px], 0x8000\n\t"
        "s_add_u32 s82, s82, %[so]\n\t"
        "s_xor_b32 s86, %[zx], 0x1000\n\t"
        "s_add_u32 s86, s86, %[sz]\n\t"
        "s_xor_b32 s83, %[px], 0x8400\n\t"
        "s_add_u32 s83, s83, %[so]\n\t"
        "s_xor_b32 s87, %[zx], 0x1800\n\t"
        "s_add_u32 s87, s87, %[sz]\n\t"
        "s_mov_b32 %[pass], 0\n\t"
        "s_mov_b32 s97, 0\n\t"
        "s_mov_b32 %[bad], 0\n\t"
        "s_setprio 3\n\t"
        "s_barrier\n\t"
        "ds_read_b32 v116, v120 offset:16384\n\t"
        "s_waitcnt lgkmcnt(0)\n\t"
        "v_readfirstlane_b32 s98, v116\n\t"
        "s_nop 3\n\t"
        "s_getpc_b64 s[90:91]\n\t"
        "Lgp_%=:\n\t"
        "s_sub_u32 s90, s90, s98\n\t"
        "s_subb_u32 s91, s91, 0\n\t"
        "s_add_u32 s90, s90, Lzp_%=-Lgp_%=\n\t"
        "s_addc_u32 s91, s91, 0\n\t"
        "s_setpc_b64 s[90:91]\n\t"
        "s_nop 0\n\ts_nop 0\n\ts_nop 0\n\ts_nop 0\n\t"
        "s_nop 0\n\ts_nop 0\n\ts_nop 0\n\ts_nop 0\n\t"
        "s_nop 0\n\ts_nop 0\n\ts_nop 0\n\ts_nop 0\n\t"
        "s_nop 0\n\ts_nop 0\n\ts_nop 0\n\ts_nop 0\n\t"
        "s_nop 0\n\ts_nop 0\n\ts_nop 0\n\ts_nop 0\n\t"
        "s_nop 0\n\ts_nop 0\n\ts_nop 0\n\ts_nop 0\n\t"
        "s_nop 0\n\ts_nop 0\n\ts_nop 0\n\ts_nop 0\n\t"
        "s_nop 0\n\ts_nop 0\n\ts_nop 0\n\ts_nop 0\n\t"
        "s_nop 0\n\ts_nop 0\n\ts_nop 0\n\ts_nop 0\n\t"
        "s_nop 0\n\ts_nop 0\n\ts_nop 0\n\ts_nop 0\n\t"
        "s_nop 0\n\ts_nop 0\n\ts_nop 0\n\ts_nop 0\n\t"
        "s_nop 0\n\ts_nop 0\n\ts_nop 0\n\ts_nop 0\n\t"
        "s_nop 0\n\ts_nop 0\n\ts_nop 0\n\ts_nop 0\n\t"
        "s_nop 0\n\ts_nop 0\n\ts_nop 0\n\ts_nop 0\n\t"
        "s_nop 0\n\ts_nop 0\n\ts_nop 0\n\ts_nop 0\n\t"
        "s_nop 0\n\ts_nop 0\n\ts_nop 0\n\ts_nop 0\n\t"
        "s_nop 0\n\ts_nop 0\n\ts_nop 0\n\ts_nop 0\n\t"
        "s_nop 0\n\ts_nop 0\n\ts_nop 0\n\ts_nop 0\n\t"
        "s_nop 0\n\ts_nop 0\n\ts_nop 0\n\ts_nop 0\n\t"
        "s_nop 0\n\ts_nop 0\n\ts_nop 0\n\ts_nop 0\n\t"
        "s_nop 0\n\ts_nop 0\n\ts_nop 0\n\ts_nop 0\n\t"
        "s_nop 0\n\ts_nop 0\n\ts_nop 0\n\ts_nop 0\n\t"
        "s_nop 0\n\ts_nop 0\n\ts_nop 0\n\ts_nop 0\n\t"
        "s_nop 0\n\ts_nop 0\n\ts_nop 0\n\ts_nop 0\n\t"
        "s_nop 0\n\ts_nop 0\n\ts_nop 0\n\ts_nop 0\n\t"
        "s_nop 0\n\ts_nop 0\n\ts_nop 0\n\ts_nop 0\n\t"
        "s_nop 0\n\ts_nop 0\n\ts_nop 0\n\ts_nop 0\n\t"
        "s_nop 0\n\ts_nop 0\n\ts_nop 0\n\ts_nop 0\n\t"
        "s_nop 0\n\ts_nop 0\n\ts_nop 0\n\ts_nop 0\n\t"
        "s_nop 0\n\ts_nop 0\n\ts_nop 0\n\ts_nop 0\n\t"
        "s_nop 0\n\ts_nop 0\n\ts_nop 0\n\ts_nop 0\n\t"
        "s_nop 0\n\ts_nop 0\n\ts_nop 0\n\ts_nop 0\n\t"
        "Lzp_%=:\n\t"
        "buffer_load_dwordx4 v[48:51], %[zv], %[rs], s84 offen offset:0 sc1\n\t"
        "buffer_load_dwordx4 v[52:55], %[zv], %[rs], s84 offen offset:1024 sc1\n\t"
        "buffer_load_dwordx4 v[56:59], %[zv], %[rs], s85 offen offset:0 sc1\n\t"
        "s_getpc_b64 s[90:91]\n\t"
        "Lgq_%=:\n\t"
        "s_sub_u32 s90, s90, %[zp]\n\t"
        "s_subb_u32 s91, s91, 0\n\t"
        "s_add_u32 s90, s90, Lzq_%=-Lgq_%=\n\t"
        "s_addc_u32 s91, s91, 0\n\t"
        "s_setpc_b64 s[90:91]\n\t"
        "s_nop 0\n\ts_nop 0\n\ts_nop 0\n\ts_nop 0\n\t"
        "s_nop 0\n\ts_nop 0\n\ts_nop 0\n\ts_nop 0\n\t"
        "s_nop 0\n\ts_nop 0\n\ts_nop 0\n\ts_nop 0\n\t"
        "s_nop 0\n\ts_nop 0\n\ts_nop 0\n\ts_nop 0\n\t"
        "s_nop 0\n\ts_nop 0\n\ts_nop 0\n\ts_nop 0\n\t"
        "s_nop 0\n\ts_nop 0\n\ts_nop 0\n\ts_nop 0\n\t"
        "s_nop 0\n\ts_nop 0\n\ts_nop 0\n\ts_nop 0\n\t"
        "s_nop 0\n\ts_nop 0\n\ts_nop 0\n\ts_nop 0\n\t"
        "s_nop 0\n\ts_nop 0\n\ts_nop 0\n\ts_nop 0\n\t"
        "s_nop 0\n\ts_nop 0\n\ts_nop 0\n\ts_nop 0\n\t"
        "s_nop 0\n\ts_nop 0\n\ts_nop 0\n\ts_nop 0\n\t"
        "s_nop 0\n\ts_nop 0\n\ts_nop 0\n\ts_nop 0\n\t"
        "s_nop 0\n\ts_nop 0\n\ts_nop 0\n\ts_nop 0\n\t"
        "s_nop 0\n\ts_nop 0\n\ts_nop 0\n\ts_nop 0\n\t"
        "s_nop 0\n\ts_nop 0\n\ts_nop 0\n\ts_nop 0\n\t"
        "s_nop 0\n\ts_nop 0\n\ts_nop 0\n\ts_nop 0\n\t"
        "Lzq_%=:\n\t"
        "buffer_load_dwordx4 v[14:17], %[pv], %[rs], s80 offen offset:0 sc1\n\t"
        "buffer_load_dwordx4 v[60:63], %[zv], %[rs], s85 offen offset:1024 sc1\n\t"
        "buffer_load_dwordx4 v[18:21], %[pv], %[rs], s80 offen offset:256 sc1\n\t"
        "buffer_load_dwordx4 v[64:67], %[zv], %[rs], s86 offen offset:0 sc1\n\t"
        "buffer_load_dwordx4 v[22:25], %[pv], %[rs], s81 offen offset:0 sc1\n\t"
        "buffer_load_dwordx4 v[68:71], %[zv], %[rs], s86 offen offset:1024 sc1\n\t"
        "buffer_load_dwordx4 v[26:29], %[pv], %[rs], s81 offen offset:256 sc1\n\t"
        "buffer_load_dwordx4 v[72:75], %[zv], %[rs], s87 offen offset:0 sc1\n\t"
        "buffer_load_dwordx4 v[30:33], %[pv], %[rs], s82 offen offset:0 sc1\n\t"
        "buffer_load_dwordx4 v[76:79], %[zv], %[rs], s87 offen offset:1024 sc1\n\t"
        "buffer_load_dwordx4 v[34:37], %[pv], %[rs], s82 offen offset:256 sc1\n\t"
        "buffer_load_dwordx4 v[38:41], %[pv], %[rs], s83 offen offset:0 sc1\n\t"
        "buffer_load_dwordx4 v[42:45], %[pv], %[rs], s83 offen offset:256 sc1\n\t"
        "v_mov_b64 v[112:113], 0\n\t"
        "v_mov_b64 v[114:115], 0\n\t"
        "s_cmp_lg_u32 s97, 0\n\t"
        "s_cbranch_scc1 Lwu%=\n\t"
        "Lrd%=:\n\t"
        "s_waitcnt vmcnt(13)\n\t"
        "ds_read_b128 v[80:83], %[ra] offset:0\n\t"
        "ds_read_b128 v[84:87], %[ra] offset:1024\n\t"
        "s_waitcnt vmcnt(12)\n\t"
        "v_mov_b64 v[46:47], v[14:15]\n\t"
        "s_waitcnt vmcnt(10)\n\t"
        "v_smfmac_f32_16x16x64_bf16 v[112:115], v[16:19], v[48:55], %[sidx]\n\t"
        "s_waitcnt vmcnt(9)\n\t"
        "ds_read_b128 v[88:91], %[ra] offset:2048\n\t"
        "ds_read_b128 v[92:95], %[ra] offset:3072\n\t"
        "s_waitcnt vmcnt(8) lgkmcnt(2)\n\t"
        "v_smfmac_f32_16x16x64_bf16 v[112:115], v[20:23], v[80:87], %[sidx]\n\t"
        "s_waitcnt vmcnt(6)\n\t"
        "v_smfmac_f32_16x16x64_bf16 v[112:115], v[24:27], v[56:63], %[sidx]\n\t"
        "s_waitcnt vmcnt(5)\n\t"
        "ds_read_b128 v[96:99], %[ra] offset:4096\n\t"
        "ds_read_b128 v[100:103], %[ra] offset:5120\n\t"
        "ds_read_b128 v[104:107], %[rb] offset:6144\n\t"
        "ds_read_b128 v[108:111], %[rb] offset:7168\n\t"
        "s_waitcnt vmcnt(4) lgkmcnt(4)\n\t"
        "v_smfmac_f32_16x16x64_bf16 v[112:115], v[28:31], v[88:95], %[sidx]\n\t"
        "s_waitcnt vmcnt(2)\n\t"
        "v_smfmac_f32_16x16x64_bf16 v[112:115], v[32:35], v[64:71], %[sidx]\n\t"
        "s_waitcnt vmcnt(1) lgkmcnt(2)\n\t"
        "v_smfmac_f32_16x16x64_bf16 v[112:115], v[36:39], v[96:103], %[sidx]\n\t"
        "s_waitcnt vmcnt(0)\n\t"
        "v_smfmac_f32_16x16x64_bf16 v[112:115], v[40:43], v[72:79], %[sidx]\n\t"
        "s_waitcnt vmcnt(0) lgkmcnt(0)\n\t"
        "v_smfmac_f32_16x16x64_bf16 v[112:115], v[44:47], v[104:111], %[sidx]\n\t"
        "s_nop 7\n\t"
        "v_add_f32 v116, v112, v114\n\t"
        "v_add_f32 v117, v113, v115\n\t"
        "v_add_f32 v118, v113, v115\n\t"
        "v_mov_b32_dpp v118, v116 row_ror:8 row_mask:0xf bank_mask:0x3\n\t"
        "v_mov_b32_dpp v116, v117 row_ror:8 row_mask:0xf bank_mask:0xc\n\t"
        "v_cvt_pk_bf16_f32 v119, v116, v118\n\t"
        "v_cmpx_o_f32 vcc, v116, v118\n\t"
        "buffer_store_dword v119, %[mo], %[rs], %[sm] offen sc1\n\t"
        "buffer_store_dword v119, %[mo], %[rs], s92 offen sc1\n\t"
        "buffer_store_dword v119, %[mo], %[rs], s93 offen sc1\n\t"
        "buffer_store_dword v119, %[mo], %[rs], s94 offen sc1\n\t"
        "s_nop 2\n\t"
        "s_cmp_eq_u64 exec, -1\n\t"
        "s_mov_b64 exec, -1\n\t"
        "s_cbranch_scc0 Lag%=\n\t"
        "s_barrier\n\t"
        "s_branch Lend%=\n\t"
        "Lag%=:\n\t"
        "s_add_u32 %[pass], %[pass], 1\n\t"
        "s_cmp_eq_u32 s97, 0\n\t"
        "s_cbranch_scc1 Lfi%=\n\t"
        "v_max_u32 v116, v16, v17\n\t"
        "v_max3_u32 v116, v116, v18, v19\n\t"
        "v_max3_u32 v116, v116, v20, v21\n\t"
        "v_max3_u32 v116, v116, v22, v23\n\t"
        "v_max3_u32 v116, v116, v24, v25\n\t"
        "v_max3_u32 v116, v116, v26, v27\n\t"
        "v_max3_u32 v116, v116, v28, v29\n\t"
        "v_max3_u32 v116, v116, v30, v31\n\t"
        "v_max3_u32 v116, v116, v32, v33\n\t"
        "v_max3_u32 v116, v116, v34, v35\n\t"
        "v_max3_u32 v116, v116, v36, v37\n\t"
        "v_max3_u32 v116, v116, v38, v39\n\t"
        "v_max3_u32 v116, v116, v40, v41\n\t"
        "v_max3_u32 v116, v116, v42, v43\n\t"
        "v_max3_u32 v116, v116, v44, v45\n\t"
        "v_max3_u32 v116, v116, v46, v47\n\t"
        "v_max3_u32 v116, v116, v48, v49\n\t"
        "v_max3_u32 v116, v116, v50, v51\n\t"
        "v_max3_u32 v116, v116, v52, v53\n\t"
        "v_max3_u32 v116, v116, v54, v55\n\t"
        "v_max3_u32 v116, v116, v56, v57\n\t"
        "v_max3_u32 v116, v116, v58, v59\n\t"
        "v_max3_u32 v116, v116, v60, v61\n\t"
        "v_max3_u32 v116, v116, v62, v63\n\t"
        "v_max3_u32 v116, v116, v64, v65\n\t"
        "v_max3_u32 v116, v116, v66, v67\n\t"
        "v_max3_u32 v116, v116, v68, v69\n\t"
        "v_max3_u32 v116, v116, v70, v71\n\t"
        "v_max3_u32 v116, v116, v72, v73\n\t"
        "v_max3_u32 v116, v116, v74, v75\n\t"
        "v_max3_u32 v116, v116, v76, v77\n\t"
        "v_max3_u32 v116, v116, v78, v79\n\t"
        "v_max3_u32 v116, v116, v80, v81\n\t"
        "v_max3_u32 v116, v116, v82, v83\n\t"
        "v_max3_u32 v116, v116, v84, v85\n\t"
        "v_max3_u32 v116, v116, v86, v87\n\t"
        "v_max3_u32 v116, v116, v88, v89\n\t"
        "v_max3_u32 v116, v116, v90, v91\n\t"
        "v_max3_u32 v116, v116, v92, v93\n\t"
        "v_max3_u32 v116, v116, v94, v95\n\t"
        "v_max3_u32 v116, v116, v96, v97\n\t"
        "v_max3_u32 v116, v116, v98, v99\n\t"
        "v_max3_u32 v116, v116, v100, v101\n\t"
        "v_max3_u32 v116, v116, v102, v103\n\t"
        "v_max3_u32 v116, v116, v104, v105\n\t"
        "v_max3_u32 v116, v116, v106, v107\n\t"
        "v_max3_u32 v116, v116, v108, v109\n\t"
        "v_max3_u32 v116, v116, v110, v111\n\t"
        "v_cmp_eq_u32 vcc, 0xffbfffbf, v116\n\t"
        "s_cmp_lt_u32 %[pass], %[cap]\n\t"
        "s_cbranch_vccz Lbad%=\n\t"
        "s_cbranch_scc1 Lzp_%=\n\t"
        "Lbad%=:\n\t"
        "s_mov_b32 %[bad], 1\n\t"
        "s_branch Lend%=\n\t"
        "Lfi%=:\n\t"
        "s_mov_b32 s97, 1\n\t"
        "v_mov_b32 v116, %[fv]\n\t"
        "ds_write_b32 v120, v116 offset:16392\n\t"
        "s_waitcnt lgkmcnt(0)\n\t"
        "s_barrier\n\t"
        "s_sleep 2\n\t"
        "s_branch Lzp_%=\n\t"
        "Lwu%=:\n\t"
        "ds_read_b32 v121, %[uf]\n\t"
        "s_waitcnt lgkmcnt(0)\n\t"
        "v_cmp_ne_u32 vcc, %[fv], v121\n\t"
        "s_cbranch_vccnz Lwu%=\n\t"
        "s_branch Lrd%=\n\t"
        "Lend%=:\n\t"
        "s_setprio 0\n\t"
        : [bad] "=&s"(bad), [pass] "=&s"(pass)
        : [sidx] "v"(sidx), [pv] "v"(pv), [zv] "v"(zv), [ra] "v"(ra), [rb] "v"(rb), [mo] "v"(mo), [uf] "v"(uf), [rs] "s"(rs), [so] "s"(so), [sz] "s"(sz), [sm] "s"(sm), [lb] "s"(lb), [fv] "s"(fv), [cap] "s"(cap), [zp] "s"(zp), [px] "s"(px), [zx] "s"(zx)
        : "s80", "s81", "s82", "s83", "s84", "s85", "s86", "s87", "s88", "s89", "s90", "s91", "s92", "s93", "s94", "s95", "s96", "s97", "s98", "v14", "v15", "v16", "v17", "v18", "v19", "v20", "v21", "v22", "v23", "v24", "v25", "v26", "v27", "v28", "v29", "v30", "v31", "v32", "v33", "v34", "v35", "v36", "v37", "v38", "v39", "v40", "v41", "v42", "v43", "v44", "v45", "v46", "v47", "v48", "v49", "v50", "v51", "v52", "v53", "v54", "v55", "v56", "v57", "v58", "v59", "v60", "v61", "v62", "v63", "v64", "v65", "v66", "v67", "v68", "v69", "v70", "v71", "v72", "v73", "v74", "v75", "v76", "v77", "v78", "v79", "v80", "v81", "v82", "v83", "v84", "v85", "v86", "v87", "v88", "v89", "v90", "v91", "v92", "v93", "v94", "v95", "v96", "v97", "v98", "v99", "v100", "v101", "v102", "v103", "v104", "v105", "v106", "v107", "v108", "v109", "v110", "v111", "v112", "v113", "v114", "v115", "v116", "v117", "v118", "v119", "v120", "v121", "vcc", "scc", "memory");
}

__device__ __forceinline__ void wx0(const u32x32 (&W)[5], u32 xa, u32 oa, u32x4 rs, u32 xv, u32 xs, u32 xd, u32 o0, u32 o1 STP) {
    asm volatile(
        "ds_read_b128 v[192:195], %[xa] offset:0\n\t"
        "ds_read_b128 v[196:199], %[xa] offset:20480\n\t"
        "ds_read_b128 v[200:203], %[xa] offset:1024\n\t"
        "ds_read_b128 v[204:207], %[xa] offset:21504\n\t"
        "ds_read_b128 v[208:211], %[xa] offset:2048\n\t"
        "ds_read_b128 v[212:215], %[xa] offset:22528\n\t"
        "s_waitcnt vmcnt(8)\n\t"
        "s_barrier\n\t"
        "s_waitcnt lgkmcnt(4)\n\t"
        "v_mfma_f32_16x16x32_bf16 v[176:179], v[16:19], v[192:195], 0\n\t"
        "ds_read_b128 v[216:219], %[xa] offset:3072\n\t"
        "v_mfma_f32_16x16x32_bf16 v[184:187], v[16:19], v[196:199], 0\n\t"
        "ds_read_b128 v[220:223], %[xa] offset:23552\n\t"
        "v_mfma_f32_16x16x32_bf16 v[180:183], v[96:99], v[192:195], 0\n\t"
        "v_mfma_f32_16x16x32_bf16 v[188:191], v[96:99], v[196:199], 0\n\t"
        "s_waitcnt lgkmcnt(4)\n\t"
        "v_mfma_f32_16x16x32_bf16 v[176:179], v[20:23], v[200:203], v[176:179]\n\t"
        "ds_read_b128 v[192:195], %[xa] offset:4096\n\t"
        "v_mfma_f32_16x16x32_bf16 v[184:187], v[20:23], v[204:207], v[184:187]\n\t"
        "ds_read_b128 v[196:199], %[xa] offset:24576\n\t"
        "v_mfma_f32_16x16x32_bf16 v[180:183], v[100:103], v[200:203], v[180:183]\n\t"
        "v_mfma_f32_16x16x32_bf16 v[188:191], v[100:103], v[204:207], v[188:191]\n\t"
        "s_waitcnt lgkmcnt(4)\n\t"
        "v_mfma_f32_16x16x32_bf16 v[176:179], v[24:27], v[208:211], v[176:179]\n\t"
        "ds_read_b128 v[200:203], %[xa] offset:5120\n\t"
        "v_mfma_f32_16x16x32_bf16 v[184:187], v[24:27], v[212:215], v[184:187]\n\t"
        "ds_read_b128 v[204:207], %[xa] offset:25600\n\t"
        "v_mfma_f32_16x16x32_bf16 v[180:183], v[104:107], v[208:211], v[180:183]\n\t"
        "v_mfma_f32_16x16x32_bf16 v[188:191], v[104:107], v[212:215], v[188:191]\n\t"
        "s_waitcnt lgkmcnt(4)\n\t"
        "v_mfma_f32_16x16x32_bf16 v[176:179], v[28:31], v[216:219], v[176:179]\n\t"
        "ds_read_b128 v[208:211], %[xa] offset:6144\n\t"
        "v_mfma_f32_16x16x32_bf16 v[184:187], v[28:31], v[220:223], v[184:187]\n\t"
        "ds_read_b128 v[212:215], %[xa] offset:26624\n\t"
        "v_mfma_f32_16x16x32_bf16 v[180:183], v[108:111], v[216:219], v[180:183]\n\t"
        "v_mfma_f32_16x16x32_bf16 v[188:191], v[108:111], v[220:223], v[188:191]\n\t"
        "s_waitcnt lgkmcnt(4)\n\t"
        "v_mfma_f32_16x16x32_bf16 v[176:179], v[32:35], v[192:195], v[176:179]\n\t"
        "ds_read_b128 v[216:219], %[xa] offset:7168\n\t"
        "v_mfma_f32_16x16x32_bf16 v[184:187], v[32:35], v[196:199], v[184:187]\n\t"
        "ds_read_b128 v[220:223], %[xa] offset:27648\n\t"
        "v_mfma_f32_16x16x32_bf16 v[180:183], v[112:115], v[192:195], v[180:183]\n\t"
        "v_mfma_f32_16x16x32_bf16 v[188:191], v[112:115], v[196:199], v[188:191]\n\t"
        "s_waitcnt lgkmcnt(4)\n\t"
        "v_mfma_f32_16x16x32_bf16 v[176:179], v[36:39], v[200:203], v[176:179]\n\t"
        "ds_read_b128 v[192:195], %[xa] offset:8192\n\t"
        "v_mfma_f32_16x16x32_bf16 v[184:187], v[36:39], v[204:207], v[184:187]\n\t"
        "ds_read_b128 v[196:199], %[xa] offset:28672\n\t"
        "v_mfma_f32_16x16x32_bf16 v[180:183], v[116:119], v[200:203], v[180:183]\n\t"
        "v_mfma_f32_16x16x32_bf16 v[188:191], v[116:119], v[204:207], v[188:191]\n\t"
        "s_waitcnt lgkmcnt(4)\n\t"
        "v_mfma_f32_16x16x32_bf16 v[176:179], v[40:43], v[208:211], v[176:179]\n\t"
        "ds_read_b128 v[200:203], %[xa] offset:9216\n\t"
        "v_mfma_f32_16x16x32_bf16 v[184:187], v[40:43], v[212:215], v[184:187]\n\t"
        "ds_read_b128 v[204:207], %[xa] offset:29696\n\t"
        "v_mfma_f32_16x16x32_bf16 v[180:183], v[120:123], v[208:211], v[180:183]\n\t"
        "v_mfma_f32_16x16x32_bf16 v[188:191], v[120:123], v[212:215], v[188:191]\n\t"
        "s_waitcnt lgkmcnt(4)\n\t"
        "v_mfma_f32_16x16x32_bf16 v[176:179], v[44:47], v[216:219], v[176:179]\n\t"
        "ds_read_b128 v[208:211], %[xa] offset:10240\n\t"
        "v_mfma_f32_16x16x32_bf16 v[184:187], v[44:47], v[220:223], v[184:187]\n\t"
        "ds_read_b128 v[212:215], %[xa] offset:30720\n\t"
        "v_mfma_f32_16x16x32_bf16 v[180:183], v[124:127], v[216:219], v[180:183]\n\t"
        "v_mfma_f32_16x16x32_bf16 v[188:191], v[124:127], v[220:223], v[188:191]\n\t"
        "s_waitcnt lgkmcnt(4)\n\t"
        "v_mfma_f32_16x16x32_bf16 v[176:179], v[48:51], v[192:195], v[176:179]\n\t"
        "ds_read_b128 v[216:219], %[xa] offset:11264\n\t"
        "v_mfma_f32_16x16x32_bf16 v[184:187], v[48:51], v[196:199], v[184:187]\n\t"
        "ds_read_b128 v[220:223], %[xa] offset:31744\n\t"
        "v_mfma_f32_16x16x32_bf16 v[180:183], v[128:131], v[192:195], v[180:183]\n\t"
        "v_mfma_f32_16x16x32_bf16 v[188:191], v[128:131], v[196:199], v[188:191]\n\t"
        "s_waitcnt lgkmcnt(4)\n\t"
        "v_mfma_f32_16x16x32_bf16 v[176:179], v[52:55], v[200:203], v[176:179]\n\t"
        "ds_read_b128 v[192:195], %[xa] offset:12288\n\t"
        "v_mfma_f32_16x16x32_bf16 v[184:187], v[52:55], v[204:207], v[184:187]\n\t"
        "ds_read_b128 v[196:199], %[xa] offset:32768\n\t"
        "v_mfma_f32_16x16x32_bf16 v[180:183], v[132:135], v[200:203], v[180:183]\n\t"
        "v_mfma_f32_16x16x32_bf16 v[188:191], v[132:135], v[204:207], v[188:191]\n\t"
        "s_waitcnt lgkmcnt(4)\n\t"
        "v_mfma_f32_16x16x32_bf16 v[176:179], v[56:59], v[208:211], v[176:179]\n\t"
        "ds_read_b128 v[200:203], %[xa] offset:13312\n\t"
        "v_mfma_f32_16x16x32_bf16 v[184:187], v[56:59], v[212:215], v[184:187]\n\t"
        "ds_read_b128 v[204:207], %[xa] offset:33792\n\t"
        "v_mfma_f32_16x16x32_bf16 v[180:183], v[136:139], v[208:211], v[180:183]\n\t"
        "v_mfma_f32_16x16x32_bf16 v[188:191], v[136:139], v[212:215], v[188:191]\n\t"
        "s_waitcnt lgkmcnt(4)\n\t"
        "v_mfma_f32_16x16x32_bf16 v[176:179], v[60:63], v[216:219], v[176:179]\n\t"
        "ds_read_b128 v[208:211], %[xa] offset:14336\n\t"
        "v_mfma_f32_16x16x32_bf16 v[184:187], v[60:63], v[220:223], v[184:187]\n\t"
        "ds_read_b128 v[212:215], %[xa] offset:34816\n\t"
        "v_mfma_f32_16x16x32_bf16 v[180:183], v[140:143], v[216:219], v[180:183]\n\t"
        "v_mfma_f32_16x16x32_bf16 v[188:191], v[140:143], v[220:223], v[188:191]\n\t"
        "s_waitcnt lgkmcnt(4)\n\t"
        "v_mfma_f32_16x16x32_bf16 v[176:179], v[64:67], v[192:195], v[176:179]\n\t"
        "ds_read_b128 v[216:219], %[xa] offset:15360\n\t"
        "v_mfma_f32_16x16x32_bf16 v[184:187], v[64:67], v[196:199], v[184:187]\n\t"
        "ds_read_b128 v[220:223], %[xa] offset:35840\n\t"
        "v_mfma_f32_16x16x32_bf16 v[180:183], v[144:147], v[192:195], v[180:183]\n\t"
        "v_mfma_f32_16x16x32_bf16 v[188:191], v[144:147], v[196:199], v[188:191]\n\t"
        "s_waitcnt lgkmcnt(4)\n\t"
        "v_mfma_f32_16x16x32_bf16 v[176:179], v[68:71], v[200:203], v[176:179]\n\t"
        "ds_read_b128 v[192:195], %[xa] offset:16384\n\t"
        "v_mfma_f32_16x16x32_bf16 v[184:187], v[68:71], v[204:207], v[184:187]\n\t"
        "ds_read_b128 v[196:199], %[xa] offset:36864\n\t"
        "v_mfma_f32_16x16x32_bf16 v[180:183], v[148:151], v[200:203], v[180:183]\n\t"
        "v_mfma_f32_16x16x32_bf16 v[188:191], v[148:151], v[204:207], v[188:191]\n\t"
        "s_waitcnt lgkmcnt(4)\n\t"
        "v_mfma_f32_16x16x32_bf16 v[176:179], v[72:75], v[208:211], v[176:179]\n\t"
        "ds_read_b128 v[200:203], %[xa] offset:17408\n\t"
        "v_mfma_f32_16x16x32_bf16 v[184:187], v[72:75], v[212:215], v[184:187]\n\t"
        "ds_read_b128 v[204:207], %[xa] offset:37888\n\t"
        "v_mfma_f32_16x16x32_bf16 v[180:183], v[152:155], v[208:211], v[180:183]\n\t"
        "v_mfma_f32_16x16x32_bf16 v[188:191], v[152:155], v[212:215], v[188:191]\n\t"
        "s_waitcnt lgkmcnt(4)\n\t"
        "v_mfma_f32_16x16x32_bf16 v[176:179], v[76:79], v[216:219], v[176:179]\n\t"
        "ds_read_b128 v[208:211], %[xa] offset:18432\n\t"
        "v_mfma_f32_16x16x32_bf16 v[184:187], v[76:79], v[220:223], v[184:187]\n\t"
        "ds_read_b128 v[212:215], %[xa] offset:38912\n\t"
        "v_mfma_f32_16x16x32_bf16 v[180:183], v[156:159], v[216:219], v[180:183]\n\t"
        "v_mfma_f32_16x16x32_bf16 v[188:191], v[156:159], v[220:223], v[188:191]\n\t"
        "s_waitcnt lgkmcnt(4)\n\t"
        "v_mfma_f32_16x16x32_bf16 v[176:179], v[80:83], v[192:195], v[176:179]\n\t"
        "ds_read_b128 v[216:219], %[xa] offset:19456\n\t"
        "v_mfma_f32_16x16x32_bf16 v[184:187], v[80:83], v[196:199], v[184:187]\n\t"
        "ds_read_b128 v[220:223], %[xa] offset:39936\n\t"
        "v_mfma_f32_16x16x32_bf16 v[180:183], v[160:163], v[192:195], v[180:183]\n\t"
        "v_mfma_f32_16x16x32_bf16 v[188:191], v[160:163], v[196:199], v[188:191]\n\t"
        "s_waitcnt lgkmcnt(4)\n\t"
        "v_mfma_f32_16x16x32_bf16 v[176:179], v[84:87], v[200:203], v[176:179]\n\t"
        "v_mfma_f32_16x16x32_bf16 v[184:187], v[84:87], v[204:207], v[184:187]\n\t"
        "v_mfma_f32_16x16x32_bf16 v[180:183], v[164:167], v[200:203], v[180:183]\n\t"
        "v_mfma_f32_16x16x32_bf16 v[188:191], v[164:167], v[204:207], v[188:191]\n\t"
        "s_waitcnt lgkmcnt(2)\n\t"
        "v_mfma_f32_16x16x32_bf16 v[176:179], v[88:91], v[208:211], v[176:179]\n\t"
        "v_mfma_f32_16x16x32_bf16 v[184:187], v[88:91], v[212:215], v[184:187]\n\t"
        "v_mfma_f32_16x16x32_bf16 v[180:183], v[168:171], v[208:211], v[180:183]\n\t"
        "v_mfma_f32_16x16x32_bf16 v[188:191], v[168:171], v[212:215], v[188:191]\n\t"
        "s_waitcnt lgkmcnt(0)\n\t"
        "v_mfma_f32_16x16x32_bf16 v[176:179], v[92:95], v[216:219], v[176:179]\n\t"
        "v_mfma_f32_16x16x32_bf16 v[184:187], v[92:95], v[220:223], v[184:187]\n\t"
        "v_mfma_f32_16x16x32_bf16 v[180:183], v[172:175], v[216:219], v[180:183]\n\t"
        "v_mfma_f32_16x16x32_bf16 v[188:191], v[172:175], v[220:223], v[188:191]\n\t"
        "s_nop 4\n\t"
        "v_cvt_pk_bf16_f32 v224, v176, v177\n\t"
        "v_cvt_pk_bf16_f32 v225, v178, v179\n\t"
        "v_cvt_pk_bf16_f32 v226, v180, v181\n\t"
        "v_cvt_pk_bf16_f32 v227, v182, v183\n\t"
        "buffer_store_dwordx4 v[224:227], %[oa], %[rs], %[o0] offen sc1\n\t"
        "v_cvt_pk_bf16_f32 v228, v184, v185\n\t"
        "v_cvt_pk_bf16_f32 v229, v186, v187\n\t"
        "v_cvt_pk_bf16_f32 v230, v188, v189\n\t"
        "v_cvt_pk_bf16_f32 v231, v190, v191\n\t"
        "buffer_store_dwordx4 v[228:231], %[oa], %[rs], %[o1] offen sc1\n\t"
        "s_mov_b32 s88, m0\n\t"
        "s_mov_b32 m0, %[xd]\n\t"
        "s_add_u32 s89, %[xs], 0x1000\n\t"
        "buffer_load_dwordx4 %[xv], %[rs], %[xs] offen offset:0 sc1 lds\n\t"
        "buffer_load_dwordx4 %[xv], %[rs], %[xs] offen offset:1024 sc1 lds\n\t"
        "buffer_load_dwordx4 %[xv], %[rs], %[xs] offen offset:2048 sc1 lds\n\t"
        "buffer_load_dwordx4 %[xv], %[rs], %[xs] offen offset:3072 sc1 lds\n\t"
        "s_add_u32 m0, m0, 0x1000\n\t"
        "s_nop 0\n\t"
        "buffer_load_dwordx4 %[xv], %[rs], s89 offen offset:0 sc1 lds\n\t"
        "buffer_load_dwordx4 %[xv], %[rs], s89 offen offset:1024 sc1 lds\n\t"
        "buffer_load_dwordx4 %[xv], %[rs], s89 offen offset:2048 sc1 lds\n\t"
        "buffer_load_dwordx4 %[xv], %[rs], s89 offen offset:3072 sc1 lds\n\t"
        "s_mov_b32 m0, s88\n\t"
        :
        : [w0] "{v[16:47]}"(W[0]), [w1] "{v[48:79]}"(W[1]), [w2] "{v[80:111]}"(W[2]), [w3] "{v[112:143]}"(W[3]), [w4] "{v[144:175]}"(W[4]), [xa] "v"(xa), [oa] "v"(oa), [xv] "v"(xv), [xs] "s"(xs), [xd] "s"(xd), [rs] "s"(rs), [o0] "s"(o0), [o1] "s"(o1)
        : "s88", "s89", "v176", "v177", "v178", "v179", "v180", "v181", "v182", "v183", "v184", "v185", "v186", "v187", "v188", "v189", "v190", "v191", "v192", "v193", "v194", "v195", "v196", "v197", "v198", "v199", "v200", "v201", "v202", "v203", "v204", "v205", "v206", "v207", "v208", "v209", "v210", "v211", "v212", "v213", "v214", "v215", "v216", "v217", "v218", "v219", "v220", "v221", "v222", "v223", "v224", "v225", "v226", "v227", "v228", "v229", "v230", "v231", "vcc", "scc", "memory");
}

__device__ __forceinline__ void wx1(const u32x32 (&W)[5], u32 xa, u32 oa STP) {
    asm volatile(
        "ds_read_b128 v[192:195], %[xa] offset:0\n\t"
        "ds_read_b128 v[196:199], %[xa] offset:20480\n\t"
        "ds_read_b128 v[200:203], %[xa] offset:1024\n\t"
        "ds_read_b128 v[204:207], %[xa] offset:21504\n\t"
        "ds_read_b128 v[208:211], %[xa] offset:2048\n\t"
        "ds_read_b128 v[212:215], %[xa] offset:22528\n\t"
        "s_waitcnt lgkmcnt(6)\n\t"
        "s_barrier\n\t"
        "s_waitcnt lgkmcnt(4)\n\t"
        "v_mfma_f32_16x16x32_bf16 v[176:179], v[16:19], v[192:195], 0\n\t"
        "ds_read_b128 v[216:219], %[xa] offset:3072\n\t"
        "v_mfma_f32_16x16x32_bf16 v[184:187], v[16:19], v[196:199], 0\n\t"
        "ds_read_b128 v[220:223], %[xa] offset:23552\n\t"
        "v_mfma_f32_16x16x32_bf16 v[180:183], v[96:99], v[192:195], 0\n\t"
        "v_mfma_f32_16x16x32_bf16 v[188:191], v[96:99], v[196:199], 0\n\t"
        "s_waitcnt lgkmcnt(4)\n\t"
        "v_mfma_f32_16x16x32_bf16 v[176:179], v[20:23], v[200:203], v[176:179]\n\t"
        "ds_read_b128 v[192:195], %[xa] offset:4096\n\t"
        "v_mfma_f32_16x16x32_bf16 v[184:187], v[20:23], v[204:207], v[184:187]\n\t"
        "ds_read_b128 v[196:199], %[xa] offset:24576\n\t"
        "v_mfma_f32_16x16x32_bf16 v[180:183], v[100:103], v[200:203], v[180:183]\n\t"
        "v_mfma_f32_16x16x32_bf16 v[188:191], v[100:103], v[204:207], v[188:191]\n\t"
        "s_waitcnt lgkmcnt(4)\n\t"
        "v_mfma_f32_16x16x32_bf16 v[176:179], v[24:27], v[208:211], v[176:179]\n\t"
        "ds_read_b128 v[200:203], %[xa] offset:5120\n\t"
        "v_mfma_f32_16x16x32_bf16 v[184:187], v[24:27], v[212:215], v[184:187]\n\t"
        "ds_read_b128 v[204:207], %[xa] offset:25600\n\t"
        "v_mfma_f32_16x16x32_bf16 v[180:183], v[104:107], v[208:211], v[180:183]\n\t"
        "v_mfma_f32_16x16x32_bf16 v[188:191], v[104:107], v[212:215], v[188:191]\n\t"
        "s_waitcnt lgkmcnt(4)\n\t"
        "v_mfma_f32_16x16x32_bf16 v[176:179], v[28:31], v[216:219], v[176:179]\n\t"
        "ds_read_b128 v[208:211], %[xa] offset:6144\n\t"
        "v_mfma_f32_16x16x32_bf16 v[184:187], v[28:31], v[220:223], v[184:187]\n\t"
        "ds_read_b128 v[212:215], %[xa] offset:26624\n\t"
        "v_mfma_f32_16x16x32_bf16 v[180:183], v[108:111], v[216:219], v[180:183]\n\t"
        "v_mfma_f32_16x16x32_bf16 v[188:191], v[108:111], v[220:223], v[188:191]\n\t"
        "s_waitcnt lgkmcnt(4)\n\t"
        "v_mfma_f32_16x16x32_bf16 v[176:179], v[32:35], v[192:195], v[176:179]\n\t"
        "ds_read_b128 v[216:219], %[xa] offset:7168\n\t"
        "v_mfma_f32_16x16x32_bf16 v[184:187], v[32:35], v[196:199], v[184:187]\n\t"
        "ds_read_b128 v[220:223], %[xa] offset:27648\n\t"
        "v_mfma_f32_16x16x32_bf16 v[180:183], v[112:115], v[192:195], v[180:183]\n\t"
        "v_mfma_f32_16x16x32_bf16 v[188:191], v[112:115], v[196:199], v[188:191]\n\t"
        "s_waitcnt lgkmcnt(4)\n\t"
        "v_mfma_f32_16x16x32_bf16 v[176:179], v[36:39], v[200:203], v[176:179]\n\t"
        "ds_read_b128 v[192:195], %[xa] offset:8192\n\t"
        "v_mfma_f32_16x16x32_bf16 v[184:187], v[36:39], v[204:207], v[184:187]\n\t"
        "ds_read_b128 v[196:199], %[xa] offset:28672\n\t"
        "v_mfma_f32_16x16x32_bf16 v[180:183], v[116:119], v[200:203], v[180:183]\n\t"
        "v_mfma_f32_16x16x32_bf16 v[188:191], v[116:119], v[204:207], v[188:191]\n\t"
        "s_waitcnt lgkmcnt(4)\n\t"
        "v_mfma_f32_16x16x32_bf16 v[176:179], v[40:43], v[208:211], v[176:179]\n\t"
        "ds_read_b128 v[200:203], %[xa] offset:9216\n\t"
        "v_mfma_f32_16x16x32_bf16 v[184:187], v[40:43], v[212:215], v[184:187]\n\t"
        "ds_read_b128 v[204:207], %[xa] offset:29696\n\t"
        "v_mfma_f32_16x16x32_bf16 v[180:183], v[120:123], v[208:211], v[180:183]\n\t"
        "v_mfma_f32_16x16x32_bf16 v[188:191], v[120:123], v[212:215], v[188:191]\n\t"
        "s_waitcnt lgkmcnt(4)\n\t"
        "v_mfma_f32_16x16x32_bf16 v[176:179], v[44:47], v[216:219], v[176:179]\n\t"
        "ds_read_b128 v[208:211], %[xa] offset:10240\n\t"
        "v_mfma_f32_16x16x32_bf16 v[184:187], v[44:47], v[220:223], v[184:187]\n\t"
        "ds_read_b128 v[212:215], %[xa] offset:30720\n\t"
        "v_mfma_f32_16x16x32_bf16 v[180:183], v[124:127], v[216:219], v[180:183]\n\t"
        "v_mfma_f32_16x16x32_bf16 v[188:191], v[124:127], v[220:223], v[188:191]\n\t"
        "s_waitcnt lgkmcnt(4)\n\t"
        "v_mfma_f32_16x16x32_bf16 v[176:179], v[48:51], v[192:195], v[176:179]\n\t"
        "ds_read_b128 v[216:219], %[xa] offset:11264\n\t"
        "v_mfma_f32_16x16x32_bf16 v[184:187], v[48:51], v[196:199], v[184:187]\n\t"
        "ds_read_b128 v[220:223], %[xa] offset:31744\n\t"
        "v_mfma_f32_16x16x32_bf16 v[180:183], v[128:131], v[192:195], v[180:183]\n\t"
        "v_mfma_f32_16x16x32_bf16 v[188:191], v[128:131], v[196:199], v[188:191]\n\t"
        "s_waitcnt lgkmcnt(4)\n\t"
        "v_mfma_f32_16x16x32_bf16 v[176:179], v[52:55], v[200:203], v[176:179]\n\t"
        "ds_read_b128 v[192:195], %[xa] offset:12288\n\t"
        "v_mfma_f32_16x16x32_bf16 v[184:187], v[52:55], v[204:207], v[184:187]\n\t"
        "ds_read_b128 v[196:199], %[xa] offset:32768\n\t"
        "v_mfma_f32_16x16x32_bf16 v[180:183], v[132:135], v[200:203], v[180:183]\n\t"
        "v_mfma_f32_16x16x32_bf16 v[188:191], v[132:135], v[204:207], v[188:191]\n\t"
        "s_waitcnt lgkmcnt(4)\n\t"
        "v_mfma_f32_16x16x32_bf16 v[176:179], v[56:59], v[208:211], v[176:179]\n\t"
        "ds_read_b128 v[200:203], %[xa] offset:13312\n\t"
        "v_mfma_f32_16x16x32_bf16 v[184:187], v[56:59], v[212:215], v[184:187]\n\t"
        "ds_read_b128 v[204:207], %[xa] offset:33792\n\t"
        "v_mfma_f32_16x16x32_bf16 v[180:183], v[136:139], v[208:211], v[180:183]\n\t"
        "v_mfma_f32_16x16x32_bf16 v[188:191], v[136:139], v[212:215], v[188:191]\n\t"
        "s_waitcnt lgkmcnt(4)\n\t"
        "v_mfma_f32_16x16x32_bf16 v[176:179], v[60:63], v[216:219], v[176:179]\n\t"
        "ds_read_b128 v[208:211], %[xa] offset:14336\n\t"
        "v_mfma_f32_16x16x32_bf16 v[184:187], v[60:63], v[220:223], v[184:187]\n\t"
        "ds_read_b128 v[212:215], %[xa] offset:34816\n\t"
        "v_mfma_f32_16x16x32_bf16 v[180:183], v[140:143], v[216:219], v[180:183]\n\t"
        "v_mfma_f32_16x16x32_bf16 v[188:191], v[140:143], v[220:223], v[188:191]\n\t"
        "s_waitcnt lgkmcnt(4)\n\t"
        "v_mfma_f32_16x16x32_bf16 v[176:179], v[64:67], v[192:195], v[176:179]\n\t"
        "ds_read_b128 v[216:219], %[xa] offset:15360\n\t"
        "v_mfma_f32_16x16x32_bf16 v[184:187], v[64:67], v[196:199], v[184:187]\n\t"
        "ds_read_b128 v[220:223], %[xa] offset:35840\n\t"
        "v_mfma_f32_16x16x32_bf16 v[180:183], v[144:147], v[192:195], v[180:183]\n\t"
        "v_mfma_f32_16x16x32_bf16 v[188:191], v[144:147], v[196:199], v[188:191]\n\t"
        "s_waitcnt lgkmcnt(4)\n\t"
        "v_mfma_f32_16x16x32_bf16 v[176:179], v[68:71], v[200:203], v[176:179]\n\t"
        "ds_read_b128 v[192:195], %[xa] offset:16384\n\t"
        "v_mfma_f32_16x16x32_bf16 v[184:187], v[68:71], v[204:207], v[184:187]\n\t"
        "ds_read_b128 v[196:199], %[xa] offset:36864\n\t"
        "v_mfma_f32_16x16x32_bf16 v[180:183], v[148:151], v[200:203], v[180:183]\n\t"
        "v_mfma_f32_16x16x32_bf16 v[188:191], v[148:151], v[204:207], v[188:191]\n\t"
        "s_waitcnt lgkmcnt(4)\n\t"
        "v_mfma_f32_16x16x32_bf16 v[176:179], v[72:75], v[208:211], v[176:179]\n\t"
        "ds_read_b128 v[200:203], %[xa] offset:17408\n\t"
        "v_mfma_f32_16x16x32_bf16 v[184:187], v[72:75], v[212:215], v[184:187]\n\t"
        "ds_read_b128 v[204:207], %[xa] offset:37888\n\t"
        "v_mfma_f32_16x16x32_bf16 v[180:183], v[152:155], v[208:211], v[180:183]\n\t"
        "v_mfma_f32_16x16x32_bf16 v[188:191], v[152:155], v[212:215], v[188:191]\n\t"
        "s_waitcnt lgkmcnt(4)\n\t"
        "v_mfma_f32_16x16x32_bf16 v[176:179], v[76:79], v[216:219], v[176:179]\n\t"
        "ds_read_b128 v[208:211], %[xa] offset:18432\n\t"
        "v_mfma_f32_16x16x32_bf16 v[184:187], v[76:79], v[220:223], v[184:187]\n\t"
        "ds_read_b128 v[212:215], %[xa] offset:38912\n\t"
        "v_mfma_f32_16x16x32_bf16 v[180:183], v[156:159], v[216:219], v[180:183]\n\t"
        "v_mfma_f32_16x16x32_bf16 v[188:191], v[156:159], v[220:223], v[188:191]\n\t"
        "s_waitcnt lgkmcnt(4)\n\t"
        "v_mfma_f32_16x16x32_bf16 v[176:179], v[80:83], v[192:195], v[176:179]\n\t"
        "ds_read_b128 v[216:219], %[xa] offset:19456\n\t"
        "v_mfma_f32_16x16x32_bf16 v[184:187], v[80:83], v[196:199], v[184:187]\n\t"
        "ds_read_b128 v[220:223], %[xa] offset:39936\n\t"
        "v_mfma_f32_16x16x32_bf16 v[180:183], v[160:163], v[192:195], v[180:183]\n\t"
        "v_mfma_f32_16x16x32_bf16 v[188:191], v[160:163], v[196:199], v[188:191]\n\t"
        "s_waitcnt lgkmcnt(4)\n\t"
        "v_mfma_f32_16x16x32_bf16 v[176:179], v[84:87], v[200:203], v[176:179]\n\t"
        "v_mfma_f32_16x16x32_bf16 v[184:187], v[84:87], v[204:207], v[184:187]\n\t"
        "v_mfma_f32_16x16x32_bf16 v[180:183], v[164:167], v[200:203], v[180:183]\n\t"
        "v_mfma_f32_16x16x32_bf16 v[188:191], v[164:167], v[204:207], v[188:191]\n\t"
        "s_waitcnt lgkmcnt(2)\n\t"
        "v_mfma_f32_16x16x32_bf16 v[176:179], v[88:91], v[208:211], v[176:179]\n\t"
        "v_mfma_f32_16x16x32_bf16 v[184:187], v[88:91], v[212:215], v[184:187]\n\t"
        "v_mfma_f32_16x16x32_bf16 v[180:183], v[168:171], v[208:211], v[180:183]\n\t"
        "v_mfma_f32_16x16x32_bf16 v[188:191], v[168:171], v[212:215], v[188:191]\n\t"
        "s_waitcnt lgkmcnt(0)\n\t"
        "v_mfma_f32_16x16x32_bf16 v[176:179], v[92:95], v[216:219], v[176:179]\n\t"
        "v_mfma_f32_16x16x32_bf16 v[184:187], v[92:95], v[220:223], v[184:187]\n\t"
        "v_mfma_f32_16x16x32_bf16 v[180:183], v[172:175], v[216:219], v[180:183]\n\t"
        "v_mfma_f32_16x16x32_bf16 v[188:191], v[172:175], v[220:223], v[188:191]\n\t"
        "s_nop 4\n\t"
        "v_cvt_pk_bf16_f32 v224, v176, v177\n\t"
        "v_cvt_pk_bf16_f32 v225, v178, v179\n\t"
        "v_cvt_pk_bf16_f32 v226, v180, v181\n\t"
        "v_cvt_pk_bf16_f32 v227, v182, v183\n\t"
        "ds_write_b128 %[oa], v[224:227] offset:0\n\t"
        "v_cvt_pk_bf16_f32 v228, v184, v185\n\t"
        "v_cvt_pk_bf16_f32 v229, v186, v187\n\t"
        "v_cvt_pk_bf16_f32 v230, v188, v189\n\t"
        "v_cvt_pk_bf16_f32 v231, v190, v191\n\t"
        "ds_write_b128 %[oa], v[228:231] offset:4096\n\t"
        :
        : [w0] "{v[16:47]}"(W[0]), [w1] "{v[48:79]}"(W[1]), [w2] "{v[80:111]}"(W[2]), [w3] "{v[112:143]}"(W[3]), [w4] "{v[144:175]}"(W[4]), [xa] "v"(xa), [oa] "v"(oa)
        : "v176", "v177", "v178", "v179", "v180", "v181", "v182", "v183", "v184", "v185", "v186", "v187", "v188", "v189", "v190", "v191", "v192", "v193", "v194", "v195", "v196", "v197", "v198", "v199", "v200", "v201", "v202", "v203", "v204", "v205", "v206", "v207", "v208", "v209", "v210", "v211", "v212", "v213", "v214", "v215", "v216", "v217", "v218", "v219", "v220", "v221", "v222", "v223", "v224", "v225", "v226", "v227", "v228", "v229", "v230", "v231", "vcc", "scc", "memory");
}

__device__ __forceinline__ void wk(const u32x32 (&W)[5], u32 xa, u32 oa, u32x4 rs, u32 o0, u32 o1, float sc, u32 xv, u32 xs, u32 xd, u32 ca, u32 fc, u32 fw, u32 fo STP) {
    asm volatile(
        "ds_read_b128 v[192:195], %[xa] offset:0\n\t"
        "ds_read_b128 v[196:199], %[xa] offset:20480\n\t"
        "ds_read_b128 v[200:203], %[xa] offset:1024\n\t"
        "ds_read_b128 v[204:207], %[xa] offset:21504\n\t"
        "ds_read_b128 v[208:211], %[xa] offset:2048\n\t"
        "ds_read_b128 v[212:215], %[xa] offset:22528\n\t"
        "v_mov_b32 v232, %[ca]\n\t"
        "v_mov_b32 v233, 1\n\t"
        "v_mov_b32 v234, %[fw]\n\t"
        "s_mov_b32 s88, m0\n\t"
        "s_mov_b32 m0, %[xd]\n\t"
        "s_add_u32 s89, %[xs], 0x1000\n\t"
        "buffer_load_dwordx4 %[xv], %[rs], %[xs] offen offset:0 sc1 lds\n\t"
        "buffer_load_dwordx4 %[xv], %[rs], %[xs] offen offset:1024 sc1 lds\n\t"
        "s_mov_b32 m0, s88\n\t"
        "s_waitcnt lgkmcnt(4)\n\t"
        "v_mfma_f32_16x16x32_bf16 v[176:179], v[16:19], v[192:195], 0\n\t"
        "ds_read_b128 v[216:219], %[xa] offset:3072\n\t"
        "v_mfma_f32_16x16x32_bf16 v[184:187], v[16:19], v[196:199], 0\n\t"
        "ds_read_b128 v[220:223], %[xa] offset:23552\n\t"
        "v_mfma_f32_16x16x32_bf16 v[180:183], v[96:99], v[192:195], 0\n\t"
        "v_mfma_f32_16x16x32_bf16 v[188:191], v[96:99], v[196:199], 0\n\t"
        "s_waitcnt lgkmcnt(4)\n\t"
        "v_mfma_f32_16x16x32_bf16 v[176:179], v[20:23], v[200:203], v[176:179]\n\t"
        "ds_read_b128 v[192:195], %[xa] offset:4096\n\t"
        "v_mfma_f32_16x16x32_bf16 v[184:187], v[20:23], v[204:207], v[184:187]\n\t"
        "ds_read_b128 v[196:199], %[xa] offset:24576\n\t"
        "v_mfma_f32_16x16x32_bf16 v[180:183], v[100:103], v[200:203], v[180:183]\n\t"
        "v_mfma_f32_16x16x32_bf16 v[188:191], v[100:103], v[204:207], v[188:191]\n\t"
        "s_waitcnt lgkmcnt(4)\n\t"
        "v_mfma_f32_16x16x32_bf16 v[176:179], v[24:27], v[208:211], v[176:179]\n\t"
        "ds_read_b128 v[200:203], %[xa] offset:5120\n\t"
        "v_mfma_f32_16x16x32_bf16 v[184:187], v[24:27], v[212:215], v[184:187]\n\t"
        "ds_read_b128 v[204:207], %[xa] offset:25600\n\t"
        "v_mfma_f32_16x16x32_bf16 v[180:183], v[104:107], v[208:211], v[180:183]\n\t"
        "v_mfma_f32_16x16x32_bf16 v[188:191], v[104:107], v[212:215], v[188:191]\n\t"
        "s_waitcnt lgkmcnt(4)\n\t"
        "v_mfma_f32_16x16x32_bf16 v[176:179], v[28:31], v[216:219], v[176:179]\n\t"
        "ds_read_b128 v[208:211], %[xa] offset:6144\n\t"
        "v_mfma_f32_16x16x32_bf16 v[184:187], v[28:31], v[220:223], v[184:187]\n\t"
        "ds_read_b128 v[212:215], %[xa] offset:26624\n\t"
        "v_mfma_f32_16x16x32_bf16 v[180:183], v[108:111], v[216:219], v[180:183]\n\t"
        "v_mfma_f32_16x16x32_bf16 v[188:191], v[108:111], v[220:223], v[188:191]\n\t"
        "s_waitcnt lgkmcnt(4)\n\t"
        "v_mfma_f32_16x16x32_bf16 v[176:179], v[32:35], v[192:195], v[176:179]\n\t"
        "ds_read_b128 v[216:219], %[xa] offset:7168\n\t"
        "v_mfma_f32_16x16x32_bf16 v[184:187], v[32:35], v[196:199], v[184:187]\n\t"
        "ds_read_b128 v[220:223], %[xa] offset:27648\n\t"
        "v_mfma_f32_16x16x32_bf16 v[180:183], v[112:115], v[192:195], v[180:183]\n\t"
        "v_mfma_f32_16x16x32_bf16 v[188:191], v[112:115], v[196:199], v[188:191]\n\t"
        "s_waitcnt lgkmcnt(4)\n\t"
        "v_mfma_f32_16x16x32_bf16 v[176:179], v[36:39], v[200:203], v[176:179]\n\t"
        "ds_read_b128 v[192:195], %[xa] offset:8192\n\t"
        "v_mfma_f32_16x16x32_bf16 v[184:187], v[36:39], v[204:207], v[184:187]\n\t"
        "ds_read_b128 v[196:199], %[xa] offset:28672\n\t"
        "v_mfma_f32_16x16x32_bf16 v[180:183], v[116:119], v[200:203], v[180:183]\n\t"
        "v_mfma_f32_16x16x32_bf16 v[188:191], v[116:119], v[204:207], v[188:191]\n\t"
        "s_waitcnt lgkmcnt(4)\n\t"
        "v_mfma_f32_16x16x32_bf16 v[176:179], v[40:43], v[208:211], v[176:179]\n\t"
        "ds_read_b128 v[200:203], %[xa] offset:9216\n\t"
        "v_mfma_f32_16x16x32_bf16 v[184:187], v[40:43], v[212:215], v[184:187]\n\t"
        "ds_read_b128 v[204:207], %[xa] offset:29696\n\t"
        "v_mfma_f32_16x16x32_bf16 v[180:183], v[120:123], v[208:211], v[180:183]\n\t"
        "s_waitcnt vmcnt(0)\n\t"
        "s_mov_b64 exec, 1\n\t"
        "ds_add_rtn_u32 v233, v232, v233\n\t"
        "s_mov_b64 exec, -1\n\t"
        "v_mfma_f32_16x16x32_bf16 v[188:191], v[120:123], v[212:215], v[188:191]\n\t"
        "s_waitcnt lgkmcnt(5)\n\t"
        "v_mfma_f32_16x16x32_bf16 v[176:179], v[44:47], v[216:219], v[176:179]\n\t"
        "ds_read_b128 v[208:211], %[xa] offset:10240\n\t"
        "v_mfma_f32_16x16x32_bf16 v[184:187], v[44:47], v[220:223], v[184:187]\n\t"
        "ds_read_b128 v[212:215], %[xa] offset:30720\n\t"
        "v_mfma_f32_16x16x32_bf16 v[180:183], v[124:127], v[216:219], v[180:183]\n\t"
        "v_mfma_f32_16x16x32_bf16 v[188:191], v[124:127], v[220:223], v[188:191]\n\t"
        "s_waitcnt lgkmcnt(5)\n\t"
        "v_mfma_f32_16x16x32_bf16 v[176:179], v[48:51], v[192:195], v[176:179]\n\t"
        "ds_read_b128 v[216:219], %[xa] offset:11264\n\t"
        "v_mfma_f32_16x16x32_bf16 v[184:187], v[48:51], v[196:199], v[184:187]\n\t"
        "ds_read_b128 v[220:223], %[xa] offset:31744\n\t"
        "v_mfma_f32_16x16x32_bf16 v[180:183], v[128:131], v[192:195], v[180:183]\n\t"
        "s_waitcnt lgkmcnt(4)\n\t"
        "s_mov_b64 exec, 1\n\t"
        "v_cmpx_eq_u32 vcc, %[fc], v233\n\t"
        "buffer_store_dword v234, off, %[rs], %[fo] sc1\n\t"
        "s_mov_b64 exec, -1\n\t"
        "v_mfma_f32_16x16x32_bf16 v[188:191], v[128:131], v[196:199], v[188:191]\n\t"
        "s_waitcnt lgkmcnt(5)\n\t"
        "v_mfma_f32_16x16x32_bf16 v[176:179], v[52:55], v[200:203], v[176:179]\n\t"
        "ds_read_b128 v[192:195], %[xa] offset:12288\n\t"
        "v_mfma_f32_16x16x32_bf16 v[184:187], v[52:55], v[204:207], v[184:187]\n\t"
        "ds_read_b128 v[196:199], %[xa] offset:32768\n\t"
        "v_mfma_f32_16x16x32_bf16 v[180:183], v[132:135], v[200:203], v[180:183]\n\t"
        "v_mfma_f32_16x16x32_bf16 v[188:191], v[132:135], v[204:207], v[188:191]\n\t"
        "s_waitcnt lgkmcnt(4)\n\t"
        "v_mfma_f32_16x16x32_bf16 v[176:179], v[56:59], v[208:211], v[176:179]\n\t"
        "ds_read_b128 v[200:203], %[xa] offset:13312\n\t"
        "v_mfma_f32_16x16x32_bf16 v[184:187], v[56:59], v[212:215], v[184:187]\n\t"
        "ds_read_b128 v[204:207], %[xa] offset:33792\n\t"
        "v_mfma_f32_16x16x32_bf16 v[180:183], v[136:139], v[208:211], v[180:183]\n\t"
        "v_mfma_f32_16x16x32_bf16 v[188:191], v[136:139], v[212:215], v[188:191]\n\t"
        "s_waitcnt lgkmcnt(4)\n\t"
        "v_mfma_f32_16x16x32_bf16 v[176:179], v[60:63], v[216:219], v[176:179]\n\t"
        "ds_read_b128 v[208:211], %[xa] offset:14336\n\t"
        "v_mfma_f32_16x16x32_bf16 v[184:187], v[60:63], v[220:223], v[184:187]\n\t"
        "ds_read_b128 v[212:215], %[xa] offset:34816\n\t"
        "v_mfma_f32_16x16x32_bf16 v[180:183], v[140:143], v[216:219], v[180:183]\n\t"
        "v_mfma_f32_16x16x32_bf16 v[188:191], v[140:143], v[220:223], v[188:191]\n\t"
        "s_waitcnt lgkmcnt(4)\n\t"
        "v_mfma_f32_16x16x32_bf16 v[176:179], v[64:67], v[192:195], v[176:179]\n\t"
        "ds_read_b128 v[216:219], %[xa] offset:15360\n\t"
        "v_mfma_f32_16x16x32_bf16 v[184:187], v[64:67], v[196:199], v[184:187]\n\t"
        "ds_read_b128 v[220:223], %[xa] offset:35840\n\t"
        "v_mfma_f32_16x16x32_bf16 v[180:183], v[144:147], v[192:195], v[180:183]\n\t"
        "v_mfma_f32_16x16x32_bf16 v[188:191], v[144:147], v[196:199], v[188:191]\n\t"
        "s_waitcnt lgkmcnt(4)\n\t"
        "v_mfma_f32_16x16x32_bf16 v[176:179], v[68:71], v[200:203], v[176:179]\n\t"
        "ds_read_b128 v[192:195], %[xa] offset:16384\n\t"
        "v_mfma_f32_16x16x32_bf16 v[184:187], v[68:71], v[204:207], v[184:187]\n\t"
        "ds_read_b128 v[196:199], %[xa] offset:36864\n\t"
        "v_mfma_f32_16x16x32_bf16 v[180:183], v[148:151], v[200:203], v[180:183]\n\t"
        "v_mfma_f32_16x16x32_bf16 v[188:191], v[148:151], v[204:207], v[188:191]\n\t"
        "s_waitcnt lgkmcnt(4)\n\t"
        "v_mfma_f32_16x16x32_bf16 v[176:179], v[72:75], v[208:211], v[176:179]\n\t"
        "ds_read_b128 v[200:203], %[xa] offset:17408\n\t"
        "v_mfma_f32_16x16x32_bf16 v[184:187], v[72:75], v[212:215], v[184:187]\n\t"
        "ds_read_b128 v[204:207], %[xa] offset:37888\n\t"
        "v_mfma_f32_16x16x32_bf16 v[180:183], v[152:155], v[208:211], v[180:183]\n\t"
        "v_mfma_f32_16x16x32_bf16 v[188:191], v[152:155], v[212:215], v[188:191]\n\t"
        "s_waitcnt lgkmcnt(4)\n\t"
        "v_mfma_f32_16x16x32_bf16 v[176:179], v[76:79], v[216:219], v[176:179]\n\t"
        "ds_read_b128 v[208:211], %[xa] offset:18432\n\t"
        "v_mfma_f32_16x16x32_bf16 v[184:187], v[76:79], v[220:223], v[184:187]\n\t"
        "ds_read_b128 v[212:215], %[xa] offset:38912\n\t"
        "v_mfma_f32_16x16x32_bf16 v[180:183], v[156:159], v[216:219], v[180:183]\n\t"
        "v_mfma_f32_16x16x32_bf16 v[188:191], v[156:159], v[220:223], v[188:191]\n\t"
        "s_waitcnt lgkmcnt(4)\n\t"
        "v_mfma_f32_16x16x32_bf16 v[176:179], v[80:83], v[192:195], v[176:179]\n\t"
        "ds_read_b128 v[216:219], %[xa] offset:19456\n\t"
        "v_mfma_f32_16x16x32_bf16 v[184:187], v[80:83], v[196:199], v[184:187]\n\t"
        "ds_read_b128 v[220:223], %[xa] offset:39936\n\t"
        "v_mfma_f32_16x16x32_bf16 v[180:183], v[160:163], v[192:195], v[180:183]\n\t"
        "v_mfma_f32_16x16x32_bf16 v[188:191], v[160:163], v[196:199], v[188:191]\n\t"
        "s_waitcnt lgkmcnt(4)\n\t"
        "v_mfma_f32_16x16x32_bf16 v[176:179], v[84:87], v[200:203], v[176:179]\n\t"
        "v_mfma_f32_16x16x32_bf16 v[184:187], v[84:87], v[204:207], v[184:187]\n\t"
        "v_mfma_f32_16x16x32_bf16 v[180:183], v[164:167], v[200:203], v[180:183]\n\t"
        "v_mfma_f32_16x16x32_bf16 v[188:191], v[164:167], v[204:207], v[188:191]\n\t"
        "s_waitcnt lgkmcnt(2)\n\t"
        "v_mfma_f32_16x16x32_bf16 v[176:179], v[88:91], v[208:211], v[176:179]\n\t"
        "v_mfma_f32_16x16x32_bf16 v[184:187], v[88:91], v[212:215], v[184:187]\n\t"
        "v_mfma_f32_16x16x32_bf16 v[180:183], v[168:171], v[208:211], v[180:183]\n\t"
        "v_mfma_f32_16x16x32_bf16 v[188:191], v[168:171], v[212:215], v[188:191]\n\t"
        "s_waitcnt lgkmcnt(0)\n\t"
        "v_mfma_f32_16x16x32_bf16 v[176:179], v[92:95], v[216:219], v[176:179]\n\t"
        "v_mfma_f32_16x16x32_bf16 v[184:187], v[92:95], v[220:223], v[184:187]\n\t"
        "v_mfma_f32_16x16x32_bf16 v[180:183], v[172:175], v[216:219], v[180:183]\n\t"
        "v_mfma_f32_16x16x32_bf16 v[188:191], v[172:175], v[220:223], v[188:191]\n\t"
        "s_nop 0\n\t"
        "s_barrier\n\t"
        "s_nop 4\n\t"
        "v_mul_f32 v176, %[sc], v176\n\t"
        "v_mul_f32 v177, %[sc], v177\n\t"
        "v_mul_f32 v178, %[sc], v178\n\t"
        "v_mul_f32 v179, %[sc], v179\n\t"
        "v_mul_f32 v180, %[sc], v180\n\t"
        "v_mul_f32 v181, %[sc], v181\n\t"
        "v_mul_f32 v182, %[sc], v182\n\t"
        "v_mul_f32 v183, %[sc], v183\n\t"
        "v_cvt_pk_bf16_f32 v224, v176, v177\n\t"
        "v_cvt_pk_bf16_f32 v225, v178, v179\n\t"
        "v_cvt_pk_bf16_f32 v226, v180, v181\n\t"
        "v_cvt_pk_bf16_f32 v227, v182, v183\n\t"
        "buffer_store_dwordx4 v[224:227], %[oa], %[rs], %[o0] offen sc1\n\t"
        "v_mul_f32 v184, %[sc], v184\n\t"
        "v_mul_f32 v185, %[sc], v185\n\t"
        "v_mul_f32 v186, %[sc], v186\n\t"
        "v_mul_f32 v187, %[sc], v187\n\t"
        "v_mul_f32 v188, %[sc], v188\n\t"
        "v_mul_f32 v189, %[sc], v189\n\t"
        "v_mul_f32 v190, %[sc], v190\n\t"
        "v_mul_f32 v191, %[sc], v191\n\t"
        "v_cvt_pk_bf16_f32 v228, v184, v185\n\t"
        "v_cvt_pk_bf16_f32 v229, v186, v187\n\t"
        "v_cvt_pk_bf16_f32 v230, v188, v189\n\t"
        "v_cvt_pk_bf16_f32 v231, v190, v191\n\t"
        "buffer_store_dwordx4 v[228:231], %[oa], %[rs], %[o1] offen sc1\n\t"
        "s_nop 1\n\t"
        :
        : [w0] "{v[16:47]}"(W[0]), [w1] "{v[48:79]}"(W[1]), [w2] "{v[80:111]}"(W[2]), [w3] "{v[112:143]}"(W[3]), [w4] "{v[144:175]}"(W[4]), [xa] "v"(xa), [oa] "v"(oa), [rs] "s"(rs), [o0] "s"(o0), [o1] "s"(o1), [sc] "s"(sc), [ca] "s"(ca), [fc] "s"(fc), [fw] "s"(fw), [fo] "s"(fo), [xv] "v"(xv), [xs] "s"(xs), [xd] "s"(xd)
        : "s88", "s89", "v176", "v177", "v178", "v179", "v180", "v181", "v182", "v183", "v184", "v185", "v186", "v187", "v188", "v189", "v190", "v191", "v192", "v193", "v194", "v195", "v196", "v197", "v198", "v199", "v200", "v201", "v202", "v203", "v204", "v205", "v206", "v207", "v208", "v209", "v210", "v211", "v212", "v213", "v214", "v215", "v216", "v217", "v218", "v219", "v220", "v221", "v222", "v223", "v224", "v225", "v226", "v227", "v228", "v229", "v230", "v231", "v232", "v233", "v234", "vcc", "scc", "memory");
}

__device__ __forceinline__ void wb(const u32x32 (&W)[5], u32 xa, u32 oa, u32x4 rs, u32 bl, u32 wp, u32 it, u32 xv, u32 xs, u32 xd, u32 ys, u32 yd, u32 ca, u32 fc, u32 fw, u32 fo STP) {
    asm volatile(
        "ds_read_b128 v[192:195], %[xa] offset:0\n\t"
        "ds_read_b128 v[196:199], %[xa] offset:1024\n\t"
        "ds_read_b128 v[200:203], %[xa] offset:2048\n\t"
        "ds_read_b128 v[204:207], %[xa] offset:3072\n\t"
        "ds_read_b128 v[208:211], %[xa] offset:4096\n\t"
        "ds_read_b128 v[212:215], %[xa] offset:5120\n\t"
        "ds_read_b128 v[216:219], %[xa] offset:6144\n\t"
        "ds_read_b128 v[220:223], %[xa] offset:7168\n\t"
        "v_mov_b32 v232, %[ca]\n\t"
        "v_mov_b32 v233, 1\n\t"
        "v_mov_b32 v234, %[fw]\n\t"
        "s_andn2_b32 s91, %[wp], 15\n\t"
        "s_mov_b32 s92, %[bl]\n\t"
        "s_mov_b32 s88, m0\n\t"
        "s_mov_b32 m0, %[xd]\n\t"
        "s_add_u32 s89, %[xs], 0x1000\n\t"
        "buffer_load_dwordx4 %[xv], %[rs], %[xs] offen offset:0 sc1 lds\n\t"
        "buffer_load_dwordx4 %[xv], %[rs], %[xs] offen offset:1024 sc1 lds\n\t"
        "s_mov_b32 m0, s88\n\t"
        "s_setprio 1\n\t"
        "s_waitcnt lgkmcnt(0)\n\t"
        "v_mfma_f32_16x16x32_bf16 v[176:179], v[192:195], v[16:19], 0\n\t"
        "s_lshr_b32 s95, s92, 20\n\t"
        "s_xnor_b32 s95, s95, %[wp]\n\t"
        "v_mfma_f32_16x16x32_bf16 v[180:183], v[208:211], v[16:19], 0\n\t"
        "s_and_b32 s95, s95, 1\n\t"
        "s_lshl_b32 s95, s95, 13\n\t"
        "v_mfma_f32_16x16x32_bf16 v[176:179], v[196:199], v[20:23], v[176:179]\n\t"
        "v_mfma_f32_16x16x32_bf16 v[180:183], v[212:215], v[20:23], v[180:183]\n\t"
        "v_mfma_f32_16x16x32_bf16 v[176:179], v[200:203], v[24:27], v[176:179]\n\t"
        "v_mfma_f32_16x16x32_bf16 v[180:183], v[216:219], v[24:27], v[180:183]\n\t"
        "v_mfma_f32_16x16x32_bf16 v[176:179], v[204:207], v[28:31], v[176:179]\n\t"
        "v_mfma_f32_16x16x32_bf16 v[180:183], v[220:223], v[28:31], v[180:183]\n\t"
        "s_lshl1_add_u32 s93, s92, s91\n\t"
        "s_add_u32 s93, s93, s95\n\t"
        "v_mfma_f32_16x16x32_bf16 v[184:187], v[192:195], v[96:99], 0\n\t"
        "s_add_u32 s92, s92, 0x40000\n\t"
        "v_mfma_f32_16x16x32_bf16 v[188:191], v[208:211], v[96:99], 0\n\t"
        "s_add_u32 s94, s93, 0x10000\n\t"
        "v_mfma_f32_16x16x32_bf16 v[184:187], v[196:199], v[100:103], v[184:187]\n\t"
        "v_cvt_pk_bf16_f32 v224, v176, v177\n\t"
        "v_mfma_f32_16x16x32_bf16 v[188:191], v[212:215], v[100:103], v[188:191]\n\t"
        "v_cvt_pk_bf16_f32 v225, v178, v179\n\t"
        "v_mfma_f32_16x16x32_bf16 v[184:187], v[200:203], v[104:107], v[184:187]\n\t"
        "v_cvt_pk_bf16_f32 v226, v180, v181\n\t"
        "v_mfma_f32_16x16x32_bf16 v[188:191], v[216:219], v[104:107], v[188:191]\n\t"
        "v_cvt_pk_bf16_f32 v227, v182, v183\n\t"
        "v_mfma_f32_16x16x32_bf16 v[184:187], v[204:207], v[108:111], v[184:187]\n\t"
        "buffer_store_dwordx4 v[224:227], %[oa], %[rs], s93 offen sc1\n\t"
        "v_mfma_f32_16x16x32_bf16 v[188:191], v[220:223], v[108:111], v[188:191]\n\t"
        "v_mfma_f32_16x16x32_bf16 v[176:179], v[192:195], v[32:35], 0\n\t"
        "s_lshr_b32 s95, s92, 20\n\t"
        "s_xnor_b32 s95, s95, %[wp]\n\t"
        "v_mfma_f32_16x16x32_bf16 v[180:183], v[208:211], v[32:35], 0\n\t"
        "s_and_b32 s95, s95, 1\n\t"
        "s_lshl_b32 s95, s95, 13\n\t"
        "v_mfma_f32_16x16x32_bf16 v[176:179], v[196:199], v[36:39], v[176:179]\n\t"
        "v_cvt_pk_bf16_f32 v228, v184, v185\n\t"
        "v_mfma_f32_16x16x32_bf16 v[180:183], v[212:215], v[36:39], v[180:183]\n\t"
        "v_cvt_pk_bf16_f32 v229, v186, v187\n\t"
        "v_mfma_f32_16x16x32_bf16 v[176:179], v[200:203], v[40:43], v[176:179]\n\t"
        "v_cvt_pk_bf16_f32 v230, v188, v189\n\t"
        "v_mfma_f32_16x16x32_bf16 v[180:183], v[216:219], v[40:43], v[180:183]\n\t"
        "v_cvt_pk_bf16_f32 v231, v190, v191\n\t"
        "v_mfma_f32_16x16x32_bf16 v[176:179], v[204:207], v[44:47], v[176:179]\n\t"
        "buffer_store_dwordx4 v[228:231], %[oa], %[rs], s94 offen sc1\n\t"
        "v_mfma_f32_16x16x32_bf16 v[180:183], v[220:223], v[44:47], v[180:183]\n\t"
        "s_lshl1_add_u32 s93, s92, s91\n\t"
        "s_add_u32 s93, s93, s95\n\t"
        "v_mfma_f32_16x16x32_bf16 v[184:187], v[192:195], v[112:115], 0\n\t"
        "s_add_u32 s92, s92, 0x40000\n\t"
        "v_mfma_f32_16x16x32_bf16 v[188:191], v[208:211], v[112:115], 0\n\t"
        "s_add_u32 s94, s93, 0x10000\n\t"
        "v_mfma_f32_16x16x32_bf16 v[184:187], v[196:199], v[116:119], v[184:187]\n\t"
        "v_cvt_pk_bf16_f32 v224, v176, v177\n\t"
        "v_mfma_f32_16x16x32_bf16 v[188:191], v[212:215], v[116:119], v[188:191]\n\t"
        "v_cvt_pk_bf16_f32 v225, v178, v179\n\t"
        "v_mfma_f32_16x16x32_bf16 v[184:187], v[200:203], v[120:123], v[184:187]\n\t"
        "v_cvt_pk_bf16_f32 v226, v180, v181\n\t"
        "v_mfma_f32_16x16x32_bf16 v[188:191], v[216:219], v[120:123], v[188:191]\n\t"
        "v_cvt_pk_bf16_f32 v227, v182, v183\n\t"
        "v_mfma_f32_16x16x32_bf16 v[184:187], v[204:207], v[124:127], v[184:187]\n\t"
        "buffer_store_dwordx4 v[224:227], %[oa], %[rs], s93 offen sc1\n\t"
        "v_mfma_f32_16x16x32_bf16 v[188:191], v[220:223], v[124:127], v[188:191]\n\t"
        "s_waitcnt vmcnt(5)\n\t"
        "s_mov_b64 exec, 1\n\t"
        "ds_add_rtn_u32 v233, v232, v233\n\t"
        "s_mov_b64 exec, -1\n\t"
        "v_mfma_f32_16x16x32_bf16 v[176:179], v[192:195], v[48:51], 0\n\t"
        "s_lshr_b32 s95, s92, 20\n\t"
        "s_xnor_b32 s95, s95, %[wp]\n\t"
        "v_mfma_f32_16x16x32_bf16 v[180:183], v[208:211], v[48:51], 0\n\t"
        "s_and_b32 s95, s95, 1\n\t"
        "s_lshl_b32 s95, s95, 13\n\t"
        "v_mfma_f32_16x16x32_bf16 v[176:179], v[196:199], v[52:55], v[176:179]\n\t"
        "v_cvt_pk_bf16_f32 v228, v184, v185\n\t"
        "v_mfma_f32_16x16x32_bf16 v[180:183], v[212:215], v[52:55], v[180:183]\n\t"
        "v_cvt_pk_bf16_f32 v229, v186, v187\n\t"
        "v_mfma_f32_16x16x32_bf16 v[176:179], v[200:203], v[56:59], v[176:179]\n\t"
        "v_cvt_pk_bf16_f32 v230, v188, v189\n\t"
        "v_mfma_f32_16x16x32_bf16 v[180:183], v[216:219], v[56:59], v[180:183]\n\t"
        "v_cvt_pk_bf16_f32 v231, v190, v191\n\t"
        "v_mfma_f32_16x16x32_bf16 v[176:179], v[204:207], v[60:63], v[176:179]\n\t"
        "buffer_store_dwordx4 v[228:231], %[oa], %[rs], s94 offen sc1\n\t"
        "v_mfma_f32_16x16x32_bf16 v[180:183], v[220:223], v[60:63], v[180:183]\n\t"
        "s_lshl1_add_u32 s93, s92, s91\n\t"
        "s_add_u32 s93, s93, s95\n\t"
        "v_mfma_f32_16x16x32_bf16 v[184:187], v[192:195], v[128:131], 0\n\t"
        "s_add_u32 s92, s92, 0x40000\n\t"
        "v_mfma_f32_16x16x32_bf16 v[188:191], v[208:211], v[128:131], 0\n\t"
        "s_add_u32 s94, s93, 0x10000\n\t"
        "v_mfma_f32_16x16x32_bf16 v[184:187], v[196:199], v[132:135], v[184:187]\n\t"
        "v_cvt_pk_bf16_f32 v224, v176, v177\n\t"
        "v_mfma_f32_16x16x32_bf16 v[188:191], v[212:215], v[132:135], v[188:191]\n\t"
        "v_cvt_pk_bf16_f32 v225, v178, v179\n\t"
        "v_mfma_f32_16x16x32_bf16 v[184:187], v[200:203], v[136:139], v[184:187]\n\t"
        "v_cvt_pk_bf16_f32 v226, v180, v181\n\t"
        "v_mfma_f32_16x16x32_bf16 v[188:191], v[216:219], v[136:139], v[188:191]\n\t"
        "v_cvt_pk_bf16_f32 v227, v182, v183\n\t"
        "v_mfma_f32_16x16x32_bf16 v[184:187], v[204:207], v[140:143], v[184:187]\n\t"
        "buffer_store_dwordx4 v[224:227], %[oa], %[rs], s93 offen sc1\n\t"
        "v_mfma_f32_16x16x32_bf16 v[188:191], v[220:223], v[140:143], v[188:191]\n\t"
        "s_waitcnt lgkmcnt(0)\n\t"
        "s_mov_b64 exec, 1\n\t"
        "v_cmpx_eq_u32 vcc, %[fc], v233\n\t"
        "buffer_store_dword v234, off, %[rs], %[fo] sc1\n\t"
        "s_mov_b64 exec, -1\n\t"
        "s_cmp_le_u32 %[it], %[nowv]\n\t"
        "s_cbranch_scc1 Lp%=\n\t"
        "s_setprio 0\n\t"
        "Lp%=:\n\t"
        "s_mov_b32 s88, m0\n\t"
        "s_mov_b32 m0, %[yd]\n\t"
        "s_add_u32 s89, %[ys], 0x1000\n\t"
        "buffer_load_dwordx4 %[xv], %[rs], %[ys] offen offset:0 sc1 lds\n\t"
        "buffer_load_dwordx4 %[xv], %[rs], %[ys] offen offset:1024 sc1 lds\n\t"
        "buffer_load_dwordx4 %[xv], %[rs], %[ys] offen offset:2048 sc1 lds\n\t"
        "buffer_load_dwordx4 %[xv], %[rs], %[ys] offen offset:3072 sc1 lds\n\t"
        "s_add_u32 m0, m0, 0x1000\n\t"
        "s_nop 0\n\t"
        "buffer_load_dwordx4 %[xv], %[rs], s89 offen offset:0 sc1 lds\n\t"
        "buffer_load_dwordx4 %[xv], %[rs], s89 offen offset:1024 sc1 lds\n\t"
        "buffer_load_dwordx4 %[xv], %[rs], s89 offen offset:2048 sc1 lds\n\t"
        "buffer_load_dwordx4 %[xv], %[rs], s89 offen offset:3072 sc1 lds\n\t"
        "s_mov_b32 m0, s88\n\t"
        "v_mfma_f32_16x16x32_bf16 v[176:179], v[192:195], v[64:67], 0\n\t"
        "s_lshr_b32 s95, s92, 20\n\t"
        "s_xnor_b32 s95, s95, %[wp]\n\t"
        "v_mfma_f32_16x16x32_bf16 v[180:183], v[208:211], v[64:67], 0\n\t"
        "s_and_b32 s95, s95, 1\n\t"
        "s_lshl_b32 s95, s95, 13\n\t"
        "v_mfma_f32_16x16x32_bf16 v[176:179], v[196:199], v[68:71], v[176:179]\n\t"
        "v_cvt_pk_bf16_f32 v228, v184, v185\n\t"
        "v_mfma_f32_16x16x32_bf16 v[180:183], v[212:215], v[68:71], v[180:183]\n\t"
        "v_cvt_pk_bf16_f32 v229, v186, v187\n\t"
        "v_mfma_f32_16x16x32_bf16 v[176:179], v[200:203], v[72:75], v[176:179]\n\t"
        "v_cvt_pk_bf16_f32 v230, v188, v189\n\t"
        "v_mfma_f32_16x16x32_bf16 v[180:183], v[216:219], v[72:75], v[180:183]\n\t"
        "v_cvt_pk_bf16_f32 v231, v190, v191\n\t"
        "v_mfma_f32_16x16x32_bf16 v[176:179], v[204:207], v[76:79], v[176:179]\n\t"
        "buffer_store_dwordx4 v[228:231], %[oa], %[rs], s94 offen sc1\n\t"
        "v_mfma_f32_16x16x32_bf16 v[180:183], v[220:223], v[76:79], v[180:183]\n\t"
        "s_lshl1_add_u32 s93, s92, s91\n\t"
        "s_add_u32 s93, s93, s95\n\t"
        "v_mfma_f32_16x16x32_bf16 v[184:187], v[192:195], v[144:147], 0\n\t"
        "s_add_u32 s92, s92, 0x40000\n\t"
        "v_mfma_f32_16x16x32_bf16 v[188:191], v[208:211], v[144:147], 0\n\t"
        "s_add_u32 s94, s93, 0x10000\n\t"
        "v_mfma_f32_16x16x32_bf16 v[184:187], v[196:199], v[148:151], v[184:187]\n\t"
        "v_cvt_pk_bf16_f32 v224, v176, v177\n\t"
        "v_mfma_f32_16x16x32_bf16 v[188:191], v[212:215], v[148:151], v[188:191]\n\t"
        "v_cvt_pk_bf16_f32 v225, v178, v179\n\t"
        "v_mfma_f32_16x16x32_bf16 v[184:187], v[200:203], v[152:155], v[184:187]\n\t"
        "v_cvt_pk_bf16_f32 v226, v180, v181\n\t"
        "v_mfma_f32_16x16x32_bf16 v[188:191], v[216:219], v[152:155], v[188:191]\n\t"
        "v_cvt_pk_bf16_f32 v227, v182, v183\n\t"
        "v_mfma_f32_16x16x32_bf16 v[184:187], v[204:207], v[156:159], v[184:187]\n\t"
        "buffer_store_dwordx4 v[224:227], %[oa], %[rs], s93 offen sc1\n\t"
        "v_mfma_f32_16x16x32_bf16 v[188:191], v[220:223], v[156:159], v[188:191]\n\t"
        "v_mfma_f32_16x16x32_bf16 v[176:179], v[192:195], v[80:83], 0\n\t"
        "s_lshr_b32 s95, s92, 20\n\t"
        "s_xnor_b32 s95, s95, %[wp]\n\t"
        "v_mfma_f32_16x16x32_bf16 v[180:183], v[208:211], v[80:83], 0\n\t"
        "s_and_b32 s95, s95, 1\n\t"
        "s_lshl_b32 s95, s95, 13\n\t"
        "v_mfma_f32_16x16x32_bf16 v[176:179], v[196:199], v[84:87], v[176:179]\n\t"
        "v_cvt_pk_bf16_f32 v228, v184, v185\n\t"
        "v_mfma_f32_16x16x32_bf16 v[180:183], v[212:215], v[84:87], v[180:183]\n\t"
        "v_cvt_pk_bf16_f32 v229, v186, v187\n\t"
        "v_mfma_f32_16x16x32_bf16 v[176:179], v[200:203], v[88:91], v[176:179]\n\t"
        "v_cvt_pk_bf16_f32 v230, v188, v189\n\t"
        "v_mfma_f32_16x16x32_bf16 v[180:183], v[216:219], v[88:91], v[180:183]\n\t"
        "v_cvt_pk_bf16_f32 v231, v190, v191\n\t"
        "v_mfma_f32_16x16x32_bf16 v[176:179], v[204:207], v[92:95], v[176:179]\n\t"
        "buffer_store_dwordx4 v[228:231], %[oa], %[rs], s94 offen sc1\n\t"
        "v_mfma_f32_16x16x32_bf16 v[180:183], v[220:223], v[92:95], v[180:183]\n\t"
        "s_lshl1_add_u32 s93, s92, s91\n\t"
        "s_add_u32 s93, s93, s95\n\t"
        "v_mfma_f32_16x16x32_bf16 v[184:187], v[192:195], v[160:163], 0\n\t"
        "s_add_u32 s92, s92, 0x40000\n\t"
        "v_mfma_f32_16x16x32_bf16 v[188:191], v[208:211], v[160:163], 0\n\t"
        "s_add_u32 s94, s93, 0x10000\n\t"
        "v_mfma_f32_16x16x32_bf16 v[184:187], v[196:199], v[164:167], v[184:187]\n\t"
        "v_cvt_pk_bf16_f32 v224, v176, v177\n\t"
        "v_mfma_f32_16x16x32_bf16 v[188:191], v[212:215], v[164:167], v[188:191]\n\t"
        "v_cvt_pk_bf16_f32 v225, v178, v179\n\t"
        "v_mfma_f32_16x16x32_bf16 v[184:187], v[200:203], v[168:171], v[184:187]\n\t"
        "v_cvt_pk_bf16_f32 v226, v180, v181\n\t"
        "v_mfma_f32_16x16x32_bf16 v[188:191], v[216:219], v[168:171], v[188:191]\n\t"
        "v_cvt_pk_bf16_f32 v227, v182, v183\n\t"
        "v_mfma_f32_16x16x32_bf16 v[184:187], v[204:207], v[172:175], v[184:187]\n\t"
        "buffer_store_dwordx4 v[224:227], %[oa], %[rs], s93 offen sc1\n\t"
        "v_mfma_f32_16x16x32_bf16 v[188:191], v[220:223], v[172:175], v[188:191]\n\t"
        "s_and_b32 s95, %[it], 31\n\t"
        "s_cmp_le_u32 s95, %[nowv]\n\t"
        "s_cbranch_scc1 Lnb%=\n\t"
        "s_waitcnt vmcnt(17)\n\t"
        "s_barrier\n\t"
        "Lnb%=:\n\t"
        "s_setprio 0\n\t"
        "s_nop 3\n\t"
        "v_cvt_pk_bf16_f32 v228, v184, v185\n\t"
        "v_cvt_pk_bf16_f32 v229, v186, v187\n\t"
        "v_cvt_pk_bf16_f32 v230, v188, v189\n\t"
        "v_cvt_pk_bf16_f32 v231, v190, v191\n\t"
        "buffer_store_dwordx4 v[228:231], %[oa], %[rs], s94 offen sc1\n\t"
        "s_nop 1\n\t"
        :
        : [w0] "{v[16:47]}"(W[0]), [w1] "{v[48:79]}"(W[1]), [w2] "{v[80:111]}"(W[2]), [w3] "{v[112:143]}"(W[3]), [w4] "{v[144:175]}"(W[4]), [xa] "v"(xa), [oa] "v"(oa), [rs] "s"(rs), [bl] "s"(bl), [wp] "s"(wp), [it] "s"(it), [nowv] "n"(NOWV), [ca] "s"(ca), [fc] "s"(fc), [fw] "s"(fw), [fo] "s"(fo), [xv] "v"(xv), [xs] "s"(xs), [xd] "s"(xd), [ys] "s"(ys), [yd] "s"(yd)
        : "s88", "s89", "s91", "s92", "s93", "s94", "s95", "v176", "v177", "v178", "v179", "v180", "v181", "v182", "v183", "v184", "v185", "v186", "v187", "v188", "v189", "v190", "v191", "v192", "v193", "v194", "v195", "v196", "v197", "v198", "v199", "v200", "v201", "v202", "v203", "v204", "v205", "v206", "v207", "v208", "v209", "v210", "v211", "v212", "v213", "v214", "v215", "v216", "v217", "v218", "v219", "v220", "v221", "v222", "v223", "v224", "v225", "v226", "v227", "v228", "v229", "v230", "v231", "v232", "v233", "v234", "vcc", "scc", "memory");
}

#define TD0 186
#define TD1 18
#define TD2 184
#define TD3 50
#define TD4 150
#define TD5 130
#define TD6 104
#define TD7 240
#define TD8 8
#define TD9 14
#define TD10 3
#define TD11 2
#define TD12 1
#define TD13 150
#define TD14 110
#define TD15 4
#define TP0 TD0
#define TP1 TD1
#define TP2 TD2
#define TP3 TD3
#define TP4 TD4
#define TP5 TD5
#define TP6 TD6
#define TP7 TD7
#define TP8 TD8
#define TP9 TD9
#define TP10 TD10
#define TP11 TD11
#define TP12 TD12
#define TP13 TD13
#define TP14 TD14
#define TP15 TD15
#define WAHEAD 3
#define FILLG 4
#define WFIRST 2
#define ROT xg
#define SLOW_RT 620u
#define WLATE 600u
#define EARLY_N 35
#define BUMP_N 3
#define BUMP_WIN 0xffu
#define BUMP_S0 8
#define BUMP_MAX 3
#define TV(idx) ((u32)(TP##idx) * 8u)
#define TLOAD() do { } while (0)

__device__ __forceinline__ void nap(u32 n) {
    u32 hi = n >> 6, lo = (n >> 4) & 3u;
    asm volatile("s_cmp_eq_u32 %0, 0\n\ts_cbranch_scc1 Ln%=\nLm%=:\n\ts_sleep 1\n\ts_sub_u32 %0, %0, 1\n\ts_cmp_lg_u32 %0, 0\n\ts_cbranch_scc1 Lm%=\nLn%=:\n\t"
                 "s_cmp_eq_u32 %1, 0\n\ts_cbranch_scc1 Lp%=\nLo%=:\n\ts_sub_u32 %1, %1, 1\n\ts_cmp_lg_u32 %1, 0\n\ts_cbranch_scc1 Lo%=\nLp%=:"
                 : "+s"(hi), "+s"(lo) :: "scc", "memory");
}
typedef __attribute__((address_space(3))) u32* lds_u32p;
typedef __attribute__((address_space(3))) volatile u32* lds_vu32p;
typedef __attribute__((address_space(3))) u16* lds_u16p;
typedef __attribute__((address_space(3))) u32x2* lds_u32x2p;
typedef __attribute__((address_space(3))) u32x4* lds_u32x4p;
__device__ __forceinline__ u32 rfl(u32 x) { return __builtin_amdgcn_readfirstlane(x); }
__device__ __forceinline__ bool has_sent(u32x4 v) { return (v[0] == SENT) | (v[1] == SENT) | (v[2] == SENT) | (v[3] == SENT); }
__device__ __forceinline__ u32 now32() { return (u32)__builtin_amdgcn_s_memtime(); }
__device__ __forceinline__ u32 step_tag(int s) { return (u32)(s + 1) * 0x10001u; }

#define UNI(a, b, c, d, e, f, g, h, i, j) do { } while (0)
template <int S0 = 0>
__device__ __forceinline__ u32 unless_neg(u32 v, int s) {
    u32 r;
    asm("s_cmp_lt_i32 %2, %3\n\ts_cselect_b32 %0, 0, %1" : "=s"(r) : "s"(v), "s"(s), "n"(S0) : "scc");
    return r;
}
template <int S0 = 0>
__device__ __forceinline__ u32x4 live(u32x4 srd, int s) { srd[2] = unless_neg<S0>(srd[2], s); return srd; }
__device__ __forceinline__ float shx(float v, int m, int lane) { return __int_as_float(__builtin_amdgcn_ds_bpermute((lane ^ m) << 2, __float_as_int(v))); }
__device__ __forceinline__ void mem0_image(const u16* __restrict__ mem0, char* smem, const int tid) {
    for (int c = tid; c < 640; c += 512) {
        u32x4 v = *(const u32x4*)(mem0 + (c & 7) * D_ + (c >> 3) * 8);
#pragma unroll
        for (int i = 0; i < 4; ++i) v[i] = v[i] == SENT ? ~0u : v[i];
        ((u32x4*)smem)[c + (c >= 576 ? 64 : 0)] = v;
    }
}
__device__ __forceinline__ u32 once_more(u32& left, u32 lbase, u32* errw) {
    if (__builtin_expect(left != 0, 1)) { --left; return 2u; }
    atomicOr(errw, 1u);
    const int lane = lane_id();
    for (int k = 0; k < 11; ++k) *(lds_u32x4p)(lbase + (u32)(k * 1024 + lane * 16)) = (u32x4){0u, 0u, 0u, 0u};
    asm volatile("s_waitcnt lgkmcnt(0)" ::: "memory");
    return 0u;
}
constexpr u32 TS_MISS = 0x10000u;
__device__ __forceinline__ u32 read_ts(rsrc_t rs, u32 off, int s, u32& m, u32* t2 = nullptr) {
    const int lane = lane_id();
    const u32 vo = lane < 8 ? (u32)lane * 128u : OOB, base = (u32)__builtin_amdgcn_s_memrealtime() * 24u - 0x100000u;
    u32x4 t;
#pragma unroll 1
    for (u32 spins = 1;; ++spins) {
        CFENCE();
        t = ldx4(rs, vo, off);
        m = (u32)__ballot(t[3] == (u32)s + 1u);
        if (((m | (m >> 4)) & 15u) == 15u) break;
        m = 0;
        if (spins >= 0x4000u) break;
        __builtin_amdgcn_s_sleep(4);
    }
    u32 v = t[3] == (u32)s + 1u ? t[1] - base : 0u;
    v = max(v, (u32)__builtin_amdgcn_update_dpp(0u, v, 0xb1, 0xf, 0xf, false));
    v = max(v, (u32)__builtin_amdgcn_update_dpp(0u, v, 0x4e, 0xf, 0xf, false));
    v = max(v, (u32)__builtin_amdgcn_update_dpp(0u, v, 0x141, 0xf, 0xf, false));
    if (t2) {
        u32 w = t[3] == (u32)s + 1u ? t[2] : 0u;
        w = max(w, (u32)__builtin_amdgcn_update_dpp(0u, w, 0xb1, 0xf, 0xf, false));
        w = max(w, (u32)__builtin_amdgcn_update_dpp(0u, w, 0x4e, 0xf, 0xf, false));
        w = max(w, (u32)__builtin_amdgcn_update_dpp(0u, w, 0x141, 0xf, 0xf, false));
        *t2 = to_sgpr(w);
    }
    return to_sgpr(v + base);
}
__device__ __forceinline__ u32 read_ts_late(rsrc_t rs, u32x4 srd, u32 off, int s, u32& m, u32& t2, u32 gap = 1024) {
    u32 t;
    ts_look(t, t2, m, srd, off, (u32)s + 1u);
    if (m) return t;
    nap(gap);
    return read_ts(rs, off, s, m, &t2);
}
__device__ __forceinline__ void until(u32 t) { for (int q = 0; q < 256 && (int)(t - (u32)__builtin_amdgcn_s_memrealtime() * 24u) > 0; ++q) __builtin_amdgcn_s_sleep(1); }
__device__ __forceinline__ u32 umin3(u32 a, u32 b, u32 c) { return min(min(a, b), c); }
__device__ __forceinline__ u32 dist(u32 d, u32x4 v, u32 k) { return umin3(umin3(d, v[0] ^ k, v[1] ^ k), v[2] ^ k, v[3] ^ k); }
#define COUNT(i) do { } while (0)
#define COUNT0() do { } while (0)
#define COUNT1(i) do { } while (0)
__device__ __forceinline__ void touch(u32x4 srd, u32 wp, int s, u32 lds) {
    u32 sv;
    const u32 off = slot_off(wp, s) + SL_TS + 2048u + ((~((wp >> 3) ^ ((u32)s >> 5)) & 1u) << 18);
    asm volatile("s_mov_b32 %0, m0\n\ts_mov_b32 m0, %3\n\ts_nop 3\n\tbuffer_load_dword off, %1, %2 lds\n\ts_mov_b32 m0, %0" : "=&s"(sv) : "s"(srd), "s"(off), "s"(lds) : "memory");
}
__device__ __forceinline__ void track_rate(u32 c0, u32 r0, u32& pc, u32& pr, u32& kr) {
    const u32 dr = r0 - pr;
    if (dr >= 6000u) {
        if (dr < 40000u) kr = to_sgpr(min(max((u32)(4096.f * (float)(c0 - pc) * __builtin_amdgcn_rcpf((float)dr) + 0.5f), 2560u), 4352u));
        pc = c0; pr = r0;
    }
}

#define LANES() const int lane = lane_id(), n = lane & 15, g = lane >> 4; (void)n; (void)g
constexpr u32 L_Q = 12288, L_K = 18432, L_E = 24576, L_DR = 25088, L_MSK = 25600, L_KQ = 32768;
constexpr float LIM_X = 32.f, LIM_D = 1.0995116e12f;
__device__ __forceinline__ u32 kq_buf(int sg) { return L_KQ + (u32)(sg % 3) * 16384u; }

__device__ __forceinline__ u32 p_pos(int row, int g, int dg) { return (u32)((row >> 1) * 64 + (g & 1) * 32 + (row & 1) * 16 + dg * 8 + (g >> 1) * 4); }
__device__ __forceinline__ void s_slow(const int h, const int s, rsrc_t rs, u32 so, u32 lbase) {
    const int lane = lane_id(), row = lane & 7, j = lane >> 3;
    const float ce = 0.08838834764831845f * 1.4426950408889634f;
    float sc[5] = {0.f, 0.f, 0.f, 0.f, 0.f};
    const u32 kx = lbase + kq_buf(s) + (u32)((j >> 2) * 4096 + (j & 3) * 64);
#pragma unroll 1
    for (int e = 0; e < DH_; ++e) {
        const u32 qa = lbase + L_Q + (u32)((e >> 4) * 768 + (row >> 2) * 192 + (e & 15) * 8 + (row & 3) * 2);
        const float q = bf2f(*(lds_u16p)qa);
        const u32 ka = kx + (u32)((e >> 5) * 1024 + ((e >> 2) & 3) * 256 + ((e >> 4) & 1) * 8 + (e & 3) * 2);
#pragma unroll
        for (int i = 0; i < 4; ++i) sc[i] += q * bf2f(*(lds_u16p)(ka + i * 16));
        sc[4] += q * bf2f(*(lds_u16p)(lbase + L_K + (u32)((e >> 4) * 768 + (j >> 1) * 192 + (e & 15) * 8 + (j & 1) * 2)));
    }
    sc[4] *= ce;
    const u32x2 ev = *(lds_u32x2p)(lbase + L_E + (u32)((j >> 1) * 128 + row * 16 + (j & 1) * 8));
    float se = (bf2f((u16)ev[0]) + bf2f((u16)(ev[0] >> 16))) + (bf2f((u16)ev[1]) + bf2f((u16)(ev[1] >> 16)));
    float mx = fmaxf(fmaxf(fmaxf(sc[0], sc[1]), fmaxf(sc[2], sc[3])), sc[4]);
#pragma unroll
    for (int o = 8; o < 64; o <<= 1) { se += shx(se, o, lane); mx = fmaxf(mx, shx(mx, o, lane)); }
    const bool rew = ev[0] == ~0u || ev[1] == ~0u;
    u64 rw = __ballot(rew);
    rw |= rw >> 32; rw |= rw >> 16; rw |= rw >> 8;
    const bool again = (rw >> row) & 1u;
    float al = exp2f(-mx);
    if (again) {
        const u32x2 en = {pack2(exp2f(sc[0] - mx), exp2f(sc[1] - mx)), pack2(exp2f(sc[2] - mx), exp2f(sc[3] - mx))};
        se = (bf2f((u16)en[0]) + bf2f((u16)(en[0] >> 16))) + (bf2f((u16)en[1]) + bf2f((u16)(en[1] >> 16)));
        al = 1.f;
    }
    float sn = se;
#pragma unroll
    for (int o = 8; o < 64; o <<= 1) sn += shx(sn, o, lane);
    if (again) se = sn;
    const float em = exp2f(sc[4] - mx);
    float sm = em;
#pragma unroll
    for (int o = 8; o < 64; o <<= 1) sm += shx(sm, o, lane);
    const float rd = 1.f / (sm + al * se);
    if (rw & 255u) {
#pragma unroll 1
        for (u32 spins = 0; spins < SPIN_CAP && !__all(*(lds_vu32p)(lbase + L_DR + (u32)(lane & 1) * 4) == step_tag(s)); ++spins) __builtin_amdgcn_s_sleep(1);
        const u32x2 en = {pack2(exp2f(sc[0] - mx), exp2f(sc[1] - mx)), pack2(exp2f(sc[2] - mx), exp2f(sc[3] - mx))};
        const u32 eo = again ? (u32)((h >> 3) * 32768 + ((h >> 1) & 3) * 1024 + (j >> 1) * 256 + (h & 1) * 128 + row * 16 + (j & 1) * 8) : OOB;
#pragma unroll
        for (int c = 0; c < NCP; ++c) stx2(rs, eo, en, so + cpy(c));
    }
    DRAIN();
    const u32 pm = pack2(em * rd, shx(em * rd, 8, lane));
    const int g = j >> 1;
    u32 dg = pack2(row == 2 * g ? al * rd : 0.f, row == 2 * g + 1 ? al * rd : 0.f);
    if (again && (row >> 1) == (g ^ 1)) dg = MARK;
    const int gj = h >> 2;
    const u32 po = SL_P + (u32)((gj >> 2) * 32768 + (gj & 1) * 2048 + ((gj >> 1) & 1) * 1024 + (h & 3) * 256) + p_pos(row, g, (j ^ ~h) & 1);
    const bool done = ldx1(rs, po, so) != SENT;
#pragma unroll
    for (int c = 0; c < NCP; ++c) stx1(rs, done ? OOB : po, (j & 1) ? dg : pm, so + cpy(c));
}

__device__ __forceinline__ void s_role(const int wave, const int h, const int xg, const u64 tag, const u16* __restrict__ mem0, const u32x32 (&W)[5],
                                       char* __restrict__ ws, char* smem) {
    const int lane = lane_id(), tid = wave * 64 + lane, n = lane & 15, g = lane >> 4;
    u32* errw = (u32*)(ws + OFF_ERR);
    const rsrc_t rs = mk_rsrc(ws);
    const u32x4 srd = mk_srd(ws);
    const u32 lbase = (u32)(uintptr_t)(__attribute__((address_space(3))) char*)smem;
    TLOAD();
    int wslot = 0; const int wid = 128 + h;
    WSTAMP(2);
    mem0_image(mem0, smem, tid);
    if (tid < 448) *(u32x4*)(smem + L_K + tid * 16) = (u32x4){0u, 0u, 0u, 0u};
    const u32 wp = wait_init(rs, tag, 1);
    if (wave == 2) XSTAMP(wid, 224);
    const u32 sidx = (n & 2) ? 0xEEEE : 0x4444;
    const u32 va = lbase + (u32)(g * 256 + (n & 2) * 64 + (n >> 2) * 32 + (n & 1) * 16);
    const u32 ow = lbase + (u32)((wave & 3) * 768 + n * 8) + (wave >= 4 ? L_Q + (u32)((g >> 1) * 192 + (g & 1) * 4) : L_K + (u32)g * 192);
    u32 rnd = 0;
    const float ce = 0.08838834764831845f * 1.4426950408889634f;
    STAMPS();
    COUNT0();
#define BAR0() asm volatile("s_waitcnt vmcnt(0) lgkmcnt(0)\n\ts_barrier" ::: "memory")
#define Q_LAST() do { u32 redo; do s_l(rnd, redo, lbase); while (__builtin_expect(redo, 0)); } while (0)

    if ((wave >> 1) == 2) {
        LANES();
        const int kt = wave & 1;
        const u32 kxa = lbase + (u32)(kt * 4096 + lane * 16), qtr = lbase + L_Q + (u32)((lane & 1) * 192 + (lane >> 2) * 8);
        const u32 ep = (u32)((n & 7) * 16 + (g & 1) * 8 + (n >> 3) * 4);
        const u32 eo = (u32)((h >> 3) * 32768 + ((h >> 1) & 3) * 1024 + (2 * kt + (g >> 1)) * 256 + (h & 1) * 128) + ep;
        const u32 ew = lbase + L_E + (u32)((2 * kt + (g >> 1)) * 128) + ep;
        const u32 ts = slot_v(wp, lane) + put_off(wp, 2u, lane), tk = kq_buf(lane);
#pragma unroll 1
        for (int s = -1; s + 1 < NSEG; ++s) {
            s_e(W, rnd, va, sidx, ow, lbase, kxa + tab(tk, s & 63), qtr, (u32)kt * 768u, eo, ew, LIM_X, live(srd, s), tab(ts, s & 63) STA);
            DRAIN();
            *(lds_u32p)(lbase + L_DR + (u32)kt * 4) = step_tag(s);
            FLUSH();
        }
        BAR0();
        Q_LAST();
    } else if (wave == 6) {
        LANES();
        const int gj = h >> 2;
        const u32 po = SL_P + (u32)((gj >> 2) * 32768 + (gj & 1) * 2048 + ((gj >> 1) & 1) * 1024 + (h & 3) * 256) + p_pos(n & 7, g, ((n >> 3) ^ ~h) & 1);
        const u32 er = lbase + L_E + (u32)(g * 128 + (n & 7) * 16);
        const u32 qtr = lbase + L_Q + (u32)((lane & 1) * 192 + (lane >> 2) * 8), ktr = lbase + L_K + (u32)((lane & 3) * 192 + (lane >> 2) * 8);
        const u32x4 ones = {0x3f803f80u, 0x3f803f80u, 0x3f803f80u, 0x3f803f80u};
        const u32 ts = slot_v(wp, lane) + put_off(wp, 2u, lane);
#pragma unroll 1
        for (int s = -1; s + 1 < NSEG; ++s) {
            const u32 so = tab(ts, s & 63);
            u32 bad;
            s_f(W, rnd, va, sidx, ow, lbase, bad, ones, qtr, ktr, er, po, n < 8 ? 1.f : 0.f, n == 8 + 2 * g ? 1.f : 0.f, n == 9 + 2 * g ? 1.f : 0.f, live(srd, s), so, ce, LIM_D STA);
            if (__builtin_expect(bad && s >= 0, 0)) s_slow(h, s, rs, so, lbase);
            FLUSH();
        }
        BAR0();
        Q_LAST();
    } else if (wave == 7) {
        LANES();
        const u32 vm = (u32)lane * 16, cp = (u32)xg ^ (wp >> 1);
        u32 c0, r0, kr = 3950;
        calib(c0, r0);
        const u32 tc = slot_v(wp ^ 8u, lane) + copy_off(cp, lane);
        u32 tg = r0, so = 0, left = SPIN_CAP, s = ~0u, poll = 2, cnt = 0;
        LOGS();
        asm volatile("s_waitcnt lgkmcnt(0)" ::: "memory");
#pragma unroll 1
        while (s + 1 != NSEG) {
            u32 ok;
            s_m(W, rnd, va, sidx, ow, lbase, left, s, so, tg, kr, c0, r0, ok, cnt, vm, tc, lane < 8 ? vm * 8u : OOB, lbase + (poll & 2u) * 40960u, live(srd, (int)s), poll,
                NSEG - 1 STA LGA);
            UNI(rnd, left, s, so, tg, kr, c0, r0, ok, cnt);
            if (__builtin_expect(ok == 2u, 0)) { once_more(left, lbase, errw); poll = 0; continue; }
            if (s == 0) { BAR0(); poll = 0; continue; }
            poll = 1;
            if (!ok) tg = read_ts(rs, so + SL_TS, (int)s - 1, ok);
        }
        for (u32 poll = 1;;) {
            if (poll) gather(tg, kr, vm, so, lbase, srd);
            if (poll == 1) LBAR();
            DRAIN();
            u32 redo;
            s_l(rnd, redo, lbase);
            if (__builtin_expect(!redo, 1)) break;
            poll = once_more(left, lbase, errw);
        }
    } else if (wave < 2) {
        LANES();
        const int nt = wave & 1;
        const u32 ktr = lbase + L_K + (u32)((lane & 3) * 192 + (lane >> 2) * 8);
        f32x4* msk = (f32x4*)(smem + L_MSK) + nt * 128 + lane;
#pragma unroll
        for (int q = 0; q < 2; ++q) {
            f32x4 m;
#pragma unroll
            for (int i = 0; i < 4; ++i) m[i] = q * 16 + 4 * g + i <= nt * 16 + n ? 0.f : -__builtin_huge_valf();
            msk[q * 64] = m;
        }
        const u32 vl = lbase + (u32)lane * 16;
        const u32 o0 = (u32)OFF_PX + (u32)((h * 2 + nt) * 1024 + lane * 16);
        const u32 o1 = (u32)OFF_PXM + (u32)(((h >> 2) * 2 + nt) * 1024 + ((h & 3) * 16 + n) * 16 + g * 4);
        u32x4 p0 = {0u, 0u, 0u, 0u};
        u32 p1 = 0;
#pragma unroll 1
        for (int s = -1; s + 1 < NSEG; ++s) {
            const u32 kb = vl + kq_buf(s + 3);
            s_k1(W, rnd, va, sidx, ow, lbase, p0, p1, kb, kb + 8192 + (u32)nt * 4096, vl + L_MSK + (u32)nt * 2048, ktr, o0 + (u32)max(s - 1, 0) * 65536,
                 o1 + (u32)max(s - 1, 0) * 16384, ce, live<1>(srd, s) STA);
            rnd = to_sgpr(rnd);
            FLUSH();
        }
        s_k3(W, rnd, va, sidx, ow, lbase, p0, p1, vl + kq_buf(NSEG - 1), vl + kq_buf(NSEG - 1) + 8192 + (u32)nt * 4096, vl + L_MSK + (u32)nt * 2048, ktr,
             o0 + (NSEG - 2) * 65536, o1 + (NSEG - 2) * 16384, ce, srd, lane == 0 ? (u32)(OFF_FP + (h >> 2) * 128 + ((h & 3) * 2 + nt) * 4) : OOB);
        stx4(rs, o0 + (NSEG - 1) * 65536, p0);
        stx1(rs, o1 + (NSEG - 1) * 16384, p1);
        DRAIN();
        stx1(rs, lane == 0 ? (u32)(OFF_FZ + ((h >> 2) * 2 + nt) * 128 + (h & 3) * 4) : OOB, 1u);
    } else if (wave == 2) {
        LANES();
        u32 cap = SPIN_CAP;
        u32 have = 0;
        auto seen = [&](u32x4 f) {
            u32 a = min(2 * rfl(f[0]), 2 * rfl(f[1]) + 1);
            if (h < 16) a = min(a, max(rfl(f[2]), 4u) - 1u);
            have = max(have, a);
        };
        const u32 tt = slot_v(wp ^ 8u, lane) + SL_TS + 2048u;
        const u32 tp = slot_v(wp ^ 8u, lane) + copy_off((u32)xg ^ (wp >> 1), lane) + SL_PG - 128u;
        *(lds_u32p)(lbase + 11520u + (u32)lane * 4) = 0u;
#pragma unroll 1
        for (int s = -1; s + 1 < NSEG; ++s) {
            const u32 pfo = far_off(wp ^ KQX, NEAR_KX + (u32)((s + 1) * H_ + h) * 8192);
            s_k0(W, rnd, va, sidx, ow, lbase STA);
            have = (u32)max((int)have, (int)rfl(*(lds_vu32p)(lbase + 11520u + (u32)((s + 63) & 63) * 4)));
            u32 spins = 0;
#pragma unroll 1
            for (; have <= (u32)(s + 1) && spins < cap; ++spins) {
                CFENCE();
                seen(ldx4(rs, 0u, (u32)(OFF_FA + h * 128)));
                if (spins) __builtin_amdgcn_s_sleep(2);
            }
            if (spins >= cap) { atomicOr(errw, 8u); cap = 1; }
            CFENCE();
            if (s < 0) XSTAMP(wid, 226);
            s_fetch((u32)lane * 16, lane == s + 1 ? tt : OOB, srd, pfo, lbase + kq_buf(s + 1), lbase + 11264u);
            s_fetch((u32)lane * 16, lane == s ? tp : OOB, srd, pfo - 2u * (NEAR_KX - NEAR_QX),
                    lbase + kq_buf(s + 1) + 8192, lbase + 11520u);
            FLUSH();
        }
        s_k2(W, rnd, va, sidx, ow, lbase);
    } else {
#pragma unroll 1
        for (int s = -1; s + 1 < NSEG; ++s) {
            s_k0(W, rnd, va, sidx, ow, lbase STA);
            FLUSH();
        }
        s_k2(W, rnd, va, sidx, ow, lbase);
    }
    COUNT1(0);
}

__device__ __forceinline__ void v_role(const int wave, const int h, const int xg, const u64 tag, const u16* __restrict__ mem0, const u32x32 (&W)[5],
                                       char* __restrict__ ws, char* smem) {
    const int lane = lane_id(), tid = wave * 64 + lane, n = lane & 15, g = lane >> 4;
    u32* errw = (u32*)(ws + OFF_ERR);
    const rsrc_t rs = mk_rsrc(ws);
    const u32x4 srd = mk_srd(ws);
    const u32 lbase = (u32)(uintptr_t)(__attribute__((address_space(3))) char*)smem;
    TLOAD();
    int wslot = 0; const int wid = 160 + h;
    WSTAMP(2);
    mem0_image(mem0, smem, tid);
    *(lds_u32p)(lbase + 14336) = 0u;
    const u32 wp = wait_init(rs, tag, 1);
    const u32 sidx = (n & (wave < 4 ? 2 : 8)) ? 0xEEEE : 0x4444;
    asm volatile("s_waitcnt vmcnt(0) lgkmcnt(0)" ::: "memory");
    STAMPS();
    COUNT0();
    u32 rnd = 0;
    if (wave < 4) {
        const u32 va = lbase + (u32)(g * 256 + (n & 2) * 64 + (n >> 2) * 32 + (n & 1) * 16), vw = lbase + 12288 + (u32)((wave * 16 + n) * 16 + g * 4);
        if (wave) {
#pragma unroll 1
            for (int s = 0; s < NSEG; ++s) {
                v_a0(W, rnd, va, sidx, vw, lbase STA);
                FLUSH();
                if (wave == 1 && s + 1 < NSEG) touch(srd, wp, s + 1, lbase + 15360u);
            }
        } else {
            const u32 tc = slot_v(wp ^ 8u, lane) + copy_off((u32)xg ^ (wp >> 1), lane);
            u32 c0, r0, kr = 3950;
            calib(c0, r0);
            u32 tg = r0, so = 0, left = SPIN_CAP, s = 0, poll = 2, cnt = 0;
            LOGS();
#pragma unroll 1
            while (s != NSEG) {
                u32 ok;
                v_a1(W, rnd, va, sidx, vw, lbase, left, s, so, tg, kr, c0, r0, ok, cnt, (u32)lane * 16, tc, lane < 8 ? (u32)lane * 128u : OOB, lbase + (poll & 2u) * 16384u,
                     live<1>(srd, (int)s), poll, NSEG, TV(15) STA LGA);
                UNI(rnd, left, s, so, tg, kr, c0, r0, ok, cnt);
                if (__builtin_expect(ok == 2u, 0)) { once_more(left, lbase, errw); poll = 0; continue; }
                poll = 1;
                if (!ok && s != NSEG) tg = read_ts(rs, so + SL_TS, (int)s - 1, ok);
            }
        }
    } else {
        const u32 vr = lbase + 12288 + (u32)((g * 8 + (n >> 2)) * 16 + (n & 1) * 8);
        u32 z[3];
#pragma unroll
        for (int p = 0; p < 3; ++p) z[p] = p == 2 && (g & 1) ? OOB : z_off(wp, (u32)((p * 2 + (g & 1)) * 8 + (wave & 3) * 2 + (g >> 1))) + (u32)(h * 256 + n * 16);
#pragma unroll 1
        for (int s = 0; s < NSEG; ++s) {
            const u32 sz = slot_off(wp, s);
            v_b(W, rnd, sidx, vr, z[0] + sz, z[1] + sz, z[2] + sz, srd, lbase STA);
            FLUSH();
        }
    }
    DRAIN();
    LBAR();
    stx1(rs, wave == 0 && lane_id() < 2 ? (u32)(OFF_FZ + ((h >> 2) * 2 + lane_id()) * 128 + 16 + (h & 3) * 4) : OOB, 1u);
    COUNT1(3);
}

constexpr u32 L_U = 8192, L_T = 16384;
__device__ __forceinline__ u32x4 sub4(const u32x32& v, const int i) { return (u32x4){v[i], v[i + 1], v[i + 2], v[i + 3]}; }
__device__ __forceinline__ void r_slow(const int s, rsrc_t rs, u32 so, u32 sz, u32 sm, u32 pv, u32 zv, u32 ra, u32 rb, u32 mo, u32 sidx, u32 lbase, u32 gx, u32& cap, u32* errw) {
    const int lane = lane_id(), n = lane & 15;
    u32 spins = 0;
    u32x4 p[8], z[8];
#pragma unroll 1
    for (; spins < cap; ++spins) {
        CFENCE();
        u32 d = 1;
#pragma unroll
        for (int j = 0; j < 8; ++j) p[j] = ldx4(rs, pv + (u32)(j & 1) * 256, so + ((gx ^ (u32)(j >> 1)) & 1u) * 1024u + ((gx ^ (u32)(j >> 1)) >> 1) * 32768u);
#pragma unroll
        for (int j = 0; j < 8; ++j) z[j] = ldx4(rs, zv + (u32)(j & 1) * 1024, sz + (gx ^ (u32)(j >> 1)) * 2048u);
#pragma unroll
        for (int j = 0; j < 8; ++j) d = dist(dist(d, p[j], SENT), z[j], SENT);
        if (!__any(d == 0)) break;
    }
    if (spins >= cap) { atomicOr(errw, 2u); cap = 2; }
#pragma unroll
    for (int j = 0; j < 8; ++j) {
#pragma unroll
        for (int i = 0; i < 4; ++i) p[j][i] = p[j][i] == MARK ? 0u : p[j][i];
    }
    *(lds_vu32p)(lbase + L_T + 12) = step_tag(s);
    LBAR();
    LBAR();
    f32x4 acc = {0.f, 0.f, 0.f, 0.f};
#pragma unroll
    for (int j = 0; j < 8; j += 2) {
        const u32 ua = (j == 6 ? rb : ra) + (u32)j * 1024;
        acc = smf((u32x4){p[j][2], p[j][3], p[j + 1][0], p[j + 1][1]}, z[j], z[j + 1], acc, (int)sidx);
        acc = smf((u32x4){p[j + 1][2], p[j + 1][3], p[(j + 2) & 7][0], p[(j + 2) & 7][1]}, *(lds_u32x4p)ua, *(lds_u32x4p)(ua + 1024), acc, (int)sidx);
    }
    const float x0 = acc[0] + acc[2], x1 = acc[1] + acc[3], o = __shfl_xor(n < 8 ? x1 : x0, 8);
    const u32 r = n < 8 ? pack2(x0, o) : pack2(o, x1);
#pragma unroll
    for (int c = 0; c < NCP; ++c) stx1(rs, mo, r, sm + cpy(c));
    LBAR();
}

__device__ __forceinline__ void r_role(const int wave, const int ct, const int xg, const u32 wp, char* __restrict__ ws, char* smem) {
    const int lane = lane_id(), tid = wave * 64 + lane, n = lane & 15, g = lane >> 4;
    u32* errw = (u32*)(ws + OFF_ERR);
    const rsrc_t rs = mk_rsrc(ws);
    const u32x4 srd = mk_srd(ws);
    const u32 lbase = (u32)(uintptr_t)(__attribute__((address_space(3))) char*)smem;
    if (tid < 6) *(u32*)(smem + L_T + 8 + tid * 4) = ~0u;
    asm volatile("s_waitcnt lgkmcnt(0)" ::: "memory");
    int wslot = 0; const int wid = 192 + ct;
    WSTAMP(1);
    TLOAD();
    const u32 cp = (u32)xg ^ (wp >> 1);
    u32 cap = SPIN_CAP;
    STAMPS();
    LOGS();
    COUNT0();
    if (wave == 0) {
        const int kp = ct >= 36 ? 10 : ct >> 2;
        const u32 mb = (u32)((kp >> 2) * 32768 + (kp & 3) * 1024 + (ct & 3) * 256);
        const u32 mo = mb + (u32)(((n & 7) >> 2) * 128 + (2 * g + (n >> 3)) * 16 + (n & 3) * 4);
        const u32 pv = (u32)((g >> 1) * 2048 + (g & 1) * 512 + n * 16), zv = (u32)(g * 256 + (n & 7) * 32 + (n >> 3) * 16), sidx = (n & 2) ? 0xEEEE : 0x4444;
        const u32 ra = lbase + L_U + (u32)(((g & 1) ? 2048 : 256) + (g >> 1) * 512 + (n & 7) * 32 + (n >> 3) * 16), rb = ra - (u32)(g & 1) * 8192;
        const u32 te = slot_v(wp, lane) + copy_off(cp, lane) + SL_P, tm = slot_v(wp ^ 8u, lane) + put_off(wp, 0u, lane), tz = slot_z(wp, lane) + z_off(wp, (u32)ct);
#pragma unroll 1
        for (int s = -1; s + 1 < NSEG; ++s) {
            const u32 so = tab(te, s & 63), sm = tab(tm, s & 63), sz = tab(tz, s & 63);
            u32 bad, pass;
            r_f(bad, pass, sidx, pv, zv, ra, rb, mo, lbase + L_T + 16 + (u32)(lane & 3) * 4, live(srd, s), so, sz, sm, lbase, step_tag(s), unless_neg(cap, s), min(TV(8), 256u), (u32)((ROT >> 1) * 32768 + (ROT & 1) * 1024), (u32)ROT * 2048u STA);
            if (__builtin_expect(bad, 0)) { if (s < 0) LBAR(); else r_slow(s, rs, so, sz, sm, pv, zv, ra, rb, mo, sidx, lbase, (u32)ROT, cap, errw); }
            if (pass) COUNT(2);
            touch(srd, wp, s + 1, lbase + 17408u);
            LOG();
        }
    } else if (wave == 4) {
        u32 c0, r0, kr = 3950;
        calib(c0, r0);
        const u32 tc = slot_v(wp ^ 8u, lane) + copy_off(cp, lane) + SL_TS;
        u32 pc = c0, pr = r0, tg = r0 - 0x10000u, t2 = 0, fp = 1;
#pragma unroll 1
        for (int s = -1; s + 1 < NSEG; ++s) {
            u32 t1 = tg, ok = 0;
            if (s) r_t0(c0, r0, t1, t2, ok, tg + TV(6), tg + (t2 & 0xfffu), kr, (u32)(s & 127) + 1u, fp, lbase, live(srd, s), tab(tc, s & 63) STA);
            else r_te(lbase, fp);
            fp = step_tag(s);
            if (s < 0) { if (ok == 0u || ok == 3u) LBAR(); }
            else if (__builtin_expect(ok == 0u || ok == 3u, 0)) {
                if (!ok) until(tg + TV(6) + TV(7) - 256u);
                r_t1(c0, r0, t1, t2, lane < 8 ? (u32)lane * 128u : OOB, kr, (u32)s + 1u, lbase, srd, tab(tc, s) STA);
            }
            if (__builtin_expect(ok == 2u && s >= 0, 0)) { r_te(lbase, fp); fp = 1; t1 = read_ts(rs, tab(tc, s), s, ok); }
            tg = t1;
            track_rate(c0, r0, pc, pr, kr);
            LOG();
        }
        r_te(lbase, fp);
    } else {
        const int o = wave == 5 ? 3 : wave - 1, q = o ^ ROT;
        u32 have = 0;
        u32x32 bn = {};
        auto seen = [&](u32x2 f) {
            u32 a = min(2 * f[0], 2 * f[1] + 1);
            a = min(a, (u32)__builtin_amdgcn_update_dpp(0u, a, 0xb1, 0xf, 0xf, false));
            a = min(a, (u32)__builtin_amdgcn_update_dpp(0u, a, 0x4e, 0xf, 0xf, false));
            a = min(a, (u32)__builtin_amdgcn_update_dpp(0u, a, 0x124, 0xf, 0xf, false));
            have = max(have, rfl(a));
        };
        const u32 fo = (u32)(OFF_FB + (q * 8 + (lane & 7)) * 128);
        auto load_b = [&](int sg, u32x32& bx) {
            u32 spins = 0;
#pragma unroll 1
            for (; have <= (u32)sg && spins < cap; ++spins) {
                CFENCE();
                seen(ldx2(rs, fo));
                if (spins) __builtin_amdgcn_s_sleep(4);
            }
            if (spins >= cap) { atomicOr(errw, 16u); cap = 1; }
            CFENCE();
            const u32 bs = far_off(wp, FAR_BX + (u32)(sg * NRED + ct) * 32768 + (u32)q * 8192);
            pull((u32)lane * 128, bs, srd, 1u, 0u, lbase + 17664u);
            u32x4 t[8];
#pragma unroll
            for (int i = 0; i < 8; ++i) t[i] = ldx4(rs, (u32)(lane * 16 + i * 1024), bs);
#pragma unroll
            for (int i = 0; i < 8; ++i)
#pragma unroll
                for (int j = 0; j < 4; ++j) bx[(i >> 1) * 8 + j * 2 + (i & 1)] = t[i][j];
        };
        const u32 eo = (u32)(q * 32768 + lane * 16), te = slot_v(wp, lane) + copy_off(cp, lane), sidx = n >= 8 ? 0xEEEE : 0x4444;
        const u32 uw = lbase + L_U + (u32)(o * 2048 + (g >> 1) * 256 + n * 16 + (g & 1) * 8);
        const u32 tb = far_v(wp, FAR_BX + (u32)(min(lane + 1, NSEG - 1) * NRED + ct) * 32768 + (u32)q * 8192);
        const u32 tp = slot_v(wp ^ 8u, lane) + copy_off(cp, lane) + SL_PG + (u32)q * 128;
        u32 s = ~0u, why = 0, left = 0;
#pragma unroll 1
        for (;;) {
            if (why & 1u) {
#pragma unroll
                for (int i = 0; i < 4; ++i) {
                    const f32x4 u = smf(ldx4(rs, eo + (u32)i * 1024, tab(te, (int)s - 1)), sub4(bn, i * 8), sub4(bn, i * 8 + 4), (f32x4){0.f, 0.f, 0.f, 0.f}, (int)sidx);
                    *(lds_u32x2p)(uw + (u32)i * 512) = (u32x2){pack2(u[0], u[1]), pack2(u[2], u[3])};
                }
                LBAR();
                LBAR();
            }
            if (s + 1 == NSEG) break;
            if (s == 0) left = SPIN_CAP;
            if ((int)s >= 0) load_b((int)s, bn);
            have = to_sgpr(have); left = to_sgpr(left);
            r_u(bn, s, have, left, why, sidx, eo, uw, lbase + L_T + 16 + (u32)q * 4, (u32)lane * 16, tp, te, tb, live(srd, (int)s), (u32)o * 4u, lbase STA LGA);
            s = to_sgpr(s); have = to_sgpr(have); left = to_sgpr(left); why = to_sgpr(why);
        }
        if (left == 0) atomicOr(errw, 4u);
    }
    COUNT1(wave ? 1 : 2);
    LOGOUT(32 + ct);
}

__device__ __forceinline__ void relay_role(const u32 wp, const int xg, char* __restrict__ ws) {
    const int lane = lane_id();
    const rsrc_t rs = mk_rsrc(ws);
    const u32 tc = slot_v(wp ^ 8u, lane) + copy_off((u32)xg ^ (wp >> 1), lane) + SL_TS;
#pragma unroll 1
    for (int k = 0; k + 1 < NSEG; ++k) {
        u32 t;
#pragma unroll 1
        for (u32 spins = 0;; ++spins) {
            u32 ok;
            t = read_ts(rs, tab(tc, k), k, ok);
            if (ok) break;
            if (spins >= 16u || ldx1(rs, (u32)OFF_ERR) != 0u) return;
        }
        if (k >= 3) stx1(rs, lane < 8 ? (u32)(OFF_RDY + lane * 128) : OOB, (u32)k - 2u);
        for (int q = 0; q < 64 && (int)((u32)__builtin_amdgcn_s_memrealtime() * 24u - t) < 2400; ++q) __builtin_amdgcn_s_sleep(2);
    }
}

__device__ __forceinline__ void lead_role(const u32 wp, const int xg, char* __restrict__ ws, char* smem) {
    const int lane = lane_id();
    u32* errw = (u32*)(ws + OFF_ERR);
    const rsrc_t rs = mk_rsrc(ws);
    const u32x4 srd = mk_srd(ws);
    TLOAD();
    STAMPS();
    const u32 tsw = lane < 4 ? SL_TS + (u32)((lane & 1) * 4096 + (lane >> 1) * 16384 + xg * 128) : OOB;
    const u32 tm = slot_v(wp ^ 8u, lane), tc = tm + copy_off((u32)xg ^ (wp >> 1), lane);
    const u32 m0o = lane < 40 ? (u32)((lane >> 4) * 32768 + (lane & 15) * 256 + (lane >= 36 ? 1024 : 0) + 128) : OOB;
    u32 c0, r0, kr = 3950, ki = 4240, bump = 0, hist = 0, dp = 0;
    calib(c0, r0);
    u32 pc = c0, pr = r0, tg = r0, cap = SPIN_CAP, m = 15, de, dd, per, kc = ki;
    auto plan = [&](int s) {
        const u32 mg = bump + (s < EARLY_N ? TV(10) : 0u) + (s < 4 ? TV(3) : 0u);
        de = (TV(0) * kc >> 12) + mg;
        dd = de | ((TV(1) * kc >> 12) << 12) | ((ki - 2048u) << 20);
        per = max(de, dp) + (TV(2) * kc >> 12) + mg;
        dp = de;
    };
    plan(0);
    LOGS();
#pragma unroll 1
    for (int s = 0; s + 1 < NSEG; ++s) {
        u32 sr = tsw + tab(tm, s), look = 0;
        const u32 so = tab(tc, (s + 63) & 63);
#pragma unroll 1
        for (;; ++look) {
            u64 tl;
            u32 miss;
            l_g(miss, c0, r0, tl, tg, look ? 0u : kr, m0o, so, live<1>(srd, s), (u32x4){tg + de, tg + per, dd, (u32)s + 1u}, look || !s ? OOB : sr, SLOW_RT, TV(4) STA);
            if (!s) break;
            if (look && (int)((u32)tl * 24u + TV(14) - tg) > 0) tg = (u32)tl * 24u + TV(14);
            if (__builtin_expect(!miss, 1)) break;
            if (!look) {
                sr += 512u;
                tg += TV(7);
            }
            if (look >= cap) { atomicOr(errw, 4u); cap = 1; break; }
        }
        if (!s) {
            bool dead = false;
            wait_word(rs, (u32)(OFF_FA + lane * 128), true, 1u, dead, errw, 8u, lane);
            XSTAMP(192 + xg, 227);
            tg = (u32)__builtin_amdgcn_s_memrealtime() * 24u + TV(13) - 512u;
        }
        if (!s || look) stx4(rs, sr, (u32x4){tg + de, tg + per, dd, (u32)s + 1u});
        touch(srd, wp, s + 1, (u32)(uintptr_t)(__attribute__((address_space(3))) char*)smem);
        track_rate(c0, r0, pc, pr, kr);
        if (pc == c0) { const u32 kn = to_sgpr((u32)(16777216.f * __builtin_amdgcn_rcpf((float)kr) + 0.5f)); ki = min(max(kn, (ki * 3u + kn) >> 2), 6000u); }
        hist = ((hist << 1) | (s > BUMP_S0 && m != 15u ? 1u : 0u)) & BUMP_WIN;
        if (__builtin_popcount(hist) >= BUMP_N && bump < BUMP_MAX * TV(11)) { bump += TV(11); hist = 0; }
        plan(s + 1);
        asm volatile("s_sleep 3" : "+s"(de), "+s"(dd), "+s"(per));
        u32 t2;
        tg = look ? read_ts(rs, tab(tc, s) + SL_TS, s, m, &t2) : read_ts_late(rs, srd, tab(tc, s) + SL_TS, s, m, t2, 256u);
        kc = max(t2 >> 20, 1800u) + 2048u;
        LOG();
    }
    if (xg == 0) LOGOUT(104);
}

__device__ __forceinline__ void fill_role(const u32 wp, const int fi, char* __restrict__ ws) {
    const int lane = lane_id();
    const rsrc_t rs = mk_rsrc(ws);
    TLOAD();
    const u32 oz = (u32)(fi * 65536 + lane * 16);
#pragma unroll 1
    for (int s = 5; s < NSEG; ++s) {
        const u32 un = 0x38u << ((~((wp >> 3) ^ ((u32)s >> 5)) & 1u) * 8u), a = oz + slot_off(wp, s);
        const u32 a0 = (un >> (2 * fi)) & 1u ? OOB : a, a1 = (un >> (2 * fi + 1)) & 1u ? OOB : a + 32768u;
#pragma unroll
        for (u32 i = 0; i < 32; ++i) {
            stx4(rs, (i < 16 ? a0 : a1) + ((i >> 3) & 1u) * 16384 + (i & 7) * 1024, (u32x4){SENT, SENT, SENT, SENT});
            if (i == 15) {
                asm volatile("s_waitcnt vmcnt(16)" ::: "memory");
                stx1(rs, lane == 0 ? (u32)(OFF_FA + fi * 128 + 8) : OOB, (u32)s - 1u);
            }
            if (i % FILLG == FILLG - 1) nap(TV(9));
        }
    }
    DRAIN();
    stx1(rs, lane == 0 ? (u32)(OFF_FA + fi * 128 + 8) : OOB, (u32)NSEG + 1u);
}

__device__ __forceinline__ int tile_of(const int xg, const int hl, const int i) {
    const u32 pieces = hl ? 0x85410u : 0x97632u;
    return CFG ? (int)((pieces >> ((i >> 1) * 4)) & 15u) * 4 + (i & 1) * 2 + (xg & 1) : (int)((pieces >> (i * 4)) & 15u) * 4 + xg;
}
__device__ __forceinline__ void warm_role(const u32 wp, const int xg, const int wi, char* __restrict__ ws, char* smem) {
    const int lane = lane_id();
    const rsrc_t rs = mk_rsrc(ws);
    const u32x4 srd = mk_srd(ws);
    const u32 lbase = (u32)(uintptr_t)(__attribute__((address_space(3))) char*)smem;
    TLOAD();
    u32 have = 0, ta = 0;
    int next = 1;
    const u32 tc = slot_v(wp ^ 8u, lane) + copy_off((u32)xg ^ (wp >> 1), lane) + SL_TS;
    const u32 wq = wi < 0 ? wp ^ KQX : wp, ls = wi < 0 ? H_ * 8192u : NRED * 32768u;
    const u32 l0 = wi < 0 ? (wi & 1 ? NEAR_KX : NEAR_QX) + (u32)(xg * 8 + (lane & 7)) * 8192 : FAR_BX + (u32)(min(lane, 9) * 4 + xg) * 32768 + (u32)wi * 8192;
    auto look = [&] {
        const u32x4 f = ldx4(rs, wi < 0 ? (u32)(OFF_FA + (xg * 8 + (lane & 7)) * 128) : (u32)(OFF_FB + (wi * 8 + (lane & 7)) * 128));
        u32 a = min(2 * f[0], 2 * f[1] + 1);
        if (wi < 0 && xg < 2) a = min(a, max(f[2], 4u) - 1u);
#pragma unroll
        for (int q = 1; q < 8; q <<= 1) a = min(a, (u32)__shfl_xor((int)a, q));
        have = rfl(a);
    };
#pragma unroll 1
    for (u32 spins = 0; next < WFIRST && spins < 0x4000u; ++spins) {
        CFENCE();
        if (have <= (u32)next) { __builtin_amdgcn_s_sleep(4); look(); }
        else { pull((u32)lane * 128, far_v(wq, l0 + (u32)next * ls), srd, wi < 0 ? 8u : 10u, 64u, lbase + 69632u); ++next; }
    }
#pragma unroll 1
    for (int s = 0; s + 2 < NSEG; ++s) {
        if (s) until(ta + 1000u);
        u32 tm, t2;
        ta = read_ts_late(rs, srd, tab(tc, s), s, tm, t2);
        if (have <= (u32)s + 8u) look();
        for (int q = 0; q < 64 && have <= (u32)min(s + WAHEAD, NSEG - 1) && (int)(ta + TV(5) - 1024u + WLATE - (u32)__builtin_amdgcn_s_memrealtime() * 24u) > 0; ++q) { CFENCE(); look(); }
        stx1(rs, lane == 0 ? (u32)(SL_PG - SL_TS + wi * 128) : OOB, have, tab(tc, s + 1));
        if (next < s + 2) next = s + 2;
#pragma unroll 1
        for (; next <= s + WAHEAD && next < NSEG && have > (u32)next; ++next) {
            const u32 tb = far_v(wq, l0 + (u32)next * ls);
            until(ta + TV(5) - 1024u);
            pull((u32)lane * 128, tb, srd, wi < 0 ? 8u : 10u, min(TV(12), 256u), lbase + 69632u);
        }
    }
    DRAIN();
}

__device__ __forceinline__ void topk_role(const u64 tag, char* __restrict__ ws) {
    const int lane = lane_id();
    const rsrc_t rs = mk_rsrc(ws);
    wait_init(rs, tag, 2);
    u32 v[32];
#pragma unroll
    for (int i = 0; i < 8; ++i) {
        const u32x4 t = ldx4(rs, (u32)lane * 16, (u32)(OFF_R + i * 1024));
#pragma unroll
        for (int j = 0; j < 4; ++j) v[i * 4 + j] = ((t[j] ^ ((u32)((int)t[j] >> 31) | 0x80000000u)) & 0xffff0000u) | (u32)(2047 - i * 256 - j - lane * 4);
    }
    u32 top = ~0u, res = 0;
#pragma unroll 1
    for (int c = 0; c < TOPC; ++c) {
        u32 d = ~0u;
#pragma unroll
        for (int i = 0; i < 32; ++i) d = min(d, top - v[i]);
        d = min(d, (u32)__builtin_amdgcn_update_dpp(0u, d, 0xb1, 0xf, 0xf, false));
        d = min(d, (u32)__builtin_amdgcn_update_dpp(0u, d, 0x4e, 0xf, 0xf, false));
        d = min(d, (u32)__builtin_amdgcn_update_dpp(0u, d, 0x124, 0xf, 0xf, false));
        d = min(d, (u32)__builtin_amdgcn_update_dpp(0u, d, 0x128, 0xf, 0xf, false));
        const u32 dm = min(min((u32)__builtin_amdgcn_readlane(d, 0), (u32)__builtin_amdgcn_readlane(d, 16)), min((u32)__builtin_amdgcn_readlane(d, 32), (u32)__builtin_amdgcn_readlane(d, 48)));
        top -= dm;
        if (lane == c) res = 2048u - (top & 2047u);
        top -= 1u;
    }
    stx1(rs, lane < TOPC ? (u32)(OFF_IDX + lane * 4) : OOB, res);
    XSTAMP(192, 224);
}

constexpr int NGU = FF_ / 32, NDN = 30;
template <bool UP>
__device__ __forceinline__ void moe_task(const int id, const int tid, const u32 row, const u16* __restrict__ w0, const u16* __restrict__ w1,
                                         char* __restrict__ ws, bool& dead, char* redp, u32* nextp, u32* tctr) {
    constexpr int LD = UP ? FF_ : D_;
    const int wave = __builtin_amdgcn_readfirstlane(tid >> 6), lane = tid & 63, n = lane & 15, g = lane >> 4;
    const rsrc_t rs = mk_rsrc(ws);
    const int part = UP ? id & 1 : id / 10, grp = UP ? id >> 1 : id - part * 10;
    const int k0 = (UP ? 0 : part * 18) + wave, nk = wave < (UP ? 4 : 2) ? 3 : 2;
    const u16* wp = (UP && part ? w1 : w0) + (size_t)(k0 * 32 + g * 8) * LD + grp * 64 + n * 4;
    u32x2 r[3][8];
#pragma unroll
    for (int k = 0; k < 3; ++k)
#pragma unroll
        for (int i = 0; i < 8; ++i) r[k][i] = __builtin_nontemporal_load((const u32x2*)(wp + (size_t)(min(k, nk - 1) * 256 + i) * LD));
    if (!UP) wait_word(rs, (u32)OFF_CNT, true, (u32)NGU, dead, (u32*)(ws + OFF_ERR), 16u, lane);
    u32x4 af[3];
#pragma unroll
    for (int k = 0; k < 3; ++k) {
        const u32 o = k >= nk ? OOB : UP ? (u32)OFF_XN + (row * D_ + (u32)((k0 + k * 8) * 32 + g * 8)) * 2 : (u32)(OFF_HB + ((size_t)(n & 7) * FF_ + (k0 + k * 8) * 32 + g * 8) * 2);
        af[k] = ldx4(rs, o);
        if (!UP) {
            const u32x4 uu = ldx4(rs, o + 8 * FF_ * 2);
#pragma unroll
            for (int j = 0; j < 4; ++j) {
                const float g0 = __uint_as_float(af[k][j] << 16), g1 = __uint_as_float(af[k][j] & 0xffff0000u);
                af[k][j] = pack2(bfr(g0 / (1.0f + __expf(-g0))) * __uint_as_float(uu[j] << 16), bfr(g1 / (1.0f + __expf(-g1))) * __uint_as_float(uu[j] & 0xffff0000u));
            }
        }
    }
    CFENCE();
    u32 next = 0;
    if (tid == 0) next = __hip_atomic_fetch_add(tctr, 1u, __ATOMIC_RELAXED, __HIP_MEMORY_SCOPE_AGENT);
    f32x4* red = (f32x4*)redp;
#pragma unroll
    for (int c = 0; c < 4; ++c) {
        f32x4 acc = {0.f, 0.f, 0.f, 0.f};
#pragma unroll
        for (int k = 0; k < 3; ++k) {
            u32x4 bf;
#pragma unroll
            for (int j = 0; j < 4; ++j) bf[j] = __builtin_amdgcn_perm(r[k][2 * j + 1][c >> 1], r[k][2 * j][c >> 1], (c & 1) ? 0x07060302u : 0x05040100u);
            acc = mfma16(af[k], bf, acc);
        }
        red[(wave * 4 + c) * 64 + lane] = acc;
    }
    if (tid == 0) *nextp = next;
    LBAR();
    if (wave == 7) {
        f32x4 t[4];
#pragma unroll
        for (int c = 0; c < 4; ++c) {
            const f32x4* p = red + c * 64 + lane;
            t[c] = ((p[0] + p[256]) + (p[512] + p[768])) + ((p[1024] + p[1280]) + (p[1536] + p[1792]));
        }
#pragma unroll
        for (int i = 0; i < 4; ++i) {
            const u32 e = (u32)((part * 8 + 4 * g + i) * LD + grp * 64 + n * 4);
            if (UP) stx2(rs, g < 2 ? (u32)OFF_HB + e * 2 : OOB, (u32x2){pack2(t[0][i], t[1][i]), pack2(t[2][i], t[3][i])});
            else stx4(rs, g < 2 ? (u32)OFF_UPD + e * 4 : OOB, (u32x4){__float_as_uint(t[0][i]), __float_as_uint(t[1][i]), __float_as_uint(t[2][i]), __float_as_uint(t[3][i])});
        }
        DRAIN();
        if (lane == 0) __hip_atomic_fetch_add((u32*)(ws + OFF_CNT + (UP ? 0 : 256)), 1u, __ATOMIC_RELAXED, __HIP_MEMORY_SCOPE_AGENT);
    }
}

#define LOOKST 18
#define RUNS 3
#define NRUN 32
#define T_DN (NRUN * RUNS)
constexpr int NBIG = NRUN * RUNS + (NSEG - 1 - NRUN) * 10, NTASK = NGU + NBIG + NDN + 80;

struct OutCtx { rsrc_t rs; const u16* x; u16* out; u32* errw; u32 wp; int wid; char* ws; };
template <int OT, int NT>
__device__ __forceinline__ void out_task(const OutCtx& cx, const int sg, const int c0, const int n0, const int tid, const int (&sel_idx)[TOPC], bool& adone, u32& rdy,
                                         bool& dead, char* redp, u32* nextp, u32* tctr) {
    const int wave = __builtin_amdgcn_readfirstlane(tid >> 6), lane = tid & 63, n = lane & 15, g = lane >> 4;
    const rsrc_t rs = cx.rs;
    if (!adone && rdy <= (u32)sg) {
        wait_word(rs, (u32)(OFF_FB + (wave * 4 + (lane & 3)) * 128 + (sg & 1) * 4), true, (u32)(sg >> 1) + 1, dead, cx.errw, 32u, lane);
        u32x2 f = ldx2(rs, (u32)(OFF_FB + (wave * 4 + (lane & 3)) * 128));
        adone = __all(f[0] >= NSEG / 2 && f[1] >= NSEG / 2);
    }
    u32x4 af[OT][5], bq[NT][5];
    const u32 bo = amap((u32)(wave * 4096 + lane * 16));
#pragma unroll
    for (int c = 0; c < OT; ++c) {
        const u32 bb = far_off(cx.wp, FAR_BX + (u32)(sg * 40 + c0 + c) * 32768);
#pragma unroll
        for (int hh = 0; hh < 4; ++hh) af[c][hh] = ldx4(rs, bo + hh * 1024, bb);
    }
    const int ti = (tid >> 6) & (OT * NT - 1);
    const int token = sg * SEGL + (n0 + (ti & (NT - 1))) * 16 + n;
    const int col = (c0 + ti / NT) * 16 + 4 * g;
    int sel = -1;
#pragma unroll
    for (int c = 0; c < TOPC; ++c) if (sel_idx[c] == token) sel = c;
    const u32x2 xv = *(const u32x2*)(cx.x + (size_t)token * D_ + col);
    const u32x2 nv = ldx2(rs, (u32)(OFF_XN + ((size_t)token * D_ + col) * 2));
    f32x4 uv = {0.f, 0.f, 0.f, 0.f};
    auto expert = [&] {
    if (__any(sel >= 0)) {
        wait_word(rs, (u32)(OFF_CNT + 256), true, (u32)NDN, dead, cx.errw, 256u, lane);
        const u32 o = sel >= 0 ? (u32)(OFF_UPD + ((size_t)sel * D_ + col) * 4) : OOB;
        const float rv = __uint_as_float(ldx1(rs, (u32)OFF_R + (u32)token * 4)), rg = bfr(1.0f / (1.0f + __expf(-rv)));
        uv = (__builtin_bit_cast(f32x4, ldx4(rs, o)) + __builtin_bit_cast(f32x4, ldx4(rs, o + 8 * D_ * 4))) + __builtin_bit_cast(f32x4, ldx4(rs, o + 16 * D_ * 4));
#pragma unroll
        for (int q = 0; q < 4; ++q) uv[q] = bfr(bfr(uv[q]) * rg);
    }
    };
    const bool early = sg + 4 >= NSEG || rdy <= (u32)sg;
    if (early) expert();
    __builtin_amdgcn_sched_barrier(0);
    const bool late = sg + 4 >= NSEG;
    if (late) wait_flags(rs, (u32)((OT == 1 ? OFF_FZ + (wave * 2 + n0) * 128 : OFF_FP + wave * 128) + (lane & 1) * 16), true, 1u, dead, cx.errw, 64u, lane);
    else if (rdy <= (u32)sg) {
#pragma unroll 1
        for (u32 spins = 0; !dead; ++spins) {
            CFENCE();
            rdy = __builtin_amdgcn_readfirstlane(ldx1(rs, (u32)(OFF_RDY + (cx.wid & 7) * 128)));
            if (rdy > (u32)sg) break;
            __builtin_amdgcn_s_sleep(8);
            if (spins > SPIN_CAP) { if (lane == 0) atomicOr(cx.errw, 64u); dead = true; }
            if ((spins & 63) == 63 && ldx1(rs, (u32)OFF_ERR) != 0u) dead = true;
        }
        CFENCE();
    }
#pragma unroll
    for (int c = 0; c < OT; ++c) af[c][4] = ldx4(rs, (u32)(wave * 64 + lane) * 16, slot_off(cx.wp, sg) + z_off(cx.wp, (u32)(c0 + c)));
#pragma unroll
    for (int nt = 0; nt < NT; ++nt) {
#pragma unroll
        for (int hh = 0; hh < 4; ++hh) bq[nt][hh] = ldx4(rs, (u32)lane * 16, (u32)OFF_PX + (u32)((sg * H_ + wave * 4 + hh) * 2 + n0 + nt) * 1024);
        bq[nt][4] = ldx4(rs, (u32)lane * 16, (u32)OFF_PXM + (u32)((sg * 8 + wave) * 2 + n0 + nt) * 1024);
    }
    CFENCE();
    u32 next = 0;
    if (tid == 0) next = !late ? __hip_atomic_fetch_add(tctr, 1u, __ATOMIC_RELAXED, __HIP_MEMORY_SCOPE_AGENT)
                                 : __hip_atomic_load(tctr, __ATOMIC_RELAXED, __HIP_MEMORY_SCOPE_AGENT);
    if (!early) expert();
    f32x4 (*red)[OT * NT][64] = (f32x4(*)[OT * NT][64])redp;
#pragma unroll
    for (int c = 0; c < OT; ++c)
#pragma unroll
        for (int nt = 0; nt < NT; ++nt) {
            f32x4 acc = {0.f, 0.f, 0.f, 0.f};
#pragma unroll
            for (int k = 0; k < 5; ++k) acc = mfma16(af[c][k], bq[nt][k], acc);
            red[wave][c * NT + nt][lane] = acc;
        }
    if (tid == 0) {
        if (late && next < (u32)NTASK) next = __hip_atomic_fetch_add(tctr, 1u, __ATOMIC_RELAXED, __HIP_MEMORY_SCOPE_AGENT);
        *nextp = next;
    }
    LBAR();
    if (tid < OT * NT * 64) {
        f32x4 sum = red[0][ti][lane];
#pragma unroll
        for (int wv = 1; wv < 8; ++wv) sum += red[wv][ti][lane];
        float res[4];
#pragma unroll
        for (int q = 0; q < 4; ++q) {
            float xx = bf2f((u16)((xv[q >> 1] >> ((q & 1) * 16)) & 0xffff));
            float nn = bf2f((u16)((nv[q >> 1] >> ((q & 1) * 16)) & 0xffff));
            float bo2 = nn;
            if (sel >= 0) bo2 = bfr(nn + uv[q]);
            float gg = bfr(bo2 / (1.0f + __expf(-bo2)));
            float sv = bfr(sum[q]);
            res[q] = bfr(xx + bfr(sv * gg));
        }
        *(u32x2*)(cx.out + (size_t)token * D_ + col) = (u32x2){pack2(res[0], res[1]), pack2(res[2], res[3])};
    }
}

__device__ __forceinline__ void out_run(const OutCtx& cx, const int sg, const int c0, const int nr, const int tid, u32& rdy, bool& dead, char* smem, int& par,
                                        u32* nextp, u32* tctr) {
    const int wave = __builtin_amdgcn_readfirstlane(tid >> 6), lane = tid & 63, n = lane & 15, g = lane >> 4;
    const rsrc_t rs = cx.rs;
    if (rdy <= (u32)sg) {
        wait_word(rs, (u32)(OFF_RDY + (cx.wid & 7) * 128), true, (u32)sg + 1u, dead, cx.errw, 64u, lane);
        rdy = __builtin_amdgcn_readfirstlane(ldx1(rs, (u32)(OFF_RDY + (cx.wid & 7) * 128)));
    }
    struct Rd { u32x4 af[4][5]; u32x2 xv, nv; } r0, r1;
    u32x4 bq[2][5];
    const u32 bo = amap((u32)(wave * 4096 + lane * 16)), eo = (u32)((sg * SEGL + (wave & 1) * 16 + n) * D_ + (wave >> 1) * 16 + 4 * g);
    auto load = [&](Rd& r, int ct) {
#pragma unroll
        for (int c = 0; c < 4; ++c) {
            const u32 bb = far_off(cx.wp, FAR_BX + (u32)(sg * 40 + ct + c) * 32768);
#pragma unroll
            for (int hh = 0; hh < 4; ++hh) r.af[c][hh] = ldx4(rs, bo + hh * 1024, bb);
            r.af[c][4] = ldx4(rs, (u32)(wave * 64 + lane) * 16, slot_off(cx.wp, sg) + z_off(cx.wp, (u32)(ct + c)));
        }
        r.xv = *(const u32x2*)(cx.x + eo + ct * 16);
        r.nv = ldx2(rs, (u32)OFF_XN + (eo + (u32)ct * 16) * 2);
        CFENCE();
    };
    auto work = [&](const Rd& r, int ct, bool last) {
        u32 next = 0;
        if (last && tid == 0) next = __hip_atomic_fetch_add(tctr, 1u, __ATOMIC_RELAXED, __HIP_MEMORY_SCOPE_AGENT);
        f32x4 (*red)[8][64] = (f32x4(*)[8][64])(smem + par * 65536);
        par ^= 1;
#pragma unroll
        for (int c = 0; c < 4; ++c)
#pragma unroll
            for (int nt = 0; nt < 2; ++nt) {
                f32x4 acc = {0.f, 0.f, 0.f, 0.f};
#pragma unroll
                for (int k = 0; k < 5; ++k) acc = mfma16(r.af[c][k], bq[nt][k], acc);
                red[wave][c * 2 + nt][lane] = acc;
            }
        if (last && tid == 0) *nextp = next;
        LBAR();
        f32x4 sum = red[0][wave][lane];
#pragma unroll
        for (int wv = 1; wv < 8; ++wv) sum += red[wv][wave][lane];
        float res[4];
#pragma unroll
        for (int q = 0; q < 4; ++q) {
            const float xx = bf2f((u16)((r.xv[q >> 1] >> ((q & 1) * 16)) & 0xffff)), nn = bf2f((u16)((r.nv[q >> 1] >> ((q & 1) * 16)) & 0xffff));
            res[q] = bfr(xx + bfr(bfr(sum[q]) * bfr(nn / (1.0f + __expf(-nn)))));
        }
        *(u32x2*)(cx.out + eo + ct * 16) = (u32x2){pack2(res[0], res[1]), pack2(res[2], res[3])};
        CFENCE();
    };
#pragma unroll
    for (int nt = 0; nt < 2; ++nt) {
#pragma unroll
        for (int hh = 0; hh < 4; ++hh) bq[nt][hh] = ldx4(rs, (u32)lane * 16, (u32)OFF_PX + (u32)((sg * H_ + wave * 4 + hh) * 2 + nt) * 1024);
        bq[nt][4] = ldx4(rs, (u32)lane * 16, (u32)OFF_PXM + (u32)((sg * 8 + wave) * 2 + nt) * 1024);
    }
    load(r0, c0);
    load(r1, c0 + 4);
    work(r0, c0, false);
    if (nr == 2) { work(r1, c0 + 4, true); return; }
    load(r0, c0 + 8);
    work(r1, c0 + 4, false);
    if (nr == 3) { work(r0, c0 + 8, true); return; }
    load(r1, c0 + 12);
    work(r0, c0 + 8, false);
    work(r1, c0 + 12, true);
}

constexpr u32 L_VS = 40960;
__device__ __forceinline__ u32 xbuf(int b) { return b == 0 ? 65536u : (b == 1 ? 106496u : 0u); }
__device__ __forceinline__ void worker_role(const int wave, const int wid, const u64 tag, const u32 wp, const u16* __restrict__ x, const u32x32 (&W)[5],
                                            const u16* __restrict__ wg, const u16* __restrict__ wu,
                                            const u16* __restrict__ wd, char* __restrict__ ws, u16* __restrict__ out, char* smem) {
    const int lane = lane_id(), tid = wave * 64 + lane;
    const int n = lane & 15, g = lane >> 4;
    const int par = (wid >> 2) & 1, kind = (wid >> 3) & 1, h = (wid & 3) * 8 + (wid >> 4), type = kind * 2 + par;
    u32* errw = (u32*)(ws + OFF_ERR);
    const rsrc_t rs = mk_rsrc(ws);
    bool dead = false;
    const bool isqk = type < 2;
    int wslot = 1;
    u32* sh = (u32*)(smem + L_MISC);

    {
        const u32 lbase = (u32)(uintptr_t)(__attribute__((address_space(3))) char*)smem;
        const u32x4 srd = mk_srd(ws);
        const u32 vl = lbase + (u32)lane * 16, xp = (u32)(((wave >> 1) & 1) * 20480 + (wave < 4 ? 4096 + (wave & 1) * 8192 : (wave & 1) * 2048)), xv = xp + (u32)lane * 16, ca = lbase + L_MISC + 64;
        const u32 fo = (u32)(OFF_FA + kind * 4096 + h * 128 + par * 4);
        auto xsrc = [&](int it) { return it < NSEG / 2 ? (u32)OFF_XNP + (u32)(it * 2 + par) * 40960u : (u32)OFF_XNP; };
        auto now = [&](u32 v) {
            DRAIN();
            if (lane_id() == 0 && __hip_atomic_fetch_add((u32*)(smem + L_MISC + 68), 1u, __ATOMIC_RELAXED, __HIP_MEMORY_SCOPE_WORKGROUP) == v * 4 - 1) { stx1(rs, fo, v); XSTAMP(wid, 224 + v); }
        };
        WSTAMP(4);
        STAMPS();
        u32 b0 = xbuf(0), b1 = xbuf(1), b2 = xbuf(2);
        auto rot = [&] { const u32 t = b0; b0 = b1; b1 = b2; b2 = t; };
        if (wave < 4) {
            const u32 oq = (u32)(wave * 64 + lane) * 16;
#pragma unroll 1
            for (int it = 0; it < NSEG / 2; ++it) {
                if (isqk) {
                    if ((it & ~4) == 2) wait_init(rs, tag, 1 + (it >> 2));
                    const u32 ob = far_off(wp ^ KQX, NEAR_QX + (u32)((it * 2 + par) * H_ + h) * 8192);
                    wx0(W, vl + b0, oq, srd, xv, xsrc(it + 2), lbase + b2 + xp, ob, ob + 4096 STA);
                } else wx1(W, vl + b0, lbase + L_VS + (u32)(it & 1) * 8192 + oq STA);
                rot();
                CFLUSH();
                if (it < 3 || (it & 7) == 7) WSTAMP(0x10 + it);
            }
            DRAIN();
            LBAR();
        } else if (isqk) {
            const u32 oq = (u32)(tid & 255) * 16;
            LBAR();
#pragma unroll 1
            for (int it = 0; it < NSEG / 2; ++it) {
                const int sg = it * 2 + par;
                if ((it & ~4) == 2) sh[18] = wait_init(rs, tag, 1 + (it >> 2));
                const u32 ob = far_off(wp ^ KQX, NEAR_KX + (u32)(sg * H_ + h) * 8192);
                wk(W, vl + b0, oq, srd, ob, ob + 4096, 0.08838834764831845f * 1.4426950408889634f, xv, xsrc(it + 2), lbase + b2 + xp,
                   ca, (u32)it * 4 + 3, (u32)it, fo STA);
                rot();
                CFLUSH();
                if (it < NOWV) now((u32)it + 1);
            }
        } else {
            const u32 ob = amap((u32)(h * 1024 + lane * 16)), yp = 4096u + (u32)(wave & 1) * 6144u;
            LBAR();
#pragma unroll
            for (int i = 0; i < 10; ++i) {
                const u32 o = i < 2 ? (u32)i * 1024 : yp + (u32)(i - 2) * 1024;
                dma16_sc1(ws + xsrc(2) + xv + o, __builtin_amdgcn_readfirstlane(lbase + b2 + xp + o));
            }
            DRAIN();
            LBAR();
#pragma unroll 1
            for (int it = 1; it <= NSEG / 2; ++it) {
                rot();
                if ((it & ~4) == 2) sh[18] = wait_init(rs, tag, 1 + (it >> 2));
                wb(W, vl + L_VS + (u32)(~it & 1) * 8192, ob, srd, FAR_BX + (u32)((it * 2 - 2 + par) * NRED + (wave & 3) * 2) * 32768, wp, (u32)it,
                   xv, xsrc(it + 2), lbase + b2 + xp, xsrc(it + 2) + yp, lbase + b2 + xp + yp, ca, (u32)it * 4 - 1, (u32)it - 1, fo STA);
                CFLUSH();
                if (it <= NOWV) { now((u32)it); LBAR(); }
            }
        }
        DRAIN();
        LBAR();
        stx1(rs, wave == 0 && lane_id() == 0 ? fo : OOB, NSEG / 2);
    }
    WSTAMP(2);

    {
    const int lane = lane_id(), tid = wave * 64 + lane;
    u32* nextp = (u32*)(smem + L_MISC + 96);
    u32* tctr = (u32*)(ws + OFF_CNT + 512);
    if (tid == 0) nextp[0] = __hip_atomic_fetch_add(tctr, 1u, __ATOMIC_RELAXED, __HIP_MEMORY_SCOPE_AGENT);
    wait_flags(rs, (u32)OFF_IDX + (lane & 1) * 16, true, 1u, dead, errw, 256u, lane);
    __syncthreads();
    int sel_idx[TOPC];
    {
        u32x4 i0 = ldx4(rs, (u32)OFF_IDX), i1 = ldx4(rs, (u32)OFF_IDX + 16);
#pragma unroll
        for (int c = 0; c < 4; ++c) { sel_idx[c] = (int)i0[c] - 1; sel_idx[4 + c] = (int)i1[c] - 1; }
    }
    const u32 mrow = (ldx1(rs, (u32)(OFF_IDX + (lane & 7) * 4)) - 1u) & (S_ - 1);
    u32 order;
    {
        bool hit = false;
#pragma unroll
        for (int c = 0; c < TOPC; ++c) hit |= (sel_idx[c] >> 5) == lane;
        const u64 m = __ballot(hit && lane < 40), below = ((u64)1 << lane) - 1;
        const int pos = lane >= 40 ? lane : ((m >> lane) & 1 ? 40 - __popcll(m) + __popcll(m & below) : lane - __popcll(m & below));
        order = (u32)__builtin_amdgcn_ds_permute(pos * 4, lane);
    }
    WSTAMP(3);
    const OutCtx cx = {rs, x, out, errw, to_sgpr(sh[18]), wid, ws};
    bool adone = false;
    u32 rdy = 0;
    int par2 = 0;
#pragma unroll 1
    for (int tc = 0;; ++tc) {
        const int task = __builtin_amdgcn_readfirstlane(nextp[tc & 1]);
        if (task >= NTASK) break;
        char* redp = smem + par2 * 65536;
        par2 ^= 1;
        u32* np = nextp + ((tc + 1) & 1);
        const int t = task - NGU - (task >= NGU + T_DN ? NDN : 0);
        if (task < NGU) moe_task<true>(task, tid, mrow, wg, wu, ws, dead, redp, np, tctr);
        else if (task >= NGU + T_DN && t < T_DN) { moe_task<false>(task - NGU - T_DN, tid, mrow, wd, wd, ws, dead, redp, np, tctr); WSTAMP(5); }
        else if (t >= NBIG) {
            WSTAMP(0x100 + NSEG - 1);
            out_task<1, 1>(cx, NSEG - 1, (t - NBIG) >> 1, (t - NBIG) & 1, tid, sel_idx, adone, rdy, dead, redp, np, tctr);
        } else if (t < NRUN * RUNS) {
            const int j = t % RUNS;
            WSTAMP(0x100 + t / RUNS);
            par2 ^= 1;
            out_run(cx, (int)tab(order, t / RUNS), RUNS == 5 ? j * 8 : j * 12 + (j ? 4 : 0), RUNS == 5 ? 2 : j ? 3 : 4, tid, rdy, dead, smem, par2, np, tctr);
        } else {
            const int u = t - NRUN * RUNS + NRUN * 10;
            WSTAMP(0x100 + u / 10);
            out_task<4, 2>(cx, (int)tab(order, u / 10), (u % 10) * 4, 0, tid, sel_idx, adone, rdy, dead, redp, np, tctr);
        }
        WSTAMP(0x300);
        if (task == NTASK - 1) {
            wait_word(rs, (u32)(OFF_CNT + 768), true, 8u, dead, errw, 64u, lane);
            if (tid < 216) stx2(rs, tid < 192 ? (u32)(OFF_INI + tid * 8) : (u32)(OFF_SUM + (tid - 192) * 128), (u32x2){~(u32)tag, ~cal_tag(tag, ~(u32)tag)});
        }
    }
    }
}

constexpr int FUSED_LDS = L_MISC + 1024;
#define ORD_A 0x143210
#define ORD_C 0x101234
#define KERNEL_SYM "fused_kernel"
#define CODE_KB 112
extern "C" __global__ __launch_bounds__(512, 2) void fused_kernel(const u16* __restrict__ x, const u16* __restrict__ mem0, const u16* __restrict__ w_r,
                                                        const u16* __restrict__ Wq, const u16* __restrict__ Wk,
                                                        const u16* __restrict__ Wv, const u16* __restrict__ Wo,
                                                        const u16* __restrict__ wg, const u16* __restrict__ wu,
                                                        const u16* __restrict__ wd, char* __restrict__ ws, u16* __restrict__ out) {
    extern __shared__ __attribute__((aligned(16))) char smem[];
    const int wave = __builtin_amdgcn_readfirstlane(threadIdx.x >> 6);
    u64 gen = 0, tag = 0, old = 0;
    u32 xcc, se;
    asm volatile("s_getreg_b32 %0, hwreg(HW_REG_XCC_ID, 0, 3)" : "=s"(xcc));
    asm volatile("s_getreg_b32 %0, hwreg(HW_REG_HW_ID, 13, 2)" : "=s"(se));
    u32 bid = blockIdx.x;
    asm volatile("" : "+s"(bid));
    const bool first = (int)bid >= 0;
    u32 bq = first ? (bid & ~31u) + se * 8u + xcc : bid & 255u;
    asm volatile("" : "+s"(bq));
    const int b = (int)bq, k = b >> 3, xg = b & 3;
    if (threadIdx.x >= 448 && threadIdx.x < 453) {
        const int lane = threadIdx.x & 63;
        const u32 pg = (bid >> 3 & 31u) + 32u * (u32)lane;
        const char* tp = lane < 4 ? ws + min(pg << 21, (u32)WS_TOTAL - 4u) : (const char*)out + (size_t)(bid >> 3 & 3u) * 655360;
        u32 sv;
        asm volatile("s_mov_b32 %0, m0\n\ts_mov_b32 m0, %2\n\ts_nop 0\n\tglobal_load_lds_dword %1, off\n\ts_mov_b32 m0, %0"
                     : "=&s"(sv) : "v"(tp), "s"((u32)(uintptr_t)(__attribute__((address_space(3))) char*)smem + L_MISC + 256u) : "memory");
    }
    if (wave == 6 && (bid >> 3 & 31u) < CODE_KB / 8) {
        u32 sv;
        asm volatile("s_getpc_b64 s[98:99]\nLc%=:\n\ts_sub_u32 s98, s98, Lc%=-" KERNEL_SYM "\n\ts_subb_u32 s99, s99, 0\n\t"
                     "s_mov_b32 %0, m0\n\ts_mov_b32 m0, %2\n\ts_nop 0\n\tglobal_load_lds_dword %1, s[98:99]\n\ts_mov_b32 m0, %0"
                     : "=&s"(sv) : "v"((bid >> 3 & 31u) * 8192u + (threadIdx.x & 63u) * 128u),
                       "s"((u32)(uintptr_t)(__attribute__((address_space(3))) char*)smem + L_MISC + 768u) : "s98", "s99", "scc", "memory");
    }
    u64* claim = (u64*)(ws + OFF_CLAIM);
    u32* sh = (u32*)(smem + L_MISC);
    auto try_claim = [&] {
        if (wave == 0 && first) {
            asm volatile("s_load_dwordx2 %0, %1, 0x0\n\ts_waitcnt lgkmcnt(0)" : "=s"(gen) : "s"(ws + OFF_GEN) : "memory");
            tag = (gen + 0x9E3779B97F4A7C15ull) * 0xBF58476D1CE4E5B9ull;
            tag ^= tag >> 31;
            tag *= 0x94D049BB133111EBull;
            tag ^= tag >> 29;
            if (lane_id() == 0) {
                old = __hip_atomic_exchange(claim + b * 8, tag, __ATOMIC_RELAXED, __HIP_MEMORY_SCOPE_AGENT);
                sh[1] = (u32)tag; sh[2] = (u32)(tag >> 32); sh[3] = (u32)gen; sh[4] = (u32)(gen >> 32);
            }
        }
    };
    auto settle = [&] {
        const int tid = wave * 64 + lane_id();
        if (tid == 0 && first) {
            u32 vb = (u32)b;
#pragma unroll 1
            for (int i = 0; i < 256 && old == tag; ++i) {
                vb = (vb + 1) & 255u;
                old = __hip_atomic_exchange(claim + vb * 8, tag, __ATOMIC_RELAXED, __HIP_MEMORY_SCOPE_AGENT);
            }
            sh[0] = vb;
        }
        LBAR();
        const u32 bc = __builtin_amdgcn_readfirstlane(sh[0]);
        asm volatile("s_cmp_eq_u32 %0, %1\n\ts_cbranch_scc1 Lgo%=\n\t"
                     "s_or_b32 s2, %0, 0x80000000\n\ts_mov_b64 s[0:1], %2\n\tv_mov_b32 v0, %3\n\ts_waitcnt vmcnt(0) lgkmcnt(0)\n\t"
                     "s_getpc_b64 s[4:5]\nLpc%=:\n\ts_sub_u32 s4, s4, Lpc%=-" KERNEL_SYM "\n\ts_subb_u32 s5, s5, 0\n\ts_setpc_b64 s[4:5]\nLgo%=:"
                     :: "s"(bc), "s"(b), "s"(__builtin_amdgcn_kernarg_segment_ptr()), "v"(tid) : "s2", "scc", "memory");
        tag = (u64)(u32)__builtin_amdgcn_readfirstlane(sh[1]) | ((u64)(u32)__builtin_amdgcn_readfirstlane(sh[2]) << 32);
    };
    if (b & 4) {
        const int wid = k * 4 + xg, h = (wid & 3) * 8 + (wid >> 4), par = (wid >> 2) & 1;
        const bool isqk = ((wid >> 3) & 1) == 0;
        const int lane = lane_id();
        u32x32 W[5];
        u32 wp = 0;
        u32x2 f0;
        u64 tg2 = 0;
        bool got = false;
        auto xtile = [&](int t) {
#pragma unroll
            for (int i = 0; i < 5; ++i)
                dma16_sc1(ws + OFF_XNP + (size_t)(par + 2 * t) * 40960 + (wave * 5 + i) * 1024 + lane * 16,
                          __builtin_amdgcn_readfirstlane((u32)(uintptr_t)(__attribute__((address_space(3))) char*)smem + xbuf(t) + (u32)(wave * 5 + i) * 1024));
        };
        load_weights<ORD_A>([&](int st) {
            if (st == 0) try_claim();
            if (st == LOOKST) {
                asm volatile("global_load_dwordx2 %0, %1, off sc1" : "=&v"(f0) : "v"(ws + OFF_SUM + xcc * 128u) : "memory");
                tg2 = (u64)(u32)__builtin_amdgcn_readfirstlane(sh[1]) | ((u64)(u32)__builtin_amdgcn_readfirstlane(sh[2]) << 32);
            }
            if (st == 3) {
                asm volatile("" : "+v"(f0));
                got = __all(f0[1] == cal_tag(tg2, f0[0]));
                if (got) { xtile(0); xtile(1); }
                wp = to_sgpr(f0[0]);
            }
        }, (isqk ? Wq : Wv) + h * DH_, isqk ? Wk + h * DH_ : Wo + (size_t)h * DH_ * D_, !isqk, smem, wave, lane,
           wave >> 2, 2 * (wave & 3), wave >> 2, 2 * (wave & 3) + 1, W);
        settle();
        const rsrc_t rs = mk_rsrc(ws);
        if (!got) { wp = wait_init(rs, tag, 0); xtile(0); xtile(1); }
        sh[16] = sh[17] = 0u;
        DRAIN();
        LBAR();
        PIN_W(W);
        worker_role(wave, wid, tag, wp, x, W, wg, wu, wd, ws, out, smem);
        __builtin_amdgcn_endpgm();
    }
    const int hl = ((k >> 1) ^ (xg >> 1)) & 1, j = (k >> 2) * 2 + (k & 1);
    if (CFG ? xg < 2 : j < 8) {
        const int h = CFG ? xg * 16 + (j >> 3) * 8 + hl * 4 + ((j >> 1) & 3) : xg * 8 + hl * 4 + (j >> 1);
        u32x32 W[5];
        auto hook = [&](int st) {
            if (st == 0) try_claim();
        };
        if ((j & 1) == 0) {
            load_weights<ORD_C>(hook, Wq + h * DH_, Wk + h * DH_, false, smem, wave, lane_id(), wave < 4, wave & 3, wave < 4, 4 + (wave & 3), W);
            settle();
            PIN_W(W);
            s_role(wave, h, xg, tag, mem0, W, ws, smem);
            __builtin_amdgcn_endpgm();
        }
        if (wave < 4) { load_weights<ORD_C>(hook, Wv + h * DH_, Wo + (size_t)h * DH_ * D_, true, smem, wave, lane_id(), 0, wave, 0, 4 + wave, W); PIN_W(W); }
        else { load_weights<ORD_C | (1 << 21)>(hook, Wv + h * DH_, Wo + (size_t)h * DH_ * D_, true, smem, wave, lane_id(), 1, 2 * (wave & 3), 1, 2 * (wave & 3) + 1, W); PIN_W(W); }
        settle();
        PIN_W(W);
        v_role(wave, h, xg, tag, mem0, W, ws, smem);
        __builtin_amdgcn_endpgm();
    }
    constexpr int NR = CFG ? 10 : 5, NS = CFG ? 6 : 3, NP = CFG ? 4 : 2;
    const int jr = CFG ? j : j - 8, hi = (CFG ? xg - 2 : xg) * 2 + hl;
    try_claim();
    init_role([&] { settle(); return tag; }, wave, jr < NR ? hi * NR + jr : jr == NR ? 40 + hi : 48 + hi * NP + jr - (NR + NS - NP), xcc, ((se ^ xcc) >> 1) & 1u, x, w_r, Wq, Wk, Wv, Wo, ws, smem);
    gen = (u64)sh[3] | ((u64)sh[4] << 32);
    tag = (u64)(u32)__builtin_amdgcn_readfirstlane(sh[1]) | ((u64)(u32)__builtin_amdgcn_readfirstlane(sh[2]) << 32);
    const int wi = hl * NS + jr - NR;
    const bool aux = wi == 2 * NS - 1;
    if (jr < NR) { if (wave < 6) r_role(wave, tile_of(xg, hl, jr), xg, wait_init(mk_rsrc(ws), tag, 1), ws, smem); }
    else if (!aux) {
        if (wave >= 4) { if (wi == 0 && xg == 0 && wave == 7) topk_role(tag, ws); return; }
        const u32 wp = wait_init(mk_rsrc(ws), tag, 1);
        LBAR();
        if (wave == 0 && wi == 0 && lane_id() == 0) __hip_atomic_fetch_add((u32*)(ws + OFF_CNT + 768), 1u, __ATOMIC_RELAXED, __HIP_MEMORY_SCOPE_AGENT);
        if (wave == 2 && wi) fill_role(wp, xg * 4 + wi - 1, ws);
        if (wave == 3 && xg == 3 && wi == 4) relay_role(wp, xg, ws);
        if (wave == 1 || (wave == 2 && !wi)) warm_role(wp, xg, wi - wave, ws, smem);
    }
    else if (wave == 1) {
        const u32 wp = wait_init(mk_rsrc(ws), tag, 1);
        if (lane_id() == 0) __hip_atomic_fetch_add((u32*)(ws + OFF_CNT + 768), 1u, __ATOMIC_RELAXED, __HIP_MEMORY_SCOPE_AGENT);
        lead_role(wp, xg, ws, smem);
    }
    else if (aux && xg == 2 && wave == 0) {
        const rsrc_t rs = mk_rsrc(ws);
        const int lane = lane_id();
#pragma unroll 1
        for (u32 spins = 0; spins < SPIN_CAP; ++spins) {
            CFENCE();
            bool ok = true;
#pragma unroll
            for (int q = 0; q < 4; ++q) {
                u32x2 f = ldx2(rs, (u32)(OFF_CLAIM + (q * 64 + lane) * 64));
                ok = ok && f[0] == (u32)tag && f[1] == (u32)(tag >> 32);
            }
            if (__all(ok)) break;
            __builtin_amdgcn_s_sleep(32);
        }
        if (lane == 0) __hip_atomic_store((u64*)(ws + OFF_GEN), gen + 1ull, __ATOMIC_RELAXED, __HIP_MEMORY_SCOPE_AGENT);
#pragma unroll
        for (int q = 0; q < 4; ++q) stx2(rs, (u32)(OFF_CLAIM + (q * 64 + lane) * 64), (u32x2){~(u32)tag, 0u});
    }
}

extern "C" void kernel_launch(void* const* d_in, const int* in_sizes, int n_in, void* d_out, int out_size,
                              void* d_ws, size_t ws_size, hipStream_t stream) {
    const u16* x = (const u16*)d_in[0];
    const u16* mem0 = (const u16*)d_in[1];
    const u16* Wq = (const u16*)d_in[2];
    const u16* Wk = (const u16*)d_in[3];
    const u16* Wv = (const u16*)d_in[4];
    const u16* Wo = (const u16*)d_in[5];
    const u16* w_r = (const u16*)d_in[8];
    const u16* w_gate = (const u16*)d_in[9];
    const u16* w_up = (const u16*)d_in[10];
    const u16* w_down = (const u16*)d_in[11];
    char* ws = (char*)d_ws;
    u16* out = (u16*)d_out;

    hipFuncSetAttribute((const void*)fused_kernel, hipFuncAttributeMaxDynamicSharedMemorySize, FUSED_LDS);
    fused_kernel<<<256, 512, FUSED_LDS, stream>>>(x, mem0, w_r, Wq, Wk, Wv, Wo, w_gate, w_up, w_down, ws, out);
}
```
